```python
import torch

def _softmax(x, axis=-1):
    return torch.softmax(x, dim=axis)
def _gelu(x):
    return torch.nn.functional.gelu(x, approximate="tanh")
def _silu(x):
    return torch.nn.functional.silu(x)
def _sigmoid(x):
    return torch.sigmoid(x)
def _layernorm(x, axis=-1, eps=1e-5):
    m = x.mean(dim=axis, keepdim=True)
    v = x.var(dim=axis, keepdim=True, unbiased=False)
    return (x - m) / torch.sqrt(v + eps)
def _scatter_add(tgt, idx, src, axis=0):
    return tgt.clone().index_add_(axis, idx, src)
def _segment_sum(src, seg, n):
    out = torch.zeros((n,) + src.shape[1:], dtype=src.dtype)
    return out.index_add_(0, seg, src)
def _pad(x, pw):
    flat = tuple(v for pair in reversed(pw) for v in pair)
    return torch.nn.functional.pad(x, flat)

kernel_name = 'standalone__window_attn_swin__vision'

def setup_inputs():

    _w = (lambda a: (lambda t: t.to(torch.bfloat16) if t.is_floating_point() else t)(torch.as_tensor(a)))
    H = W = 512
    C = 192
    ws = 8
    nh = 4
    shift = ws // 2
    coords = torch.stack(torch.meshgrid(torch.arange(ws), torch.arange(ws), indexing='ij'), 0).reshape(2, -1)
    rel = coords[:, :, None] - coords[:, None, :]
    rel[0] += ws - 1
    rel[1] += ws - 1
    rel[0] *= 2 * ws - 1
    rp_idx = rel.sum(0).to(torch.int64)
    img_mask = torch.zeros((1, H, W, 1), dtype=float)
    cnt = 0
    for hs in (slice(0, H - ws), slice(H - ws, H - shift), slice(H - shift, H)):
        for vs in (slice(0, W - ws), slice(W - ws, W - shift), slice(W - shift, W)):
            img_mask[:, hs, vs, :] = cnt
            cnt += 1
    mw = img_mask.reshape(1, H // ws, ws, W // ws, ws, 1).permute(0, 1, 3, 2, 4, 5).reshape(-1, ws * ws)
    am = mw[:, :, None] - mw[:, None, :]
    attn_mask = torch.where(am != 0, -100.0, 0.0).to(float)
    return {
        "x": _w((lambda *s: torch.randn(*s, dtype=torch.bfloat16) * 1.6)(1, H, W, C)),
        "wqkv": _w((lambda *s: torch.randn(*s, dtype=torch.bfloat16) * 1.6)(C, 3 * C) * 0.02),
        "wo": _w((lambda *s: torch.randn(*s, dtype=torch.bfloat16) * 0.05)(C, C) * 0.02),
        "rpb": _w((lambda *s: torch.randn(*s, dtype=torch.bfloat16) * 0.2)((2 * ws - 1) * (2 * ws - 1), nh)),
        "rp_idx": _w(rp_idx),
        "attn_mask": _w(attn_mask),
    }

def reference(x, wqkv, wo, rpb, rp_idx, attn_mask):
    B, H, W, C = x.shape
    N = rp_idx.shape[0]
    ws = int(round(N ** 0.5))
    nh = rpb.shape[-1]
    hd = C // nh
    shift = ws // 2
    xs = torch.roll(x, (-shift, -shift), (1, 2))
    nWh = H // ws
    nWw = W // ws
    xw = (lambda x, *d: x.permute(*d))(xs.reshape(B, nWh, ws, nWw, ws, C), 0, 1, 3, 2, 4, 5).reshape(B * nWh * nWw, N, C)
    qkv = (lambda x, *d: x.permute(*d))(torch.einsum('wnc,cd->wnd', xw, wqkv).reshape(-1, N, 3, nh, hd), 2, 0, 3, 1, 4)
    q, k, v = qkv[0], qkv[1], qkv[2]
    attn = torch.einsum('whid,whjd->whij', q, k) * (hd ** -0.5)
    bias = (lambda x, *d: x.permute(*d))(rpb[rp_idx.reshape(-1)].reshape(N, N, nh), 2, 0, 1)
    attn = attn + bias[None, :, :, :]
    attn = (attn.reshape(B, nWh * nWw, nh, N, N) + attn_mask[None, :, None, :, :]).reshape(-1, nh, N, N)
    attn = _softmax(attn, axis=-1)
    out = (lambda x, *d: x.permute(*d))(torch.einsum('whij,whjd->whid', attn, v), 0, 2, 1, 3).reshape(-1, N, C)
    out = torch.einsum('wnc,cd->wnd', out, wo)
    out = (lambda x, *d: x.permute(*d))(out.reshape(B, nWh, nWw, ws, ws, C), 0, 1, 3, 2, 4, 5).reshape(B, H, W, C)
    return torch.roll(out, (shift, shift), (1, 2))
```

```cpp
#include <hip/hip_runtime.h>
#include <hip/hip_bf16.h>

typedef unsigned int u32x4 __attribute__((ext_vector_type(4)));

static __device__ __forceinline__ float bf2f(unsigned short u) { return __uint_as_float((unsigned)u << 16); }
static __device__ __forceinline__ unsigned short f2bf(float f) { return __builtin_bit_cast(unsigned short, (__bf16)f); }

__global__ __launch_bounds__(256, 1) void win_attn_kernel(const char* x_, const char* wqkv_, const char* wo_, const char* rpb_,
                                                          const char* rpi_, const char* mask_, char* out_) {
    __shared__ __attribute__((aligned(16))) char smem[163840];
    const void* ka = (const void*)__builtin_amdgcn_kernarg_segment_ptr();
    unsigned bid = blockIdx.x, tid = threadIdx.x, flg, w;
    unsigned ldsb = (unsigned)(size_t)smem;
    asm volatile(
        "s_load_dwordx8 s[68:75], s[0:1], 0x0\n"
        "s_load_dwordx4 s[76:79], s[0:1], 0x20\n"
        "s_load_dwordx2 s[16:17], s[0:1], 0x30\n"
        "v_readfirstlane_b32 s42, v0\n"
        "v_and_b32 v1, 63, v0\n"
        "s_lshr_b32 s31, s2, 1\n"
        "s_lshr_b32 s42, s42, 6\n"
        "s_and_b32 s31, s31, 0x7c\n"
        "v_lshlrev_b32 v2, 4, v1\n"
        "s_add_u32 s31, s31, s42\n"
        "s_and_b32 s35, s31, 31\n"
        "s_min_u32 s35, s35, 25\n"
        "s_getpc_b64 s[66:67]\n"
        "v_lshl_or_b32 v10, s35, 10, v2\n"
        "global_load_dwordx4 v[60:63], v10, s[66:67]\n"
        "s_lshl_b32 s31, s31, 6\n"
        "v_lshl_or_b32 v21, v1, 13, s31\n"
        "s_min_u32 s50, s31, 1792\n"
        "v_mul_u32_u24 v11, 171, v1\n"
        "v_lshrrev_b32 v4, 10, v11\n"
        "v_bfe_u32 v7, v11, 13, 1\n"
        "v_add_u32 v11, 10944, v11\n"
        "v_lshrrev_b32 v5, 10, v11\n"
        "v_bfe_u32 v8, v11, 13, 1\n"
        "v_add_u32 v11, 10944, v11\n"
        "v_lshrrev_b32 v6, 10, v11\n"
        "v_bfe_u32 v9, v11, 13, 1\n"
        "s_mul_i32 s65, s42, 18432\n"
        "s_mul_i32 s40, s42, 3072\n"
        "s_mov_b32 s14, 33554432\n"
        "s_mov_b32 s15, 0x20000\n"
        "s_mov_b32 s10, 100663296\n"
        "s_mov_b32 s11, 0x20000\n"
        "s_and_b32 s51, s42, 1\n"
        "s_mul_i32 s35, s42, 96\n"
        "s_movk_i32 s31, 1056\n"
        "s_movk_i32 s45, 4608\n"
        "v_add_u32 v3, s35, v2\n"
        "v_bfe_u32 v12, v4, 2, 1\n"
        "v_mad_u32_u24 v128, v4, s31, v3\n"
        "v_sub_u32 v12, v12, v7\n"
        "v_mad_i32_i24 v128, v12, s45, v128\n"
        "v_bfe_u32 v13, v5, 2, 1\n"
        "v_mad_u32_u24 v129, v5, s31, v3\n"
        "v_sub_u32 v13, v13, v8\n"
        "v_mad_i32_i24 v129, v13, s45, v129\n"
        "v_bfe_u32 v14, v6, 2, 1\n"
        "v_mad_u32_u24 v130, v6, s31, v3\n"
        "v_sub_u32 v14, v14, v9\n"
        "v_mad_i32_i24 v130, v14, s45, v130\n"
        "s_mov_b32 s22, 221184\n"
        "s_mov_b32 s23, 0x20000\n"
        "s_lshl_b32 s48, s42, 11\n"
        "s_lshl_b32 s36, s2, 13\n"
        "v_and_b32 v19, 16, v1\n"
        "s_add_u32 s36, s36, s48\n"
        "s_add_u32 s36, s36, 4194304\n"
        "s_add_u32 s48, s48, 155648\n"
        "s_lshl_b32 s35, s51, 6\n"
        "v_xor_b32 v249, v2, v19\n"
        "v_xor_b32 v249, s35, v249\n"
        "v_xor_b32 v253, 32, v249\n"
        "s_and_b32 s31, s2, 63\n"
        "s_lshl_b32 s31, s31, 3\n"
        "s_add_u32 s31, s31, 4\n"
        "s_lshl_b32 s35, s42, 5\n"
        "s_movk_i32 s41, 384\n"
        "s_movk_i32 s45, -384\n"
        "v_lshrrev_b32 v10, 2, v4\n"
        "v_mad_i32_i24 v11, v10, s45, v2\n"
        "v_add_u32 v10, s31, v10\n"
        "v_lshl_or_b32 v242, v7, 4, s35\n"
        "v_and_b32 v10, 511, v10\n"
        "v_xor_b32 v11, v11, v242\n"
        "v_mad_u32_u24 v242, v10, s41, v11\n"
        "v_lshrrev_b32 v10, 2, v5\n"
        "v_mad_i32_i24 v11, v10, s45, v2\n"
        "v_add_u32 v10, s31, v10\n"
        "v_add_u32 v11, 1024, v11\n"
        "v_lshl_or_b32 v243, v8, 4, s35\n"
        "v_and_b32 v10, 511, v10\n"
        "v_xor_b32 v11, v11, v243\n"
        "v_mad_u32_u24 v243, v10, s41, v11\n"
        "v_lshrrev_b32 v10, 2, v6\n"
        "v_mad_i32_i24 v11, v10, s45, v2\n"
        "v_add_u32 v10, s31, v10\n"
        "v_add_u32 v11, 2048, v11\n"
        "v_lshl_or_b32 v244, v9, 4, s35\n"
        "v_and_b32 v10, 511, v10\n"
        "v_xor_b32 v11, v11, v244\n"
        "v_mad_u32_u24 v244, v10, s41, v11\n"
        "s_mul_i32 s43, s31, 384\n"
        "s_add_u32 s31, s31, 4\n"
        "s_and_b32 s31, s31, 511\n"
        "s_mul_i32 s44, s31, 384\n"
        "s_lshr_b32 s31, s2, 6\n"
        "s_lshl_b32 s31, s31, 3\n"
        "s_add_u32 s47, s31, s42\n"
        "s_waitcnt lgkmcnt(0)\n"
        "s_mov_b32 s20, s70\n"
        "s_and_b32 s21, s71, 0xffff\n"
        "s_mov_b32 s12, s78\n"
        "s_and_b32 s13, s79, 0xffff\n"
        "s_sub_u32 s52, s72, 221184\n"
        "s_subb_u32 s53, s73, 0\n"
        "s_sub_u32 s56, s76, 294912\n"
        "s_subb_u32 s57, s77, 0\n"
        "v_cmp_gt_u32 vcc, 38, v1\n"
        "v_mov_b32 v22, s50\n"
        "v_mov_b32 v136, s74\n"
        "v_cndmask_b32 v21, v22, v21, vcc\n"
        "v_mov_b32 v137, s75\n"
        "s_bfm_b64 exec, 38, 0\n"
        "v_mov_b32 v136, s56\n"
        "v_mov_b32 v137, s57\n"
        "s_bfm_b64 exec, 36, 0\n"
        "v_mov_b32 v136, s52\n"
        "v_mov_b32 v137, s53\n"
        "s_bfm_b64 exec, 27, 0\n"
        "v_mov_b32 v136, s70\n"
        "v_mov_b32 v137, s71\n"
        "s_mov_b64 exec, -1\n"
        "v_add_co_u32 v136, vcc, v136, v21\n"
        "s_nop 1\n"
        "v_addc_co_u32 v137, vcc, 0, v137, vcc\n"
        "global_load_dword v136, v[136:137], off\n"
        "s_add_u32 m0, s65, 0\n"
        "s_mov_b32 s64, 0\n"
        "buffer_load_dwordx4 v128, s[20:23], s64 offen lds\n"
        "buffer_load_dwordx4 v129, s[20:23], s64 offen offset:1024 lds\n"
        "buffer_load_dwordx4 v130, s[20:23], s64 offen offset:2048 lds\n"
        "s_add_u32 m0, s65, 3072\n"
        "s_mov_b32 s64, 36864\n"
        "buffer_load_dwordx4 v128, s[20:23], s64 offen lds\n"
        "buffer_load_dwordx4 v129, s[20:23], s64 offen offset:1024 lds\n"
        "buffer_load_dwordx4 v130, s[20:23], s64 offen offset:2048 lds\n"
        "s_mov_b32 m0, s48\n"
        "s_sub_u32 s64, s36, 4194304\n"
        "buffer_load_dwordx4 v249, s[12:15], s64 offen lds\n"
        "buffer_load_dwordx4 v253, s[12:15], s64 offen offset:1024 lds\n"
        "s_mov_b32 s52, s72\n"
        "s_and_b32 s53, s73, 0xffff\n"
        "s_mov_b32 s8, s68\n"
        "s_and_b32 s9, s69, 0xffff\n"
        "s_mov_b32 s60, s74\n"
        "s_and_b32 s61, s75, 0xffff\n"
        "s_mov_b32 s56, s76\n"
        "s_and_b32 s57, s77, 0xffff\n"
        "s_and_b32 s17, s17, 0xffff\n"
        "s_add_u32 s47, s47, 68\n"
        "s_mul_i32 s47, s47, 196608\n"
        "s_lshl2_add_u32 s37, s51, s31\n"
        "s_add_u32 s37, s37, 4\n"
        "s_mul_i32 s37, s37, 196608\n"
        "v_and_b32 v15, 15, v1\n"
        "v_lshrrev_b32 v16, 4, v1\n"
        "v_and_b32 v17, 12, v1\n"
        "v_and_b32 v18, 0xf0, v2\n"
        "v_add_u32 v17, v17, v15\n"
        "v_bfe_u32 v10, v1, 2, 3\n"
        "v_lshrrev_b32 v11, 5, v1\n"
        "v_lshl_or_b32 v10, v11, 4, v10\n"
        "v_and_b32 v255, 3, v1\n"
        "v_mul_u32_u24 v10, 96, v10\n"
        "v_lshl_add_u32 v255, v255, 3, s65\n"
        "v_add_u32 v255, v255, v10\n"
        "v_lshrrev_b32 v20, 1, v15\n"
        "v_xor_b32 v20, v20, v16\n"
        "v_lshlrev_b32 v20, 4, v20\n"
        "v_mad_u32_u24 v240, v17, s41, v20\n"
        "v_lshl_or_b32 v251, v17, 7, v20\n"
        "v_add_u32 v240, 73728, v240\n"
        "v_add_u32 v251, 155648, v251\n"
        "v_xor_b32 v241, 64, v240\n"
        "v_xor_b32 v252, 64, v251\n"
        "s_lshl_b32 s67, s42, 10\n"
        "s_add_u32 s67, s67, 139264\n"
        "v_lshlrev_b32 v134, 3, v0\n"
        "v_lshlrev_b32 v135, 2, v0\n"
        "s_lshl_b32 s35, s51, 7\n"
        "v_and_b32 v10, 48, v1\n"
        "v_xor_b32 v131, s35, v2\n"
        "v_add_u32 v135, 139264, v135\n"
        "v_xor_b32 v131, v131, v10\n"
        "s_lshl_b32 s46, s42, 12\n"
        "v_xor_b32 v132, 64, v131\n"
        "v_lshlrev_b32 v11, 5, v16\n"
        "v_xor_b32 v11, v11, v18\n"
        "v_lshl_or_b32 v11, v17, 8, v11\n"
        "v_add_u32 v140, 122880, v11\n"
        "v_xor_b32 v141, 16, v140\n"
        "v_xor_b32 v142, 128, v140\n"
        "v_xor_b32 v143, 144, v140\n"
        "v_lshrrev_b32 v10, 2, v15\n"
        "v_cmp_eq_u32 vcc, v10, v16\n"
        "v_mov_b32 v11, 0x3fb9\n"
        "v_mov_b32 v189, 0\n"
        "v_and_b32 v10, 48, v2\n"
        "v_cndmask_b32 v188, 0, v11, vcc\n"
        "v_lshlrev_b64 v[188:189], v10, v[188:189]\n"
        "v_mov_b32 v11, 15957\n"
        "v_mov_b32 v149, 0\n"
        "v_cndmask_b32 v148, 0, v11, vcc\n"
        "v_lshlrev_b64 v[148:149], v10, v[148:149]\n"
        "v_mov_b32 v11, 14702\n"
        "v_mov_b32 v151, 0\n"
        "v_cndmask_b32 v150, 0, v11, vcc\n"
        "v_lshlrev_b64 v[150:151], v10, v[150:151]\n"
        "v_and_b32 v237, 1, v1\n"
        "v_mul_u32_u24 v237, 0xaaaa, v237\n"
        "v_mov_b64 v[170:171], v[188:189]\n"
        "v_mov_b64 v[210:211], v[188:189]\n"
        "v_mov_b64 v[228:229], v[188:189]\n"
        "v_add_u32 v237, 0x4444, v237\n"
        "v_mov_b32 v11, 0x3f803f80\n"
        "v_accvgpr_write_b32 a252, v11\n"
        "v_accvgpr_write_b32 a253, v11\n"
        "v_accvgpr_write_b32 a254, v11\n"
        "v_accvgpr_write_b32 a255, v11\n"
        "v_lshl_or_b32 v10, v16, 10, v18\n"
        "s_lshl_b32 s31, s42, 12\n"
        "s_add_u32 s31, s31, 73728\n"
        "s_lshl_b32 s35, s51, 9\n"
        "s_add_u32 s35, s35, 73728\n"
        "v_add_u32 v245, s31, v10\n"
        "v_add_u32 v247, s35, v10\n"
        "v_lshrrev_b32 v10, 5, v1\n"
        "v_lshrrev_b32 v11, 1, v19\n"
        "s_sub_u32 s31, s48, 65536\n"
        "v_lshl_or_b32 v10, v10, 10, v18\n"
        "v_add3_u32 v246, v10, v11, s31\n"
        "v_mul_u32_u24 v10, 768, v16\n"
        "v_xor_b32 v11, v18, v19\n"
        "v_add3_u32 v235, v10, v11, s65\n"
        "s_lshr_b32 s31, s42, 1\n"
        "s_mul_i32 s35, s31, 36864\n"
        "s_movk_i32 s45, 17664\n"
        "v_mul_u32_u24 v11, 96, v15\n"
        "v_lshrrev_b32 v248, 3, v15\n"
        "v_add3_u32 v10, v10, v11, s35\n"
        "v_mad_u32_u24 v10, v248, s45, v10\n"
        "v_add_u32 v248, v10, v19\n"
        "v_sub_u32 v254, v10, v19\n"
        "s_mul_i32 s31, s31, 192\n"
        "v_mul_u32_u24 v10, 196608, v16\n"
        "v_mul_u32_u24 v11, 12, v15\n"
        "v_add3_u32 v250, v10, v11, s31\n"
        "s_movk_i32 s31, 288\n"
        "s_movk_i32 s35, 3072\n"
        "s_movk_i32 s41, 1536\n"
        "s_movk_i32 s45, 12288\n"
        "v_mad_u32_u24 v10, v4, s31, v3\n"
        "v_lshrrev_b32 v11, 4, v4\n"
        "v_mad_i32_i24 v238, v12, s41, v10\n"
        "v_sub_u32 v7, v7, v11\n"
        "v_mad_u32_u24 v238, v11, s45, v238\n"
        "v_mad_i32_i24 v232, v7, s35, v10\n"
        "v_mad_u32_u24 v10, v5, s31, v3\n"
        "v_lshrrev_b32 v11, 4, v5\n"
        "v_mad_i32_i24 v239, v13, s41, v10\n"
        "v_sub_u32 v8, v8, v11\n"
        "v_mad_u32_u24 v239, v11, s45, v239\n"
        "v_mad_i32_i24 v233, v8, s35, v10\n"
        "v_mad_u32_u24 v10, v6, s31, v3\n"
        "v_lshrrev_b32 v11, 4, v6\n"
        "v_mad_i32_i24 v236, v14, s41, v10\n"
        "v_sub_u32 v9, v9, v11\n"
        "v_mad_u32_u24 v236, v11, s45, v236\n"
        "v_mad_i32_i24 v234, v9, s35, v10\n"
        "s_mov_b32 s26, 0x3fb8aa3b\n"
        "s_mov_b32 s24, 0x5d800000\n"
        "s_mov_b32 s27, 14\n"
        "s_mov_b32 s28, 98304\n"
        "s_mov_b32 s29, 73728\n"
        "s_mov_b32 s30, 122880\n"
        "s_mov_b32 s45, 0\n"
        "s_mov_b32 s25, 0\n"
        "s_mov_b32 s54, 73728\n"
        "s_mov_b32 s55, 0x20000\n"
        "s_mov_b32 s18, 100663296\n"
        "s_mov_b32 s19, 0x20000\n"
        "s_mov_b32 s58, 16384\n"
        "s_mov_b32 s59, 0x20000\n"
        "s_mov_b32 s62, 1800\n"
        "s_mov_b32 s63, 0x20000\n"
        "s_add_u32 m0, s65, 6144\n"
        "s_mov_b32 s64, 73728\n"
        "buffer_load_dwordx4 v128, s[20:23], s64 offen lds\n"
        "buffer_load_dwordx4 v129, s[20:23], s64 offen offset:1024 lds\n"
        "buffer_load_dwordx4 v130, s[20:23], s64 offen offset:2048 lds\n"
        "s_waitcnt vmcnt(8)\n"
        "ds_read_b64_tr_b16 v[192:193], v255 offset:0\n"
        "ds_read_b64_tr_b16 v[194:195], v255 offset:0\n"
        "ds_read_b64_tr_b16 v[212:213], v255 offset:768\n"
        "ds_read_b64_tr_b16 v[214:215], v255 offset:768\n"
        "ds_read_b64_tr_b16 v[216:217], v255 offset:32\n"
        "ds_read_b64_tr_b16 v[218:219], v255 offset:32\n"
        "ds_read_b64_tr_b16 v[156:157], v255 offset:800\n"
        "ds_read_b64_tr_b16 v[158:159], v255 offset:800\n"
        "ds_read_b64_tr_b16 v[176:177], v255 offset:64\n"
        "ds_read_b64_tr_b16 v[178:179], v255 offset:64\n"
        "ds_read_b64_tr_b16 v[196:197], v255 offset:832\n"
        "ds_read_b64_tr_b16 v[198:199], v255 offset:832\n"
        "s_waitcnt lgkmcnt(10)\n"
        "v_mfma_f32_16x16x32_bf16 v[96:99], v[148:151], v[192:195], 0\n"
        "s_waitcnt lgkmcnt(8)\n"
        "s_add_u32 m0, s65, 9216\n"
        "s_mov_b32 s64, 110592\n"
        "buffer_load_dwordx4 v128, s[20:23], s64 offen lds\n"
        "v_mfma_f32_16x16x32_bf16 v[100:103], v[148:151], v[212:215], 0\n"
        "s_waitcnt lgkmcnt(6)\n"
        "buffer_load_dwordx4 v129, s[20:23], s64 offen offset:1024 lds\n"
        "v_mfma_f32_16x16x32_bf16 v[104:107], v[148:151], v[216:219], 0\n"
        "s_waitcnt lgkmcnt(4)\n"
        "buffer_load_dwordx4 v130, s[20:23], s64 offen offset:2048 lds\n"
        "s_add_u32 m0, s65, 12288\n"
        "s_waitcnt vmcnt(8)\n"
        "ds_read_b64_tr_b16 v[160:161], v255 offset:3072\n"
        "ds_read_b64_tr_b16 v[162:163], v255 offset:3072\n"
        "ds_read_b64_tr_b16 v[164:165], v255 offset:3840\n"
        "ds_read_b64_tr_b16 v[166:167], v255 offset:3840\n"
        "v_mfma_f32_16x16x32_bf16 v[108:111], v[148:151], v[156:159], 0\n"
        "s_waitcnt lgkmcnt(6)\n"
        "s_mov_b32 s64, 147456\n"
        "ds_read_b64_tr_b16 v[180:181], v255 offset:3104\n"
        "ds_read_b64_tr_b16 v[182:183], v255 offset:3104\n"
        "ds_read_b64_tr_b16 v[184:185], v255 offset:3872\n"
        "ds_read_b64_tr_b16 v[186:187], v255 offset:3872\n"
        "v_cvt_pk_bf16_f32 v96, v96, v97\n"
        "v_cvt_pk_bf16_f32 v98, v98, v99\n"
        "v_accvgpr_write_b32 a0, v96\n"
        "v_accvgpr_write_b32 a1, v98\n"
        "v_mfma_f32_16x16x32_bf16 v[96:99], v[148:151], v[176:179], 0\n"
        "s_waitcnt lgkmcnt(8)\n"
        "buffer_load_dwordx4 v128, s[20:23], s64 offen lds\n"
        "ds_read_b64_tr_b16 v[200:201], v255 offset:3136\n"
        "ds_read_b64_tr_b16 v[202:203], v255 offset:3136\n"
        "ds_read_b64_tr_b16 v[204:205], v255 offset:3904\n"
        "ds_read_b64_tr_b16 v[206:207], v255 offset:3904\n"
        "v_cvt_pk_bf16_f32 v100, v100, v101\n"
        "v_cvt_pk_bf16_f32 v102, v102, v103\n"
        "v_accvgpr_write_b32 a2, v100\n"
        "v_accvgpr_write_b32 a3, v102\n"
        "v_mfma_f32_16x16x32_bf16 v[100:103], v[148:151], v[196:199], 0\n"
        "s_waitcnt lgkmcnt(10)\n"
        "buffer_load_dwordx4 v129, s[20:23], s64 offen offset:1024 lds\n"
        "v_cvt_pk_bf16_f32 v104, v104, v105\n"
        "v_cvt_pk_bf16_f32 v106, v106, v107\n"
        "v_accvgpr_write_b32 a24, v104\n"
        "v_accvgpr_write_b32 a25, v106\n"
        "v_mfma_f32_16x16x32_bf16 v[104:107], v[148:151], v[160:163], 0\n"
        "s_waitcnt lgkmcnt(8)\n"
        "v_cvt_pk_bf16_f32 v108, v108, v109\n"
        "v_cvt_pk_bf16_f32 v110, v110, v111\n"
        "v_accvgpr_write_b32 a26, v108\n"
        "v_accvgpr_write_b32 a27, v110\n"
        "v_mfma_f32_16x16x32_bf16 v[108:111], v[148:151], v[164:167], 0\n"
        "s_waitcnt lgkmcnt(6)\n"
        "buffer_load_dwordx4 v130, s[20:23], s64 offen offset:2048 lds\n"
        "s_add_u32 m0, s65, 15360\n"
        "v_cvt_pk_bf16_f32 v96, v96, v97\n"
        "v_cvt_pk_bf16_f32 v98, v98, v99\n"
        "v_accvgpr_write_b32 a48, v96\n"
        "v_accvgpr_write_b32 a49, v98\n"
        "v_mfma_f32_16x16x32_bf16 v[96:99], v[148:151], v[180:183], 0\n"
        "s_waitcnt lgkmcnt(4)\n"
        "s_mov_b32 s64, 184320\n"
        "s_waitcnt vmcnt(6)\n"
        "ds_read_b64_tr_b16 v[192:193], v255 offset:6144\n"
        "ds_read_b64_tr_b16 v[194:195], v255 offset:6144\n"
        "ds_read_b64_tr_b16 v[212:213], v255 offset:6912\n"
        "ds_read_b64_tr_b16 v[214:215], v255 offset:6912\n"
        "v_cvt_pk_bf16_f32 v100, v100, v101\n"
        "v_cvt_pk_bf16_f32 v102, v102, v103\n"
        "v_accvgpr_write_b32 a50, v100\n"
        "v_accvgpr_write_b32 a51, v102\n"
        "v_mfma_f32_16x16x32_bf16 v[100:103], v[148:151], v[184:187], 0\n"
        "s_waitcnt lgkmcnt(6)\n"
        "buffer_load_dwordx4 v128, s[20:23], s64 offen lds\n"
        "ds_read_b64_tr_b16 v[216:217], v255 offset:6176\n"
        "ds_read_b64_tr_b16 v[218:219], v255 offset:6176\n"
        "ds_read_b64_tr_b16 v[156:157], v255 offset:6944\n"
        "ds_read_b64_tr_b16 v[158:159], v255 offset:6944\n"
        "v_cvt_pk_bf16_f32 v104, v104, v105\n"
        "v_cvt_pk_bf16_f32 v106, v106, v107\n"
        "v_accvgpr_write_b32 a4, v104\n"
        "v_accvgpr_write_b32 a5, v106\n"
        "v_mfma_f32_16x16x32_bf16 v[104:107], v[148:151], v[200:203], 0\n"
        "s_waitcnt lgkmcnt(8)\n"
        "buffer_load_dwordx4 v129, s[20:23], s64 offen offset:1024 lds\n"
        "ds_read_b64_tr_b16 v[176:177], v255 offset:6208\n"
        "ds_read_b64_tr_b16 v[178:179], v255 offset:6208\n"
        "ds_read_b64_tr_b16 v[196:197], v255 offset:6976\n"
        "ds_read_b64_tr_b16 v[198:199], v255 offset:6976\n"
        "v_cvt_pk_bf16_f32 v108, v108, v109\n"
        "v_cvt_pk_bf16_f32 v110, v110, v111\n"
        "v_accvgpr_write_b32 a6, v108\n"
        "v_accvgpr_write_b32 a7, v110\n"
        "v_mfma_f32_16x16x32_bf16 v[108:111], v[148:151], v[204:207], 0\n"
        "s_waitcnt lgkmcnt(10)\n"
        "buffer_load_dwordx4 v130, s[20:23], s64 offen offset:2048 lds\n"
        "v_cvt_pk_bf16_f32 v96, v96, v97\n"
        "v_cvt_pk_bf16_f32 v98, v98, v99\n"
        "v_accvgpr_write_b32 a28, v96\n"
        "v_accvgpr_write_b32 a29, v98\n"
        "v_mfma_f32_16x16x32_bf16 v[96:99], v[148:151], v[192:195], 0\n"
        "s_waitcnt lgkmcnt(8)\n"
        "v_cvt_pk_bf16_f32 v100, v100, v101\n"
        "v_cvt_pk_bf16_f32 v102, v102, v103\n"
        "v_accvgpr_write_b32 a30, v100\n"
        "v_accvgpr_write_b32 a31, v102\n"
        "v_mfma_f32_16x16x32_bf16 v[100:103], v[148:151], v[212:215], 0\n"
        "s_waitcnt lgkmcnt(6)\n"
        "v_cvt_pk_bf16_f32 v104, v104, v105\n"
        "v_cvt_pk_bf16_f32 v106, v106, v107\n"
        "v_accvgpr_write_b32 a52, v104\n"
        "v_accvgpr_write_b32 a53, v106\n"
        "v_mfma_f32_16x16x32_bf16 v[104:107], v[148:151], v[216:219], 0\n"
        "s_waitcnt lgkmcnt(4)\n"
        "buffer_load_dwordx2 v[144:145], v134, s[60:63], 0 offen\n"
        "s_add_u32 m0, s46, 122880\n"
        "s_mov_b32 s64, s46\n"
        "s_waitcnt vmcnt(7)\n"
        "ds_read_b64_tr_b16 v[160:161], v255 offset:9216\n"
        "ds_read_b64_tr_b16 v[162:163], v255 offset:9216\n"
        "ds_read_b64_tr_b16 v[164:165], v255 offset:9984\n"
        "ds_read_b64_tr_b16 v[166:167], v255 offset:9984\n"
        "v_cvt_pk_bf16_f32 v108, v108, v109\n"
        "v_cvt_pk_bf16_f32 v110, v110, v111\n"
        "v_accvgpr_write_b32 a54, v108\n"
        "v_accvgpr_write_b32 a55, v110\n"
        "v_mfma_f32_16x16x32_bf16 v[108:111], v[148:151], v[156:159], 0\n"
        "s_waitcnt lgkmcnt(6)\n"
        "buffer_load_dwordx4 v131, s[56:59], s64 offen lds\n"
        "ds_read_b64_tr_b16 v[180:181], v255 offset:9248\n"
        "ds_read_b64_tr_b16 v[182:183], v255 offset:9248\n"
        "ds_read_b64_tr_b16 v[184:185], v255 offset:10016\n"
        "ds_read_b64_tr_b16 v[186:187], v255 offset:10016\n"
        "v_cvt_pk_bf16_f32 v96, v96, v97\n"
        "v_cvt_pk_bf16_f32 v98, v98, v99\n"
        "v_accvgpr_write_b32 a8, v96\n"
        "v_accvgpr_write_b32 a9, v98\n"
        "v_mfma_f32_16x16x32_bf16 v[96:99], v[148:151], v[176:179], 0\n"
        "s_waitcnt lgkmcnt(8)\n"
        "buffer_load_dwordx4 v131, s[56:59], s64 offen offset:1024 lds\n"
        "ds_read_b64_tr_b16 v[200:201], v255 offset:9280\n"
        "ds_read_b64_tr_b16 v[202:203], v255 offset:9280\n"
        "ds_read_b64_tr_b16 v[204:205], v255 offset:10048\n"
        "ds_read_b64_tr_b16 v[206:207], v255 offset:10048\n"
        "v_cvt_pk_bf16_f32 v100, v100, v101\n"
        "v_cvt_pk_bf16_f32 v102, v102, v103\n"
        "v_accvgpr_write_b32 a10, v100\n"
        "v_accvgpr_write_b32 a11, v102\n"
        "v_mfma_f32_16x16x32_bf16 v[100:103], v[148:151], v[196:199], 0\n"
        "s_waitcnt lgkmcnt(10)\n"
        "v_cvt_pk_bf16_f32 v104, v104, v105\n"
        "v_cvt_pk_bf16_f32 v106, v106, v107\n"
        "v_accvgpr_write_b32 a32, v104\n"
        "v_accvgpr_write_b32 a33, v106\n"
        "v_mfma_f32_16x16x32_bf16 v[104:107], v[148:151], v[160:163], 0\n"
        "s_waitcnt lgkmcnt(8)\n"
        "buffer_load_dwordx4 v132, s[56:59], s64 offen offset:2048 lds\n"
        "v_cvt_pk_bf16_f32 v108, v108, v109\n"
        "v_cvt_pk_bf16_f32 v110, v110, v111\n"
        "v_accvgpr_write_b32 a34, v108\n"
        "v_accvgpr_write_b32 a35, v110\n"
        "v_mfma_f32_16x16x32_bf16 v[108:111], v[148:151], v[164:167], 0\n"
        "s_waitcnt lgkmcnt(6)\n"
        "buffer_load_dwordx4 v132, s[56:59], s64 offen offset:3072 lds\n"
        "s_add_u32 m0, s40, 73728\n"
        "v_cvt_pk_bf16_f32 v96, v96, v97\n"
        "v_cvt_pk_bf16_f32 v98, v98, v99\n"
        "v_accvgpr_write_b32 a56, v96\n"
        "v_accvgpr_write_b32 a57, v98\n"
        "v_mfma_f32_16x16x32_bf16 v[96:99], v[148:151], v[180:183], 0\n"
        "s_waitcnt lgkmcnt(4)\n"
        "s_sub_u32 s64, s47, 12582912\n"
        "s_waitcnt vmcnt(8)\n"
        "ds_read_b64_tr_b16 v[192:193], v255 offset:12288\n"
        "ds_read_b64_tr_b16 v[194:195], v255 offset:12288\n"
        "ds_read_b64_tr_b16 v[212:213], v255 offset:13056\n"
        "ds_read_b64_tr_b16 v[214:215], v255 offset:13056\n"
        "v_cvt_pk_bf16_f32 v100, v100, v101\n"
        "v_cvt_pk_bf16_f32 v102, v102, v103\n"
        "v_accvgpr_write_b32 a58, v100\n"
        "v_accvgpr_write_b32 a59, v102\n"
        "v_mfma_f32_16x16x32_bf16 v[100:103], v[148:151], v[184:187], 0\n"
        "s_waitcnt lgkmcnt(6)\n"
        "buffer_load_dwordx4 v242, s[8:11], s64 offen lds\n"
        "s_add_u32 m0, s40, 74752\n"
        "ds_read_b64_tr_b16 v[216:217], v255 offset:12320\n"
        "ds_read_b64_tr_b16 v[218:219], v255 offset:12320\n"
        "ds_read_b64_tr_b16 v[156:157], v255 offset:13088\n"
        "ds_read_b64_tr_b16 v[158:159], v255 offset:13088\n"
        "v_cvt_pk_bf16_f32 v104, v104, v105\n"
        "v_cvt_pk_bf16_f32 v106, v106, v107\n"
        "v_accvgpr_write_b32 a12, v104\n"
        "v_accvgpr_write_b32 a13, v106\n"
        "v_mfma_f32_16x16x32_bf16 v[104:107], v[148:151], v[200:203], 0\n"
        "s_waitcnt lgkmcnt(8)\n"
        "ds_read_b64_tr_b16 v[176:177], v255 offset:12352\n"
        "ds_read_b64_tr_b16 v[178:179], v255 offset:12352\n"
        "ds_read_b64_tr_b16 v[196:197], v255 offset:13120\n"
        "ds_read_b64_tr_b16 v[198:199], v255 offset:13120\n"
        "v_cvt_pk_bf16_f32 v108, v108, v109\n"
        "v_cvt_pk_bf16_f32 v110, v110, v111\n"
        "v_accvgpr_write_b32 a14, v108\n"
        "v_accvgpr_write_b32 a15, v110\n"
        "v_mfma_f32_16x16x32_bf16 v[108:111], v[148:151], v[204:207], 0\n"
        "s_waitcnt lgkmcnt(10)\n"
        "buffer_load_dwordx4 v243, s[8:11], s64 offen lds\n"
        "s_add_u32 m0, s40, 75776\n"
        "v_cvt_pk_bf16_f32 v96, v96, v97\n"
        "v_cvt_pk_bf16_f32 v98, v98, v99\n"
        "v_accvgpr_write_b32 a36, v96\n"
        "v_accvgpr_write_b32 a37, v98\n"
        "v_mfma_f32_16x16x32_bf16 v[96:99], v[148:151], v[192:195], 0\n"
        "s_waitcnt lgkmcnt(8)\n"
        "v_cvt_pk_bf16_f32 v100, v100, v101\n"
        "v_cvt_pk_bf16_f32 v102, v102, v103\n"
        "v_accvgpr_write_b32 a38, v100\n"
        "v_accvgpr_write_b32 a39, v102\n"
        "v_mfma_f32_16x16x32_bf16 v[100:103], v[148:151], v[212:215], 0\n"
        "s_waitcnt lgkmcnt(6)\n"
        "buffer_load_dwordx4 v244, s[8:11], s64 offen lds\n"
        "s_add_u32 m0, s40, 86016\n"
        "v_cvt_pk_bf16_f32 v104, v104, v105\n"
        "v_cvt_pk_bf16_f32 v106, v106, v107\n"
        "v_accvgpr_write_b32 a60, v104\n"
        "v_accvgpr_write_b32 a61, v106\n"
        "v_mfma_f32_16x16x32_bf16 v[104:107], v[148:151], v[216:219], 0\n"
        "s_waitcnt lgkmcnt(4)\n"
        "s_sub_u32 s64, s47, 11796480\n"
        "s_waitcnt vmcnt(8)\n"
        "ds_read_b64_tr_b16 v[160:161], v255 offset:15360\n"
        "ds_read_b64_tr_b16 v[162:163], v255 offset:15360\n"
        "ds_read_b64_tr_b16 v[164:165], v255 offset:16128\n"
        "ds_read_b64_tr_b16 v[166:167], v255 offset:16128\n"
        "v_cvt_pk_bf16_f32 v108, v108, v109\n"
        "v_cvt_pk_bf16_f32 v110, v110, v111\n"
        "v_accvgpr_write_b32 a62, v108\n"
        "v_accvgpr_write_b32 a63, v110\n"
        "v_mfma_f32_16x16x32_bf16 v[108:111], v[148:151], v[156:159], 0\n"
        "s_waitcnt lgkmcnt(6)\n"
        "buffer_load_dwordx4 v242, s[8:11], s64 offen lds\n"
        "s_add_u32 m0, s40, 87040\n"
        "ds_read_b64_tr_b16 v[180:181], v255 offset:15392\n"
        "ds_read_b64_tr_b16 v[182:183], v255 offset:15392\n"
        "ds_read_b64_tr_b16 v[184:185], v255 offset:16160\n"
        "ds_read_b64_tr_b16 v[186:187], v255 offset:16160\n"
        "v_cvt_pk_bf16_f32 v96, v96, v97\n"
        "v_cvt_pk_bf16_f32 v98, v98, v99\n"
        "v_accvgpr_write_b32 a16, v96\n"
        "v_accvgpr_write_b32 a17, v98\n"
        "v_mfma_f32_16x16x32_bf16 v[96:99], v[148:151], v[176:179], 0\n"
        "s_waitcnt lgkmcnt(8)\n"
        "ds_read_b64_tr_b16 v[200:201], v255 offset:15424\n"
        "ds_read_b64_tr_b16 v[202:203], v255 offset:15424\n"
        "ds_read_b64_tr_b16 v[204:205], v255 offset:16192\n"
        "ds_read_b64_tr_b16 v[206:207], v255 offset:16192\n"
        "v_cvt_pk_bf16_f32 v100, v100, v101\n"
        "v_cvt_pk_bf16_f32 v102, v102, v103\n"
        "v_accvgpr_write_b32 a18, v100\n"
        "v_accvgpr_write_b32 a19, v102\n"
        "v_mfma_f32_16x16x32_bf16 v[100:103], v[148:151], v[196:199], 0\n"
        "s_waitcnt lgkmcnt(10)\n"
        "buffer_load_dwordx4 v243, s[8:11], s64 offen lds\n"
        "s_add_u32 m0, s40, 88064\n"
        "v_cvt_pk_bf16_f32 v104, v104, v105\n"
        "v_cvt_pk_bf16_f32 v106, v106, v107\n"
        "v_accvgpr_write_b32 a40, v104\n"
        "v_accvgpr_write_b32 a41, v106\n"
        "v_mfma_f32_16x16x32_bf16 v[104:107], v[148:151], v[160:163], 0\n"
        "s_waitcnt lgkmcnt(8)\n"
        "v_cvt_pk_bf16_f32 v108, v108, v109\n"
        "v_cvt_pk_bf16_f32 v110, v110, v111\n"
        "v_accvgpr_write_b32 a42, v108\n"
        "v_accvgpr_write_b32 a43, v110\n"
        "v_mfma_f32_16x16x32_bf16 v[108:111], v[148:151], v[164:167], 0\n"
        "s_waitcnt lgkmcnt(6)\n"
        "buffer_load_dwordx4 v244, s[8:11], s64 offen lds\n"
        "v_cvt_pk_bf16_f32 v96, v96, v97\n"
        "v_cvt_pk_bf16_f32 v98, v98, v99\n"
        "v_accvgpr_write_b32 a64, v96\n"
        "v_accvgpr_write_b32 a65, v98\n"
        "v_mfma_f32_16x16x32_bf16 v[96:99], v[148:151], v[180:183], 0\n"
        "s_waitcnt lgkmcnt(4)\n"
        "s_waitcnt vmcnt(10)\n"
        "v_and_b32 v146, 0xffff0000, v144\n"
        "v_and_b32 v147, 0xffff0000, v145\n"
        "v_lshlrev_b32 v144, 16, v144\n"
        "v_lshlrev_b32 v145, 16, v145\n"
        "v_cvt_pk_bf16_f32 v100, v100, v101\n"
        "v_cvt_pk_bf16_f32 v102, v102, v103\n"
        "v_accvgpr_write_b32 a66, v100\n"
        "v_accvgpr_write_b32 a67, v102\n"
        "v_mfma_f32_16x16x32_bf16 v[100:103], v[148:151], v[184:187], 0\n"
        "s_waitcnt lgkmcnt(2)\n"
        "v_mul_f32 v146, s26, v146\n"
        "v_mul_f32 v147, s26, v147\n"
        "v_mul_f32 v144, s26, v144\n"
        "v_mul_f32 v145, s26, v145\n"
        "v_cvt_pk_bf16_f32 v104, v104, v105\n"
        "v_cvt_pk_bf16_f32 v106, v106, v107\n"
        "v_accvgpr_write_b32 a20, v104\n"
        "v_accvgpr_write_b32 a21, v106\n"
        "v_mfma_f32_16x16x32_bf16 v[104:107], v[148:151], v[200:203], 0\n"
        "s_waitcnt lgkmcnt(0)\n"
        "ds_write2st64_b32 v135, v144, v146 offset0:0 offset1:4\n"
        "v_cvt_pk_bf16_f32 v108, v108, v109\n"
        "v_cvt_pk_bf16_f32 v110, v110, v111\n"
        "v_accvgpr_write_b32 a22, v108\n"
        "v_accvgpr_write_b32 a23, v110\n"
        "v_mfma_f32_16x16x32_bf16 v[108:111], v[148:151], v[204:207], 0\n"
        "v_cvt_pk_bf16_f32 v96, v96, v97\n"
        "v_cvt_pk_bf16_f32 v98, v98, v99\n"
        "ds_write2st64_b32 v135, v145, v147 offset0:8 offset1:12\n"
        "s_waitcnt lgkmcnt(0)\n"
        "s_waitcnt vmcnt(6)\n"
        "s_barrier\n"
        "ds_read_b128 v[0:3], v140 offset:0\n"
        "ds_read_b128 v[4:7], v141 offset:0\n"
        "ds_read_b128 v[16:19], v140 offset:1024\n"
        "ds_read_b128 v[20:23], v141 offset:1024\n"
        "ds_read_b128 v[32:35], v140 offset:8192\n"
        "ds_read_b128 v[36:39], v141 offset:8192\n"
        "ds_read_b128 v[48:51], v140 offset:9216\n"
        "ds_read_b128 v[52:55], v141 offset:9216\n"
        "ds_read_b128 v[8:11], v142 offset:0\n"
        "ds_read_b128 v[12:15], v143 offset:0\n"
        "ds_read_b128 v[24:27], v142 offset:1024\n"
        "ds_read_b128 v[28:31], v143 offset:1024\n"
        "ds_read_b128 v[40:43], v142 offset:8192\n"
        "ds_read_b128 v[44:47], v143 offset:8192\n"
        "ds_read_b128 v[56:59], v142 offset:9216\n"
        "ds_read_b128 v[60:63], v143 offset:9216\n"
        "v_cvt_pk_bf16_f32 v100, v100, v101\n"
        "v_cvt_pk_bf16_f32 v102, v102, v103\n"
        "v_cvt_pk_bf16_f32 v104, v104, v105\n"
        "v_cvt_pk_bf16_f32 v106, v106, v107\n"
        "v_cvt_pk_bf16_f32 v108, v108, v109\n"
        "v_cvt_pk_bf16_f32 v110, v110, v111\n"
        "v_accvgpr_write_b32 a44, v96\n"
        "v_accvgpr_write_b32 a45, v98\n"
        "v_accvgpr_write_b32 a46, v100\n"
        "v_accvgpr_write_b32 a47, v102\n"
        "v_accvgpr_write_b32 a68, v104\n"
        "v_accvgpr_write_b32 a69, v106\n"
        "v_accvgpr_write_b32 a70, v108\n"
        "v_accvgpr_write_b32 a71, v110\n"
        "s_waitcnt lgkmcnt(15)\n"
        "v_lshl_add_u32 v0, v0, 2, s67\n"
        "v_lshl_add_u32 v1, v1, 2, s67\n"
        "v_lshl_add_u32 v2, v2, 2, s67\n"
        "v_lshl_add_u32 v3, v3, 2, s67\n"
        "s_waitcnt lgkmcnt(14)\n"
        "v_lshl_add_u32 v4, v4, 2, s67\n"
        "v_lshl_add_u32 v5, v5, 2, s67\n"
        "v_lshl_add_u32 v6, v6, 2, s67\n"
        "v_lshl_add_u32 v7, v7, 2, s67\n"
        "s_waitcnt lgkmcnt(13)\n"
        "v_lshl_add_u32 v16, v16, 2, s67\n"
        "v_lshl_add_u32 v17, v17, 2, s67\n"
        "v_lshl_add_u32 v18, v18, 2, s67\n"
        "v_lshl_add_u32 v19, v19, 2, s67\n"
        "ds_read_b32 v0, v0\n"
        "ds_read_b32 v1, v1\n"
        "ds_read_b32 v2, v2\n"
        "ds_read_b32 v3, v3\n"
        "ds_read_b32 v4, v4\n"
        "ds_read_b32 v5, v5\n"
        "ds_read_b32 v6, v6\n"
        "ds_read_b32 v7, v7\n"
        "ds_read_b32 v16, v16\n"
        "ds_read_b32 v17, v17\n"
        "ds_read_b32 v18, v18\n"
        "ds_read_b32 v19, v19\n"
        "s_add_u32 m0, s65, 0\n"
        "s_mov_b32 s64, 384\n"
        "buffer_load_dwordx4 v128, s[20:23], s64 offen lds\n"
        "s_waitcnt lgkmcnt(15)\n"
        "v_lshl_add_u32 v20, v20, 2, s67\n"
        "v_lshl_add_u32 v21, v21, 2, s67\n"
        "v_lshl_add_u32 v22, v22, 2, s67\n"
        "v_lshl_add_u32 v23, v23, 2, s67\n"
        "v_lshl_add_u32 v32, v32, 2, s67\n"
        "v_lshl_add_u32 v33, v33, 2, s67\n"
        "v_lshl_add_u32 v34, v34, 2, s67\n"
        "v_lshl_add_u32 v35, v35, 2, s67\n"
        "v_lshl_add_u32 v36, v36, 2, s67\n"
        "v_lshl_add_u32 v37, v37, 2, s67\n"
        "v_lshl_add_u32 v38, v38, 2, s67\n"
        "v_lshl_add_u32 v39, v39, 2, s67\n"
        "ds_read_b32 v20, v20\n"
        "ds_read_b32 v21, v21\n"
        "ds_read_b32 v22, v22\n"
        "ds_read_b32 v23, v23\n"
        "ds_read_b32 v32, v32\n"
        "ds_read_b32 v33, v33\n"
        "ds_read_b32 v34, v34\n"
        "ds_read_b32 v35, v35\n"
        "ds_read_b32 v36, v36\n"
        "ds_read_b32 v37, v37\n"
        "ds_read_b32 v38, v38\n"
        "ds_read_b32 v39, v39\n"
        "buffer_load_dwordx4 v129, s[20:23], s64 offen offset:1024 lds\n"
        "v_lshl_add_u32 v48, v48, 2, s67\n"
        "v_lshl_add_u32 v49, v49, 2, s67\n"
        "v_lshl_add_u32 v50, v50, 2, s67\n"
        "v_lshl_add_u32 v51, v51, 2, s67\n"
        "v_lshl_add_u32 v52, v52, 2, s67\n"
        "v_lshl_add_u32 v53, v53, 2, s67\n"
        "v_lshl_add_u32 v54, v54, 2, s67\n"
        "v_lshl_add_u32 v55, v55, 2, s67\n"
        "v_lshl_add_u32 v8, v8, 2, s67\n"
        "v_lshl_add_u32 v9, v9, 2, s67\n"
        "v_lshl_add_u32 v10, v10, 2, s67\n"
        "v_lshl_add_u32 v11, v11, 2, s67\n"
        "ds_read_b32 v48, v48\n"
        "ds_read_b32 v49, v49\n"
        "ds_read_b32 v50, v50\n"
        "ds_read_b32 v51, v51\n"
        "ds_read_b32 v52, v52\n"
        "ds_read_b32 v53, v53\n"
        "ds_read_b32 v54, v54\n"
        "ds_read_b32 v55, v55\n"
        "ds_read_b32 v8, v8\n"
        "ds_read_b32 v9, v9\n"
        "ds_read_b32 v10, v10\n"
        "ds_read_b32 v11, v11\n"
        "buffer_load_dwordx4 v130, s[20:23], s64 offen offset:2048 lds\n"
        "v_lshl_add_u32 v12, v12, 2, s67\n"
        "v_lshl_add_u32 v13, v13, 2, s67\n"
        "v_lshl_add_u32 v14, v14, 2, s67\n"
        "v_lshl_add_u32 v15, v15, 2, s67\n"
        "v_lshl_add_u32 v24, v24, 2, s67\n"
        "v_lshl_add_u32 v25, v25, 2, s67\n"
        "v_lshl_add_u32 v26, v26, 2, s67\n"
        "v_lshl_add_u32 v27, v27, 2, s67\n"
        "v_lshl_add_u32 v28, v28, 2, s67\n"
        "v_lshl_add_u32 v29, v29, 2, s67\n"
        "v_lshl_add_u32 v30, v30, 2, s67\n"
        "v_lshl_add_u32 v31, v31, 2, s67\n"
        "ds_read_b32 v12, v12\n"
        "ds_read_b32 v13, v13\n"
        "ds_read_b32 v14, v14\n"
        "ds_read_b32 v15, v15\n"
        "ds_read_b32 v24, v24\n"
        "ds_read_b32 v25, v25\n"
        "ds_read_b32 v26, v26\n"
        "ds_read_b32 v27, v27\n"
        "ds_read_b32 v28, v28\n"
        "ds_read_b32 v29, v29\n"
        "ds_read_b32 v30, v30\n"
        "ds_read_b32 v31, v31\n"
        "v_lshl_add_u32 v40, v40, 2, s67\n"
        "v_lshl_add_u32 v41, v41, 2, s67\n"
        "v_lshl_add_u32 v42, v42, 2, s67\n"
        "v_lshl_add_u32 v43, v43, 2, s67\n"
        "s_waitcnt lgkmcnt(15)\n"
        "v_lshl_add_u32 v44, v44, 2, s67\n"
        "v_lshl_add_u32 v45, v45, 2, s67\n"
        "v_lshl_add_u32 v46, v46, 2, s67\n"
        "v_lshl_add_u32 v47, v47, 2, s67\n"
        "v_lshl_add_u32 v56, v56, 2, s67\n"
        "v_lshl_add_u32 v57, v57, 2, s67\n"
        "v_lshl_add_u32 v58, v58, 2, s67\n"
        "v_lshl_add_u32 v59, v59, 2, s67\n"
        "ds_read_b32 v40, v40\n"
        "ds_read_b32 v41, v41\n"
        "ds_read_b32 v42, v42\n"
        "ds_read_b32 v43, v43\n"
        "ds_read_b32 v44, v44\n"
        "ds_read_b32 v45, v45\n"
        "ds_read_b32 v46, v46\n"
        "ds_read_b32 v47, v47\n"
        "ds_read_b32 v56, v56\n"
        "ds_read_b32 v57, v57\n"
        "ds_read_b32 v58, v58\n"
        "ds_read_b32 v59, v59\n"
        "v_lshl_add_u32 v60, v60, 2, s67\n"
        "v_lshl_add_u32 v61, v61, 2, s67\n"
        "v_lshl_add_u32 v62, v62, 2, s67\n"
        "v_lshl_add_u32 v63, v63, 2, s67\n"
        "ds_read_b32 v60, v60\n"
        "ds_read_b32 v61, v61\n"
        "ds_read_b32 v62, v62\n"
        "ds_read_b32 v63, v63\n"
        "s_waitcnt vmcnt(6)\n"
        "s_barrier\n"
        "ds_read_b128 a[216:219], v240 offset:0\n"
        "ds_read_b128 a[220:223], v241 offset:0\n"
        "ds_read_b128 a[224:227], v240 offset:128\n"
        "ds_read_b128 a[228:231], v241 offset:128\n"
        "ds_read_b128 a[232:235], v240 offset:256\n"
        "ds_read_b128 a[236:239], v241 offset:256\n"
        "s_add_u32 m0, s65, 3072\n"
        "s_mov_b32 s64, 37248\n"
        "buffer_load_dwordx4 v128, s[20:23], s64 offen lds\n"
        "buffer_load_dwordx4 v129, s[20:23], s64 offen offset:1024 lds\n"
        "ds_read_b128 a[240:243], v240 offset:1536\n"
        "ds_read_b128 a[244:247], v241 offset:1536\n"
        "buffer_load_dwordx4 v130, s[20:23], s64 offen offset:2048 lds\n"
        "ds_read_b128 a[248:251], v240 offset:1664\n"
        "s_waitcnt lgkmcnt(8)\n"
        "v_mfma_f32_16x16x32_bf16 v[112:115], a[0:3], a[216:219], 0\n"
        "s_waitcnt lgkmcnt(7)\n"
        "v_mfma_f32_16x16x32_bf16 v[112:115], a[4:7], a[220:223], v[112:115]\n"
        "s_waitcnt lgkmcnt(6)\n"
        "v_mfma_f32_16x16x32_bf16 v[112:115], a[8:11], a[224:227], v[112:115]\n"
        "s_waitcnt lgkmcnt(5)\n"
        "v_mfma_f32_16x16x32_bf16 v[112:115], a[12:15], a[228:231], v[112:115]\n"
        "s_waitcnt lgkmcnt(4)\n"
        "v_mfma_f32_16x16x32_bf16 v[112:115], a[16:19], a[232:235], v[112:115]\n"
        "s_waitcnt lgkmcnt(3)\n"
        "v_mfma_f32_16x16x32_bf16 v[112:115], a[20:23], a[236:239], v[112:115]\n"
        "v_mfma_f32_16x16x32_bf16 v[116:119], a[44:47], a[236:239], 0\n"
        "s_nop 2\n"
        "v_mfma_f32_16x16x32_bf16 v[116:119], a[40:43], a[232:235], v[116:119]\n"
        "s_nop 2\n"
        "v_mfma_f32_16x16x32_bf16 v[116:119], a[36:39], a[228:231], v[116:119]\n"
        "v_cvt_pk_bf16_f32 v156, v112, v113\n"
        "v_cvt_pk_bf16_f32 v157, v114, v115\n"
        "v_mfma_f32_16x16x32_bf16 v[116:119], a[32:35], a[224:227], v[116:119]\n"
        "v_mfma_f32_16x16x32_bf16 v[116:119], a[28:31], a[220:223], v[116:119]\n"
        "v_mfma_f32_16x16x32_bf16 v[116:119], a[24:27], a[216:219], v[116:119]\n"
        "v_mfma_f32_16x16x32_bf16 v[120:123], a[48:51], a[216:219], 0\n"
        "ds_read_b128 a[216:219], v241 offset:1664\n"
        "s_nop 0\n"
        "v_mfma_f32_16x16x32_bf16 v[120:123], a[52:55], a[220:223], v[120:123]\n"
        "ds_read_b128 a[220:223], v240 offset:1792\n"
        "s_nop 0\n"
        "v_mfma_f32_16x16x32_bf16 v[120:123], a[56:59], a[224:227], v[120:123]\n"
        "ds_read_b128 a[224:227], v241 offset:1792\n"
        "v_cvt_pk_bf16_f32 v158, v116, v117\n"
        "v_mfma_f32_16x16x32_bf16 v[120:123], a[60:63], a[228:231], v[120:123]\n"
        "v_cvt_pk_bf16_f32 v159, v118, v119\n"
        "v_mfma_f32_16x16x32_bf16 v[120:123], a[64:67], a[232:235], v[120:123]\n"
        "v_mfma_f32_16x16x32_bf16 v[120:123], a[68:71], a[236:239], v[120:123]\n"
        "s_waitcnt lgkmcnt(5)\n"
        "v_mfma_f32_16x16x32_bf16 v[112:115], a[0:3], a[240:243], 0\n"
        "s_waitcnt lgkmcnt(4)\n"
        "s_nop 1\n"
        "v_mfma_f32_16x16x32_bf16 v[112:115], a[4:7], a[244:247], v[112:115]\n"
        "s_waitcnt lgkmcnt(3)\n"
        "s_nop 1\n"
        "v_mfma_f32_16x16x32_bf16 v[112:115], a[8:11], a[248:251], v[112:115]\n"
        "s_waitcnt lgkmcnt(2)\n"
        "s_nop 0\n"
        "v_cvt_pk_bf16_f32 v160, v120, v121\n"
        "v_mfma_f32_16x16x32_bf16 v[112:115], a[12:15], a[216:219], v[112:115]\n"
        "s_waitcnt lgkmcnt(1)\n"
        "v_cvt_pk_bf16_f32 v161, v122, v123\n"
        "v_cvt_pk_bf16_f32 v166, v120, v121\n"
        "v_mfma_f32_16x16x32_bf16 v[112:115], a[16:19], a[220:223], v[112:115]\n"
        "s_waitcnt lgkmcnt(0)\n"
        "s_add_u32 m0, s65, 6144\n"
        "v_cvt_pk_bf16_f32 v167, v122, v123\n"
        "v_mfma_f32_16x16x32_bf16 v[112:115], a[20:23], a[224:227], v[112:115]\n"
        "s_mov_b32 s64, 74112\n"
        "v_mfma_f32_16x16x32_bf16 v[116:119], a[44:47], a[224:227], 0\n"
        "buffer_load_dwordx4 v128, s[20:23], s64 offen lds\n"
        "v_mfma_f32_16x16x32_bf16 v[116:119], a[40:43], a[220:223], v[116:119]\n"
        "s_nop 2\n"
        "v_mfma_f32_16x16x32_bf16 v[116:119], a[36:39], a[216:219], v[116:119]\n"
        "s_waitcnt vmcnt(7)\n"
        "s_barrier\n"
        "ds_read_b128 a[228:231], v240 offset:12288\n"
        "ds_read_b128 a[232:235], v241 offset:12288\n"
        "v_mfma_f32_16x16x32_bf16 v[116:119], a[32:35], a[248:251], v[116:119]\n"
        "buffer_load_dwordx4 v129, s[20:23], s64 offen offset:1024 lds\n"
        "v_mfma_f32_16x16x32_bf16 v[116:119], a[28:31], a[244:247], v[116:119]\n"
        "ds_read_b128 a[236:239], v240 offset:12416\n"
        "v_cvt_pk_bf16_f32 v176, v112, v113\n"
        "v_mfma_f32_16x16x32_bf16 v[116:119], a[24:27], a[240:243], v[116:119]\n"
        "v_cvt_pk_bf16_f32 v177, v114, v115\n"
        "v_mfma_f32_16x16x32_bf16 v[120:123], a[48:51], a[240:243], 0\n"
        "ds_read_b128 a[240:243], v241 offset:12416\n"
        "buffer_load_dwordx4 v130, s[20:23], s64 offen offset:2048 lds\n"
        "v_mfma_f32_16x16x32_bf16 v[120:123], a[52:55], a[244:247], v[120:123]\n"
        "ds_read_b128 a[244:247], v240 offset:12544\n"
        "s_nop 0\n"
        "v_mfma_f32_16x16x32_bf16 v[120:123], a[56:59], a[248:251], v[120:123]\n"
        "ds_read_b128 a[248:251], v241 offset:12544\n"
        "v_cvt_pk_bf16_f32 v178, v116, v117\n"
        "v_mfma_f32_16x16x32_bf16 v[120:123], a[60:63], a[216:219], v[120:123]\n"
        "ds_read_b128 a[216:219], v240 offset:13824\n"
        "v_cvt_pk_bf16_f32 v179, v118, v119\n"
        "v_mfma_f32_16x16x32_bf16 v[120:123], a[64:67], a[220:223], v[120:123]\n"
        "ds_read_b128 a[220:223], v241 offset:13824\n"
        "v_mfma_f32_16x16x32_bf16 v[120:123], a[68:71], a[224:227], v[120:123]\n"
        "s_waitcnt lgkmcnt(7)\n"
        "ds_read_b128 a[224:227], v240 offset:13952\n"
        "v_mfma_f32_16x16x32_bf16 v[112:115], a[0:3], a[228:231], 0\n"
        "s_waitcnt lgkmcnt(7)\n"
        "s_nop 1\n"
        "v_mfma_f32_16x16x32_bf16 v[112:115], a[4:7], a[232:235], v[112:115]\n"
        "s_waitcnt lgkmcnt(6)\n"
        "s_nop 1\n"
        "v_mfma_f32_16x16x32_bf16 v[112:115], a[8:11], a[236:239], v[112:115]\n"
        "s_waitcnt lgkmcnt(5)\n"
        "v_cvt_pk_bf16_f32 v180, v120, v121\n"
        "v_cvt_pk_bf16_f32 v181, v122, v123\n"
        "v_mfma_f32_16x16x32_bf16 v[112:115], a[12:15], a[240:243], v[112:115]\n"
        "s_waitcnt lgkmcnt(4)\n"
        "v_cvt_pk_bf16_f32 v186, v120, v121\n"
        "v_cvt_pk_bf16_f32 v187, v122, v123\n"
        "v_mfma_f32_16x16x32_bf16 v[112:115], a[16:19], a[244:247], v[112:115]\n"
        "s_waitcnt lgkmcnt(3)\n"
        "v_mfma_f32_16x16x32_bf16 v[112:115], a[20:23], a[248:251], v[112:115]\n"
        "v_mfma_f32_16x16x32_bf16 v[116:119], a[44:47], a[248:251], 0\n"
        "s_add_u32 m0, s65, 9216\n"
        "s_nop 1\n"
        "v_mfma_f32_16x16x32_bf16 v[116:119], a[40:43], a[244:247], v[116:119]\n"
        "s_mov_b32 s64, 110976\n"
        "s_nop 1\n"
        "v_mfma_f32_16x16x32_bf16 v[116:119], a[36:39], a[240:243], v[116:119]\n"
        "buffer_load_dwordx4 v128, s[20:23], s64 offen lds\n"
        "v_mfma_f32_16x16x32_bf16 v[116:119], a[32:35], a[236:239], v[116:119]\n"
        "v_cvt_pk_bf16_f32 v196, v112, v113\n"
        "v_cvt_pk_bf16_f32 v197, v114, v115\n"
        "v_mfma_f32_16x16x32_bf16 v[116:119], a[28:31], a[232:235], v[116:119]\n"
        "v_mfma_f32_16x16x32_bf16 v[116:119], a[24:27], a[228:231], v[116:119]\n"
        "buffer_load_dwordx4 v129, s[20:23], s64 offen offset:1024 lds\n"
        "v_mfma_f32_16x16x32_bf16 v[120:123], a[48:51], a[228:231], 0\n"
        "ds_read_b128 a[228:231], v241 offset:13952\n"
        "s_nop 0\n"
        "v_mfma_f32_16x16x32_bf16 v[120:123], a[52:55], a[232:235], v[120:123]\n"
        "ds_read_b128 a[232:235], v240 offset:14080\n"
        "s_nop 0\n"
        "v_mfma_f32_16x16x32_bf16 v[120:123], a[56:59], a[236:239], v[120:123]\n"
        "ds_read_b128 a[236:239], v241 offset:14080\n"
        "buffer_load_dwordx4 v130, s[20:23], s64 offen offset:2048 lds\n"
        "v_mfma_f32_16x16x32_bf16 v[120:123], a[60:63], a[240:243], v[120:123]\n"
        "ds_read_b128 a[240:243], v240 offset:0\n"
        "v_cvt_pk_bf16_f32 v198, v116, v117\n"
        "v_mfma_f32_16x16x32_bf16 v[120:123], a[64:67], a[244:247], v[120:123]\n"
        "ds_read_b128 a[244:247], v241 offset:0\n"
        "v_cvt_pk_bf16_f32 v199, v118, v119\n"
        "v_mfma_f32_16x16x32_bf16 v[120:123], a[68:71], a[248:251], v[120:123]\n"
        "s_waitcnt lgkmcnt(7)\n"
        "ds_read_b128 a[248:251], v240 offset:128\n"
        "v_mfma_f32_16x16x32_bf16 v[112:115], a[0:3], a[216:219], 0\n"
        "s_waitcnt lgkmcnt(7)\n"
        "s_add_u32 m0, s65, 12288\n"
        "s_nop 1\n"
        "v_mfma_f32_16x16x32_bf16 v[112:115], a[4:7], a[220:223], v[112:115]\n"
        "s_waitcnt lgkmcnt(6)\n"
        "s_mov_b32 s64, 147840\n"
        "s_nop 0\n"
        "v_cvt_pk_bf16_f32 v200, v120, v121\n"
        "v_mfma_f32_16x16x32_bf16 v[112:115], a[8:11], a[224:227], v[112:115]\n"
        "s_waitcnt lgkmcnt(5)\n"
        "buffer_load_dwordx4 v128, s[20:23], s64 offen lds\n"
        "s_waitcnt vmcnt(10)\n"
        "ds_read_b64_tr_b16 a[72:73], v255 offset:0\n"
        "ds_read_b64_tr_b16 a[74:75], v255 offset:768\n"
        "v_mfma_f32_16x16x32_bf16 v[112:115], a[12:15], a[228:231], v[112:115]\n"
        "s_waitcnt lgkmcnt(6)\n"
        "ds_read_b64_tr_b16 a[96:97], v255 offset:32\n"
        "ds_read_b64_tr_b16 a[98:99], v255 offset:800\n"
        "v_mfma_f32_16x16x32_bf16 v[112:115], a[16:19], a[232:235], v[112:115]\n"
        "s_waitcnt lgkmcnt(7)\n"
        "ds_read_b64_tr_b16 a[120:121], v255 offset:64\n"
        "ds_read_b64_tr_b16 a[122:123], v255 offset:832\n"
        "v_mfma_f32_16x16x32_bf16 v[112:115], a[20:23], a[236:239], v[112:115]\n"
        "buffer_load_dwordx4 v129, s[20:23], s64 offen offset:1024 lds\n"
        "v_mfma_f32_16x16x32_bf16 v[116:119], a[44:47], a[236:239], 0\n"
        "v_cvt_pk_bf16_f32 v201, v122, v123\n"
        "v_cvt_pk_bf16_f32 v206, v120, v121\n"
        "v_cvt_pk_bf16_f32 v207, v122, v123\n"
        "v_mfma_f32_16x16x32_bf16 v[116:119], a[40:43], a[232:235], v[116:119]\n"
        "s_nop 1\n"
        "v_cvt_pk_bf16_f32 v216, v112, v113\n"
        "v_mfma_f32_16x16x32_bf16 v[116:119], a[36:39], a[228:231], v[116:119]\n"
        "buffer_load_dwordx4 v130, s[20:23], s64 offen offset:2048 lds\n"
        "s_waitcnt vmcnt(9)\n"
        "ds_read_b64_tr_b16 a[76:77], v255 offset:3072\n"
        "ds_read_b64_tr_b16 a[78:79], v255 offset:3840\n"
        "v_mfma_f32_16x16x32_bf16 v[116:119], a[32:35], a[224:227], v[116:119]\n"
        "ds_read_b64_tr_b16 a[100:101], v255 offset:3104\n"
        "ds_read_b64_tr_b16 a[102:103], v255 offset:3872\n"
        "v_mfma_f32_16x16x32_bf16 v[116:119], a[28:31], a[220:223], v[116:119]\n"
        "ds_read_b64_tr_b16 a[124:125], v255 offset:3136\n"
        "ds_read_b64_tr_b16 a[126:127], v255 offset:3904\n"
        "v_mfma_f32_16x16x32_bf16 v[116:119], a[24:27], a[216:219], v[116:119]\n"
        "v_cvt_pk_bf16_f32 v217, v114, v115\n"
        "v_mfma_f32_16x16x32_bf16 v[120:123], a[48:51], a[216:219], 0\n"
        "s_add_u32 m0, s65, 15360\n"
        "ds_read_b128 a[216:219], v240 offset:1536\n"
        "s_nop 0\n"
        "v_mfma_f32_16x16x32_bf16 v[120:123], a[52:55], a[220:223], v[120:123]\n"
        "s_mov_b32 s64, 184704\n"
        "ds_read_b128 a[220:223], v241 offset:1536\n"
        "v_cvt_pk_bf16_f32 v218, v116, v117\n"
        "v_mfma_f32_16x16x32_bf16 v[120:123], a[56:59], a[224:227], v[120:123]\n"
        "buffer_load_dwordx4 v128, s[20:23], s64 offen lds\n"
        "s_waitcnt vmcnt(7)\n"
        "ds_read_b64_tr_b16 a[80:81], v255 offset:6144\n"
        "ds_read_b64_tr_b16 a[82:83], v255 offset:6912\n"
        "v_mfma_f32_16x16x32_bf16 v[120:123], a[60:63], a[228:231], v[120:123]\n"
        "ds_read_b64_tr_b16 a[104:105], v255 offset:6176\n"
        "ds_read_b64_tr_b16 a[106:107], v255 offset:6944\n"
        "v_mfma_f32_16x16x32_bf16 v[120:123], a[64:67], a[232:235], v[120:123]\n"
        "ds_read_b64_tr_b16 a[128:129], v255 offset:6208\n"
        "ds_read_b64_tr_b16 a[130:131], v255 offset:6976\n"
        "v_mfma_f32_16x16x32_bf16 v[120:123], a[68:71], a[236:239], v[120:123]\n"
        "s_waitcnt lgkmcnt(15)\n"
        "buffer_load_dwordx4 v129, s[20:23], s64 offen offset:1024 lds\n"
        "v_mfma_f32_16x16x32_bf16 v[96:99], a[72:75], a[240:243], 0\n"
        "s_waitcnt lgkmcnt(12)\n"
        "ds_read_b128 a[224:227], v240 offset:1664\n"
        "ds_read_b128 a[228:231], v240 offset:12288\n"
        "v_cvt_pk_bf16_f32 v219, v118, v119\n"
        "v_mfma_f32_16x16x32_bf16 v[96:99], a[76:79], a[244:247], v[96:99]\n"
        "s_waitcnt lgkmcnt(6)\n"
        "ds_read_b128 a[232:235], v241 offset:12288\n"
        "ds_read_b128 a[236:239], v240 offset:12416\n"
        "v_mfma_f32_16x16x32_bf16 v[96:99], a[80:83], a[248:251], v[96:99]\n"
        "s_waitcnt lgkmcnt(6)\n"
        "buffer_load_dwordx4 v130, s[20:23], s64 offen offset:2048 lds\n"
        "v_mfma_f32_16x16x32_bf16 v[100:103], a[104:107], a[248:251], 0\n"
        "v_cvt_pk_bf16_f32 v220, v120, v121\n"
        "v_cvt_pk_bf16_f32 v221, v122, v123\n"
        "v_mfma_f32_16x16x32_bf16 v[100:103], a[100:103], a[244:247], v[100:103]\n"
        "v_cvt_pk_bf16_f32 v226, v120, v121\n"
        "v_cvt_pk_bf16_f32 v227, v122, v123\n"
        "v_mfma_f32_16x16x32_bf16 v[100:103], a[96:99], a[240:243], v[100:103]\n"
        "v_mfma_f32_16x16x32_bf16 v[104:107], a[120:123], a[240:243], 0\n"
        "s_add_u32 m0, s65, 0\n"
        "ds_read_b128 a[240:243], v240 offset:13824\n"
        "v_mfma_f32_16x16x32_bf16 v[104:107], a[124:127], a[244:247], v[104:107]\n"
        "s_waitcnt lgkmcnt(5)\n"
        "s_mov_b32 s64, 768\n"
        "ds_read_b128 a[244:247], v241 offset:13824\n"
        "v_mfma_f32_16x16x32_bf16 v[104:107], a[128:131], a[248:251], v[104:107]\n"
        "buffer_load_dwordx4 v128, s[20:23], s64 offen lds\n"
        "v_mfma_f32_16x16x32_bf16 v[108:111], a[72:75], a[216:219], 0\n"
        "ds_read_b128 a[248:251], v240 offset:13952\n"
        "v_mfma_f32_16x16x32_bf16 v[108:111], a[76:79], a[220:223], v[108:111]\n"
        "s_waitcnt lgkmcnt(6)\n"
        "v_mfma_f32_16x16x32_bf16 v[108:111], a[80:83], a[224:227], v[108:111]\n"
        "buffer_load_dwordx4 v129, s[20:23], s64 offen offset:1024 lds\n"
        "v_mfma_f32_16x16x32_bf16 v[64:67], a[104:107], a[224:227], 0\n"
        "v_mfma_f32_16x16x32_bf16 v[64:67], a[100:103], a[220:223], v[64:67]\n"
        "v_mfma_f32_16x16x32_bf16 v[64:67], a[96:99], a[216:219], v[64:67]\n"
        "buffer_load_dwordx4 v130, s[20:23], s64 offen offset:2048 lds\n"
        "v_mfma_f32_16x16x32_bf16 v[68:71], a[120:123], a[216:219], 0\n"
        "ds_read_b128 a[216:219], v241 offset:128\n"
        "v_mfma_f32_16x16x32_bf16 v[68:71], a[124:127], a[220:223], v[68:71]\n"
        "s_waitcnt vmcnt(9)\n"
        "ds_read_b64_tr_b16 a[84:85], v255 offset:9216\n"
        "ds_read_b64_tr_b16 a[86:87], v255 offset:9984\n"
        "v_mfma_f32_16x16x32_bf16 v[68:71], a[128:131], a[224:227], v[68:71]\n"
        "s_waitcnt lgkmcnt(8)\n"
        "ds_read_b64_tr_b16 a[108:109], v255 offset:9248\n"
        "ds_read_b64_tr_b16 a[110:111], v255 offset:10016\n"
        "v_mfma_f32_16x16x32_bf16 v[72:75], a[72:75], a[228:231], 0\n"
        "s_waitcnt lgkmcnt(9)\n"
        "s_add_u32 m0, s65, 3072\n"
        "ds_read_b64_tr_b16 a[132:133], v255 offset:9280\n"
        "ds_read_b64_tr_b16 a[134:135], v255 offset:10048\n"
        "v_mfma_f32_16x16x32_bf16 v[72:75], a[76:79], a[232:235], v[72:75]\n"
        "s_waitcnt lgkmcnt(10)\n"
        "s_mov_b32 s64, 37632\n"
        "ds_read_b128 a[220:223], v240 offset:256\n"
        "ds_read_b128 a[224:227], v241 offset:256\n"
        "v_mfma_f32_16x16x32_bf16 v[72:75], a[80:83], a[236:239], v[72:75]\n"
        "buffer_load_dwordx4 v128, s[20:23], s64 offen lds\n"
        "v_mfma_f32_16x16x32_bf16 v[76:79], a[104:107], a[236:239], 0\n"
        "v_mfma_f32_16x16x32_bf16 v[76:79], a[100:103], a[232:235], v[76:79]\n"
        "v_mfma_f32_16x16x32_bf16 v[76:79], a[96:99], a[228:231], v[76:79]\n"
        "buffer_load_dwordx4 v129, s[20:23], s64 offen offset:1024 lds\n"
        "v_mfma_f32_16x16x32_bf16 v[80:83], a[120:123], a[228:231], 0\n"
        "s_waitcnt vmcnt(8)\n"
        "ds_read_b64_tr_b16 a[88:89], v255 offset:12288\n"
        "ds_read_b64_tr_b16 a[90:91], v255 offset:13056\n"
        "v_mfma_f32_16x16x32_bf16 v[80:83], a[124:127], a[232:235], v[80:83]\n"
        "ds_read_b64_tr_b16 a[112:113], v255 offset:12320\n"
        "ds_read_b64_tr_b16 a[114:115], v255 offset:13088\n"
        "v_mfma_f32_16x16x32_bf16 v[80:83], a[128:131], a[236:239], v[80:83]\n"
        "s_waitcnt lgkmcnt(15)\n"
        "buffer_load_dwordx4 v130, s[20:23], s64 offen offset:2048 lds\n"
        "ds_read_b64_tr_b16 a[136:137], v255 offset:12352\n"
        "ds_read_b64_tr_b16 a[138:139], v255 offset:13120\n"
        "v_mfma_f32_16x16x32_bf16 v[84:87], a[72:75], a[240:243], 0\n"
        "s_waitcnt lgkmcnt(15)\n"
        "ds_read_b128 a[228:231], v241 offset:1664\n"
        "ds_read_b128 a[232:235], v240 offset:1792\n"
        "v_mfma_f32_16x16x32_bf16 v[84:87], a[76:79], a[244:247], v[84:87]\n"
        "s_waitcnt lgkmcnt(15)\n"
        "ds_read_b128 a[236:239], v241 offset:1792\n"
        "v_mfma_f32_16x16x32_bf16 v[84:87], a[80:83], a[248:251], v[84:87]\n"
        "v_mfma_f32_16x16x32_bf16 v[88:91], a[104:107], a[248:251], 0\n"
        "s_add_u32 m0, s65, 6144\n"
        "v_mfma_f32_16x16x32_bf16 v[88:91], a[100:103], a[244:247], v[88:91]\n"
        "s_mov_b32 s64, 74496\n"
        "v_mfma_f32_16x16x32_bf16 v[88:91], a[96:99], a[240:243], v[88:91]\n"
        "buffer_load_dwordx4 v128, s[20:23], s64 offen lds\n"
        "s_waitcnt vmcnt(7)\n"
        "ds_read_b64_tr_b16 a[92:93], v255 offset:15360\n"
        "ds_read_b64_tr_b16 a[94:95], v255 offset:16128\n"
        "v_mfma_f32_16x16x32_bf16 v[92:95], a[120:123], a[240:243], 0\n"
        "ds_read_b64_tr_b16 a[116:117], v255 offset:15392\n"
        "ds_read_b64_tr_b16 a[118:119], v255 offset:16160\n"
        "v_mfma_f32_16x16x32_bf16 v[92:95], a[124:127], a[244:247], v[92:95]\n"
        "ds_read_b64_tr_b16 a[140:141], v255 offset:15424\n"
        "ds_read_b64_tr_b16 a[142:143], v255 offset:16192\n"
        "v_mfma_f32_16x16x32_bf16 v[92:95], a[128:131], a[248:251], v[92:95]\n"
        "s_waitcnt lgkmcnt(15)\n"
        "buffer_load_dwordx4 v129, s[20:23], s64 offen offset:1024 lds\n"
        "v_mfma_f32_16x16x32_bf16 v[96:99], a[84:87], a[216:219], v[96:99]\n"
        "s_waitcnt lgkmcnt(13)\n"
        "ds_read_b128 a[240:243], v241 offset:12416\n"
        "ds_read_b128 a[244:247], v240 offset:12544\n"
        "v_mfma_f32_16x16x32_bf16 v[96:99], a[88:91], a[220:223], v[96:99]\n"
        "s_waitcnt lgkmcnt(6)\n"
        "ds_read_b128 a[248:251], v241 offset:12544\n"
        "ds_read_b128 v[162:165], v251 offset:0\n"
        "v_mfma_f32_16x16x32_bf16 v[96:99], a[92:95], a[224:227], v[96:99]\n"
        "s_waitcnt lgkmcnt(6)\n"
        "buffer_load_dwordx4 v130, s[20:23], s64 offen offset:2048 lds\n"
        "v_mfma_f32_16x16x32_bf16 v[100:103], a[116:119], a[224:227], v[100:103]\n"
        "s_nop 1\n"
        "v_mfma_f32_16x16x32_bf16 v[100:103], a[112:115], a[220:223], v[100:103]\n"
        "s_nop 1\n"
        "v_mfma_f32_16x16x32_bf16 v[100:103], a[108:111], a[216:219], v[100:103]\n"
        "v_cvt_pk_bf16_f32 v152, v96, v97\n"
        "v_cvt_pk_bf16_f32 v153, v98, v99\n"
        "v_mfma_f32_16x16x32_bf16 v[104:107], a[132:135], a[216:219], v[104:107]\n"
        "s_add_u32 m0, s65, 9216\n"
        "ds_read_b128 a[216:219], v241 offset:13952\n"
        "s_nop 0\n"
        "v_mfma_f32_16x16x32_bf16 v[104:107], a[136:139], a[220:223], v[104:107]\n"
        "s_waitcnt lgkmcnt(5)\n"
        "s_mov_b32 s64, 111360\n"
        "ds_read_b128 a[220:223], v240 offset:14080\n"
        "v_cvt_pk_bf16_f32 v154, v100, v101\n"
        "v_mfma_f32_16x16x32_bf16 v[104:107], a[140:143], a[224:227], v[104:107]\n"
        "buffer_load_dwordx4 v128, s[20:23], s64 offen lds\n"
        "v_mfma_f32_16x16x32_bf16 v[108:111], a[84:87], a[228:231], v[108:111]\n"
        "ds_read_b128 a[224:227], v241 offset:14080\n"
        "v_cvt_pk_bf16_f32 v155, v102, v103\n"
        "v_mfma_f32_16x16x32_bf16 v[108:111], a[88:91], a[232:235], v[108:111]\n"
        "ds_read_b128 v[182:185], v251 offset:512\n"
        "s_nop 0\n"
        "v_mfma_f32_16x16x32_bf16 v[108:111], a[92:95], a[236:239], v[108:111]\n"
        "buffer_load_dwordx4 v129, s[20:23], s64 offen offset:1024 lds\n"
        "v_mfma_f32_16x16x32_bf16 v[64:67], a[116:119], a[236:239], v[64:67]\n"
        "s_waitcnt vmcnt(8)\n"
        "ds_read_b64_tr_b16 a[144:145], v255 offset:0\n"
        "ds_read_b64_tr_b16 a[146:147], v255 offset:768\n"
        "v_mfma_f32_16x16x32_bf16 v[64:67], a[112:115], a[232:235], v[64:67]\n"
        "ds_read_b64_tr_b16 a[168:169], v255 offset:32\n"
        "ds_read_b64_tr_b16 a[170:171], v255 offset:800\n"
        "v_mfma_f32_16x16x32_bf16 v[64:67], a[108:111], a[228:231], v[64:67]\n"
        "buffer_load_dwordx4 v130, s[20:23], s64 offen offset:2048 lds\n"
        "ds_read_b64_tr_b16 a[192:193], v255 offset:64\n"
        "ds_read_b64_tr_b16 a[194:195], v255 offset:832\n"
        "v_mfma_f32_16x16x32_bf16 v[68:71], a[132:135], a[228:231], v[68:71]\n"
        "ds_read_b128 v[202:205], v251 offset:4096\n"
        "v_cvt_pk_bf16_f32 v168, v104, v105\n"
        "v_cvt_pk_bf16_f32 v169, v106, v107\n"
        "v_mfma_f32_16x16x32_bf16 v[68:71], a[136:139], a[232:235], v[68:71]\n"
        "v_cvt_pk_bf16_f32 v172, v108, v109\n"
        "v_cvt_pk_bf16_f32 v173, v110, v111\n"
        "v_mfma_f32_16x16x32_bf16 v[68:71], a[140:143], a[236:239], v[68:71]\n"
        "s_waitcnt lgkmcnt(14)\n"
        "v_cvt_pk_bf16_f32 v174, v64, v65\n"
        "v_cvt_pk_bf16_f32 v175, v66, v67\n"
        "v_mfma_f32_16x16x32_bf16 v[72:75], a[84:87], a[240:243], v[72:75]\n"
        "s_waitcnt lgkmcnt(13)\n"
        "s_add_u32 m0, s65, 12288\n"
        "s_nop 1\n"
        "v_mfma_f32_16x16x32_bf16 v[72:75], a[88:91], a[244:247], v[72:75]\n"
        "s_waitcnt lgkmcnt(12)\n"
        "s_mov_b32 s64, 148224\n"
        "v_cvt_pk_bf16_f32 v190, v68, v69\n"
        "v_cvt_pk_bf16_f32 v191, v70, v71\n"
        "v_mfma_f32_16x16x32_bf16 v[72:75], a[92:95], a[248:251], v[72:75]\n"
        "buffer_load_dwordx4 v128, s[20:23], s64 offen lds\n"
        "v_mfma_f32_16x16x32_bf16 v[76:79], a[116:119], a[248:251], v[76:79]\n"
        "s_nop 2\n"
        "v_mfma_f32_16x16x32_bf16 v[76:79], a[112:115], a[244:247], v[76:79]\n"
        "s_waitcnt vmcnt(7)\n"
        "ds_read_b64_tr_b16 a[148:149], v255 offset:3072\n"
        "ds_read_b64_tr_b16 a[150:151], v255 offset:3840\n"
        "v_mfma_f32_16x16x32_bf16 v[76:79], a[108:111], a[240:243], v[76:79]\n"
        "buffer_load_dwordx4 v129, s[20:23], s64 offen offset:1024 lds\n"
        "ds_read_b64_tr_b16 a[172:173], v255 offset:3104\n"
        "ds_read_b64_tr_b16 a[174:175], v255 offset:3872\n"
        "v_mfma_f32_16x16x32_bf16 v[80:83], a[132:135], a[240:243], v[80:83]\n"
        "ds_read_b64_tr_b16 a[196:197], v255 offset:3136\n"
        "ds_read_b64_tr_b16 a[198:199], v255 offset:3904\n"
        "v_mfma_f32_16x16x32_bf16 v[80:83], a[136:139], a[244:247], v[80:83]\n"
        "ds_read_b128 a[228:231], v240 offset:0\n"
        "ds_read_b128 a[232:235], v241 offset:0\n"
        "v_mfma_f32_16x16x32_bf16 v[80:83], a[140:143], a[248:251], v[80:83]\n"
        "s_waitcnt lgkmcnt(15)\n"
        "buffer_load_dwordx4 v130, s[20:23], s64 offen offset:2048 lds\n"
        "v_mfma_f32_16x16x32_bf16 v[84:87], a[84:87], a[216:219], v[84:87]\n"
        "s_waitcnt lgkmcnt(15)\n"
        "ds_read_b128 a[236:239], v240 offset:128\n"
        "ds_read_b128 a[240:243], v241 offset:128\n"
        "v_cvt_pk_bf16_f32 v192, v72, v73\n"
        "v_cvt_pk_bf16_f32 v193, v74, v75\n"
        "v_mfma_f32_16x16x32_bf16 v[84:87], a[88:91], a[220:223], v[84:87]\n"
        "s_waitcnt lgkmcnt(15)\n"
        "s_add_u32 m0, s65, 15360\n"
        "ds_read_b128 a[244:247], v240 offset:256\n"
        "ds_read_b128 a[248:251], v241 offset:256\n"
        "v_mfma_f32_16x16x32_bf16 v[84:87], a[92:95], a[224:227], v[84:87]\n"
        "s_mov_b32 s64, 185088\n"
        "v_cvt_pk_bf16_f32 v194, v76, v77\n"
        "v_cvt_pk_bf16_f32 v195, v78, v79\n"
        "v_mfma_f32_16x16x32_bf16 v[124:127], v[152:155], v[156:159], v[0:3]\n"
        "buffer_load_dwordx4 v128, s[20:23], s64 offen lds\n"
        "v_mfma_f32_16x16x32_bf16 v[124:127], v[168:171], v[160:163], v[124:127]\n"
        "ds_read_b128 v[222:225], v251 offset:4608\n"
        "v_cvt_pk_bf16_f32 v208, v80, v81\n"
        "v_mfma_f32_16x16x32_bf16 v[88:91], a[116:119], a[224:227], v[88:91]\n"
        "s_waitcnt vmcnt(7)\n"
        "ds_read_b64_tr_b16 a[152:153], v255 offset:6144\n"
        "ds_read_b64_tr_b16 a[154:155], v255 offset:6912\n"
        "v_mfma_f32_16x16x32_bf16 v[88:91], a[112:115], a[220:223], v[88:91]\n"
        "buffer_load_dwordx4 v129, s[20:23], s64 offen offset:1024 lds\n"
        "ds_read_b64_tr_b16 a[176:177], v255 offset:6176\n"
        "ds_read_b64_tr_b16 a[178:179], v255 offset:6944\n"
        "v_mfma_f32_16x16x32_bf16 v[88:91], a[108:111], a[216:219], v[88:91]\n"
        "ds_read_b64_tr_b16 a[200:201], v255 offset:6208\n"
        "ds_read_b64_tr_b16 a[202:203], v255 offset:6976\n"
        "v_cvt_pk_bf16_f32 v209, v82, v83\n"
        "v_mfma_f32_16x16x32_bf16 v[96:99], v[172:175], v[156:159], v[4:7]\n"
        "v_cvt_pk_bf16_f32 v212, v84, v85\n"
        "v_cvt_pk_bf16_f32 v213, v86, v87\n"
        "v_mfma_f32_16x16x32_bf16 v[96:99], v[188:191], v[164:167], v[96:99]\n"
        "buffer_load_dwordx4 v130, s[20:23], s64 offen offset:2048 lds\n"
        "v_mfma_f32_16x16x32_bf16 v[92:95], a[132:135], a[216:219], v[92:95]\n"
        "ds_read_b128 v[162:165], v252 offset:0\n"
        "v_cvt_pk_bf16_f32 v214, v88, v89\n"
        "v_mfma_f32_16x16x32_bf16 v[92:95], a[136:139], a[220:223], v[92:95]\n"
        "v_cvt_pk_bf16_f32 v215, v90, v91\n"
        "v_mfma_f32_16x16x32_bf16 v[92:95], a[140:143], a[224:227], v[92:95]\n"
        "v_exp_f32 v124, v124\n"
        "v_mfma_f32_16x16x32_bf16 v[100:103], v[152:155], v[176:179], v[16:19]\n"
        "s_waitcnt lgkmcnt(15)\n"
        "s_waitcnt vmcnt(6)\n"
        "ds_read_b64_tr_b16 a[156:157], v255 offset:9216\n"
        "ds_read_b64_tr_b16 a[158:159], v255 offset:9984\n"
        "v_mfma_f32_16x16x32_bf16 v[100:103], v[168:171], v[180:183], v[100:103]\n"
        "ds_read_b64_tr_b16 a[180:181], v255 offset:9248\n"
        "ds_read_b64_tr_b16 a[182:183], v255 offset:10016\n"
        "v_mfma_f32_16x16x32_bf16 v[104:107], v[172:175], v[176:179], v[20:23]\n"
        "ds_read_b64_tr_b16 a[204:205], v255 offset:9280\n"
        "ds_read_b64_tr_b16 a[206:207], v255 offset:10048\n"
        "v_mfma_f32_16x16x32_bf16 v[104:107], v[188:191], v[184:187], v[104:107]\n"
        "ds_read_b128 v[182:185], v252 offset:512\n"
        "v_cvt_pk_bf16_f32 v230, v92, v93\n"
        "v_mfma_f32_16x16x32_bf16 v[108:111], v[152:155], v[196:199], v[32:35]\n"
        "s_waitcnt lgkmcnt(15)\n"
        "s_add_u32 m0, s40, 98304\n"
        "v_cvt_pk_bf16_f32 v231, v94, v95\n"
        "v_mfma_f32_16x16x32_bf16 v[108:111], v[168:171], v[200:203], v[108:111]\n"
        "s_waitcnt lgkmcnt(15)\n"
        "s_sub_u32 s64, s47, 6291456\n"
        "v_exp_f32 v125, v125\n"
        "v_mfma_f32_16x16x32_bf16 v[112:115], a[228:231], a[144:147], 0\n"
        "s_waitcnt lgkmcnt(15)\n"
        "buffer_load_dwordx4 v242, s[8:11], s64 offen lds\n"
        "v_mfma_f32_16x16x32_bf16 v[112:115], a[232:235], a[148:151], v[112:115]\n"
        "s_waitcnt lgkmcnt(12)\n"
        "s_add_u32 m0, s40, 99328\n"
        "v_exp_f32 v126, v126\n"
        "v_mfma_f32_16x16x32_bf16 v[112:115], a[236:239], a[152:155], v[112:115]\n"
        "s_waitcnt lgkmcnt(5)\n"
        "s_waitcnt vmcnt(4)\n"
        "ds_read_b64_tr_b16 a[160:161], v255 offset:12288\n"
        "ds_read_b64_tr_b16 a[162:163], v255 offset:13056\n"
        "v_mfma_f32_16x16x32_bf16 v[112:115], a[240:243], a[156:159], v[112:115]\n"
        "s_waitcnt lgkmcnt(0)\n"
        "buffer_load_dwordx4 v243, s[8:11], s64 offen lds\n"
        "ds_read_b64_tr_b16 a[184:185], v255 offset:12320\n"
        "ds_read_b64_tr_b16 a[186:187], v255 offset:13088\n"
        "v_mfma_f32_16x16x32_bf16 v[112:115], a[244:247], a[160:163], v[112:115]\n"
        "s_add_u32 m0, s40, 100352\n"
        "ds_read_b64_tr_b16 a[208:209], v255 offset:12352\n"
        "ds_read_b64_tr_b16 a[210:211], v255 offset:13120\n"
        "s_waitcnt vmcnt(2)\n"
        "ds_read_b64_tr_b16 a[164:165], v255 offset:15360\n"
        "ds_read_b64_tr_b16 a[166:167], v255 offset:16128\n"
        "s_waitcnt lgkmcnt(0)\n"
        "v_mfma_f32_16x16x32_bf16 v[112:115], a[248:251], a[164:167], v[112:115]\n"
        "ds_read_b128 a[216:219], v240 offset:1536\n"
        "ds_read_b128 a[220:223], v241 offset:1536\n"
        "v_cvt_pk_bf16_f32 v64, v124, v125\n"
        "v_exp_f32 v127, v127\n"
        "v_exp_f32 v96, v96\n"
        "v_cvt_pk_bf16_f32 v65, v126, v127\n"
        "v_mfma_f32_16x16x32_bf16 v[124:127], v[172:175], v[196:199], v[36:39]\n"
        "buffer_load_dwordx4 v244, s[8:11], s64 offen lds\n"
        "v_mfma_f32_16x16x32_bf16 v[124:127], v[188:191], v[204:207], v[124:127]\n"
        "s_add_u32 m0, s40, 110592\n"
        "ds_read_b128 a[224:227], v240 offset:1664\n"
        "ds_read_b128 v[202:205], v252 offset:4096\n"
        "ds_read_b64_tr_b16 a[188:189], v255 offset:15392\n"
        "ds_read_b64_tr_b16 a[190:191], v255 offset:16160\n"
        "s_waitcnt lgkmcnt(0)\n"
        "v_mfma_f32_16x16x32_bf16 v[116:119], a[248:251], a[188:191], 0\n"
        "s_sub_u32 s64, s47, 5505024\n"
        "ds_read_b64_tr_b16 a[212:213], v255 offset:15424\n"
        "ds_read_b64_tr_b16 a[214:215], v255 offset:16192\n"
        "v_mfma_f32_16x16x32_bf16 v[116:119], a[244:247], a[184:187], v[116:119]\n"
        "buffer_load_dwordx4 v242, s[8:11], s64 offen lds\n"
        "v_mfma_f32_16x16x32_bf16 v[116:119], a[240:243], a[180:183], v[116:119]\n"
        "s_add_u32 m0, s40, 111616\n"
        "v_cvt_pk_bf16_f32 v128, v112, v113\n"
        "v_cvt_pk_bf16_f32 v129, v114, v115\n"
        "v_mfma_f32_16x16x32_bf16 v[116:119], a[236:239], a[176:179], v[116:119]\n"
        "v_exp_f32 v97, v97\n"
        "v_mfma_f32_16x16x32_bf16 v[116:119], a[232:235], a[172:175], v[116:119]\n"
        "buffer_load_dwordx4 v243, s[8:11], s64 offen lds\n"
        "v_mfma_f32_16x16x32_bf16 v[116:119], a[228:231], a[168:171], v[116:119]\n"
        "s_add_u32 m0, s40, 112640\n"
        "v_exp_f32 v98, v98\n"
        "v_cvt_pk_bf16_f32 v66, v96, v97\n"
        "v_exp_f32 v99, v99\n"
        "v_exp_f32 v100, v100\n"
        "v_cvt_pk_bf16_f32 v67, v98, v99\n"
        "v_mfma_f32_16x16x32_bf16 v[96:99], v[152:155], v[216:219], v[48:51]\n"
        "v_exp_f32 v101, v101\n"
        "v_mfma_f32_16x16x32_bf16 v[96:99], v[168:171], v[220:223], v[96:99]\n"
        "buffer_load_dwordx4 v244, s[8:11], s64 offen lds\n"
        "v_mfma_f32_16x16x32_bf16 v[120:123], a[228:231], a[192:195], 0\n"
        "s_xor_b32 m0, s48, 8192\n"
        "ds_read_b128 a[228:231], v241 offset:1664\n"
        "v_cvt_pk_bf16_f32 v136, v116, v117\n"
        "v_mfma_f32_16x16x32_bf16 v[120:123], a[232:235], a[196:199], v[120:123]\n"
        "s_sub_u32 s64, s36, 2097152\n"
        "ds_read_b128 a[232:235], v240 offset:1792\n"
        "v_cvt_pk_bf16_f32 v137, v118, v119\n"
        "v_mfma_f32_16x16x32_bf16 v[120:123], a[236:239], a[200:203], v[120:123]\n"
        "buffer_load_dwordx4 v249, s[12:15], s64 offen lds\n"
        "v_mfma_f32_16x16x32_bf16 v[120:123], a[240:243], a[204:207], v[120:123]\n"
        "ds_read_b128 a[236:239], v241 offset:1792\n"
        "v_cvt_pk_bf16_f32 v72, v100, v101\n"
        "v_mfma_f32_16x16x32_bf16 v[120:123], a[244:247], a[208:211], v[120:123]\n"
        "s_waitcnt lgkmcnt(3)\n"
        "v_exp_f32 v102, v102\n"
        "v_mfma_f32_16x16x32_bf16 v[120:123], a[248:251], a[212:215], v[120:123]\n"
        "buffer_load_dwordx4 v253, s[12:15], s64 offen offset:1024 lds\n"
        "v_exp_f32 v103, v103\n"
        "v_exp_f32 v104, v104\n"
        "v_cvt_pk_bf16_f32 v73, v102, v103\n"
        "v_mfma_f32_16x16x32_bf16 v[100:103], v[172:175], v[216:219], v[52:55]\n"
        "v_exp_f32 v105, v105\n"
        "v_mfma_f32_16x16x32_bf16 v[100:103], v[188:191], v[224:227], v[100:103]\n"
        "ds_read_b128 v[222:225], v252 offset:4608\n"
        "v_cvt_pk_bf16_f32 v144, v120, v121\n"
        "v_exp_f32 v106, v106\n"
        "v_cvt_pk_bf16_f32 v74, v104, v105\n"
        "v_exp_f32 v107, v107\n"
        "v_cvt_pk_bf16_f32 v145, v122, v123\n"
        "v_cvt_pk_bf16_f32 v75, v106, v107\n"
        "v_mfma_f32_16x16x32_bf16 v[104:107], v[192:195], v[156:159], v[8:11]\n"
        "v_exp_f32 v108, v108\n"
        "v_mfma_f32_16x16x32_bf16 v[104:107], v[208:211], v[160:163], v[104:107]\n"
        "v_exp_f32 v109, v109\n"
        "v_mfma_f32_16x16x32_bf16 v[112:115], a[216:219], a[144:147], 0\n"
        "ds_read_b128 a[240:243], v240 offset:12288\n"
        "v_cvt_pk_bf16_f32 v80, v108, v109\n"
        "v_mfma_f32_16x16x32_bf16 v[112:115], a[220:223], a[148:151], v[112:115]\n"
        "ds_read_b128 a[244:247], v241 offset:12288\n"
        "v_mfma_f32_16x16x32_bf16 v[112:115], a[224:227], a[152:155], v[112:115]\n"
        "s_waitcnt lgkmcnt(5)\n"
        "ds_read_b128 a[248:251], v240 offset:12416\n"
        "v_mfma_f32_16x16x32_bf16 v[112:115], a[228:231], a[156:159], v[112:115]\n"
        "s_waitcnt lgkmcnt(5)\n"
        "v_exp_f32 v110, v110\n"
        "v_mfma_f32_16x16x32_bf16 v[112:115], a[232:235], a[160:163], v[112:115]\n"
        "s_waitcnt lgkmcnt(4)\n"
        "v_exp_f32 v111, v111\n"
        "v_mfma_f32_16x16x32_bf16 v[112:115], a[236:239], a[164:167], v[112:115]\n"
        "v_cvt_pk_bf16_f32 v81, v110, v111\n"
        "v_mfma_f32_16x16x32_bf16 v[108:111], v[212:215], v[156:159], v[12:15]\n"
        "v_exp_f32 v124, v124\n"
        "v_mfma_f32_16x16x32_bf16 v[108:111], v[228:231], v[164:167], v[108:111]\n"
        "v_exp_f32 v125, v125\n"
        "v_mfma_f32_16x16x32_bf16 v[116:119], a[236:239], a[188:191], 0\n"
        "v_exp_f32 v126, v126\n"
        "v_mfma_f32_16x16x32_bf16 v[116:119], a[232:235], a[184:187], v[116:119]\n"
        "v_cvt_pk_bf16_f32 v130, v112, v113\n"
        "v_cvt_pk_bf16_f32 v131, v114, v115\n"
        "v_mfma_f32_16x16x32_bf16 v[116:119], a[228:231], a[180:183], v[116:119]\n"
        "v_exp_f32 v127, v127\n"
        "v_mfma_f32_16x16x32_bf16 v[116:119], a[224:227], a[176:179], v[116:119]\n"
        "v_cvt_pk_bf16_f32 v82, v124, v125\n"
        "v_cvt_pk_bf16_f32 v83, v126, v127\n"
        "v_mfma_f32_16x16x32_bf16 v[116:119], a[220:223], a[172:175], v[116:119]\n"
        "v_exp_f32 v96, v96\n"
        "v_mfma_f32_16x16x32_bf16 v[116:119], a[216:219], a[168:171], v[116:119]\n"
        "v_exp_f32 v97, v97\n"
        "v_mfma_f32_16x16x32_bf16 v[124:127], v[192:195], v[176:179], v[24:27]\n"
        "v_exp_f32 v98, v98\n"
        "v_mfma_f32_16x16x32_bf16 v[124:127], v[208:211], v[180:183], v[124:127]\n"
        "v_exp_f32 v99, v99\n"
        "v_mfma_f32_16x16x32_bf16 v[120:123], a[216:219], a[192:195], 0\n"
        "ds_read_b128 a[216:219], v241 offset:12416\n"
        "v_cvt_pk_bf16_f32 v88, v96, v97\n"
        "v_mfma_f32_16x16x32_bf16 v[120:123], a[220:223], a[196:199], v[120:123]\n"
        "ds_read_b128 a[220:223], v240 offset:12544\n"
        "v_cvt_pk_bf16_f32 v138, v116, v117\n"
        "v_mfma_f32_16x16x32_bf16 v[120:123], a[224:227], a[200:203], v[120:123]\n"
        "ds_read_b128 a[224:227], v241 offset:12544\n"
        "v_cvt_pk_bf16_f32 v139, v118, v119\n"
        "v_mfma_f32_16x16x32_bf16 v[120:123], a[228:231], a[204:207], v[120:123]\n"
        "v_exp_f32 v100, v100\n"
        "v_mfma_f32_16x16x32_bf16 v[120:123], a[232:235], a[208:211], v[120:123]\n"
        "v_exp_f32 v101, v101\n"
        "v_mfma_f32_16x16x32_bf16 v[120:123], a[236:239], a[212:215], v[120:123]\n"
        "v_cvt_pk_bf16_f32 v89, v98, v99\n"
        "v_cvt_pk_bf16_f32 v90, v100, v101\n"
        "v_mfma_f32_16x16x32_bf16 v[96:99], v[212:215], v[176:179], v[28:31]\n"
        "s_add_u32 m0, s65, 12288\n"
        "v_exp_f32 v102, v102\n"
        "v_mfma_f32_16x16x32_bf16 v[96:99], v[228:231], v[184:187], v[96:99]\n"
        "s_waitcnt lgkmcnt(5)\n"
        "s_mov_b32 s64, 12288\n"
        "v_exp_f32 v104, v104\n"
        "v_mfma_f32_16x16x32_bf16 v[112:115], a[240:243], a[144:147], 0\n"
        "s_waitcnt lgkmcnt(4)\n"
        "buffer_load_dwordx4 v238, s[52:55], s64 offen lds\n"
        "v_mfma_f32_16x16x32_bf16 v[112:115], a[244:247], a[148:151], v[112:115]\n"
        "s_waitcnt lgkmcnt(3)\n"
        "ds_read_b128 a[228:231], v240 offset:13824\n"
        "ds_read_b128 a[232:235], v241 offset:13824\n"
        "v_mfma_f32_16x16x32_bf16 v[112:115], a[248:251], a[152:155], v[112:115]\n"
        "s_waitcnt lgkmcnt(4)\n"
        "ds_read_b128 a[236:239], v240 offset:13952\n"
        "v_cvt_pk_bf16_f32 v146, v120, v121\n"
        "v_mfma_f32_16x16x32_bf16 v[112:115], a[216:219], a[156:159], v[112:115]\n"
        "s_waitcnt lgkmcnt(4)\n"
        "buffer_load_dwordx4 v239, s[52:55], s64 offen offset:1024 lds\n"
        "v_mfma_f32_16x16x32_bf16 v[112:115], a[220:223], a[160:163], v[112:115]\n"
        "s_waitcnt lgkmcnt(3)\n"
        "v_cvt_pk_bf16_f32 v147, v122, v123\n"
        "v_mfma_f32_16x16x32_bf16 v[112:115], a[224:227], a[164:167], v[112:115]\n"
        "v_exp_f32 v103, v103\n"
        "v_exp_f32 v105, v105\n"
        "v_cvt_pk_bf16_f32 v91, v102, v103\n"
        "v_mfma_f32_16x16x32_bf16 v[100:103], v[192:195], v[196:199], v[40:43]\n"
        "buffer_load_dwordx4 v236, s[52:55], s64 offen offset:2048 lds\n"
        "v_mfma_f32_16x16x32_bf16 v[100:103], v[208:211], v[200:203], v[100:103]\n"
        "s_add_u32 m0, s65, 15360\n"
        "v_exp_f32 v106, v106\n"
        "v_mfma_f32_16x16x32_bf16 v[116:119], a[224:227], a[188:191], 0\n"
        "s_mov_b32 s64, 49152\n"
        "v_cvt_pk_bf16_f32 v68, v104, v105\n"
        "v_cvt_pk_bf16_f32 v132, v112, v113\n"
        "v_mfma_f32_16x16x32_bf16 v[116:119], a[220:223], a[184:187], v[116:119]\n"
        "buffer_load_dwordx4 v238, s[52:55], s64 offen lds\n"
        "v_exp_f32 v107, v107\n"
        "v_mfma_f32_16x16x32_bf16 v[116:119], a[216:219], a[180:183], v[116:119]\n"
        "v_cvt_pk_bf16_f32 v69, v106, v107\n"
        "v_cvt_pk_bf16_f32 v133, v114, v115\n"
        "v_mfma_f32_16x16x32_bf16 v[116:119], a[248:251], a[176:179], v[116:119]\n"
        "v_exp_f32 v108, v108\n"
        "v_mfma_f32_16x16x32_bf16 v[116:119], a[244:247], a[172:175], v[116:119]\n"
        "buffer_load_dwordx4 v239, s[52:55], s64 offen offset:1024 lds\n"
        "v_mfma_f32_16x16x32_bf16 v[116:119], a[240:243], a[168:171], v[116:119]\n"
        "v_exp_f32 v109, v109\n"
        "v_mfma_f32_16x16x32_bf16 v[104:107], v[212:215], v[196:199], v[44:47]\n"
        "v_exp_f32 v110, v110\n"
        "v_mfma_f32_16x16x32_bf16 v[104:107], v[228:231], v[204:207], v[104:107]\n"
        "buffer_load_dwordx4 v236, s[52:55], s64 offen offset:2048 lds\n"
        "v_mfma_f32_16x16x32_bf16 v[120:123], a[240:243], a[192:195], 0\n"
        "ds_read_b128 a[240:243], v241 offset:13952\n"
        "v_cvt_pk_bf16_f32 v70, v108, v109\n"
        "v_mfma_f32_16x16x32_bf16 v[120:123], a[244:247], a[196:199], v[120:123]\n"
        "ds_read_b128 a[244:247], v240 offset:14080\n"
        "v_exp_f32 v111, v111\n"
        "v_mfma_f32_16x16x32_bf16 v[120:123], a[248:251], a[200:203], v[120:123]\n"
        "ds_read_b128 a[248:251], v241 offset:14080\n"
        "v_cvt_pk_bf16_f32 v71, v110, v111\n"
        "v_mfma_f32_16x16x32_bf16 v[120:123], a[216:219], a[204:207], v[120:123]\n"
        "v_cvt_pk_bf16_f32 v140, v116, v117\n"
        "v_cvt_pk_bf16_f32 v141, v118, v119\n"
        "v_mfma_f32_16x16x32_bf16 v[120:123], a[220:223], a[208:211], v[120:123]\n"
        "v_exp_f32 v124, v124\n"
        "v_mfma_f32_16x16x32_bf16 v[120:123], a[224:227], a[212:215], v[120:123]\n"
        "v_exp_f32 v125, v125\n"
        "v_mfma_f32_16x16x32_bf16 v[108:111], v[192:195], v[216:219], v[56:59]\n"
        "v_exp_f32 v126, v126\n"
        "v_mfma_f32_16x16x32_bf16 v[108:111], v[208:211], v[220:223], v[108:111]\n"
        "s_waitcnt lgkmcnt(5)\n"
        "v_cvt_pk_bf16_f32 v76, v124, v125\n"
        "s_nop 0\n"
        "v_mfma_f32_16x16x32_bf16 v[112:115], a[228:231], a[144:147], 0\n"
        "s_waitcnt lgkmcnt(4)\n"
        "v_exp_f32 v127, v127\n"
        "v_mfma_f32_16x16x32_bf16 v[112:115], a[232:235], a[148:151], v[112:115]\n"
        "s_waitcnt lgkmcnt(3)\n"
        "v_cvt_pk_bf16_f32 v77, v126, v127\n"
        "v_cvt_pk_bf16_f32 v148, v120, v121\n"
        "v_mfma_f32_16x16x32_bf16 v[112:115], a[236:239], a[152:155], v[112:115]\n"
        "s_waitcnt lgkmcnt(2)\n"
        "v_cvt_pk_bf16_f32 v149, v122, v123\n"
        "v_mfma_f32_16x16x32_bf16 v[112:115], a[240:243], a[156:159], v[112:115]\n"
        "s_waitcnt lgkmcnt(1)\n"
        "v_exp_f32 v96, v96\n"
        "v_mfma_f32_16x16x32_bf16 v[112:115], a[244:247], a[160:163], v[112:115]\n"
        "s_waitcnt lgkmcnt(0)\n"
        "v_exp_f32 v97, v97\n"
        "v_mfma_f32_16x16x32_bf16 v[112:115], a[248:251], a[164:167], v[112:115]\n"
        "v_exp_f32 v98, v98\n"
        "v_mfma_f32_16x16x32_bf16 v[124:127], v[212:215], v[216:219], v[60:63]\n"
        "v_cvt_pk_bf16_f32 v78, v96, v97\n"
        "s_nop 0\n"
        "v_mfma_f32_16x16x32_bf16 v[124:127], v[228:231], v[224:227], v[124:127]\n"
        "v_exp_f32 v99, v99\n"
        "v_mfma_f32_16x16x32_bf16 v[116:119], a[248:251], a[188:191], 0\n"
        "v_cvt_pk_bf16_f32 v79, v98, v99\n"
        "v_mov_b64 v[96:97], 0\n"
        "v_mfma_f32_16x16x32_bf16 v[116:119], a[244:247], a[184:187], v[116:119]\n"
        "v_cvt_pk_bf16_f32 v134, v112, v113\n"
        "v_cvt_pk_bf16_f32 v135, v114, v115\n"
        "v_mfma_f32_16x16x32_bf16 v[116:119], a[240:243], a[180:183], v[116:119]\n"
        "v_exp_f32 v100, v100\n"
        "v_mfma_f32_16x16x32_bf16 v[116:119], a[236:239], a[176:179], v[116:119]\n"
        "v_exp_f32 v101, v101\n"
        "v_mfma_f32_16x16x32_bf16 v[116:119], a[232:235], a[172:175], v[116:119]\n"
        "v_exp_f32 v102, v102\n"
        "v_mfma_f32_16x16x32_bf16 v[116:119], a[228:231], a[168:171], v[116:119]\n"
        "v_cvt_pk_bf16_f32 v84, v100, v101\n"
        "v_add_u32 v240, 24576, v240\n"
        "v_mfma_f32_16x16x32_bf16 v[120:123], a[228:231], a[192:195], 0\n"
        "v_exp_f32 v103, v103\n"
        "v_mfma_f32_16x16x32_bf16 v[120:123], a[232:235], a[196:199], v[120:123]\n"
        "v_mov_b64 v[172:173], 0\n"
        "v_cvt_pk_bf16_f32 v85, v102, v103\n"
        "v_mfma_f32_16x16x32_bf16 v[120:123], a[236:239], a[200:203], v[120:123]\n"
        "v_add_u32 v241, 24576, v241\n"
        "v_cvt_pk_bf16_f32 v142, v116, v117\n"
        "v_mfma_f32_16x16x32_bf16 v[120:123], a[240:243], a[204:207], v[120:123]\n"
        "v_cvt_pk_bf16_f32 v143, v118, v119\n"
        "v_mfma_f32_16x16x32_bf16 v[120:123], a[244:247], a[208:211], v[120:123]\n"
        "s_waitcnt vmcnt(6)\n"
        "s_barrier\n"
        "v_exp_f32 v104, v104\n"
        "v_mfma_f32_16x16x32_bf16 v[120:123], a[248:251], a[212:215], v[120:123]\n"
        "v_exp_f32 v105, v105\n"
        "v_smfmac_f32_16x16x64_bf16 v[96:99], a[252:255], v[64:71], v237\n"
        "v_cvt_pk_bf16_f32 v86, v104, v105\n"
        "v_mfma_f32_16x16x32_bf16 v[100:103], v[132:135], v[68:71], 0\n"
        "v_exp_f32 v106, v106\n"
        "v_mfma_f32_16x16x32_bf16 v[100:103], v[128:131], v[64:67], v[100:103]\n"
        "v_mov_b64 v[192:193], 0\n"
        "s_nop 0\n"
        "v_exp_f32 v107, v107\n"
        "v_cvt_pk_bf16_f32 v150, v120, v121\n"
        "v_cvt_pk_bf16_f32 v87, v106, v107\n"
        "v_mfma_f32_16x16x32_bf16 v[104:107], v[136:139], v[64:67], 0\n"
        "v_cvt_pk_bf16_f32 v151, v122, v123\n"
        "v_add_f32 v97, v96, v97\n"
        "v_mfma_f32_16x16x32_bf16 v[104:107], v[140:143], v[68:71], v[104:107]\n"
        "v_exp_f32 v108, v108\n"
        "v_exp_f32 v109, v109\n"
        "v_exp_f32 v110, v110\n"
        "v_cvt_pk_bf16_f32 v92, v108, v109\n"
        "v_exp_f32 v111, v111\n"
        "v_rcp_f32 v96, v97\n"
        "v_cvt_pk_bf16_f32 v93, v110, v111\n"
        "v_mfma_f32_16x16x32_bf16 v[108:111], v[148:151], v[68:71], 0\n"
        "ds_read_b128 a[236:239], v240 offset:0\n"
        "ds_read_b128 a[240:243], v241 offset:0\n"
        "v_mfma_f32_16x16x32_bf16 v[108:111], v[144:147], v[64:67], v[108:111]\n"
        "ds_read_b128 a[244:247], v240 offset:128\n"
        "ds_read_b128 a[248:251], v241 offset:128\n"
        "v_smfmac_f32_16x16x64_bf16 v[172:175], a[252:255], v[72:79], v237\n"
        "ds_read_b128 a[216:219], v240 offset:256\n"
        "v_mfma_f32_16x16x32_bf16 v[176:179], v[132:135], v[76:79], 0\n"
        "ds_read_b128 a[220:223], v241 offset:256\n"
        "v_mul_f32 v100, v96, v100\n"
        "v_mfma_f32_16x16x32_bf16 v[176:179], v[128:131], v[72:75], v[176:179]\n"
        "v_mul_f32 v101, v96, v101\n"
        "v_mul_f32 v102, v96, v102\n"
        "v_mfma_f32_16x16x32_bf16 v[180:183], v[136:139], v[72:75], 0\n"
        "v_mul_f32 v103, v96, v103\n"
        "v_cvt_pk_bf16_f32 v100, v100, v101\n"
        "v_mfma_f32_16x16x32_bf16 v[180:183], v[140:143], v[76:79], v[180:183]\n"
        "v_cvt_pk_bf16_f32 v101, v102, v103\n"
        "v_mul_f32 v104, v96, v104\n"
        "v_mfma_f32_16x16x32_bf16 v[184:187], v[148:151], v[76:79], 0\n"
        "v_mul_f32 v105, v96, v105\n"
        "v_mul_f32 v106, v96, v106\n"
        "v_mfma_f32_16x16x32_bf16 v[184:187], v[144:147], v[72:75], v[184:187]\n"
        "v_mul_f32 v107, v96, v107\n"
        "v_cvt_pk_bf16_f32 v102, v104, v105\n"
        "v_smfmac_f32_16x16x64_bf16 v[192:195], a[252:255], v[80:87], v237\n"
        "v_cvt_pk_bf16_f32 v103, v106, v107\n"
        "v_mfma_f32_16x16x32_bf16 v[196:199], v[132:135], v[84:87], 0\n"
        "ds_write_b128 v245, v[100:103] offset:0\n"
        "v_mfma_f32_16x16x32_bf16 v[196:199], v[128:131], v[80:83], v[196:199]\n"
        "v_exp_f32 v124, v124\n"
        "v_mfma_f32_16x16x32_bf16 v[200:203], v[136:139], v[80:83], 0\n"
        "v_mul_f32 v108, v96, v108\n"
        "v_mul_f32 v109, v96, v109\n"
        "v_mfma_f32_16x16x32_bf16 v[200:203], v[140:143], v[84:87], v[200:203]\n"
        "v_mul_f32 v110, v96, v110\n"
        "v_mul_f32 v111, v96, v111\n"
        "v_mfma_f32_16x16x32_bf16 v[204:207], v[148:151], v[84:87], 0\n"
        "v_exp_f32 v125, v125\n"
        "v_mfma_f32_16x16x32_bf16 v[204:207], v[144:147], v[80:83], v[204:207]\n"
        "s_waitcnt lgkmcnt(6)\n"
        "v_exp_f32 v126, v126\n"
        "v_cvt_pk_bf16_f32 v94, v124, v125\n"
        "v_mov_b64 v[124:125], 0\n"
        "v_mfma_f32_16x16x32_bf16 v[112:115], a[72:75], a[236:239], 0\n"
        "s_waitcnt lgkmcnt(5)\n"
        "v_cvt_pk_bf16_f32 v108, v108, v109\n"
        "v_cvt_pk_bf16_f32 v109, v110, v111\n"
        "v_mfma_f32_16x16x32_bf16 v[112:115], a[76:79], a[240:243], v[112:115]\n"
        "s_waitcnt lgkmcnt(4)\n"
        "ds_write_b64 v246, v[108:109] offset:0\n"
        "v_mfma_f32_16x16x32_bf16 v[112:115], a[80:83], a[244:247], v[112:115]\n"
        "s_waitcnt lgkmcnt(4)\n"
        "v_exp_f32 v127, v127\n"
        "v_mfma_f32_16x16x32_bf16 v[112:115], a[84:87], a[248:251], v[112:115]\n"
        "s_waitcnt lgkmcnt(3)\n"
        "v_cvt_pk_bf16_f32 v95, v126, v127\n"
        "v_max_u32 v236, v96, v97\n"
        "v_mfma_f32_16x16x32_bf16 v[112:115], a[88:91], a[216:219], v[112:115]\n"
        "s_waitcnt lgkmcnt(2)\n"
        "v_add_f32 v173, v172, v173\n"
        "v_add_f32 v193, v192, v193\n"
        "v_mfma_f32_16x16x32_bf16 v[112:115], a[92:95], a[220:223], v[112:115]\n"
        "v_rcp_f32 v172, v173\n"
        "v_smfmac_f32_16x16x64_bf16 v[124:127], a[252:255], v[88:95], v237\n"
        "v_mul_f32 v176, v172, v176\n"
        "v_mfma_f32_16x16x32_bf16 v[80:83], v[132:135], v[92:95], 0\n"
        "v_mul_f32 v177, v172, v177\n"
        "v_mul_f32 v178, v172, v178\n"
        "v_mfma_f32_16x16x32_bf16 v[80:83], v[128:131], v[88:91], v[80:83]\n"
        "v_mul_f32 v179, v172, v179\n"
        "v_cvt_pk_bf16_f32 v176, v176, v177\n"
        "v_mfma_f32_16x16x32_bf16 v[84:87], v[136:139], v[88:91], 0\n"
        "v_cvt_pk_bf16_f32 v177, v178, v179\n"
        "v_cvt_pk_bf16_f32 v152, v112, v113\n"
        "v_mfma_f32_16x16x32_bf16 v[84:87], v[140:143], v[92:95], v[84:87]\n"
        "v_cvt_pk_bf16_f32 v153, v114, v115\n"
        "v_mul_f32 v180, v172, v180\n"
        "v_mfma_f32_16x16x32_bf16 v[96:99], v[148:151], v[92:95], 0\n"
        "v_mul_f32 v181, v172, v181\n"
        "v_mul_f32 v182, v172, v182\n"
        "v_mfma_f32_16x16x32_bf16 v[96:99], v[144:147], v[88:91], v[96:99]\n"
        "v_mul_f32 v183, v172, v183\n"
        "v_cvt_pk_bf16_f32 v178, v180, v181\n"
        "v_mfma_f32_16x16x32_bf16 v[116:119], a[116:119], a[220:223], 0\n"
        "s_add_u32 m0, s65, 0\n"
        "v_cvt_pk_bf16_f32 v179, v182, v183\n"
        "v_mul_f32 v184, v172, v184\n"
        "v_mfma_f32_16x16x32_bf16 v[116:119], a[112:115], a[216:219], v[116:119]\n"
        "s_mov_b32 s64, 0\n"
        "ds_write_b128 v245, v[176:179] offset:256\n"
        "v_mfma_f32_16x16x32_bf16 v[116:119], a[108:111], a[248:251], v[116:119]\n"
        "buffer_load_dwordx4 v232, s[52:55], s64 offen lds\n"
        "v_mfma_f32_16x16x32_bf16 v[116:119], a[104:107], a[244:247], v[116:119]\n"
        "v_mul_f32 v185, v172, v185\n"
        "v_mul_f32 v186, v172, v186\n"
        "v_mfma_f32_16x16x32_bf16 v[116:119], a[100:103], a[240:243], v[116:119]\n"
        "v_mul_f32 v187, v172, v187\n"
        "v_add_f32 v125, v124, v125\n"
        "v_mfma_f32_16x16x32_bf16 v[116:119], a[96:99], a[236:239], v[116:119]\n"
        "v_rcp_f32 v124, v125\n"
        "v_mfma_f32_16x16x32_bf16 v[120:123], a[120:123], a[236:239], 0\n"
        "v_cvt_pk_bf16_f32 v184, v184, v185\n"
        "v_cvt_pk_bf16_f32 v185, v186, v187\n"
        "v_mfma_f32_16x16x32_bf16 v[120:123], a[124:127], a[240:243], v[120:123]\n"
        "ds_write_b64 v246, v[184:185] offset:256\n"
        "v_mfma_f32_16x16x32_bf16 v[120:123], a[128:131], a[244:247], v[120:123]\n"
        "buffer_load_dwordx4 v233, s[52:55], s64 offen offset:1024 lds\n"
        "v_mfma_f32_16x16x32_bf16 v[120:123], a[132:135], a[248:251], v[120:123]\n"
        "v_mul_f32 v80, v124, v80\n"
        "v_mul_f32 v81, v124, v81\n"
        "v_mfma_f32_16x16x32_bf16 v[120:123], a[136:139], a[216:219], v[120:123]\n"
        "v_mul_f32 v82, v124, v82\n"
        "v_mul_f32 v83, v124, v83\n"
        "v_mfma_f32_16x16x32_bf16 v[120:123], a[140:143], a[220:223], v[120:123]\n"
        "v_max3_u32 v236, v236, v172, v173\n"
        "v_cvt_pk_bf16_f32 v80, v80, v81\n"
        "v_mfma_f32_16x16x32_bf16 v[112:115], a[20:23], a[220:223], 0\n"
        "v_cvt_pk_bf16_f32 v81, v82, v83\n"
        "v_mul_f32 v84, v124, v84\n"
        "v_mfma_f32_16x16x32_bf16 v[112:115], a[16:19], a[216:219], v[112:115]\n"
        "v_mul_f32 v85, v124, v85\n"
        "v_mul_f32 v86, v124, v86\n"
        "v_mfma_f32_16x16x32_bf16 v[112:115], a[12:15], a[248:251], v[112:115]\n"
        "buffer_load_dwordx4 v234, s[52:55], s64 offen offset:2048 lds\n"
        "v_mfma_f32_16x16x32_bf16 v[112:115], a[8:11], a[244:247], v[112:115]\n"
        "v_mul_f32 v87, v124, v87\n"
        "v_cvt_pk_bf16_f32 v82, v84, v85\n"
        "v_mfma_f32_16x16x32_bf16 v[112:115], a[4:7], a[240:243], v[112:115]\n"
        "v_cvt_pk_bf16_f32 v83, v86, v87\n"
        "v_mul_f32 v96, v124, v96\n"
        "v_mfma_f32_16x16x32_bf16 v[112:115], a[0:3], a[236:239], v[112:115]\n"
        "ds_write_b128 v245, v[80:83] offset:768\n"
        "v_cvt_pk_bf16_f32 v154, v116, v117\n"
        "v_cvt_pk_bf16_f32 v155, v118, v119\n"
        "v_mfma_f32_16x16x32_bf16 v[116:119], a[24:27], a[236:239], 0\n"
        "s_add_u32 m0, s65, 3072\n"
        "v_mul_f32 v97, v124, v97\n"
        "v_mul_f32 v98, v124, v98\n"
        "v_mfma_f32_16x16x32_bf16 v[116:119], a[28:31], a[240:243], v[116:119]\n"
        "s_mov_b32 s64, 18432\n"
        "v_mul_f32 v99, v124, v99\n"
        "v_cvt_pk_bf16_f32 v96, v96, v97\n"
        "v_mfma_f32_16x16x32_bf16 v[116:119], a[32:35], a[244:247], v[116:119]\n"
        "buffer_load_dwordx4 v232, s[52:55], s64 offen lds\n"
        "v_mfma_f32_16x16x32_bf16 v[116:119], a[36:39], a[248:251], v[116:119]\n"
        "v_rcp_f32 v192, v193\n"
        "v_mfma_f32_16x16x32_bf16 v[116:119], a[40:43], a[216:219], v[116:119]\n"
        "v_cvt_pk_bf16_f32 v97, v98, v99\n"
        "v_mul_f32 v196, v192, v196\n"
        "v_mfma_f32_16x16x32_bf16 v[116:119], a[44:47], a[220:223], v[116:119]\n"
        "ds_write_b64 v246, v[96:97] offset:768\n"
        "v_cvt_pk_bf16_f32 v168, v120, v121\n"
        "v_cvt_pk_bf16_f32 v169, v122, v123\n"
        "v_mfma_f32_16x16x32_bf16 v[120:123], a[68:71], a[220:223], 0\n"
        "v_mul_f32 v197, v192, v197\n"
        "v_mul_f32 v198, v192, v198\n"
        "v_mfma_f32_16x16x32_bf16 v[120:123], a[64:67], a[216:219], v[120:123]\n"
        "v_mul_f32 v199, v192, v199\n"
        "v_cvt_pk_bf16_f32 v196, v196, v197\n"
        "v_mfma_f32_16x16x32_bf16 v[120:123], a[60:63], a[248:251], v[120:123]\n"
        "buffer_load_dwordx4 v233, s[52:55], s64 offen offset:1024 lds\n"
        "v_mfma_f32_16x16x32_bf16 v[120:123], a[56:59], a[244:247], v[120:123]\n"
        "ds_read_b128 a[224:227], v240 offset:1536\n"
        "v_cvt_pk_bf16_f32 v197, v198, v199\n"
        "v_mfma_f32_16x16x32_bf16 v[120:123], a[52:55], a[240:243], v[120:123]\n"
        "ds_read_b128 a[228:231], v241 offset:1536\n"
        "v_mul_f32 v200, v192, v200\n"
        "v_mfma_f32_16x16x32_bf16 v[120:123], a[48:51], a[236:239], v[120:123]\n"
        "ds_read_b128 a[232:235], v240 offset:1664\n"
        "v_mul_f32 v201, v192, v201\n"
        "v_cvt_pk_bf16_f32 v156, v112, v113\n"
        "v_cvt_pk_bf16_f32 v157, v114, v115\n"
        "v_mfma_f32_16x16x32_bf16 v[112:115], a[236:239], a[144:147], 0\n"
        "v_mul_f32 v202, v192, v202\n"
        "v_mul_f32 v203, v192, v203\n"
        "v_mfma_f32_16x16x32_bf16 v[112:115], a[240:243], a[148:151], v[112:115]\n"
        "v_cvt_pk_bf16_f32 v198, v200, v201\n"
        "v_cvt_pk_bf16_f32 v199, v202, v203\n"
        "v_mfma_f32_16x16x32_bf16 v[112:115], a[244:247], a[152:155], v[112:115]\n"
        "ds_write_b128 v245, v[196:199] offset:512\n"
        "v_mfma_f32_16x16x32_bf16 v[112:115], a[248:251], a[156:159], v[112:115]\n"
        "buffer_load_dwordx4 v234, s[52:55], s64 offen offset:2048 lds\n"
        "v_mfma_f32_16x16x32_bf16 v[112:115], a[216:219], a[160:163], v[112:115]\n"
        "v_mul_f32 v204, v192, v204\n"
        "v_mul_f32 v205, v192, v205\n"
        "v_mfma_f32_16x16x32_bf16 v[112:115], a[220:223], a[164:167], v[112:115]\n"
        "v_mul_f32 v206, v192, v206\n"
        "v_mul_f32 v207, v192, v207\n"
        "v_cvt_pk_bf16_f32 v158, v116, v117\n"
        "v_cvt_pk_bf16_f32 v159, v118, v119\n"
        "v_mfma_f32_16x16x32_bf16 v[116:119], a[220:223], a[188:191], 0\n"
        "s_add_u32 m0, s65, 6144\n"
        "v_cvt_pk_bf16_f32 v204, v204, v205\n"
        "v_cvt_pk_bf16_f32 v205, v206, v207\n"
        "v_mfma_f32_16x16x32_bf16 v[116:119], a[216:219], a[184:187], v[116:119]\n"
        "ds_write_b64 v246, v[204:205] offset:512\n"
        "s_mov_b32 s64, 36864\n"
        "v_mfma_f32_16x16x32_bf16 v[116:119], a[248:251], a[180:183], v[116:119]\n"
        "buffer_load_dwordx4 v232, s[52:55], s64 offen lds\n"
        "v_mfma_f32_16x16x32_bf16 v[116:119], a[244:247], a[176:179], v[116:119]\n"
        "v_max3_u32 v236, v236, v192, v193\n"
        "v_max3_u32 v236, v236, v124, v125\n"
        "v_mfma_f32_16x16x32_bf16 v[116:119], a[240:243], a[172:175], v[116:119]\n"
        "v_cmp_lt_u32 vcc, s24, v236\n"
        "v_cvt_pk_bf16_f32 v160, v120, v121\n"
        "v_mfma_f32_16x16x32_bf16 v[116:119], a[236:239], a[168:171], v[116:119]\n"
        "s_cmp_lg_u64 vcc, 0\n"
        "s_addc_u32 s25, s25, s25\n"
        "v_cvt_pk_bf16_f32 v161, v122, v123\n"
        "v_cvt_pk_bf16_f32 v166, v120, v121\n"
        "v_cvt_pk_bf16_f32 v167, v122, v123\n"
        "v_mfma_f32_16x16x32_bf16 v[120:123], a[236:239], a[192:195], 0\n"
        "ds_read_b128 a[236:239], v241 offset:1664\n"
        "v_cvt_pk_bf16_f32 v128, v112, v113\n"
        "v_mfma_f32_16x16x32_bf16 v[120:123], a[240:243], a[196:199], v[120:123]\n"
        "ds_read_b128 a[240:243], v240 offset:1792\n"
        "v_cvt_pk_bf16_f32 v129, v114, v115\n"
        "v_mfma_f32_16x16x32_bf16 v[120:123], a[244:247], a[200:203], v[120:123]\n"
        "ds_read_b128 a[244:247], v241 offset:1792\n"
        "s_waitcnt vmcnt(10)\n"
        "ds_read_b64_tr_b16 v[212:213], v255 offset:12288\n"
        "v_mfma_f32_16x16x32_bf16 v[120:123], a[248:251], a[204:207], v[120:123]\n"
        "buffer_load_dwordx4 v233, s[52:55], s64 offen offset:1024 lds\n"
        "v_mfma_f32_16x16x32_bf16 v[120:123], a[216:219], a[208:211], v[120:123]\n"
        "ds_read_b64_tr_b16 v[214:215], v255 offset:13056\n"
        "ds_read_b64_tr_b16 v[216:217], v255 offset:12320\n"
        "v_mfma_f32_16x16x32_bf16 v[120:123], a[220:223], a[212:215], v[120:123]\n"
        "s_waitcnt lgkmcnt(10)\n"
        "ds_read_b64_tr_b16 v[218:219], v255 offset:13088\n"
        "v_cvt_pk_bf16_f32 v136, v116, v117\n"
        "v_mfma_f32_16x16x32_bf16 v[112:115], a[72:75], a[224:227], 0\n"
        "s_waitcnt lgkmcnt(10)\n"
        "ds_read_b64_tr_b16 v[220:221], v255 offset:12352\n"
        "v_cvt_pk_bf16_f32 v137, v118, v119\n"
        "v_mfma_f32_16x16x32_bf16 v[112:115], a[76:79], a[228:231], v[112:115]\n"
        "s_waitcnt lgkmcnt(10)\n"
        "ds_read_b64_tr_b16 v[222:223], v255 offset:13120\n"
        "v_xor_b32 v251, 0x2000, v251\n"
        "v_mfma_f32_16x16x32_bf16 v[112:115], a[80:83], a[232:235], v[112:115]\n"
        "s_waitcnt lgkmcnt(8)\n"
        "buffer_load_dwordx4 v234, s[52:55], s64 offen offset:2048 lds\n"
        "v_mfma_f32_16x16x32_bf16 v[112:115], a[84:87], a[236:239], v[112:115]\n"
        "s_waitcnt lgkmcnt(7)\n"
        "v_xor_b32 v252, 0x2000, v252\n"
        "v_cvt_pk_bf16_f32 v144, v120, v121\n"
        "v_mfma_f32_16x16x32_bf16 v[112:115], a[88:91], a[240:243], v[112:115]\n"
        "s_waitcnt lgkmcnt(6)\n"
        "v_cvt_pk_bf16_f32 v145, v122, v123\n"
        "v_add_u32 v247, s45, v247\n"
        "v_mfma_f32_16x16x32_bf16 v[112:115], a[92:95], a[244:247], v[112:115]\n"
        "s_nop 2\n"
        "v_mfma_f32_16x16x32_bf16 v[116:119], a[116:119], a[244:247], 0\n"
        "s_add_u32 m0, s65, 9216\n"
        "s_waitcnt lgkmcnt(4)\n"
        "ds_write_b128 v235, v[212:215] offset:12288\n"
        "v_mfma_f32_16x16x32_bf16 v[116:119], a[112:115], a[240:243], v[116:119]\n"
        "s_mov_b32 s64, 55296\n"
        "v_cvt_pk_bf16_f32 v172, v112, v113\n"
        "v_cvt_pk_bf16_f32 v173, v114, v115\n"
        "v_mfma_f32_16x16x32_bf16 v[116:119], a[108:111], a[236:239], v[116:119]\n"
        "buffer_load_dwordx4 v232, s[52:55], s64 offen lds\n"
        "v_mfma_f32_16x16x32_bf16 v[116:119], a[104:107], a[232:235], v[116:119]\n"
        "v_mfma_f32_16x16x32_bf16 v[116:119], a[100:103], a[228:231], v[116:119]\n"
        "v_mfma_f32_16x16x32_bf16 v[116:119], a[96:99], a[224:227], v[116:119]\n"
        "s_waitcnt lgkmcnt(3)\n"
        "ds_write_b128 v235, v[216:219] offset:12544\n"
        "v_mfma_f32_16x16x32_bf16 v[120:123], a[120:123], a[224:227], 0\n"
        "ds_read_b128 v[162:165], v251 offset:0\n"
        "s_nop 0\n"
        "v_mfma_f32_16x16x32_bf16 v[120:123], a[124:127], a[228:231], v[120:123]\n"
        "s_nop 2\n"
        "v_mfma_f32_16x16x32_bf16 v[120:123], a[128:131], a[232:235], v[120:123]\n"
        "buffer_load_dwordx4 v233, s[52:55], s64 offen offset:1024 lds\n"
        "v_mfma_f32_16x16x32_bf16 v[120:123], a[132:135], a[236:239], v[120:123]\n"
        "s_waitcnt vmcnt(11)\n"
        "ds_read_b64_tr_b16 v[224:225], v255 offset:15360\n"
        "ds_read_b64_tr_b16 v[226:227], v255 offset:16128\n"
        "v_mfma_f32_16x16x32_bf16 v[120:123], a[136:139], a[240:243], v[120:123]\n"
        "s_waitcnt lgkmcnt(5)\n"
        "ds_write_b128 v235, v[220:223] offset:12800\n"
        "v_mfma_f32_16x16x32_bf16 v[120:123], a[140:143], a[244:247], v[120:123]\n"
        "ds_read_b64_tr_b16 v[212:213], v255 offset:15392\n"
        "ds_read_b64_tr_b16 v[214:215], v255 offset:16160\n"
        "v_mfma_f32_16x16x32_bf16 v[112:115], a[20:23], a[244:247], 0\n"
        "ds_read_b64_tr_b16 v[216:217], v255 offset:15424\n"
        "v_cvt_pk_bf16_f32 v174, v116, v117\n"
        "v_mfma_f32_16x16x32_bf16 v[112:115], a[16:19], a[240:243], v[112:115]\n"
        "ds_read_b64_tr_b16 v[218:219], v255 offset:16192\n"
        "v_cvt_pk_bf16_f32 v175, v118, v119\n"
        "v_mfma_f32_16x16x32_bf16 v[112:115], a[12:15], a[236:239], v[112:115]\n"
        "buffer_load_dwordx4 v234, s[52:55], s64 offen offset:2048 lds\n"
        "s_add_u32 s41, s30, s40\n"
        "s_add_u32 s35, s47, 786432\n"
        "s_sub_u32 s46, s35, 100663296\n"
        "s_cmp_lt_u32 s46, 786432\n"
        "s_mov_b32 m0, s48\n"
        "v_mfma_f32_16x16x32_bf16 v[112:115], a[8:11], a[232:235], v[112:115]\n"
        "s_cselect_b32 s35, s46, s35\n"
        "buffer_load_dwordx4 v249, s[12:15], s36 offen offset:0 lds\n"
        "v_mfma_f32_16x16x32_bf16 v[112:115], a[4:7], a[228:231], v[112:115]\n"
        "ds_read_b128 v[182:185], v251 offset:512\n"
        "v_cvt_pk_bf16_f32 v190, v120, v121\n"
        "v_mfma_f32_16x16x32_bf16 v[112:115], a[0:3], a[224:227], v[112:115]\n"
        "v_cvt_pk_bf16_f32 v191, v122, v123\n"
        "s_nop 0\n"
        "v_mfma_f32_16x16x32_bf16 v[116:119], a[24:27], a[224:227], 0\n"
        "s_waitcnt lgkmcnt(6)\n"
        "ds_write_b128 v235, v[224:227] offset:15360\n"
        "v_mfma_f32_16x16x32_bf16 v[116:119], a[28:31], a[228:231], v[116:119]\n"
        "s_nop 2\n"
        "v_mfma_f32_16x16x32_bf16 v[116:119], a[32:35], a[232:235], v[116:119]\n"
        "v_cvt_pk_bf16_f32 v176, v112, v113\n"
        "v_cvt_pk_bf16_f32 v177, v114, v115\n"
        "v_mfma_f32_16x16x32_bf16 v[116:119], a[36:39], a[236:239], v[116:119]\n"
        "buffer_load_dwordx4 v253, s[12:15], s36 offen offset:1024 lds\n"
        "v_mfma_f32_16x16x32_bf16 v[116:119], a[40:43], a[240:243], v[116:119]\n"
        "v_mfma_f32_16x16x32_bf16 v[116:119], a[44:47], a[244:247], v[116:119]\n"
        "s_waitcnt lgkmcnt(4)\n"
        "ds_write_b128 v235, v[212:215] offset:15616\n"
        "v_mfma_f32_16x16x32_bf16 v[120:123], a[68:71], a[244:247], 0\n"
        "s_nop 2\n"
        "v_mfma_f32_16x16x32_bf16 v[120:123], a[64:67], a[240:243], v[120:123]\n"
        "s_nop 1\n"
        "v_cvt_pk_bf16_f32 v178, v116, v117\n"
        "v_mfma_f32_16x16x32_bf16 v[120:123], a[60:63], a[236:239], v[120:123]\n"
        "v_cvt_pk_bf16_f32 v179, v118, v119\n"
        "v_mfma_f32_16x16x32_bf16 v[120:123], a[56:59], a[232:235], v[120:123]\n"
        "v_mfma_f32_16x16x32_bf16 v[120:123], a[52:55], a[228:231], v[120:123]\n"
        "s_waitcnt lgkmcnt(3)\n"
        "ds_write_b128 v235, v[216:219] offset:15872\n"
        "v_mfma_f32_16x16x32_bf16 v[120:123], a[48:51], a[224:227], v[120:123]\n"
        "s_waitcnt vmcnt(11)\n"
        "ds_read_b64_tr_b16 v[220:221], v255 offset:0\n"
        "ds_read_b64_tr_b16 v[222:223], v255 offset:768\n"
        "v_mfma_f32_16x16x32_bf16 v[112:115], a[224:227], a[144:147], 0\n"
        "ds_read_b64_tr_b16 v[224:225], v255 offset:32\n"
        "s_nop 0\n"
        "v_mfma_f32_16x16x32_bf16 v[112:115], a[228:231], a[148:151], v[112:115]\n"
        "ds_read_b64_tr_b16 v[226:227], v255 offset:800\n"
        "s_nop 0\n"
        "v_mfma_f32_16x16x32_bf16 v[112:115], a[232:235], a[152:155], v[112:115]\n"
        "ds_read_b64_tr_b16 v[212:213], v255 offset:64\n"
        "v_cvt_pk_bf16_f32 v180, v120, v121\n"
        "v_mfma_f32_16x16x32_bf16 v[112:115], a[236:239], a[156:159], v[112:115]\n"
        "ds_read_b64_tr_b16 v[214:215], v255 offset:832\n"
        "s_add_u32 m0, s41, 0\n"
        "v_cvt_pk_bf16_f32 v181, v122, v123\n"
        "v_mfma_f32_16x16x32_bf16 v[112:115], a[240:243], a[160:163], v[112:115]\n"
        "ds_read_b128 a[248:251], v240 offset:12288\n"
        "v_cvt_pk_bf16_f32 v186, v120, v121\n"
        "v_mfma_f32_16x16x32_bf16 v[112:115], a[244:247], a[164:167], v[112:115]\n"
        "buffer_load_dwordx4 v242, s[8:11], s47 offen lds\n"
        "v_mfma_f32_16x16x32_bf16 v[96:99], v[152:155], v[156:159], v[0:3]\n"
        "ds_read_b128 a[216:219], v241 offset:12288\n"
        "v_cvt_pk_bf16_f32 v187, v122, v123\n"
        "v_mfma_f32_16x16x32_bf16 v[96:99], v[168:171], v[160:163], v[96:99]\n"
        "ds_read_b128 a[220:223], v240 offset:12416\n"
        "s_sub_u32 s31, s30, s28\n"
        "s_nop 0\n"
        "v_mfma_f32_16x16x32_bf16 v[100:103], v[172:175], v[156:159], v[4:7]\n"
        "s_waitcnt lgkmcnt(7)\n"
        "ds_write_b128 v235, v[220:223] offset:0\n"
        "s_sub_u32 s45, s28, s29\n"
        "v_mfma_f32_16x16x32_bf16 v[100:103], v[188:191], v[164:167], v[100:103]\n"
        "s_add_u32 s28, s28, s31\n"
        "ds_read_b128 v[162:165], v252 offset:0\n"
        "v_cvt_pk_bf16_f32 v130, v112, v113\n"
        "v_mfma_f32_16x16x32_bf16 v[116:119], a[244:247], a[188:191], 0\n"
        "s_mov_b32 s30, s29\n"
        "v_cvt_pk_bf16_f32 v131, v114, v115\n"
        "v_mfma_f32_16x16x32_bf16 v[116:119], a[240:243], a[184:187], v[116:119]\n"
        "s_add_u32 s29, s29, s45\n"
        "v_exp_f32 v96, v96\n"
        "v_mfma_f32_16x16x32_bf16 v[116:119], a[236:239], a[180:183], v[116:119]\n"
        "v_exp_f32 v97, v97\n"
        "v_mfma_f32_16x16x32_bf16 v[116:119], a[232:235], a[176:179], v[116:119]\n"
        "s_waitcnt lgkmcnt(7)\n"
        "ds_write_b128 v235, v[224:227] offset:256\n"
        "v_mfma_f32_16x16x32_bf16 v[116:119], a[228:231], a[172:175], v[116:119]\n"
        "v_exp_f32 v98, v98\n"
        "v_mfma_f32_16x16x32_bf16 v[116:119], a[224:227], a[168:171], v[116:119]\n"
        "v_exp_f32 v100, v100\n"
        "v_mfma_f32_16x16x32_bf16 v[104:107], v[152:155], v[176:179], v[16:19]\n"
        "v_exp_f32 v99, v99\n"
        "v_mfma_f32_16x16x32_bf16 v[104:107], v[168:171], v[180:183], v[104:107]\n"
        "s_add_u32 m0, s41, 1024\n"
        "v_cvt_pk_bf16_f32 v64, v96, v97\n"
        "v_cvt_pk_bf16_f32 v65, v98, v99\n"
        "v_mfma_f32_16x16x32_bf16 v[108:111], v[172:175], v[176:179], v[20:23]\n"
        "s_waitcnt lgkmcnt(6)\n"
        "ds_write_b128 v235, v[212:215] offset:512\n"
        "v_mfma_f32_16x16x32_bf16 v[108:111], v[188:191], v[184:187], v[108:111]\n"
        "s_waitcnt vmcnt(9)\n"
        "ds_read_b64_tr_b16 v[216:217], v255 offset:3072\n"
        "ds_read_b64_tr_b16 v[218:219], v255 offset:3840\n"
        "v_mfma_f32_16x16x32_bf16 v[120:123], a[224:227], a[192:195], 0\n"
        "ds_read_b128 a[224:227], v241 offset:12416\n"
        "ds_read_b64_tr_b16 v[220:221], v255 offset:3104\n"
        "v_mfma_f32_16x16x32_bf16 v[120:123], a[228:231], a[196:199], v[120:123]\n"
        "buffer_load_dwordx4 v243, s[8:11], s47 offen lds\n"
        "v_mfma_f32_16x16x32_bf16 v[120:123], a[232:235], a[200:203], v[120:123]\n"
        "ds_read_b128 a[228:231], v240 offset:12544\n"
        "ds_read_b64_tr_b16 v[222:223], v255 offset:3872\n"
        "v_mfma_f32_16x16x32_bf16 v[120:123], a[236:239], a[204:207], v[120:123]\n"
        "ds_read_b128 a[232:235], v241 offset:12544\n"
        "ds_read_b64_tr_b16 v[224:225], v255 offset:3136\n"
        "v_mfma_f32_16x16x32_bf16 v[120:123], a[240:243], a[208:211], v[120:123]\n"
        "ds_read_b64_tr_b16 v[226:227], v255 offset:3904\n"
        "v_cvt_pk_bf16_f32 v138, v116, v117\n"
        "v_mfma_f32_16x16x32_bf16 v[120:123], a[244:247], a[212:215], v[120:123]\n"
        "s_waitcnt lgkmcnt(15)\n"
        "ds_read_b128 v[202:205], v251 offset:4096\n"
        "v_cvt_pk_bf16_f32 v139, v118, v119\n"
        "v_mfma_f32_16x16x32_bf16 v[112:115], a[0:3], a[248:251], 0\n"
        "s_waitcnt lgkmcnt(15)\n"
        "ds_read_b128 v[182:185], v252 offset:512\n"
        "s_nop 0\n"
        "v_mfma_f32_16x16x32_bf16 v[112:115], a[4:7], a[216:219], v[112:115]\n"
        "s_waitcnt lgkmcnt(15)\n"
        "v_exp_f32 v101, v101\n"
        "v_mfma_f32_16x16x32_bf16 v[112:115], a[8:11], a[220:223], v[112:115]\n"
        "s_waitcnt lgkmcnt(8)\n"
        "s_waitcnt lgkmcnt(9)\n"
        "ds_write_b128 v235, v[216:219] offset:3072\n"
        "v_mfma_f32_16x16x32_bf16 v[112:115], a[12:15], a[224:227], v[112:115]\n"
        "s_waitcnt lgkmcnt(7)\n"
        "v_exp_f32 v102, v102\n"
        "v_mfma_f32_16x16x32_bf16 v[112:115], a[16:19], a[228:231], v[112:115]\n"
        "s_waitcnt lgkmcnt(5)\n"
        "v_cvt_pk_bf16_f32 v66, v100, v101\n"
        "v_cvt_pk_bf16_f32 v146, v120, v121\n"
        "v_mfma_f32_16x16x32_bf16 v[112:115], a[20:23], a[232:235], v[112:115]\n"
        "s_add_u32 m0, s41, 2048\n"
        "v_exp_f32 v103, v103\n"
        "v_mfma_f32_16x16x32_bf16 v[116:119], a[44:47], a[232:235], 0\n"
        "v_cvt_pk_bf16_f32 v67, v102, v103\n"
        "v_cvt_pk_bf16_f32 v147, v122, v123\n"
        "v_mfma_f32_16x16x32_bf16 v[116:119], a[40:43], a[228:231], v[116:119]\n"
        "buffer_load_dwordx4 v244, s[8:11], s47 offen lds\n"
        "v_mfma_f32_16x16x32_bf16 v[116:119], a[36:39], a[224:227], v[116:119]\n"
        "ds_write_b128 v235, v[220:223] offset:3328\n"
        "v_mfma_f32_16x16x32_bf16 v[116:119], a[32:35], a[220:223], v[116:119]\n"
        "v_exp_f32 v104, v104\n"
        "v_mfma_f32_16x16x32_bf16 v[116:119], a[28:31], a[216:219], v[116:119]\n"
        "v_exp_f32 v105, v105\n"
        "v_mfma_f32_16x16x32_bf16 v[116:119], a[24:27], a[248:251], v[116:119]\n"
        "v_exp_f32 v106, v106\n"
        "v_mfma_f32_16x16x32_bf16 v[120:123], a[48:51], a[248:251], 0\n"
        "s_waitcnt lgkmcnt(4)\n"
        "ds_write_b128 v235, v[224:227] offset:3584\n"
        "v_mfma_f32_16x16x32_bf16 v[120:123], a[52:55], a[216:219], v[120:123]\n"
        "v_cvt_pk_bf16_f32 v72, v104, v105\n"
        "v_cvt_pk_bf16_f32 v196, v112, v113\n"
        "v_mfma_f32_16x16x32_bf16 v[120:123], a[56:59], a[220:223], v[120:123]\n"
        "v_exp_f32 v108, v108\n"
        "v_mfma_f32_16x16x32_bf16 v[120:123], a[60:63], a[224:227], v[120:123]\n"
        "v_exp_f32 v107, v107\n"
        "v_mfma_f32_16x16x32_bf16 v[120:123], a[64:67], a[228:231], v[120:123]\n"
        "v_exp_f32 v109, v109\n"
        "v_mfma_f32_16x16x32_bf16 v[120:123], a[68:71], a[232:235], v[120:123]\n"
        "s_waitcnt vmcnt(8)\n"
        "ds_read_b64_tr_b16 v[212:213], v255 offset:6144\n"
        "v_cvt_pk_bf16_f32 v73, v106, v107\n"
        "v_cvt_pk_bf16_f32 v197, v114, v115\n"
        "v_mfma_f32_16x16x32_bf16 v[112:115], a[92:95], a[232:235], 0\n"
        "ds_read_b64_tr_b16 v[214:215], v255 offset:6912\n"
        "v_cvt_pk_bf16_f32 v74, v108, v109\n"
        "v_mfma_f32_16x16x32_bf16 v[112:115], a[88:91], a[228:231], v[112:115]\n"
        "ds_read_b64_tr_b16 v[216:217], v255 offset:6176\n"
        "s_add_u32 m0, s41, 12288\n"
        "v_cvt_pk_bf16_f32 v198, v116, v117\n"
        "v_mfma_f32_16x16x32_bf16 v[112:115], a[84:87], a[224:227], v[112:115]\n"
        "ds_read_b64_tr_b16 v[218:219], v255 offset:6944\n"
        "v_cvt_pk_bf16_f32 v199, v118, v119\n"
        "v_mfma_f32_16x16x32_bf16 v[112:115], a[80:83], a[220:223], v[112:115]\n"
        "ds_read_b64_tr_b16 v[220:221], v255 offset:6208\n"
        "v_cvt_pk_bf16_f32 v200, v120, v121\n"
        "v_mfma_f32_16x16x32_bf16 v[112:115], a[76:79], a[216:219], v[112:115]\n"
        "buffer_load_dwordx4 v242, s[8:11], s35 offen lds\n"
        "v_mfma_f32_16x16x32_bf16 v[112:115], a[72:75], a[248:251], v[112:115]\n"
        "ds_read_b64_tr_b16 v[222:223], v255 offset:6976\n"
        "v_cvt_pk_bf16_f32 v201, v122, v123\n"
        "v_mfma_f32_16x16x32_bf16 v[96:99], v[152:155], v[196:199], v[32:35]\n"
        "s_waitcnt lgkmcnt(10)\n"
        "v_exp_f32 v110, v110\n"
        "v_mfma_f32_16x16x32_bf16 v[96:99], v[168:171], v[200:203], v[96:99]\n"
        "v_exp_f32 v111, v111\n"
        "v_mfma_f32_16x16x32_bf16 v[100:103], v[172:175], v[196:199], v[36:39]\n"
        "v_cvt_pk_bf16_f32 v75, v110, v111\n"
        "v_cvt_pk_bf16_f32 v206, v120, v121\n"
        "v_cvt_pk_bf16_f32 v207, v122, v123\n"
        "s_nop 1\n"
        "v_mfma_f32_16x16x32_bf16 v[100:103], v[188:191], v[204:207], v[100:103]\n"
        "s_waitcnt lgkmcnt(4)\n"
        "ds_write_b128 v235, v[212:215] offset:6144\n"
        "s_cmp_eq_u32 s37, 100663296\n"
        "v_mfma_f32_16x16x32_bf16 v[116:119], a[96:99], a[248:251], 0\n"
        "s_cselect_b32 s46, 0, s37\n"
        "ds_read_b128 v[202:205], v252 offset:4096\n"
        "v_cvt_pk_bf16_f32 v192, v112, v113\n"
        "v_mfma_f32_16x16x32_bf16 v[116:119], a[100:103], a[216:219], v[116:119]\n"
        "s_add_u32 s38, s46, s43\n"
        "v_cvt_pk_bf16_f32 v193, v114, v115\n"
        "v_add_u32 v245, s45, v245\n"
        "v_mfma_f32_16x16x32_bf16 v[116:119], a[104:107], a[220:223], v[116:119]\n"
        "s_add_u32 s39, s46, s44\n"
        "v_exp_f32 v96, v96\n"
        "v_mfma_f32_16x16x32_bf16 v[116:119], a[108:111], a[224:227], v[116:119]\n"
        "s_add_u32 s37, s37, 6291456\n"
        "v_exp_f32 v97, v97\n"
        "v_mfma_f32_16x16x32_bf16 v[116:119], a[112:115], a[228:231], v[116:119]\n"
        "s_waitcnt lgkmcnt(4)\n"
        "ds_write_b128 v235, v[216:219] offset:6400\n"
        "v_mfma_f32_16x16x32_bf16 v[116:119], a[116:119], a[232:235], v[116:119]\n"
        "s_add_u32 m0, s41, 13312\n"
        "v_exp_f32 v98, v98\n"
        "v_mfma_f32_16x16x32_bf16 v[120:123], a[140:143], a[232:235], 0\n"
        "v_exp_f32 v100, v100\n"
        "v_mfma_f32_16x16x32_bf16 v[120:123], a[136:139], a[228:231], v[120:123]\n"
        "buffer_load_dwordx4 v243, s[8:11], s35 offen lds\n"
        "v_mfma_f32_16x16x32_bf16 v[120:123], a[132:135], a[224:227], v[120:123]\n"
        "v_cvt_pk_bf16_f32 v80, v96, v97\n"
        "v_cvt_pk_bf16_f32 v194, v116, v117\n"
        "v_mfma_f32_16x16x32_bf16 v[120:123], a[128:131], a[220:223], v[120:123]\n"
        "s_waitcnt lgkmcnt(3)\n"
        "ds_write_b128 v235, v[220:223] offset:6656\n"
        "v_mfma_f32_16x16x32_bf16 v[120:123], a[124:127], a[216:219], v[120:123]\n"
        "s_waitcnt vmcnt(7)\n"
        "ds_read_b64_tr_b16 v[224:225], v255 offset:9216\n"
        "ds_read_b64_tr_b16 v[226:227], v255 offset:9984\n"
        "v_mfma_f32_16x16x32_bf16 v[120:123], a[120:123], a[248:251], v[120:123]\n"
        "ds_read_b64_tr_b16 v[212:213], v255 offset:9248\n"
        "v_cvt_pk_bf16_f32 v195, v118, v119\n"
        "v_mfma_f32_16x16x32_bf16 v[112:115], a[248:251], a[144:147], 0\n"
        "ds_read_b64_tr_b16 v[214:215], v255 offset:10016\n"
        "v_add_u32 v246, s45, v246\n"
        "v_mfma_f32_16x16x32_bf16 v[112:115], a[216:219], a[148:151], v[112:115]\n"
        "ds_read_b64_tr_b16 v[216:217], v255 offset:9280\n"
        "s_nop 0\n"
        "v_mfma_f32_16x16x32_bf16 v[112:115], a[220:223], a[152:155], v[112:115]\n"
        "ds_read_b64_tr_b16 v[218:219], v255 offset:10048\n"
        "v_cvt_pk_bf16_f32 v208, v120, v121\n"
        "v_mfma_f32_16x16x32_bf16 v[112:115], a[224:227], a[156:159], v[112:115]\n"
        "ds_read_b128 a[236:239], v240 offset:13824\n"
        "v_cvt_pk_bf16_f32 v209, v122, v123\n"
        "v_mfma_f32_16x16x32_bf16 v[112:115], a[228:231], a[160:163], v[112:115]\n"
        "ds_read_b128 a[240:243], v241 offset:13824\n"
        "v_mfma_f32_16x16x32_bf16 v[112:115], a[232:235], a[164:167], v[112:115]\n"
        "ds_read_b128 a[244:247], v240 offset:13952\n"
        "s_nop 0\n"
        "v_mfma_f32_16x16x32_bf16 v[104:107], v[192:195], v[156:159], v[8:11]\n"
        "v_exp_f32 v99, v99\n"
        "v_mfma_f32_16x16x32_bf16 v[104:107], v[208:211], v[160:163], v[104:107]\n"
        "s_add_u32 m0, s41, 14336\n"
        "s_waitcnt lgkmcnt(7)\n"
        "ds_write_b128 v235, v[224:227] offset:9216\n"
        "v_mfma_f32_16x16x32_bf16 v[116:119], a[232:235], a[188:191], 0\n"
        "v_exp_f32 v101, v101\n"
        "v_mfma_f32_16x16x32_bf16 v[116:119], a[228:231], a[184:187], v[116:119]\n"
        "buffer_load_dwordx4 v244, s[8:11], s35 offen lds\n"
        "v_mfma_f32_16x16x32_bf16 v[116:119], a[224:227], a[180:183], v[116:119]\n"
        "v_cvt_pk_bf16_f32 v81, v98, v99\n"
        "v_cvt_pk_bf16_f32 v82, v100, v101\n"
        "v_mfma_f32_16x16x32_bf16 v[116:119], a[220:223], a[176:179], v[116:119]\n"
        "v_exp_f32 v102, v102\n"
        "v_mfma_f32_16x16x32_bf16 v[116:119], a[216:219], a[172:175], v[116:119]\n"
        "s_waitcnt lgkmcnt(6)\n"
        "ds_write_b128 v235, v[212:215] offset:9472\n"
        "v_mfma_f32_16x16x32_bf16 v[116:119], a[248:251], a[168:171], v[116:119]\n"
        "v_exp_f32 v103, v103\n"
        "v_mfma_f32_16x16x32_bf16 v[108:111], v[192:195], v[176:179], v[24:27]\n"
        "v_cvt_pk_bf16_f32 v83, v102, v103\n"
        "v_cvt_pk_bf16_f32 v132, v112, v113\n"
        "v_mfma_f32_16x16x32_bf16 v[108:111], v[208:211], v[180:183], v[108:111]\n"
        "v_cvt_pk_bf16_f32 v133, v114, v115\n"
        "s_nop 0\n"
        "v_mfma_f32_16x16x32_bf16 v[120:123], a[248:251], a[192:195], 0\n"
        "ds_read_b128 a[248:251], v241 offset:13952\n"
        "v_cvt_pk_bf16_f32 v140, v116, v117\n"
        "v_mfma_f32_16x16x32_bf16 v[120:123], a[216:219], a[196:199], v[120:123]\n"
        "s_waitcnt lgkmcnt(6)\n"
        "ds_write_b128 v235, v[216:219] offset:9728\n"
        "v_mfma_f32_16x16x32_bf16 v[120:123], a[220:223], a[200:203], v[120:123]\n"
        "ds_read_b128 a[216:219], v240 offset:14080\n"
        "v_cvt_pk_bf16_f32 v141, v118, v119\n"
        "v_mfma_f32_16x16x32_bf16 v[120:123], a[224:227], a[204:207], v[120:123]\n"
        "ds_read_b128 a[220:223], v241 offset:14080\n"
        "v_mfma_f32_16x16x32_bf16 v[120:123], a[228:231], a[208:211], v[120:123]\n"
        "v_exp_f32 v104, v104\n"
        "v_mfma_f32_16x16x32_bf16 v[120:123], a[232:235], a[212:215], v[120:123]\n"
        "v_exp_f32 v105, v105\n"
        "v_mfma_f32_16x16x32_bf16 v[96:99], v[192:195], v[196:199], v[40:43]\n"
        "v_exp_f32 v106, v106\n"
        "v_mfma_f32_16x16x32_bf16 v[96:99], v[208:211], v[200:203], v[96:99]\n"
        "s_waitcnt lgkmcnt(8)\n"
        "v_cvt_pk_bf16_f32 v68, v104, v105\n"
        "s_nop 0\n"
        "v_mfma_f32_16x16x32_bf16 v[112:115], a[72:75], a[236:239], 0\n"
        "s_waitcnt lgkmcnt(7)\n"
        "v_exp_f32 v107, v107\n"
        "v_mfma_f32_16x16x32_bf16 v[112:115], a[76:79], a[240:243], v[112:115]\n"
        "s_waitcnt lgkmcnt(6)\n"
        "v_cvt_pk_bf16_f32 v69, v106, v107\n"
        "v_cvt_pk_bf16_f32 v148, v120, v121\n"
        "v_mfma_f32_16x16x32_bf16 v[112:115], a[80:83], a[244:247], v[112:115]\n"
        "s_waitcnt lgkmcnt(3)\n"
        "v_exp_f32 v108, v108\n"
        "v_mfma_f32_16x16x32_bf16 v[112:115], a[84:87], a[248:251], v[112:115]\n"
        "s_waitcnt lgkmcnt(1)\n"
        "v_cvt_pk_bf16_f32 v149, v122, v123\n"
        "v_mfma_f32_16x16x32_bf16 v[112:115], a[88:91], a[216:219], v[112:115]\n"
        "s_waitcnt lgkmcnt(0)\n"
        "v_exp_f32 v109, v109\n"
        "v_mfma_f32_16x16x32_bf16 v[112:115], a[92:95], a[220:223], v[112:115]\n"
        "v_exp_f32 v110, v110\n"
        "v_mfma_f32_16x16x32_bf16 v[116:119], a[116:119], a[220:223], 0\n"
        "v_cvt_pk_bf16_f32 v76, v108, v109\n"
        "v_add_u32 v240, s31, v240\n"
        "v_mfma_f32_16x16x32_bf16 v[116:119], a[112:115], a[216:219], v[116:119]\n"
        "v_exp_f32 v111, v111\n"
        "v_mfma_f32_16x16x32_bf16 v[116:119], a[108:111], a[248:251], v[116:119]\n"
        "v_cvt_pk_bf16_f32 v77, v110, v111\n"
        "v_cvt_pk_bf16_f32 v212, v112, v113\n"
        "v_mfma_f32_16x16x32_bf16 v[116:119], a[104:107], a[244:247], v[116:119]\n"
        "v_cvt_pk_bf16_f32 v213, v114, v115\n"
        "v_add_u32 v241, s31, v241\n"
        "v_mfma_f32_16x16x32_bf16 v[116:119], a[100:103], a[240:243], v[116:119]\n"
        "v_exp_f32 v96, v96\n"
        "v_mfma_f32_16x16x32_bf16 v[116:119], a[96:99], a[236:239], v[116:119]\n"
        "v_exp_f32 v97, v97\n"
        "v_mfma_f32_16x16x32_bf16 v[120:123], a[120:123], a[236:239], 0\n"
        "v_exp_f32 v98, v98\n"
        "v_mfma_f32_16x16x32_bf16 v[120:123], a[124:127], a[240:243], v[120:123]\n"
        "s_add_u32 s47, s47, 6291456\n"
        "v_cvt_pk_bf16_f32 v84, v96, v97\n"
        "s_nop 0\n"
        "v_mfma_f32_16x16x32_bf16 v[120:123], a[128:131], a[244:247], v[120:123]\n"
        "s_add_u32 s36, s36, 2097152\n"
        "v_cvt_pk_bf16_f32 v214, v116, v117\n"
        "v_cvt_pk_bf16_f32 v215, v118, v119\n"
        "v_mfma_f32_16x16x32_bf16 v[120:123], a[132:135], a[248:251], v[120:123]\n"
        "s_xor_b32 s48, s48, 8192\n"
        "v_exp_f32 v99, v99\n"
        "v_mfma_f32_16x16x32_bf16 v[120:123], a[136:139], a[216:219], v[120:123]\n"
        "v_cvt_pk_bf16_f32 v85, v98, v99\n"
        "v_mfma_f32_16x16x32_bf16 v[120:123], a[140:143], a[220:223], v[120:123]\n"
        "s_nop 2\n"
        "v_mfma_f32_16x16x32_bf16 v[112:115], a[220:223], a[164:167], 0\n"
        "s_nop 2\n"
        "v_mfma_f32_16x16x32_bf16 v[112:115], a[216:219], a[160:163], v[112:115]\n"
        "v_cvt_pk_bf16_f32 v230, v120, v121\n"
        "v_cvt_pk_bf16_f32 v231, v122, v123\n"
        "v_mfma_f32_16x16x32_bf16 v[112:115], a[248:251], a[156:159], v[112:115]\n"
        "s_barrier\n"
        "v_mfma_f32_16x16x32_bf16 v[112:115], a[244:247], a[152:155], v[112:115]\n"
        "v_mfma_f32_16x16x32_bf16 v[112:115], a[240:243], a[148:151], v[112:115]\n"
        "v_mfma_f32_16x16x32_bf16 v[112:115], a[236:239], a[144:147], v[112:115]\n"
        "s_nop 2\n"
        "v_mfma_f32_16x16x32_bf16 v[100:103], v[212:215], v[156:159], v[12:15]\n"
        "s_nop 2\n"
        "v_mfma_f32_16x16x32_bf16 v[100:103], v[228:231], v[164:167], v[100:103]\n"
        "v_cvt_pk_bf16_f32 v134, v112, v113\n"
        "v_cvt_pk_bf16_f32 v135, v114, v115\n"
        "v_mfma_f32_16x16x32_bf16 v[116:119], a[236:239], a[168:171], 0\n"
        "v_mfma_f32_16x16x32_bf16 v[116:119], a[240:243], a[172:175], v[116:119]\n"
        "v_mfma_f32_16x16x32_bf16 v[116:119], a[244:247], a[176:179], v[116:119]\n"
        "v_mfma_f32_16x16x32_bf16 v[116:119], a[248:251], a[180:183], v[116:119]\n"
        "v_mfma_f32_16x16x32_bf16 v[116:119], a[216:219], a[184:187], v[116:119]\n"
        "s_nop 0\n"
        "v_exp_f32 v100, v100\n"
        "v_mfma_f32_16x16x32_bf16 v[116:119], a[220:223], a[188:191], v[116:119]\n"
        "v_exp_f32 v101, v101\n"
        "v_mfma_f32_16x16x32_bf16 v[104:107], v[212:215], v[176:179], v[28:31]\n"
        "v_exp_f32 v102, v102\n"
        "v_mfma_f32_16x16x32_bf16 v[104:107], v[228:231], v[184:187], v[104:107]\n"
        "v_exp_f32 v103, v103\n"
        "v_mfma_f32_16x16x32_bf16 v[120:123], a[220:223], a[212:215], 0\n"
        "v_cvt_pk_bf16_f32 v70, v100, v101\n"
        "v_cvt_pk_bf16_f32 v71, v102, v103\n"
        "v_mfma_f32_16x16x32_bf16 v[120:123], a[216:219], a[208:211], v[120:123]\n"
        "v_cvt_pk_bf16_f32 v142, v116, v117\n"
        "v_cvt_pk_bf16_f32 v143, v118, v119\n"
        "v_mfma_f32_16x16x32_bf16 v[120:123], a[248:251], a[204:207], v[120:123]\n"
        "v_mov_b64 v[96:97], 0\n"
        "v_mfma_f32_16x16x32_bf16 v[120:123], a[244:247], a[200:203], v[120:123]\n"
        "v_exp_f32 v104, v104\n"
        "v_mfma_f32_16x16x32_bf16 v[120:123], a[240:243], a[196:199], v[120:123]\n"
        "v_exp_f32 v105, v105\n"
        "v_mfma_f32_16x16x32_bf16 v[120:123], a[236:239], a[192:195], v[120:123]\n"
        "v_exp_f32 v106, v106\n"
        "v_mfma_f32_16x16x32_bf16 v[108:111], v[212:215], v[196:199], v[44:47]\n"
        "v_exp_f32 v107, v107\n"
        "v_mfma_f32_16x16x32_bf16 v[108:111], v[228:231], v[204:207], v[108:111]\n"
        "v_cvt_pk_bf16_f32 v78, v104, v105\n"
        "v_cvt_pk_bf16_f32 v79, v106, v107\n"
        "v_mfma_f32_16x16x32_bf16 v[112:115], a[0:3], a[236:239], 0\n"
        "s_nop 0\n"
        "v_cvt_pk_bf16_f32 v150, v120, v121\n"
        "v_cvt_pk_bf16_f32 v151, v122, v123\n"
        "v_mfma_f32_16x16x32_bf16 v[112:115], a[4:7], a[240:243], v[112:115]\n"
        "v_mfma_f32_16x16x32_bf16 v[112:115], a[8:11], a[244:247], v[112:115]\n"
        "v_exp_f32 v108, v108\n"
        "v_mfma_f32_16x16x32_bf16 v[112:115], a[12:15], a[248:251], v[112:115]\n"
        "v_exp_f32 v109, v109\n"
        "v_mfma_f32_16x16x32_bf16 v[112:115], a[16:19], a[216:219], v[112:115]\n"
        "v_exp_f32 v110, v110\n"
        "v_mfma_f32_16x16x32_bf16 v[112:115], a[20:23], a[220:223], v[112:115]\n"
        "v_exp_f32 v111, v111\n"
        "v_smfmac_f32_16x16x64_bf16 v[96:99], a[252:255], v[64:71], v237\n"
        "v_cvt_pk_bf16_f32 v86, v108, v109\n"
        "v_mfma_f32_16x16x32_bf16 v[100:103], v[132:135], v[68:71], 0\n"
        "v_mov_b64 v[124:125], 0\n"
        "v_cvt_pk_bf16_f32 v87, v110, v111\n"
        "v_mfma_f32_16x16x32_bf16 v[100:103], v[128:131], v[64:67], v[100:103]\n"
        "s_nop 0\n"
        "v_cvt_pk_bf16_f32 v216, v112, v113\n"
        "v_cvt_pk_bf16_f32 v217, v114, v115\n"
        "v_mfma_f32_16x16x32_bf16 v[104:107], v[136:139], v[64:67], 0\n"
        "ds_read_b128 v[222:225], v251 offset:4608\n"
        "v_add_f32 v97, v96, v97\n"
        "v_mfma_f32_16x16x32_bf16 v[104:107], v[140:143], v[68:71], v[104:107]\n"
        "v_rcp_f32 v96, v97\n"
        "v_mfma_f32_16x16x32_bf16 v[108:111], v[148:151], v[68:71], 0\n"
        "v_mul_f32 v100, v96, v100\n"
        "v_mul_f32 v101, v96, v101\n"
        "v_mfma_f32_16x16x32_bf16 v[108:111], v[144:147], v[64:67], v[108:111]\n"
        "v_mul_f32 v102, v96, v102\n"
        "v_mul_f32 v103, v96, v103\n"
        "v_mfma_f32_16x16x32_bf16 v[116:119], a[44:47], a[220:223], 0\n"
        "v_cvt_pk_bf16_f32 v100, v100, v101\n"
        "v_cvt_pk_bf16_f32 v101, v102, v103\n"
        "v_mfma_f32_16x16x32_bf16 v[116:119], a[40:43], a[216:219], v[116:119]\n"
        "v_mul_f32 v104, v96, v104\n"
        "v_mul_f32 v105, v96, v105\n"
        "v_mfma_f32_16x16x32_bf16 v[116:119], a[36:39], a[248:251], v[116:119]\n"
        "v_mul_f32 v106, v96, v106\n"
        "v_mul_f32 v107, v96, v107\n"
        "v_mfma_f32_16x16x32_bf16 v[116:119], a[32:35], a[244:247], v[116:119]\n"
        "v_cvt_pk_bf16_f32 v102, v104, v105\n"
        "v_cvt_pk_bf16_f32 v103, v106, v107\n"
        "v_mfma_f32_16x16x32_bf16 v[116:119], a[28:31], a[240:243], v[116:119]\n"
        "ds_write_b128 v245, v[100:103] offset:0\n"
        "v_mfma_f32_16x16x32_bf16 v[116:119], a[24:27], a[236:239], v[116:119]\n"
        "v_mul_f32 v108, v96, v108\n"
        "v_mul_f32 v109, v96, v109\n"
        "v_smfmac_f32_16x16x64_bf16 v[124:127], a[252:255], v[72:79], v237\n"
        "v_mul_f32 v110, v96, v110\n"
        "v_mul_f32 v111, v96, v111\n"
        "v_mfma_f32_16x16x32_bf16 v[64:67], v[132:135], v[76:79], 0\n"
        "v_cvt_pk_bf16_f32 v108, v108, v109\n"
        "v_cvt_pk_bf16_f32 v109, v110, v111\n"
        "v_mfma_f32_16x16x32_bf16 v[64:67], v[128:131], v[72:75], v[64:67]\n"
        "ds_read_b128 v[156:159], v247 offset:0\n"
        "v_cvt_pk_bf16_f32 v218, v116, v117\n"
        "v_mfma_f32_16x16x32_bf16 v[68:71], v[136:139], v[72:75], 0\n"
        "ds_write_b64 v246, v[108:109] offset:0\n"
        "v_mfma_f32_16x16x32_bf16 v[68:71], v[140:143], v[76:79], v[68:71]\n"
        "ds_read_b128 v[160:163], v247 offset:4096\n"
        "v_cvt_pk_bf16_f32 v219, v118, v119\n"
        "v_mfma_f32_16x16x32_bf16 v[120:123], v[148:151], v[76:79], 0\n"
        "ds_read_b128 v[164:167], v247 offset:4352\n"
        "v_max_u32 v236, v96, v97\n"
        "v_mfma_f32_16x16x32_bf16 v[120:123], v[144:147], v[72:75], v[120:123]\n"
        "v_mov_b64 v[116:117], 0\n"
        "v_add_f32 v125, v124, v125\n"
        "v_mfma_f32_16x16x32_bf16 v[112:115], a[48:51], a[236:239], 0\n"
        "v_rcp_f32 v124, v125\n"
        "v_mfma_f32_16x16x32_bf16 v[112:115], a[52:55], a[240:243], v[112:115]\n"
        "v_mul_f32 v64, v124, v64\n"
        "v_mul_f32 v65, v124, v65\n"
        "v_mfma_f32_16x16x32_bf16 v[112:115], a[56:59], a[244:247], v[112:115]\n"
        "v_mul_f32 v66, v124, v66\n"
        "v_mul_f32 v67, v124, v67\n"
        "v_mfma_f32_16x16x32_bf16 v[112:115], a[60:63], a[248:251], v[112:115]\n"
        "v_cvt_pk_bf16_f32 v64, v64, v65\n"
        "v_cvt_pk_bf16_f32 v65, v66, v67\n"
        "v_mfma_f32_16x16x32_bf16 v[112:115], a[64:67], a[216:219], v[112:115]\n"
        "v_mul_f32 v68, v124, v68\n"
        "v_mul_f32 v69, v124, v69\n"
        "v_mfma_f32_16x16x32_bf16 v[112:115], a[68:71], a[220:223], v[112:115]\n"
        "ds_read_b128 a[216:219], v248 offset:0\n"
        "v_mul_f32 v70, v124, v70\n"
        "v_smfmac_f32_16x16x64_bf16 v[116:119], a[252:255], v[80:87], v237\n"
        "ds_read_b128 a[220:223], v248 offset:3072\n"
        "v_mul_f32 v71, v124, v71\n"
        "v_mfma_f32_16x16x32_bf16 v[72:75], v[132:135], v[84:87], 0\n"
        "v_cvt_pk_bf16_f32 v66, v68, v69\n"
        "v_cvt_pk_bf16_f32 v67, v70, v71\n"
        "v_mfma_f32_16x16x32_bf16 v[72:75], v[128:131], v[80:83], v[72:75]\n"
        "ds_write_b128 v245, v[64:67] offset:256\n"
        "v_mfma_f32_16x16x32_bf16 v[76:79], v[136:139], v[80:83], 0\n"
        "ds_read_b128 a[224:227], v254 offset:16\n"
        "v_cvt_pk_bf16_f32 v220, v112, v113\n"
        "v_mfma_f32_16x16x32_bf16 v[76:79], v[140:143], v[84:87], v[76:79]\n"
        "ds_read_b128 a[228:231], v254 offset:3088\n"
        "v_cvt_pk_bf16_f32 v221, v114, v115\n"
        "v_mfma_f32_16x16x32_bf16 v[96:99], v[148:151], v[84:87], 0\n"
        "v_cvt_pk_bf16_f32 v226, v112, v113\n"
        "v_cvt_pk_bf16_f32 v227, v114, v115\n"
        "v_mfma_f32_16x16x32_bf16 v[96:99], v[144:147], v[80:83], v[96:99]\n"
        "v_mul_f32 v120, v124, v120\n"
        "v_mul_f32 v121, v124, v121\n"
        "v_mfma_f32_16x16x32_bf16 v[100:103], v[152:155], v[216:219], v[48:51]\n"
        "s_waitcnt lgkmcnt(10)\n"
        "ds_read_b128 v[152:155], v247 offset:256\n"
        "v_mul_f32 v122, v124, v122\n"
        "v_mfma_f32_16x16x32_bf16 v[100:103], v[168:171], v[220:223], v[100:103]\n"
        "v_mul_f32 v123, v124, v123\n"
        "v_cvt_pk_bf16_f32 v120, v120, v121\n"
        "v_mfma_f32_16x16x32_bf16 v[104:107], v[172:175], v[216:219], v[52:55]\n"
        "v_cvt_pk_bf16_f32 v121, v122, v123\n"
        "v_max3_u32 v236, v236, v124, v125\n"
        "v_mfma_f32_16x16x32_bf16 v[104:107], v[188:191], v[224:227], v[104:107]\n"
        "s_waitcnt lgkmcnt(5)\n"
        "ds_write_b64 v246, v[120:121] offset:256\n"
        "v_mfma_f32_16x16x32_bf16 v[176:179], v[156:159], a[216:219], 0\n"
        "s_waitcnt lgkmcnt(5)\n"
        "ds_read_b128 v[222:225], v252 offset:4608\n"
        "v_add_f32 v117, v116, v117\n"
        "v_mfma_f32_16x16x32_bf16 v[176:179], v[160:163], a[220:223], v[176:179]\n"
        "ds_read_b128 a[232:235], v248 offset:32\n"
        "v_mfma_f32_16x16x32_bf16 v[196:199], v[164:167], a[220:223], 0\n"
        "s_waitcnt lgkmcnt(3)\n"
        "ds_read_b128 a[220:223], v248 offset:3104\n"
        "v_mfma_f32_16x16x32_bf16 v[196:199], v[152:155], a[216:219], v[196:199]\n"
        "v_rcp_f32 v116, v117\n"
        "v_mfma_f32_16x16x32_bf16 v[200:203], v[152:155], a[224:227], 0\n"
        "ds_read_b128 a[216:219], v254 offset:48\n"
        "v_mul_f32 v72, v116, v72\n"
        "v_mfma_f32_16x16x32_bf16 v[200:203], v[164:167], a[228:231], v[200:203]\n"
        "v_exp_f32 v100, v100\n"
        "v_mfma_f32_16x16x32_bf16 v[180:183], v[160:163], a[228:231], 0\n"
        "ds_read_b128 a[228:231], v254 offset:3120\n"
        "v_mul_f32 v73, v116, v73\n"
        "v_mfma_f32_16x16x32_bf16 v[180:183], v[156:159], a[224:227], v[180:183]\n"
        "ds_read_b128 a[236:239], v247 offset:8192\n"
        "v_mul_f32 v74, v116, v74\n"
        "v_mfma_f32_16x16x32_bf16 v[108:111], v[192:195], v[216:219], v[56:59]\n"
        "s_waitcnt lgkmcnt(5)\n"
        "ds_read_b128 a[224:227], v248 offset:64\n"
        "v_mul_f32 v75, v116, v75\n"
        "v_mfma_f32_16x16x32_bf16 v[108:111], v[208:211], v[220:223], v[108:111]\n"
        "ds_read_b128 a[240:243], v247 offset:12288\n"
        "v_cvt_pk_bf16_f32 v72, v72, v73\n"
        "v_mfma_f32_16x16x32_bf16 v[112:115], v[212:215], v[216:219], v[60:63]\n"
        "ds_read_b128 a[244:247], v247 offset:12544\n"
        "v_cvt_pk_bf16_f32 v73, v74, v75\n"
        "v_mfma_f32_16x16x32_bf16 v[112:115], v[228:231], v[224:227], v[112:115]\n"
        "s_waitcnt lgkmcnt(7)\n"
        "ds_read_b128 a[248:251], v247 offset:8448\n"
        "v_mul_f32 v76, v116, v76\n"
        "v_mfma_f32_16x16x32_bf16 v[184:187], v[156:159], a[232:235], 0\n"
        "s_waitcnt lgkmcnt(7)\n"
        "v_exp_f32 v101, v101\n"
        "v_mfma_f32_16x16x32_bf16 v[184:187], v[160:163], a[220:223], v[184:187]\n"
        "v_mul_f32 v77, v116, v77\n"
        "v_mul_f32 v78, v116, v78\n"
        "v_mfma_f32_16x16x32_bf16 v[204:207], v[164:167], a[220:223], 0\n"
        "ds_read_b128 a[220:223], v248 offset:3136\n"
        "v_mul_f32 v79, v116, v79\n"
        "v_mfma_f32_16x16x32_bf16 v[204:207], v[152:155], a[232:235], v[204:207]\n"
        "s_waitcnt lgkmcnt(7)\n"
        "ds_read_b128 a[232:235], v254 offset:80\n"
        "v_cvt_pk_bf16_f32 v88, v100, v101\n"
        "v_mfma_f32_16x16x32_bf16 v[192:195], v[152:155], a[216:219], 0\n"
        "s_waitcnt lgkmcnt(7)\n"
        "v_exp_f32 v102, v102\n"
        "v_mfma_f32_16x16x32_bf16 v[192:195], v[164:167], a[228:231], v[192:195]\n"
        "v_exp_f32 v104, v104\n"
        "v_mfma_f32_16x16x32_bf16 v[172:175], v[160:163], a[228:231], 0\n"
        "ds_read_b128 a[228:231], v254 offset:3152\n"
        "v_cvt_pk_bf16_f32 v74, v76, v77\n"
        "v_mfma_f32_16x16x32_bf16 v[172:175], v[156:159], a[216:219], v[172:175]\n"
        "s_waitcnt lgkmcnt(6)\n"
        "ds_read_b128 a[216:219], v248 offset:6144\n"
        "v_cvt_pk_bf16_f32 v75, v78, v79\n"
        "v_mfma_f32_16x16x32_bf16 v[212:215], v[156:159], a[224:227], 0\n"
        "s_waitcnt lgkmcnt(3)\n"
        "ds_write_b128 v245, v[72:75] offset:512\n"
        "v_mfma_f32_16x16x32_bf16 v[212:215], v[160:163], a[220:223], v[212:215]\n"
        "v_exp_f32 v103, v103\n"
        "v_mfma_f32_16x16x32_bf16 v[216:219], v[164:167], a[220:223], 0\n"
        "ds_read_b128 a[220:223], v248 offset:9216\n"
        "v_cvt_pk_bf16_f32 v89, v102, v103\n"
        "v_mfma_f32_16x16x32_bf16 v[216:219], v[152:155], a[224:227], v[216:219]\n"
        "s_waitcnt lgkmcnt(4)\n"
        "ds_read_b128 a[224:227], v254 offset:6160\n"
        "v_mul_f32 v96, v116, v96\n"
        "v_mfma_f32_16x16x32_bf16 v[224:227], v[152:155], a[232:235], 0\n"
        "s_waitcnt lgkmcnt(4)\n"
        "v_exp_f32 v105, v105\n"
        "v_mfma_f32_16x16x32_bf16 v[224:227], v[164:167], a[228:231], v[224:227]\n"
        "v_exp_f32 v106, v106\n"
        "v_mfma_f32_16x16x32_bf16 v[220:223], v[160:163], a[228:231], 0\n"
        "ds_read_b128 a[228:231], v254 offset:9232\n"
        "v_cvt_pk_bf16_f32 v90, v104, v105\n"
        "v_mfma_f32_16x16x32_bf16 v[220:223], v[156:159], a[232:235], v[220:223]\n"
        "s_waitcnt lgkmcnt(4)\n"
        "ds_read_b128 a[232:235], v248 offset:6176\n"
        "v_mul_f32 v97, v116, v97\n"
        "v_mfma_f32_16x16x32_bf16 v[176:179], a[236:239], a[216:219], v[176:179]\n"
        "s_waitcnt lgkmcnt(3)\n"
        "v_mul_f32 v98, v116, v98\n"
        "v_mul_f32 v99, v116, v99\n"
        "v_mfma_f32_16x16x32_bf16 v[176:179], a[240:243], a[220:223], v[176:179]\n"
        "v_exp_f32 v107, v107\n"
        "v_mfma_f32_16x16x32_bf16 v[196:199], a[244:247], a[220:223], v[196:199]\n"
        "ds_read_b128 a[220:223], v248 offset:9248\n"
        "v_cvt_pk_bf16_f32 v91, v106, v107\n"
        "v_mfma_f32_16x16x32_bf16 v[196:199], a[248:251], a[216:219], v[196:199]\n"
        "s_waitcnt lgkmcnt(3)\n"
        "ds_read_b128 a[216:219], v254 offset:6192\n"
        "v_cvt_pk_bf16_f32 v96, v96, v97\n"
        "v_mfma_f32_16x16x32_bf16 v[200:203], a[248:251], a[224:227], v[200:203]\n"
        "s_waitcnt lgkmcnt(3)\n"
        "ds_read_b128 v[156:159], v247 offset:16384\n"
        "v_cvt_pk_bf16_f32 v97, v98, v99\n"
        "v_mfma_f32_16x16x32_bf16 v[200:203], a[244:247], a[228:231], v[200:203]\n"
        "ds_write_b64 v246, v[96:97] offset:512\n"
        "v_mfma_f32_16x16x32_bf16 v[180:183], a[240:243], a[228:231], v[180:183]\n"
        "ds_read_b128 a[228:231], v254 offset:9264\n"
        "v_max3_u32 v236, v236, v116, v117\n"
        "v_mfma_f32_16x16x32_bf16 v[180:183], a[236:239], a[224:227], v[180:183]\n"
        "s_waitcnt lgkmcnt(5)\n"
        "ds_read_b128 a[224:227], v248 offset:6208\n"
        "v_mfma_f32_16x16x32_bf16 v[184:187], a[236:239], a[232:235], v[184:187]\n"
        "s_waitcnt lgkmcnt(5)\n"
        "ds_read_b128 v[160:163], v247 offset:20480\n"
        "v_mfma_f32_16x16x32_bf16 v[184:187], a[240:243], a[220:223], v[184:187]\n"
        "ds_read_b128 v[164:167], v247 offset:20736\n"
        "v_mfma_f32_16x16x32_bf16 v[204:207], a[244:247], a[220:223], v[204:207]\n"
        "ds_read_b128 a[220:223], v248 offset:9280\n"
        "v_mfma_f32_16x16x32_bf16 v[204:207], a[248:251], a[232:235], v[204:207]\n"
        "s_waitcnt lgkmcnt(7)\n"
        "ds_read_b128 a[232:235], v254 offset:6224\n"
        "v_mfma_f32_16x16x32_bf16 v[192:195], a[248:251], a[216:219], v[192:195]\n"
        "s_waitcnt lgkmcnt(5)\n"
        "ds_read_b128 v[152:155], v247 offset:16640\n"
        "v_mfma_f32_16x16x32_bf16 v[192:195], a[244:247], a[228:231], v[192:195]\n"
        "v_exp_f32 v108, v108\n"
        "v_mfma_f32_16x16x32_bf16 v[172:175], a[240:243], a[228:231], v[172:175]\n"
        "ds_read_b128 a[228:231], v254 offset:9296\n"
        "v_mfma_f32_16x16x32_bf16 v[172:175], a[236:239], a[216:219], v[172:175]\n"
        "s_waitcnt lgkmcnt(6)\n"
        "ds_read_b128 a[216:219], v248 offset:12288\n"
        "v_mfma_f32_16x16x32_bf16 v[212:215], a[236:239], a[224:227], v[212:215]\n"
        "s_waitcnt lgkmcnt(4)\n"
        "v_exp_f32 v109, v109\n"
        "v_mfma_f32_16x16x32_bf16 v[212:215], a[240:243], a[220:223], v[212:215]\n"
        "v_exp_f32 v110, v110\n"
        "v_mfma_f32_16x16x32_bf16 v[216:219], a[244:247], a[220:223], v[216:219]\n"
        "ds_read_b128 a[220:223], v248 offset:15360\n"
        "v_cvt_pk_bf16_f32 v92, v108, v109\n"
        "v_mfma_f32_16x16x32_bf16 v[216:219], a[248:251], a[224:227], v[216:219]\n"
        "s_waitcnt lgkmcnt(4)\n"
        "ds_read_b128 a[224:227], v254 offset:12304\n"
        "v_mfma_f32_16x16x32_bf16 v[224:227], a[248:251], a[232:235], v[224:227]\n"
        "s_waitcnt lgkmcnt(3)\n"
        "v_exp_f32 v112, v112\n"
        "v_mfma_f32_16x16x32_bf16 v[224:227], a[244:247], a[228:231], v[224:227]\n"
        "v_exp_f32 v111, v111\n"
        "v_mfma_f32_16x16x32_bf16 v[220:223], a[240:243], a[228:231], v[220:223]\n"
        "ds_read_b128 a[228:231], v254 offset:15376\n"
        "v_cvt_pk_bf16_f32 v93, v110, v111\n"
        "v_mfma_f32_16x16x32_bf16 v[220:223], a[236:239], a[232:235], v[220:223]\n"
        "s_waitcnt lgkmcnt(3)\n"
        "ds_read_b128 a[232:235], v248 offset:12320\n"
        "v_mfma_f32_16x16x32_bf16 v[176:179], v[156:159], a[216:219], v[176:179]\n"
        "s_waitcnt lgkmcnt(3)\n"
        "v_exp_f32 v113, v113\n"
        "v_mfma_f32_16x16x32_bf16 v[176:179], v[160:163], a[220:223], v[176:179]\n"
        "v_exp_f32 v114, v114\n"
        "v_mfma_f32_16x16x32_bf16 v[196:199], v[164:167], a[220:223], v[196:199]\n"
        "ds_read_b128 a[220:223], v248 offset:15392\n"
        "v_cvt_pk_bf16_f32 v94, v112, v113\n"
        "v_mfma_f32_16x16x32_bf16 v[196:199], v[152:155], a[216:219], v[196:199]\n"
        "s_waitcnt lgkmcnt(3)\n"
        "ds_read_b128 a[216:219], v254 offset:12336\n"
        "v_mfma_f32_16x16x32_bf16 v[200:203], v[152:155], a[224:227], v[200:203]\n"
        "s_waitcnt lgkmcnt(3)\n"
        "v_exp_f32 v115, v115\n"
        "v_mfma_f32_16x16x32_bf16 v[200:203], v[164:167], a[228:231], v[200:203]\n"
        "v_cvt_pk_bf16_f32 v95, v114, v115\n"
        "v_mfma_f32_16x16x32_bf16 v[180:183], v[160:163], a[228:231], v[180:183]\n"
        "ds_read_b128 a[228:231], v254 offset:15408\n"
        "v_mfma_f32_16x16x32_bf16 v[180:183], v[156:159], a[224:227], v[180:183]\n"
        "s_waitcnt lgkmcnt(3)\n"
        "ds_read_b128 a[224:227], v248 offset:12352\n"
        "v_mfma_f32_16x16x32_bf16 v[184:187], v[156:159], a[232:235], v[184:187]\n"
        "s_waitcnt lgkmcnt(3)\n"
        "s_waitcnt vmcnt(0)\n"
        "s_barrier\n"
        "v_mfma_f32_16x16x32_bf16 v[184:187], v[160:163], a[220:223], v[184:187]\n"
        "v_mfma_f32_16x16x32_bf16 v[204:207], v[164:167], a[220:223], v[204:207]\n"
        "ds_read_b128 a[220:223], v248 offset:15424\n"
        "v_mfma_f32_16x16x32_bf16 v[204:207], v[152:155], a[232:235], v[204:207]\n"
        "s_waitcnt lgkmcnt(3)\n"
        "ds_read_b128 a[232:235], v254 offset:12368\n"
        "v_mfma_f32_16x16x32_bf16 v[192:195], v[152:155], a[216:219], v[192:195]\n"
        "s_waitcnt lgkmcnt(3)\n"
        "ds_read_b128 a[236:239], v240 offset:0\n"
        "v_mfma_f32_16x16x32_bf16 v[192:195], v[164:167], a[228:231], v[192:195]\n"
        "ds_read_b128 a[240:243], v241 offset:0\n"
        "v_mfma_f32_16x16x32_bf16 v[172:175], v[160:163], a[228:231], v[172:175]\n"
        "ds_read_b128 a[228:231], v254 offset:15440\n"
        "v_mfma_f32_16x16x32_bf16 v[172:175], v[156:159], a[216:219], v[172:175]\n"
        "s_waitcnt lgkmcnt(5)\n"
        "ds_read_b128 a[244:247], v240 offset:128\n"
        "v_mfma_f32_16x16x32_bf16 v[212:215], v[156:159], a[224:227], v[212:215]\n"
        "s_waitcnt lgkmcnt(5)\n"
        "ds_read_b128 a[248:251], v241 offset:128\n"
        "v_mfma_f32_16x16x32_bf16 v[212:215], v[160:163], a[220:223], v[212:215]\n"
        "ds_read_b128 a[216:219], v240 offset:256\n"
        "v_mfma_f32_16x16x32_bf16 v[216:219], v[164:167], a[220:223], v[216:219]\n"
        "ds_read_b128 a[220:223], v241 offset:256\n"
        "v_mfma_f32_16x16x32_bf16 v[216:219], v[152:155], a[224:227], v[216:219]\n"
        "s_waitcnt lgkmcnt(7)\n"
        "v_mfma_f32_16x16x32_bf16 v[224:227], v[152:155], a[232:235], v[224:227]\n"
        "s_waitcnt lgkmcnt(4)\n"
        "v_mfma_f32_16x16x32_bf16 v[224:227], v[164:167], a[228:231], v[224:227]\n"
        "v_mfma_f32_16x16x32_bf16 v[220:223], v[160:163], a[228:231], v[220:223]\n"
        "v_mfma_f32_16x16x32_bf16 v[220:223], v[156:159], a[232:235], v[220:223]\n"
        "s_waitcnt lgkmcnt(0)\n"
        ".p2align 6\n"
        "s_nop 0\n"
        "s_nop 0\n"
        "s_nop 0\n"
        "s_nop 0\n"
        "10:\n"
        "v_mfma_f32_16x16x32_bf16 v[112:115], a[72:75], a[236:239], 0\n"
        "v_mov_b64 v[124:125], 0\n"
        "v_mfma_f32_16x16x32_bf16 v[112:115], a[76:79], a[240:243], v[112:115]\n"
        "s_waitcnt lgkmcnt(3)\n"
        "s_add_u32 s41, s30, s40\n"
        "v_cvt_pk_bf16_f32 v232, v176, v180\n"
        "v_cvt_pk_bf16_f32 v233, v184, v172\n"
        "v_mfma_f32_16x16x32_bf16 v[112:115], a[80:83], a[244:247], v[112:115]\n"
        "s_waitcnt lgkmcnt(2)\n"
        "v_cvt_pk_bf16_f32 v234, v212, v220\n"
        "v_mfma_f32_16x16x32_bf16 v[112:115], a[84:87], a[248:251], v[112:115]\n"
        "s_waitcnt lgkmcnt(1)\n"
        "buffer_store_dwordx3 v[232:234], v250, s[16:19], s38 offen offset:0 sc1\n"
        "v_mfma_f32_16x16x32_bf16 v[112:115], a[88:91], a[216:219], v[112:115]\n"
        "s_waitcnt lgkmcnt(0)\n"
        "s_nop 0\n"
        "v_mfma_f32_16x16x32_bf16 v[112:115], a[92:95], a[220:223], v[112:115]\n"
        "v_cvt_pk_bf16_f32 v232, v177, v181\n"
        "v_cvt_pk_bf16_f32 v233, v185, v173\n"
        "v_smfmac_f32_16x16x64_bf16 v[124:127], a[252:255], v[88:95], v237\n"
        "v_cvt_pk_bf16_f32 v234, v213, v221\n"
        "v_mfma_f32_16x16x32_bf16 v[80:83], v[132:135], v[92:95], 0\n"
        "s_nop 2\n"
        "v_mfma_f32_16x16x32_bf16 v[80:83], v[128:131], v[88:91], v[80:83]\n"
        "v_cvt_pk_bf16_f32 v152, v112, v113\n"
        "v_cvt_pk_bf16_f32 v153, v114, v115\n"
        "v_mfma_f32_16x16x32_bf16 v[84:87], v[136:139], v[88:91], 0\n"
        "v_add_f32 v125, v124, v125\n"
        "v_mfma_f32_16x16x32_bf16 v[84:87], v[140:143], v[92:95], v[84:87]\n"
        "v_rcp_f32 v124, v125\n"
        "v_mfma_f32_16x16x32_bf16 v[96:99], v[148:151], v[92:95], 0\n"
        "buffer_store_dwordx3 v[232:234], v250, s[16:19], s38 offen offset:384 sc1\n"
        "v_mfma_f32_16x16x32_bf16 v[96:99], v[144:147], v[88:91], v[96:99]\n"
        "s_add_u32 s35, s47, 786432\n"
        "v_mul_f32 v80, v124, v80\n"
        "v_mul_f32 v81, v124, v81\n"
        "v_mfma_f32_16x16x32_bf16 v[116:119], a[116:119], a[220:223], 0\n"
        "s_sub_u32 s46, s35, 100663296\n"
        "v_mul_f32 v82, v124, v82\n"
        "v_mul_f32 v83, v124, v83\n"
        "v_mfma_f32_16x16x32_bf16 v[116:119], a[112:115], a[216:219], v[116:119]\n"
        "s_cmp_lt_u32 s46, 786432\n"
        "v_cvt_pk_bf16_f32 v80, v80, v81\n"
        "v_cvt_pk_bf16_f32 v81, v82, v83\n"
        "v_mfma_f32_16x16x32_bf16 v[116:119], a[108:111], a[248:251], v[116:119]\n"
        "s_cselect_b32 s35, s46, s35\n"
        "v_mul_f32 v84, v124, v84\n"
        "v_mul_f32 v85, v124, v85\n"
        "v_mfma_f32_16x16x32_bf16 v[116:119], a[104:107], a[244:247], v[116:119]\n"
        "v_mul_f32 v86, v124, v86\n"
        "v_mul_f32 v87, v124, v87\n"
        "v_mfma_f32_16x16x32_bf16 v[116:119], a[100:103], a[240:243], v[116:119]\n"
        "v_cvt_pk_bf16_f32 v232, v178, v182\n"
        "v_cvt_pk_bf16_f32 v233, v186, v174\n"
        "v_mfma_f32_16x16x32_bf16 v[116:119], a[96:99], a[236:239], v[116:119]\n"
        "v_cvt_pk_bf16_f32 v234, v214, v222\n"
        "v_cvt_pk_bf16_f32 v82, v84, v85\n"
        "v_mfma_f32_16x16x32_bf16 v[120:123], a[120:123], a[236:239], 0\n"
        "v_cvt_pk_bf16_f32 v83, v86, v87\n"
        "v_mul_f32 v96, v124, v96\n"
        "v_mfma_f32_16x16x32_bf16 v[120:123], a[124:127], a[240:243], v[120:123]\n"
        "buffer_store_dwordx3 v[232:234], v250, s[16:19], s38 offen offset:768 sc1\n"
        "v_mfma_f32_16x16x32_bf16 v[120:123], a[128:131], a[244:247], v[120:123]\n"
        "v_mul_f32 v97, v124, v97\n"
        "v_mul_f32 v98, v124, v98\n"
        "v_mfma_f32_16x16x32_bf16 v[120:123], a[132:135], a[248:251], v[120:123]\n"
        "v_mul_f32 v99, v124, v99\n"
        "v_cvt_pk_bf16_f32 v154, v116, v117\n"
        "v_mfma_f32_16x16x32_bf16 v[120:123], a[136:139], a[216:219], v[120:123]\n"
        "ds_write_b128 v245, v[80:83] offset:768\n"
        "v_mfma_f32_16x16x32_bf16 v[120:123], a[140:143], a[220:223], v[120:123]\n"
        "v_cvt_pk_bf16_f32 v155, v118, v119\n"
        "v_cvt_pk_bf16_f32 v96, v96, v97\n"
        "v_mfma_f32_16x16x32_bf16 v[112:115], a[20:23], a[220:223], 0\n"
        "v_cvt_pk_bf16_f32 v97, v98, v99\n"
        "v_max3_u32 v236, v236, v124, v125\n"
        "v_mfma_f32_16x16x32_bf16 v[112:115], a[16:19], a[216:219], v[112:115]\n"
        "v_cmp_lt_u32 vcc, s24, v236\n"
        "v_cvt_pk_bf16_f32 v232, v179, v183\n"
        "v_mfma_f32_16x16x32_bf16 v[112:115], a[12:15], a[248:251], v[112:115]\n"
        "ds_write_b64 v246, v[96:97] offset:768\n"
        "s_cmp_lg_u64 vcc, 0\n"
        "s_addc_u32 s25, s25, s25\n"
        "v_mfma_f32_16x16x32_bf16 v[112:115], a[8:11], a[244:247], v[112:115]\n"
        "v_cvt_pk_bf16_f32 v233, v187, v175\n"
        "v_cvt_pk_bf16_f32 v234, v215, v223\n"
        "v_mfma_f32_16x16x32_bf16 v[112:115], a[4:7], a[240:243], v[112:115]\n"
        "v_cvt_pk_bf16_f32 v168, v120, v121\n"
        "v_cvt_pk_bf16_f32 v169, v122, v123\n"
        "v_mfma_f32_16x16x32_bf16 v[112:115], a[0:3], a[236:239], v[112:115]\n"
        "buffer_store_dwordx3 v[232:234], v250, s[16:19], s38 offen offset:1152 sc1\n"
        "v_mfma_f32_16x16x32_bf16 v[116:119], a[24:27], a[236:239], 0\n"
        "v_xor_b32 v251, 0x2000, v251\n"
        "v_xor_b32 v252, 0x2000, v252\n"
        "v_mfma_f32_16x16x32_bf16 v[116:119], a[28:31], a[240:243], v[116:119]\n"
        "v_cvt_pk_bf16_f32 v232, v196, v200\n"
        "v_cvt_pk_bf16_f32 v233, v204, v192\n"
        "v_mfma_f32_16x16x32_bf16 v[116:119], a[32:35], a[244:247], v[116:119]\n"
        "v_cvt_pk_bf16_f32 v156, v112, v113\n"
        "v_cvt_pk_bf16_f32 v157, v114, v115\n"
        "v_mfma_f32_16x16x32_bf16 v[116:119], a[36:39], a[248:251], v[116:119]\n"
        "v_cvt_pk_bf16_f32 v234, v216, v224\n"
        "v_mfma_f32_16x16x32_bf16 v[116:119], a[40:43], a[216:219], v[116:119]\n"
        "v_mfma_f32_16x16x32_bf16 v[116:119], a[44:47], a[220:223], v[116:119]\n"
        "s_nop 2\n"
        "v_mfma_f32_16x16x32_bf16 v[120:123], a[68:71], a[220:223], 0\n"
        "s_nop 2\n"
        "v_mfma_f32_16x16x32_bf16 v[120:123], a[64:67], a[216:219], v[120:123]\n"
        "buffer_store_dwordx3 v[232:234], v250, s[16:19], s39 offen offset:0 sc1\n"
        "v_mfma_f32_16x16x32_bf16 v[120:123], a[60:63], a[248:251], v[120:123]\n"
        "ds_read_b128 a[224:227], v240 offset:1536\n"
        "v_cvt_pk_bf16_f32 v158, v116, v117\n"
        "v_mfma_f32_16x16x32_bf16 v[120:123], a[56:59], a[244:247], v[120:123]\n"
        "ds_read_b128 a[228:231], v241 offset:1536\n"
        "v_cvt_pk_bf16_f32 v159, v118, v119\n"
        "v_mfma_f32_16x16x32_bf16 v[120:123], a[52:55], a[240:243], v[120:123]\n"
        "ds_read_b128 a[232:235], v240 offset:1664\n"
        "v_cvt_pk_bf16_f32 v232, v197, v201\n"
        "v_mfma_f32_16x16x32_bf16 v[120:123], a[48:51], a[236:239], v[120:123]\n"
        "v_cvt_pk_bf16_f32 v233, v205, v193\n"
        "v_cvt_pk_bf16_f32 v234, v217, v225\n"
        "v_mfma_f32_16x16x32_bf16 v[112:115], a[236:239], a[144:147], 0\n"
        "v_add_u32 v247, s45, v247\n"
        "s_nop 0\n"
        "v_mfma_f32_16x16x32_bf16 v[112:115], a[240:243], a[148:151], v[112:115]\n"
        "s_nop 1\n"
        "v_cvt_pk_bf16_f32 v160, v120, v121\n"
        "v_mfma_f32_16x16x32_bf16 v[112:115], a[244:247], a[152:155], v[112:115]\n"
        "v_cvt_pk_bf16_f32 v161, v122, v123\n"
        "v_cvt_pk_bf16_f32 v166, v120, v121\n"
        "v_mfma_f32_16x16x32_bf16 v[112:115], a[248:251], a[156:159], v[112:115]\n"
        "v_cvt_pk_bf16_f32 v167, v122, v123\n"
        "v_mfma_f32_16x16x32_bf16 v[112:115], a[216:219], a[160:163], v[112:115]\n"
        "buffer_store_dwordx3 v[232:234], v250, s[16:19], s39 offen offset:384 sc1\n"
        "v_mfma_f32_16x16x32_bf16 v[112:115], a[220:223], a[164:167], v[112:115]\n"
        "s_nop 2\n"
        "v_mfma_f32_16x16x32_bf16 v[116:119], a[220:223], a[188:191], 0\n"
        "s_nop 2\n"
        "v_mfma_f32_16x16x32_bf16 v[116:119], a[216:219], a[184:187], v[116:119]\n"
        "v_cvt_pk_bf16_f32 v128, v112, v113\n"
        "v_cvt_pk_bf16_f32 v129, v114, v115\n"
        "v_mfma_f32_16x16x32_bf16 v[116:119], a[248:251], a[180:183], v[116:119]\n"
        "v_cvt_pk_bf16_f32 v232, v198, v202\n"
        "v_cvt_pk_bf16_f32 v233, v206, v194\n"
        "v_mfma_f32_16x16x32_bf16 v[116:119], a[244:247], a[176:179], v[116:119]\n"
        "v_cvt_pk_bf16_f32 v234, v218, v226\n"
        "v_mfma_f32_16x16x32_bf16 v[116:119], a[240:243], a[172:175], v[116:119]\n"
        "v_mfma_f32_16x16x32_bf16 v[116:119], a[236:239], a[168:171], v[116:119]\n"
        "s_nop 2\n"
        "v_mfma_f32_16x16x32_bf16 v[120:123], a[236:239], a[192:195], 0\n"
        "ds_read_b128 a[236:239], v241 offset:1664\n"
        "s_nop 0\n"
        "v_mfma_f32_16x16x32_bf16 v[120:123], a[240:243], a[196:199], v[120:123]\n"
        "ds_read_b128 a[240:243], v240 offset:1792\n"
        "v_cvt_pk_bf16_f32 v136, v116, v117\n"
        "v_mfma_f32_16x16x32_bf16 v[120:123], a[244:247], a[200:203], v[120:123]\n"
        "ds_read_b128 a[244:247], v241 offset:1792\n"
        "v_cvt_pk_bf16_f32 v137, v118, v119\n"
        "v_mfma_f32_16x16x32_bf16 v[120:123], a[248:251], a[204:207], v[120:123]\n"
        "buffer_store_dwordx3 v[232:234], v250, s[16:19], s39 offen offset:768 sc1\n"
        "v_mfma_f32_16x16x32_bf16 v[120:123], a[216:219], a[208:211], v[120:123]\n"
        "s_nop 0\n"
        "v_cvt_pk_bf16_f32 v232, v199, v203\n"
        "v_cvt_pk_bf16_f32 v233, v207, v195\n"
        "v_mfma_f32_16x16x32_bf16 v[120:123], a[220:223], a[212:215], v[120:123]\n"
        "s_waitcnt lgkmcnt(5)\n"
        "v_cvt_pk_bf16_f32 v234, v219, v227\n"
        "s_nop 0\n"
        "v_mfma_f32_16x16x32_bf16 v[112:115], a[72:75], a[224:227], 0\n"
        "s_waitcnt lgkmcnt(4)\n"
        "s_nop 1\n"
        "v_mfma_f32_16x16x32_bf16 v[112:115], a[76:79], a[228:231], v[112:115]\n"
        "s_waitcnt lgkmcnt(3)\n"
        "s_nop 1\n"
        "v_mfma_f32_16x16x32_bf16 v[112:115], a[80:83], a[232:235], v[112:115]\n"
        "s_waitcnt lgkmcnt(2)\n"
        "v_cvt_pk_bf16_f32 v144, v120, v121\n"
        "v_cvt_pk_bf16_f32 v145, v122, v123\n"
        "v_mfma_f32_16x16x32_bf16 v[112:115], a[84:87], a[236:239], v[112:115]\n"
        "s_waitcnt lgkmcnt(1)\n"
        "v_mfma_f32_16x16x32_bf16 v[112:115], a[88:91], a[240:243], v[112:115]\n"
        "s_waitcnt lgkmcnt(0)\n"
        "buffer_store_dwordx3 v[232:234], v250, s[16:19], s39 offen offset:1152 sc1\n"
        "v_mfma_f32_16x16x32_bf16 v[112:115], a[92:95], a[244:247], v[112:115]\n"
        "s_nop 2\n"
        "v_mfma_f32_16x16x32_bf16 v[116:119], a[116:119], a[244:247], 0\n"
        "s_nop 2\n"
        "v_mfma_f32_16x16x32_bf16 v[116:119], a[112:115], a[240:243], v[116:119]\n"
        "v_cvt_pk_bf16_f32 v172, v112, v113\n"
        "v_cvt_pk_bf16_f32 v173, v114, v115\n"
        "v_mfma_f32_16x16x32_bf16 v[116:119], a[108:111], a[236:239], v[116:119]\n"
        "v_mfma_f32_16x16x32_bf16 v[116:119], a[104:107], a[232:235], v[116:119]\n"
        "v_mfma_f32_16x16x32_bf16 v[116:119], a[100:103], a[228:231], v[116:119]\n"
        "v_mfma_f32_16x16x32_bf16 v[116:119], a[96:99], a[224:227], v[116:119]\n"
        "s_mov_b32 m0, s48\n"
        "s_nop 1\n"
        "v_mfma_f32_16x16x32_bf16 v[120:123], a[120:123], a[224:227], 0\n"
        "ds_read_b128 v[162:165], v251 offset:0\n"
        "s_nop 0\n"
        "v_mfma_f32_16x16x32_bf16 v[120:123], a[124:127], a[228:231], v[120:123]\n"
        "buffer_load_dwordx4 v249, s[12:15], s36 offen offset:0 lds\n"
        "v_mfma_f32_16x16x32_bf16 v[120:123], a[128:131], a[232:235], v[120:123]\n"
        "v_cvt_pk_bf16_f32 v174, v116, v117\n"
        "v_cvt_pk_bf16_f32 v175, v118, v119\n"
        "v_mfma_f32_16x16x32_bf16 v[120:123], a[132:135], a[236:239], v[120:123]\n"
        "v_mfma_f32_16x16x32_bf16 v[120:123], a[136:139], a[240:243], v[120:123]\n"
        "v_mfma_f32_16x16x32_bf16 v[120:123], a[140:143], a[244:247], v[120:123]\n"
        "s_nop 2\n"
        "v_mfma_f32_16x16x32_bf16 v[112:115], a[20:23], a[244:247], 0\n"
        "s_nop 2\n"
        "v_mfma_f32_16x16x32_bf16 v[112:115], a[16:19], a[240:243], v[112:115]\n"
        "v_cvt_pk_bf16_f32 v190, v120, v121\n"
        "v_cvt_pk_bf16_f32 v191, v122, v123\n"
        "v_mfma_f32_16x16x32_bf16 v[112:115], a[12:15], a[236:239], v[112:115]\n"
        "v_mfma_f32_16x16x32_bf16 v[112:115], a[8:11], a[232:235], v[112:115]\n"
        "v_mfma_f32_16x16x32_bf16 v[112:115], a[4:7], a[228:231], v[112:115]\n"
        "ds_read_b128 v[182:185], v251 offset:512\n"
        "v_mfma_f32_16x16x32_bf16 v[112:115], a[0:3], a[224:227], v[112:115]\n"
        "s_nop 2\n"
        "v_mfma_f32_16x16x32_bf16 v[116:119], a[24:27], a[224:227], 0\n"
        "s_nop 2\n"
        "v_mfma_f32_16x16x32_bf16 v[116:119], a[28:31], a[228:231], v[116:119]\n"
        "v_cvt_pk_bf16_f32 v176, v112, v113\n"
        "v_cvt_pk_bf16_f32 v177, v114, v115\n"
        "v_mfma_f32_16x16x32_bf16 v[116:119], a[32:35], a[232:235], v[116:119]\n"
        "v_mfma_f32_16x16x32_bf16 v[116:119], a[36:39], a[236:239], v[116:119]\n"
        "buffer_load_dwordx4 v253, s[12:15], s36 offen offset:1024 lds\n"
        "v_mfma_f32_16x16x32_bf16 v[116:119], a[40:43], a[240:243], v[116:119]\n"
        "v_mfma_f32_16x16x32_bf16 v[116:119], a[44:47], a[244:247], v[116:119]\n"
        "s_nop 2\n"
        "v_mfma_f32_16x16x32_bf16 v[120:123], a[68:71], a[244:247], 0\n"
        "s_nop 2\n"
        "v_mfma_f32_16x16x32_bf16 v[120:123], a[64:67], a[240:243], v[120:123]\n"
        "v_cvt_pk_bf16_f32 v178, v116, v117\n"
        "v_cvt_pk_bf16_f32 v179, v118, v119\n"
        "v_mfma_f32_16x16x32_bf16 v[120:123], a[60:63], a[236:239], v[120:123]\n"
        "v_mfma_f32_16x16x32_bf16 v[120:123], a[56:59], a[232:235], v[120:123]\n"
        "v_mfma_f32_16x16x32_bf16 v[120:123], a[52:55], a[228:231], v[120:123]\n"
        "v_mfma_f32_16x16x32_bf16 v[120:123], a[48:51], a[224:227], v[120:123]\n"
        "s_nop 2\n"
        "v_mfma_f32_16x16x32_bf16 v[112:115], a[224:227], a[144:147], 0\n"
        "s_nop 2\n"
        "v_mfma_f32_16x16x32_bf16 v[112:115], a[228:231], a[148:151], v[112:115]\n"
        "v_cvt_pk_bf16_f32 v180, v120, v121\n"
        "v_cvt_pk_bf16_f32 v181, v122, v123\n"
        "v_mfma_f32_16x16x32_bf16 v[112:115], a[232:235], a[152:155], v[112:115]\n"
        "v_cvt_pk_bf16_f32 v186, v120, v121\n"
        "v_cvt_pk_bf16_f32 v187, v122, v123\n"
        "v_mfma_f32_16x16x32_bf16 v[112:115], a[236:239], a[156:159], v[112:115]\n"
        "s_add_u32 m0, s41, 0\n"
        "v_mfma_f32_16x16x32_bf16 v[112:115], a[240:243], a[160:163], v[112:115]\n"
        "ds_read_b128 a[248:251], v240 offset:12288\n"
        "v_mfma_f32_16x16x32_bf16 v[112:115], a[244:247], a[164:167], v[112:115]\n"
        "buffer_load_dwordx4 v242, s[8:11], s47 offen lds\n"
        "v_mfma_f32_16x16x32_bf16 v[96:99], v[152:155], v[156:159], v[0:3]\n"
        "s_waitcnt lgkmcnt(2)\n"
        "ds_read_b128 a[216:219], v241 offset:12288\n"
        "s_nop 0\n"
        "v_mfma_f32_16x16x32_bf16 v[96:99], v[168:171], v[160:163], v[96:99]\n"
        "ds_read_b128 a[220:223], v240 offset:12416\n"
        "s_sub_u32 s31, s30, s28\n"
        "s_nop 0\n"
        "v_mfma_f32_16x16x32_bf16 v[100:103], v[172:175], v[156:159], v[4:7]\n"
        "s_sub_u32 s45, s28, s29\n"
        "v_cvt_pk_bf16_f32 v130, v112, v113\n"
        "v_cvt_pk_bf16_f32 v131, v114, v115\n"
        "v_mfma_f32_16x16x32_bf16 v[100:103], v[188:191], v[164:167], v[100:103]\n"
        "s_add_u32 s28, s28, s31\n"
        "ds_read_b128 v[162:165], v252 offset:0\n"
        "v_mfma_f32_16x16x32_bf16 v[116:119], a[244:247], a[188:191], 0\n"
        "s_mov_b32 s30, s29\n"
        "v_exp_f32 v96, v96\n"
        "v_mfma_f32_16x16x32_bf16 v[116:119], a[240:243], a[184:187], v[116:119]\n"
        "s_add_u32 s29, s29, s45\n"
        "v_exp_f32 v97, v97\n"
        "v_mfma_f32_16x16x32_bf16 v[116:119], a[236:239], a[180:183], v[116:119]\n"
        "v_exp_f32 v98, v98\n"
        "v_mfma_f32_16x16x32_bf16 v[116:119], a[232:235], a[176:179], v[116:119]\n"
        "v_exp_f32 v100, v100\n"
        "v_mfma_f32_16x16x32_bf16 v[116:119], a[228:231], a[172:175], v[116:119]\n"
        "v_exp_f32 v99, v99\n"
        "v_mfma_f32_16x16x32_bf16 v[116:119], a[224:227], a[168:171], v[116:119]\n"
        "v_exp_f32 v101, v101\n"
        "v_mfma_f32_16x16x32_bf16 v[104:107], v[152:155], v[176:179], v[16:19]\n"
        "s_waitcnt lgkmcnt(4)\n"
        "v_exp_f32 v102, v102\n"
        "v_mfma_f32_16x16x32_bf16 v[104:107], v[168:171], v[180:183], v[104:107]\n"
        "s_add_u32 m0, s41, 1024\n"
        "v_cvt_pk_bf16_f32 v64, v96, v97\n"
        "v_cvt_pk_bf16_f32 v65, v98, v99\n"
        "v_mfma_f32_16x16x32_bf16 v[108:111], v[172:175], v[176:179], v[20:23]\n"
        "v_cvt_pk_bf16_f32 v138, v116, v117\n"
        "v_cvt_pk_bf16_f32 v139, v118, v119\n"
        "v_mfma_f32_16x16x32_bf16 v[108:111], v[188:191], v[184:187], v[108:111]\n"
        "buffer_load_dwordx4 v243, s[8:11], s47 offen lds\n"
        "v_mfma_f32_16x16x32_bf16 v[120:123], a[224:227], a[192:195], 0\n"
        "ds_read_b128 a[224:227], v241 offset:12416\n"
        "v_cvt_pk_bf16_f32 v66, v100, v101\n"
        "v_mfma_f32_16x16x32_bf16 v[120:123], a[228:231], a[196:199], v[120:123]\n"
        "ds_read_b128 a[228:231], v240 offset:12544\n"
        "v_mfma_f32_16x16x32_bf16 v[120:123], a[232:235], a[200:203], v[120:123]\n"
        "ds_read_b128 a[232:235], v241 offset:12544\n"
        "v_mfma_f32_16x16x32_bf16 v[120:123], a[236:239], a[204:207], v[120:123]\n"
        "ds_read_b128 v[202:205], v251 offset:4096\n"
        "v_mfma_f32_16x16x32_bf16 v[120:123], a[240:243], a[208:211], v[120:123]\n"
        "ds_read_b128 v[182:185], v252 offset:512\n"
        "v_mfma_f32_16x16x32_bf16 v[120:123], a[244:247], a[212:215], v[120:123]\n"
        "s_waitcnt lgkmcnt(8)\n"
        "v_exp_f32 v103, v103\n"
        "v_mfma_f32_16x16x32_bf16 v[112:115], a[0:3], a[248:251], 0\n"
        "s_waitcnt lgkmcnt(7)\n"
        "v_cvt_pk_bf16_f32 v67, v102, v103\n"
        "s_nop 0\n"
        "v_mfma_f32_16x16x32_bf16 v[112:115], a[4:7], a[216:219], v[112:115]\n"
        "s_waitcnt lgkmcnt(6)\n"
        "v_exp_f32 v104, v104\n"
        "v_mfma_f32_16x16x32_bf16 v[112:115], a[8:11], a[220:223], v[112:115]\n"
        "s_waitcnt lgkmcnt(4)\n"
        "v_exp_f32 v105, v105\n"
        "v_mfma_f32_16x16x32_bf16 v[112:115], a[12:15], a[224:227], v[112:115]\n"
        "s_waitcnt lgkmcnt(3)\n"
        "v_exp_f32 v106, v106\n"
        "v_mfma_f32_16x16x32_bf16 v[112:115], a[16:19], a[228:231], v[112:115]\n"
        "s_waitcnt lgkmcnt(2)\n"
        "v_cvt_pk_bf16_f32 v72, v104, v105\n"
        "v_cvt_pk_bf16_f32 v146, v120, v121\n"
        "v_mfma_f32_16x16x32_bf16 v[112:115], a[20:23], a[232:235], v[112:115]\n"
        "s_add_u32 m0, s41, 2048\n"
        "v_exp_f32 v108, v108\n"
        "v_mfma_f32_16x16x32_bf16 v[116:119], a[44:47], a[232:235], 0\n"
        "v_cvt_pk_bf16_f32 v147, v122, v123\n"
        "v_add_u32 v245, s45, v245\n"
        "v_mfma_f32_16x16x32_bf16 v[116:119], a[40:43], a[228:231], v[116:119]\n"
        "buffer_load_dwordx4 v244, s[8:11], s47 offen lds\n"
        "v_mfma_f32_16x16x32_bf16 v[116:119], a[36:39], a[224:227], v[116:119]\n"
        "v_exp_f32 v107, v107\n"
        "v_mfma_f32_16x16x32_bf16 v[116:119], a[32:35], a[220:223], v[116:119]\n"
        "v_exp_f32 v109, v109\n"
        "v_mfma_f32_16x16x32_bf16 v[116:119], a[28:31], a[216:219], v[116:119]\n"
        "v_cvt_pk_bf16_f32 v73, v106, v107\n"
        "v_cvt_pk_bf16_f32 v74, v108, v109\n"
        "v_mfma_f32_16x16x32_bf16 v[116:119], a[24:27], a[248:251], v[116:119]\n"
        "v_exp_f32 v110, v110\n"
        "v_mfma_f32_16x16x32_bf16 v[120:123], a[48:51], a[248:251], 0\n"
        "v_exp_f32 v111, v111\n"
        "v_mfma_f32_16x16x32_bf16 v[120:123], a[52:55], a[216:219], v[120:123]\n"
        "v_cvt_pk_bf16_f32 v75, v110, v111\n"
        "v_cvt_pk_bf16_f32 v196, v112, v113\n"
        "v_mfma_f32_16x16x32_bf16 v[120:123], a[56:59], a[220:223], v[120:123]\n"
        "v_cvt_pk_bf16_f32 v197, v114, v115\n"
        "v_cvt_pk_bf16_f32 v198, v116, v117\n"
        "v_mfma_f32_16x16x32_bf16 v[120:123], a[60:63], a[224:227], v[120:123]\n"
        "v_cvt_pk_bf16_f32 v199, v118, v119\n"
        "v_add_u32 v246, s45, v246\n"
        "v_mfma_f32_16x16x32_bf16 v[120:123], a[64:67], a[228:231], v[120:123]\n"
        "v_mfma_f32_16x16x32_bf16 v[120:123], a[68:71], a[232:235], v[120:123]\n"
        "s_nop 2\n"
        "v_mfma_f32_16x16x32_bf16 v[112:115], a[92:95], a[232:235], 0\n"
        "s_nop 2\n"
        "v_mfma_f32_16x16x32_bf16 v[112:115], a[88:91], a[228:231], v[112:115]\n"
        "s_add_u32 m0, s41, 12288\n"
        "v_cvt_pk_bf16_f32 v200, v120, v121\n"
        "v_cvt_pk_bf16_f32 v201, v122, v123\n"
        "v_mfma_f32_16x16x32_bf16 v[112:115], a[84:87], a[224:227], v[112:115]\n"
        "v_cvt_pk_bf16_f32 v206, v120, v121\n"
        "v_cvt_pk_bf16_f32 v207, v122, v123\n"
        "v_mfma_f32_16x16x32_bf16 v[112:115], a[80:83], a[220:223], v[112:115]\n"
        "buffer_load_dwordx4 v242, s[8:11], s35 offen lds\n"
        "v_mfma_f32_16x16x32_bf16 v[112:115], a[76:79], a[216:219], v[112:115]\n"
        "v_mfma_f32_16x16x32_bf16 v[112:115], a[72:75], a[248:251], v[112:115]\n"
        "s_nop 2\n"
        "v_mfma_f32_16x16x32_bf16 v[96:99], v[152:155], v[196:199], v[32:35]\n"
        "s_waitcnt lgkmcnt(1)\n"
        "s_nop 1\n"
        "v_mfma_f32_16x16x32_bf16 v[96:99], v[168:171], v[200:203], v[96:99]\n"
        "s_nop 0\n"
        "v_cvt_pk_bf16_f32 v192, v112, v113\n"
        "v_cvt_pk_bf16_f32 v193, v114, v115\n"
        "v_mfma_f32_16x16x32_bf16 v[100:103], v[172:175], v[196:199], v[36:39]\n"
        "v_mfma_f32_16x16x32_bf16 v[100:103], v[188:191], v[204:207], v[100:103]\n"
        "ds_read_b128 v[202:205], v252 offset:4096\n"
        "s_cmp_eq_u32 s37, 100663296\n"
        "v_mfma_f32_16x16x32_bf16 v[116:119], a[96:99], a[248:251], 0\n"
        "s_cselect_b32 s46, 0, s37\n"
        "v_exp_f32 v96, v96\n"
        "v_mfma_f32_16x16x32_bf16 v[116:119], a[100:103], a[216:219], v[116:119]\n"
        "s_add_u32 s38, s46, s43\n"
        "v_exp_f32 v97, v97\n"
        "v_mfma_f32_16x16x32_bf16 v[116:119], a[104:107], a[220:223], v[116:119]\n"
        "s_add_u32 s39, s46, s44\n"
        "v_exp_f32 v98, v98\n"
        "v_mfma_f32_16x16x32_bf16 v[116:119], a[108:111], a[224:227], v[116:119]\n"
        "s_add_u32 s37, s37, 6291456\n"
        "v_exp_f32 v100, v100\n"
        "v_mfma_f32_16x16x32_bf16 v[116:119], a[112:115], a[228:231], v[116:119]\n"
        "v_exp_f32 v99, v99\n"
        "v_mfma_f32_16x16x32_bf16 v[116:119], a[116:119], a[232:235], v[116:119]\n"
        "s_add_u32 m0, s41, 13312\n"
        "v_exp_f32 v101, v101\n"
        "v_mfma_f32_16x16x32_bf16 v[120:123], a[140:143], a[232:235], 0\n"
        "v_exp_f32 v102, v102\n"
        "v_mfma_f32_16x16x32_bf16 v[120:123], a[136:139], a[228:231], v[120:123]\n"
        "buffer_load_dwordx4 v243, s[8:11], s35 offen lds\n"
        "v_mfma_f32_16x16x32_bf16 v[120:123], a[132:135], a[224:227], v[120:123]\n"
        "v_cvt_pk_bf16_f32 v80, v96, v97\n"
        "v_cvt_pk_bf16_f32 v194, v116, v117\n"
        "v_mfma_f32_16x16x32_bf16 v[120:123], a[128:131], a[220:223], v[120:123]\n"
        "v_cvt_pk_bf16_f32 v195, v118, v119\n"
        "v_cvt_pk_bf16_f32 v81, v98, v99\n"
        "v_mfma_f32_16x16x32_bf16 v[120:123], a[124:127], a[216:219], v[120:123]\n"
        "v_cvt_pk_bf16_f32 v82, v100, v101\n"
        "v_mfma_f32_16x16x32_bf16 v[120:123], a[120:123], a[248:251], v[120:123]\n"
        "v_exp_f32 v103, v103\n"
        "v_mfma_f32_16x16x32_bf16 v[112:115], a[248:251], a[144:147], 0\n"
        "v_cvt_pk_bf16_f32 v83, v102, v103\n"
        "s_nop 0\n"
        "v_mfma_f32_16x16x32_bf16 v[112:115], a[216:219], a[148:151], v[112:115]\n"
        "s_nop 2\n"
        "v_mfma_f32_16x16x32_bf16 v[112:115], a[220:223], a[152:155], v[112:115]\n"
        "ds_read_b128 a[236:239], v240 offset:13824\n"
        "v_cvt_pk_bf16_f32 v208, v120, v121\n"
        "v_mfma_f32_16x16x32_bf16 v[112:115], a[224:227], a[156:159], v[112:115]\n"
        "ds_read_b128 a[240:243], v241 offset:13824\n"
        "v_cvt_pk_bf16_f32 v209, v122, v123\n"
        "v_mfma_f32_16x16x32_bf16 v[112:115], a[228:231], a[160:163], v[112:115]\n"
        "ds_read_b128 a[244:247], v240 offset:13952\n"
        "v_mfma_f32_16x16x32_bf16 v[112:115], a[232:235], a[164:167], v[112:115]\n"
        "s_nop 2\n"
        "v_mfma_f32_16x16x32_bf16 v[104:107], v[192:195], v[156:159], v[8:11]\n"
        "s_nop 1\n"
        "v_mfma_f32_16x16x32_bf16 v[104:107], v[208:211], v[160:163], v[104:107]\n"
        "s_add_u32 m0, s41, 14336\n"
        "v_cvt_pk_bf16_f32 v132, v112, v113\n"
        "v_cvt_pk_bf16_f32 v133, v114, v115\n"
        "v_mfma_f32_16x16x32_bf16 v[116:119], a[232:235], a[188:191], 0\n"
        "v_mfma_f32_16x16x32_bf16 v[116:119], a[228:231], a[184:187], v[116:119]\n"
        "buffer_load_dwordx4 v244, s[8:11], s35 offen lds\n"
        "v_mfma_f32_16x16x32_bf16 v[116:119], a[224:227], a[180:183], v[116:119]\n"
        "v_mfma_f32_16x16x32_bf16 v[116:119], a[220:223], a[176:179], v[116:119]\n"
        "v_exp_f32 v104, v104\n"
        "v_mfma_f32_16x16x32_bf16 v[116:119], a[216:219], a[172:175], v[116:119]\n"
        "v_exp_f32 v105, v105\n"
        "v_mfma_f32_16x16x32_bf16 v[116:119], a[248:251], a[168:171], v[116:119]\n"
        "v_exp_f32 v106, v106\n"
        "v_mfma_f32_16x16x32_bf16 v[108:111], v[192:195], v[176:179], v[24:27]\n"
        "s_waitcnt lgkmcnt(4)\n"
        "v_exp_f32 v107, v107\n"
        "v_mfma_f32_16x16x32_bf16 v[108:111], v[208:211], v[180:183], v[108:111]\n"
        "v_cvt_pk_bf16_f32 v68, v104, v105\n"
        "v_cvt_pk_bf16_f32 v69, v106, v107\n"
        "v_mfma_f32_16x16x32_bf16 v[120:123], a[248:251], a[192:195], 0\n"
        "ds_read_b128 a[248:251], v241 offset:13952\n"
        "v_cvt_pk_bf16_f32 v140, v116, v117\n"
        "v_mfma_f32_16x16x32_bf16 v[120:123], a[216:219], a[196:199], v[120:123]\n"
        "ds_read_b128 a[216:219], v240 offset:14080\n"
        "v_cvt_pk_bf16_f32 v141, v118, v119\n"
        "v_mfma_f32_16x16x32_bf16 v[120:123], a[220:223], a[200:203], v[120:123]\n"
        "ds_read_b128 a[220:223], v241 offset:14080\n"
        "v_mfma_f32_16x16x32_bf16 v[120:123], a[224:227], a[204:207], v[120:123]\n"
        "v_exp_f32 v108, v108\n"
        "v_mfma_f32_16x16x32_bf16 v[120:123], a[228:231], a[208:211], v[120:123]\n"
        "v_exp_f32 v109, v109\n"
        "v_mfma_f32_16x16x32_bf16 v[120:123], a[232:235], a[212:215], v[120:123]\n"
        "v_exp_f32 v110, v110\n"
        "v_mfma_f32_16x16x32_bf16 v[96:99], v[192:195], v[196:199], v[40:43]\n"
        "s_waitcnt lgkmcnt(6)\n"
        "v_exp_f32 v111, v111\n"
        "v_mfma_f32_16x16x32_bf16 v[96:99], v[208:211], v[200:203], v[96:99]\n"
        "s_waitcnt lgkmcnt(5)\n"
        "v_cvt_pk_bf16_f32 v76, v108, v109\n"
        "v_cvt_pk_bf16_f32 v77, v110, v111\n"
        "v_mfma_f32_16x16x32_bf16 v[112:115], a[72:75], a[236:239], 0\n"
        "s_waitcnt lgkmcnt(4)\n"
        "s_nop 0\n"
        "v_cvt_pk_bf16_f32 v148, v120, v121\n"
        "v_mfma_f32_16x16x32_bf16 v[112:115], a[76:79], a[240:243], v[112:115]\n"
        "s_waitcnt lgkmcnt(3)\n"
        "v_cvt_pk_bf16_f32 v149, v122, v123\n"
        "v_mfma_f32_16x16x32_bf16 v[112:115], a[80:83], a[244:247], v[112:115]\n"
        "s_waitcnt lgkmcnt(2)\n"
        "v_exp_f32 v96, v96\n"
        "v_mfma_f32_16x16x32_bf16 v[112:115], a[84:87], a[248:251], v[112:115]\n"
        "s_waitcnt lgkmcnt(1)\n"
        "v_exp_f32 v97, v97\n"
        "v_mfma_f32_16x16x32_bf16 v[112:115], a[88:91], a[216:219], v[112:115]\n"
        "s_waitcnt lgkmcnt(0)\n"
        "v_exp_f32 v98, v98\n"
        "v_mfma_f32_16x16x32_bf16 v[112:115], a[92:95], a[220:223], v[112:115]\n"
        "v_exp_f32 v99, v99\n"
        "v_mfma_f32_16x16x32_bf16 v[116:119], a[116:119], a[220:223], 0\n"
        "v_cvt_pk_bf16_f32 v84, v96, v97\n"
        "v_cvt_pk_bf16_f32 v85, v98, v99\n"
        "v_mfma_f32_16x16x32_bf16 v[116:119], a[112:115], a[216:219], v[116:119]\n"
        "v_add_u32 v240, s31, v240\n"
        "v_add_u32 v241, s31, v241\n"
        "v_mfma_f32_16x16x32_bf16 v[116:119], a[108:111], a[248:251], v[116:119]\n"
        "v_cvt_pk_bf16_f32 v212, v112, v113\n"
        "v_cvt_pk_bf16_f32 v213, v114, v115\n"
        "v_mfma_f32_16x16x32_bf16 v[116:119], a[104:107], a[244:247], v[116:119]\n"
        "v_mfma_f32_16x16x32_bf16 v[116:119], a[100:103], a[240:243], v[116:119]\n"
        "v_mfma_f32_16x16x32_bf16 v[116:119], a[96:99], a[236:239], v[116:119]\n"
        "s_nop 2\n"
        "v_mfma_f32_16x16x32_bf16 v[120:123], a[120:123], a[236:239], 0\n"
        "s_nop 2\n"
        "v_mfma_f32_16x16x32_bf16 v[120:123], a[124:127], a[240:243], v[120:123]\n"
        "s_add_u32 s47, s47, 6291456\n"
        "v_cvt_pk_bf16_f32 v214, v116, v117\n"
        "v_cvt_pk_bf16_f32 v215, v118, v119\n"
        "v_mfma_f32_16x16x32_bf16 v[120:123], a[128:131], a[244:247], v[120:123]\n"
        "s_add_u32 s36, s36, 2097152\n"
        "v_mfma_f32_16x16x32_bf16 v[120:123], a[132:135], a[248:251], v[120:123]\n"
        "s_xor_b32 s48, s48, 8192\n"
        "v_mfma_f32_16x16x32_bf16 v[120:123], a[136:139], a[216:219], v[120:123]\n"
        "v_mfma_f32_16x16x32_bf16 v[120:123], a[140:143], a[220:223], v[120:123]\n"
        "s_nop 2\n"
        "v_mfma_f32_16x16x32_bf16 v[112:115], a[220:223], a[164:167], 0\n"
        "s_nop 2\n"
        "v_mfma_f32_16x16x32_bf16 v[112:115], a[216:219], a[160:163], v[112:115]\n"
        "v_cvt_pk_bf16_f32 v230, v120, v121\n"
        "v_cvt_pk_bf16_f32 v231, v122, v123\n"
        "v_mfma_f32_16x16x32_bf16 v[112:115], a[248:251], a[156:159], v[112:115]\n"
        "s_barrier\n"
        "v_mfma_f32_16x16x32_bf16 v[112:115], a[244:247], a[152:155], v[112:115]\n"
        "v_mfma_f32_16x16x32_bf16 v[112:115], a[240:243], a[148:151], v[112:115]\n"
        "v_mfma_f32_16x16x32_bf16 v[112:115], a[236:239], a[144:147], v[112:115]\n"
        "s_nop 2\n"
        "v_mfma_f32_16x16x32_bf16 v[100:103], v[212:215], v[156:159], v[12:15]\n"
        "s_nop 2\n"
        "v_mfma_f32_16x16x32_bf16 v[100:103], v[228:231], v[164:167], v[100:103]\n"
        "v_cvt_pk_bf16_f32 v134, v112, v113\n"
        "v_cvt_pk_bf16_f32 v135, v114, v115\n"
        "v_mfma_f32_16x16x32_bf16 v[116:119], a[236:239], a[168:171], 0\n"
        "v_mfma_f32_16x16x32_bf16 v[116:119], a[240:243], a[172:175], v[116:119]\n"
        "v_mfma_f32_16x16x32_bf16 v[116:119], a[244:247], a[176:179], v[116:119]\n"
        "v_mfma_f32_16x16x32_bf16 v[116:119], a[248:251], a[180:183], v[116:119]\n"
        "v_mfma_f32_16x16x32_bf16 v[116:119], a[216:219], a[184:187], v[116:119]\n"
        "s_nop 0\n"
        "v_exp_f32 v100, v100\n"
        "v_mfma_f32_16x16x32_bf16 v[116:119], a[220:223], a[188:191], v[116:119]\n"
        "v_exp_f32 v101, v101\n"
        "v_mfma_f32_16x16x32_bf16 v[104:107], v[212:215], v[176:179], v[28:31]\n"
        "v_exp_f32 v102, v102\n"
        "v_mfma_f32_16x16x32_bf16 v[104:107], v[228:231], v[184:187], v[104:107]\n"
        "v_exp_f32 v103, v103\n"
        "v_mfma_f32_16x16x32_bf16 v[120:123], a[220:223], a[212:215], 0\n"
        "v_cvt_pk_bf16_f32 v70, v100, v101\n"
        "v_cvt_pk_bf16_f32 v71, v102, v103\n"
        "v_mfma_f32_16x16x32_bf16 v[120:123], a[216:219], a[208:211], v[120:123]\n"
        "v_cvt_pk_bf16_f32 v142, v116, v117\n"
        "v_cvt_pk_bf16_f32 v143, v118, v119\n"
        "v_mfma_f32_16x16x32_bf16 v[120:123], a[248:251], a[204:207], v[120:123]\n"
        "v_mov_b64 v[96:97], 0\n"
        "v_mfma_f32_16x16x32_bf16 v[120:123], a[244:247], a[200:203], v[120:123]\n"
        "v_exp_f32 v104, v104\n"
        "v_mfma_f32_16x16x32_bf16 v[120:123], a[240:243], a[196:199], v[120:123]\n"
        "v_exp_f32 v105, v105\n"
        "v_mfma_f32_16x16x32_bf16 v[120:123], a[236:239], a[192:195], v[120:123]\n"
        "v_exp_f32 v106, v106\n"
        "v_mfma_f32_16x16x32_bf16 v[108:111], v[212:215], v[196:199], v[44:47]\n"
        "v_exp_f32 v107, v107\n"
        "v_mfma_f32_16x16x32_bf16 v[108:111], v[228:231], v[204:207], v[108:111]\n"
        "v_cvt_pk_bf16_f32 v78, v104, v105\n"
        "v_cvt_pk_bf16_f32 v79, v106, v107\n"
        "v_mfma_f32_16x16x32_bf16 v[112:115], a[0:3], a[236:239], 0\n"
        "s_nop 0\n"
        "v_cvt_pk_bf16_f32 v150, v120, v121\n"
        "v_cvt_pk_bf16_f32 v151, v122, v123\n"
        "v_mfma_f32_16x16x32_bf16 v[112:115], a[4:7], a[240:243], v[112:115]\n"
        "v_mfma_f32_16x16x32_bf16 v[112:115], a[8:11], a[244:247], v[112:115]\n"
        "v_exp_f32 v108, v108\n"
        "v_mfma_f32_16x16x32_bf16 v[112:115], a[12:15], a[248:251], v[112:115]\n"
        "v_exp_f32 v109, v109\n"
        "v_mfma_f32_16x16x32_bf16 v[112:115], a[16:19], a[216:219], v[112:115]\n"
        "v_exp_f32 v110, v110\n"
        "v_mfma_f32_16x16x32_bf16 v[112:115], a[20:23], a[220:223], v[112:115]\n"
        "v_exp_f32 v111, v111\n"
        "v_smfmac_f32_16x16x64_bf16 v[96:99], a[252:255], v[64:71], v237\n"
        "v_cvt_pk_bf16_f32 v86, v108, v109\n"
        "v_mfma_f32_16x16x32_bf16 v[100:103], v[132:135], v[68:71], 0\n"
        "v_mov_b64 v[124:125], 0\n"
        "v_cvt_pk_bf16_f32 v87, v110, v111\n"
        "v_mfma_f32_16x16x32_bf16 v[100:103], v[128:131], v[64:67], v[100:103]\n"
        "s_nop 0\n"
        "v_cvt_pk_bf16_f32 v216, v112, v113\n"
        "v_cvt_pk_bf16_f32 v217, v114, v115\n"
        "v_mfma_f32_16x16x32_bf16 v[104:107], v[136:139], v[64:67], 0\n"
        "ds_read_b128 v[222:225], v251 offset:4608\n"
        "v_add_f32 v97, v96, v97\n"
        "v_mfma_f32_16x16x32_bf16 v[104:107], v[140:143], v[68:71], v[104:107]\n"
        "v_rcp_f32 v96, v97\n"
        "v_mfma_f32_16x16x32_bf16 v[108:111], v[148:151], v[68:71], 0\n"
        "v_mul_f32 v100, v96, v100\n"
        "v_mul_f32 v101, v96, v101\n"
        "v_mfma_f32_16x16x32_bf16 v[108:111], v[144:147], v[64:67], v[108:111]\n"
        "v_mul_f32 v102, v96, v102\n"
        "v_mul_f32 v103, v96, v103\n"
        "v_mfma_f32_16x16x32_bf16 v[116:119], a[44:47], a[220:223], 0\n"
        "v_cvt_pk_bf16_f32 v100, v100, v101\n"
        "v_cvt_pk_bf16_f32 v101, v102, v103\n"
        "v_mfma_f32_16x16x32_bf16 v[116:119], a[40:43], a[216:219], v[116:119]\n"
        "v_mul_f32 v104, v96, v104\n"
        "v_mul_f32 v105, v96, v105\n"
        "v_mfma_f32_16x16x32_bf16 v[116:119], a[36:39], a[248:251], v[116:119]\n"
        "v_mul_f32 v106, v96, v106\n"
        "v_mul_f32 v107, v96, v107\n"
        "v_mfma_f32_16x16x32_bf16 v[116:119], a[32:35], a[244:247], v[116:119]\n"
        "v_cvt_pk_bf16_f32 v102, v104, v105\n"
        "v_cvt_pk_bf16_f32 v103, v106, v107\n"
        "v_mfma_f32_16x16x32_bf16 v[116:119], a[28:31], a[240:243], v[116:119]\n"
        "ds_write_b128 v245, v[100:103] offset:0\n"
        "v_mfma_f32_16x16x32_bf16 v[116:119], a[24:27], a[236:239], v[116:119]\n"
        "v_mul_f32 v108, v96, v108\n"
        "v_mul_f32 v109, v96, v109\n"
        "v_smfmac_f32_16x16x64_bf16 v[124:127], a[252:255], v[72:79], v237\n"
        "v_mul_f32 v110, v96, v110\n"
        "v_mfma_f32_16x16x32_bf16 v[64:67], v[132:135], v[76:79], 0\n"
        "v_mul_f32 v111, v96, v111\n"
        "v_cvt_pk_bf16_f32 v108, v108, v109\n"
        "v_mfma_f32_16x16x32_bf16 v[64:67], v[128:131], v[72:75], v[64:67]\n"
        "ds_read_b128 v[156:159], v247 offset:0\n"
        "v_cvt_pk_bf16_f32 v218, v116, v117\n"
        "v_mfma_f32_16x16x32_bf16 v[68:71], v[136:139], v[72:75], 0\n"
        "ds_read_b128 v[160:163], v247 offset:4096\n"
        "v_cvt_pk_bf16_f32 v219, v118, v119\n"
        "v_mfma_f32_16x16x32_bf16 v[68:71], v[140:143], v[76:79], v[68:71]\n"
        "ds_read_b128 v[164:167], v247 offset:4352\n"
        "v_cvt_pk_bf16_f32 v109, v110, v111\n"
        "v_mfma_f32_16x16x32_bf16 v[120:123], v[148:151], v[76:79], 0\n"
        "ds_write_b64 v246, v[108:109] offset:0\n"
        "v_mfma_f32_16x16x32_bf16 v[120:123], v[144:147], v[72:75], v[120:123]\n"
        "v_max_u32 v236, v96, v97\n"
        "v_mov_b64 v[116:117], 0\n"
        "v_mfma_f32_16x16x32_bf16 v[112:115], a[48:51], a[236:239], 0\n"
        "v_add_f32 v125, v124, v125\n"
        "v_mfma_f32_16x16x32_bf16 v[112:115], a[52:55], a[240:243], v[112:115]\n"
        "v_rcp_f32 v124, v125\n"
        "v_mfma_f32_16x16x32_bf16 v[112:115], a[56:59], a[244:247], v[112:115]\n"
        "v_mul_f32 v64, v124, v64\n"
        "v_mul_f32 v65, v124, v65\n"
        "v_mfma_f32_16x16x32_bf16 v[112:115], a[60:63], a[248:251], v[112:115]\n"
        "v_mul_f32 v66, v124, v66\n"
        "v_mul_f32 v67, v124, v67\n"
        "v_mfma_f32_16x16x32_bf16 v[112:115], a[64:67], a[216:219], v[112:115]\n"
        "v_cvt_pk_bf16_f32 v64, v64, v65\n"
        "v_cvt_pk_bf16_f32 v65, v66, v67\n"
        "v_mfma_f32_16x16x32_bf16 v[112:115], a[68:71], a[220:223], v[112:115]\n"
        "ds_read_b128 a[216:219], v248 offset:0\n"
        "v_mul_f32 v68, v124, v68\n"
        "v_smfmac_f32_16x16x64_bf16 v[116:119], a[252:255], v[80:87], v237\n"
        "ds_read_b128 a[220:223], v248 offset:3072\n"
        "v_mfma_f32_16x16x32_bf16 v[72:75], v[132:135], v[84:87], 0\n"
        "v_mul_f32 v69, v124, v69\n"
        "v_mul_f32 v70, v124, v70\n"
        "v_mfma_f32_16x16x32_bf16 v[72:75], v[128:131], v[80:83], v[72:75]\n"
        "v_cvt_pk_bf16_f32 v220, v112, v113\n"
        "v_cvt_pk_bf16_f32 v221, v114, v115\n"
        "v_mfma_f32_16x16x32_bf16 v[76:79], v[136:139], v[80:83], 0\n"
        "ds_read_b128 a[224:227], v254 offset:16\n"
        "v_cvt_pk_bf16_f32 v226, v112, v113\n"
        "v_mfma_f32_16x16x32_bf16 v[76:79], v[140:143], v[84:87], v[76:79]\n"
        "ds_read_b128 a[228:231], v254 offset:3088\n"
        "v_cvt_pk_bf16_f32 v227, v114, v115\n"
        "v_mfma_f32_16x16x32_bf16 v[96:99], v[148:151], v[84:87], 0\n"
        "v_mul_f32 v71, v124, v71\n"
        "v_cvt_pk_bf16_f32 v66, v68, v69\n"
        "v_mfma_f32_16x16x32_bf16 v[96:99], v[144:147], v[80:83], v[96:99]\n"
        "v_cvt_pk_bf16_f32 v67, v70, v71\n"
        "v_mul_f32 v120, v124, v120\n"
        "v_mfma_f32_16x16x32_bf16 v[100:103], v[152:155], v[216:219], v[48:51]\n"
        "s_waitcnt lgkmcnt(9)\n"
        "ds_read_b128 v[152:155], v247 offset:256\n"
        "v_mul_f32 v121, v124, v121\n"
        "v_mfma_f32_16x16x32_bf16 v[100:103], v[168:171], v[220:223], v[100:103]\n"
        "ds_write_b128 v245, v[64:67] offset:256\n"
        "v_mfma_f32_16x16x32_bf16 v[104:107], v[172:175], v[216:219], v[52:55]\n"
        "v_mul_f32 v122, v124, v122\n"
        "v_mul_f32 v123, v124, v123\n"
        "v_mfma_f32_16x16x32_bf16 v[104:107], v[188:191], v[224:227], v[104:107]\n"
        "s_waitcnt lgkmcnt(5)\n"
        "ds_read_b128 v[222:225], v252 offset:4608\n"
        "v_cvt_pk_bf16_f32 v120, v120, v121\n"
        "v_mfma_f32_16x16x32_bf16 v[176:179], v[156:159], a[216:219], 0\n"
        "s_waitcnt lgkmcnt(5)\n"
        "ds_read_b128 a[232:235], v248 offset:32\n"
        "v_cvt_pk_bf16_f32 v121, v122, v123\n"
        "v_mfma_f32_16x16x32_bf16 v[176:179], v[160:163], a[220:223], v[176:179]\n"
        "ds_write_b64 v246, v[120:121] offset:256\n"
        "v_mfma_f32_16x16x32_bf16 v[196:199], v[164:167], a[220:223], 0\n"
        "s_waitcnt lgkmcnt(4)\n"
        "ds_read_b128 a[220:223], v248 offset:3104\n"
        "v_max3_u32 v236, v236, v124, v125\n"
        "v_mfma_f32_16x16x32_bf16 v[196:199], v[152:155], a[216:219], v[196:199]\n"
        "v_exp_f32 v100, v100\n"
        "v_mfma_f32_16x16x32_bf16 v[200:203], v[152:155], a[224:227], 0\n"
        "ds_read_b128 a[216:219], v254 offset:48\n"
        "v_add_f32 v117, v116, v117\n"
        "v_mfma_f32_16x16x32_bf16 v[200:203], v[164:167], a[228:231], v[200:203]\n"
        "v_rcp_f32 v116, v117\n"
        "v_mfma_f32_16x16x32_bf16 v[180:183], v[160:163], a[228:231], 0\n"
        "ds_read_b128 a[228:231], v254 offset:3120\n"
        "v_mul_f32 v72, v116, v72\n"
        "v_mfma_f32_16x16x32_bf16 v[180:183], v[156:159], a[224:227], v[180:183]\n"
        "ds_read_b128 a[236:239], v247 offset:8192\n"
        "v_mul_f32 v73, v116, v73\n"
        "v_mfma_f32_16x16x32_bf16 v[108:111], v[192:195], v[216:219], v[56:59]\n"
        "s_waitcnt lgkmcnt(6)\n"
        "ds_read_b128 a[224:227], v248 offset:64\n"
        "v_mul_f32 v74, v116, v74\n"
        "v_mfma_f32_16x16x32_bf16 v[108:111], v[208:211], v[220:223], v[108:111]\n"
        "ds_read_b128 a[240:243], v247 offset:12288\n"
        "v_mul_f32 v75, v116, v75\n"
        "v_mfma_f32_16x16x32_bf16 v[112:115], v[212:215], v[216:219], v[60:63]\n"
        "ds_read_b128 a[244:247], v247 offset:12544\n"
        "v_cvt_pk_bf16_f32 v72, v72, v73\n"
        "v_mfma_f32_16x16x32_bf16 v[112:115], v[228:231], v[224:227], v[112:115]\n"
        "s_waitcnt lgkmcnt(8)\n"
        "ds_read_b128 a[248:251], v247 offset:8448\n"
        "v_cvt_pk_bf16_f32 v73, v74, v75\n"
        "v_mfma_f32_16x16x32_bf16 v[184:187], v[156:159], a[232:235], 0\n"
        "s_waitcnt lgkmcnt(7)\n"
        "v_exp_f32 v101, v101\n"
        "v_mfma_f32_16x16x32_bf16 v[184:187], v[160:163], a[220:223], v[184:187]\n"
        "v_mul_f32 v76, v116, v76\n"
        "v_mul_f32 v77, v116, v77\n"
        "v_mfma_f32_16x16x32_bf16 v[204:207], v[164:167], a[220:223], 0\n"
        "ds_read_b128 a[220:223], v248 offset:3136\n"
        "v_mul_f32 v78, v116, v78\n"
        "v_mfma_f32_16x16x32_bf16 v[204:207], v[152:155], a[232:235], v[204:207]\n"
        "s_waitcnt lgkmcnt(7)\n"
        "ds_read_b128 a[232:235], v254 offset:80\n"
        "v_mul_f32 v79, v116, v79\n"
        "v_mfma_f32_16x16x32_bf16 v[192:195], v[152:155], a[216:219], 0\n"
        "s_waitcnt lgkmcnt(7)\n"
        "v_exp_f32 v102, v102\n"
        "v_mfma_f32_16x16x32_bf16 v[192:195], v[164:167], a[228:231], v[192:195]\n"
        "v_cvt_pk_bf16_f32 v88, v100, v101\n"
        "v_cvt_pk_bf16_f32 v74, v76, v77\n"
        "v_mfma_f32_16x16x32_bf16 v[172:175], v[160:163], a[228:231], 0\n"
        "ds_read_b128 a[228:231], v254 offset:3152\n"
        "v_cvt_pk_bf16_f32 v75, v78, v79\n"
        "v_mfma_f32_16x16x32_bf16 v[172:175], v[156:159], a[216:219], v[172:175]\n"
        "s_waitcnt lgkmcnt(6)\n"
        "ds_write_b128 v245, v[72:75] offset:512\n"
        "v_mfma_f32_16x16x32_bf16 v[212:215], v[156:159], a[224:227], 0\n"
        "s_waitcnt lgkmcnt(3)\n"
        "ds_read_b128 a[216:219], v248 offset:6144\n"
        "v_mul_f32 v96, v116, v96\n"
        "v_mfma_f32_16x16x32_bf16 v[212:215], v[160:163], a[220:223], v[212:215]\n"
        "v_exp_f32 v104, v104\n"
        "v_mfma_f32_16x16x32_bf16 v[216:219], v[164:167], a[220:223], 0\n"
        "ds_read_b128 a[220:223], v248 offset:9216\n"
        "v_mul_f32 v97, v116, v97\n"
        "v_mfma_f32_16x16x32_bf16 v[216:219], v[152:155], a[224:227], v[216:219]\n"
        "s_waitcnt lgkmcnt(4)\n"
        "ds_read_b128 a[224:227], v254 offset:6160\n"
        "v_mul_f32 v98, v116, v98\n"
        "v_mfma_f32_16x16x32_bf16 v[224:227], v[152:155], a[232:235], 0\n"
        "s_waitcnt lgkmcnt(4)\n"
        "v_exp_f32 v103, v103\n"
        "v_mfma_f32_16x16x32_bf16 v[224:227], v[164:167], a[228:231], v[224:227]\n"
        "v_exp_f32 v105, v105\n"
        "v_mfma_f32_16x16x32_bf16 v[220:223], v[160:163], a[228:231], 0\n"
        "ds_read_b128 a[228:231], v254 offset:9232\n"
        "v_cvt_pk_bf16_f32 v89, v102, v103\n"
        "v_mfma_f32_16x16x32_bf16 v[220:223], v[156:159], a[232:235], v[220:223]\n"
        "s_waitcnt lgkmcnt(3)\n"
        "ds_read_b128 a[232:235], v248 offset:6176\n"
        "v_cvt_pk_bf16_f32 v90, v104, v105\n"
        "v_mfma_f32_16x16x32_bf16 v[176:179], a[236:239], a[216:219], v[176:179]\n"
        "s_waitcnt lgkmcnt(3)\n"
        "v_exp_f32 v106, v106\n"
        "v_mfma_f32_16x16x32_bf16 v[176:179], a[240:243], a[220:223], v[176:179]\n"
        "v_mul_f32 v99, v116, v99\n"
        "v_cvt_pk_bf16_f32 v96, v96, v97\n"
        "v_mfma_f32_16x16x32_bf16 v[196:199], a[244:247], a[220:223], v[196:199]\n"
        "ds_read_b128 a[220:223], v248 offset:9248\n"
        "v_cvt_pk_bf16_f32 v97, v98, v99\n"
        "v_mfma_f32_16x16x32_bf16 v[196:199], a[248:251], a[216:219], v[196:199]\n"
        "s_waitcnt lgkmcnt(3)\n"
        "ds_write_b64 v246, v[96:97] offset:512\n"
        "v_mfma_f32_16x16x32_bf16 v[200:203], a[248:251], a[224:227], v[200:203]\n"
        "s_waitcnt lgkmcnt(3)\n"
        "ds_read_b128 a[216:219], v254 offset:6192\n"
        "v_max3_u32 v236, v236, v116, v117\n"
        "v_mfma_f32_16x16x32_bf16 v[200:203], a[244:247], a[228:231], v[200:203]\n"
        "ds_read_b128 v[156:159], v247 offset:16384\n"
        "v_mfma_f32_16x16x32_bf16 v[180:183], a[240:243], a[228:231], v[180:183]\n"
        "ds_read_b128 a[228:231], v254 offset:9264\n"
        "v_mfma_f32_16x16x32_bf16 v[180:183], a[236:239], a[224:227], v[180:183]\n"
        "s_waitcnt lgkmcnt(5)\n"
        "ds_read_b128 a[224:227], v248 offset:6208\n"
        "v_mfma_f32_16x16x32_bf16 v[184:187], a[236:239], a[232:235], v[184:187]\n"
        "s_waitcnt lgkmcnt(5)\n"
        "ds_read_b128 v[160:163], v247 offset:20480\n"
        "v_mfma_f32_16x16x32_bf16 v[184:187], a[240:243], a[220:223], v[184:187]\n"
        "ds_read_b128 v[164:167], v247 offset:20736\n"
        "v_mfma_f32_16x16x32_bf16 v[204:207], a[244:247], a[220:223], v[204:207]\n"
        "ds_read_b128 a[220:223], v248 offset:9280\n"
        "v_mfma_f32_16x16x32_bf16 v[204:207], a[248:251], a[232:235], v[204:207]\n"
        "s_waitcnt lgkmcnt(6)\n"
        "ds_read_b128 a[232:235], v254 offset:6224\n"
        "v_mfma_f32_16x16x32_bf16 v[192:195], a[248:251], a[216:219], v[192:195]\n"
        "s_waitcnt lgkmcnt(5)\n"
        "ds_read_b128 v[152:155], v247 offset:16640\n"
        "v_mfma_f32_16x16x32_bf16 v[192:195], a[244:247], a[228:231], v[192:195]\n"
        "v_exp_f32 v107, v107\n"
        "v_mfma_f32_16x16x32_bf16 v[172:175], a[240:243], a[228:231], v[172:175]\n"
        "ds_read_b128 a[228:231], v254 offset:9296\n"
        "v_cvt_pk_bf16_f32 v91, v106, v107\n"
        "v_mfma_f32_16x16x32_bf16 v[172:175], a[236:239], a[216:219], v[172:175]\n"
        "s_waitcnt lgkmcnt(6)\n"
        "ds_read_b128 a[216:219], v248 offset:12288\n"
        "v_mfma_f32_16x16x32_bf16 v[212:215], a[236:239], a[224:227], v[212:215]\n"
        "s_waitcnt lgkmcnt(4)\n"
        "v_exp_f32 v108, v108\n"
        "v_mfma_f32_16x16x32_bf16 v[212:215], a[240:243], a[220:223], v[212:215]\n"
        "v_exp_f32 v109, v109\n"
        "v_mfma_f32_16x16x32_bf16 v[216:219], a[244:247], a[220:223], v[216:219]\n"
        "ds_read_b128 a[220:223], v248 offset:15360\n"
        "v_cvt_pk_bf16_f32 v92, v108, v109\n"
        "v_mfma_f32_16x16x32_bf16 v[216:219], a[248:251], a[224:227], v[216:219]\n"
        "s_waitcnt lgkmcnt(4)\n"
        "ds_read_b128 a[224:227], v254 offset:12304\n"
        "v_mfma_f32_16x16x32_bf16 v[224:227], a[248:251], a[232:235], v[224:227]\n"
        "s_waitcnt lgkmcnt(3)\n"
        "v_exp_f32 v110, v110\n"
        "v_mfma_f32_16x16x32_bf16 v[224:227], a[244:247], a[228:231], v[224:227]\n"
        "v_exp_f32 v112, v112\n"
        "v_mfma_f32_16x16x32_bf16 v[220:223], a[240:243], a[228:231], v[220:223]\n"
        "ds_read_b128 a[228:231], v254 offset:15376\n"
        "v_mfma_f32_16x16x32_bf16 v[220:223], a[236:239], a[232:235], v[220:223]\n"
        "s_waitcnt lgkmcnt(3)\n"
        "ds_read_b128 a[232:235], v248 offset:12320\n"
        "v_mfma_f32_16x16x32_bf16 v[176:179], v[156:159], a[216:219], v[176:179]\n"
        "s_waitcnt lgkmcnt(3)\n"
        "v_exp_f32 v111, v111\n"
        "v_mfma_f32_16x16x32_bf16 v[176:179], v[160:163], a[220:223], v[176:179]\n"
        "v_exp_f32 v113, v113\n"
        "v_mfma_f32_16x16x32_bf16 v[196:199], v[164:167], a[220:223], v[196:199]\n"
        "ds_read_b128 a[220:223], v248 offset:15392\n"
        "v_cvt_pk_bf16_f32 v93, v110, v111\n"
        "v_mfma_f32_16x16x32_bf16 v[196:199], v[152:155], a[216:219], v[196:199]\n"
        "s_waitcnt lgkmcnt(3)\n"
        "ds_read_b128 a[216:219], v254 offset:12336\n"
        "v_cvt_pk_bf16_f32 v94, v112, v113\n"
        "v_mfma_f32_16x16x32_bf16 v[200:203], v[152:155], a[224:227], v[200:203]\n"
        "s_waitcnt lgkmcnt(3)\n"
        "v_exp_f32 v114, v114\n"
        "v_mfma_f32_16x16x32_bf16 v[200:203], v[164:167], a[228:231], v[200:203]\n"
        "v_exp_f32 v115, v115\n"
        "v_mfma_f32_16x16x32_bf16 v[180:183], v[160:163], a[228:231], v[180:183]\n"
        "ds_read_b128 a[228:231], v254 offset:15408\n"
        "v_cvt_pk_bf16_f32 v95, v114, v115\n"
        "v_mfma_f32_16x16x32_bf16 v[180:183], v[156:159], a[224:227], v[180:183]\n"
        "s_waitcnt lgkmcnt(3)\n"
        "ds_read_b128 a[224:227], v248 offset:12352\n"
        "v_mfma_f32_16x16x32_bf16 v[184:187], v[156:159], a[232:235], v[184:187]\n"
        "s_waitcnt lgkmcnt(3)\n"
        "s_waitcnt vmcnt(0)\n"
        "s_barrier\n"
        "v_mfma_f32_16x16x32_bf16 v[184:187], v[160:163], a[220:223], v[184:187]\n"
        "v_mfma_f32_16x16x32_bf16 v[204:207], v[164:167], a[220:223], v[204:207]\n"
        "ds_read_b128 a[220:223], v248 offset:15424\n"
        "v_mfma_f32_16x16x32_bf16 v[204:207], v[152:155], a[232:235], v[204:207]\n"
        "s_waitcnt lgkmcnt(3)\n"
        "ds_read_b128 a[232:235], v254 offset:12368\n"
        "v_mfma_f32_16x16x32_bf16 v[192:195], v[152:155], a[216:219], v[192:195]\n"
        "s_waitcnt lgkmcnt(3)\n"
        "ds_read_b128 a[236:239], v240 offset:0\n"
        "v_mfma_f32_16x16x32_bf16 v[192:195], v[164:167], a[228:231], v[192:195]\n"
        "ds_read_b128 a[240:243], v241 offset:0\n"
        "v_mfma_f32_16x16x32_bf16 v[172:175], v[160:163], a[228:231], v[172:175]\n"
        "ds_read_b128 a[228:231], v254 offset:15440\n"
        "v_mfma_f32_16x16x32_bf16 v[172:175], v[156:159], a[216:219], v[172:175]\n"
        "s_waitcnt lgkmcnt(5)\n"
        "ds_read_b128 a[244:247], v240 offset:128\n"
        "v_mfma_f32_16x16x32_bf16 v[212:215], v[156:159], a[224:227], v[212:215]\n"
        "s_waitcnt lgkmcnt(5)\n"
        "ds_read_b128 a[248:251], v241 offset:128\n"
        "v_mfma_f32_16x16x32_bf16 v[212:215], v[160:163], a[220:223], v[212:215]\n"
        "ds_read_b128 a[216:219], v240 offset:256\n"
        "v_mfma_f32_16x16x32_bf16 v[216:219], v[164:167], a[220:223], v[216:219]\n"
        "ds_read_b128 a[220:223], v241 offset:256\n"
        "v_mfma_f32_16x16x32_bf16 v[216:219], v[152:155], a[224:227], v[216:219]\n"
        "s_waitcnt lgkmcnt(7)\n"
        "v_mfma_f32_16x16x32_bf16 v[224:227], v[152:155], a[232:235], v[224:227]\n"
        "s_waitcnt lgkmcnt(4)\n"
        "v_mfma_f32_16x16x32_bf16 v[224:227], v[164:167], a[228:231], v[224:227]\n"
        "s_sub_u32 s27, s27, 1\n"
        "v_mfma_f32_16x16x32_bf16 v[220:223], v[160:163], a[228:231], v[220:223]\n"
        "s_cmp_lg_u32 s27, 0\n"
        "v_mfma_f32_16x16x32_bf16 v[220:223], v[156:159], a[232:235], v[220:223]\n"
        "s_cbranch_scc1 10b\n"
        "s_waitcnt lgkmcnt(0)\n"
        "s_and_b32 s46, s42, 1\n"
        "s_lshl_b32 s31, s46, 8\n"
        "s_sub_u32 s31, s45, s31\n"
        "v_add_u32 v247, s31, v247\n"
        "ds_read_b128 a[144:147], v247 offset:0\n"
        "ds_read_b128 a[0:3], v248 offset:0\n"
        "ds_read_b128 a[148:151], v247 offset:4096\n"
        "ds_read_b128 a[4:7], v248 offset:3072\n"
        "v_mfma_f32_16x16x32_bf16 v[80:83], v[132:135], v[92:95], 0\n"
        "ds_read_b128 a[152:155], v247 offset:8192\n"
        "ds_read_b128 a[8:11], v248 offset:6144\n"
        "s_mul_i32 s45, s46, 786432\n"
        "v_mov_b64 v[124:125], 0\n"
        "v_mfma_f32_16x16x32_bf16 v[80:83], v[128:131], v[88:91], v[80:83]\n"
        "ds_read_b128 a[156:159], v247 offset:12288\n"
        "ds_read_b128 a[12:15], v248 offset:9216\n"
        "s_sub_u32 s37, s37, s45\n"
        "v_cvt_pk_bf16_f32 v232, v176, v180\n"
        "v_cvt_pk_bf16_f32 v233, v184, v172\n"
        "v_mfma_f32_16x16x32_bf16 v[84:87], v[136:139], v[88:91], 0\n"
        "ds_read_b128 a[160:163], v247 offset:16384\n"
        "ds_read_b128 a[16:19], v248 offset:12288\n"
        "s_cmp_eq_u32 s46, 0\n"
        "s_cselect_b32 s43, s43, s44\n"
        "v_cvt_pk_bf16_f32 v234, v212, v220\n"
        "v_mfma_f32_16x16x32_bf16 v[84:87], v[140:143], v[92:95], v[84:87]\n"
        "buffer_store_dwordx3 v[232:234], v250, s[16:19], s38 offen offset:0 sc1\n"
        "s_add_u32 s28, s37, 786432\n"
        "v_mfma_f32_16x16x32_bf16 v[96:99], v[148:151], v[92:95], 0\n"
        "ds_read_b128 a[164:167], v247 offset:20480\n"
        "ds_read_b128 a[20:23], v248 offset:15360\n"
        "ds_read_b128 a[44:47], v254 offset:15376\n"
        "s_cmp_eq_u32 s28, 100663296\n"
        "s_cselect_b32 s28, 0, s28\n"
        "v_mfma_f32_16x16x32_bf16 v[96:99], v[144:147], v[88:91], v[96:99]\n"
        "ds_read_b128 a[40:43], v254 offset:12304\n"
        "ds_read_b128 a[36:39], v254 offset:9232\n"
        "s_lshl_b32 s29, s42, 2\n"
        "v_cvt_pk_bf16_f32 v232, v177, v181\n"
        "v_cvt_pk_bf16_f32 v233, v185, v173\n"
        "v_smfmac_f32_16x16x64_bf16 v[124:127], a[252:255], v[88:95], v237\n"
        "s_waitcnt lgkmcnt(13)\n"
        "ds_read_b128 a[32:35], v254 offset:6160\n"
        "v_cvt_pk_bf16_f32 v234, v213, v221\n"
        "v_mfma_f32_16x16x32_bf16 v[0:3], a[144:147], a[0:3], 0\n"
        "s_waitcnt lgkmcnt(12)\n"
        "buffer_store_dwordx3 v[232:234], v250, s[16:19], s38 offen offset:384 sc1\n"
        "s_add_u32 s29, s29, 147456\n"
        "v_mfma_f32_16x16x32_bf16 v[0:3], a[148:151], a[4:7], v[0:3]\n"
        "s_waitcnt lgkmcnt(10)\n"
        "ds_read_b128 a[28:31], v254 offset:3088\n"
        "ds_read_b128 a[24:27], v254 offset:16\n"
        "v_cvt_pk_bf16_f32 v232, v178, v182\n"
        "v_cvt_pk_bf16_f32 v233, v186, v174\n"
        "v_mfma_f32_16x16x32_bf16 v[0:3], a[152:155], a[8:11], v[0:3]\n"
        "s_waitcnt lgkmcnt(10)\n"
        "ds_read_b128 a[48:51], v248 offset:32\n"
        "ds_read_b128 a[52:55], v248 offset:3104\n"
        "v_cvt_pk_bf16_f32 v234, v214, v222\n"
        "v_mfma_f32_16x16x32_bf16 v[0:3], a[156:159], a[12:15], v[0:3]\n"
        "s_waitcnt lgkmcnt(10)\n"
        "buffer_store_dwordx3 v[232:234], v250, s[16:19], s38 offen offset:768 sc1\n"
        "v_mfma_f32_16x16x32_bf16 v[0:3], a[160:163], a[16:19], v[0:3]\n"
        "s_waitcnt lgkmcnt(8)\n"
        "ds_read_b128 a[56:59], v248 offset:6176\n"
        "ds_read_b128 a[60:63], v248 offset:9248\n"
        "v_cvt_pk_bf16_f32 v232, v179, v183\n"
        "v_cvt_pk_bf16_f32 v233, v187, v175\n"
        "v_mfma_f32_16x16x32_bf16 v[0:3], a[164:167], a[20:23], v[0:3]\n"
        "s_waitcnt lgkmcnt(9)\n"
        "ds_read_b128 a[64:67], v248 offset:12320\n"
        "ds_read_b128 a[68:71], v248 offset:15392\n"
        "v_cvt_pk_bf16_f32 v234, v215, v223\n"
        "v_add_f32 v125, v124, v125\n"
        "v_mfma_f32_16x16x32_bf16 v[4:7], a[164:167], a[44:47], 0\n"
        "s_waitcnt lgkmcnt(10)\n"
        "buffer_store_dwordx3 v[232:234], v250, s[16:19], s38 offen offset:1152 sc1\n"
        "v_mfma_f32_16x16x32_bf16 v[4:7], a[160:163], a[40:43], v[4:7]\n"
        "s_waitcnt lgkmcnt(9)\n"
        "ds_read_b128 a[92:95], v254 offset:15408\n"
        "ds_read_b128 a[88:91], v254 offset:12336\n"
        "v_rcp_f32 v124, v125\n"
        "v_mfma_f32_16x16x32_bf16 v[4:7], a[156:159], a[36:39], v[4:7]\n"
        "s_waitcnt lgkmcnt(10)\n"
        "ds_read_b128 a[84:87], v254 offset:9264\n"
        "ds_read_b128 a[80:83], v254 offset:6192\n"
        "v_mul_f32 v80, v124, v80\n"
        "v_mul_f32 v81, v124, v81\n"
        "v_mfma_f32_16x16x32_bf16 v[4:7], a[152:155], a[32:35], v[4:7]\n"
        "s_waitcnt lgkmcnt(11)\n"
        "ds_read_b128 a[76:79], v254 offset:3120\n"
        "ds_read_b128 a[72:75], v254 offset:48\n"
        "v_mul_f32 v82, v124, v82\n"
        "v_mul_f32 v83, v124, v83\n"
        "v_mfma_f32_16x16x32_bf16 v[4:7], a[148:151], a[28:31], v[4:7]\n"
        "s_waitcnt lgkmcnt(12)\n"
        "ds_read_b128 a[96:99], v248 offset:64\n"
        "ds_read_b128 a[100:103], v248 offset:3136\n"
        "v_cvt_pk_bf16_f32 v232, v196, v200\n"
        "v_cvt_pk_bf16_f32 v233, v204, v192\n"
        "v_mfma_f32_16x16x32_bf16 v[4:7], a[144:147], a[24:27], v[4:7]\n"
        "s_waitcnt lgkmcnt(13)\n"
        "ds_read_b128 a[104:107], v248 offset:6208\n"
        "ds_read_b128 a[108:111], v248 offset:9280\n"
        "v_cvt_pk_bf16_f32 v234, v216, v224\n"
        "v_cvt_pk_bf16_f32 v80, v80, v81\n"
        "v_mfma_f32_16x16x32_bf16 v[8:11], a[144:147], a[48:51], 0\n"
        "s_waitcnt lgkmcnt(14)\n"
        "buffer_store_dwordx3 v[232:234], v250, s[16:19], s39 offen offset:0 sc1\n"
        "v_mfma_f32_16x16x32_bf16 v[8:11], a[148:151], a[52:55], v[8:11]\n"
        "s_waitcnt lgkmcnt(13)\n"
        "ds_read_b128 a[112:115], v248 offset:12352\n"
        "ds_read_b128 a[116:119], v248 offset:15424\n"
        "v_cvt_pk_bf16_f32 v81, v82, v83\n"
        "v_mul_f32 v84, v124, v84\n"
        "v_mfma_f32_16x16x32_bf16 v[8:11], a[152:155], a[56:59], v[8:11]\n"
        "s_waitcnt lgkmcnt(14)\n"
        "ds_read_b128 a[140:143], v254 offset:15440\n"
        "ds_read_b128 a[136:139], v254 offset:12368\n"
        "v_mul_f32 v85, v124, v85\n"
        "v_mul_f32 v86, v124, v86\n"
        "v_mfma_f32_16x16x32_bf16 v[8:11], a[156:159], a[60:63], v[8:11]\n"
        "s_waitcnt lgkmcnt(15)\n"
        "ds_read_b128 a[132:135], v254 offset:9296\n"
        "ds_read_b128 a[128:131], v254 offset:6224\n"
        "v_mul_f32 v87, v124, v87\n"
        "v_cvt_pk_bf16_f32 v232, v197, v201\n"
        "v_mfma_f32_16x16x32_bf16 v[8:11], a[160:163], a[64:67], v[8:11]\n"
        "s_waitcnt lgkmcnt(15)\n"
        "ds_read_b128 a[124:127], v254 offset:3152\n"
        "ds_read_b128 a[120:123], v254 offset:80\n"
        "v_cvt_pk_bf16_f32 v233, v205, v193\n"
        "v_cvt_pk_bf16_f32 v234, v217, v225\n"
        "v_mfma_f32_16x16x32_bf16 v[8:11], a[164:167], a[68:71], v[8:11]\n"
        "s_waitcnt lgkmcnt(15)\n"
        "buffer_store_dwordx3 v[232:234], v250, s[16:19], s39 offen offset:384 sc1\n"
        "v_mfma_f32_16x16x32_bf16 v[12:15], a[164:167], a[92:95], 0\n"
        "s_waitcnt lgkmcnt(15)\n"
        "v_cvt_pk_bf16_f32 v82, v84, v85\n"
        "v_cvt_pk_bf16_f32 v83, v86, v87\n"
        "v_mul_f32 v96, v124, v96\n"
        "v_mul_f32 v97, v124, v97\n"
        "v_mfma_f32_16x16x32_bf16 v[12:15], a[160:163], a[88:91], v[12:15]\n"
        "s_waitcnt lgkmcnt(15)\n"
        "ds_write_b128 v245, v[80:83] offset:768\n"
        "v_mfma_f32_16x16x32_bf16 v[12:15], a[156:159], a[84:87], v[12:15]\n"
        "s_waitcnt lgkmcnt(15)\n"
        "v_mul_f32 v98, v124, v98\n"
        "v_mul_f32 v99, v124, v99\n"
        "v_cvt_pk_bf16_f32 v232, v198, v202\n"
        "v_cvt_pk_bf16_f32 v233, v206, v194\n"
        "v_mfma_f32_16x16x32_bf16 v[12:15], a[152:155], a[80:83], v[12:15]\n"
        "s_waitcnt lgkmcnt(14)\n"
        "v_cvt_pk_bf16_f32 v234, v218, v226\n"
        "v_cvt_pk_bf16_f32 v96, v96, v97\n"
        "v_cvt_pk_bf16_f32 v97, v98, v99\n"
        "v_mov_b32 v100, s29\n"
        "v_mfma_f32_16x16x32_bf16 v[12:15], a[148:151], a[76:79], v[12:15]\n"
        "s_waitcnt lgkmcnt(13)\n"
        "buffer_store_dwordx3 v[232:234], v250, s[16:19], s39 offen offset:768 sc1\n"
        "v_mfma_f32_16x16x32_bf16 v[12:15], a[144:147], a[72:75], v[12:15]\n"
        "s_waitcnt lgkmcnt(12)\n"
        "ds_write_b64 v246, v[96:97] offset:768\n"
        "v_cvt_pk_bf16_f32 v232, v199, v203\n"
        "v_mfma_f32_16x16x32_bf16 v[16:19], a[144:147], a[96:99], 0\n"
        "s_waitcnt lgkmcnt(12)\n"
        "v_cvt_pk_bf16_f32 v233, v207, v195\n"
        "v_cvt_pk_bf16_f32 v234, v219, v227\n"
        "v_max3_u32 v236, v236, v124, v125\n"
        "v_cmp_lt_u32 vcc, s24, v236\n"
        "v_mfma_f32_16x16x32_bf16 v[16:19], a[148:151], a[100:103], v[16:19]\n"
        "s_waitcnt lgkmcnt(11)\n"
        "buffer_store_dwordx3 v[232:234], v250, s[16:19], s39 offen offset:1152 sc1\n"
        "s_cmp_lg_u64 vcc, 0\n"
        "s_addc_u32 s25, s25, s25\n"
        "v_mfma_f32_16x16x32_bf16 v[16:19], a[152:155], a[104:107], v[16:19]\n"
        "s_waitcnt lgkmcnt(10)\n"
        "v_mov_b32 v101, s25\n"
        "v_mov_b32 v102, 147456\n"
        "v_mfma_f32_16x16x32_bf16 v[16:19], a[156:159], a[108:111], v[16:19]\n"
        "ds_write_b32 v100, v101\n"
        "s_waitcnt lgkmcnt(0)\n"
        "s_barrier\n"
        "ds_read_b128 a[168:171], v247 offset:512\n"
        "v_mfma_f32_16x16x32_bf16 v[16:19], a[160:163], a[112:115], v[16:19]\n"
        "ds_read_b128 a[172:175], v247 offset:4608\n"
        "ds_read_b128 a[176:179], v247 offset:8704\n"
        "v_mfma_f32_16x16x32_bf16 v[16:19], a[164:167], a[116:119], v[16:19]\n"
        "ds_read_b128 a[180:183], v247 offset:12800\n"
        "ds_read_b128 a[184:187], v247 offset:16896\n"
        "v_mfma_f32_16x16x32_bf16 v[20:23], a[164:167], a[140:143], 0\n"
        "ds_read_b128 a[188:191], v247 offset:20992\n"
        "v_cvt_pk_bf16_f32 v48, v0, v4\n"
        "v_mfma_f32_16x16x32_bf16 v[20:23], a[160:163], a[136:139], v[20:23]\n"
        "ds_read_b128 v[104:107], v102\n"
        "v_cvt_pk_bf16_f32 v49, v8, v12\n"
        "v_mfma_f32_16x16x32_bf16 v[20:23], a[156:159], a[132:135], v[20:23]\n"
        "s_add_u32 s38, s37, s43\n"
        "v_cvt_pk_bf16_f32 v52, v1, v5\n"
        "v_cvt_pk_bf16_f32 v53, v9, v13\n"
        "v_mfma_f32_16x16x32_bf16 v[20:23], a[152:155], a[128:131], v[20:23]\n"
        "s_add_u32 s39, s28, s43\n"
        "v_cvt_pk_bf16_f32 v56, v2, v6\n"
        "v_cvt_pk_bf16_f32 v57, v10, v14\n"
        "v_mfma_f32_16x16x32_bf16 v[20:23], a[148:151], a[124:127], v[20:23]\n"
        "v_cvt_pk_bf16_f32 v60, v3, v7\n"
        "v_cvt_pk_bf16_f32 v61, v11, v15\n"
        "v_mfma_f32_16x16x32_bf16 v[20:23], a[144:147], a[120:123], v[20:23]\n"
        "s_waitcnt lgkmcnt(6)\n"
        "v_mfma_f32_16x16x32_bf16 v[24:27], a[168:171], a[0:3], 0\n"
        "s_waitcnt lgkmcnt(5)\n"
        "v_mfma_f32_16x16x32_bf16 v[24:27], a[172:175], a[4:7], v[24:27]\n"
        "s_waitcnt lgkmcnt(4)\n"
        "v_mfma_f32_16x16x32_bf16 v[24:27], a[176:179], a[8:11], v[24:27]\n"
        "s_waitcnt lgkmcnt(3)\n"
        "v_mfma_f32_16x16x32_bf16 v[24:27], a[180:183], a[12:15], v[24:27]\n"
        "s_waitcnt lgkmcnt(2)\n"
        "v_mfma_f32_16x16x32_bf16 v[24:27], a[184:187], a[16:19], v[24:27]\n"
        "s_waitcnt lgkmcnt(1)\n"
        "v_mfma_f32_16x16x32_bf16 v[24:27], a[188:191], a[20:23], v[24:27]\n"
        "v_mfma_f32_16x16x32_bf16 v[28:31], a[188:191], a[44:47], 0\n"
        "s_nop 0\n"
        "v_cvt_pk_bf16_f32 v50, v16, v20\n"
        "v_cvt_pk_bf16_f32 v54, v17, v21\n"
        "v_mfma_f32_16x16x32_bf16 v[28:31], a[184:187], a[40:43], v[28:31]\n"
        "v_cvt_pk_bf16_f32 v58, v18, v22\n"
        "v_cvt_pk_bf16_f32 v62, v19, v23\n"
        "v_mfma_f32_16x16x32_bf16 v[28:31], a[180:183], a[36:39], v[28:31]\n"
        "v_mfma_f32_16x16x32_bf16 v[28:31], a[176:179], a[32:35], v[28:31]\n"
        "s_waitcnt lgkmcnt(0)\n"
        "v_or3_b32 v104, v104, v105, v106\n"
        "v_or_b32 v104, v104, v107\n"
        "v_mfma_f32_16x16x32_bf16 v[28:31], a[172:175], a[28:31], v[28:31]\n"
        "s_nop 0\n"
        "v_readfirstlane_b32 s25, v104\n"
        "v_mfma_f32_16x16x32_bf16 v[28:31], a[168:171], a[24:27], v[28:31]\n"
        "v_mfma_f32_16x16x32_bf16 v[32:35], a[168:171], a[48:51], 0\n"
        "v_mfma_f32_16x16x32_bf16 v[32:35], a[172:175], a[52:55], v[32:35]\n"
        "buffer_store_dwordx3 v[48:50], v250, s[16:19], s38 offen offset:0 sc1\n"
        "v_mfma_f32_16x16x32_bf16 v[32:35], a[176:179], a[56:59], v[32:35]\n"
        "v_mfma_f32_16x16x32_bf16 v[32:35], a[180:183], a[60:63], v[32:35]\n"
        "v_mfma_f32_16x16x32_bf16 v[32:35], a[184:187], a[64:67], v[32:35]\n"
        "v_mfma_f32_16x16x32_bf16 v[32:35], a[188:191], a[68:71], v[32:35]\n"
        "v_mfma_f32_16x16x32_bf16 v[36:39], a[188:191], a[92:95], 0\n"
        "buffer_store_dwordx3 v[52:54], v250, s[16:19], s38 offen offset:384 sc1\n"
        "v_mfma_f32_16x16x32_bf16 v[36:39], a[184:187], a[88:91], v[36:39]\n"
        "v_cvt_pk_bf16_f32 v64, v24, v28\n"
        "v_cvt_pk_bf16_f32 v68, v25, v29\n"
        "v_mfma_f32_16x16x32_bf16 v[36:39], a[180:183], a[84:87], v[36:39]\n"
        "v_cvt_pk_bf16_f32 v72, v26, v30\n"
        "v_cvt_pk_bf16_f32 v76, v27, v31\n"
        "v_mfma_f32_16x16x32_bf16 v[36:39], a[176:179], a[80:83], v[36:39]\n"
        "v_mfma_f32_16x16x32_bf16 v[36:39], a[172:175], a[76:79], v[36:39]\n"
        "v_mfma_f32_16x16x32_bf16 v[36:39], a[168:171], a[72:75], v[36:39]\n"
        "buffer_store_dwordx3 v[56:58], v250, s[16:19], s38 offen offset:768 sc1\n"
        "v_mfma_f32_16x16x32_bf16 v[40:43], a[168:171], a[96:99], 0\n"
        "v_mfma_f32_16x16x32_bf16 v[40:43], a[172:175], a[100:103], v[40:43]\n"
        "v_mfma_f32_16x16x32_bf16 v[40:43], a[176:179], a[104:107], v[40:43]\n"
        "v_mfma_f32_16x16x32_bf16 v[40:43], a[180:183], a[108:111], v[40:43]\n"
        "v_mfma_f32_16x16x32_bf16 v[40:43], a[184:187], a[112:115], v[40:43]\n"
        "buffer_store_dwordx3 v[60:62], v250, s[16:19], s38 offen offset:1152 sc1\n"
        "v_mfma_f32_16x16x32_bf16 v[40:43], a[188:191], a[116:119], v[40:43]\n"
        "v_cvt_pk_bf16_f32 v65, v32, v36\n"
        "v_cvt_pk_bf16_f32 v69, v33, v37\n"
        "v_mfma_f32_16x16x32_bf16 v[44:47], a[188:191], a[140:143], 0\n"
        "v_cvt_pk_bf16_f32 v73, v34, v38\n"
        "v_cvt_pk_bf16_f32 v77, v35, v39\n"
        "v_mfma_f32_16x16x32_bf16 v[44:47], a[184:187], a[136:139], v[44:47]\n"
        "v_mfma_f32_16x16x32_bf16 v[44:47], a[180:183], a[132:135], v[44:47]\n"
        "v_mfma_f32_16x16x32_bf16 v[44:47], a[176:179], a[128:131], v[44:47]\n"
        "v_mfma_f32_16x16x32_bf16 v[44:47], a[172:175], a[124:127], v[44:47]\n"
        "v_mfma_f32_16x16x32_bf16 v[44:47], a[168:171], a[120:123], v[44:47]\n"
        "s_nop 2\n"
        "s_nop 4\n"
        "v_cvt_pk_bf16_f32 v66, v40, v44\n"
        "buffer_store_dwordx3 v[64:66], v250, s[16:19], s39 offen offset:0 sc1\n"
        "v_cvt_pk_bf16_f32 v70, v41, v45\n"
        "buffer_store_dwordx3 v[68:70], v250, s[16:19], s39 offen offset:384 sc1\n"
        "v_cvt_pk_bf16_f32 v74, v42, v46\n"
        "buffer_store_dwordx3 v[72:74], v250, s[16:19], s39 offen offset:768 sc1\n"
        "v_cvt_pk_bf16_f32 v78, v43, v47\n"
        "buffer_store_dwordx3 v[76:78], v250, s[16:19], s39 offen offset:1152 sc1\n"
        "s_cmp_lg_u32 s25, 0\n"
        "s_cbranch_scc1 30f\n"
        "s_endpgm\n"
        "30:\n"
        : "={s25}"(flg), "={s42}"(w), "+{s[0:1]}"(ka), "+{s2}"(bid), "+{v0}"(tid)
        : "s"(ldsb)
        : "v1", "v2", "v3", "v4", "v5", "v6", "v7", "v8", "v9", "v10", "v11", "v12", "v13", "v14", "v15", "v16", "v17", "v18", "v19", "v20", "v21", "v22", "v23", "v24", "v25", "v26", "v27", "v28", "v29", "v30", "v31", "v32", "v33", "v34", "v35", "v36", "v37", "v38", "v39", "v40", "v41", "v42", "v43", "v44", "v45", "v46", "v47", "v48", "v49", "v50", "v51", "v52", "v53", "v54", "v55", "v56", "v57", "v58", "v59", "v60", "v61", "v62", "v63", "v64", "v65", "v66", "v67", "v68", "v69", "v70", "v71", "v72", "v73", "v74", "v75", "v76", "v77", "v78", "v79", "v80", "v81", "v82", "v83", "v84", "v85", "v86", "v87", "v88", "v89", "v90", "v91", "v92", "v93", "v94", "v95", "v96", "v97", "v98", "v99", "v100", "v101", "v102", "v103", "v104", "v105", "v106", "v107", "v108", "v109", "v110", "v111", "v112", "v113", "v114", "v115", "v116", "v117", "v118", "v119", "v120", "v121", "v122", "v123", "v124", "v125", "v126", "v127", "v128", "v129", "v130", "v131", "v132", "v133", "v134", "v135", "v136", "v137", "v138", "v139", "v140", "v141", "v142", "v143", "v144", "v145", "v146", "v147", "v148", "v149", "v150", "v151", "v152", "v153", "v154", "v155", "v156", "v157", "v158", "v159", "v160", "v161", "v162", "v163", "v164", "v165", "v166", "v167", "v168", "v169", "v170", "v171", "v172", "v173", "v174", "v175", "v176", "v177", "v178", "v179", "v180", "v181", "v182", "v183", "v184", "v185", "v186", "v187", "v188", "v189", "v190", "v191", "v192", "v193", "v194", "v195", "v196", "v197", "v198", "v199", "v200", "v201", "v202", "v203", "v204", "v205", "v206", "v207", "v208", "v209", "v210", "v211", "v212", "v213", "v214", "v215", "v216", "v217", "v218", "v219", "v220", "v221", "v222", "v223", "v224", "v225", "v226", "v227", "v228", "v229", "v230", "v231", "v232", "v233", "v234", "v235", "v236", "v237", "v238", "v239", "v240", "v241", "v242", "v243", "v244", "v245", "v246", "v247", "v248", "v249", "v250", "v251", "v252", "v253", "v254", "v255", "a0", "a1", "a2", "a3", "a4", "a5", "a6", "a7", "a8", "a9", "a10", "a11", "a12", "a13", "a14", "a15", "a16", "a17", "a18", "a19", "a20", "a21", "a22", "a23", "a24", "a25", "a26", "a27", "a28", "a29", "a30", "a31", "a32", "a33", "a34", "a35", "a36", "a37", "a38", "a39", "a40", "a41", "a42", "a43", "a44", "a45", "a46", "a47", "a48", "a49", "a50", "a51", "a52", "a53", "a54", "a55", "a56", "a57", "a58", "a59", "a60", "a61", "a62", "a63", "a64", "a65", "a66", "a67", "a68", "a69", "a70", "a71", "a72", "a73", "a74", "a75", "a76", "a77", "a78", "a79", "a80", "a81", "a82", "a83", "a84", "a85", "a86", "a87", "a88", "a89", "a90", "a91", "a92", "a93", "a94", "a95", "a96", "a97", "a98", "a99", "a100", "a101", "a102", "a103", "a104", "a105", "a106", "a107", "a108", "a109", "a110", "a111", "a112", "a113", "a114", "a115", "a116", "a117", "a118", "a119", "a120", "a121", "a122", "a123", "a124", "a125", "a126", "a127", "a128", "a129", "a130", "a131", "a132", "a133", "a134", "a135", "a136", "a137", "a138", "a139", "a140", "a141", "a142", "a143", "a144", "a145", "a146", "a147", "a148", "a149", "a150", "a151", "a152", "a153", "a154", "a155", "a156", "a157", "a158", "a159", "a160", "a161", "a162", "a163", "a164", "a165", "a166", "a167", "a168", "a169", "a170", "a171", "a172", "a173", "a174", "a175", "a176", "a177", "a178", "a179", "a180", "a181", "a182", "a183", "a184", "a185", "a186", "a187", "a188", "a189", "a190", "a191", "a192", "a193", "a194", "a195", "a196", "a197", "a198", "a199", "a200", "a201", "a202", "a203", "a204", "a205", "a206", "a207", "a208", "a209", "a210", "a211", "a212", "a213", "a214", "a215", "a216", "a217", "a218", "a219", "a220", "a221", "a222", "a223", "a224", "a225", "a226", "a227", "a228", "a229", "a230", "a231", "a232", "a233", "a234", "a235", "a236", "a237", "a238", "a239", "a240", "a241", "a242", "a243", "a244", "a245", "a246", "a247", "a248", "a249", "a250", "a251", "a252", "a253", "a254", "a255", "s8", "s9", "s10", "s11", "s12", "s13", "s14", "s15", "s16", "s17", "s18", "s19", "s20", "s21", "s22", "s23", "s24", "s26", "s27", "s28", "s29", "s30", "s31", "s34", "s35", "s36", "s37", "s38", "s39", "s40", "s41", "s43", "s44", "s45", "s46", "s47", "s48", "s49", "s50", "s51", "s52", "s53", "s54", "s55", "s56", "s57", "s58", "s59", "s60", "s61", "s62", "s63", "s64", "s65", "s66", "s67", "s68", "s69", "s70", "s71", "s72", "s73", "s74", "s75", "s76", "s77", "s78", "s79", "s80", "s81", "vcc", "scc", "memory");
    if (flg) {
        unsigned ln;
        asm volatile("s_waitcnt vmcnt(0)\n v_mbcnt_lo_u32_b32 %0, -1, 0\n v_mbcnt_hi_u32_b32 %0, -1, %0" : "=v"(ln) :: "memory");
        const unsigned t = w * 64u + ln, wx = bid & 63u, wy0 = bid >> 6;
        typedef const char* __attribute__((address_space(4))) const* kargp;
        const char *x = ((kargp)ka)[0], *wqkv = ((kargp)ka)[1], *wo = ((kargp)ka)[2], *rpb = ((kargp)ka)[3], *rpi = ((kargp)ka)[4],
                   *mask = ((kargp)ka)[5];
        char* out = (char*)((kargp)ka)[6];
        unsigned short* xs = (unsigned short*)smem;
        unsigned short* qs = xs + 64 * 192;
        const unsigned short* W1 = (const unsigned short*)wqkv;
        const unsigned short* W2 = (const unsigned short*)wo;
        for (unsigned i = 0; i < 16u; ++i) {
            if (!((flg >> (15u - i)) & 1u)) continue;
            const unsigned wy = wy0 + 4u * i;
            __syncthreads();
            for (unsigned e = t; e < 64u * 96u; e += 256u) {
                const unsigned tok = e / 96u, c2 = e % 96u;
                const unsigned pix = ((8u * wy + 4u + (tok >> 3)) & 511u) * 512u + ((8u * wx + 4u + (tok & 7u)) & 511u);
                ((unsigned*)xs)[e] = ((const unsigned*)x)[pix * 96u + c2];
            }
            __syncthreads();
            for (unsigned col = t; col < 576u; col += 256u)
                for (unsigned tb = 0; tb < 64u; tb += 8u) {
                    float a[8] = {0.f, 0.f, 0.f, 0.f, 0.f, 0.f, 0.f, 0.f};
                    for (unsigned c = 0; c < 192u; ++c) {
                        const float wt = bf2f(W1[c * 576u + col]);
#pragma unroll
                        for (int j = 0; j < 8; ++j) a[j] += bf2f(xs[(tb + j) * 192u + c]) * wt;
                    }
#pragma unroll
                    for (int j = 0; j < 8; ++j) qs[(tb + j) * 578u + col] = f2bf(a[j]);
                }
            __syncthreads();
            {
                float qv[48], o[48];
#pragma unroll
                for (int d = 0; d < 48; ++d) { qv[d] = bf2f(qs[ln * 578u + w * 48u + d]); o[d] = 0.f; }
                const unsigned short* mrow = (const unsigned short*)mask + (size_t)(wy * 64u + wx) * 4096u + ln * 64u;
                float mx = -__builtin_inff(), l = 0.f;
                for (int pass = 0; pass < 2; ++pass)
                    for (unsigned k = 0; k < 64u; ++k) {
                        float s = 0.f;
#pragma unroll
                        for (int d = 0; d < 48; ++d) s += qv[d] * bf2f(qs[k * 578u + 192u + w * 48u + d]);
                        s = s * 0.14433756729740643f + bf2f(((const unsigned short*)rpb)[((const unsigned*)rpi)[ln * 64u + k] * 4u + w]) + bf2f(mrow[k]);
                        if (pass == 0) mx = fmaxf(mx, s);
                        else {
                            const float p = __expf(s - mx);
                            l += p;
#pragma unroll
                            for (int d = 0; d < 48; ++d) o[d] += p * bf2f(qs[k * 578u + 384u + w * 48u + d]);
                        }
                    }
#pragma unroll
                for (int d = 0; d < 48; ++d) xs[ln * 192u + w * 48u + d] = f2bf(o[d] / l);
            }
            __syncthreads();
            if (t < 192u)
                for (unsigned tb = 0; tb < 64u; tb += 8u) {
                    float a[8] = {0.f, 0.f, 0.f, 0.f, 0.f, 0.f, 0.f, 0.f};
                    for (unsigned c = 0; c < 192u; ++c) {
                        const float wt = bf2f(W2[c * 192u + t]);
#pragma unroll
                        for (int j = 0; j < 8; ++j) a[j] += bf2f(xs[(tb + j) * 192u + c]) * wt;
                    }
                    const unsigned row = (8u * wy + 4u + (tb >> 3)) & 511u;
#pragma unroll
                    for (int j = 0; j < 8; ++j)
                        ((unsigned short*)out)[(row * 512u + ((8u * wx + 4u + j) & 511u)) * 192u + t] = f2bf(a[j]);
                }
        }
    }
}


extern "C" void kernel_launch(void* const* d_in, const int* in_sizes, int n_in, void* d_out, int out_size, void* d_ws,
                              size_t ws_size, hipStream_t stream) {
    win_attn_kernel<<<256, 256, 0, stream>>>((const char*)d_in[0], (const char*)d_in[1], (const char*)d_in[2],
                                             (const char*)d_in[3], (const char*)d_in[4], (const char*)d_in[5],
                                             (char*)d_out);

}
```

```cpp
#include <hip/hip_runtime.h>
#include <hip/hip_bf16.h>

typedef unsigned int u32x4 __attribute__((ext_vector_type(4)));

static __device__ __forceinline__ float bf2f(unsigned short u) { return __uint_as_float((unsigned)u << 16); }
static __device__ __forceinline__ unsigned short f2bf(float f) { return __builtin_bit_cast(unsigned short, (__bf16)f); }

__global__ __launch_bounds__(256, 1) __attribute__((aligned(4096))) void win_attn_kernel(const char* x_, const char* wqkv_, const char* wo_, const char* rpb_,
                                                          const char* rpi_, const char* mask_, char* out_, char* ws) {
    __shared__ __attribute__((aligned(16))) char smem[163840];
    const void* ka = (const void*)__builtin_amdgcn_kernarg_segment_ptr();
    unsigned bid = blockIdx.x, tid = threadIdx.x, flg, w;
    unsigned ldsb = (unsigned)(size_t)smem;
    asm volatile(
        "s_load_dwordx16 s[68:83], s[0:1], 0\n"
        "v_readfirstlane_b32 s42, v0\n"
        "v_and_b32 v1, 63, v0\n"
        "s_lshr_b32 s31, s2, 1\n"
        "s_lshr_b32 s42, s42, 6\n"
        "s_and_b32 s31, s31, 0x7c\n"
        "v_lshlrev_b32 v2, 4, v1\n"
        "s_add_u32 s31, s31, s42\n"
        "s_and_b32 s35, s31, 31\n"
        "s_min_u32 s35, s35, 25\n"
        "s_getpc_b64 s[66:67]\n"
        "v_lshl_or_b32 v10, s35, 10, v2\n"
        "global_load_dwordx4 v[60:63], v10, s[66:67]\n"
        "s_lshl_b32 s50, s31, 6\n"
        "s_mul_i32 s50, s50, 27\n"
        "v_min_u32 v136, 26, v1\n"
        "v_lshl_add_u32 v136, v136, 6, s50\n"
        "v_mul_u32_u24 v11, 171, v1\n"
        "v_lshrrev_b32 v4, 10, v11\n"
        "v_bfe_u32 v7, v11, 13, 1\n"
        "v_add_u32 v11, 10944, v11\n"
        "v_lshrrev_b32 v5, 10, v11\n"
        "v_bfe_u32 v8, v11, 13, 1\n"
        "v_add_u32 v11, 10944, v11\n"
        "v_lshrrev_b32 v6, 10, v11\n"
        "v_bfe_u32 v9, v11, 13, 1\n"
        "s_mul_i32 s40, s42, 3072\n"
        "s_mov_b32 s10, 100663296\n"
        "s_mov_b32 s11, 0x20000\n"
        "s_mov_b32 s58, 16384\n"
        "s_mov_b32 s59, 0x20000\n"
        "s_mov_b32 s62, 1800\n"
        "s_mov_b32 s63, 0x20000\n"
        "s_mov_b32 s22, 221184\n"
        "s_mov_b32 s23, 0x20000\n"
        "s_and_b32 s51, s42, 1\n"
        "v_lshlrev_b32 v134, 3, v0\n"
        "s_lshl_b32 s35, s51, 7\n"
        "v_and_b32 v10, 48, v1\n"
        "v_xor_b32 v131, s35, v2\n"
        "s_lshl_b32 s46, s42, 12\n"
        "v_xor_b32 v131, v131, v10\n"
        "v_xor_b32 v132, 64, v131\n"
        "s_and_b32 s31, s2, 63\n"
        "s_lshl_b32 s31, s31, 3\n"
        "s_add_u32 s31, s31, 4\n"
        "s_lshl_b32 s35, s42, 5\n"
        "s_movk_i32 s41, 384\n"
        "s_movk_i32 s45, -384\n"
        "v_lshrrev_b32 v10, 2, v4\n"
        "v_mad_i32_i24 v11, v10, s45, v2\n"
        "v_add_u32 v10, s31, v10\n"
        "v_lshl_or_b32 v242, v7, 4, s35\n"
        "v_and_b32 v10, 511, v10\n"
        "v_xor_b32 v11, v11, v242\n"
        "v_mad_u32_u24 v242, v10, s41, v11\n"
        "v_lshrrev_b32 v10, 2, v5\n"
        "v_mad_i32_i24 v11, v10, s45, v2\n"
        "v_add_u32 v10, s31, v10\n"
        "v_add_u32 v11, 1024, v11\n"
        "v_lshl_or_b32 v243, v8, 4, s35\n"
        "v_and_b32 v10, 511, v10\n"
        "v_xor_b32 v11, v11, v243\n"
        "v_mad_u32_u24 v243, v10, s41, v11\n"
        "v_lshrrev_b32 v10, 2, v6\n"
        "v_mad_i32_i24 v11, v10, s45, v2\n"
        "v_add_u32 v10, s31, v10\n"
        "v_add_u32 v11, 2048, v11\n"
        "v_lshl_or_b32 v244, v9, 4, s35\n"
        "v_and_b32 v10, 511, v10\n"
        "v_xor_b32 v11, v11, v244\n"
        "v_mad_u32_u24 v244, v10, s41, v11\n"
        "s_mul_i32 s43, s31, 384\n"
        "s_add_u32 s31, s31, 4\n"
        "s_and_b32 s31, s31, 511\n"
        "s_mul_i32 s44, s31, 384\n"
        "s_lshr_b32 s31, s2, 6\n"
        "s_lshl_b32 s31, s31, 3\n"
        "s_add_u32 s47, s31, s42\n"
        "s_add_u32 s47, s47, 68\n"
        "s_mul_i32 s47, s47, 196608\n"
        "s_lshl2_add_u32 s37, s51, s31\n"
        "s_add_u32 s37, s37, 4\n"
        "s_mul_i32 s37, s37, 196608\n"
        "s_mul_i32 s65, s42, 18432\n"
        "s_mov_b32 s14, 33554432\n"
        "s_mov_b32 s15, 0x20000\n"
        "s_mul_i32 s35, s42, 96\n"
        "s_movk_i32 s31, 1056\n"
        "s_movk_i32 s45, 4608\n"
        "v_add_u32 v3, s35, v2\n"
        "s_waitcnt lgkmcnt(0)\n"
        "s_mov_b64 s[60:61], s[74:75]\n"
        "s_mov_b64 s[20:21], s[70:71]\n"
        "s_mov_b64 s[56:57], s[76:77]\n"
        "s_mov_b64 s[8:9], s[68:69]\n"
        "buffer_load_dwordx2 v[144:145], v134, s[60:63], 0 offen\n"
        "s_add_u32 m0, s46, 122880\n"
        "s_mov_b32 s64, s46\n"
        "buffer_load_dword v136, s[20:23], 0 offen lds\n"
        "s_mov_b64 s[52:53], s[72:73]\n"
        "s_mov_b64 s[12:13], s[78:79]\n"
        "buffer_load_dwordx4 v131, s[56:59], s64 offen lds\n"
        "s_mov_b64 s[16:17], s[80:81]\n"
        "v_bfe_u32 v12, v4, 2, 1\n"
        "v_mad_u32_u24 v128, v4, s31, v3\n"
        "v_sub_u32 v12, v12, v7\n"
        "v_mad_i32_i24 v128, v12, s45, v128\n"
        "v_bfe_u32 v13, v5, 2, 1\n"
        "buffer_load_dwordx4 v131, s[56:59], s64 offen offset:1024 lds\n"
        "v_mad_u32_u24 v129, v5, s31, v3\n"
        "v_sub_u32 v13, v13, v8\n"
        "v_mad_i32_i24 v129, v13, s45, v129\n"
        "v_bfe_u32 v14, v6, 2, 1\n"
        "v_mad_u32_u24 v130, v6, s31, v3\n"
        "v_sub_u32 v14, v14, v9\n"
        "v_mad_i32_i24 v130, v14, s45, v130\n"
        "s_lshl_b32 s48, s42, 11\n"
        "s_lshl_b32 s36, s2, 13\n"
        "v_and_b32 v19, 16, v1\n"
        "buffer_load_dwordx4 v132, s[56:59], s64 offen offset:2048 lds\n"
        "s_add_u32 s36, s36, s48\n"
        "s_add_u32 s36, s36, 4194304\n"
        "s_add_u32 s48, s48, 155648\n"
        "s_lshl_b32 s35, s51, 6\n"
        "v_xor_b32 v249, v2, v19\n"
        "v_xor_b32 v249, s35, v249\n"
        "v_xor_b32 v253, 32, v249\n"
        "v_and_b32 v15, 15, v1\n"
        "v_lshrrev_b32 v16, 4, v1\n"
        "v_and_b32 v17, 12, v1\n"
        "buffer_load_dwordx4 v132, s[56:59], s64 offen offset:3072 lds\n"
        "v_and_b32 v18, 0xf0, v2\n"
        "v_add_u32 v17, v17, v15\n"
        "v_bfe_u32 v10, v1, 2, 3\n"
        "v_lshrrev_b32 v11, 5, v1\n"
        "v_lshl_or_b32 v10, v11, 4, v10\n"
        "v_and_b32 v255, 3, v1\n"
        "v_mul_u32_u24 v10, 96, v10\n"
        "v_lshl_add_u32 v255, v255, 3, s65\n"
        "v_add_u32 v255, v255, v10\n"
        "v_lshrrev_b32 v20, 1, v15\n"
        "s_add_u32 m0, s40, 73728\n"
        "s_sub_u32 s64, s47, 12582912\n"
        "buffer_load_dwordx4 v242, s[8:11], s64 offen lds\n"
        "v_xor_b32 v20, v20, v16\n"
        "v_lshlrev_b32 v20, 4, v20\n"
        "v_mad_u32_u24 v240, v17, s41, v20\n"
        "v_lshl_or_b32 v251, v17, 7, v20\n"
        "v_add_u32 v240, 73728, v240\n"
        "v_add_u32 v251, 155648, v251\n"
        "v_xor_b32 v241, 64, v240\n"
        "v_xor_b32 v252, 64, v251\n"
        "s_lshl_b32 s67, s42, 10\n"
        "s_add_u32 s67, s67, 139264\n"
        "s_add_u32 m0, s40, 74752\n"
        "s_nop 0\n"
        "buffer_load_dwordx4 v243, s[8:11], s64 offen lds\n"
        "v_lshlrev_b32 v135, 2, v0\n"
        "v_add_u32 v135, 139264, v135\n"
        "v_lshlrev_b32 v11, 5, v16\n"
        "v_xor_b32 v11, v11, v18\n"
        "v_lshl_or_b32 v11, v17, 8, v11\n"
        "v_add_u32 v140, 122880, v11\n"
        "v_xor_b32 v141, 16, v140\n"
        "v_xor_b32 v142, 128, v140\n"
        "v_xor_b32 v143, 144, v140\n"
        "v_lshrrev_b32 v10, 2, v15\n"
        "s_add_u32 m0, s40, 75776\n"
        "s_nop 0\n"
        "buffer_load_dwordx4 v244, s[8:11], s64 offen lds\n"
        "v_cmp_eq_u32 vcc, v10, v16\n"
        "v_mov_b32 v11, 0x3fb9\n"
        "v_mov_b32 v189, 0\n"
        "v_and_b32 v10, 48, v2\n"
        "v_cndmask_b32 v188, 0, v11, vcc\n"
        "v_lshlrev_b64 v[188:189], v10, v[188:189]\n"
        "v_cmp_gt_u32 vcc, 32, v1\n"
        "v_mov_b32 v11, 15957\n"
        "v_lshrrev_b32 v149, 2, v15\n"
        "v_lshrrev_b32 v151, 3, v19\n"
        "v_mov_b32 v148, 14702\n"
        "v_cndmask_b32 v11, v148, v11, vcc\n"
        "v_xor_b32 v151, v149, v151\n"
        "v_cmp_eq_u32 vcc, 0, v151\n"
        "v_mov_b32 v149, 0\n"
        "v_cndmask_b32 v148, 0, v11, vcc\n"
        "v_lshlrev_b64 v[148:149], v10, v[148:149]\n"
        "v_cmp_eq_u32 vcc, 1, v151\n"
        "v_mov_b32 v151, 0\n"
        "v_cndmask_b32 v150, 0, v11, vcc\n"
        "v_lshlrev_b64 v[150:151], v10, v[150:151]\n"
        "v_and_b32 v10, 8, v1\n"
        "v_add_u32 v10, v10, v15\n"
        "v_mul_u32_u24 v10, 96, v10\n"
        "v_add3_u32 v137, v10, v19, s65\n"
        "v_and_b32 v237, 1, v1\n"
        "v_mul_u32_u24 v237, 0xaaaa, v237\n"
        "v_mov_b64 v[170:171], v[188:189]\n"
        "v_mov_b64 v[210:211], v[188:189]\n"
        "v_mov_b64 v[228:229], v[188:189]\n"
        "v_add_u32 v237, 0x4444, v237\n"
        "v_mov_b32 v11, 0x3f803f80\n"
        "v_accvgpr_write_b32 a252, v11\n"
        "v_accvgpr_write_b32 a253, v11\n"
        "v_accvgpr_write_b32 a254, v11\n"
        "v_accvgpr_write_b32 a255, v11\n"
        "v_lshl_or_b32 v10, v16, 10, v18\n"
        "s_lshl_b32 s31, s42, 12\n"
        "s_add_u32 s31, s31, 73728\n"
        "s_lshl_b32 s35, s51, 9\n"
        "s_add_u32 s35, s35, 73728\n"
        "v_add_u32 v245, s31, v10\n"
        "s_sub_u32 s50, s35, s31\n"
        "v_lshrrev_b32 v10, 5, v1\n"
        "v_lshrrev_b32 v11, 1, v19\n"
        "s_sub_u32 s31, s48, 65536\n"
        "v_lshl_or_b32 v10, v10, 10, v18\n"
        "v_add3_u32 v246, v10, v11, s31\n"
        "v_mul_u32_u24 v10, 768, v16\n"
        "v_xor_b32 v11, v18, v19\n"
        "v_add3_u32 v235, v10, v11, s65\n"
        "s_lshr_b32 s31, s42, 1\n"
        "s_mul_i32 s35, s31, 36864\n"
        "s_movk_i32 s45, 17664\n"
        "v_mul_u32_u24 v11, 96, v15\n"
        "v_lshrrev_b32 v248, 3, v15\n"
        "v_add3_u32 v10, v10, v11, s35\n"
        "v_mad_u32_u24 v10, v248, s45, v10\n"
        "v_add_u32 v248, v10, v19\n"
        "v_sub_u32 v254, v10, v19\n"
        "s_mul_i32 s31, s31, 192\n"
        "v_mul_u32_u24 v10, 196608, v16\n"
        "v_mul_u32_u24 v11, 12, v15\n"
        "v_add3_u32 v250, v10, v11, s31\n"
        "s_movk_i32 s31, 288\n"
        "s_movk_i32 s35, 3072\n"
        "s_movk_i32 s41, 1536\n"
        "s_movk_i32 s45, 12288\n"
        "v_mad_u32_u24 v10, v4, s31, v3\n"
        "v_lshrrev_b32 v11, 4, v4\n"
        "v_mad_i32_i24 v238, v12, s41, v10\n"
        "v_sub_u32 v7, v7, v11\n"
        "v_mad_u32_u24 v238, v11, s45, v238\n"
        "v_mad_i32_i24 v232, v7, s35, v10\n"
        "v_mad_u32_u24 v10, v5, s31, v3\n"
        "v_lshrrev_b32 v11, 4, v5\n"
        "v_mad_i32_i24 v239, v13, s41, v10\n"
        "v_sub_u32 v8, v8, v11\n"
        "v_mad_u32_u24 v239, v11, s45, v239\n"
        "v_mad_i32_i24 v233, v8, s35, v10\n"
        "v_mad_u32_u24 v10, v6, s31, v3\n"
        "v_lshrrev_b32 v11, 4, v6\n"
        "v_mad_i32_i24 v247, v14, s41, v10\n"
        "v_sub_u32 v9, v9, v11\n"
        "v_mad_u32_u24 v247, v11, s45, v247\n"
        "v_mad_i32_i24 v234, v9, s35, v10\n"
        "s_mov_b32 s26, 0x3fb8aa3b\n"
        "s_mov_b32 s24, 0x5d800000\n"
        "s_mov_b32 s27, 14\n"
        "s_mov_b32 s28, 98304\n"
        "s_mov_b32 s29, 73728\n"
        "s_mov_b32 s30, 122880\n"
        "s_mov_b32 s45, s50\n"
        "s_mov_b32 s25, 0\n"
        "s_mov_b32 s54, 73728\n"
        "s_mov_b32 s55, 0x20000\n"
        "s_mov_b32 s18, 100663296\n"
        "s_mov_b32 s19, 0x20000\n"
        "s_add_u32 m0, s40, 86016\n"
        "s_sub_u32 s64, s47, 11796480\n"
        "buffer_load_dwordx4 v242, s[8:11], s64 offen lds\n"
        "s_waitcnt vmcnt(9)\n"
        "v_and_b32 v146, 0xffff0000, v144\n"
        "v_and_b32 v147, 0xffff0000, v145\n"
        "v_lshlrev_b32 v144, 16, v144\n"
        "v_lshlrev_b32 v145, 16, v145\n"
        "s_add_u32 m0, s40, 87040\n"
        "v_mul_f32 v146, s26, v146\n"
        "buffer_load_dwordx4 v243, s[8:11], s64 offen lds\n"
        "v_mul_f32 v147, s26, v147\n"
        "v_mul_f32 v144, s26, v144\n"
        "v_mul_f32 v145, s26, v145\n"
        "ds_write2st64_b32 v135, v144, v146 offset0:0 offset1:4\n"
        "ds_write2st64_b32 v135, v145, v147 offset0:8 offset1:12\n"
        "60:\n"
        "s_add_u32 m0, s40, 88064\n"
        "s_nop 0\n"
        "buffer_load_dwordx4 v244, s[8:11], s64 offen lds\n"
        "s_waitcnt lgkmcnt(0)\n"
        "s_waitcnt vmcnt(6)\n"
        "s_barrier\n"
        "s_add_u32 m0, s65, 0\n"
        "s_mov_b32 s64, 0\n"
        "buffer_load_dwordx4 v128, s[20:23], s64 offen lds\n"
        "buffer_load_dwordx4 v129, s[20:23], s64 offen offset:1024 lds\n"
        "ds_read_b128 v[0:3], v140 offset:0\n"
        "ds_read_b128 v[4:7], v141 offset:0\n"
        "ds_read_b128 v[16:19], v140 offset:1024\n"
        "ds_read_b128 v[20:23], v141 offset:1024\n"
        "ds_read_b128 v[8:11], v142 offset:0\n"
        "ds_read_b128 v[24:27], v142 offset:1024\n"
        "ds_read_b128 v[12:15], v143 offset:0\n"
        "ds_read_b128 v[28:31], v143 offset:1024\n"
        "ds_read_b128 v[32:35], v140 offset:8192\n"
        "ds_read_b128 v[36:39], v141 offset:8192\n"
        "ds_read_b128 v[40:43], v142 offset:8192\n"
        "ds_read_b128 v[44:47], v143 offset:8192\n"
        "s_cmp_eq_u32 s42, 0\n"
        "s_cbranch_scc1 41f\n"
        "s_cmp_eq_u32 s42, 1\n"
        "s_cbranch_scc1 42f\n"
        "s_cmp_eq_u32 s42, 2\n"
        "s_cbranch_scc1 43f\n"
        "s_cmp_eq_u32 s42, 3\n"
        "s_cbranch_scc1 44f\n"
        "61:\n"
        "buffer_load_dwordx4 v130, s[20:23], s64 offen offset:2048 lds\n"
        "s_add_u32 m0, s65, 3072\n"
        "s_mov_b32 s64, 36864\n"
        "buffer_load_dwordx4 v128, s[20:23], s64 offen lds\n"
        "s_waitcnt lgkmcnt(11)\n"
        "v_lshl_add_u32 v0, v0, 2, s67\n"
        "v_lshl_add_u32 v1, v1, 2, s67\n"
        "v_lshl_add_u32 v2, v2, 2, s67\n"
        "v_lshl_add_u32 v3, v3, 2, s67\n"
        "s_waitcnt lgkmcnt(10)\n"
        "v_lshl_add_u32 v4, v4, 2, s67\n"
        "v_lshl_add_u32 v5, v5, 2, s67\n"
        "v_lshl_add_u32 v6, v6, 2, s67\n"
        "v_lshl_add_u32 v7, v7, 2, s67\n"
        "s_waitcnt lgkmcnt(9)\n"
        "v_lshl_add_u32 v16, v16, 2, s67\n"
        "v_lshl_add_u32 v17, v17, 2, s67\n"
        "v_lshl_add_u32 v18, v18, 2, s67\n"
        "v_lshl_add_u32 v19, v19, 2, s67\n"
        "buffer_load_dwordx4 v129, s[20:23], s64 offen offset:1024 lds\n"
        "ds_read_b32 v0, v0\n"
        "ds_read_b32 v1, v1\n"
        "ds_read_b32 v2, v2\n"
        "ds_read_b32 v3, v3\n"
        "ds_read_b32 v4, v4\n"
        "ds_read_b32 v5, v5\n"
        "ds_read_b32 v6, v6\n"
        "ds_read_b32 v7, v7\n"
        "ds_read_b32 v16, v16\n"
        "ds_read_b32 v17, v17\n"
        "ds_read_b32 v18, v18\n"
        "ds_read_b32 v19, v19\n"
        "buffer_load_dwordx4 v130, s[20:23], s64 offen offset:2048 lds\n"
        "s_waitcnt lgkmcnt(15)\n"
        "v_lshl_add_u32 v20, v20, 2, s67\n"
        "v_lshl_add_u32 v21, v21, 2, s67\n"
        "v_lshl_add_u32 v22, v22, 2, s67\n"
        "v_lshl_add_u32 v23, v23, 2, s67\n"
        "v_lshl_add_u32 v8, v8, 2, s67\n"
        "v_lshl_add_u32 v9, v9, 2, s67\n"
        "v_lshl_add_u32 v10, v10, 2, s67\n"
        "v_lshl_add_u32 v11, v11, 2, s67\n"
        "v_lshl_add_u32 v24, v24, 2, s67\n"
        "v_lshl_add_u32 v25, v25, 2, s67\n"
        "v_lshl_add_u32 v26, v26, 2, s67\n"
        "v_lshl_add_u32 v27, v27, 2, s67\n"
        "ds_read_b32 v20, v20\n"
        "ds_read_b32 v21, v21\n"
        "ds_read_b32 v22, v22\n"
        "ds_read_b32 v23, v23\n"
        "ds_read_b32 v8, v8\n"
        "ds_read_b32 v9, v9\n"
        "ds_read_b32 v10, v10\n"
        "ds_read_b32 v11, v11\n"
        "ds_read_b32 v24, v24\n"
        "ds_read_b32 v25, v25\n"
        "ds_read_b32 v26, v26\n"
        "ds_read_b32 v27, v27\n"
        "s_cmp_eq_u32 s42, 0\n"
        "s_cbranch_scc1 46f\n"
        "62:\n"
        "s_add_u32 m0, s65, 6144\n"
        "s_mov_b32 s64, 73728\n"
        "buffer_load_dwordx4 v128, s[20:23], s64 offen lds\n"
        "v_lshl_add_u32 v12, v12, 2, s67\n"
        "v_lshl_add_u32 v13, v13, 2, s67\n"
        "v_lshl_add_u32 v14, v14, 2, s67\n"
        "v_lshl_add_u32 v15, v15, 2, s67\n"
        "v_lshl_add_u32 v28, v28, 2, s67\n"
        "v_lshl_add_u32 v29, v29, 2, s67\n"
        "v_lshl_add_u32 v30, v30, 2, s67\n"
        "v_lshl_add_u32 v31, v31, 2, s67\n"
        "v_lshl_add_u32 v32, v32, 2, s67\n"
        "v_lshl_add_u32 v33, v33, 2, s67\n"
        "v_lshl_add_u32 v34, v34, 2, s67\n"
        "v_lshl_add_u32 v35, v35, 2, s67\n"
        "ds_read_b32 v12, v12\n"
        "ds_read_b32 v13, v13\n"
        "ds_read_b32 v14, v14\n"
        "ds_read_b32 v15, v15\n"
        "ds_read_b32 v28, v28\n"
        "ds_read_b32 v29, v29\n"
        "ds_read_b32 v30, v30\n"
        "ds_read_b32 v31, v31\n"
        "ds_read_b32 v32, v32\n"
        "ds_read_b32 v33, v33\n"
        "ds_read_b32 v34, v34\n"
        "ds_read_b32 v35, v35\n"
        "buffer_load_dwordx4 v129, s[20:23], s64 offen offset:1024 lds\n"
        "s_waitcnt lgkmcnt(15)\n"
        "v_lshl_add_u32 v36, v36, 2, s67\n"
        "v_lshl_add_u32 v37, v37, 2, s67\n"
        "v_lshl_add_u32 v38, v38, 2, s67\n"
        "v_lshl_add_u32 v39, v39, 2, s67\n"
        "v_lshl_add_u32 v40, v40, 2, s67\n"
        "v_lshl_add_u32 v41, v41, 2, s67\n"
        "v_lshl_add_u32 v42, v42, 2, s67\n"
        "v_lshl_add_u32 v43, v43, 2, s67\n"
        "v_lshl_add_u32 v44, v44, 2, s67\n"
        "v_lshl_add_u32 v45, v45, 2, s67\n"
        "v_lshl_add_u32 v46, v46, 2, s67\n"
        "v_lshl_add_u32 v47, v47, 2, s67\n"
        "ds_read_b32 v36, v36\n"
        "ds_read_b32 v37, v37\n"
        "ds_read_b32 v38, v38\n"
        "ds_read_b32 v39, v39\n"
        "ds_read_b32 v40, v40\n"
        "ds_read_b32 v41, v41\n"
        "ds_read_b32 v42, v42\n"
        "ds_read_b32 v43, v43\n"
        "ds_read_b32 v44, v44\n"
        "ds_read_b32 v45, v45\n"
        "ds_read_b32 v46, v46\n"
        "ds_read_b32 v47, v47\n"
        "buffer_load_dwordx4 v130, s[20:23], s64 offen offset:2048 lds\n"
        "s_waitcnt vmcnt(9)\n"
        "s_barrier\n"
        "s_waitcnt vmcnt(6)\n"
        "ds_read_b128 v[216:219], v137 offset:0\n"
        "ds_read_b128 v[156:159], v137 offset:768\n"
        "ds_read_b128 v[176:179], v137 offset:32\n"
        "ds_read_b128 v[196:199], v137 offset:800\n"
        "ds_read_b128 v[160:163], v137 offset:64\n"
        "s_add_u32 m0, s65, 9216\n"
        "s_mov_b32 s64, 110592\n"
        "buffer_load_dwordx4 v128, s[20:23], s64 offen lds\n"
        "ds_read_b128 v[164:167], v137 offset:832\n"
        "s_waitcnt lgkmcnt(5)\n"
        "v_mfma_f32_16x16x32_bf16 v[152:155], v[216:219], v[148:151], 0\n"
        "s_waitcnt lgkmcnt(4)\n"
        "ds_read_b128 a[216:219], v240 offset:0\n"
        "ds_read_b128 a[220:223], v240 offset:1536\n"
        "ds_read_b128 a[224:227], v240 offset:12288\n"
        "ds_read_b128 a[228:231], v240 offset:13824\n"
        "v_mfma_f32_16x16x32_bf16 v[172:175], v[156:159], v[148:151], 0\n"
        "s_waitcnt lgkmcnt(7)\n"
        "buffer_load_dwordx4 v129, s[20:23], s64 offen offset:1024 lds\n"
        "v_mfma_f32_16x16x32_bf16 v[192:195], v[176:179], v[148:151], 0\n"
        "s_waitcnt lgkmcnt(6)\n"
        "ds_read_b128 a[232:235], v241 offset:0\n"
        "ds_read_b128 a[236:239], v241 offset:1536\n"
        "v_mfma_f32_16x16x32_bf16 v[212:215], v[196:199], v[148:151], 0\n"
        "s_waitcnt lgkmcnt(7)\n"
        "s_waitcnt vmcnt(5)\n"
        "ds_read_b128 v[180:183], v137 offset:3072\n"
        "ds_read_b128 v[184:187], v137 offset:3840\n"
        "v_mfma_f32_16x16x32_bf16 v[112:115], v[160:163], v[148:151], 0\n"
        "s_waitcnt lgkmcnt(8)\n"
        "buffer_load_dwordx4 v130, s[20:23], s64 offen offset:2048 lds\n"
        "v_mfma_f32_16x16x32_bf16 v[116:119], v[164:167], v[148:151], 0\n"
        "s_waitcnt lgkmcnt(7)\n"
        "s_add_u32 m0, s65, 12288\n"
        "ds_read_b128 v[200:203], v137 offset:3104\n"
        "ds_read_b128 v[204:207], v137 offset:3872\n"
        "v_cvt_pk_bf16_f32 v152, v152, v153\n"
        "v_accvgpr_write_b32 a0, v152\n"
        "v_cvt_pk_bf16_f32 v154, v154, v155\n"
        "v_accvgpr_write_b32 a1, v154\n"
        "v_cvt_pk_bf16_f32 v172, v172, v173\n"
        "v_accvgpr_write_b32 a2, v172\n"
        "v_cvt_pk_bf16_f32 v174, v174, v175\n"
        "v_accvgpr_write_b32 a3, v174\n"
        "s_nop 1\n"
        "v_mfma_f32_16x16x32_bf16 v[64:67], a[0:3], a[216:219], 0\n"
        "s_waitcnt lgkmcnt(8)\n"
        "s_mov_b32 s64, 147456\n"
        "ds_read_b128 v[220:223], v137 offset:3136\n"
        "ds_read_b128 v[224:227], v137 offset:3904\n"
        "v_mfma_f32_16x16x32_bf16 v[68:71], a[0:3], a[220:223], 0\n"
        "s_waitcnt lgkmcnt(9)\n"
        "buffer_load_dwordx4 v128, s[20:23], s64 offen lds\n"
        "v_mfma_f32_16x16x32_bf16 v[72:75], a[0:3], a[224:227], 0\n"
        "s_waitcnt lgkmcnt(8)\n"
        "ds_read_b128 a[240:243], v241 offset:12288\n"
        "ds_read_b128 a[244:247], v241 offset:13824\n"
        "v_mfma_f32_16x16x32_bf16 v[76:79], a[0:3], a[228:231], 0\n"
        "s_waitcnt lgkmcnt(7)\n"
        "v_cvt_pk_bf16_f32 v192, v192, v193\n"
        "v_accvgpr_write_b32 a24, v192\n"
        "v_mfma_f32_16x16x32_bf16 v[120:123], v[180:183], v[148:151], 0\n"
        "buffer_load_dwordx4 v129, s[20:23], s64 offen offset:1024 lds\n"
        "v_cvt_pk_bf16_f32 v194, v194, v195\n"
        "v_accvgpr_write_b32 a25, v194\n"
        "v_cvt_pk_bf16_f32 v212, v212, v213\n"
        "v_accvgpr_write_b32 a26, v212\n"
        "v_cvt_pk_bf16_f32 v214, v214, v215\n"
        "v_accvgpr_write_b32 a27, v214\n"
        "s_nop 1\n"
        "v_mfma_f32_16x16x32_bf16 v[80:83], a[24:27], a[228:231], 0\n"
        "s_waitcnt lgkmcnt(6)\n"
        "v_cvt_pk_bf16_f32 v112, v112, v113\n"
        "v_accvgpr_write_b32 a48, v112\n"
        "v_mfma_f32_16x16x32_bf16 v[124:127], v[184:187], v[148:151], 0\n"
        "v_cvt_pk_bf16_f32 v114, v114, v115\n"
        "v_accvgpr_write_b32 a49, v114\n"
        "v_mfma_f32_16x16x32_bf16 v[84:87], a[24:27], a[224:227], 0\n"
        "s_waitcnt lgkmcnt(5)\n"
        "v_cvt_pk_bf16_f32 v116, v116, v117\n"
        "v_accvgpr_write_b32 a50, v116\n"
        "v_mfma_f32_16x16x32_bf16 v[152:155], v[200:203], v[148:151], 0\n"
        "buffer_load_dwordx4 v130, s[20:23], s64 offen offset:2048 lds\n"
        "v_cvt_pk_bf16_f32 v118, v118, v119\n"
        "v_accvgpr_write_b32 a51, v118\n"
        "v_mfma_f32_16x16x32_bf16 v[88:91], a[24:27], a[220:223], 0\n"
        "s_waitcnt lgkmcnt(4)\n"
        "s_add_u32 m0, s65, 15360\n"
        "v_cvt_pk_bf16_f32 v120, v120, v121\n"
        "v_accvgpr_write_b32 a4, v120\n"
        "v_mfma_f32_16x16x32_bf16 v[172:175], v[204:207], v[148:151], 0\n"
        "s_mov_b32 s64, 184320\n"
        "v_cvt_pk_bf16_f32 v122, v122, v123\n"
        "v_accvgpr_write_b32 a5, v122\n"
        "v_mfma_f32_16x16x32_bf16 v[92:95], a[24:27], a[216:219], 0\n"
        "s_waitcnt lgkmcnt(3)\n"
        "buffer_load_dwordx4 v128, s[20:23], s64 offen lds\n"
        "v_mfma_f32_16x16x32_bf16 v[192:195], v[220:223], v[148:151], 0\n"
        "ds_read_b128 a[248:251], v240 offset:128\n"
        "v_cvt_pk_bf16_f32 v124, v124, v125\n"
        "v_mfma_f32_16x16x32_bf16 v[96:99], a[48:51], a[216:219], 0\n"
        "s_waitcnt lgkmcnt(3)\n"
        "v_accvgpr_write_b32 a6, v124\n"
        "v_cvt_pk_bf16_f32 v126, v126, v127\n"
        "v_mfma_f32_16x16x32_bf16 v[212:215], v[224:227], v[148:151], 0\n"
        "s_waitcnt vmcnt(7)\n"
        "ds_read_b128 v[216:219], v137 offset:6144\n"
        "ds_read_b128 v[156:159], v137 offset:6912\n"
        "v_mfma_f32_16x16x32_bf16 v[100:103], a[48:51], a[220:223], 0\n"
        "buffer_load_dwordx4 v129, s[20:23], s64 offen offset:1024 lds\n"
        "v_accvgpr_write_b32 a7, v126\n"
        "v_mfma_f32_16x16x32_bf16 v[104:107], a[48:51], a[224:227], 0\n"
        "ds_read_b128 v[176:179], v137 offset:6176\n"
        "ds_read_b128 v[196:199], v137 offset:6944\n"
        "v_mfma_f32_16x16x32_bf16 v[108:111], a[48:51], a[228:231], 0\n"
        "ds_read_b128 v[160:163], v137 offset:6208\n"
        "ds_read_b128 v[164:167], v137 offset:6976\n"
        "v_cvt_pk_bf16_f32 v152, v152, v153\n"
        "v_accvgpr_write_b32 a28, v152\n"
        "v_mfma_f32_16x16x32_bf16 v[64:67], a[4:7], a[232:235], v[64:67]\n"
        "s_waitcnt lgkmcnt(5)\n"
        "buffer_load_dwordx4 v130, s[20:23], s64 offen offset:2048 lds\n"
        "v_cvt_pk_bf16_f32 v154, v154, v155\n"
        "v_accvgpr_write_b32 a29, v154\n"
        "v_mfma_f32_16x16x32_bf16 v[112:115], v[216:219], v[148:151], 0\n"
        "ds_read_b128 a[216:219], v240 offset:1664\n"
        "ds_read_b128 a[220:223], v240 offset:12416\n"
        "v_cvt_pk_bf16_f32 v172, v172, v173\n"
        "v_accvgpr_write_b32 a30, v172\n"
        "v_mfma_f32_16x16x32_bf16 v[68:71], a[4:7], a[236:239], v[68:71]\n"
        "s_add_u32 m0, s65, 0\n"
        "ds_read_b128 a[224:227], v240 offset:13952\n"
        "v_cvt_pk_bf16_f32 v174, v174, v175\n"
        "v_accvgpr_write_b32 a31, v174\n"
        "v_mfma_f32_16x16x32_bf16 v[72:75], a[4:7], a[240:243], v[72:75]\n"
        "s_waitcnt lgkmcnt(7)\n"
        "s_mov_b32 s64, 384\n"
        "v_cvt_pk_bf16_f32 v192, v192, v193\n"
        "v_accvgpr_write_b32 a52, v192\n"
        "v_cvt_pk_bf16_f32 v194, v194, v195\n"
        "v_accvgpr_write_b32 a53, v194\n"
        "v_mfma_f32_16x16x32_bf16 v[116:119], v[156:159], v[148:151], 0\n"
        "buffer_load_dwordx4 v128, s[20:23], s64 offen lds\n"
        "v_mfma_f32_16x16x32_bf16 v[76:79], a[4:7], a[244:247], v[76:79]\n"
        "v_cvt_pk_bf16_f32 v212, v212, v213\n"
        "v_accvgpr_write_b32 a54, v212\n"
        "v_cvt_pk_bf16_f32 v214, v214, v215\n"
        "v_accvgpr_write_b32 a55, v214\n"
        "v_mfma_f32_16x16x32_bf16 v[80:83], a[28:31], a[244:247], v[80:83]\n"
        "s_waitcnt lgkmcnt(6)\n"
        "v_cvt_pk_bf16_f32 v112, v112, v113\n"
        "v_accvgpr_write_b32 a8, v112\n"
        "v_mfma_f32_16x16x32_bf16 v[120:123], v[176:179], v[148:151], 0\n"
        "buffer_load_dwordx4 v129, s[20:23], s64 offen offset:1024 lds\n"
        "v_mfma_f32_16x16x32_bf16 v[84:87], a[28:31], a[240:243], v[84:87]\n"
        "v_cvt_pk_bf16_f32 v114, v114, v115\n"
        "v_accvgpr_write_b32 a9, v114\n"
        "v_mfma_f32_16x16x32_bf16 v[88:91], a[28:31], a[236:239], v[88:91]\n"
        "s_waitcnt lgkmcnt(5)\n"
        "v_cvt_pk_bf16_f32 v116, v116, v117\n"
        "v_accvgpr_write_b32 a10, v116\n"
        "v_mfma_f32_16x16x32_bf16 v[124:127], v[196:199], v[148:151], 0\n"
        "v_cvt_pk_bf16_f32 v118, v118, v119\n"
        "v_accvgpr_write_b32 a11, v118\n"
        "v_mfma_f32_16x16x32_bf16 v[92:95], a[28:31], a[232:235], v[92:95]\n"
        "buffer_load_dwordx4 v130, s[20:23], s64 offen offset:2048 lds\n"
        "v_mfma_f32_16x16x32_bf16 v[96:99], a[52:55], a[232:235], v[96:99]\n"
        "s_waitcnt lgkmcnt(4)\n"
        "s_add_u32 m0, s65, 3072\n"
        "s_waitcnt vmcnt(9)\n"
        "ds_read_b128 v[180:183], v137 offset:9216\n"
        "ds_read_b128 v[184:187], v137 offset:9984\n"
        "v_mfma_f32_16x16x32_bf16 v[152:155], v[160:163], v[148:151], 0\n"
        "s_mov_b32 s64, 37248\n"
        "ds_read_b128 v[200:203], v137 offset:9248\n"
        "ds_read_b128 v[204:207], v137 offset:10016\n"
        "v_mfma_f32_16x16x32_bf16 v[100:103], a[52:55], a[236:239], v[100:103]\n"
        ".long 0xbec200ff\n"
        "41:\n"
        "s_branch 61b\n"
        "buffer_load_dwordx4 v128, s[20:23], s64 offen lds\n"
        "v_mfma_f32_16x16x32_bf16 v[104:107], a[52:55], a[240:243], v[104:107]\n"
        "s_waitcnt lgkmcnt(7)\n"
        "ds_read_b128 v[220:223], v137 offset:9280\n"
        "ds_read_b128 v[224:227], v137 offset:10048\n"
        "v_mfma_f32_16x16x32_bf16 v[172:175], v[164:167], v[148:151], 0\n"
        "ds_read_b128 a[228:231], v241 offset:128\n"
        "ds_read_b128 a[232:235], v241 offset:1664\n"
        "v_cvt_pk_bf16_f32 v120, v120, v121\n"
        "v_accvgpr_write_b32 a32, v120\n"
        "v_mfma_f32_16x16x32_bf16 v[108:111], a[52:55], a[244:247], v[108:111]\n"
        "ds_read_b128 a[236:239], v241 offset:12416\n"
        "ds_read_b128 a[240:243], v241 offset:13952\n"
        "v_cvt_pk_bf16_f32 v122, v122, v123\n"
        "v_accvgpr_write_b32 a33, v122\n"
        "v_mfma_f32_16x16x32_bf16 v[64:67], a[8:11], a[248:251], v[64:67]\n"
        "s_waitcnt lgkmcnt(9)\n"
        "buffer_load_dwordx4 v129, s[20:23], s64 offen offset:1024 lds\n"
        "v_mfma_f32_16x16x32_bf16 v[192:195], v[180:183], v[148:151], 0\n"
        "v_cvt_pk_bf16_f32 v124, v124, v125\n"
        "v_accvgpr_write_b32 a34, v124\n"
        "v_cvt_pk_bf16_f32 v126, v126, v127\n"
        "v_accvgpr_write_b32 a35, v126\n"
        "v_mfma_f32_16x16x32_bf16 v[68:71], a[8:11], a[216:219], v[68:71]\n"
        "v_cvt_pk_bf16_f32 v152, v152, v153\n"
        "v_accvgpr_write_b32 a56, v152\n"
        "v_mfma_f32_16x16x32_bf16 v[72:75], a[8:11], a[220:223], v[72:75]\n"
        "s_waitcnt lgkmcnt(8)\n"
        "buffer_load_dwordx4 v130, s[20:23], s64 offen offset:2048 lds\n"
        "v_mfma_f32_16x16x32_bf16 v[212:215], v[184:187], v[148:151], 0\n"
        "v_cvt_pk_bf16_f32 v154, v154, v155\n"
        "v_accvgpr_write_b32 a57, v154\n"
        "v_mfma_f32_16x16x32_bf16 v[76:79], a[8:11], a[224:227], v[76:79]\n"
        "s_add_u32 m0, s65, 6144\n"
        "v_cvt_pk_bf16_f32 v172, v172, v173\n"
        "v_accvgpr_write_b32 a58, v172\n"
        "v_mfma_f32_16x16x32_bf16 v[80:83], a[32:35], a[224:227], v[80:83]\n"
        "s_waitcnt lgkmcnt(7)\n"
        "s_mov_b32 s64, 74112\n"
        "v_cvt_pk_bf16_f32 v174, v174, v175\n"
        "v_accvgpr_write_b32 a59, v174\n"
        "v_mfma_f32_16x16x32_bf16 v[112:115], v[200:203], v[148:151], 0\n"
        "buffer_load_dwordx4 v128, s[20:23], s64 offen lds\n"
        "v_mfma_f32_16x16x32_bf16 v[84:87], a[32:35], a[220:223], v[84:87]\n"
        "v_cvt_pk_bf16_f32 v192, v192, v193\n"
        "v_accvgpr_write_b32 a12, v192\n"
        "v_mfma_f32_16x16x32_bf16 v[88:91], a[32:35], a[216:219], v[88:91]\n"
        "s_waitcnt lgkmcnt(6)\n"
        "v_cvt_pk_bf16_f32 v194, v194, v195\n"
        "v_accvgpr_write_b32 a13, v194\n"
        "v_mfma_f32_16x16x32_bf16 v[116:119], v[204:207], v[148:151], 0\n"
        "buffer_load_dwordx4 v129, s[20:23], s64 offen offset:1024 lds\n"
        "v_mfma_f32_16x16x32_bf16 v[92:95], a[32:35], a[248:251], v[92:95]\n"
        "v_cvt_pk_bf16_f32 v212, v212, v213\n"
        "v_accvgpr_write_b32 a14, v212\n"
        "v_mfma_f32_16x16x32_bf16 v[96:99], a[56:59], a[248:251], v[96:99]\n"
        "s_waitcnt lgkmcnt(5)\n"
        "s_waitcnt vmcnt(11)\n"
        "ds_read_b128 v[216:219], v137 offset:12288\n"
        "ds_read_b128 v[156:159], v137 offset:13056\n"
        "v_mfma_f32_16x16x32_bf16 v[120:123], v[220:223], v[148:151], 0\n"
        "ds_read_b128 v[176:179], v137 offset:12320\n"
        "ds_read_b128 v[196:199], v137 offset:13088\n"
        "v_cvt_pk_bf16_f32 v214, v214, v215\n"
        "v_accvgpr_write_b32 a15, v214\n"
        "v_mfma_f32_16x16x32_bf16 v[100:103], a[56:59], a[216:219], v[100:103]\n"
        "buffer_load_dwordx4 v130, s[20:23], s64 offen offset:2048 lds\n"
        "v_mfma_f32_16x16x32_bf16 v[104:107], a[56:59], a[220:223], v[104:107]\n"
        "s_waitcnt lgkmcnt(8)\n"
        "s_add_u32 m0, s65, 9216\n"
        "ds_read_b128 v[160:163], v137 offset:12352\n"
        "ds_read_b128 v[164:167], v137 offset:13120\n"
        "v_mfma_f32_16x16x32_bf16 v[124:127], v[224:227], v[148:151], 0\n"
        "s_mov_b32 s64, 110976\n"
        "ds_read_b128 a[244:247], v240 offset:256\n"
        "ds_read_b128 a[248:251], v240 offset:1792\n"
        "v_cvt_pk_bf16_f32 v112, v112, v113\n"
        "v_accvgpr_write_b32 a36, v112\n"
        "v_mfma_f32_16x16x32_bf16 v[108:111], a[56:59], a[224:227], v[108:111]\n"
        "s_waitcnt lgkmcnt(11)\n"
        "buffer_load_dwordx4 v128, s[20:23], s64 offen lds\n"
        "v_cvt_pk_bf16_f32 v114, v114, v115\n"
        "v_accvgpr_write_b32 a37, v114\n"
        "v_mfma_f32_16x16x32_bf16 v[64:67], a[12:15], a[228:231], v[64:67]\n"
        "s_waitcnt lgkmcnt(7)\n"
        "ds_read_b128 a[216:219], v240 offset:12544\n"
        "ds_read_b128 a[220:223], v240 offset:14080\n"
        "v_mfma_f32_16x16x32_bf16 v[152:155], v[216:219], v[148:151], 0\n"
        "v_cvt_pk_bf16_f32 v116, v116, v117\n"
        "v_accvgpr_write_b32 a38, v116\n"
        "v_cvt_pk_bf16_f32 v118, v118, v119\n"
        "v_accvgpr_write_b32 a39, v118\n"
        "v_mfma_f32_16x16x32_bf16 v[68:71], a[12:15], a[232:235], v[68:71]\n"
        "v_cvt_pk_bf16_f32 v120, v120, v121\n"
        "v_accvgpr_write_b32 a60, v120\n"
        "v_mfma_f32_16x16x32_bf16 v[72:75], a[12:15], a[236:239], v[72:75]\n"
        "s_waitcnt lgkmcnt(8)\n"
        "buffer_load_dwordx4 v129, s[20:23], s64 offen offset:1024 lds\n"
        "v_mfma_f32_16x16x32_bf16 v[172:175], v[156:159], v[148:151], 0\n"
        "v_cvt_pk_bf16_f32 v122, v122, v123\n"
        "v_accvgpr_write_b32 a61, v122\n"
        "v_mfma_f32_16x16x32_bf16 v[76:79], a[12:15], a[240:243], v[76:79]\n"
        "v_cvt_pk_bf16_f32 v124, v124, v125\n"
        "v_accvgpr_write_b32 a62, v124\n"
        "v_mfma_f32_16x16x32_bf16 v[80:83], a[36:39], a[240:243], v[80:83]\n"
        "s_waitcnt lgkmcnt(7)\n"
        "buffer_load_dwordx4 v130, s[20:23], s64 offen offset:2048 lds\n"
        "v_mfma_f32_16x16x32_bf16 v[192:195], v[176:179], v[148:151], 0\n"
        "v_cvt_pk_bf16_f32 v126, v126, v127\n"
        "v_accvgpr_write_b32 a63, v126\n"
        "v_mfma_f32_16x16x32_bf16 v[84:87], a[36:39], a[236:239], v[84:87]\n"
        "s_add_u32 m0, s65, 12288\n"
        "s_waitcnt vmcnt(9)\n"
        "ds_read_b64_tr_b16 a[72:73], v255 offset:0\n"
        "ds_read_b64_tr_b16 a[74:75], v255 offset:768\n"
        "v_mfma_f32_16x16x32_bf16 v[88:91], a[36:39], a[232:235], v[88:91]\n"
        "s_waitcnt lgkmcnt(8)\n"
        "s_mov_b32 s64, 147840\n"
        "ds_read_b64_tr_b16 a[96:97], v255 offset:32\n"
        "ds_read_b64_tr_b16 a[98:99], v255 offset:800\n"
        "v_mfma_f32_16x16x32_bf16 v[212:215], v[196:199], v[148:151], 0\n"
        "s_waitcnt lgkmcnt(8)\n"
        "buffer_load_dwordx4 v128, s[20:23], s64 offen lds\n"
        "ds_read_b64_tr_b16 a[120:121], v255 offset:64\n"
        "ds_read_b64_tr_b16 a[122:123], v255 offset:832\n"
        "v_mfma_f32_16x16x32_bf16 v[92:95], a[36:39], a[228:231], v[92:95]\n"
        "ds_read_b128 v[180:183], v137 offset:15360\n"
        "ds_read_b128 v[184:187], v137 offset:16128\n"
        "v_cvt_pk_bf16_f32 v152, v152, v153\n"
        "v_accvgpr_write_b32 a16, v152\n"
        "v_mfma_f32_16x16x32_bf16 v[96:99], a[60:63], a[228:231], v[96:99]\n"
        "ds_read_b128 v[200:203], v137 offset:15392\n"
        "ds_read_b128 v[204:207], v137 offset:16160\n"
        "v_cvt_pk_bf16_f32 v154, v154, v155\n"
        "v_accvgpr_write_b32 a17, v154\n"
        "v_mfma_f32_16x16x32_bf16 v[112:115], v[160:163], v[148:151], 0\n"
        "buffer_load_dwordx4 v129, s[20:23], s64 offen offset:1024 lds\n"
        "v_cvt_pk_bf16_f32 v172, v172, v173\n"
        "v_accvgpr_write_b32 a18, v172\n"
        "v_mfma_f32_16x16x32_bf16 v[100:103], a[60:63], a[232:235], v[100:103]\n"
        "ds_read_b128 v[220:223], v137 offset:15424\n"
        "ds_read_b128 v[224:227], v137 offset:16192\n"
        "v_cvt_pk_bf16_f32 v174, v174, v175\n"
        "v_accvgpr_write_b32 a19, v174\n"
        "v_mfma_f32_16x16x32_bf16 v[104:107], a[60:63], a[236:239], v[104:107]\n"
        "ds_read_b128 a[224:227], v241 offset:256\n"
        "ds_read_b128 a[228:231], v241 offset:1792\n"
        "v_mfma_f32_16x16x32_bf16 v[116:119], v[164:167], v[148:151], 0\n"
        "ds_read_b128 a[232:235], v241 offset:12544\n"
        "ds_read_b128 a[236:239], v241 offset:14080\n"
        "v_cvt_pk_bf16_f32 v192, v192, v193\n"
        "v_accvgpr_write_b32 a40, v192\n"
        "v_mfma_f32_16x16x32_bf16 v[108:111], a[60:63], a[240:243], v[108:111]\n"
        "s_waitcnt lgkmcnt(15)\n"
        "buffer_load_dwordx4 v130, s[20:23], s64 offen offset:2048 lds\n"
        "v_cvt_pk_bf16_f32 v194, v194, v195\n"
        "v_accvgpr_write_b32 a41, v194\n"
        "v_mfma_f32_16x16x32_bf16 v[64:67], a[16:19], a[244:247], v[64:67]\n"
        "s_waitcnt lgkmcnt(9)\n"
        "s_add_u32 m0, s65, 15360\n"
        "v_cvt_pk_bf16_f32 v212, v212, v213\n"
        "v_accvgpr_write_b32 a42, v212\n"
        "v_mfma_f32_16x16x32_bf16 v[120:123], v[180:183], v[148:151], 0\n"
        "s_mov_b32 s64, 184704\n"
        "s_waitcnt vmcnt(9)\n"
        "ds_read_b64_tr_b16 a[76:77], v255 offset:3072\n"
        "ds_read_b64_tr_b16 a[78:79], v255 offset:3840\n"
        "v_cvt_pk_bf16_f32 v214, v214, v215\n"
        "v_accvgpr_write_b32 a43, v214\n"
        "v_mfma_f32_16x16x32_bf16 v[68:71], a[16:19], a[248:251], v[68:71]\n"
        "s_waitcnt lgkmcnt(6)\n"
        "buffer_load_dwordx4 v128, s[20:23], s64 offen lds\n"
        "ds_read_b64_tr_b16 a[100:101], v255 offset:3104\n"
        "ds_read_b64_tr_b16 a[102:103], v255 offset:3872\n"
        "v_mfma_f32_16x16x32_bf16 v[72:75], a[16:19], a[216:219], v[72:75]\n"
        "ds_read_b64_tr_b16 a[124:125], v255 offset:3136\n"
        "ds_read_b64_tr_b16 a[126:127], v255 offset:3904\n"
        "v_mfma_f32_16x16x32_bf16 v[124:127], v[184:187], v[148:151], 0\n"
        "v_cvt_pk_bf16_f32 v112, v112, v113\n"
        "v_accvgpr_write_b32 a64, v112\n"
        "v_cvt_pk_bf16_f32 v114, v114, v115\n"
        "v_accvgpr_write_b32 a65, v114\n"
        "v_mfma_f32_16x16x32_bf16 v[76:79], a[16:19], a[220:223], v[76:79]\n"
        "v_cvt_pk_bf16_f32 v116, v116, v117\n"
        "v_accvgpr_write_b32 a66, v116\n"
        "v_mfma_f32_16x16x32_bf16 v[80:83], a[40:43], a[220:223], v[80:83]\n"
        "buffer_load_dwordx4 v129, s[20:23], s64 offen offset:1024 lds\n"
        "v_mfma_f32_16x16x32_bf16 v[152:155], v[200:203], v[148:151], 0\n"
        "ds_read_b128 a[240:243], v240 offset:0\n"
        "v_cvt_pk_bf16_f32 v118, v118, v119\n"
        "v_accvgpr_write_b32 a67, v118\n"
        "v_mfma_f32_16x16x32_bf16 v[84:87], a[40:43], a[216:219], v[84:87]\n"
        "v_cvt_pk_bf16_f32 v120, v120, v121\n"
        "v_accvgpr_write_b32 a20, v120\n"
        "v_mfma_f32_16x16x32_bf16 v[88:91], a[40:43], a[248:251], v[88:91]\n"
        "buffer_load_dwordx4 v130, s[20:23], s64 offen offset:2048 lds\n"
        "v_mfma_f32_16x16x32_bf16 v[172:175], v[204:207], v[148:151], 0\n"
        "v_cvt_pk_bf16_f32 v122, v122, v123\n"
        "v_accvgpr_write_b32 a21, v122\n"
        "v_mfma_f32_16x16x32_bf16 v[92:95], a[40:43], a[244:247], v[92:95]\n"
        "v_cvt_pk_bf16_f32 v124, v124, v125\n"
        "v_accvgpr_write_b32 a22, v124\n"
        "v_mfma_f32_16x16x32_bf16 v[96:99], a[64:67], a[244:247], v[96:99]\n"
        "s_waitcnt vmcnt(9)\n"
        "ds_read_b64_tr_b16 a[80:81], v255 offset:6144\n"
        "ds_read_b64_tr_b16 a[82:83], v255 offset:6912\n"
        "v_mfma_f32_16x16x32_bf16 v[192:195], v[220:223], v[148:151], 0\n"
        "ds_read_b64_tr_b16 a[104:105], v255 offset:6176\n"
        "ds_read_b64_tr_b16 a[106:107], v255 offset:6944\n"
        "v_cvt_pk_bf16_f32 v126, v126, v127\n"
        "v_accvgpr_write_b32 a23, v126\n"
        "v_mfma_f32_16x16x32_bf16 v[100:103], a[64:67], a[248:251], v[100:103]\n"
        "ds_read_b64_tr_b16 a[128:129], v255 offset:6208\n"
        "ds_read_b64_tr_b16 a[130:131], v255 offset:6976\n"
        "v_mfma_f32_16x16x32_bf16 v[104:107], a[64:67], a[216:219], v[104:107]\n"
        "ds_read_b128 a[244:247], v241 offset:0\n"
        "ds_read_b128 a[248:251], v240 offset:128\n"
        "v_mfma_f32_16x16x32_bf16 v[212:215], v[224:227], v[148:151], 0\n"
        "ds_read_b128 a[216:219], v240 offset:1536\n"
        "v_cvt_pk_bf16_f32 v152, v152, v153\n"
        "v_accvgpr_write_b32 a44, v152\n"
        "v_cvt_pk_bf16_f32 v154, v154, v155\n"
        "v_mfma_f32_16x16x32_bf16 v[108:111], a[64:67], a[220:223], v[108:111]\n"
        "s_waitcnt lgkmcnt(15)\n"
        "ds_read_b128 a[220:223], v241 offset:1536\n"
        "v_accvgpr_write_b32 a45, v154\n"
        "v_mfma_f32_16x16x32_bf16 v[64:67], a[20:23], a[224:227], v[64:67]\n"
        "s_waitcnt lgkmcnt(15)\n"
        "s_mov_b32 m0, s48\n"
        "v_cvt_pk_bf16_f32 v172, v172, v173\n"
        "v_accvgpr_write_b32 a46, v172\n"
        "v_mfma_f32_16x16x32_bf16 v[68:71], a[20:23], a[228:231], v[68:71]\n"
        "s_waitcnt lgkmcnt(15)\n"
        "s_sub_u32 s64, s36, 4194304\n"
        "v_cvt_pk_bf16_f32 v174, v174, v175\n"
        "v_accvgpr_write_b32 a47, v174\n"
        "v_mfma_f32_16x16x32_bf16 v[72:75], a[20:23], a[232:235], v[72:75]\n"
        "s_waitcnt lgkmcnt(15)\n"
        "buffer_load_dwordx4 v249, s[12:15], s64 offen lds\n"
        "v_mfma_f32_16x16x32_bf16 v[76:79], a[20:23], a[236:239], v[76:79]\n"
        "v_cvt_pk_bf16_f32 v192, v192, v193\n"
        "v_accvgpr_write_b32 a68, v192\n"
        "v_mfma_f32_16x16x32_bf16 v[80:83], a[44:47], a[236:239], v[80:83]\n"
        "v_cvt_pk_bf16_f32 v194, v194, v195\n"
        "v_accvgpr_write_b32 a69, v194\n"
        "v_mfma_f32_16x16x32_bf16 v[84:87], a[44:47], a[232:235], v[84:87]\n"
        "v_cvt_pk_bf16_f32 v212, v212, v213\n"
        "v_accvgpr_write_b32 a70, v212\n"
        "v_mfma_f32_16x16x32_bf16 v[88:91], a[44:47], a[228:231], v[88:91]\n"
        "buffer_load_dwordx4 v253, s[12:15], s64 offen offset:1024 lds\n"
        "v_mfma_f32_16x16x32_bf16 v[92:95], a[44:47], a[224:227], v[92:95]\n"
        "v_cvt_pk_bf16_f32 v214, v214, v215\n"
        "v_accvgpr_write_b32 a71, v214\n"
        "s_nop 1\n"
        "v_mfma_f32_16x16x32_bf16 v[96:99], a[68:71], a[224:227], v[96:99]\n"
        "s_add_u32 m0, s65, 0\n"
        "s_waitcnt vmcnt(8)\n"
        "ds_read_b64_tr_b16 a[84:85], v255 offset:9216\n"
        "ds_read_b64_tr_b16 a[86:87], v255 offset:9984\n"
        "v_mfma_f32_16x16x32_bf16 v[100:103], a[68:71], a[228:231], v[100:103]\n"
        "s_mov_b32 s64, 768\n"
        "ds_read_b64_tr_b16 a[108:109], v255 offset:9248\n"
        "ds_read_b64_tr_b16 a[110:111], v255 offset:10016\n"
        "v_mfma_f32_16x16x32_bf16 v[104:107], a[68:71], a[232:235], v[104:107]\n"
        "buffer_load_dwordx4 v128, s[20:23], s64 offen lds\n"
        "ds_read_b64_tr_b16 a[132:133], v255 offset:9280\n"
        "ds_read_b64_tr_b16 a[134:135], v255 offset:10048\n"
        "v_mfma_f32_16x16x32_bf16 v[108:111], a[68:71], a[236:239], v[108:111]\n"
        "s_waitcnt lgkmcnt(15)\n"
        "ds_read_b128 a[224:227], v240 offset:1664\n"
        "ds_read_b128 a[228:231], v240 offset:12288\n"
        "v_mfma_f32_16x16x32_bf16 v[112:115], a[72:75], a[240:243], 0\n"
        "s_waitcnt lgkmcnt(11)\n"
        "ds_read_b128 a[232:235], v241 offset:12288\n"
        "ds_read_b128 a[236:239], v240 offset:12416\n"
        "v_mfma_f32_16x16x32_bf16 v[112:115], a[76:79], a[244:247], v[112:115]\n"
        "s_waitcnt lgkmcnt(12)\n"
        "v_cvt_pk_bf16_f32 v156, v64, v65\n"
        "v_cvt_pk_bf16_f32 v157, v66, v67\n"
        "v_mfma_f32_16x16x32_bf16 v[112:115], a[80:83], a[248:251], v[112:115]\n"
        "buffer_load_dwordx4 v129, s[20:23], s64 offen offset:1024 lds\n"
        "v_mfma_f32_16x16x32_bf16 v[116:119], a[104:107], a[248:251], 0\n"
        "v_cvt_pk_bf16_f32 v196, v72, v73\n"
        "v_cvt_pk_bf16_f32 v197, v74, v75\n"
        "v_mfma_f32_16x16x32_bf16 v[116:119], a[100:103], a[244:247], v[116:119]\n"
        "v_cvt_pk_bf16_f32 v176, v68, v69\n"
        "v_cvt_pk_bf16_f32 v177, v70, v71\n"
        "v_mfma_f32_16x16x32_bf16 v[116:119], a[96:99], a[240:243], v[116:119]\n"
        "v_cvt_pk_bf16_f32 v216, v76, v77\n"
        "v_cvt_pk_bf16_f32 v217, v78, v79\n"
        "v_mfma_f32_16x16x32_bf16 v[120:123], a[120:123], a[240:243], 0\n"
        "buffer_load_dwordx4 v130, s[20:23], s64 offen offset:2048 lds\n"
        "v_mfma_f32_16x16x32_bf16 v[120:123], a[124:127], a[244:247], v[120:123]\n"
        "ds_read_b128 a[240:243], v240 offset:13824\n"
        "ds_read_b128 a[244:247], v241 offset:13824\n"
        "v_mfma_f32_16x16x32_bf16 v[120:123], a[128:131], a[248:251], v[120:123]\n"
        "s_waitcnt lgkmcnt(13)\n"
        "s_add_u32 m0, s65, 3072\n"
        "ds_read_b128 a[248:251], v240 offset:13952\n"
        "v_cvt_pk_bf16_f32 v218, v80, v81\n"
        "v_mfma_f32_16x16x32_bf16 v[124:127], a[72:75], a[216:219], 0\n"
        "s_waitcnt lgkmcnt(13)\n"
        "s_mov_b32 s64, 37632\n"
        "v_cvt_pk_bf16_f32 v219, v82, v83\n"
        "v_cvt_pk_bf16_f32 v198, v84, v85\n"
        "v_mfma_f32_16x16x32_bf16 v[124:127], a[76:79], a[220:223], v[124:127]\n"
        "s_waitcnt lgkmcnt(6)\n"
        "buffer_load_dwordx4 v128, s[20:23], s64 offen lds\n"
        "s_waitcnt vmcnt(9)\n"
        "ds_read_b64_tr_b16 a[88:89], v255 offset:12288\n"
        "ds_read_b64_tr_b16 a[90:91], v255 offset:13056\n"
        "v_mfma_f32_16x16x32_bf16 v[124:127], a[80:83], a[224:227], v[124:127]\n"
        "ds_read_b64_tr_b16 a[112:113], v255 offset:12320\n"
        "ds_read_b64_tr_b16 a[114:115], v255 offset:13088\n"
        "v_mfma_f32_16x16x32_bf16 v[64:67], a[104:107], a[224:227], 0\n"
        "ds_read_b64_tr_b16 a[136:137], v255 offset:12352\n"
        "ds_read_b64_tr_b16 a[138:139], v255 offset:13120\n"
        "v_mfma_f32_16x16x32_bf16 v[64:67], a[100:103], a[220:223], v[64:67]\n"
        "v_cvt_pk_bf16_f32 v199, v86, v87\n"
        "v_cvt_pk_bf16_f32 v178, v88, v89\n"
        "v_mfma_f32_16x16x32_bf16 v[64:67], a[96:99], a[216:219], v[64:67]\n"
        "buffer_load_dwordx4 v129, s[20:23], s64 offen offset:1024 lds\n"
        "v_mfma_f32_16x16x32_bf16 v[72:75], a[120:123], a[216:219], 0\n"
        "ds_read_b128 a[216:219], v241 offset:128\n"
        "v_cvt_pk_bf16_f32 v179, v90, v91\n"
        "v_mfma_f32_16x16x32_bf16 v[72:75], a[124:127], a[220:223], v[72:75]\n"
        "ds_read_b128 a[220:223], v240 offset:256\n"
        "v_cvt_pk_bf16_f32 v158, v92, v93\n"
        "v_mfma_f32_16x16x32_bf16 v[72:75], a[128:131], a[224:227], v[72:75]\n"
        "s_waitcnt lgkmcnt(13)\n"
        "ds_read_b128 a[224:227], v241 offset:256\n"
        "v_cvt_pk_bf16_f32 v159, v94, v95\n"
        "v_mfma_f32_16x16x32_bf16 v[68:71], a[72:75], a[228:231], 0\n"
        "s_waitcnt lgkmcnt(13)\n"
        "buffer_load_dwordx4 v130, s[20:23], s64 offen offset:2048 lds\n"
        "v_mfma_f32_16x16x32_bf16 v[68:71], a[76:79], a[232:235], v[68:71]\n"
        "s_waitcnt lgkmcnt(12)\n"
        "v_cvt_pk_bf16_f32 v160, v96, v97\n"
        "v_cvt_pk_bf16_f32 v161, v98, v99\n"
        "v_mfma_f32_16x16x32_bf16 v[68:71], a[80:83], a[236:239], v[68:71]\n"
        "s_add_u32 m0, s65, 6144\n"
        "v_cvt_pk_bf16_f32 v166, v96, v97\n"
        "v_cvt_pk_bf16_f32 v167, v98, v99\n"
        "v_mfma_f32_16x16x32_bf16 v[76:79], a[104:107], a[236:239], 0\n"
        "s_mov_b32 s64, 74496\n"
        "v_cvt_pk_bf16_f32 v180, v100, v101\n"
        "v_cvt_pk_bf16_f32 v181, v102, v103\n"
        "v_mfma_f32_16x16x32_bf16 v[76:79], a[100:103], a[232:235], v[76:79]\n"
        "buffer_load_dwordx4 v128, s[20:23], s64 offen lds\n"
        "s_waitcnt vmcnt(9)\n"
        "ds_read_b64_tr_b16 a[92:93], v255 offset:15360\n"
        "ds_read_b64_tr_b16 a[94:95], v255 offset:16128\n"
        "v_mfma_f32_16x16x32_bf16 v[76:79], a[96:99], a[228:231], v[76:79]\n"
        "ds_read_b64_tr_b16 a[116:117], v255 offset:15392\n"
        "ds_read_b64_tr_b16 a[118:119], v255 offset:16160\n"
        "v_mfma_f32_16x16x32_bf16 v[80:83], a[120:123], a[228:231], 0\n"
        "ds_read_b64_tr_b16 a[140:141], v255 offset:15424\n"
        "ds_read_b64_tr_b16 a[142:143], v255 offset:16192\n"
        "v_mfma_f32_16x16x32_bf16 v[80:83], a[124:127], a[232:235], v[80:83]\n"
        "ds_read_b128 v[48:51], v140 offset:9216\n"
        "ds_read_b128 a[228:231], v241 offset:1664\n"
        "v_mfma_f32_16x16x32_bf16 v[80:83], a[128:131], a[236:239], v[80:83]\n"
        "s_waitcnt lgkmcnt(15)\n"
        "buffer_load_dwordx4 v129, s[20:23], s64 offen offset:1024 lds\n"
        "v_mfma_f32_16x16x32_bf16 v[84:87], a[72:75], a[240:243], 0\n"
        "s_waitcnt lgkmcnt(15)\n"
        "ds_read_b128 a[232:235], v240 offset:1792\n"
        "ds_read_b128 a[236:239], v241 offset:1792\n"
        "v_mfma_f32_16x16x32_bf16 v[84:87], a[76:79], a[244:247], v[84:87]\n"
        "s_waitcnt lgkmcnt(15)\n"
        "v_cvt_pk_bf16_f32 v186, v100, v101\n"
        "v_cvt_pk_bf16_f32 v187, v102, v103\n"
        "v_mfma_f32_16x16x32_bf16 v[84:87], a[80:83], a[248:251], v[84:87]\n"
        "v_cvt_pk_bf16_f32 v200, v104, v105\n"
        "v_cvt_pk_bf16_f32 v201, v106, v107\n"
        "v_mfma_f32_16x16x32_bf16 v[88:91], a[104:107], a[248:251], 0\n"
        "ds_read_b128 v[52:55], v141 offset:9216\n"
        "v_cvt_pk_bf16_f32 v206, v104, v105\n"
        "v_mfma_f32_16x16x32_bf16 v[88:91], a[100:103], a[244:247], v[88:91]\n"
        "v_cvt_pk_bf16_f32 v207, v106, v107\n"
        "v_cvt_pk_bf16_f32 v220, v108, v109\n"
        "v_mfma_f32_16x16x32_bf16 v[88:91], a[96:99], a[240:243], v[88:91]\n"
        "v_cvt_pk_bf16_f32 v221, v110, v111\n"
        "v_cvt_pk_bf16_f32 v226, v108, v109\n"
        "v_mfma_f32_16x16x32_bf16 v[92:95], a[120:123], a[240:243], 0\n"
        "ds_read_b128 a[240:243], v241 offset:12416\n"
        "v_cvt_pk_bf16_f32 v227, v110, v111\n"
        "v_mfma_f32_16x16x32_bf16 v[92:95], a[124:127], a[244:247], v[92:95]\n"
        "ds_read_b128 a[244:247], v240 offset:12544\n"
        "v_mfma_f32_16x16x32_bf16 v[92:95], a[128:131], a[248:251], v[92:95]\n"
        "ds_read_b128 a[248:251], v241 offset:12544\n"
        "s_waitcnt lgkmcnt(7)\n"
        "v_lshl_add_u32 v48, v48, 2, s67\n"
        "v_mfma_f32_16x16x32_bf16 v[112:115], a[84:87], a[216:219], v[112:115]\n"
        "ds_read_b128 v[56:59], v142 offset:9216\n"
        "ds_read_b32 v48, v48\n"
        "v_mfma_f32_16x16x32_bf16 v[112:115], a[88:91], a[220:223], v[112:115]\n"
        "v_lshl_add_u32 v49, v49, 2, s67\n"
        "v_lshl_add_u32 v50, v50, 2, s67\n"
        "v_mfma_f32_16x16x32_bf16 v[112:115], a[92:95], a[224:227], v[112:115]\n"
        "ds_read_b32 v49, v49\n"
        "ds_read_b32 v50, v50\n"
        "v_mfma_f32_16x16x32_bf16 v[116:119], a[116:119], a[224:227], v[116:119]\n"
        "v_lshl_add_u32 v51, v51, 2, s67\n"
        "s_nop 0\n"
        "v_mfma_f32_16x16x32_bf16 v[116:119], a[112:115], a[220:223], v[116:119]\n"
        "ds_read_b32 v51, v51\n"
        "s_nop 0\n"
        "v_mfma_f32_16x16x32_bf16 v[116:119], a[108:111], a[216:219], v[116:119]\n"
        "v_cvt_pk_bf16_f32 v152, v112, v113\n"
        "v_cvt_pk_bf16_f32 v153, v114, v115\n"
        "v_mfma_f32_16x16x32_bf16 v[120:123], a[132:135], a[216:219], v[120:123]\n"
        "ds_read_b128 v[60:63], v143 offset:9216\n"
        "ds_read_b128 a[216:219], v241 offset:13952\n"
        "v_mfma_f32_16x16x32_bf16 v[120:123], a[136:139], a[220:223], v[120:123]\n"
        "ds_read_b128 a[220:223], v240 offset:14080\n"
        "s_waitcnt lgkmcnt(11)\n"
        "v_lshl_add_u32 v52, v52, 2, s67\n"
        "v_mfma_f32_16x16x32_bf16 v[120:123], a[140:143], a[224:227], v[120:123]\n"
        "ds_read_b32 v52, v52\n"
        "ds_read_b128 a[224:227], v241 offset:14080\n"
        "v_mfma_f32_16x16x32_bf16 v[124:127], a[84:87], a[228:231], v[124:127]\n"
        "v_lshl_add_u32 v53, v53, 2, s67\n"
        "v_lshl_add_u32 v54, v54, 2, s67\n"
        "v_mfma_f32_16x16x32_bf16 v[124:127], a[88:91], a[232:235], v[124:127]\n"
        "ds_read_b32 v53, v53\n"
        "ds_read_b32 v54, v54\n"
        "v_mfma_f32_16x16x32_bf16 v[124:127], a[92:95], a[236:239], v[124:127]\n"
        "v_lshl_add_u32 v55, v55, 2, s67\n"
        "v_cvt_pk_bf16_f32 v154, v116, v117\n"
        "v_mfma_f32_16x16x32_bf16 v[64:67], a[116:119], a[236:239], v[64:67]\n"
        "ds_read_b32 v55, v55\n"
        "v_cvt_pk_bf16_f32 v155, v118, v119\n"
        "v_mfma_f32_16x16x32_bf16 v[64:67], a[112:115], a[232:235], v[64:67]\n"
        "v_cvt_pk_bf16_f32 v168, v120, v121\n"
        "v_cvt_pk_bf16_f32 v169, v122, v123\n"
        "v_mfma_f32_16x16x32_bf16 v[64:67], a[108:111], a[228:231], v[64:67]\n"
        "s_waitcnt lgkmcnt(12)\n"
        "v_lshl_add_u32 v56, v56, 2, s67\n"
        "v_lshl_add_u32 v57, v57, 2, s67\n"
        "v_mfma_f32_16x16x32_bf16 v[72:75], a[132:135], a[228:231], v[72:75]\n"
        "ds_read_b32 v56, v56\n"
        "ds_read_b32 v57, v57\n"
        "v_mfma_f32_16x16x32_bf16 v[72:75], a[136:139], a[232:235], v[72:75]\n"
        "buffer_load_dwordx4 v130, s[20:23], s64 offen offset:2048 lds\n"
        "v_mfma_f32_16x16x32_bf16 v[72:75], a[140:143], a[236:239], v[72:75]\n"
        "v_lshl_add_u32 v58, v58, 2, s67\n"
        "v_lshl_add_u32 v59, v59, 2, s67\n"
        "v_mfma_f32_16x16x32_bf16 v[68:71], a[84:87], a[240:243], v[68:71]\n"
        "s_add_u32 m0, s65, 9216\n"
        "ds_read_b32 v58, v58\n"
        "ds_read_b32 v59, v59\n"
        "v_cvt_pk_bf16_f32 v172, v124, v125\n"
        "v_cvt_pk_bf16_f32 v173, v126, v127\n"
        "v_mfma_f32_16x16x32_bf16 v[68:71], a[88:91], a[244:247], v[68:71]\n"
        "s_mov_b32 s64, 111360\n"
        "v_cvt_pk_bf16_f32 v174, v64, v65\n"
        "v_cvt_pk_bf16_f32 v175, v66, v67\n"
        "v_mfma_f32_16x16x32_bf16 v[68:71], a[92:95], a[248:251], v[68:71]\n"
        "buffer_load_dwordx4 v128, s[20:23], s64 offen lds\n"
        "v_mfma_f32_16x16x32_bf16 v[76:79], a[116:119], a[248:251], v[76:79]\n"
        "s_waitcnt lgkmcnt(11)\n"
        "v_lshl_add_u32 v60, v60, 2, s67\n"
        "v_lshl_add_u32 v61, v61, 2, s67\n"
        "v_mfma_f32_16x16x32_bf16 v[76:79], a[112:115], a[244:247], v[76:79]\n"
        "ds_read_b32 v60, v60\n"
        "ds_read_b32 v61, v61\n"
        "v_mfma_f32_16x16x32_bf16 v[76:79], a[108:111], a[240:243], v[76:79]\n"
        "v_lshl_add_u32 v62, v62, 2, s67\n"
        "v_lshl_add_u32 v63, v63, 2, s67\n"
        "v_mfma_f32_16x16x32_bf16 v[80:83], a[132:135], a[240:243], v[80:83]\n"
        "buffer_load_dwordx4 v129, s[20:23], s64 offen offset:1024 lds\n"
        "s_waitcnt vmcnt(11)\n"
        "s_barrier\n"
        "ds_read_b128 v[162:165], v251 offset:0\n"
        "v_cvt_pk_bf16_f32 v190, v72, v73\n"
        "v_mfma_f32_16x16x32_bf16 v[80:83], a[136:139], a[244:247], v[80:83]\n"
        "ds_read_b32 v62, v62\n"
        "ds_read_b32 v63, v63\n"
        "v_cvt_pk_bf16_f32 v191, v74, v75\n"
        "v_mfma_f32_16x16x32_bf16 v[80:83], a[140:143], a[248:251], v[80:83]\n"
        "s_waitcnt lgkmcnt(15)\n"
        "ds_read_b128 v[182:185], v251 offset:512\n"
        "ds_read_b128 v[202:205], v251 offset:4096\n"
        "v_mfma_f32_16x16x32_bf16 v[84:87], a[84:87], a[216:219], v[84:87]\n"
        "s_waitcnt lgkmcnt(15)\n"
        "v_cvt_pk_bf16_f32 v192, v68, v69\n"
        "v_cvt_pk_bf16_f32 v193, v70, v71\n"
        "v_mfma_f32_16x16x32_bf16 v[84:87], a[88:91], a[220:223], v[84:87]\n"
        "s_waitcnt lgkmcnt(14)\n"
        "buffer_load_dwordx4 v130, s[20:23], s64 offen offset:2048 lds\n"
        "v_mfma_f32_16x16x32_bf16 v[84:87], a[92:95], a[224:227], v[84:87]\n"
        "v_cvt_pk_bf16_f32 v194, v76, v77\n"
        "v_cvt_pk_bf16_f32 v195, v78, v79\n"
        "v_mfma_f32_16x16x32_bf16 v[88:91], a[116:119], a[224:227], v[88:91]\n"
        "s_add_u32 m0, s65, 12288\n"
        "ds_read_b128 a[228:231], v240 offset:0\n"
        "v_cvt_pk_bf16_f32 v208, v80, v81\n"
        "v_mfma_f32_16x16x32_bf16 v[88:91], a[112:115], a[220:223], v[88:91]\n"
        "s_mov_b32 s64, 148224\n"
        "ds_read_b128 a[232:235], v241 offset:0\n"
        "v_cvt_pk_bf16_f32 v209, v82, v83\n"
        "v_mfma_f32_16x16x32_bf16 v[88:91], a[108:111], a[216:219], v[88:91]\n"
        "buffer_load_dwordx4 v128, s[20:23], s64 offen lds\n"
        "v_mfma_f32_16x16x32_bf16 v[64:67], v[152:155], v[156:159], v[0:3]\n"
        "s_waitcnt lgkmcnt(6)\n"
        "s_waitcnt vmcnt(10)\n"
        "ds_read_b64_tr_b16 a[144:145], v255 offset:0\n"
        "ds_read_b64_tr_b16 a[146:147], v255 offset:768\n"
        "v_mfma_f32_16x16x32_bf16 v[64:67], v[168:171], v[160:163], v[64:67]\n"
        "ds_read_b64_tr_b16 a[168:169], v255 offset:32\n"
        "ds_read_b64_tr_b16 a[170:171], v255 offset:800\n"
        "v_mfma_f32_16x16x32_bf16 v[96:99], v[172:175], v[156:159], v[4:7]\n"
        "ds_read_b64_tr_b16 a[192:193], v255 offset:64\n"
        "ds_read_b64_tr_b16 a[194:195], v255 offset:832\n"
        "v_mfma_f32_16x16x32_bf16 v[96:99], v[188:191], v[164:167], v[96:99]\n"
        "ds_read_b128 v[162:165], v252 offset:0\n"
        "buffer_load_dwordx4 v129, s[20:23], s64 offen offset:1024 lds\n"
        "v_exp_f32 v64, v64\n"
        "v_cvt_pk_bf16_f32 v212, v84, v85\n"
        "v_mfma_f32_16x16x32_bf16 v[92:95], a[132:135], a[216:219], v[92:95]\n"
        "ds_read_b128 a[236:239], v240 offset:128\n"
        "v_exp_f32 v65, v65\n"
        "v_exp_f32 v66, v66\n"
        "v_cvt_pk_bf16_f32 v64, v64, v65\n"
        "v_mfma_f32_16x16x32_bf16 v[92:95], a[136:139], a[220:223], v[92:95]\n"
        "v_exp_f32 v96, v96\n"
        "v_cvt_pk_bf16_f32 v213, v86, v87\n"
        "v_exp_f32 v67, v67\n"
        "v_mfma_f32_16x16x32_bf16 v[92:95], a[140:143], a[224:227], v[92:95]\n"
        "buffer_load_dwordx4 v130, s[20:23], s64 offen offset:2048 lds\n"
        "s_add_u32 m0, s65, 15360\n"
        "v_exp_f32 v97, v97\n"
        "v_cvt_pk_bf16_f32 v65, v66, v67\n"
        "v_cvt_pk_bf16_f32 v66, v96, v97\n"
        "v_mfma_f32_16x16x32_bf16 v[72:75], v[152:155], v[176:179], v[16:19]\n"
        "s_waitcnt lgkmcnt(11)\n"
        "s_mov_b32 s64, 185088\n"
        "v_exp_f32 v98, v98\n"
        "v_exp_f32 v99, v99\n"
        "v_cvt_pk_bf16_f32 v214, v88, v89\n"
        "v_cvt_pk_bf16_f32 v215, v90, v91\n"
        "v_mfma_f32_16x16x32_bf16 v[72:75], v[168:171], v[180:183], v[72:75]\n"
        ".long 0xbec200ff\n"
        "42:\n"
        "s_branch 61b\n"
        "buffer_load_dwordx4 v128, s[20:23], s64 offen lds\n"
        "s_waitcnt vmcnt(10)\n"
        "ds_read_b64_tr_b16 a[148:149], v255 offset:3072\n"
        "ds_read_b64_tr_b16 a[150:151], v255 offset:3840\n"
        "v_cvt_pk_bf16_f32 v67, v98, v99\n"
        "v_mfma_f32_16x16x32_bf16 v[100:103], v[172:175], v[176:179], v[20:23]\n"
        "ds_read_b64_tr_b16 a[172:173], v255 offset:3104\n"
        "ds_read_b64_tr_b16 a[174:175], v255 offset:3872\n"
        "v_mfma_f32_16x16x32_bf16 v[100:103], v[188:191], v[184:187], v[100:103]\n"
        "ds_read_b128 v[182:185], v252 offset:512\n"
        "ds_read_b64_tr_b16 a[196:197], v255 offset:3136\n"
        "ds_read_b64_tr_b16 a[198:199], v255 offset:3904\n"
        "v_exp_f32 v72, v72\n"
        "v_mfma_f32_16x16x32_bf16 v[68:71], v[192:195], v[156:159], v[8:11]\n"
        "s_waitcnt lgkmcnt(8)\n"
        "buffer_load_dwordx4 v129, s[20:23], s64 offen offset:1024 lds\n"
        "v_exp_f32 v73, v73\n"
        "v_exp_f32 v74, v74\n"
        "v_mfma_f32_16x16x32_bf16 v[68:71], v[208:211], v[160:163], v[68:71]\n"
        "ds_read_b128 a[240:243], v240 offset:1536\n"
        "ds_read_b128 a[244:247], v241 offset:1536\n"
        "v_cvt_pk_bf16_f32 v72, v72, v73\n"
        "v_exp_f32 v100, v100\n"
        "v_mfma_f32_16x16x32_bf16 v[76:79], v[192:195], v[176:179], v[24:27]\n"
        "s_waitcnt lgkmcnt(4)\n"
        "buffer_load_dwordx4 v130, s[20:23], s64 offen offset:2048 lds\n"
        "v_exp_f32 v75, v75\n"
        "v_exp_f32 v101, v101\n"
        "v_mfma_f32_16x16x32_bf16 v[76:79], v[208:211], v[180:183], v[76:79]\n"
        "ds_read_b128 a[248:251], v240 offset:1664\n"
        "v_cvt_pk_bf16_f32 v73, v74, v75\n"
        "v_exp_f32 v102, v102\n"
        "v_cvt_pk_bf16_f32 v74, v100, v101\n"
        "v_cvt_pk_bf16_f32 v230, v92, v93\n"
        "v_mfma_f32_16x16x32_bf16 v[104:107], v[212:215], v[156:159], v[12:15]\n"
        "s_waitcnt vmcnt(9)\n"
        "ds_read_b64_tr_b16 a[152:153], v255 offset:6144\n"
        "ds_read_b64_tr_b16 a[154:155], v255 offset:6912\n"
        "v_exp_f32 v68, v68\n"
        "v_exp_f32 v103, v103\n"
        "v_cvt_pk_bf16_f32 v231, v94, v95\n"
        "s_nop 1\n"
        "v_mfma_f32_16x16x32_bf16 v[104:107], v[228:231], v[164:167], v[104:107]\n"
        "ds_read_b64_tr_b16 a[176:177], v255 offset:6176\n"
        "ds_read_b64_tr_b16 a[178:179], v255 offset:6944\n"
        "v_exp_f32 v69, v69\n"
        "v_cvt_pk_bf16_f32 v75, v102, v103\n"
        "v_cvt_pk_bf16_f32 v68, v68, v69\n"
        "v_mfma_f32_16x16x32_bf16 v[108:111], v[212:215], v[176:179], v[28:31]\n"
        "ds_read_b64_tr_b16 a[200:201], v255 offset:6208\n"
        "ds_read_b64_tr_b16 a[202:203], v255 offset:6976\n"
        "v_exp_f32 v70, v70\n"
        "v_exp_f32 v76, v76\n"
        "v_mfma_f32_16x16x32_bf16 v[108:111], v[228:231], v[184:187], v[108:111]\n"
        "s_add_u32 m0, s40, 98304\n"
        "v_exp_f32 v71, v71\n"
        "v_exp_f32 v77, v77\n"
        "v_cvt_pk_bf16_f32 v69, v70, v71\n"
        "v_cvt_pk_bf16_f32 v76, v76, v77\n"
        "v_mfma_f32_16x16x32_bf16 v[80:83], v[152:155], v[196:199], v[32:35]\n"
        "s_sub_u32 s64, s47, 6291456\n"
        "ds_read_b128 a[216:219], v240 offset:12288\n"
        "v_exp_f32 v78, v78\n"
        "v_exp_f32 v79, v79\n"
        "v_mfma_f32_16x16x32_bf16 v[80:83], v[168:171], v[200:203], v[80:83]\n"
        "buffer_load_dwordx4 v242, s[8:11], s64 offen lds\n"
        "v_cvt_pk_bf16_f32 v77, v78, v79\n"
        "v_mfma_f32_16x16x32_bf16 v[112:115], a[228:231], a[144:147], 0\n"
        "ds_read_b128 a[220:223], v241 offset:12288\n"
        "ds_read_b128 a[224:227], v240 offset:12416\n"
        "v_mfma_f32_16x16x32_bf16 v[112:115], a[232:235], a[148:151], v[112:115]\n"
        "s_waitcnt lgkmcnt(7)\n"
        "s_add_u32 m0, s40, 99328\n"
        "v_exp_f32 v104, v104\n"
        "v_mfma_f32_16x16x32_bf16 v[112:115], a[236:239], a[152:155], v[112:115]\n"
        "s_waitcnt lgkmcnt(5)\n"
        "v_exp_f32 v105, v105\n"
        "v_mfma_f32_16x16x32_bf16 v[116:119], a[236:239], a[176:179], 0\n"
        "buffer_load_dwordx4 v243, s[8:11], s64 offen lds\n"
        "v_mfma_f32_16x16x32_bf16 v[116:119], a[232:235], a[172:175], v[116:119]\n"
        "ds_read_b128 v[222:225], v251 offset:4608\n"
        "v_cvt_pk_bf16_f32 v70, v104, v105\n"
        "v_mfma_f32_16x16x32_bf16 v[116:119], a[228:231], a[168:171], v[116:119]\n"
        "s_add_u32 m0, s40, 100352\n"
        "v_exp_f32 v106, v106\n"
        "v_mfma_f32_16x16x32_bf16 v[96:99], v[172:175], v[196:199], v[36:39]\n"
        "s_waitcnt vmcnt(8)\n"
        "ds_read_b64_tr_b16 a[156:157], v255 offset:9216\n"
        "ds_read_b64_tr_b16 a[158:159], v255 offset:9984\n"
        "v_mfma_f32_16x16x32_bf16 v[96:99], v[188:191], v[204:207], v[96:99]\n"
        "buffer_load_dwordx4 v244, s[8:11], s64 offen lds\n"
        "ds_read_b64_tr_b16 a[180:181], v255 offset:9248\n"
        "ds_read_b64_tr_b16 a[182:183], v255 offset:10016\n"
        "v_mfma_f32_16x16x32_bf16 v[120:123], a[228:231], a[192:195], 0\n"
        "ds_read_b128 v[202:205], v252 offset:4096\n"
        "ds_read_b64_tr_b16 a[204:205], v255 offset:9280\n"
        "ds_read_b64_tr_b16 a[206:207], v255 offset:10048\n"
        "v_mfma_f32_16x16x32_bf16 v[120:123], a[232:235], a[196:199], v[120:123]\n"
        "s_waitcnt lgkmcnt(11)\n"
        "s_add_u32 m0, s40, 110592\n"
        "v_exp_f32 v108, v108\n"
        "v_mfma_f32_16x16x32_bf16 v[120:123], a[236:239], a[200:203], v[120:123]\n"
        "s_sub_u32 s64, s47, 5505024\n"
        "v_exp_f32 v107, v107\n"
        "v_mfma_f32_16x16x32_bf16 v[124:127], a[240:243], a[144:147], 0\n"
        "buffer_load_dword v136, s[52:55], 0 offen lds\n"
        "v_mfma_f32_16x16x32_bf16 v[124:127], a[244:247], a[148:151], v[124:127]\n"
        "ds_read_b128 a[228:231], v240 offset:13824\n"
        "ds_read_b128 a[232:235], v241 offset:13824\n"
        "v_mfma_f32_16x16x32_bf16 v[124:127], a[248:251], a[152:155], v[124:127]\n"
        "ds_read_b128 a[236:239], v240 offset:13952\n"
        "v_cvt_pk_bf16_f32 v71, v106, v107\n"
        "v_mfma_f32_16x16x32_bf16 v[156:159], a[248:251], a[176:179], 0\n"
        "v_exp_f32 v109, v109\n"
        "v_mfma_f32_16x16x32_bf16 v[156:159], a[244:247], a[172:175], v[156:159]\n"
        "buffer_load_dwordx4 v242, s[8:11], s64 offen lds\n"
        "v_mfma_f32_16x16x32_bf16 v[156:159], a[240:243], a[168:171], v[156:159]\n"
        "v_exp_f32 v110, v110\n"
        "v_mfma_f32_16x16x32_bf16 v[84:87], v[192:195], v[196:199], v[40:43]\n"
        "s_waitcnt lgkmcnt(5)\n"
        "s_add_u32 m0, s40, 111616\n"
        "v_cvt_pk_bf16_f32 v78, v108, v109\n"
        "v_mfma_f32_16x16x32_bf16 v[84:87], v[208:211], v[200:203], v[84:87]\n"
        "v_exp_f32 v80, v80\n"
        "v_mfma_f32_16x16x32_bf16 v[160:163], a[240:243], a[192:195], 0\n"
        "buffer_load_dwordx4 v243, s[8:11], s64 offen lds\n"
        "s_waitcnt vmcnt(9)\n"
        "ds_read_b64_tr_b16 a[160:161], v255 offset:12288\n"
        "ds_read_b64_tr_b16 a[162:163], v255 offset:13056\n"
        "v_mfma_f32_16x16x32_bf16 v[160:163], a[244:247], a[196:199], v[160:163]\n"
        "ds_read_b64_tr_b16 a[184:185], v255 offset:12320\n"
        "ds_read_b64_tr_b16 a[186:187], v255 offset:13088\n"
        "v_mfma_f32_16x16x32_bf16 v[160:163], a[248:251], a[200:203], v[160:163]\n"
        "s_add_u32 m0, s40, 112640\n"
        "ds_read_b64_tr_b16 a[208:209], v255 offset:12352\n"
        "ds_read_b64_tr_b16 a[210:211], v255 offset:13120\n"
        "v_exp_f32 v111, v111\n"
        "v_mfma_f32_16x16x32_bf16 v[164:167], a[216:219], a[144:147], 0\n"
        "ds_read_b128 a[240:243], v241 offset:128\n"
        "v_exp_f32 v81, v81\n"
        "v_cvt_pk_bf16_f32 v79, v110, v111\n"
        "v_mfma_f32_16x16x32_bf16 v[164:167], a[220:223], a[148:151], v[164:167]\n"
        "buffer_load_dwordx4 v244, s[8:11], s64 offen lds\n"
        "v_exp_f32 v82, v82\n"
        "v_mfma_f32_16x16x32_bf16 v[164:167], a[224:227], a[152:155], v[164:167]\n"
        "ds_read_b128 a[244:247], v240 offset:256\n"
        "ds_read_b128 a[248:251], v241 offset:256\n"
        "v_cvt_pk_bf16_f32 v80, v80, v81\n"
        "v_mfma_f32_16x16x32_bf16 v[100:103], v[212:215], v[196:199], v[44:47]\n"
        "s_xor_b32 m0, s48, 8192\n"
        "v_exp_f32 v83, v83\n"
        "v_mfma_f32_16x16x32_bf16 v[100:103], v[228:231], v[204:207], v[100:103]\n"
        "s_sub_u32 s64, s36, 2097152\n"
        "v_cvt_pk_bf16_f32 v81, v82, v83\n"
        "v_mfma_f32_16x16x32_bf16 v[176:179], a[224:227], a[176:179], 0\n"
        "buffer_load_dwordx4 v249, s[12:15], s64 offen lds\n"
        "v_mfma_f32_16x16x32_bf16 v[176:179], a[220:223], a[172:175], v[176:179]\n"
        "v_exp_f32 v96, v96\n"
        "v_mfma_f32_16x16x32_bf16 v[176:179], a[216:219], a[168:171], v[176:179]\n"
        "v_exp_f32 v97, v97\n"
        "v_mfma_f32_16x16x32_bf16 v[180:183], a[216:219], a[192:195], 0\n"
        "v_exp_f32 v98, v98\n"
        "v_mfma_f32_16x16x32_bf16 v[180:183], a[220:223], a[196:199], v[180:183]\n"
        "buffer_load_dwordx4 v253, s[12:15], s64 offen offset:1024 lds\n"
        "v_mfma_f32_16x16x32_bf16 v[180:183], a[224:227], a[200:203], v[180:183]\n"
        "s_waitcnt vmcnt(9)\n"
        "ds_read_b64_tr_b16 a[164:165], v255 offset:15360\n"
        "ds_read_b64_tr_b16 a[166:167], v255 offset:16128\n"
        "v_mfma_f32_16x16x32_bf16 v[88:91], v[152:155], v[216:219], v[48:51]\n"
        "ds_read_b64_tr_b16 a[188:189], v255 offset:15392\n"
        "ds_read_b64_tr_b16 a[190:191], v255 offset:16160\n"
        "v_mfma_f32_16x16x32_bf16 v[88:91], v[168:171], v[220:223], v[88:91]\n"
        "s_waitcnt lgkmcnt(15)\n"
        "ds_read_b64_tr_b16 a[212:213], v255 offset:15424\n"
        "ds_read_b64_tr_b16 a[214:215], v255 offset:16192\n"
        "v_mfma_f32_16x16x32_bf16 v[184:187], a[228:231], a[144:147], 0\n"
        "s_waitcnt lgkmcnt(15)\n"
        "ds_read_b128 a[216:219], v241 offset:1664\n"
        "ds_read_b128 a[220:223], v240 offset:1792\n"
        "v_mfma_f32_16x16x32_bf16 v[184:187], a[232:235], a[148:151], v[184:187]\n"
        "s_waitcnt lgkmcnt(15)\n"
        "ds_read_b128 a[224:227], v241 offset:1792\n"
        "v_cvt_pk_bf16_f32 v82, v96, v97\n"
        "v_mfma_f32_16x16x32_bf16 v[184:187], a[236:239], a[152:155], v[184:187]\n"
        "v_exp_f32 v99, v99\n"
        "v_mfma_f32_16x16x32_bf16 v[196:199], a[236:239], a[176:179], 0\n"
        "v_exp_f32 v84, v84\n"
        "v_mfma_f32_16x16x32_bf16 v[196:199], a[232:235], a[172:175], v[196:199]\n"
        "v_cvt_pk_bf16_f32 v83, v98, v99\n"
        "v_mfma_f32_16x16x32_bf16 v[196:199], a[228:231], a[168:171], v[196:199]\n"
        "v_exp_f32 v85, v85\n"
        "v_mfma_f32_16x16x32_bf16 v[200:203], a[228:231], a[192:195], 0\n"
        "v_exp_f32 v86, v86\n"
        "v_mfma_f32_16x16x32_bf16 v[200:203], a[232:235], a[196:199], v[200:203]\n"
        "ds_read_b128 a[228:231], v241 offset:12416\n"
        "v_cvt_pk_bf16_f32 v84, v84, v85\n"
        "v_mfma_f32_16x16x32_bf16 v[200:203], a[236:239], a[200:203], v[200:203]\n"
        "ds_read_b128 a[232:235], v240 offset:12544\n"
        "v_mfma_f32_16x16x32_bf16 v[104:107], v[172:175], v[216:219], v[52:55]\n"
        "ds_read_b128 a[236:239], v241 offset:12544\n"
        "v_mfma_f32_16x16x32_bf16 v[104:107], v[188:191], v[224:227], v[104:107]\n"
        "s_waitcnt lgkmcnt(14)\n"
        "ds_read_b128 v[222:225], v252 offset:4608\n"
        "v_mfma_f32_16x16x32_bf16 v[112:115], a[240:243], a[156:159], v[112:115]\n"
        "s_waitcnt lgkmcnt(14)\n"
        "v_exp_f32 v87, v87\n"
        "v_mfma_f32_16x16x32_bf16 v[112:115], a[244:247], a[160:163], v[112:115]\n"
        "s_waitcnt lgkmcnt(11)\n"
        "v_exp_f32 v100, v100\n"
        "v_mfma_f32_16x16x32_bf16 v[112:115], a[248:251], a[164:167], v[112:115]\n"
        "s_waitcnt lgkmcnt(9)\n"
        "v_exp_f32 v101, v101\n"
        "v_mfma_f32_16x16x32_bf16 v[116:119], a[248:251], a[188:191], v[116:119]\n"
        "v_exp_f32 v102, v102\n"
        "v_mfma_f32_16x16x32_bf16 v[116:119], a[244:247], a[184:187], v[116:119]\n"
        "v_cvt_pk_bf16_f32 v85, v86, v87\n"
        "v_cvt_pk_bf16_f32 v86, v100, v101\n"
        "v_mfma_f32_16x16x32_bf16 v[116:119], a[240:243], a[180:183], v[116:119]\n"
        "v_exp_f32 v103, v103\n"
        "v_mfma_f32_16x16x32_bf16 v[120:123], a[240:243], a[204:207], v[120:123]\n"
        "v_cvt_pk_bf16_f32 v128, v112, v113\n"
        "v_cvt_pk_bf16_f32 v129, v114, v115\n"
        "v_mfma_f32_16x16x32_bf16 v[120:123], a[244:247], a[208:211], v[120:123]\n"
        "s_waitcnt lgkmcnt(7)\n"
        "ds_read_b128 a[240:243], v241 offset:13952\n"
        "v_cvt_pk_bf16_f32 v87, v102, v103\n"
        "v_mfma_f32_16x16x32_bf16 v[120:123], a[248:251], a[212:215], v[120:123]\n"
        "s_waitcnt lgkmcnt(7)\n"
        "ds_read_b128 a[244:247], v240 offset:14080\n"
        "v_cvt_pk_bf16_f32 v136, v116, v117\n"
        "v_mfma_f32_16x16x32_bf16 v[124:127], a[216:219], a[156:159], v[124:127]\n"
        "s_waitcnt lgkmcnt(7)\n"
        "ds_read_b128 a[248:251], v241 offset:14080\n"
        "v_cvt_pk_bf16_f32 v137, v118, v119\n"
        "v_mfma_f32_16x16x32_bf16 v[124:127], a[220:223], a[160:163], v[124:127]\n"
        "s_waitcnt lgkmcnt(7)\n"
        "v_exp_f32 v88, v88\n"
        "v_mfma_f32_16x16x32_bf16 v[124:127], a[224:227], a[164:167], v[124:127]\n"
        "v_exp_f32 v89, v89\n"
        "v_mfma_f32_16x16x32_bf16 v[92:95], v[192:195], v[216:219], v[56:59]\n"
        "s_waitcnt lgkmcnt(3)\n"
        "v_cvt_pk_bf16_f32 v144, v120, v121\n"
        "v_cvt_pk_bf16_f32 v145, v122, v123\n"
        "v_mfma_f32_16x16x32_bf16 v[92:95], v[208:211], v[220:223], v[92:95]\n"
        "v_exp_f32 v90, v90\n"
        "v_mfma_f32_16x16x32_bf16 v[156:159], a[224:227], a[188:191], v[156:159]\n"
        "v_exp_f32 v91, v91\n"
        "v_mfma_f32_16x16x32_bf16 v[156:159], a[220:223], a[184:187], v[156:159]\n"
        "v_cvt_pk_bf16_f32 v130, v124, v125\n"
        "v_cvt_pk_bf16_f32 v131, v126, v127\n"
        "v_mfma_f32_16x16x32_bf16 v[156:159], a[216:219], a[180:183], v[156:159]\n"
        "v_exp_f32 v104, v104\n"
        "v_mfma_f32_16x16x32_bf16 v[160:163], a[216:219], a[204:207], v[160:163]\n"
        "v_exp_f32 v105, v105\n"
        "v_mfma_f32_16x16x32_bf16 v[160:163], a[220:223], a[208:211], v[160:163]\n"
        "v_cvt_pk_bf16_f32 v88, v88, v89\n"
        "v_cvt_pk_bf16_f32 v89, v90, v91\n"
        "v_mfma_f32_16x16x32_bf16 v[160:163], a[224:227], a[212:215], v[160:163]\n"
        "v_exp_f32 v106, v106\n"
        "v_mfma_f32_16x16x32_bf16 v[164:167], a[228:231], a[156:159], v[164:167]\n"
        "v_cvt_pk_bf16_f32 v138, v156, v157\n"
        "v_cvt_pk_bf16_f32 v139, v158, v159\n"
        "v_mfma_f32_16x16x32_bf16 v[164:167], a[232:235], a[160:163], v[164:167]\n"
        "v_exp_f32 v107, v107\n"
        "v_mfma_f32_16x16x32_bf16 v[164:167], a[236:239], a[164:167], v[164:167]\n"
        "v_exp_f32 v92, v92\n"
        "v_mfma_f32_16x16x32_bf16 v[108:111], v[212:215], v[216:219], v[60:63]\n"
        "v_cvt_pk_bf16_f32 v146, v160, v161\n"
        "v_cvt_pk_bf16_f32 v147, v162, v163\n"
        "v_mfma_f32_16x16x32_bf16 v[108:111], v[228:231], v[224:227], v[108:111]\n"
        "v_exp_f32 v93, v93\n"
        "v_mfma_f32_16x16x32_bf16 v[176:179], a[236:239], a[188:191], v[176:179]\n"
        "v_exp_f32 v94, v94\n"
        "v_mfma_f32_16x16x32_bf16 v[176:179], a[232:235], a[184:187], v[176:179]\n"
        "v_cvt_pk_bf16_f32 v132, v164, v165\n"
        "v_cvt_pk_bf16_f32 v133, v166, v167\n"
        "v_mfma_f32_16x16x32_bf16 v[176:179], a[228:231], a[180:183], v[176:179]\n"
        "v_exp_f32 v95, v95\n"
        "v_mfma_f32_16x16x32_bf16 v[180:183], a[228:231], a[204:207], v[180:183]\n"
        "v_mov_b64 v[96:97], 0\n"
        "v_cvt_pk_bf16_f32 v90, v104, v105\n"
        "v_mfma_f32_16x16x32_bf16 v[180:183], a[232:235], a[208:211], v[180:183]\n"
        "v_exp_f32 v108, v108\n"
        "v_mfma_f32_16x16x32_bf16 v[180:183], a[236:239], a[212:215], v[180:183]\n"
        "s_waitcnt lgkmcnt(2)\n"
        "v_exp_f32 v109, v109\n"
        "v_mfma_f32_16x16x32_bf16 v[184:187], a[240:243], a[156:159], v[184:187]\n"
        "s_waitcnt lgkmcnt(1)\n"
        "v_cvt_pk_bf16_f32 v91, v106, v107\n"
        "v_cvt_pk_bf16_f32 v140, v176, v177\n"
        "v_mfma_f32_16x16x32_bf16 v[184:187], a[244:247], a[160:163], v[184:187]\n"
        "s_waitcnt lgkmcnt(0)\n"
        "v_cvt_pk_bf16_f32 v141, v178, v179\n"
        "v_add_u32 v240, 24576, v240\n"
        "v_mfma_f32_16x16x32_bf16 v[184:187], a[248:251], a[164:167], v[184:187]\n"
        "v_exp_f32 v110, v110\n"
        "v_mfma_f32_16x16x32_bf16 v[196:199], a[248:251], a[188:191], v[196:199]\n"
        "v_cvt_pk_bf16_f32 v148, v180, v181\n"
        "v_cvt_pk_bf16_f32 v149, v182, v183\n"
        "v_mfma_f32_16x16x32_bf16 v[196:199], a[244:247], a[184:187], v[196:199]\n"
        "v_mov_b64 v[172:173], 0\n"
        "v_add_u32 v241, 24576, v241\n"
        "v_mfma_f32_16x16x32_bf16 v[196:199], a[240:243], a[180:183], v[196:199]\n"
        "v_exp_f32 v111, v111\n"
        "v_mfma_f32_16x16x32_bf16 v[200:203], a[240:243], a[204:207], v[200:203]\n"
        "v_cvt_pk_bf16_f32 v134, v184, v185\n"
        "v_cvt_pk_bf16_f32 v135, v186, v187\n"
        "v_mfma_f32_16x16x32_bf16 v[200:203], a[244:247], a[208:211], v[200:203]\n"
        "v_cvt_pk_bf16_f32 v92, v92, v93\n"
        "v_cvt_pk_bf16_f32 v93, v94, v95\n"
        "v_mfma_f32_16x16x32_bf16 v[200:203], a[248:251], a[212:215], v[200:203]\n"
        "v_cvt_pk_bf16_f32 v142, v196, v197\n"
        "v_cvt_pk_bf16_f32 v143, v198, v199\n"
        "v_smfmac_f32_16x16x64_bf16 v[96:99], a[252:255], v[64:71], v237\n"
        "v_cvt_pk_bf16_f32 v94, v108, v109\n"
        "v_mfma_f32_16x16x32_bf16 v[100:103], v[132:135], v[68:71], 0\n"
        "v_cvt_pk_bf16_f32 v95, v110, v111\n"
        "s_nop 0\n"
        "v_mfma_f32_16x16x32_bf16 v[100:103], v[128:131], v[64:67], v[100:103]\n"
        "v_mov_b64 v[192:193], 0\n"
        "v_cvt_pk_bf16_f32 v150, v200, v201\n"
        "v_mfma_f32_16x16x32_bf16 v[104:107], v[136:139], v[64:67], 0\n"
        "v_cvt_pk_bf16_f32 v151, v202, v203\n"
        "v_add_f32 v97, v96, v97\n"
        "v_mfma_f32_16x16x32_bf16 v[104:107], v[140:143], v[68:71], v[104:107]\n"
        "v_rcp_f32 v96, v97\n"
        "v_mfma_f32_16x16x32_bf16 v[108:111], v[148:151], v[68:71], 0\n"
        "v_mul_f32 v100, v96, v100\n"
        "v_mul_f32 v101, v96, v101\n"
        "v_mfma_f32_16x16x32_bf16 v[108:111], v[144:147], v[64:67], v[108:111]\n"
        "s_waitcnt vmcnt(6)\n"
        "s_barrier\n"
        "ds_read_b128 a[236:239], v240 offset:0\n"
        "ds_read_b128 a[240:243], v241 offset:0\n"
        "v_smfmac_f32_16x16x64_bf16 v[172:175], a[252:255], v[72:79], v237\n"
        "ds_read_b128 a[244:247], v240 offset:128\n"
        "v_mfma_f32_16x16x32_bf16 v[176:179], v[132:135], v[76:79], 0\n"
        "ds_read_b128 a[248:251], v241 offset:128\n"
        "ds_read_b128 a[216:219], v240 offset:256\n"
        "v_mfma_f32_16x16x32_bf16 v[176:179], v[128:131], v[72:75], v[176:179]\n"
        "ds_read_b128 a[220:223], v241 offset:256\n"
        "v_mul_f32 v102, v96, v102\n"
        "v_mfma_f32_16x16x32_bf16 v[180:183], v[136:139], v[72:75], 0\n"
        "s_add_u32 m0, s65, 12288\n"
        "v_mul_f32 v103, v96, v103\n"
        "v_cvt_pk_bf16_f32 v100, v100, v101\n"
        "v_mfma_f32_16x16x32_bf16 v[180:183], v[140:143], v[76:79], v[180:183]\n"
        "s_mov_b32 s64, 12288\n"
        "v_cvt_pk_bf16_f32 v101, v102, v103\n"
        "v_mul_f32 v104, v96, v104\n"
        "v_mfma_f32_16x16x32_bf16 v[184:187], v[148:151], v[76:79], 0\n"
        "buffer_load_dwordx4 v238, s[52:55], s64 offen lds\n"
        "v_mfma_f32_16x16x32_bf16 v[184:187], v[144:147], v[72:75], v[184:187]\n"
        "v_mul_f32 v105, v96, v105\n"
        "v_mul_f32 v106, v96, v106\n"
        "v_smfmac_f32_16x16x64_bf16 v[192:195], a[252:255], v[80:87], v237\n"
        "v_mul_f32 v107, v96, v107\n"
        "v_mfma_f32_16x16x32_bf16 v[196:199], v[132:135], v[84:87], 0\n"
        "v_cvt_pk_bf16_f32 v102, v104, v105\n"
        "v_cvt_pk_bf16_f32 v103, v106, v107\n"
        "v_mfma_f32_16x16x32_bf16 v[196:199], v[128:131], v[80:83], v[196:199]\n"
        "ds_write_b128 v245, v[100:103] offset:0\n"
        "v_mfma_f32_16x16x32_bf16 v[200:203], v[136:139], v[80:83], 0\n"
        "v_mov_b64 v[124:125], 0\n"
        "v_mul_f32 v108, v96, v108\n"
        "v_mfma_f32_16x16x32_bf16 v[200:203], v[140:143], v[84:87], v[200:203]\n"
        "v_mul_f32 v109, v96, v109\n"
        "v_mul_f32 v110, v96, v110\n"
        "v_mul_f32 v111, v96, v111\n"
        "v_mfma_f32_16x16x32_bf16 v[204:207], v[148:151], v[84:87], 0\n"
        "v_cvt_pk_bf16_f32 v108, v108, v109\n"
        "v_cvt_pk_bf16_f32 v109, v110, v111\n"
        "v_mfma_f32_16x16x32_bf16 v[204:207], v[144:147], v[80:83], v[204:207]\n"
        "s_waitcnt lgkmcnt(6)\n"
        "buffer_load_dwordx4 v239, s[52:55], s64 offen offset:1024 lds\n"
        "v_mfma_f32_16x16x32_bf16 v[112:115], a[72:75], a[236:239], 0\n"
        "s_waitcnt lgkmcnt(5)\n"
        "ds_write_b64 v246, v[108:109] offset:0\n"
        "v_mfma_f32_16x16x32_bf16 v[112:115], a[76:79], a[240:243], v[112:115]\n"
        "s_waitcnt lgkmcnt(5)\n"
        "v_max_u32 v236, v96, v97\n"
        "v_add_f32 v173, v172, v173\n"
        "v_mfma_f32_16x16x32_bf16 v[112:115], a[80:83], a[244:247], v[112:115]\n"
        "s_waitcnt lgkmcnt(4)\n"
        "v_rcp_f32 v172, v173\n"
        "v_mfma_f32_16x16x32_bf16 v[112:115], a[84:87], a[248:251], v[112:115]\n"
        "s_waitcnt lgkmcnt(3)\n"
        "v_mul_f32 v176, v172, v176\n"
        "v_mul_f32 v177, v172, v177\n"
        "v_mfma_f32_16x16x32_bf16 v[112:115], a[88:91], a[216:219], v[112:115]\n"
        "s_waitcnt lgkmcnt(2)\n"
        "v_mul_f32 v178, v172, v178\n"
        "v_mul_f32 v179, v172, v179\n"
        "v_mfma_f32_16x16x32_bf16 v[112:115], a[92:95], a[220:223], v[112:115]\n"
        "v_cvt_pk_bf16_f32 v176, v176, v177\n"
        "v_cvt_pk_bf16_f32 v177, v178, v179\n"
        "v_smfmac_f32_16x16x64_bf16 v[124:127], a[252:255], v[88:95], v237\n"
        "v_mul_f32 v180, v172, v180\n"
        "v_mfma_f32_16x16x32_bf16 v[80:83], v[132:135], v[92:95], 0\n"
        "buffer_load_dwordx4 v247, s[52:55], s64 offen offset:2048 lds\n"
        "v_mfma_f32_16x16x32_bf16 v[80:83], v[128:131], v[88:91], v[80:83]\n"
        "v_mul_f32 v181, v172, v181\n"
        "v_cvt_pk_bf16_f32 v152, v112, v113\n"
        "v_mfma_f32_16x16x32_bf16 v[84:87], v[136:139], v[88:91], 0\n"
        "v_cvt_pk_bf16_f32 v153, v114, v115\n"
        "v_mul_f32 v182, v172, v182\n"
        "v_mfma_f32_16x16x32_bf16 v[84:87], v[140:143], v[92:95], v[84:87]\n"
        "v_mul_f32 v183, v172, v183\n"
        "v_cvt_pk_bf16_f32 v178, v180, v181\n"
        "v_mfma_f32_16x16x32_bf16 v[96:99], v[148:151], v[92:95], 0\n"
        "v_cvt_pk_bf16_f32 v179, v182, v183\n"
        "v_mul_f32 v184, v172, v184\n"
        "v_mfma_f32_16x16x32_bf16 v[96:99], v[144:147], v[88:91], v[96:99]\n"
        "ds_write_b128 v245, v[176:179] offset:256\n"
        "v_mfma_f32_16x16x32_bf16 v[116:119], a[116:119], a[220:223], 0\n"
        "s_add_u32 m0, s65, 15360\n"
        "v_mul_f32 v185, v172, v185\n"
        "v_mul_f32 v186, v172, v186\n"
        "v_mfma_f32_16x16x32_bf16 v[116:119], a[112:115], a[216:219], v[116:119]\n"
        "s_mov_b32 s64, 49152\n"
        "v_mul_f32 v187, v172, v187\n"
        "v_add_f32 v125, v124, v125\n"
        "v_mfma_f32_16x16x32_bf16 v[116:119], a[108:111], a[248:251], v[116:119]\n"
        "buffer_load_dwordx4 v238, s[52:55], s64 offen lds\n"
        "v_mfma_f32_16x16x32_bf16 v[116:119], a[104:107], a[244:247], v[116:119]\n"
        "v_rcp_f32 v124, v125\n"
        "v_mfma_f32_16x16x32_bf16 v[116:119], a[100:103], a[240:243], v[116:119]\n"
        "v_cvt_pk_bf16_f32 v184, v184, v185\n"
        "v_cvt_pk_bf16_f32 v185, v186, v187\n"
        "v_mfma_f32_16x16x32_bf16 v[116:119], a[96:99], a[236:239], v[116:119]\n"
        "ds_write_b64 v246, v[184:185] offset:256\n"
        "v_mfma_f32_16x16x32_bf16 v[120:123], a[120:123], a[236:239], 0\n"
        "v_mul_f32 v80, v124, v80\n"
        "v_mul_f32 v81, v124, v81\n"
        "v_mfma_f32_16x16x32_bf16 v[120:123], a[124:127], a[240:243], v[120:123]\n"
        "v_mul_f32 v82, v124, v82\n"
        "v_mul_f32 v83, v124, v83\n"
        "v_mfma_f32_16x16x32_bf16 v[120:123], a[128:131], a[244:247], v[120:123]\n"
        "v_max3_u32 v236, v236, v172, v173\n"
        "v_cvt_pk_bf16_f32 v80, v80, v81\n"
        "v_mfma_f32_16x16x32_bf16 v[120:123], a[132:135], a[248:251], v[120:123]\n"
        "v_cvt_pk_bf16_f32 v81, v82, v83\n"
        "v_mul_f32 v84, v124, v84\n"
        "v_mfma_f32_16x16x32_bf16 v[120:123], a[136:139], a[216:219], v[120:123]\n"
        "buffer_load_dwordx4 v239, s[52:55], s64 offen offset:1024 lds\n"
        "v_mfma_f32_16x16x32_bf16 v[120:123], a[140:143], a[220:223], v[120:123]\n"
        "v_mul_f32 v85, v124, v85\n"
        "v_mul_f32 v86, v124, v86\n"
        "v_mfma_f32_16x16x32_bf16 v[112:115], a[20:23], a[220:223], 0\n"
        "v_mul_f32 v87, v124, v87\n"
        "v_cvt_pk_bf16_f32 v82, v84, v85\n"
        "v_mfma_f32_16x16x32_bf16 v[112:115], a[16:19], a[216:219], v[112:115]\n"
        "v_cvt_pk_bf16_f32 v83, v86, v87\n"
        "v_mul_f32 v96, v124, v96\n"
        "v_mfma_f32_16x16x32_bf16 v[112:115], a[12:15], a[248:251], v[112:115]\n"
        "ds_write_b128 v245, v[80:83] offset:768\n"
        "v_mfma_f32_16x16x32_bf16 v[112:115], a[8:11], a[244:247], v[112:115]\n"
        "v_mul_f32 v97, v124, v97\n"
        "v_mul_f32 v98, v124, v98\n"
        "v_mfma_f32_16x16x32_bf16 v[112:115], a[4:7], a[240:243], v[112:115]\n"
        "v_mul_f32 v99, v124, v99\n"
        "v_add_f32 v193, v192, v193\n"
        "v_mfma_f32_16x16x32_bf16 v[112:115], a[0:3], a[236:239], v[112:115]\n"
        "v_cvt_pk_bf16_f32 v154, v116, v117\n"
        "v_cvt_pk_bf16_f32 v155, v118, v119\n"
        "v_mfma_f32_16x16x32_bf16 v[116:119], a[24:27], a[236:239], 0\n"
        "buffer_load_dwordx4 v247, s[52:55], s64 offen offset:2048 lds\n"
        "v_mfma_f32_16x16x32_bf16 v[116:119], a[28:31], a[240:243], v[116:119]\n"
        "v_rcp_f32 v192, v193\n"
        "v_mfma_f32_16x16x32_bf16 v[116:119], a[32:35], a[244:247], v[116:119]\n"
        "v_cvt_pk_bf16_f32 v96, v96, v97\n"
        "v_cvt_pk_bf16_f32 v97, v98, v99\n"
        "v_mfma_f32_16x16x32_bf16 v[116:119], a[36:39], a[248:251], v[116:119]\n"
        "ds_write_b64 v246, v[96:97] offset:768\n"
        "v_mfma_f32_16x16x32_bf16 v[116:119], a[40:43], a[216:219], v[116:119]\n"
        "v_mul_f32 v196, v192, v196\n"
        "v_mul_f32 v197, v192, v197\n"
        "v_mfma_f32_16x16x32_bf16 v[116:119], a[44:47], a[220:223], v[116:119]\n"
        "v_mul_f32 v198, v192, v198\n"
        "v_mul_f32 v199, v192, v199\n"
        "v_cvt_pk_bf16_f32 v168, v120, v121\n"
        "v_cvt_pk_bf16_f32 v169, v122, v123\n"
        "v_mfma_f32_16x16x32_bf16 v[120:123], a[68:71], a[220:223], 0\n"
        "s_add_u32 m0, s65, 0\n"
        "v_cvt_pk_bf16_f32 v196, v196, v197\n"
        "v_cvt_pk_bf16_f32 v197, v198, v199\n"
        "v_mfma_f32_16x16x32_bf16 v[120:123], a[64:67], a[216:219], v[120:123]\n"
        "s_mov_b32 s64, 0\n"
        "v_mul_f32 v200, v192, v200\n"
        "v_mul_f32 v201, v192, v201\n"
        "v_mfma_f32_16x16x32_bf16 v[120:123], a[60:63], a[248:251], v[120:123]\n"
        "buffer_load_dwordx4 v232, s[52:55], s64 offen lds\n"
        "v_mfma_f32_16x16x32_bf16 v[120:123], a[56:59], a[244:247], v[120:123]\n"
        "ds_read_b128 a[224:227], v240 offset:1536\n"
        "v_mul_f32 v202, v192, v202\n"
        "v_mfma_f32_16x16x32_bf16 v[120:123], a[52:55], a[240:243], v[120:123]\n"
        "ds_read_b128 a[228:231], v241 offset:1536\n"
        "v_mul_f32 v203, v192, v203\n"
        "v_mfma_f32_16x16x32_bf16 v[120:123], a[48:51], a[236:239], v[120:123]\n"
        "ds_read_b128 a[232:235], v240 offset:1664\n"
        "v_cvt_pk_bf16_f32 v198, v200, v201\n"
        "v_cvt_pk_bf16_f32 v156, v112, v113\n"
        "v_cvt_pk_bf16_f32 v157, v114, v115\n"
        "v_mfma_f32_16x16x32_bf16 v[112:115], a[236:239], a[144:147], 0\n"
        "v_cvt_pk_bf16_f32 v199, v202, v203\n"
        "v_mul_f32 v204, v192, v204\n"
        "v_mfma_f32_16x16x32_bf16 v[112:115], a[240:243], a[148:151], v[112:115]\n"
        "ds_write_b128 v245, v[196:199] offset:512\n"
        "v_mfma_f32_16x16x32_bf16 v[112:115], a[244:247], a[152:155], v[112:115]\n"
        "s_waitcnt vmcnt(4)\n"
        "ds_read_b64_tr_b16 v[64:65], v255 offset:12288\n"
        "ds_read_b64_tr_b16 v[66:67], v255 offset:13056\n"
        "v_mfma_f32_16x16x32_bf16 v[112:115], a[248:251], a[156:159], v[112:115]\n"
        "v_mul_f32 v205, v192, v205\n"
        "v_mul_f32 v206, v192, v206\n"
        "v_mfma_f32_16x16x32_bf16 v[112:115], a[216:219], a[160:163], v[112:115]\n"
        "buffer_load_dwordx4 v233, s[52:55], s64 offen offset:1024 lds\n"
        "v_mfma_f32_16x16x32_bf16 v[112:115], a[220:223], a[164:167], v[112:115]\n"
        "ds_read_b64_tr_b16 v[68:69], v255 offset:12320\n"
        "ds_read_b64_tr_b16 v[70:71], v255 offset:13088\n"
        "v_cvt_pk_bf16_f32 v158, v116, v117\n"
        "v_cvt_pk_bf16_f32 v159, v118, v119\n"
        "v_mfma_f32_16x16x32_bf16 v[116:119], a[220:223], a[188:191], 0\n"
        "ds_read_b64_tr_b16 v[72:73], v255 offset:12352\n"
        "ds_read_b64_tr_b16 v[74:75], v255 offset:13120\n"
        "v_mfma_f32_16x16x32_bf16 v[116:119], a[216:219], a[184:187], v[116:119]\n"
        "v_mul_f32 v207, v192, v207\n"
        "v_cvt_pk_bf16_f32 v204, v204, v205\n"
        "v_mfma_f32_16x16x32_bf16 v[116:119], a[248:251], a[180:183], v[116:119]\n"
        "v_cvt_pk_bf16_f32 v205, v206, v207\n"
        "v_max3_u32 v236, v236, v192, v193\n"
        "v_mfma_f32_16x16x32_bf16 v[116:119], a[244:247], a[176:179], v[116:119]\n"
        "ds_write_b64 v246, v[204:205] offset:512\n"
        "v_mfma_f32_16x16x32_bf16 v[116:119], a[240:243], a[172:175], v[116:119]\n"
        "v_max3_u32 v236, v236, v124, v125\n"
        "v_cmp_lt_u32 vcc, s24, v236\n"
        "v_mfma_f32_16x16x32_bf16 v[116:119], a[236:239], a[168:171], v[116:119]\n"
        "v_cvt_pk_bf16_f32 v160, v120, v121\n"
        "v_cvt_pk_bf16_f32 v161, v122, v123\n"
        "v_cvt_pk_bf16_f32 v166, v120, v121\n"
        "v_cvt_pk_bf16_f32 v167, v122, v123\n"
        "v_mfma_f32_16x16x32_bf16 v[120:123], a[236:239], a[192:195], 0\n"
        "s_cmp_lg_u64 vcc, 0\n"
        "s_addc_u32 s25, s25, s25\n"
        "ds_read_b128 a[236:239], v241 offset:1664\n"
        "v_mfma_f32_16x16x32_bf16 v[120:123], a[240:243], a[196:199], v[120:123]\n"
        "buffer_load_dwordx4 v234, s[52:55], s64 offen offset:2048 lds\n"
        "v_mfma_f32_16x16x32_bf16 v[120:123], a[244:247], a[200:203], v[120:123]\n"
        "ds_read_b128 a[240:243], v240 offset:1792\n"
        "ds_read_b128 a[244:247], v241 offset:1792\n"
        "v_mfma_f32_16x16x32_bf16 v[120:123], a[248:251], a[204:207], v[120:123]\n"
        "s_waitcnt lgkmcnt(8)\n"
        "ds_write_b128 v235, v[64:67] offset:12288\n"
        "v_mfma_f32_16x16x32_bf16 v[120:123], a[216:219], a[208:211], v[120:123]\n"
        "s_waitcnt vmcnt(3)\n"
        "ds_read_b64_tr_b16 v[76:77], v255 offset:15360\n"
        "ds_read_b64_tr_b16 v[78:79], v255 offset:16128\n"
        "v_mfma_f32_16x16x32_bf16 v[120:123], a[220:223], a[212:215], v[120:123]\n"
        "v_cvt_pk_bf16_f32 v128, v112, v113\n"
        "v_cvt_pk_bf16_f32 v129, v114, v115\n"
        "v_mfma_f32_16x16x32_bf16 v[112:115], a[72:75], a[224:227], 0\n"
        "s_add_u32 m0, s65, 3072\n"
        "v_cvt_pk_bf16_f32 v136, v116, v117\n"
        "v_cvt_pk_bf16_f32 v137, v118, v119\n"
        "v_mfma_f32_16x16x32_bf16 v[112:115], a[76:79], a[228:231], v[112:115]\n"
        "s_mov_b32 s64, 18432\n"
        "v_xor_b32 v251, 0x2000, v251\n"
        "v_xor_b32 v252, 0x2000, v252\n"
        "v_mfma_f32_16x16x32_bf16 v[112:115], a[80:83], a[232:235], v[112:115]\n"
        "s_waitcnt lgkmcnt(5)\n"
        "buffer_load_dwordx4 v232, s[52:55], s64 offen lds\n"
        "v_mfma_f32_16x16x32_bf16 v[112:115], a[84:87], a[236:239], v[112:115]\n"
        "s_waitcnt lgkmcnt(4)\n"
        "ds_write_b128 v235, v[68:71] offset:12544\n"
        "v_mfma_f32_16x16x32_bf16 v[112:115], a[88:91], a[240:243], v[112:115]\n"
        "s_waitcnt lgkmcnt(4)\n"
        "s_barrier\n"
        "v_cvt_pk_bf16_f32 v144, v120, v121\n"
        "v_cvt_pk_bf16_f32 v145, v122, v123\n"
        "v_mfma_f32_16x16x32_bf16 v[112:115], a[92:95], a[244:247], v[112:115]\n"
        "ds_read_b64_tr_b16 v[88:89], v255 offset:15392\n"
        "ds_read_b64_tr_b16 v[90:91], v255 offset:16160\n"
        "v_mfma_f32_16x16x32_bf16 v[116:119], a[116:119], a[244:247], 0\n"
        "v_add_u32 v247, s45, v245\n"
        "s_nop 0\n"
        "v_mfma_f32_16x16x32_bf16 v[116:119], a[112:115], a[240:243], v[116:119]\n"
        "s_nop 1\n"
        "v_cvt_pk_bf16_f32 v172, v112, v113\n"
        "v_mfma_f32_16x16x32_bf16 v[116:119], a[108:111], a[236:239], v[116:119]\n"
        "v_cvt_pk_bf16_f32 v173, v114, v115\n"
        "v_mfma_f32_16x16x32_bf16 v[116:119], a[104:107], a[232:235], v[116:119]\n"
        "ds_write_b128 v235, v[72:75] offset:12800\n"
        "v_mfma_f32_16x16x32_bf16 v[116:119], a[100:103], a[228:231], v[116:119]\n"
        ".long 0xbec200ff\n"
        "43:\n"
        "s_branch 61b\n"
        "buffer_load_dwordx4 v233, s[52:55], s64 offen offset:1024 lds\n"
        "v_mfma_f32_16x16x32_bf16 v[116:119], a[96:99], a[224:227], v[116:119]\n"
        "s_nop 2\n"
        "v_mfma_f32_16x16x32_bf16 v[120:123], a[120:123], a[224:227], 0\n"
        "ds_read_b64_tr_b16 v[92:93], v255 offset:15424\n"
        "ds_read_b64_tr_b16 v[94:95], v255 offset:16192\n"
        "v_mfma_f32_16x16x32_bf16 v[120:123], a[124:127], a[228:231], v[120:123]\n"
        "ds_read_b128 v[162:165], v251 offset:0\n"
        "v_cvt_pk_bf16_f32 v174, v116, v117\n"
        "v_mfma_f32_16x16x32_bf16 v[120:123], a[128:131], a[232:235], v[120:123]\n"
        "v_cvt_pk_bf16_f32 v175, v118, v119\n"
        "v_mfma_f32_16x16x32_bf16 v[120:123], a[132:135], a[236:239], v[120:123]\n"
        "v_mfma_f32_16x16x32_bf16 v[120:123], a[136:139], a[240:243], v[120:123]\n"
        "v_mfma_f32_16x16x32_bf16 v[120:123], a[140:143], a[244:247], v[120:123]\n"
        "s_nop 2\n"
        "v_mfma_f32_16x16x32_bf16 v[112:115], a[20:23], a[244:247], 0\n"
        "buffer_load_dwordx4 v234, s[52:55], s64 offen offset:2048 lds\n"
        "v_mfma_f32_16x16x32_bf16 v[112:115], a[16:19], a[240:243], v[112:115]\n"
        "s_waitcnt vmcnt(3)\n"
        "ds_read_b64_tr_b16 v[212:213], v255 offset:0\n"
        "ds_read_b64_tr_b16 v[214:215], v255 offset:768\n"
        "v_mfma_f32_16x16x32_bf16 v[112:115], a[12:15], a[236:239], v[112:115]\n"
        "v_cvt_pk_bf16_f32 v190, v120, v121\n"
        "v_cvt_pk_bf16_f32 v191, v122, v123\n"
        "v_mfma_f32_16x16x32_bf16 v[112:115], a[8:11], a[232:235], v[112:115]\n"
        "v_mfma_f32_16x16x32_bf16 v[112:115], a[4:7], a[228:231], v[112:115]\n"
        "ds_read_b128 v[182:185], v251 offset:512\n"
        "v_mfma_f32_16x16x32_bf16 v[112:115], a[0:3], a[224:227], v[112:115]\n"
        "s_waitcnt lgkmcnt(10)\n"
        "ds_write_b128 v235, v[76:79] offset:15360\n"
        "v_mfma_f32_16x16x32_bf16 v[116:119], a[24:27], a[224:227], 0\n"
        "s_add_u32 m0, s65, 6144\n"
        "s_nop 1\n"
        "v_mfma_f32_16x16x32_bf16 v[116:119], a[28:31], a[228:231], v[116:119]\n"
        "s_mov_b32 s64, 36864\n"
        "s_nop 0\n"
        "v_cvt_pk_bf16_f32 v176, v112, v113\n"
        "v_mfma_f32_16x16x32_bf16 v[116:119], a[32:35], a[232:235], v[116:119]\n"
        "buffer_load_dwordx4 v232, s[52:55], s64 offen lds\n"
        "v_mfma_f32_16x16x32_bf16 v[116:119], a[36:39], a[236:239], v[116:119]\n"
        "ds_read_b64_tr_b16 v[216:217], v255 offset:32\n"
        "ds_read_b64_tr_b16 v[218:219], v255 offset:800\n"
        "v_mfma_f32_16x16x32_bf16 v[116:119], a[40:43], a[240:243], v[116:119]\n"
        "v_cvt_pk_bf16_f32 v177, v114, v115\n"
        "v_mfma_f32_16x16x32_bf16 v[116:119], a[44:47], a[244:247], v[116:119]\n"
        "s_nop 2\n"
        "v_mfma_f32_16x16x32_bf16 v[120:123], a[68:71], a[244:247], 0\n"
        "s_waitcnt lgkmcnt(10)\n"
        "ds_write_b128 v235, v[88:91] offset:15616\n"
        "v_mfma_f32_16x16x32_bf16 v[120:123], a[64:67], a[240:243], v[120:123]\n"
        "s_nop 1\n"
        "v_cvt_pk_bf16_f32 v178, v116, v117\n"
        "v_mfma_f32_16x16x32_bf16 v[120:123], a[60:63], a[236:239], v[120:123]\n"
        "v_cvt_pk_bf16_f32 v179, v118, v119\n"
        "v_mfma_f32_16x16x32_bf16 v[120:123], a[56:59], a[232:235], v[120:123]\n"
        "ds_read_b64_tr_b16 v[220:221], v255 offset:64\n"
        "ds_read_b64_tr_b16 v[222:223], v255 offset:832\n"
        "v_mfma_f32_16x16x32_bf16 v[120:123], a[52:55], a[228:231], v[120:123]\n"
        "buffer_load_dwordx4 v233, s[52:55], s64 offen offset:1024 lds\n"
        "v_mfma_f32_16x16x32_bf16 v[120:123], a[48:51], a[224:227], v[120:123]\n"
        "s_nop 2\n"
        "v_mfma_f32_16x16x32_bf16 v[112:115], a[224:227], a[144:147], 0\n"
        "s_nop 2\n"
        "v_mfma_f32_16x16x32_bf16 v[112:115], a[228:231], a[148:151], v[112:115]\n"
        "s_waitcnt lgkmcnt(10)\n"
        "ds_write_b128 v235, v[92:95] offset:15872\n"
        "v_mfma_f32_16x16x32_bf16 v[112:115], a[232:235], a[152:155], v[112:115]\n"
        "v_cvt_pk_bf16_f32 v180, v120, v121\n"
        "v_cvt_pk_bf16_f32 v181, v122, v123\n"
        "v_mfma_f32_16x16x32_bf16 v[112:115], a[236:239], a[156:159], v[112:115]\n"
        "v_cvt_pk_bf16_f32 v186, v120, v121\n"
        "v_cvt_pk_bf16_f32 v187, v122, v123\n"
        "v_mfma_f32_16x16x32_bf16 v[112:115], a[240:243], a[160:163], v[112:115]\n"
        "s_waitcnt vmcnt(2)\n"
        "ds_read_b64_tr_b16 v[224:225], v255 offset:3072\n"
        "ds_read_b64_tr_b16 v[226:227], v255 offset:3840\n"
        "v_mfma_f32_16x16x32_bf16 v[112:115], a[244:247], a[164:167], v[112:115]\n"
        "ds_read_b128 a[248:251], v240 offset:12288\n"
        "s_nop 0\n"
        "v_mfma_f32_16x16x32_bf16 v[96:99], v[152:155], v[156:159], v[0:3]\n"
        "s_waitcnt lgkmcnt(13)\n"
        "buffer_load_dwordx4 v234, s[52:55], s64 offen offset:2048 lds\n"
        "v_mfma_f32_16x16x32_bf16 v[96:99], v[168:171], v[160:163], v[96:99]\n"
        "ds_read_b128 a[216:219], v241 offset:12288\n"
        "s_sub_u32 s31, s30, s28\n"
        "s_nop 0\n"
        "v_mfma_f32_16x16x32_bf16 v[100:103], v[172:175], v[156:159], v[4:7]\n"
        "s_waitcnt lgkmcnt(12)\n"
        "ds_write_b128 v235, v[212:215] offset:0\n"
        "s_sub_u32 s45, s28, s29\n"
        "v_mfma_f32_16x16x32_bf16 v[100:103], v[188:191], v[164:167], v[100:103]\n"
        "ds_read_b128 a[220:223], v240 offset:12416\n"
        "s_add_u32 s28, s28, s31\n"
        "v_cvt_pk_bf16_f32 v130, v112, v113\n"
        "v_mfma_f32_16x16x32_bf16 v[116:119], a[244:247], a[188:191], 0\n"
        "ds_read_b128 v[162:165], v252 offset:0\n"
        "v_cvt_pk_bf16_f32 v131, v114, v115\n"
        "v_mfma_f32_16x16x32_bf16 v[116:119], a[240:243], a[184:187], v[116:119]\n"
        "ds_read_b64_tr_b16 v[88:89], v255 offset:3104\n"
        "ds_read_b64_tr_b16 v[90:91], v255 offset:3872\n"
        "v_mfma_f32_16x16x32_bf16 v[116:119], a[236:239], a[180:183], v[116:119]\n"
        "s_add_u32 m0, s65, 9216\n"
        "v_exp_f32 v96, v96\n"
        "v_mfma_f32_16x16x32_bf16 v[116:119], a[232:235], a[176:179], v[116:119]\n"
        "s_mov_b32 s64, 55296\n"
        "v_exp_f32 v97, v97\n"
        "v_mfma_f32_16x16x32_bf16 v[116:119], a[228:231], a[172:175], v[116:119]\n"
        "buffer_load_dwordx4 v232, s[52:55], s64 offen lds\n"
        "v_mfma_f32_16x16x32_bf16 v[116:119], a[224:227], a[168:171], v[116:119]\n"
        "s_waitcnt lgkmcnt(13)\n"
        "ds_write_b128 v235, v[216:219] offset:256\n"
        "v_mfma_f32_16x16x32_bf16 v[104:107], v[152:155], v[176:179], v[16:19]\n"
        "v_exp_f32 v98, v98\n"
        "v_mfma_f32_16x16x32_bf16 v[104:107], v[168:171], v[180:183], v[104:107]\n"
        "v_cvt_pk_bf16_f32 v64, v96, v97\n"
        "s_nop 0\n"
        "v_mfma_f32_16x16x32_bf16 v[108:111], v[172:175], v[176:179], v[20:23]\n"
        "ds_read_b64_tr_b16 v[92:93], v255 offset:3136\n"
        "ds_read_b64_tr_b16 v[94:95], v255 offset:3904\n"
        "v_mfma_f32_16x16x32_bf16 v[108:111], v[188:191], v[184:187], v[108:111]\n"
        "ds_read_b128 v[182:185], v252 offset:512\n"
        "v_cvt_pk_bf16_f32 v138, v116, v117\n"
        "v_mfma_f32_16x16x32_bf16 v[120:123], a[224:227], a[192:195], 0\n"
        "ds_read_b128 a[224:227], v241 offset:12416\n"
        "v_cvt_pk_bf16_f32 v139, v118, v119\n"
        "v_mfma_f32_16x16x32_bf16 v[120:123], a[228:231], a[196:199], v[120:123]\n"
        "ds_read_b128 a[228:231], v240 offset:12544\n"
        "v_mfma_f32_16x16x32_bf16 v[120:123], a[232:235], a[200:203], v[120:123]\n"
        "ds_read_b128 a[232:235], v241 offset:12544\n"
        "v_mfma_f32_16x16x32_bf16 v[120:123], a[236:239], a[204:207], v[120:123]\n"
        "buffer_load_dwordx4 v233, s[52:55], s64 offen offset:1024 lds\n"
        "v_mfma_f32_16x16x32_bf16 v[120:123], a[240:243], a[208:211], v[120:123]\n"
        "s_waitcnt lgkmcnt(15)\n"
        "ds_write_b128 v235, v[220:223] offset:512\n"
        "v_mfma_f32_16x16x32_bf16 v[120:123], a[244:247], a[212:215], v[120:123]\n"
        "s_waitcnt lgkmcnt(14)\n"
        "s_waitcnt vmcnt(2)\n"
        "ds_read_b64_tr_b16 v[212:213], v255 offset:6144\n"
        "ds_read_b64_tr_b16 v[214:215], v255 offset:6912\n"
        "v_mfma_f32_16x16x32_bf16 v[112:115], a[0:3], a[248:251], 0\n"
        "s_waitcnt lgkmcnt(15)\n"
        "ds_read_b128 v[202:205], v251 offset:4096\n"
        "s_nop 0\n"
        "v_mfma_f32_16x16x32_bf16 v[112:115], a[4:7], a[216:219], v[112:115]\n"
        "s_waitcnt lgkmcnt(14)\n"
        "v_exp_f32 v100, v100\n"
        "v_mfma_f32_16x16x32_bf16 v[112:115], a[8:11], a[220:223], v[112:115]\n"
        "s_waitcnt lgkmcnt(6)\n"
        "v_exp_f32 v99, v99\n"
        "v_mfma_f32_16x16x32_bf16 v[112:115], a[12:15], a[224:227], v[112:115]\n"
        "s_waitcnt lgkmcnt(5)\n"
        "ds_write_b128 v235, v[224:227] offset:3072\n"
        "v_mfma_f32_16x16x32_bf16 v[112:115], a[16:19], a[228:231], v[112:115]\n"
        "s_waitcnt lgkmcnt(5)\n"
        "buffer_load_dwordx4 v234, s[52:55], s64 offen offset:2048 lds\n"
        "s_add_u32 s41, s30, s40\n"
        "s_add_u32 s35, s47, 786432\n"
        "s_sub_u32 s46, s35, 100663296\n"
        "s_cmp_lt_u32 s46, 786432\n"
        "s_mov_b32 s30, s29\n"
        "v_mfma_f32_16x16x32_bf16 v[112:115], a[20:23], a[232:235], v[112:115]\n"
        "s_cselect_b32 s35, s46, s35\n"
        "s_add_u32 s29, s29, s45\n"
        "v_cvt_pk_bf16_f32 v65, v98, v99\n"
        "v_mfma_f32_16x16x32_bf16 v[116:119], a[44:47], a[232:235], 0\n"
        "ds_read_b64_tr_b16 v[216:217], v255 offset:6176\n"
        "ds_read_b64_tr_b16 v[218:219], v255 offset:6944\n"
        "v_mfma_f32_16x16x32_bf16 v[116:119], a[40:43], a[228:231], v[116:119]\n"
        "v_exp_f32 v101, v101\n"
        "v_mfma_f32_16x16x32_bf16 v[116:119], a[36:39], a[224:227], v[116:119]\n"
        "v_exp_f32 v102, v102\n"
        "v_mfma_f32_16x16x32_bf16 v[116:119], a[32:35], a[220:223], v[116:119]\n"
        "v_cvt_pk_bf16_f32 v66, v100, v101\n"
        "v_cvt_pk_bf16_f32 v146, v120, v121\n"
        "v_mfma_f32_16x16x32_bf16 v[116:119], a[28:31], a[216:219], v[116:119]\n"
        "ds_write_b128 v235, v[88:91] offset:3328\n"
        "s_mov_b32 m0, s48\n"
        "v_mfma_f32_16x16x32_bf16 v[116:119], a[24:27], a[248:251], v[116:119]\n"
        "v_exp_f32 v103, v103\n"
        "v_cvt_pk_bf16_f32 v147, v122, v123\n"
        "v_mfma_f32_16x16x32_bf16 v[120:123], a[48:51], a[248:251], 0\n"
        "buffer_load_dwordx4 v249, s[12:15], s36 offen offset:0 lds\n"
        "v_mfma_f32_16x16x32_bf16 v[120:123], a[52:55], a[216:219], v[120:123]\n"
        "ds_read_b64_tr_b16 v[220:221], v255 offset:6208\n"
        "ds_read_b64_tr_b16 v[222:223], v255 offset:6976\n"
        "v_mfma_f32_16x16x32_bf16 v[120:123], a[56:59], a[220:223], v[120:123]\n"
        "v_cvt_pk_bf16_f32 v67, v102, v103\n"
        "v_cvt_pk_bf16_f32 v196, v112, v113\n"
        "v_mfma_f32_16x16x32_bf16 v[120:123], a[60:63], a[224:227], v[120:123]\n"
        "v_exp_f32 v104, v104\n"
        "v_mfma_f32_16x16x32_bf16 v[120:123], a[64:67], a[228:231], v[120:123]\n"
        "v_exp_f32 v105, v105\n"
        "v_mfma_f32_16x16x32_bf16 v[120:123], a[68:71], a[232:235], v[120:123]\n"
        "ds_write_b128 v235, v[92:95] offset:3584\n"
        "v_cvt_pk_bf16_f32 v197, v114, v115\n"
        "v_mfma_f32_16x16x32_bf16 v[112:115], a[92:95], a[232:235], 0\n"
        "v_exp_f32 v106, v106\n"
        "v_mfma_f32_16x16x32_bf16 v[112:115], a[88:91], a[228:231], v[112:115]\n"
        "buffer_load_dwordx4 v253, s[12:15], s36 offen offset:1024 lds\n"
        "v_mfma_f32_16x16x32_bf16 v[112:115], a[84:87], a[224:227], v[112:115]\n"
        "s_waitcnt vmcnt(2)\n"
        "ds_read_b64_tr_b16 v[224:225], v255 offset:9216\n"
        "ds_read_b64_tr_b16 v[226:227], v255 offset:9984\n"
        "v_mfma_f32_16x16x32_bf16 v[112:115], a[80:83], a[220:223], v[112:115]\n"
        "v_cvt_pk_bf16_f32 v72, v104, v105\n"
        "v_cvt_pk_bf16_f32 v198, v116, v117\n"
        "v_mfma_f32_16x16x32_bf16 v[112:115], a[76:79], a[216:219], v[112:115]\n"
        "v_exp_f32 v108, v108\n"
        "v_mfma_f32_16x16x32_bf16 v[112:115], a[72:75], a[248:251], v[112:115]\n"
        "v_exp_f32 v107, v107\n"
        "v_cvt_pk_bf16_f32 v199, v118, v119\n"
        "s_nop 1\n"
        "v_mfma_f32_16x16x32_bf16 v[96:99], v[152:155], v[196:199], v[32:35]\n"
        "s_waitcnt lgkmcnt(9)\n"
        "s_waitcnt lgkmcnt(10)\n"
        "ds_write_b128 v235, v[212:215] offset:6144\n"
        "s_add_u32 m0, s41, 0\n"
        "v_cvt_pk_bf16_f32 v200, v120, v121\n"
        "v_cvt_pk_bf16_f32 v201, v122, v123\n"
        "s_nop 1\n"
        "v_mfma_f32_16x16x32_bf16 v[96:99], v[168:171], v[200:203], v[96:99]\n"
        "v_exp_f32 v109, v109\n"
        "v_mfma_f32_16x16x32_bf16 v[100:103], v[172:175], v[196:199], v[36:39]\n"
        "buffer_load_dwordx4 v242, s[8:11], s47 offen lds\n"
        "v_cvt_pk_bf16_f32 v206, v120, v121\n"
        "v_cvt_pk_bf16_f32 v207, v122, v123\n"
        "s_nop 1\n"
        "v_mfma_f32_16x16x32_bf16 v[100:103], v[188:191], v[204:207], v[100:103]\n"
        "ds_read_b64_tr_b16 v[88:89], v255 offset:9248\n"
        "ds_read_b64_tr_b16 v[90:91], v255 offset:10016\n"
        "s_cmp_eq_u32 s37, 100663296\n"
        "v_mfma_f32_16x16x32_bf16 v[116:119], a[96:99], a[248:251], 0\n"
        "s_cselect_b32 s46, 0, s37\n"
        "ds_read_b128 v[202:205], v252 offset:4096\n"
        "v_cvt_pk_bf16_f32 v73, v106, v107\n"
        "v_mfma_f32_16x16x32_bf16 v[116:119], a[100:103], a[216:219], v[116:119]\n"
        "s_add_u32 s38, s46, s43\n"
        "v_exp_f32 v110, v110\n"
        "v_mfma_f32_16x16x32_bf16 v[116:119], a[104:107], a[220:223], v[116:119]\n"
        "s_add_u32 s39, s46, s44\n"
        "v_cvt_pk_bf16_f32 v74, v108, v109\n"
        "v_cvt_pk_bf16_f32 v192, v112, v113\n"
        "v_mfma_f32_16x16x32_bf16 v[116:119], a[108:111], a[224:227], v[116:119]\n"
        "s_waitcnt lgkmcnt(10)\n"
        "ds_write_b128 v235, v[216:219] offset:6400\n"
        "s_add_u32 m0, s41, 1024\n"
        "v_mfma_f32_16x16x32_bf16 v[116:119], a[112:115], a[228:231], v[116:119]\n"
        "s_add_u32 s37, s37, 6291456\n"
        "v_exp_f32 v111, v111\n"
        "v_mfma_f32_16x16x32_bf16 v[116:119], a[116:119], a[232:235], v[116:119]\n"
        "buffer_load_dwordx4 v243, s[8:11], s47 offen lds\n"
        "v_mfma_f32_16x16x32_bf16 v[120:123], a[140:143], a[232:235], 0\n"
        "ds_read_b64_tr_b16 v[92:93], v255 offset:9280\n"
        "ds_read_b64_tr_b16 v[94:95], v255 offset:10048\n"
        "v_mfma_f32_16x16x32_bf16 v[120:123], a[136:139], a[228:231], v[120:123]\n"
        "v_cvt_pk_bf16_f32 v75, v110, v111\n"
        "v_cvt_pk_bf16_f32 v193, v114, v115\n"
        "v_mfma_f32_16x16x32_bf16 v[120:123], a[132:135], a[224:227], v[120:123]\n"
        "v_exp_f32 v96, v96\n"
        "v_mfma_f32_16x16x32_bf16 v[120:123], a[128:131], a[220:223], v[120:123]\n"
        "v_exp_f32 v97, v97\n"
        "v_mfma_f32_16x16x32_bf16 v[120:123], a[124:127], a[216:219], v[120:123]\n"
        "s_waitcnt lgkmcnt(10)\n"
        "ds_write_b128 v235, v[220:223] offset:6656\n"
        "s_add_u32 m0, s41, 2048\n"
        "v_mfma_f32_16x16x32_bf16 v[120:123], a[120:123], a[248:251], v[120:123]\n"
        "v_exp_f32 v98, v98\n"
        "v_mfma_f32_16x16x32_bf16 v[112:115], a[248:251], a[144:147], 0\n"
        "buffer_load_dwordx4 v244, s[8:11], s47 offen lds\n"
        "v_mfma_f32_16x16x32_bf16 v[112:115], a[216:219], a[148:151], v[112:115]\n"
        "v_cvt_pk_bf16_f32 v80, v96, v97\n"
        "v_cvt_pk_bf16_f32 v194, v116, v117\n"
        "v_mfma_f32_16x16x32_bf16 v[112:115], a[220:223], a[152:155], v[112:115]\n"
        "ds_read_b128 a[236:239], v240 offset:13824\n"
        "v_cvt_pk_bf16_f32 v195, v118, v119\n"
        "v_mfma_f32_16x16x32_bf16 v[112:115], a[224:227], a[156:159], v[112:115]\n"
        "ds_read_b128 a[240:243], v241 offset:13824\n"
        "v_cvt_pk_bf16_f32 v208, v120, v121\n"
        "v_mfma_f32_16x16x32_bf16 v[112:115], a[228:231], a[160:163], v[112:115]\n"
        "ds_read_b128 a[244:247], v240 offset:13952\n"
        "v_cvt_pk_bf16_f32 v209, v122, v123\n"
        "v_mfma_f32_16x16x32_bf16 v[112:115], a[232:235], a[164:167], v[112:115]\n"
        "s_waitcnt lgkmcnt(11)\n"
        "ds_write_b128 v235, v[224:227] offset:9216\n"
        "s_add_u32 m0, s41, 12288\n"
        "v_mfma_f32_16x16x32_bf16 v[104:107], v[192:195], v[156:159], v[8:11]\n"
        "v_exp_f32 v100, v100\n"
        "v_mfma_f32_16x16x32_bf16 v[104:107], v[208:211], v[160:163], v[104:107]\n"
        "buffer_load_dwordx4 v242, s[8:11], s35 offen lds\n"
        "v_mfma_f32_16x16x32_bf16 v[116:119], a[232:235], a[188:191], 0\n"
        "v_exp_f32 v99, v99\n"
        "v_mfma_f32_16x16x32_bf16 v[116:119], a[228:231], a[184:187], v[116:119]\n"
        "v_exp_f32 v101, v101\n"
        "v_mfma_f32_16x16x32_bf16 v[116:119], a[224:227], a[180:183], v[116:119]\n"
        "v_cvt_pk_bf16_f32 v81, v98, v99\n"
        "v_cvt_pk_bf16_f32 v82, v100, v101\n"
        "v_mfma_f32_16x16x32_bf16 v[116:119], a[220:223], a[176:179], v[116:119]\n"
        "v_exp_f32 v102, v102\n"
        "v_mfma_f32_16x16x32_bf16 v[116:119], a[216:219], a[172:175], v[116:119]\n"
        "s_waitcnt lgkmcnt(9)\n"
        "ds_write_b128 v235, v[88:91] offset:9472\n"
        "s_add_u32 m0, s41, 13312\n"
        "v_mfma_f32_16x16x32_bf16 v[116:119], a[248:251], a[168:171], v[116:119]\n"
        "v_exp_f32 v103, v103\n"
        "v_mfma_f32_16x16x32_bf16 v[108:111], v[192:195], v[176:179], v[24:27]\n"
        "buffer_load_dwordx4 v243, s[8:11], s35 offen lds\n"
        "v_mfma_f32_16x16x32_bf16 v[108:111], v[208:211], v[180:183], v[108:111]\n"
        "v_cvt_pk_bf16_f32 v83, v102, v103\n"
        "v_cvt_pk_bf16_f32 v132, v112, v113\n"
        "v_mfma_f32_16x16x32_bf16 v[120:123], a[248:251], a[192:195], 0\n"
        "ds_read_b128 a[248:251], v241 offset:13952\n"
        "v_cvt_pk_bf16_f32 v133, v114, v115\n"
        "v_mfma_f32_16x16x32_bf16 v[120:123], a[216:219], a[196:199], v[120:123]\n"
        "ds_read_b128 a[216:219], v240 offset:14080\n"
        "v_cvt_pk_bf16_f32 v140, v116, v117\n"
        "v_mfma_f32_16x16x32_bf16 v[120:123], a[220:223], a[200:203], v[120:123]\n"
        "ds_read_b128 a[220:223], v241 offset:14080\n"
        "v_cvt_pk_bf16_f32 v141, v118, v119\n"
        "v_mfma_f32_16x16x32_bf16 v[120:123], a[224:227], a[204:207], v[120:123]\n"
        "s_waitcnt lgkmcnt(9)\n"
        "ds_write_b128 v235, v[92:95] offset:9728\n"
        "s_add_u32 m0, s41, 14336\n"
        "v_mfma_f32_16x16x32_bf16 v[120:123], a[228:231], a[208:211], v[120:123]\n"
        "v_exp_f32 v104, v104\n"
        "v_mfma_f32_16x16x32_bf16 v[120:123], a[232:235], a[212:215], v[120:123]\n"
        "buffer_load_dwordx4 v244, s[8:11], s35 offen lds\n"
        "v_mfma_f32_16x16x32_bf16 v[96:99], v[192:195], v[196:199], v[40:43]\n"
        "v_exp_f32 v105, v105\n"
        "v_mfma_f32_16x16x32_bf16 v[96:99], v[208:211], v[200:203], v[96:99]\n"
        "s_waitcnt lgkmcnt(8)\n"
        "v_exp_f32 v106, v106\n"
        "v_mfma_f32_16x16x32_bf16 v[112:115], a[72:75], a[236:239], 0\n"
        "s_waitcnt lgkmcnt(7)\n"
        "v_cvt_pk_bf16_f32 v68, v104, v105\n"
        "v_add_u32 v245, s45, v245\n"
        "v_mfma_f32_16x16x32_bf16 v[112:115], a[76:79], a[240:243], v[112:115]\n"
        "s_waitcnt lgkmcnt(6)\n"
        "v_exp_f32 v107, v107\n"
        "v_mfma_f32_16x16x32_bf16 v[112:115], a[80:83], a[244:247], v[112:115]\n"
        "s_waitcnt lgkmcnt(3)\n"
        "v_cvt_pk_bf16_f32 v69, v106, v107\n"
        "v_cvt_pk_bf16_f32 v148, v120, v121\n"
        "v_mfma_f32_16x16x32_bf16 v[112:115], a[84:87], a[248:251], v[112:115]\n"
        "s_waitcnt lgkmcnt(2)\n"
        "v_exp_f32 v108, v108\n"
        "v_mfma_f32_16x16x32_bf16 v[112:115], a[88:91], a[216:219], v[112:115]\n"
        "s_waitcnt lgkmcnt(1)\n"
        "v_cvt_pk_bf16_f32 v149, v122, v123\n"
        "v_add_u32 v246, s45, v246\n"
        "v_mfma_f32_16x16x32_bf16 v[112:115], a[92:95], a[220:223], v[112:115]\n"
        "v_exp_f32 v109, v109\n"
        "v_mfma_f32_16x16x32_bf16 v[116:119], a[116:119], a[220:223], 0\n"
        "v_exp_f32 v110, v110\n"
        "v_mfma_f32_16x16x32_bf16 v[116:119], a[112:115], a[216:219], v[116:119]\n"
        "v_cvt_pk_bf16_f32 v76, v108, v109\n"
        "v_add_u32 v240, s31, v240\n"
        "v_mfma_f32_16x16x32_bf16 v[116:119], a[108:111], a[248:251], v[116:119]\n"
        "v_exp_f32 v111, v111\n"
        "v_mfma_f32_16x16x32_bf16 v[116:119], a[104:107], a[244:247], v[116:119]\n"
        "v_cvt_pk_bf16_f32 v212, v112, v113\n"
        "v_cvt_pk_bf16_f32 v213, v114, v115\n"
        "v_mfma_f32_16x16x32_bf16 v[116:119], a[100:103], a[240:243], v[116:119]\n"
        "v_cvt_pk_bf16_f32 v77, v110, v111\n"
        "v_add_u32 v241, s31, v241\n"
        "v_mfma_f32_16x16x32_bf16 v[116:119], a[96:99], a[236:239], v[116:119]\n"
        "v_exp_f32 v96, v96\n"
        "v_mfma_f32_16x16x32_bf16 v[120:123], a[120:123], a[236:239], 0\n"
        "v_exp_f32 v97, v97\n"
        "v_mfma_f32_16x16x32_bf16 v[120:123], a[124:127], a[240:243], v[120:123]\n"
        "s_add_u32 s47, s47, 6291456\n"
        "v_exp_f32 v98, v98\n"
        "v_mfma_f32_16x16x32_bf16 v[120:123], a[128:131], a[244:247], v[120:123]\n"
        "s_add_u32 s36, s36, 2097152\n"
        "v_cvt_pk_bf16_f32 v214, v116, v117\n"
        "v_cvt_pk_bf16_f32 v215, v118, v119\n"
        "v_mfma_f32_16x16x32_bf16 v[120:123], a[132:135], a[248:251], v[120:123]\n"
        "s_xor_b32 s48, s48, 8192\n"
        "v_cvt_pk_bf16_f32 v84, v96, v97\n"
        "v_mfma_f32_16x16x32_bf16 v[120:123], a[136:139], a[216:219], v[120:123]\n"
        "v_exp_f32 v99, v99\n"
        "v_mfma_f32_16x16x32_bf16 v[120:123], a[140:143], a[220:223], v[120:123]\n"
        "v_cvt_pk_bf16_f32 v85, v98, v99\n"
        "s_nop 0\n"
        "v_mfma_f32_16x16x32_bf16 v[112:115], a[220:223], a[164:167], 0\n"
        "s_nop 2\n"
        "v_mfma_f32_16x16x32_bf16 v[112:115], a[216:219], a[160:163], v[112:115]\n"
        "s_nop 0\n"
        "v_cvt_pk_bf16_f32 v230, v120, v121\n"
        "v_cvt_pk_bf16_f32 v231, v122, v123\n"
        "v_mfma_f32_16x16x32_bf16 v[112:115], a[248:251], a[156:159], v[112:115]\n"
        "s_waitcnt lgkmcnt(0)\n"
        "s_barrier\n"
        "v_mfma_f32_16x16x32_bf16 v[112:115], a[244:247], a[152:155], v[112:115]\n"
        "v_mfma_f32_16x16x32_bf16 v[112:115], a[240:243], a[148:151], v[112:115]\n"
        "v_mfma_f32_16x16x32_bf16 v[112:115], a[236:239], a[144:147], v[112:115]\n"
        "s_nop 2\n"
        "v_mfma_f32_16x16x32_bf16 v[100:103], v[212:215], v[156:159], v[12:15]\n"
        "s_nop 2\n"
        "v_mfma_f32_16x16x32_bf16 v[100:103], v[228:231], v[164:167], v[100:103]\n"
        "v_cvt_pk_bf16_f32 v134, v112, v113\n"
        "v_cvt_pk_bf16_f32 v135, v114, v115\n"
        "v_mfma_f32_16x16x32_bf16 v[116:119], a[236:239], a[168:171], 0\n"
        "v_mfma_f32_16x16x32_bf16 v[116:119], a[240:243], a[172:175], v[116:119]\n"
        "v_mfma_f32_16x16x32_bf16 v[116:119], a[244:247], a[176:179], v[116:119]\n"
        "v_mfma_f32_16x16x32_bf16 v[116:119], a[248:251], a[180:183], v[116:119]\n"
        "v_mfma_f32_16x16x32_bf16 v[116:119], a[216:219], a[184:187], v[116:119]\n"
        "s_nop 0\n"
        "v_exp_f32 v100, v100\n"
        "v_mfma_f32_16x16x32_bf16 v[116:119], a[220:223], a[188:191], v[116:119]\n"
        "v_exp_f32 v101, v101\n"
        "v_mfma_f32_16x16x32_bf16 v[104:107], v[212:215], v[176:179], v[28:31]\n"
        "v_exp_f32 v102, v102\n"
        "v_mfma_f32_16x16x32_bf16 v[104:107], v[228:231], v[184:187], v[104:107]\n"
        "v_exp_f32 v103, v103\n"
        "v_mfma_f32_16x16x32_bf16 v[120:123], a[220:223], a[212:215], 0\n"
        "v_cvt_pk_bf16_f32 v70, v100, v101\n"
        "v_cvt_pk_bf16_f32 v71, v102, v103\n"
        "v_mfma_f32_16x16x32_bf16 v[120:123], a[216:219], a[208:211], v[120:123]\n"
        "v_cvt_pk_bf16_f32 v142, v116, v117\n"
        "v_cvt_pk_bf16_f32 v143, v118, v119\n"
        "v_mfma_f32_16x16x32_bf16 v[120:123], a[248:251], a[204:207], v[120:123]\n"
        "v_mov_b64 v[96:97], 0\n"
        "v_mfma_f32_16x16x32_bf16 v[120:123], a[244:247], a[200:203], v[120:123]\n"
        "v_exp_f32 v104, v104\n"
        "v_mfma_f32_16x16x32_bf16 v[120:123], a[240:243], a[196:199], v[120:123]\n"
        "v_exp_f32 v105, v105\n"
        "v_mfma_f32_16x16x32_bf16 v[120:123], a[236:239], a[192:195], v[120:123]\n"
        "v_exp_f32 v106, v106\n"
        "v_mfma_f32_16x16x32_bf16 v[108:111], v[212:215], v[196:199], v[44:47]\n"
        "v_exp_f32 v107, v107\n"
        "v_mfma_f32_16x16x32_bf16 v[108:111], v[228:231], v[204:207], v[108:111]\n"
        "v_cvt_pk_bf16_f32 v78, v104, v105\n"
        "v_cvt_pk_bf16_f32 v79, v106, v107\n"
        "v_mfma_f32_16x16x32_bf16 v[112:115], a[0:3], a[236:239], 0\n"
        "s_nop 0\n"
        "v_cvt_pk_bf16_f32 v150, v120, v121\n"
        "v_cvt_pk_bf16_f32 v151, v122, v123\n"
        "v_mfma_f32_16x16x32_bf16 v[112:115], a[4:7], a[240:243], v[112:115]\n"
        "v_mfma_f32_16x16x32_bf16 v[112:115], a[8:11], a[244:247], v[112:115]\n"
        "v_exp_f32 v108, v108\n"
        "v_mfma_f32_16x16x32_bf16 v[112:115], a[12:15], a[248:251], v[112:115]\n"
        "v_exp_f32 v109, v109\n"
        "v_mfma_f32_16x16x32_bf16 v[112:115], a[16:19], a[216:219], v[112:115]\n"
        "v_exp_f32 v110, v110\n"
        "v_mfma_f32_16x16x32_bf16 v[112:115], a[20:23], a[220:223], v[112:115]\n"
        "v_exp_f32 v111, v111\n"
        "v_smfmac_f32_16x16x64_bf16 v[96:99], a[252:255], v[64:71], v237\n"
        "v_cvt_pk_bf16_f32 v86, v108, v109\n"
        "v_mfma_f32_16x16x32_bf16 v[100:103], v[132:135], v[68:71], 0\n"
        "v_mov_b64 v[124:125], 0\n"
        "v_cvt_pk_bf16_f32 v87, v110, v111\n"
        "v_mfma_f32_16x16x32_bf16 v[100:103], v[128:131], v[64:67], v[100:103]\n"
        "s_nop 0\n"
        "v_cvt_pk_bf16_f32 v216, v112, v113\n"
        "v_cvt_pk_bf16_f32 v217, v114, v115\n"
        "v_mfma_f32_16x16x32_bf16 v[104:107], v[136:139], v[64:67], 0\n"
        "ds_read_b128 v[222:225], v251 offset:4608\n"
        "v_add_f32 v97, v96, v97\n"
        "v_mfma_f32_16x16x32_bf16 v[104:107], v[140:143], v[68:71], v[104:107]\n"
        "v_rcp_f32 v96, v97\n"
        "v_mfma_f32_16x16x32_bf16 v[108:111], v[148:151], v[68:71], 0\n"
        "v_mul_f32 v100, v96, v100\n"
        "v_mul_f32 v101, v96, v101\n"
        "v_mfma_f32_16x16x32_bf16 v[108:111], v[144:147], v[64:67], v[108:111]\n"
        "v_mul_f32 v102, v96, v102\n"
        "v_mul_f32 v103, v96, v103\n"
        "v_mfma_f32_16x16x32_bf16 v[116:119], a[44:47], a[220:223], 0\n"
        "v_cvt_pk_bf16_f32 v100, v100, v101\n"
        "v_cvt_pk_bf16_f32 v101, v102, v103\n"
        "v_mfma_f32_16x16x32_bf16 v[116:119], a[40:43], a[216:219], v[116:119]\n"
        "v_mul_f32 v104, v96, v104\n"
        "v_mul_f32 v105, v96, v105\n"
        "v_mfma_f32_16x16x32_bf16 v[116:119], a[36:39], a[248:251], v[116:119]\n"
        "v_mul_f32 v106, v96, v106\n"
        "v_mul_f32 v107, v96, v107\n"
        "v_mfma_f32_16x16x32_bf16 v[116:119], a[32:35], a[244:247], v[116:119]\n"
        "v_cvt_pk_bf16_f32 v102, v104, v105\n"
        "v_cvt_pk_bf16_f32 v103, v106, v107\n"
        "v_mfma_f32_16x16x32_bf16 v[116:119], a[28:31], a[240:243], v[116:119]\n"
        "ds_write_b128 v245, v[100:103] offset:0\n"
        "v_mfma_f32_16x16x32_bf16 v[116:119], a[24:27], a[236:239], v[116:119]\n"
        "v_mul_f32 v108, v96, v108\n"
        "v_mul_f32 v109, v96, v109\n"
        "v_smfmac_f32_16x16x64_bf16 v[124:127], a[252:255], v[72:79], v237\n"
        "v_mul_f32 v110, v96, v110\n"
        "v_mul_f32 v111, v96, v111\n"
        "v_mfma_f32_16x16x32_bf16 v[64:67], v[132:135], v[76:79], 0\n"
        "v_cvt_pk_bf16_f32 v108, v108, v109\n"
        "v_cvt_pk_bf16_f32 v109, v110, v111\n"
        "v_mfma_f32_16x16x32_bf16 v[64:67], v[128:131], v[72:75], v[64:67]\n"
        "ds_read_b128 v[156:159], v247 offset:0\n"
        "v_cvt_pk_bf16_f32 v218, v116, v117\n"
        "v_mfma_f32_16x16x32_bf16 v[68:71], v[136:139], v[72:75], 0\n"
        "ds_write_b64 v246, v[108:109] offset:0\n"
        "v_mfma_f32_16x16x32_bf16 v[68:71], v[140:143], v[76:79], v[68:71]\n"
        "ds_read_b128 v[160:163], v247 offset:4096\n"
        "v_cvt_pk_bf16_f32 v219, v118, v119\n"
        "v_mfma_f32_16x16x32_bf16 v[120:123], v[148:151], v[76:79], 0\n"
        "ds_read_b128 v[164:167], v247 offset:4352\n"
        "v_max_u32 v236, v96, v97\n"
        "v_mfma_f32_16x16x32_bf16 v[120:123], v[144:147], v[72:75], v[120:123]\n"
        "v_mov_b64 v[116:117], 0\n"
        "v_add_f32 v125, v124, v125\n"
        "v_mfma_f32_16x16x32_bf16 v[112:115], a[48:51], a[236:239], 0\n"
        "v_rcp_f32 v124, v125\n"
        "v_mfma_f32_16x16x32_bf16 v[112:115], a[52:55], a[240:243], v[112:115]\n"
        "v_mul_f32 v64, v124, v64\n"
        "v_mul_f32 v65, v124, v65\n"
        "v_mfma_f32_16x16x32_bf16 v[112:115], a[56:59], a[244:247], v[112:115]\n"
        "v_mul_f32 v66, v124, v66\n"
        "v_mul_f32 v67, v124, v67\n"
        "v_mfma_f32_16x16x32_bf16 v[112:115], a[60:63], a[248:251], v[112:115]\n"
        "v_cvt_pk_bf16_f32 v64, v64, v65\n"
        "v_cvt_pk_bf16_f32 v65, v66, v67\n"
        "v_mfma_f32_16x16x32_bf16 v[112:115], a[64:67], a[216:219], v[112:115]\n"
        "v_mul_f32 v68, v124, v68\n"
        "v_mul_f32 v69, v124, v69\n"
        "v_mfma_f32_16x16x32_bf16 v[112:115], a[68:71], a[220:223], v[112:115]\n"
        "ds_read_b128 a[216:219], v248 offset:0\n"
        "v_mul_f32 v70, v124, v70\n"
        "v_smfmac_f32_16x16x64_bf16 v[116:119], a[252:255], v[80:87], v237\n"
        "ds_read_b128 a[220:223], v248 offset:3072\n"
        "v_mul_f32 v71, v124, v71\n"
        "v_mfma_f32_16x16x32_bf16 v[72:75], v[132:135], v[84:87], 0\n"
        "v_cvt_pk_bf16_f32 v66, v68, v69\n"
        "v_cvt_pk_bf16_f32 v67, v70, v71\n"
        "v_mfma_f32_16x16x32_bf16 v[72:75], v[128:131], v[80:83], v[72:75]\n"
        "ds_write_b128 v245, v[64:67] offset:256\n"
        "v_mfma_f32_16x16x32_bf16 v[76:79], v[136:139], v[80:83], 0\n"
        "ds_read_b128 a[224:227], v254 offset:16\n"
        "v_cvt_pk_bf16_f32 v220, v112, v113\n"
        "v_mfma_f32_16x16x32_bf16 v[76:79], v[140:143], v[84:87], v[76:79]\n"
        "ds_read_b128 a[228:231], v254 offset:3088\n"
        "v_cvt_pk_bf16_f32 v221, v114, v115\n"
        "v_mfma_f32_16x16x32_bf16 v[96:99], v[148:151], v[84:87], 0\n"
        "v_cvt_pk_bf16_f32 v226, v112, v113\n"
        "v_cvt_pk_bf16_f32 v227, v114, v115\n"
        "v_mfma_f32_16x16x32_bf16 v[96:99], v[144:147], v[80:83], v[96:99]\n"
        "v_mul_f32 v120, v124, v120\n"
        "v_mul_f32 v121, v124, v121\n"
        "v_mfma_f32_16x16x32_bf16 v[100:103], v[152:155], v[216:219], v[48:51]\n"
        "s_waitcnt lgkmcnt(10)\n"
        "ds_read_b128 v[152:155], v247 offset:256\n"
        "v_mul_f32 v122, v124, v122\n"
        "v_mfma_f32_16x16x32_bf16 v[100:103], v[168:171], v[220:223], v[100:103]\n"
        "v_mul_f32 v123, v124, v123\n"
        "v_cvt_pk_bf16_f32 v120, v120, v121\n"
        "v_mfma_f32_16x16x32_bf16 v[104:107], v[172:175], v[216:219], v[52:55]\n"
        "v_cvt_pk_bf16_f32 v121, v122, v123\n"
        "v_max3_u32 v236, v236, v124, v125\n"
        "v_mfma_f32_16x16x32_bf16 v[104:107], v[188:191], v[224:227], v[104:107]\n"
        "s_waitcnt lgkmcnt(5)\n"
        "ds_write_b64 v246, v[120:121] offset:256\n"
        "v_mfma_f32_16x16x32_bf16 v[176:179], v[156:159], a[216:219], 0\n"
        "s_waitcnt lgkmcnt(5)\n"
        "ds_read_b128 v[222:225], v252 offset:4608\n"
        "v_add_f32 v117, v116, v117\n"
        "v_mfma_f32_16x16x32_bf16 v[176:179], v[160:163], a[220:223], v[176:179]\n"
        "ds_read_b128 a[232:235], v248 offset:32\n"
        "v_mfma_f32_16x16x32_bf16 v[196:199], v[164:167], a[220:223], 0\n"
        "s_waitcnt lgkmcnt(3)\n"
        "ds_read_b128 a[220:223], v248 offset:3104\n"
        "v_mfma_f32_16x16x32_bf16 v[196:199], v[152:155], a[216:219], v[196:199]\n"
        "v_rcp_f32 v116, v117\n"
        "v_mfma_f32_16x16x32_bf16 v[200:203], v[152:155], a[224:227], 0\n"
        "ds_read_b128 a[216:219], v254 offset:48\n"
        "v_mul_f32 v72, v116, v72\n"
        "v_mfma_f32_16x16x32_bf16 v[200:203], v[164:167], a[228:231], v[200:203]\n"
        "v_exp_f32 v100, v100\n"
        "v_mfma_f32_16x16x32_bf16 v[180:183], v[160:163], a[228:231], 0\n"
        "ds_read_b128 a[228:231], v254 offset:3120\n"
        "v_mul_f32 v73, v116, v73\n"
        "v_mfma_f32_16x16x32_bf16 v[180:183], v[156:159], a[224:227], v[180:183]\n"
        "ds_read_b128 a[236:239], v247 offset:8192\n"
        "v_mul_f32 v74, v116, v74\n"
        "v_mfma_f32_16x16x32_bf16 v[108:111], v[192:195], v[216:219], v[56:59]\n"
        "s_waitcnt lgkmcnt(5)\n"
        "ds_read_b128 a[224:227], v248 offset:64\n"
        "v_mul_f32 v75, v116, v75\n"
        "v_mfma_f32_16x16x32_bf16 v[108:111], v[208:211], v[220:223], v[108:111]\n"
        "ds_read_b128 a[240:243], v247 offset:12288\n"
        "v_cvt_pk_bf16_f32 v72, v72, v73\n"
        "v_mfma_f32_16x16x32_bf16 v[112:115], v[212:215], v[216:219], v[60:63]\n"
        "ds_read_b128 a[244:247], v247 offset:12544\n"
        "v_cvt_pk_bf16_f32 v73, v74, v75\n"
        "v_mfma_f32_16x16x32_bf16 v[112:115], v[228:231], v[224:227], v[112:115]\n"
        "s_waitcnt lgkmcnt(7)\n"
        "ds_read_b128 a[248:251], v247 offset:8448\n"
        "v_mul_f32 v76, v116, v76\n"
        "v_mfma_f32_16x16x32_bf16 v[184:187], v[156:159], a[232:235], 0\n"
        "s_waitcnt lgkmcnt(7)\n"
        "v_exp_f32 v101, v101\n"
        "v_mfma_f32_16x16x32_bf16 v[184:187], v[160:163], a[220:223], v[184:187]\n"
        "v_mul_f32 v77, v116, v77\n"
        "v_mul_f32 v78, v116, v78\n"
        "v_mfma_f32_16x16x32_bf16 v[204:207], v[164:167], a[220:223], 0\n"
        ".long 0xbec200ff\n"
        "44:\n"
        "s_branch 61b\n"
        "ds_read_b128 a[220:223], v248 offset:3136\n"
        "v_mul_f32 v79, v116, v79\n"
        "v_mfma_f32_16x16x32_bf16 v[204:207], v[152:155], a[232:235], v[204:207]\n"
        "s_waitcnt lgkmcnt(7)\n"
        "ds_read_b128 a[232:235], v254 offset:80\n"
        "v_cvt_pk_bf16_f32 v88, v100, v101\n"
        "v_mfma_f32_16x16x32_bf16 v[192:195], v[152:155], a[216:219], 0\n"
        "s_waitcnt lgkmcnt(7)\n"
        "v_exp_f32 v102, v102\n"
        "v_mfma_f32_16x16x32_bf16 v[192:195], v[164:167], a[228:231], v[192:195]\n"
        "v_exp_f32 v104, v104\n"
        "v_mfma_f32_16x16x32_bf16 v[172:175], v[160:163], a[228:231], 0\n"
        "ds_read_b128 a[228:231], v254 offset:3152\n"
        "v_cvt_pk_bf16_f32 v74, v76, v77\n"
        "v_mfma_f32_16x16x32_bf16 v[172:175], v[156:159], a[216:219], v[172:175]\n"
        "s_waitcnt lgkmcnt(6)\n"
        "ds_read_b128 a[216:219], v248 offset:6144\n"
        "v_cvt_pk_bf16_f32 v75, v78, v79\n"
        "v_mfma_f32_16x16x32_bf16 v[212:215], v[156:159], a[224:227], 0\n"
        "s_waitcnt lgkmcnt(3)\n"
        "ds_write_b128 v245, v[72:75] offset:512\n"
        "v_mfma_f32_16x16x32_bf16 v[212:215], v[160:163], a[220:223], v[212:215]\n"
        "v_exp_f32 v103, v103\n"
        "v_mfma_f32_16x16x32_bf16 v[216:219], v[164:167], a[220:223], 0\n"
        "ds_read_b128 a[220:223], v248 offset:9216\n"
        "v_cvt_pk_bf16_f32 v89, v102, v103\n"
        "v_mfma_f32_16x16x32_bf16 v[216:219], v[152:155], a[224:227], v[216:219]\n"
        "s_waitcnt lgkmcnt(4)\n"
        "ds_read_b128 a[224:227], v254 offset:6160\n"
        "v_mul_f32 v96, v116, v96\n"
        "v_mfma_f32_16x16x32_bf16 v[224:227], v[152:155], a[232:235], 0\n"
        "s_waitcnt lgkmcnt(4)\n"
        "v_exp_f32 v105, v105\n"
        "v_mfma_f32_16x16x32_bf16 v[224:227], v[164:167], a[228:231], v[224:227]\n"
        "v_exp_f32 v106, v106\n"
        "v_mfma_f32_16x16x32_bf16 v[220:223], v[160:163], a[228:231], 0\n"
        "ds_read_b128 a[228:231], v254 offset:9232\n"
        "v_cvt_pk_bf16_f32 v90, v104, v105\n"
        "v_mfma_f32_16x16x32_bf16 v[220:223], v[156:159], a[232:235], v[220:223]\n"
        "s_waitcnt lgkmcnt(4)\n"
        "ds_read_b128 a[232:235], v248 offset:6176\n"
        "v_mul_f32 v97, v116, v97\n"
        "v_mfma_f32_16x16x32_bf16 v[176:179], a[236:239], a[216:219], v[176:179]\n"
        "s_waitcnt lgkmcnt(3)\n"
        "v_mul_f32 v98, v116, v98\n"
        "v_mul_f32 v99, v116, v99\n"
        "v_mfma_f32_16x16x32_bf16 v[176:179], a[240:243], a[220:223], v[176:179]\n"
        "v_exp_f32 v107, v107\n"
        "v_mfma_f32_16x16x32_bf16 v[196:199], a[244:247], a[220:223], v[196:199]\n"
        "ds_read_b128 a[220:223], v248 offset:9248\n"
        "v_cvt_pk_bf16_f32 v91, v106, v107\n"
        "v_mfma_f32_16x16x32_bf16 v[196:199], a[248:251], a[216:219], v[196:199]\n"
        "s_waitcnt lgkmcnt(3)\n"
        "ds_read_b128 a[216:219], v254 offset:6192\n"
        "v_cvt_pk_bf16_f32 v96, v96, v97\n"
        "v_mfma_f32_16x16x32_bf16 v[200:203], a[248:251], a[224:227], v[200:203]\n"
        "s_waitcnt lgkmcnt(3)\n"
        "ds_read_b128 v[156:159], v247 offset:16384\n"
        "v_cvt_pk_bf16_f32 v97, v98, v99\n"
        "v_mfma_f32_16x16x32_bf16 v[200:203], a[244:247], a[228:231], v[200:203]\n"
        "ds_write_b64 v246, v[96:97] offset:512\n"
        "v_mfma_f32_16x16x32_bf16 v[180:183], a[240:243], a[228:231], v[180:183]\n"
        "ds_read_b128 a[228:231], v254 offset:9264\n"
        "v_max3_u32 v236, v236, v116, v117\n"
        "v_mfma_f32_16x16x32_bf16 v[180:183], a[236:239], a[224:227], v[180:183]\n"
        "s_waitcnt lgkmcnt(5)\n"
        "ds_read_b128 a[224:227], v248 offset:6208\n"
        "v_mfma_f32_16x16x32_bf16 v[184:187], a[236:239], a[232:235], v[184:187]\n"
        "s_waitcnt lgkmcnt(5)\n"
        "ds_read_b128 v[160:163], v247 offset:20480\n"
        "v_mfma_f32_16x16x32_bf16 v[184:187], a[240:243], a[220:223], v[184:187]\n"
        "ds_read_b128 v[164:167], v247 offset:20736\n"
        "v_mfma_f32_16x16x32_bf16 v[204:207], a[244:247], a[220:223], v[204:207]\n"
        "ds_read_b128 a[220:223], v248 offset:9280\n"
        "v_mfma_f32_16x16x32_bf16 v[204:207], a[248:251], a[232:235], v[204:207]\n"
        "s_waitcnt lgkmcnt(7)\n"
        "ds_read_b128 a[232:235], v254 offset:6224\n"
        "v_mfma_f32_16x16x32_bf16 v[192:195], a[248:251], a[216:219], v[192:195]\n"
        "s_waitcnt lgkmcnt(5)\n"
        "ds_read_b128 v[152:155], v247 offset:16640\n"
        "v_mfma_f32_16x16x32_bf16 v[192:195], a[244:247], a[228:231], v[192:195]\n"
        "v_exp_f32 v108, v108\n"
        "v_mfma_f32_16x16x32_bf16 v[172:175], a[240:243], a[228:231], v[172:175]\n"
        "ds_read_b128 a[228:231], v254 offset:9296\n"
        "v_mfma_f32_16x16x32_bf16 v[172:175], a[236:239], a[216:219], v[172:175]\n"
        "s_waitcnt lgkmcnt(6)\n"
        "ds_read_b128 a[216:219], v248 offset:12288\n"
        "v_mfma_f32_16x16x32_bf16 v[212:215], a[236:239], a[224:227], v[212:215]\n"
        "s_waitcnt lgkmcnt(4)\n"
        "v_exp_f32 v109, v109\n"
        "v_mfma_f32_16x16x32_bf16 v[212:215], a[240:243], a[220:223], v[212:215]\n"
        "v_exp_f32 v110, v110\n"
        "v_mfma_f32_16x16x32_bf16 v[216:219], a[244:247], a[220:223], v[216:219]\n"
        "ds_read_b128 a[220:223], v248 offset:15360\n"
        "v_cvt_pk_bf16_f32 v92, v108, v109\n"
        "v_mfma_f32_16x16x32_bf16 v[216:219], a[248:251], a[224:227], v[216:219]\n"
        "s_waitcnt lgkmcnt(4)\n"
        "ds_read_b128 a[224:227], v254 offset:12304\n"
        "v_mfma_f32_16x16x32_bf16 v[224:227], a[248:251], a[232:235], v[224:227]\n"
        "s_waitcnt lgkmcnt(3)\n"
        "v_exp_f32 v112, v112\n"
        "v_mfma_f32_16x16x32_bf16 v[224:227], a[244:247], a[228:231], v[224:227]\n"
        "v_exp_f32 v111, v111\n"
        "v_mfma_f32_16x16x32_bf16 v[220:223], a[240:243], a[228:231], v[220:223]\n"
        "ds_read_b128 a[228:231], v254 offset:15376\n"
        "v_cvt_pk_bf16_f32 v93, v110, v111\n"
        "v_mfma_f32_16x16x32_bf16 v[220:223], a[236:239], a[232:235], v[220:223]\n"
        "s_waitcnt lgkmcnt(3)\n"
        "ds_read_b128 a[232:235], v248 offset:12320\n"
        "v_mfma_f32_16x16x32_bf16 v[176:179], v[156:159], a[216:219], v[176:179]\n"
        "s_waitcnt lgkmcnt(3)\n"
        "v_exp_f32 v113, v113\n"
        "v_mfma_f32_16x16x32_bf16 v[176:179], v[160:163], a[220:223], v[176:179]\n"
        "v_exp_f32 v114, v114\n"
        "v_mfma_f32_16x16x32_bf16 v[196:199], v[164:167], a[220:223], v[196:199]\n"
        "ds_read_b128 a[220:223], v248 offset:15392\n"
        "v_cvt_pk_bf16_f32 v94, v112, v113\n"
        "v_mfma_f32_16x16x32_bf16 v[196:199], v[152:155], a[216:219], v[196:199]\n"
        "s_waitcnt lgkmcnt(3)\n"
        "ds_read_b128 a[216:219], v254 offset:12336\n"
        "v_mfma_f32_16x16x32_bf16 v[200:203], v[152:155], a[224:227], v[200:203]\n"
        "s_waitcnt lgkmcnt(3)\n"
        "v_exp_f32 v115, v115\n"
        "v_mfma_f32_16x16x32_bf16 v[200:203], v[164:167], a[228:231], v[200:203]\n"
        "v_cvt_pk_bf16_f32 v95, v114, v115\n"
        "v_mfma_f32_16x16x32_bf16 v[180:183], v[160:163], a[228:231], v[180:183]\n"
        "ds_read_b128 a[228:231], v254 offset:15408\n"
        "v_mfma_f32_16x16x32_bf16 v[180:183], v[156:159], a[224:227], v[180:183]\n"
        "s_waitcnt lgkmcnt(3)\n"
        "ds_read_b128 a[224:227], v248 offset:12352\n"
        "v_mfma_f32_16x16x32_bf16 v[184:187], v[156:159], a[232:235], v[184:187]\n"
        "s_waitcnt lgkmcnt(3)\n"
        "s_waitcnt vmcnt(0)\n"
        "s_barrier\n"
        "v_mfma_f32_16x16x32_bf16 v[184:187], v[160:163], a[220:223], v[184:187]\n"
        "v_mfma_f32_16x16x32_bf16 v[204:207], v[164:167], a[220:223], v[204:207]\n"
        "ds_read_b128 a[220:223], v248 offset:15424\n"
        "v_mfma_f32_16x16x32_bf16 v[204:207], v[152:155], a[232:235], v[204:207]\n"
        "s_waitcnt lgkmcnt(3)\n"
        "ds_read_b128 a[232:235], v254 offset:12368\n"
        "v_mfma_f32_16x16x32_bf16 v[192:195], v[152:155], a[216:219], v[192:195]\n"
        "s_waitcnt lgkmcnt(3)\n"
        "ds_read_b128 a[236:239], v240 offset:0\n"
        "v_mfma_f32_16x16x32_bf16 v[192:195], v[164:167], a[228:231], v[192:195]\n"
        "ds_read_b128 a[240:243], v241 offset:0\n"
        "v_mfma_f32_16x16x32_bf16 v[172:175], v[160:163], a[228:231], v[172:175]\n"
        "ds_read_b128 a[228:231], v254 offset:15440\n"
        "v_mfma_f32_16x16x32_bf16 v[172:175], v[156:159], a[216:219], v[172:175]\n"
        "s_waitcnt lgkmcnt(5)\n"
        "ds_read_b128 a[244:247], v240 offset:128\n"
        "v_mfma_f32_16x16x32_bf16 v[212:215], v[156:159], a[224:227], v[212:215]\n"
        "s_waitcnt lgkmcnt(5)\n"
        "ds_read_b128 a[248:251], v241 offset:128\n"
        "v_mfma_f32_16x16x32_bf16 v[212:215], v[160:163], a[220:223], v[212:215]\n"
        "ds_read_b128 a[216:219], v240 offset:256\n"
        "v_mfma_f32_16x16x32_bf16 v[216:219], v[164:167], a[220:223], v[216:219]\n"
        "ds_read_b128 a[220:223], v241 offset:256\n"
        "v_mfma_f32_16x16x32_bf16 v[216:219], v[152:155], a[224:227], v[216:219]\n"
        "s_waitcnt lgkmcnt(7)\n"
        "v_mfma_f32_16x16x32_bf16 v[224:227], v[152:155], a[232:235], v[224:227]\n"
        "s_waitcnt lgkmcnt(4)\n"
        "v_mfma_f32_16x16x32_bf16 v[224:227], v[164:167], a[228:231], v[224:227]\n"
        "v_mfma_f32_16x16x32_bf16 v[220:223], v[160:163], a[228:231], v[220:223]\n"
        "v_mfma_f32_16x16x32_bf16 v[220:223], v[156:159], a[232:235], v[220:223]\n"
        "s_waitcnt lgkmcnt(0)\n"
        ".p2align 6\n"
        "s_nop 0\n"
        "s_nop 0\n"
        "s_nop 0\n"
        "s_nop 0\n"
        "10:\n"
        "v_mfma_f32_16x16x32_bf16 v[112:115], a[72:75], a[236:239], 0\n"
        "v_mov_b64 v[124:125], 0\n"
        "v_mfma_f32_16x16x32_bf16 v[112:115], a[76:79], a[240:243], v[112:115]\n"
        "s_waitcnt lgkmcnt(3)\n"
        "s_add_u32 s41, s30, s40\n"
        "v_cvt_pk_bf16_f32 v232, v176, v180\n"
        "v_cvt_pk_bf16_f32 v233, v184, v172\n"
        "v_mfma_f32_16x16x32_bf16 v[112:115], a[80:83], a[244:247], v[112:115]\n"
        "s_waitcnt lgkmcnt(2)\n"
        "v_cvt_pk_bf16_f32 v234, v212, v220\n"
        "v_mfma_f32_16x16x32_bf16 v[112:115], a[84:87], a[248:251], v[112:115]\n"
        "s_waitcnt lgkmcnt(1)\n"
        "buffer_store_dwordx3 v[232:234], v250, s[16:19], s38 offen offset:0 sc1\n"
        "v_mfma_f32_16x16x32_bf16 v[112:115], a[88:91], a[216:219], v[112:115]\n"
        "s_waitcnt lgkmcnt(0)\n"
        "s_nop 0\n"
        "v_mfma_f32_16x16x32_bf16 v[112:115], a[92:95], a[220:223], v[112:115]\n"
        "v_cvt_pk_bf16_f32 v232, v177, v181\n"
        "v_cvt_pk_bf16_f32 v233, v185, v173\n"
        "v_smfmac_f32_16x16x64_bf16 v[124:127], a[252:255], v[88:95], v237\n"
        "v_cvt_pk_bf16_f32 v234, v213, v221\n"
        "v_mfma_f32_16x16x32_bf16 v[80:83], v[132:135], v[92:95], 0\n"
        "s_nop 2\n"
        "v_mfma_f32_16x16x32_bf16 v[80:83], v[128:131], v[88:91], v[80:83]\n"
        "v_cvt_pk_bf16_f32 v152, v112, v113\n"
        "v_cvt_pk_bf16_f32 v153, v114, v115\n"
        "v_mfma_f32_16x16x32_bf16 v[84:87], v[136:139], v[88:91], 0\n"
        "v_add_f32 v125, v124, v125\n"
        "v_mfma_f32_16x16x32_bf16 v[84:87], v[140:143], v[92:95], v[84:87]\n"
        "v_rcp_f32 v124, v125\n"
        "v_mfma_f32_16x16x32_bf16 v[96:99], v[148:151], v[92:95], 0\n"
        "buffer_store_dwordx3 v[232:234], v250, s[16:19], s38 offen offset:384 sc1\n"
        "v_mfma_f32_16x16x32_bf16 v[96:99], v[144:147], v[88:91], v[96:99]\n"
        "s_add_u32 s35, s47, 786432\n"
        "v_mul_f32 v80, v124, v80\n"
        "v_mul_f32 v81, v124, v81\n"
        "v_mfma_f32_16x16x32_bf16 v[116:119], a[116:119], a[220:223], 0\n"
        "s_sub_u32 s46, s35, 100663296\n"
        "v_mul_f32 v82, v124, v82\n"
        "v_mul_f32 v83, v124, v83\n"
        "v_mfma_f32_16x16x32_bf16 v[116:119], a[112:115], a[216:219], v[116:119]\n"
        "s_cmp_lt_u32 s46, 786432\n"
        "v_cvt_pk_bf16_f32 v80, v80, v81\n"
        "v_cvt_pk_bf16_f32 v81, v82, v83\n"
        "v_mfma_f32_16x16x32_bf16 v[116:119], a[108:111], a[248:251], v[116:119]\n"
        "s_cselect_b32 s35, s46, s35\n"
        "v_mul_f32 v84, v124, v84\n"
        "v_mul_f32 v85, v124, v85\n"
        "v_mfma_f32_16x16x32_bf16 v[116:119], a[104:107], a[244:247], v[116:119]\n"
        "v_mul_f32 v86, v124, v86\n"
        "v_mul_f32 v87, v124, v87\n"
        "v_mfma_f32_16x16x32_bf16 v[116:119], a[100:103], a[240:243], v[116:119]\n"
        "v_cvt_pk_bf16_f32 v232, v178, v182\n"
        "v_cvt_pk_bf16_f32 v233, v186, v174\n"
        "v_mfma_f32_16x16x32_bf16 v[116:119], a[96:99], a[236:239], v[116:119]\n"
        "v_cvt_pk_bf16_f32 v234, v214, v222\n"
        "v_cvt_pk_bf16_f32 v82, v84, v85\n"
        "v_mfma_f32_16x16x32_bf16 v[120:123], a[120:123], a[236:239], 0\n"
        "v_cvt_pk_bf16_f32 v83, v86, v87\n"
        "v_mul_f32 v96, v124, v96\n"
        "v_mfma_f32_16x16x32_bf16 v[120:123], a[124:127], a[240:243], v[120:123]\n"
        "buffer_store_dwordx3 v[232:234], v250, s[16:19], s38 offen offset:768 sc1\n"
        "v_mfma_f32_16x16x32_bf16 v[120:123], a[128:131], a[244:247], v[120:123]\n"
        "v_mul_f32 v97, v124, v97\n"
        "v_mul_f32 v98, v124, v98\n"
        "v_mfma_f32_16x16x32_bf16 v[120:123], a[132:135], a[248:251], v[120:123]\n"
        "v_mul_f32 v99, v124, v99\n"
        "v_cvt_pk_bf16_f32 v154, v116, v117\n"
        "v_mfma_f32_16x16x32_bf16 v[120:123], a[136:139], a[216:219], v[120:123]\n"
        "ds_write_b128 v245, v[80:83] offset:768\n"
        "v_mfma_f32_16x16x32_bf16 v[120:123], a[140:143], a[220:223], v[120:123]\n"
        "v_cvt_pk_bf16_f32 v155, v118, v119\n"
        "v_cvt_pk_bf16_f32 v96, v96, v97\n"
        "v_mfma_f32_16x16x32_bf16 v[112:115], a[20:23], a[220:223], 0\n"
        "v_cvt_pk_bf16_f32 v97, v98, v99\n"
        "v_max3_u32 v236, v236, v124, v125\n"
        "v_mfma_f32_16x16x32_bf16 v[112:115], a[16:19], a[216:219], v[112:115]\n"
        "v_cmp_lt_u32 vcc, s24, v236\n"
        "v_cvt_pk_bf16_f32 v232, v179, v183\n"
        "v_mfma_f32_16x16x32_bf16 v[112:115], a[12:15], a[248:251], v[112:115]\n"
        "ds_write_b64 v246, v[96:97] offset:768\n"
        "s_cmp_lg_u64 vcc, 0\n"
        "s_addc_u32 s25, s25, s25\n"
        "v_mfma_f32_16x16x32_bf16 v[112:115], a[8:11], a[244:247], v[112:115]\n"
        "v_cvt_pk_bf16_f32 v233, v187, v175\n"
        "v_cvt_pk_bf16_f32 v234, v215, v223\n"
        "v_mfma_f32_16x16x32_bf16 v[112:115], a[4:7], a[240:243], v[112:115]\n"
        "v_cvt_pk_bf16_f32 v168, v120, v121\n"
        "v_cvt_pk_bf16_f32 v169, v122, v123\n"
        "v_mfma_f32_16x16x32_bf16 v[112:115], a[0:3], a[236:239], v[112:115]\n"
        "buffer_store_dwordx3 v[232:234], v250, s[16:19], s38 offen offset:1152 sc1\n"
        "v_mfma_f32_16x16x32_bf16 v[116:119], a[24:27], a[236:239], 0\n"
        "v_xor_b32 v251, 0x2000, v251\n"
        "v_xor_b32 v252, 0x2000, v252\n"
        "v_mfma_f32_16x16x32_bf16 v[116:119], a[28:31], a[240:243], v[116:119]\n"
        "v_cvt_pk_bf16_f32 v232, v196, v200\n"
        "v_cvt_pk_bf16_f32 v233, v204, v192\n"
        "v_mfma_f32_16x16x32_bf16 v[116:119], a[32:35], a[244:247], v[116:119]\n"
        "v_cvt_pk_bf16_f32 v156, v112, v113\n"
        "v_cvt_pk_bf16_f32 v157, v114, v115\n"
        "v_mfma_f32_16x16x32_bf16 v[116:119], a[36:39], a[248:251], v[116:119]\n"
        "v_cvt_pk_bf16_f32 v234, v216, v224\n"
        "v_mfma_f32_16x16x32_bf16 v[116:119], a[40:43], a[216:219], v[116:119]\n"
        "v_mfma_f32_16x16x32_bf16 v[116:119], a[44:47], a[220:223], v[116:119]\n"
        "s_nop 2\n"
        "v_mfma_f32_16x16x32_bf16 v[120:123], a[68:71], a[220:223], 0\n"
        "s_nop 2\n"
        "v_mfma_f32_16x16x32_bf16 v[120:123], a[64:67], a[216:219], v[120:123]\n"
        "buffer_store_dwordx3 v[232:234], v250, s[16:19], s39 offen offset:0 sc1\n"
        "v_mfma_f32_16x16x32_bf16 v[120:123], a[60:63], a[248:251], v[120:123]\n"
        "ds_read_b128 a[224:227], v240 offset:1536\n"
        "v_cvt_pk_bf16_f32 v158, v116, v117\n"
        "v_mfma_f32_16x16x32_bf16 v[120:123], a[56:59], a[244:247], v[120:123]\n"
        "ds_read_b128 a[228:231], v241 offset:1536\n"
        "v_cvt_pk_bf16_f32 v159, v118, v119\n"
        "v_mfma_f32_16x16x32_bf16 v[120:123], a[52:55], a[240:243], v[120:123]\n"
        "ds_read_b128 a[232:235], v240 offset:1664\n"
        "v_cvt_pk_bf16_f32 v232, v197, v201\n"
        "v_mfma_f32_16x16x32_bf16 v[120:123], a[48:51], a[236:239], v[120:123]\n"
        "v_cvt_pk_bf16_f32 v233, v205, v193\n"
        "v_cvt_pk_bf16_f32 v234, v217, v225\n"
        "v_mfma_f32_16x16x32_bf16 v[112:115], a[236:239], a[144:147], 0\n"
        "v_add_u32 v247, s45, v247\n"
        "s_nop 0\n"
        "v_mfma_f32_16x16x32_bf16 v[112:115], a[240:243], a[148:151], v[112:115]\n"
        "s_nop 1\n"
        "v_cvt_pk_bf16_f32 v160, v120, v121\n"
        "v_mfma_f32_16x16x32_bf16 v[112:115], a[244:247], a[152:155], v[112:115]\n"
        "v_cvt_pk_bf16_f32 v161, v122, v123\n"
        "v_cvt_pk_bf16_f32 v166, v120, v121\n"
        "v_mfma_f32_16x16x32_bf16 v[112:115], a[248:251], a[156:159], v[112:115]\n"
        "v_cvt_pk_bf16_f32 v167, v122, v123\n"
        "v_mfma_f32_16x16x32_bf16 v[112:115], a[216:219], a[160:163], v[112:115]\n"
        "buffer_store_dwordx3 v[232:234], v250, s[16:19], s39 offen offset:384 sc1\n"
        "v_mfma_f32_16x16x32_bf16 v[112:115], a[220:223], a[164:167], v[112:115]\n"
        "s_nop 2\n"
        "v_mfma_f32_16x16x32_bf16 v[116:119], a[220:223], a[188:191], 0\n"
        "s_nop 2\n"
        "v_mfma_f32_16x16x32_bf16 v[116:119], a[216:219], a[184:187], v[116:119]\n"
        "v_cvt_pk_bf16_f32 v128, v112, v113\n"
        "v_cvt_pk_bf16_f32 v129, v114, v115\n"
        "v_mfma_f32_16x16x32_bf16 v[116:119], a[248:251], a[180:183], v[116:119]\n"
        "v_cvt_pk_bf16_f32 v232, v198, v202\n"
        "v_cvt_pk_bf16_f32 v233, v206, v194\n"
        "v_mfma_f32_16x16x32_bf16 v[116:119], a[244:247], a[176:179], v[116:119]\n"
        "v_cvt_pk_bf16_f32 v234, v218, v226\n"
        "v_mfma_f32_16x16x32_bf16 v[116:119], a[240:243], a[172:175], v[116:119]\n"
        "v_mfma_f32_16x16x32_bf16 v[116:119], a[236:239], a[168:171], v[116:119]\n"
        "s_nop 2\n"
        "v_mfma_f32_16x16x32_bf16 v[120:123], a[236:239], a[192:195], 0\n"
        "ds_read_b128 a[236:239], v241 offset:1664\n"
        "s_nop 0\n"
        "v_mfma_f32_16x16x32_bf16 v[120:123], a[240:243], a[196:199], v[120:123]\n"
        "ds_read_b128 a[240:243], v240 offset:1792\n"
        "v_cvt_pk_bf16_f32 v136, v116, v117\n"
        "v_mfma_f32_16x16x32_bf16 v[120:123], a[244:247], a[200:203], v[120:123]\n"
        "ds_read_b128 a[244:247], v241 offset:1792\n"
        "v_cvt_pk_bf16_f32 v137, v118, v119\n"
        "v_mfma_f32_16x16x32_bf16 v[120:123], a[248:251], a[204:207], v[120:123]\n"
        "buffer_store_dwordx3 v[232:234], v250, s[16:19], s39 offen offset:768 sc1\n"
        "v_mfma_f32_16x16x32_bf16 v[120:123], a[216:219], a[208:211], v[120:123]\n"
        "s_nop 0\n"
        "v_cvt_pk_bf16_f32 v232, v199, v203\n"
        "v_cvt_pk_bf16_f32 v233, v207, v195\n"
        "v_mfma_f32_16x16x32_bf16 v[120:123], a[220:223], a[212:215], v[120:123]\n"
        "s_waitcnt lgkmcnt(5)\n"
        "v_cvt_pk_bf16_f32 v234, v219, v227\n"
        "s_nop 0\n"
        "v_mfma_f32_16x16x32_bf16 v[112:115], a[72:75], a[224:227], 0\n"
        "s_waitcnt lgkmcnt(4)\n"
        "s_nop 1\n"
        "v_mfma_f32_16x16x32_bf16 v[112:115], a[76:79], a[228:231], v[112:115]\n"
        "s_waitcnt lgkmcnt(3)\n"
        "s_nop 1\n"
        "v_mfma_f32_16x16x32_bf16 v[112:115], a[80:83], a[232:235], v[112:115]\n"
        "s_waitcnt lgkmcnt(2)\n"
        "v_cvt_pk_bf16_f32 v144, v120, v121\n"
        "v_cvt_pk_bf16_f32 v145, v122, v123\n"
        "v_mfma_f32_16x16x32_bf16 v[112:115], a[84:87], a[236:239], v[112:115]\n"
        "s_waitcnt lgkmcnt(1)\n"
        "v_mfma_f32_16x16x32_bf16 v[112:115], a[88:91], a[240:243], v[112:115]\n"
        "s_waitcnt lgkmcnt(0)\n"
        "buffer_store_dwordx3 v[232:234], v250, s[16:19], s39 offen offset:1152 sc1\n"
        "v_mfma_f32_16x16x32_bf16 v[112:115], a[92:95], a[244:247], v[112:115]\n"
        "s_nop 2\n"
        "v_mfma_f32_16x16x32_bf16 v[116:119], a[116:119], a[244:247], 0\n"
        "s_nop 2\n"
        "v_mfma_f32_16x16x32_bf16 v[116:119], a[112:115], a[240:243], v[116:119]\n"
        "v_cvt_pk_bf16_f32 v172, v112, v113\n"
        "v_cvt_pk_bf16_f32 v173, v114, v115\n"
        "v_mfma_f32_16x16x32_bf16 v[116:119], a[108:111], a[236:239], v[116:119]\n"
        "v_mfma_f32_16x16x32_bf16 v[116:119], a[104:107], a[232:235], v[116:119]\n"
        "v_mfma_f32_16x16x32_bf16 v[116:119], a[100:103], a[228:231], v[116:119]\n"
        "v_mfma_f32_16x16x32_bf16 v[116:119], a[96:99], a[224:227], v[116:119]\n"
        "s_mov_b32 m0, s48\n"
        "s_nop 1\n"
        "v_mfma_f32_16x16x32_bf16 v[120:123], a[120:123], a[224:227], 0\n"
        "ds_read_b128 v[162:165], v251 offset:0\n"
        "s_nop 0\n"
        "v_mfma_f32_16x16x32_bf16 v[120:123], a[124:127], a[228:231], v[120:123]\n"
        "buffer_load_dwordx4 v249, s[12:15], s36 offen offset:0 lds\n"
        "v_mfma_f32_16x16x32_bf16 v[120:123], a[128:131], a[232:235], v[120:123]\n"
        "v_cvt_pk_bf16_f32 v174, v116, v117\n"
        "v_cvt_pk_bf16_f32 v175, v118, v119\n"
        "v_mfma_f32_16x16x32_bf16 v[120:123], a[132:135], a[236:239], v[120:123]\n"
        "v_mfma_f32_16x16x32_bf16 v[120:123], a[136:139], a[240:243], v[120:123]\n"
        "v_mfma_f32_16x16x32_bf16 v[120:123], a[140:143], a[244:247], v[120:123]\n"
        "s_nop 2\n"
        "v_mfma_f32_16x16x32_bf16 v[112:115], a[20:23], a[244:247], 0\n"
        "s_nop 2\n"
        "v_mfma_f32_16x16x32_bf16 v[112:115], a[16:19], a[240:243], v[112:115]\n"
        "v_cvt_pk_bf16_f32 v190, v120, v121\n"
        "v_cvt_pk_bf16_f32 v191, v122, v123\n"
        "v_mfma_f32_16x16x32_bf16 v[112:115], a[12:15], a[236:239], v[112:115]\n"
        "v_mfma_f32_16x16x32_bf16 v[112:115], a[8:11], a[232:235], v[112:115]\n"
        "v_mfma_f32_16x16x32_bf16 v[112:115], a[4:7], a[228:231], v[112:115]\n"
        "ds_read_b128 v[182:185], v251 offset:512\n"
        "v_mfma_f32_16x16x32_bf16 v[112:115], a[0:3], a[224:227], v[112:115]\n"
        "s_nop 2\n"
        "v_mfma_f32_16x16x32_bf16 v[116:119], a[24:27], a[224:227], 0\n"
        "s_nop 2\n"
        "v_mfma_f32_16x16x32_bf16 v[116:119], a[28:31], a[228:231], v[116:119]\n"
        "v_cvt_pk_bf16_f32 v176, v112, v113\n"
        "v_cvt_pk_bf16_f32 v177, v114, v115\n"
        "v_mfma_f32_16x16x32_bf16 v[116:119], a[32:35], a[232:235], v[116:119]\n"
        "v_mfma_f32_16x16x32_bf16 v[116:119], a[36:39], a[236:239], v[116:119]\n"
        "buffer_load_dwordx4 v253, s[12:15], s36 offen offset:1024 lds\n"
        "v_mfma_f32_16x16x32_bf16 v[116:119], a[40:43], a[240:243], v[116:119]\n"
        "v_mfma_f32_16x16x32_bf16 v[116:119], a[44:47], a[244:247], v[116:119]\n"
        "s_nop 2\n"
        "v_mfma_f32_16x16x32_bf16 v[120:123], a[68:71], a[244:247], 0\n"
        "s_nop 2\n"
        "v_mfma_f32_16x16x32_bf16 v[120:123], a[64:67], a[240:243], v[120:123]\n"
        "v_cvt_pk_bf16_f32 v178, v116, v117\n"
        "v_cvt_pk_bf16_f32 v179, v118, v119\n"
        "v_mfma_f32_16x16x32_bf16 v[120:123], a[60:63], a[236:239], v[120:123]\n"
        "v_mfma_f32_16x16x32_bf16 v[120:123], a[56:59], a[232:235], v[120:123]\n"
        "v_mfma_f32_16x16x32_bf16 v[120:123], a[52:55], a[228:231], v[120:123]\n"
        "v_mfma_f32_16x16x32_bf16 v[120:123], a[48:51], a[224:227], v[120:123]\n"
        "s_nop 2\n"
        "v_mfma_f32_16x16x32_bf16 v[112:115], a[224:227], a[144:147], 0\n"
        "s_nop 2\n"
        "v_mfma_f32_16x16x32_bf16 v[112:115], a[228:231], a[148:151], v[112:115]\n"
        "v_cvt_pk_bf16_f32 v180, v120, v121\n"
        "v_cvt_pk_bf16_f32 v181, v122, v123\n"
        "v_mfma_f32_16x16x32_bf16 v[112:115], a[232:235], a[152:155], v[112:115]\n"
        "v_cvt_pk_bf16_f32 v186, v120, v121\n"
        "v_cvt_pk_bf16_f32 v187, v122, v123\n"
        "v_mfma_f32_16x16x32_bf16 v[112:115], a[236:239], a[156:159], v[112:115]\n"
        "s_add_u32 m0, s41, 0\n"
        "v_mfma_f32_16x16x32_bf16 v[112:115], a[240:243], a[160:163], v[112:115]\n"
        "ds_read_b128 a[248:251], v240 offset:12288\n"
        "v_mfma_f32_16x16x32_bf16 v[112:115], a[244:247], a[164:167], v[112:115]\n"
        "buffer_load_dwordx4 v242, s[8:11], s47 offen lds\n"
        "v_mfma_f32_16x16x32_bf16 v[96:99], v[152:155], v[156:159], v[0:3]\n"
        "s_waitcnt lgkmcnt(2)\n"
        "ds_read_b128 a[216:219], v241 offset:12288\n"
        "s_nop 0\n"
        "v_mfma_f32_16x16x32_bf16 v[96:99], v[168:171], v[160:163], v[96:99]\n"
        "ds_read_b128 a[220:223], v240 offset:12416\n"
        "s_sub_u32 s31, s30, s28\n"
        "s_nop 0\n"
        "v_mfma_f32_16x16x32_bf16 v[100:103], v[172:175], v[156:159], v[4:7]\n"
        "s_sub_u32 s45, s28, s29\n"
        "v_cvt_pk_bf16_f32 v130, v112, v113\n"
        "v_cvt_pk_bf16_f32 v131, v114, v115\n"
        "v_mfma_f32_16x16x32_bf16 v[100:103], v[188:191], v[164:167], v[100:103]\n"
        "s_add_u32 s28, s28, s31\n"
        "ds_read_b128 v[162:165], v252 offset:0\n"
        "v_mfma_f32_16x16x32_bf16 v[116:119], a[244:247], a[188:191], 0\n"
        "s_mov_b32 s30, s29\n"
        "v_exp_f32 v96, v96\n"
        "v_mfma_f32_16x16x32_bf16 v[116:119], a[240:243], a[184:187], v[116:119]\n"
        "s_add_u32 s29, s29, s45\n"
        "v_exp_f32 v97, v97\n"
        "v_mfma_f32_16x16x32_bf16 v[116:119], a[236:239], a[180:183], v[116:119]\n"
        "v_exp_f32 v98, v98\n"
        "v_mfma_f32_16x16x32_bf16 v[116:119], a[232:235], a[176:179], v[116:119]\n"
        "v_exp_f32 v100, v100\n"
        "v_mfma_f32_16x16x32_bf16 v[116:119], a[228:231], a[172:175], v[116:119]\n"
        "v_exp_f32 v99, v99\n"
        "v_mfma_f32_16x16x32_bf16 v[116:119], a[224:227], a[168:171], v[116:119]\n"
        "v_exp_f32 v101, v101\n"
        "v_mfma_f32_16x16x32_bf16 v[104:107], v[152:155], v[176:179], v[16:19]\n"
        "s_waitcnt lgkmcnt(4)\n"
        "v_exp_f32 v102, v102\n"
        "v_mfma_f32_16x16x32_bf16 v[104:107], v[168:171], v[180:183], v[104:107]\n"
        "s_add_u32 m0, s41, 1024\n"
        "v_cvt_pk_bf16_f32 v64, v96, v97\n"
        "v_cvt_pk_bf16_f32 v65, v98, v99\n"
        "v_mfma_f32_16x16x32_bf16 v[108:111], v[172:175], v[176:179], v[20:23]\n"
        "v_cvt_pk_bf16_f32 v138, v116, v117\n"
        "v_cvt_pk_bf16_f32 v139, v118, v119\n"
        "v_mfma_f32_16x16x32_bf16 v[108:111], v[188:191], v[184:187], v[108:111]\n"
        "buffer_load_dwordx4 v243, s[8:11], s47 offen lds\n"
        "v_mfma_f32_16x16x32_bf16 v[120:123], a[224:227], a[192:195], 0\n"
        "ds_read_b128 a[224:227], v241 offset:12416\n"
        "v_cvt_pk_bf16_f32 v66, v100, v101\n"
        "v_mfma_f32_16x16x32_bf16 v[120:123], a[228:231], a[196:199], v[120:123]\n"
        "ds_read_b128 a[228:231], v240 offset:12544\n"
        "v_mfma_f32_16x16x32_bf16 v[120:123], a[232:235], a[200:203], v[120:123]\n"
        "ds_read_b128 a[232:235], v241 offset:12544\n"
        "v_mfma_f32_16x16x32_bf16 v[120:123], a[236:239], a[204:207], v[120:123]\n"
        "ds_read_b128 v[202:205], v251 offset:4096\n"
        "v_mfma_f32_16x16x32_bf16 v[120:123], a[240:243], a[208:211], v[120:123]\n"
        "ds_read_b128 v[182:185], v252 offset:512\n"
        "v_mfma_f32_16x16x32_bf16 v[120:123], a[244:247], a[212:215], v[120:123]\n"
        "s_waitcnt lgkmcnt(8)\n"
        "v_exp_f32 v103, v103\n"
        "v_mfma_f32_16x16x32_bf16 v[112:115], a[0:3], a[248:251], 0\n"
        "s_waitcnt lgkmcnt(7)\n"
        "v_cvt_pk_bf16_f32 v67, v102, v103\n"
        "s_nop 0\n"
        "v_mfma_f32_16x16x32_bf16 v[112:115], a[4:7], a[216:219], v[112:115]\n"
        "s_waitcnt lgkmcnt(6)\n"
        "v_exp_f32 v104, v104\n"
        "v_mfma_f32_16x16x32_bf16 v[112:115], a[8:11], a[220:223], v[112:115]\n"
        "s_waitcnt lgkmcnt(4)\n"
        "v_exp_f32 v105, v105\n"
        "v_mfma_f32_16x16x32_bf16 v[112:115], a[12:15], a[224:227], v[112:115]\n"
        "s_waitcnt lgkmcnt(3)\n"
        "v_exp_f32 v106, v106\n"
        "v_mfma_f32_16x16x32_bf16 v[112:115], a[16:19], a[228:231], v[112:115]\n"
        "s_waitcnt lgkmcnt(2)\n"
        "v_cvt_pk_bf16_f32 v72, v104, v105\n"
        "v_cvt_pk_bf16_f32 v146, v120, v121\n"
        "v_mfma_f32_16x16x32_bf16 v[112:115], a[20:23], a[232:235], v[112:115]\n"
        "s_add_u32 m0, s41, 2048\n"
        "v_exp_f32 v108, v108\n"
        "v_mfma_f32_16x16x32_bf16 v[116:119], a[44:47], a[232:235], 0\n"
        "v_cvt_pk_bf16_f32 v147, v122, v123\n"
        "v_add_u32 v245, s45, v245\n"
        "v_mfma_f32_16x16x32_bf16 v[116:119], a[40:43], a[228:231], v[116:119]\n"
        "buffer_load_dwordx4 v244, s[8:11], s47 offen lds\n"
        "v_mfma_f32_16x16x32_bf16 v[116:119], a[36:39], a[224:227], v[116:119]\n"
        "v_exp_f32 v107, v107\n"
        "v_mfma_f32_16x16x32_bf16 v[116:119], a[32:35], a[220:223], v[116:119]\n"
        "v_exp_f32 v109, v109\n"
        "v_mfma_f32_16x16x32_bf16 v[116:119], a[28:31], a[216:219], v[116:119]\n"
        "v_cvt_pk_bf16_f32 v73, v106, v107\n"
        "v_cvt_pk_bf16_f32 v74, v108, v109\n"
        "v_mfma_f32_16x16x32_bf16 v[116:119], a[24:27], a[248:251], v[116:119]\n"
        "v_exp_f32 v110, v110\n"
        "v_mfma_f32_16x16x32_bf16 v[120:123], a[48:51], a[248:251], 0\n"
        "v_exp_f32 v111, v111\n"
        "v_mfma_f32_16x16x32_bf16 v[120:123], a[52:55], a[216:219], v[120:123]\n"
        "v_cvt_pk_bf16_f32 v75, v110, v111\n"
        "v_cvt_pk_bf16_f32 v196, v112, v113\n"
        "v_mfma_f32_16x16x32_bf16 v[120:123], a[56:59], a[220:223], v[120:123]\n"
        "v_cvt_pk_bf16_f32 v197, v114, v115\n"
        "v_cvt_pk_bf16_f32 v198, v116, v117\n"
        "v_mfma_f32_16x16x32_bf16 v[120:123], a[60:63], a[224:227], v[120:123]\n"
        "v_cvt_pk_bf16_f32 v199, v118, v119\n"
        "v_add_u32 v246, s45, v246\n"
        "v_mfma_f32_16x16x32_bf16 v[120:123], a[64:67], a[228:231], v[120:123]\n"
        "v_mfma_f32_16x16x32_bf16 v[120:123], a[68:71], a[232:235], v[120:123]\n"
        "s_nop 2\n"
        "v_mfma_f32_16x16x32_bf16 v[112:115], a[92:95], a[232:235], 0\n"
        "s_nop 2\n"
        "v_mfma_f32_16x16x32_bf16 v[112:115], a[88:91], a[228:231], v[112:115]\n"
        "s_add_u32 m0, s41, 12288\n"
        "v_cvt_pk_bf16_f32 v200, v120, v121\n"
        "v_cvt_pk_bf16_f32 v201, v122, v123\n"
        "v_mfma_f32_16x16x32_bf16 v[112:115], a[84:87], a[224:227], v[112:115]\n"
        "v_cvt_pk_bf16_f32 v206, v120, v121\n"
        "v_cvt_pk_bf16_f32 v207, v122, v123\n"
        "v_mfma_f32_16x16x32_bf16 v[112:115], a[80:83], a[220:223], v[112:115]\n"
        "buffer_load_dwordx4 v242, s[8:11], s35 offen lds\n"
        "v_mfma_f32_16x16x32_bf16 v[112:115], a[76:79], a[216:219], v[112:115]\n"
        "v_mfma_f32_16x16x32_bf16 v[112:115], a[72:75], a[248:251], v[112:115]\n"
        "s_nop 2\n"
        "v_mfma_f32_16x16x32_bf16 v[96:99], v[152:155], v[196:199], v[32:35]\n"
        "s_waitcnt lgkmcnt(1)\n"
        "s_nop 1\n"
        "v_mfma_f32_16x16x32_bf16 v[96:99], v[168:171], v[200:203], v[96:99]\n"
        "s_nop 0\n"
        "v_cvt_pk_bf16_f32 v192, v112, v113\n"
        "v_cvt_pk_bf16_f32 v193, v114, v115\n"
        "v_mfma_f32_16x16x32_bf16 v[100:103], v[172:175], v[196:199], v[36:39]\n"
        "v_mfma_f32_16x16x32_bf16 v[100:103], v[188:191], v[204:207], v[100:103]\n"
        "ds_read_b128 v[202:205], v252 offset:4096\n"
        "s_cmp_eq_u32 s37, 100663296\n"
        "v_mfma_f32_16x16x32_bf16 v[116:119], a[96:99], a[248:251], 0\n"
        "s_cselect_b32 s46, 0, s37\n"
        "v_exp_f32 v96, v96\n"
        "v_mfma_f32_16x16x32_bf16 v[116:119], a[100:103], a[216:219], v[116:119]\n"
        "s_add_u32 s38, s46, s43\n"
        "v_exp_f32 v97, v97\n"
        "v_mfma_f32_16x16x32_bf16 v[116:119], a[104:107], a[220:223], v[116:119]\n"
        "s_add_u32 s39, s46, s44\n"
        "v_exp_f32 v98, v98\n"
        "v_mfma_f32_16x16x32_bf16 v[116:119], a[108:111], a[224:227], v[116:119]\n"
        "s_add_u32 s37, s37, 6291456\n"
        "v_exp_f32 v100, v100\n"
        "v_mfma_f32_16x16x32_bf16 v[116:119], a[112:115], a[228:231], v[116:119]\n"
        "v_exp_f32 v99, v99\n"
        "v_mfma_f32_16x16x32_bf16 v[116:119], a[116:119], a[232:235], v[116:119]\n"
        "s_add_u32 m0, s41, 13312\n"
        "v_exp_f32 v101, v101\n"
        "v_mfma_f32_16x16x32_bf16 v[120:123], a[140:143], a[232:235], 0\n"
        "v_exp_f32 v102, v102\n"
        "v_mfma_f32_16x16x32_bf16 v[120:123], a[136:139], a[228:231], v[120:123]\n"
        "buffer_load_dwordx4 v243, s[8:11], s35 offen lds\n"
        "v_mfma_f32_16x16x32_bf16 v[120:123], a[132:135], a[224:227], v[120:123]\n"
        "v_cvt_pk_bf16_f32 v80, v96, v97\n"
        "v_cvt_pk_bf16_f32 v194, v116, v117\n"
        "v_mfma_f32_16x16x32_bf16 v[120:123], a[128:131], a[220:223], v[120:123]\n"
        "v_cvt_pk_bf16_f32 v195, v118, v119\n"
        "v_cvt_pk_bf16_f32 v81, v98, v99\n"
        "v_mfma_f32_16x16x32_bf16 v[120:123], a[124:127], a[216:219], v[120:123]\n"
        "v_cvt_pk_bf16_f32 v82, v100, v101\n"
        "v_mfma_f32_16x16x32_bf16 v[120:123], a[120:123], a[248:251], v[120:123]\n"
        "v_exp_f32 v103, v103\n"
        "v_mfma_f32_16x16x32_bf16 v[112:115], a[248:251], a[144:147], 0\n"
        "v_cvt_pk_bf16_f32 v83, v102, v103\n"
        "s_nop 0\n"
        "v_mfma_f32_16x16x32_bf16 v[112:115], a[216:219], a[148:151], v[112:115]\n"
        "s_nop 2\n"
        "v_mfma_f32_16x16x32_bf16 v[112:115], a[220:223], a[152:155], v[112:115]\n"
        "ds_read_b128 a[236:239], v240 offset:13824\n"
        "v_cvt_pk_bf16_f32 v208, v120, v121\n"
        "v_mfma_f32_16x16x32_bf16 v[112:115], a[224:227], a[156:159], v[112:115]\n"
        "ds_read_b128 a[240:243], v241 offset:13824\n"
        "v_cvt_pk_bf16_f32 v209, v122, v123\n"
        "v_mfma_f32_16x16x32_bf16 v[112:115], a[228:231], a[160:163], v[112:115]\n"
        "ds_read_b128 a[244:247], v240 offset:13952\n"
        "v_mfma_f32_16x16x32_bf16 v[112:115], a[232:235], a[164:167], v[112:115]\n"
        "s_nop 2\n"
        "v_mfma_f32_16x16x32_bf16 v[104:107], v[192:195], v[156:159], v[8:11]\n"
        "s_nop 1\n"
        "v_mfma_f32_16x16x32_bf16 v[104:107], v[208:211], v[160:163], v[104:107]\n"
        "s_add_u32 m0, s41, 14336\n"
        "v_cvt_pk_bf16_f32 v132, v112, v113\n"
        "v_cvt_pk_bf16_f32 v133, v114, v115\n"
        "v_mfma_f32_16x16x32_bf16 v[116:119], a[232:235], a[188:191], 0\n"
        "v_mfma_f32_16x16x32_bf16 v[116:119], a[228:231], a[184:187], v[116:119]\n"
        "buffer_load_dwordx4 v244, s[8:11], s35 offen lds\n"
        "v_mfma_f32_16x16x32_bf16 v[116:119], a[224:227], a[180:183], v[116:119]\n"
        "v_mfma_f32_16x16x32_bf16 v[116:119], a[220:223], a[176:179], v[116:119]\n"
        "v_exp_f32 v104, v104\n"
        "v_mfma_f32_16x16x32_bf16 v[116:119], a[216:219], a[172:175], v[116:119]\n"
        "v_exp_f32 v105, v105\n"
        "v_mfma_f32_16x16x32_bf16 v[116:119], a[248:251], a[168:171], v[116:119]\n"
        "v_exp_f32 v106, v106\n"
        "v_mfma_f32_16x16x32_bf16 v[108:111], v[192:195], v[176:179], v[24:27]\n"
        "s_waitcnt lgkmcnt(4)\n"
        "v_exp_f32 v107, v107\n"
        "v_mfma_f32_16x16x32_bf16 v[108:111], v[208:211], v[180:183], v[108:111]\n"
        "v_cvt_pk_bf16_f32 v68, v104, v105\n"
        "v_cvt_pk_bf16_f32 v69, v106, v107\n"
        "v_mfma_f32_16x16x32_bf16 v[120:123], a[248:251], a[192:195], 0\n"
        "ds_read_b128 a[248:251], v241 offset:13952\n"
        "v_cvt_pk_bf16_f32 v140, v116, v117\n"
        "v_mfma_f32_16x16x32_bf16 v[120:123], a[216:219], a[196:199], v[120:123]\n"
        "ds_read_b128 a[216:219], v240 offset:14080\n"
        "v_cvt_pk_bf16_f32 v141, v118, v119\n"
        "v_mfma_f32_16x16x32_bf16 v[120:123], a[220:223], a[200:203], v[120:123]\n"
        "ds_read_b128 a[220:223], v241 offset:14080\n"
        "v_mfma_f32_16x16x32_bf16 v[120:123], a[224:227], a[204:207], v[120:123]\n"
        "v_exp_f32 v108, v108\n"
        "v_mfma_f32_16x16x32_bf16 v[120:123], a[228:231], a[208:211], v[120:123]\n"
        "v_exp_f32 v109, v109\n"
        "v_mfma_f32_16x16x32_bf16 v[120:123], a[232:235], a[212:215], v[120:123]\n"
        "v_exp_f32 v110, v110\n"
        "v_mfma_f32_16x16x32_bf16 v[96:99], v[192:195], v[196:199], v[40:43]\n"
        "s_waitcnt lgkmcnt(6)\n"
        "v_exp_f32 v111, v111\n"
        "v_mfma_f32_16x16x32_bf16 v[96:99], v[208:211], v[200:203], v[96:99]\n"
        "s_waitcnt lgkmcnt(5)\n"
        "v_cvt_pk_bf16_f32 v76, v108, v109\n"
        "v_cvt_pk_bf16_f32 v77, v110, v111\n"
        "v_mfma_f32_16x16x32_bf16 v[112:115], a[72:75], a[236:239], 0\n"
        "s_waitcnt lgkmcnt(4)\n"
        "s_nop 0\n"
        "v_cvt_pk_bf16_f32 v148, v120, v121\n"
        "v_mfma_f32_16x16x32_bf16 v[112:115], a[76:79], a[240:243], v[112:115]\n"
        "s_waitcnt lgkmcnt(3)\n"
        "v_cvt_pk_bf16_f32 v149, v122, v123\n"
        "v_mfma_f32_16x16x32_bf16 v[112:115], a[80:83], a[244:247], v[112:115]\n"
        "s_waitcnt lgkmcnt(2)\n"
        "v_exp_f32 v96, v96\n"
        "v_mfma_f32_16x16x32_bf16 v[112:115], a[84:87], a[248:251], v[112:115]\n"
        "s_waitcnt lgkmcnt(1)\n"
        "v_exp_f32 v97, v97\n"
        "v_mfma_f32_16x16x32_bf16 v[112:115], a[88:91], a[216:219], v[112:115]\n"
        "s_waitcnt lgkmcnt(0)\n"
        "v_exp_f32 v98, v98\n"
        "v_mfma_f32_16x16x32_bf16 v[112:115], a[92:95], a[220:223], v[112:115]\n"
        "v_exp_f32 v99, v99\n"
        "v_mfma_f32_16x16x32_bf16 v[116:119], a[116:119], a[220:223], 0\n"
        "v_cvt_pk_bf16_f32 v84, v96, v97\n"
        "v_cvt_pk_bf16_f32 v85, v98, v99\n"
        "v_mfma_f32_16x16x32_bf16 v[116:119], a[112:115], a[216:219], v[116:119]\n"
        "v_add_u32 v240, s31, v240\n"
        "v_add_u32 v241, s31, v241\n"
        "v_mfma_f32_16x16x32_bf16 v[116:119], a[108:111], a[248:251], v[116:119]\n"
        "v_cvt_pk_bf16_f32 v212, v112, v113\n"
        "v_cvt_pk_bf16_f32 v213, v114, v115\n"
        "v_mfma_f32_16x16x32_bf16 v[116:119], a[104:107], a[244:247], v[116:119]\n"
        "v_mfma_f32_16x16x32_bf16 v[116:119], a[100:103], a[240:243], v[116:119]\n"
        "v_mfma_f32_16x16x32_bf16 v[116:119], a[96:99], a[236:239], v[116:119]\n"
        "s_nop 2\n"
        "v_mfma_f32_16x16x32_bf16 v[120:123], a[120:123], a[236:239], 0\n"
        "s_nop 2\n"
        "v_mfma_f32_16x16x32_bf16 v[120:123], a[124:127], a[240:243], v[120:123]\n"
        "s_add_u32 s47, s47, 6291456\n"
        "v_cvt_pk_bf16_f32 v214, v116, v117\n"
        "v_cvt_pk_bf16_f32 v215, v118, v119\n"
        "v_mfma_f32_16x16x32_bf16 v[120:123], a[128:131], a[244:247], v[120:123]\n"
        "s_add_u32 s36, s36, 2097152\n"
        "v_mfma_f32_16x16x32_bf16 v[120:123], a[132:135], a[248:251], v[120:123]\n"
        "s_xor_b32 s48, s48, 8192\n"
        "v_mfma_f32_16x16x32_bf16 v[120:123], a[136:139], a[216:219], v[120:123]\n"
        "v_mfma_f32_16x16x32_bf16 v[120:123], a[140:143], a[220:223], v[120:123]\n"
        "s_nop 2\n"
        "v_mfma_f32_16x16x32_bf16 v[112:115], a[220:223], a[164:167], 0\n"
        "s_nop 2\n"
        "v_mfma_f32_16x16x32_bf16 v[112:115], a[216:219], a[160:163], v[112:115]\n"
        "v_cvt_pk_bf16_f32 v230, v120, v121\n"
        "v_cvt_pk_bf16_f32 v231, v122, v123\n"
        "v_mfma_f32_16x16x32_bf16 v[112:115], a[248:251], a[156:159], v[112:115]\n"
        "s_barrier\n"
        "v_mfma_f32_16x16x32_bf16 v[112:115], a[244:247], a[152:155], v[112:115]\n"
        "v_mfma_f32_16x16x32_bf16 v[112:115], a[240:243], a[148:151], v[112:115]\n"
        "v_mfma_f32_16x16x32_bf16 v[112:115], a[236:239], a[144:147], v[112:115]\n"
        "s_nop 2\n"
        "v_mfma_f32_16x16x32_bf16 v[100:103], v[212:215], v[156:159], v[12:15]\n"
        "s_nop 2\n"
        "v_mfma_f32_16x16x32_bf16 v[100:103], v[228:231], v[164:167], v[100:103]\n"
        "v_cvt_pk_bf16_f32 v134, v112, v113\n"
        "v_cvt_pk_bf16_f32 v135, v114, v115\n"
        "v_mfma_f32_16x16x32_bf16 v[116:119], a[236:239], a[168:171], 0\n"
        "v_mfma_f32_16x16x32_bf16 v[116:119], a[240:243], a[172:175], v[116:119]\n"
        "v_mfma_f32_16x16x32_bf16 v[116:119], a[244:247], a[176:179], v[116:119]\n"
        "v_mfma_f32_16x16x32_bf16 v[116:119], a[248:251], a[180:183], v[116:119]\n"
        "v_mfma_f32_16x16x32_bf16 v[116:119], a[216:219], a[184:187], v[116:119]\n"
        "s_nop 0\n"
        "v_exp_f32 v100, v100\n"
        "v_mfma_f32_16x16x32_bf16 v[116:119], a[220:223], a[188:191], v[116:119]\n"
        "v_exp_f32 v101, v101\n"
        "v_mfma_f32_16x16x32_bf16 v[104:107], v[212:215], v[176:179], v[28:31]\n"
        "v_exp_f32 v102, v102\n"
        "v_mfma_f32_16x16x32_bf16 v[104:107], v[228:231], v[184:187], v[104:107]\n"
        "v_exp_f32 v103, v103\n"
        "v_mfma_f32_16x16x32_bf16 v[120:123], a[220:223], a[212:215], 0\n"
        "v_cvt_pk_bf16_f32 v70, v100, v101\n"
        "v_cvt_pk_bf16_f32 v71, v102, v103\n"
        "v_mfma_f32_16x16x32_bf16 v[120:123], a[216:219], a[208:211], v[120:123]\n"
        "v_cvt_pk_bf16_f32 v142, v116, v117\n"
        "v_cvt_pk_bf16_f32 v143, v118, v119\n"
        "v_mfma_f32_16x16x32_bf16 v[120:123], a[248:251], a[204:207], v[120:123]\n"
        "v_mov_b64 v[96:97], 0\n"
        "v_mfma_f32_16x16x32_bf16 v[120:123], a[244:247], a[200:203], v[120:123]\n"
        "v_exp_f32 v104, v104\n"
        "v_mfma_f32_16x16x32_bf16 v[120:123], a[240:243], a[196:199], v[120:123]\n"
        "v_exp_f32 v105, v105\n"
        "v_mfma_f32_16x16x32_bf16 v[120:123], a[236:239], a[192:195], v[120:123]\n"
        "v_exp_f32 v106, v106\n"
        "v_mfma_f32_16x16x32_bf16 v[108:111], v[212:215], v[196:199], v[44:47]\n"
        "v_exp_f32 v107, v107\n"
        "v_mfma_f32_16x16x32_bf16 v[108:111], v[228:231], v[204:207], v[108:111]\n"
        "v_cvt_pk_bf16_f32 v78, v104, v105\n"
        "v_cvt_pk_bf16_f32 v79, v106, v107\n"
        "v_mfma_f32_16x16x32_bf16 v[112:115], a[0:3], a[236:239], 0\n"
        "s_nop 0\n"
        "v_cvt_pk_bf16_f32 v150, v120, v121\n"
        "v_cvt_pk_bf16_f32 v151, v122, v123\n"
        "v_mfma_f32_16x16x32_bf16 v[112:115], a[4:7], a[240:243], v[112:115]\n"
        "v_mfma_f32_16x16x32_bf16 v[112:115], a[8:11], a[244:247], v[112:115]\n"
        "v_exp_f32 v108, v108\n"
        "v_mfma_f32_16x16x32_bf16 v[112:115], a[12:15], a[248:251], v[112:115]\n"
        "v_exp_f32 v109, v109\n"
        "v_mfma_f32_16x16x32_bf16 v[112:115], a[16:19], a[216:219], v[112:115]\n"
        "v_exp_f32 v110, v110\n"
        "v_mfma_f32_16x16x32_bf16 v[112:115], a[20:23], a[220:223], v[112:115]\n"
        "v_exp_f32 v111, v111\n"
        "v_smfmac_f32_16x16x64_bf16 v[96:99], a[252:255], v[64:71], v237\n"
        "v_cvt_pk_bf16_f32 v86, v108, v109\n"
        "v_mfma_f32_16x16x32_bf16 v[100:103], v[132:135], v[68:71], 0\n"
        "v_mov_b64 v[124:125], 0\n"
        "v_cvt_pk_bf16_f32 v87, v110, v111\n"
        "v_mfma_f32_16x16x32_bf16 v[100:103], v[128:131], v[64:67], v[100:103]\n"
        "s_nop 0\n"
        "v_cvt_pk_bf16_f32 v216, v112, v113\n"
        "v_cvt_pk_bf16_f32 v217, v114, v115\n"
        "v_mfma_f32_16x16x32_bf16 v[104:107], v[136:139], v[64:67], 0\n"
        "ds_read_b128 v[222:225], v251 offset:4608\n"
        "v_add_f32 v97, v96, v97\n"
        "v_mfma_f32_16x16x32_bf16 v[104:107], v[140:143], v[68:71], v[104:107]\n"
        "v_rcp_f32 v96, v97\n"
        "v_mfma_f32_16x16x32_bf16 v[108:111], v[148:151], v[68:71], 0\n"
        "v_mul_f32 v100, v96, v100\n"
        "v_mul_f32 v101, v96, v101\n"
        "v_mfma_f32_16x16x32_bf16 v[108:111], v[144:147], v[64:67], v[108:111]\n"
        "v_mul_f32 v102, v96, v102\n"
        "v_mul_f32 v103, v96, v103\n"
        "v_mfma_f32_16x16x32_bf16 v[116:119], a[44:47], a[220:223], 0\n"
        "v_cvt_pk_bf16_f32 v100, v100, v101\n"
        "v_cvt_pk_bf16_f32 v101, v102, v103\n"
        "v_mfma_f32_16x16x32_bf16 v[116:119], a[40:43], a[216:219], v[116:119]\n"
        "v_mul_f32 v104, v96, v104\n"
        "v_mul_f32 v105, v96, v105\n"
        "v_mfma_f32_16x16x32_bf16 v[116:119], a[36:39], a[248:251], v[116:119]\n"
        "v_mul_f32 v106, v96, v106\n"
        "v_mul_f32 v107, v96, v107\n"
        "v_mfma_f32_16x16x32_bf16 v[116:119], a[32:35], a[244:247], v[116:119]\n"
        "v_cvt_pk_bf16_f32 v102, v104, v105\n"
        "v_cvt_pk_bf16_f32 v103, v106, v107\n"
        "v_mfma_f32_16x16x32_bf16 v[116:119], a[28:31], a[240:243], v[116:119]\n"
        "ds_write_b128 v245, v[100:103] offset:0\n"
        "v_mfma_f32_16x16x32_bf16 v[116:119], a[24:27], a[236:239], v[116:119]\n"
        "v_mul_f32 v108, v96, v108\n"
        "v_mul_f32 v109, v96, v109\n"
        "v_smfmac_f32_16x16x64_bf16 v[124:127], a[252:255], v[72:79], v237\n"
        "v_mul_f32 v110, v96, v110\n"
        "v_mfma_f32_16x16x32_bf16 v[64:67], v[132:135], v[76:79], 0\n"
        "v_mul_f32 v111, v96, v111\n"
        "v_cvt_pk_bf16_f32 v108, v108, v109\n"
        "v_mfma_f32_16x16x32_bf16 v[64:67], v[128:131], v[72:75], v[64:67]\n"
        "ds_read_b128 v[156:159], v247 offset:0\n"
        "v_cvt_pk_bf16_f32 v218, v116, v117\n"
        "v_mfma_f32_16x16x32_bf16 v[68:71], v[136:139], v[72:75], 0\n"
        "ds_read_b128 v[160:163], v247 offset:4096\n"
        "v_cvt_pk_bf16_f32 v219, v118, v119\n"
        "v_mfma_f32_16x16x32_bf16 v[68:71], v[140:143], v[76:79], v[68:71]\n"
        "ds_read_b128 v[164:167], v247 offset:4352\n"
        "v_cvt_pk_bf16_f32 v109, v110, v111\n"
        "v_mfma_f32_16x16x32_bf16 v[120:123], v[148:151], v[76:79], 0\n"
        "ds_write_b64 v246, v[108:109] offset:0\n"
        "v_mfma_f32_16x16x32_bf16 v[120:123], v[144:147], v[72:75], v[120:123]\n"
        "v_max_u32 v236, v96, v97\n"
        "v_mov_b64 v[116:117], 0\n"
        "v_mfma_f32_16x16x32_bf16 v[112:115], a[48:51], a[236:239], 0\n"
        "v_add_f32 v125, v124, v125\n"
        "v_mfma_f32_16x16x32_bf16 v[112:115], a[52:55], a[240:243], v[112:115]\n"
        "v_rcp_f32 v124, v125\n"
        "v_mfma_f32_16x16x32_bf16 v[112:115], a[56:59], a[244:247], v[112:115]\n"
        "v_mul_f32 v64, v124, v64\n"
        "v_mul_f32 v65, v124, v65\n"
        "v_mfma_f32_16x16x32_bf16 v[112:115], a[60:63], a[248:251], v[112:115]\n"
        "v_mul_f32 v66, v124, v66\n"
        "v_mul_f32 v67, v124, v67\n"
        "v_mfma_f32_16x16x32_bf16 v[112:115], a[64:67], a[216:219], v[112:115]\n"
        "v_cvt_pk_bf16_f32 v64, v64, v65\n"
        "v_cvt_pk_bf16_f32 v65, v66, v67\n"
        "v_mfma_f32_16x16x32_bf16 v[112:115], a[68:71], a[220:223], v[112:115]\n"
        "ds_read_b128 a[216:219], v248 offset:0\n"
        "v_mul_f32 v68, v124, v68\n"
        "v_smfmac_f32_16x16x64_bf16 v[116:119], a[252:255], v[80:87], v237\n"
        "ds_read_b128 a[220:223], v248 offset:3072\n"
        "v_mfma_f32_16x16x32_bf16 v[72:75], v[132:135], v[84:87], 0\n"
        "v_mul_f32 v69, v124, v69\n"
        "v_mul_f32 v70, v124, v70\n"
        "v_mfma_f32_16x16x32_bf16 v[72:75], v[128:131], v[80:83], v[72:75]\n"
        "v_cvt_pk_bf16_f32 v220, v112, v113\n"
        "v_cvt_pk_bf16_f32 v221, v114, v115\n"
        "v_mfma_f32_16x16x32_bf16 v[76:79], v[136:139], v[80:83], 0\n"
        "ds_read_b128 a[224:227], v254 offset:16\n"
        "v_cvt_pk_bf16_f32 v226, v112, v113\n"
        "v_mfma_f32_16x16x32_bf16 v[76:79], v[140:143], v[84:87], v[76:79]\n"
        "ds_read_b128 a[228:231], v254 offset:3088\n"
        "v_cvt_pk_bf16_f32 v227, v114, v115\n"
        "v_mfma_f32_16x16x32_bf16 v[96:99], v[148:151], v[84:87], 0\n"
        "v_mul_f32 v71, v124, v71\n"
        "v_cvt_pk_bf16_f32 v66, v68, v69\n"
        "v_mfma_f32_16x16x32_bf16 v[96:99], v[144:147], v[80:83], v[96:99]\n"
        "v_cvt_pk_bf16_f32 v67, v70, v71\n"
        "v_mul_f32 v120, v124, v120\n"
        "v_mfma_f32_16x16x32_bf16 v[100:103], v[152:155], v[216:219], v[48:51]\n"
        "s_waitcnt lgkmcnt(9)\n"
        "ds_read_b128 v[152:155], v247 offset:256\n"
        "v_mul_f32 v121, v124, v121\n"
        "v_mfma_f32_16x16x32_bf16 v[100:103], v[168:171], v[220:223], v[100:103]\n"
        "ds_write_b128 v245, v[64:67] offset:256\n"
        "v_mfma_f32_16x16x32_bf16 v[104:107], v[172:175], v[216:219], v[52:55]\n"
        "v_mul_f32 v122, v124, v122\n"
        "v_mul_f32 v123, v124, v123\n"
        "v_mfma_f32_16x16x32_bf16 v[104:107], v[188:191], v[224:227], v[104:107]\n"
        "s_waitcnt lgkmcnt(5)\n"
        "ds_read_b128 v[222:225], v252 offset:4608\n"
        "v_cvt_pk_bf16_f32 v120, v120, v121\n"
        "v_mfma_f32_16x16x32_bf16 v[176:179], v[156:159], a[216:219], 0\n"
        "s_waitcnt lgkmcnt(5)\n"
        "ds_read_b128 a[232:235], v248 offset:32\n"
        "v_cvt_pk_bf16_f32 v121, v122, v123\n"
        "v_mfma_f32_16x16x32_bf16 v[176:179], v[160:163], a[220:223], v[176:179]\n"
        "ds_write_b64 v246, v[120:121] offset:256\n"
        "v_mfma_f32_16x16x32_bf16 v[196:199], v[164:167], a[220:223], 0\n"
        "s_waitcnt lgkmcnt(4)\n"
        "ds_read_b128 a[220:223], v248 offset:3104\n"
        "v_max3_u32 v236, v236, v124, v125\n"
        "v_mfma_f32_16x16x32_bf16 v[196:199], v[152:155], a[216:219], v[196:199]\n"
        "v_exp_f32 v100, v100\n"
        "v_mfma_f32_16x16x32_bf16 v[200:203], v[152:155], a[224:227], 0\n"
        "ds_read_b128 a[216:219], v254 offset:48\n"
        "v_add_f32 v117, v116, v117\n"
        "v_mfma_f32_16x16x32_bf16 v[200:203], v[164:167], a[228:231], v[200:203]\n"
        "v_rcp_f32 v116, v117\n"
        "v_mfma_f32_16x16x32_bf16 v[180:183], v[160:163], a[228:231], 0\n"
        "ds_read_b128 a[228:231], v254 offset:3120\n"
        "v_mul_f32 v72, v116, v72\n"
        "v_mfma_f32_16x16x32_bf16 v[180:183], v[156:159], a[224:227], v[180:183]\n"
        "ds_read_b128 a[236:239], v247 offset:8192\n"
        "v_mul_f32 v73, v116, v73\n"
        "v_mfma_f32_16x16x32_bf16 v[108:111], v[192:195], v[216:219], v[56:59]\n"
        "s_waitcnt lgkmcnt(6)\n"
        "ds_read_b128 a[224:227], v248 offset:64\n"
        "v_mul_f32 v74, v116, v74\n"
        "v_mfma_f32_16x16x32_bf16 v[108:111], v[208:211], v[220:223], v[108:111]\n"
        "ds_read_b128 a[240:243], v247 offset:12288\n"
        "v_mul_f32 v75, v116, v75\n"
        "v_mfma_f32_16x16x32_bf16 v[112:115], v[212:215], v[216:219], v[60:63]\n"
        "ds_read_b128 a[244:247], v247 offset:12544\n"
        "v_cvt_pk_bf16_f32 v72, v72, v73\n"
        "v_mfma_f32_16x16x32_bf16 v[112:115], v[228:231], v[224:227], v[112:115]\n"
        "s_waitcnt lgkmcnt(8)\n"
        "ds_read_b128 a[248:251], v247 offset:8448\n"
        "v_cvt_pk_bf16_f32 v73, v74, v75\n"
        "v_mfma_f32_16x16x32_bf16 v[184:187], v[156:159], a[232:235], 0\n"
        "s_waitcnt lgkmcnt(7)\n"
        "v_exp_f32 v101, v101\n"
        "v_mfma_f32_16x16x32_bf16 v[184:187], v[160:163], a[220:223], v[184:187]\n"
        "v_mul_f32 v76, v116, v76\n"
        "v_mul_f32 v77, v116, v77\n"
        "v_mfma_f32_16x16x32_bf16 v[204:207], v[164:167], a[220:223], 0\n"
        "ds_read_b128 a[220:223], v248 offset:3136\n"
        "v_mul_f32 v78, v116, v78\n"
        "v_mfma_f32_16x16x32_bf16 v[204:207], v[152:155], a[232:235], v[204:207]\n"
        "s_waitcnt lgkmcnt(7)\n"
        "ds_read_b128 a[232:235], v254 offset:80\n"
        "v_mul_f32 v79, v116, v79\n"
        "v_mfma_f32_16x16x32_bf16 v[192:195], v[152:155], a[216:219], 0\n"
        "s_waitcnt lgkmcnt(7)\n"
        "v_exp_f32 v102, v102\n"
        "v_mfma_f32_16x16x32_bf16 v[192:195], v[164:167], a[228:231], v[192:195]\n"
        "v_cvt_pk_bf16_f32 v88, v100, v101\n"
        "v_cvt_pk_bf16_f32 v74, v76, v77\n"
        "v_mfma_f32_16x16x32_bf16 v[172:175], v[160:163], a[228:231], 0\n"
        "ds_read_b128 a[228:231], v254 offset:3152\n"
        "v_cvt_pk_bf16_f32 v75, v78, v79\n"
        "v_mfma_f32_16x16x32_bf16 v[172:175], v[156:159], a[216:219], v[172:175]\n"
        "s_waitcnt lgkmcnt(6)\n"
        "ds_write_b128 v245, v[72:75] offset:512\n"
        "v_mfma_f32_16x16x32_bf16 v[212:215], v[156:159], a[224:227], 0\n"
        "s_waitcnt lgkmcnt(3)\n"
        "ds_read_b128 a[216:219], v248 offset:6144\n"
        "v_mul_f32 v96, v116, v96\n"
        "v_mfma_f32_16x16x32_bf16 v[212:215], v[160:163], a[220:223], v[212:215]\n"
        "v_exp_f32 v104, v104\n"
        "v_mfma_f32_16x16x32_bf16 v[216:219], v[164:167], a[220:223], 0\n"
        "ds_read_b128 a[220:223], v248 offset:9216\n"
        "v_mul_f32 v97, v116, v97\n"
        "v_mfma_f32_16x16x32_bf16 v[216:219], v[152:155], a[224:227], v[216:219]\n"
        "s_waitcnt lgkmcnt(4)\n"
        "ds_read_b128 a[224:227], v254 offset:6160\n"
        "v_mul_f32 v98, v116, v98\n"
        "v_mfma_f32_16x16x32_bf16 v[224:227], v[152:155], a[232:235], 0\n"
        "s_waitcnt lgkmcnt(4)\n"
        "v_exp_f32 v103, v103\n"
        "v_mfma_f32_16x16x32_bf16 v[224:227], v[164:167], a[228:231], v[224:227]\n"
        "v_exp_f32 v105, v105\n"
        "v_mfma_f32_16x16x32_bf16 v[220:223], v[160:163], a[228:231], 0\n"
        "ds_read_b128 a[228:231], v254 offset:9232\n"
        "v_cvt_pk_bf16_f32 v89, v102, v103\n"
        "v_mfma_f32_16x16x32_bf16 v[220:223], v[156:159], a[232:235], v[220:223]\n"
        "s_waitcnt lgkmcnt(3)\n"
        "ds_read_b128 a[232:235], v248 offset:6176\n"
        "v_cvt_pk_bf16_f32 v90, v104, v105\n"
        "v_mfma_f32_16x16x32_bf16 v[176:179], a[236:239], a[216:219], v[176:179]\n"
        "s_waitcnt lgkmcnt(3)\n"
        "v_exp_f32 v106, v106\n"
        "v_mfma_f32_16x16x32_bf16 v[176:179], a[240:243], a[220:223], v[176:179]\n"
        "v_mul_f32 v99, v116, v99\n"
        "v_cvt_pk_bf16_f32 v96, v96, v97\n"
        "v_mfma_f32_16x16x32_bf16 v[196:199], a[244:247], a[220:223], v[196:199]\n"
        "ds_read_b128 a[220:223], v248 offset:9248\n"
        "v_cvt_pk_bf16_f32 v97, v98, v99\n"
        "v_mfma_f32_16x16x32_bf16 v[196:199], a[248:251], a[216:219], v[196:199]\n"
        "s_waitcnt lgkmcnt(3)\n"
        "ds_write_b64 v246, v[96:97] offset:512\n"
        "v_mfma_f32_16x16x32_bf16 v[200:203], a[248:251], a[224:227], v[200:203]\n"
        "s_waitcnt lgkmcnt(3)\n"
        "ds_read_b128 a[216:219], v254 offset:6192\n"
        "v_max3_u32 v236, v236, v116, v117\n"
        "v_mfma_f32_16x16x32_bf16 v[200:203], a[244:247], a[228:231], v[200:203]\n"
        "ds_read_b128 v[156:159], v247 offset:16384\n"
        "v_mfma_f32_16x16x32_bf16 v[180:183], a[240:243], a[228:231], v[180:183]\n"
        "ds_read_b128 a[228:231], v254 offset:9264\n"
        "v_mfma_f32_16x16x32_bf16 v[180:183], a[236:239], a[224:227], v[180:183]\n"
        "s_waitcnt lgkmcnt(5)\n"
        "ds_read_b128 a[224:227], v248 offset:6208\n"
        "v_mfma_f32_16x16x32_bf16 v[184:187], a[236:239], a[232:235], v[184:187]\n"
        "s_waitcnt lgkmcnt(5)\n"
        "ds_read_b128 v[160:163], v247 offset:20480\n"
        "v_mfma_f32_16x16x32_bf16 v[184:187], a[240:243], a[220:223], v[184:187]\n"
        "ds_read_b128 v[164:167], v247 offset:20736\n"
        "v_mfma_f32_16x16x32_bf16 v[204:207], a[244:247], a[220:223], v[204:207]\n"
        "ds_read_b128 a[220:223], v248 offset:9280\n"
        "v_mfma_f32_16x16x32_bf16 v[204:207], a[248:251], a[232:235], v[204:207]\n"
        "s_waitcnt lgkmcnt(6)\n"
        "ds_read_b128 a[232:235], v254 offset:6224\n"
        "v_mfma_f32_16x16x32_bf16 v[192:195], a[248:251], a[216:219], v[192:195]\n"
        "s_waitcnt lgkmcnt(5)\n"
        "ds_read_b128 v[152:155], v247 offset:16640\n"
        "v_mfma_f32_16x16x32_bf16 v[192:195], a[244:247], a[228:231], v[192:195]\n"
        "v_exp_f32 v107, v107\n"
        "v_mfma_f32_16x16x32_bf16 v[172:175], a[240:243], a[228:231], v[172:175]\n"
        "ds_read_b128 a[228:231], v254 offset:9296\n"
        "v_cvt_pk_bf16_f32 v91, v106, v107\n"
        "v_mfma_f32_16x16x32_bf16 v[172:175], a[236:239], a[216:219], v[172:175]\n"
        "s_waitcnt lgkmcnt(6)\n"
        "ds_read_b128 a[216:219], v248 offset:12288\n"
        "v_mfma_f32_16x16x32_bf16 v[212:215], a[236:239], a[224:227], v[212:215]\n"
        "s_waitcnt lgkmcnt(4)\n"
        "v_exp_f32 v108, v108\n"
        "v_mfma_f32_16x16x32_bf16 v[212:215], a[240:243], a[220:223], v[212:215]\n"
        "v_exp_f32 v109, v109\n"
        "v_mfma_f32_16x16x32_bf16 v[216:219], a[244:247], a[220:223], v[216:219]\n"
        "ds_read_b128 a[220:223], v248 offset:15360\n"
        "v_cvt_pk_bf16_f32 v92, v108, v109\n"
        "v_mfma_f32_16x16x32_bf16 v[216:219], a[248:251], a[224:227], v[216:219]\n"
        "s_waitcnt lgkmcnt(4)\n"
        "ds_read_b128 a[224:227], v254 offset:12304\n"
        "v_mfma_f32_16x16x32_bf16 v[224:227], a[248:251], a[232:235], v[224:227]\n"
        "s_waitcnt lgkmcnt(3)\n"
        "v_exp_f32 v110, v110\n"
        "v_mfma_f32_16x16x32_bf16 v[224:227], a[244:247], a[228:231], v[224:227]\n"
        "v_exp_f32 v112, v112\n"
        "v_mfma_f32_16x16x32_bf16 v[220:223], a[240:243], a[228:231], v[220:223]\n"
        "ds_read_b128 a[228:231], v254 offset:15376\n"
        "v_mfma_f32_16x16x32_bf16 v[220:223], a[236:239], a[232:235], v[220:223]\n"
        "s_waitcnt lgkmcnt(3)\n"
        "ds_read_b128 a[232:235], v248 offset:12320\n"
        "v_mfma_f32_16x16x32_bf16 v[176:179], v[156:159], a[216:219], v[176:179]\n"
        "s_waitcnt lgkmcnt(3)\n"
        "v_exp_f32 v111, v111\n"
        "v_mfma_f32_16x16x32_bf16 v[176:179], v[160:163], a[220:223], v[176:179]\n"
        "v_exp_f32 v113, v113\n"
        "v_mfma_f32_16x16x32_bf16 v[196:199], v[164:167], a[220:223], v[196:199]\n"
        "ds_read_b128 a[220:223], v248 offset:15392\n"
        "v_cvt_pk_bf16_f32 v93, v110, v111\n"
        "v_mfma_f32_16x16x32_bf16 v[196:199], v[152:155], a[216:219], v[196:199]\n"
        "s_waitcnt lgkmcnt(3)\n"
        "ds_read_b128 a[216:219], v254 offset:12336\n"
        "v_cvt_pk_bf16_f32 v94, v112, v113\n"
        "v_mfma_f32_16x16x32_bf16 v[200:203], v[152:155], a[224:227], v[200:203]\n"
        "s_waitcnt lgkmcnt(3)\n"
        "v_exp_f32 v114, v114\n"
        "v_mfma_f32_16x16x32_bf16 v[200:203], v[164:167], a[228:231], v[200:203]\n"
        "v_exp_f32 v115, v115\n"
        "v_mfma_f32_16x16x32_bf16 v[180:183], v[160:163], a[228:231], v[180:183]\n"
        "ds_read_b128 a[228:231], v254 offset:15408\n"
        "v_cvt_pk_bf16_f32 v95, v114, v115\n"
        "v_mfma_f32_16x16x32_bf16 v[180:183], v[156:159], a[224:227], v[180:183]\n"
        "s_waitcnt lgkmcnt(3)\n"
        "ds_read_b128 a[224:227], v248 offset:12352\n"
        "v_mfma_f32_16x16x32_bf16 v[184:187], v[156:159], a[232:235], v[184:187]\n"
        "s_waitcnt lgkmcnt(3)\n"
        "s_waitcnt vmcnt(0)\n"
        "s_barrier\n"
        "v_mfma_f32_16x16x32_bf16 v[184:187], v[160:163], a[220:223], v[184:187]\n"
        "v_mfma_f32_16x16x32_bf16 v[204:207], v[164:167], a[220:223], v[204:207]\n"
        "ds_read_b128 a[220:223], v248 offset:15424\n"
        "v_mfma_f32_16x16x32_bf16 v[204:207], v[152:155], a[232:235], v[204:207]\n"
        "s_waitcnt lgkmcnt(3)\n"
        "ds_read_b128 a[232:235], v254 offset:12368\n"
        "v_mfma_f32_16x16x32_bf16 v[192:195], v[152:155], a[216:219], v[192:195]\n"
        "s_waitcnt lgkmcnt(3)\n"
        "ds_read_b128 a[236:239], v240 offset:0\n"
        "v_mfma_f32_16x16x32_bf16 v[192:195], v[164:167], a[228:231], v[192:195]\n"
        "ds_read_b128 a[240:243], v241 offset:0\n"
        "v_mfma_f32_16x16x32_bf16 v[172:175], v[160:163], a[228:231], v[172:175]\n"
        "ds_read_b128 a[228:231], v254 offset:15440\n"
        "v_mfma_f32_16x16x32_bf16 v[172:175], v[156:159], a[216:219], v[172:175]\n"
        "s_waitcnt lgkmcnt(5)\n"
        "ds_read_b128 a[244:247], v240 offset:128\n"
        "v_mfma_f32_16x16x32_bf16 v[212:215], v[156:159], a[224:227], v[212:215]\n"
        "s_waitcnt lgkmcnt(5)\n"
        "ds_read_b128 a[248:251], v241 offset:128\n"
        "v_mfma_f32_16x16x32_bf16 v[212:215], v[160:163], a[220:223], v[212:215]\n"
        "ds_read_b128 a[216:219], v240 offset:256\n"
        "v_mfma_f32_16x16x32_bf16 v[216:219], v[164:167], a[220:223], v[216:219]\n"
        "ds_read_b128 a[220:223], v241 offset:256\n"
        "v_mfma_f32_16x16x32_bf16 v[216:219], v[152:155], a[224:227], v[216:219]\n"
        "s_waitcnt lgkmcnt(7)\n"
        "v_mfma_f32_16x16x32_bf16 v[224:227], v[152:155], a[232:235], v[224:227]\n"
        "s_waitcnt lgkmcnt(4)\n"
        "v_mfma_f32_16x16x32_bf16 v[224:227], v[164:167], a[228:231], v[224:227]\n"
        "s_sub_u32 s27, s27, 1\n"
        "v_mfma_f32_16x16x32_bf16 v[220:223], v[160:163], a[228:231], v[220:223]\n"
        "s_cmp_lg_u32 s27, 0\n"
        "v_mfma_f32_16x16x32_bf16 v[220:223], v[156:159], a[232:235], v[220:223]\n"
        "s_cbranch_scc1 10b\n"
        "s_waitcnt lgkmcnt(0)\n"
        "s_and_b32 s46, s42, 1\n"
        "s_lshl_b32 s31, s46, 8\n"
        "s_sub_u32 s31, s45, s31\n"
        "v_add_u32 v247, s31, v247\n"
        "ds_read_b128 a[144:147], v247 offset:0\n"
        "ds_read_b128 a[0:3], v248 offset:0\n"
        "ds_read_b128 a[148:151], v247 offset:4096\n"
        "ds_read_b128 a[4:7], v248 offset:3072\n"
        "v_mfma_f32_16x16x32_bf16 v[80:83], v[132:135], v[92:95], 0\n"
        "ds_read_b128 a[152:155], v247 offset:8192\n"
        "ds_read_b128 a[8:11], v248 offset:6144\n"
        "s_mul_i32 s45, s46, 786432\n"
        "v_mov_b64 v[124:125], 0\n"
        "v_mfma_f32_16x16x32_bf16 v[80:83], v[128:131], v[88:91], v[80:83]\n"
        "ds_read_b128 a[156:159], v247 offset:12288\n"
        "ds_read_b128 a[12:15], v248 offset:9216\n"
        "s_sub_u32 s37, s37, s45\n"
        "v_cvt_pk_bf16_f32 v232, v176, v180\n"
        "v_cvt_pk_bf16_f32 v233, v184, v172\n"
        "v_mfma_f32_16x16x32_bf16 v[84:87], v[136:139], v[88:91], 0\n"
        "ds_read_b128 a[160:163], v247 offset:16384\n"
        "ds_read_b128 a[16:19], v248 offset:12288\n"
        "s_cmp_eq_u32 s46, 0\n"
        "s_cselect_b32 s43, s43, s44\n"
        "v_cvt_pk_bf16_f32 v234, v212, v220\n"
        "v_mfma_f32_16x16x32_bf16 v[84:87], v[140:143], v[92:95], v[84:87]\n"
        "buffer_store_dwordx3 v[232:234], v250, s[16:19], s38 offen offset:0 sc1\n"
        "s_add_u32 s28, s37, 786432\n"
        "v_mfma_f32_16x16x32_bf16 v[96:99], v[148:151], v[92:95], 0\n"
        "ds_read_b128 a[164:167], v247 offset:20480\n"
        "ds_read_b128 a[20:23], v248 offset:15360\n"
        "ds_read_b128 a[44:47], v254 offset:15376\n"
        "s_cmp_eq_u32 s28, 100663296\n"
        "s_cselect_b32 s28, 0, s28\n"
        "v_mfma_f32_16x16x32_bf16 v[96:99], v[144:147], v[88:91], v[96:99]\n"
        "ds_read_b128 a[40:43], v254 offset:12304\n"
        "ds_read_b128 a[36:39], v254 offset:9232\n"
        "s_lshl_b32 s29, s42, 2\n"
        "v_cvt_pk_bf16_f32 v232, v177, v181\n"
        "v_cvt_pk_bf16_f32 v233, v185, v173\n"
        "v_smfmac_f32_16x16x64_bf16 v[124:127], a[252:255], v[88:95], v237\n"
        "s_waitcnt lgkmcnt(13)\n"
        "ds_read_b128 a[32:35], v254 offset:6160\n"
        "v_cvt_pk_bf16_f32 v234, v213, v221\n"
        "v_mfma_f32_16x16x32_bf16 v[0:3], a[144:147], a[0:3], 0\n"
        "s_waitcnt lgkmcnt(12)\n"
        "buffer_store_dwordx3 v[232:234], v250, s[16:19], s38 offen offset:384 sc1\n"
        "s_add_u32 s29, s29, 147456\n"
        "v_mfma_f32_16x16x32_bf16 v[0:3], a[148:151], a[4:7], v[0:3]\n"
        "s_waitcnt lgkmcnt(10)\n"
        "ds_read_b128 a[28:31], v254 offset:3088\n"
        "ds_read_b128 a[24:27], v254 offset:16\n"
        "v_cvt_pk_bf16_f32 v232, v178, v182\n"
        "v_cvt_pk_bf16_f32 v233, v186, v174\n"
        "v_mfma_f32_16x16x32_bf16 v[0:3], a[152:155], a[8:11], v[0:3]\n"
        "s_waitcnt lgkmcnt(10)\n"
        "ds_read_b128 a[48:51], v248 offset:32\n"
        "ds_read_b128 a[52:55], v248 offset:3104\n"
        "v_cvt_pk_bf16_f32 v234, v214, v222\n"
        "v_mfma_f32_16x16x32_bf16 v[0:3], a[156:159], a[12:15], v[0:3]\n"
        "s_waitcnt lgkmcnt(10)\n"
        "buffer_store_dwordx3 v[232:234], v250, s[16:19], s38 offen offset:768 sc1\n"
        "v_mfma_f32_16x16x32_bf16 v[0:3], a[160:163], a[16:19], v[0:3]\n"
        "s_waitcnt lgkmcnt(8)\n"
        "ds_read_b128 a[56:59], v248 offset:6176\n"
        "ds_read_b128 a[60:63], v248 offset:9248\n"
        "v_cvt_pk_bf16_f32 v232, v179, v183\n"
        "v_cvt_pk_bf16_f32 v233, v187, v175\n"
        "v_mfma_f32_16x16x32_bf16 v[0:3], a[164:167], a[20:23], v[0:3]\n"
        "s_waitcnt lgkmcnt(9)\n"
        "ds_read_b128 a[64:67], v248 offset:12320\n"
        "ds_read_b128 a[68:71], v248 offset:15392\n"
        "v_cvt_pk_bf16_f32 v234, v215, v223\n"
        "v_add_f32 v125, v124, v125\n"
        "v_mfma_f32_16x16x32_bf16 v[4:7], a[164:167], a[44:47], 0\n"
        "s_waitcnt lgkmcnt(10)\n"
        "buffer_store_dwordx3 v[232:234], v250, s[16:19], s38 offen offset:1152 sc1\n"
        "v_mfma_f32_16x16x32_bf16 v[4:7], a[160:163], a[40:43], v[4:7]\n"
        "s_waitcnt lgkmcnt(9)\n"
        "ds_read_b128 a[92:95], v254 offset:15408\n"
        "ds_read_b128 a[88:91], v254 offset:12336\n"
        "v_rcp_f32 v124, v125\n"
        "v_mfma_f32_16x16x32_bf16 v[4:7], a[156:159], a[36:39], v[4:7]\n"
        "s_waitcnt lgkmcnt(10)\n"
        "ds_read_b128 a[84:87], v254 offset:9264\n"
        "ds_read_b128 a[80:83], v254 offset:6192\n"
        "v_mul_f32 v80, v124, v80\n"
        "v_mul_f32 v81, v124, v81\n"
        "v_mfma_f32_16x16x32_bf16 v[4:7], a[152:155], a[32:35], v[4:7]\n"
        "s_waitcnt lgkmcnt(11)\n"
        "ds_read_b128 a[76:79], v254 offset:3120\n"
        "ds_read_b128 a[72:75], v254 offset:48\n"
        "v_mul_f32 v82, v124, v82\n"
        "v_mul_f32 v83, v124, v83\n"
        "v_mfma_f32_16x16x32_bf16 v[4:7], a[148:151], a[28:31], v[4:7]\n"
        "s_waitcnt lgkmcnt(12)\n"
        "ds_read_b128 a[96:99], v248 offset:64\n"
        "ds_read_b128 a[100:103], v248 offset:3136\n"
        "v_cvt_pk_bf16_f32 v232, v196, v200\n"
        "v_cvt_pk_bf16_f32 v233, v204, v192\n"
        "v_mfma_f32_16x16x32_bf16 v[4:7], a[144:147], a[24:27], v[4:7]\n"
        "s_waitcnt lgkmcnt(13)\n"
        "ds_read_b128 a[104:107], v248 offset:6208\n"
        "ds_read_b128 a[108:111], v248 offset:9280\n"
        "v_cvt_pk_bf16_f32 v234, v216, v224\n"
        "v_cvt_pk_bf16_f32 v80, v80, v81\n"
        "v_mfma_f32_16x16x32_bf16 v[8:11], a[144:147], a[48:51], 0\n"
        "s_waitcnt lgkmcnt(14)\n"
        "buffer_store_dwordx3 v[232:234], v250, s[16:19], s39 offen offset:0 sc1\n"
        "v_mfma_f32_16x16x32_bf16 v[8:11], a[148:151], a[52:55], v[8:11]\n"
        "s_waitcnt lgkmcnt(13)\n"
        "ds_read_b128 a[112:115], v248 offset:12352\n"
        "ds_read_b128 a[116:119], v248 offset:15424\n"
        "v_cvt_pk_bf16_f32 v81, v82, v83\n"
        "v_mul_f32 v84, v124, v84\n"
        "v_mfma_f32_16x16x32_bf16 v[8:11], a[152:155], a[56:59], v[8:11]\n"
        "s_waitcnt lgkmcnt(14)\n"
        "ds_read_b128 a[140:143], v254 offset:15440\n"
        "ds_read_b128 a[136:139], v254 offset:12368\n"
        "v_mul_f32 v85, v124, v85\n"
        "v_mul_f32 v86, v124, v86\n"
        "v_mfma_f32_16x16x32_bf16 v[8:11], a[156:159], a[60:63], v[8:11]\n"
        "s_waitcnt lgkmcnt(15)\n"
        "ds_read_b128 a[132:135], v254 offset:9296\n"
        "ds_read_b128 a[128:131], v254 offset:6224\n"
        "v_mul_f32 v87, v124, v87\n"
        "v_cvt_pk_bf16_f32 v232, v197, v201\n"
        "v_mfma_f32_16x16x32_bf16 v[8:11], a[160:163], a[64:67], v[8:11]\n"
        "s_waitcnt lgkmcnt(15)\n"
        "ds_read_b128 a[124:127], v254 offset:3152\n"
        "ds_read_b128 a[120:123], v254 offset:80\n"
        "v_cvt_pk_bf16_f32 v233, v205, v193\n"
        "v_cvt_pk_bf16_f32 v234, v217, v225\n"
        "v_mfma_f32_16x16x32_bf16 v[8:11], a[164:167], a[68:71], v[8:11]\n"
        "s_waitcnt lgkmcnt(15)\n"
        "buffer_store_dwordx3 v[232:234], v250, s[16:19], s39 offen offset:384 sc1\n"
        "v_mfma_f32_16x16x32_bf16 v[12:15], a[164:167], a[92:95], 0\n"
        "s_waitcnt lgkmcnt(15)\n"
        "v_cvt_pk_bf16_f32 v82, v84, v85\n"
        "v_cvt_pk_bf16_f32 v83, v86, v87\n"
        "v_mul_f32 v96, v124, v96\n"
        "v_mul_f32 v97, v124, v97\n"
        "v_mfma_f32_16x16x32_bf16 v[12:15], a[160:163], a[88:91], v[12:15]\n"
        ".long 0xbec200ff\n"
        "46:\n"
        "s_branch 62b\n"
        "s_waitcnt lgkmcnt(15)\n"
        "ds_write_b128 v245, v[80:83] offset:768\n"
        "v_mfma_f32_16x16x32_bf16 v[12:15], a[156:159], a[84:87], v[12:15]\n"
        "s_waitcnt lgkmcnt(15)\n"
        "v_mul_f32 v98, v124, v98\n"
        "v_mul_f32 v99, v124, v99\n"
        "v_cvt_pk_bf16_f32 v232, v198, v202\n"
        "v_cvt_pk_bf16_f32 v233, v206, v194\n"
        "v_mfma_f32_16x16x32_bf16 v[12:15], a[152:155], a[80:83], v[12:15]\n"
        "s_waitcnt lgkmcnt(14)\n"
        "v_cvt_pk_bf16_f32 v234, v218, v226\n"
        "v_cvt_pk_bf16_f32 v96, v96, v97\n"
        "v_cvt_pk_bf16_f32 v97, v98, v99\n"
        "v_mov_b32 v100, s29\n"
        "v_mfma_f32_16x16x32_bf16 v[12:15], a[148:151], a[76:79], v[12:15]\n"
        "s_waitcnt lgkmcnt(13)\n"
        "buffer_store_dwordx3 v[232:234], v250, s[16:19], s39 offen offset:768 sc1\n"
        "v_mfma_f32_16x16x32_bf16 v[12:15], a[144:147], a[72:75], v[12:15]\n"
        "s_waitcnt lgkmcnt(12)\n"
        "ds_write_b64 v246, v[96:97] offset:768\n"
        "v_cvt_pk_bf16_f32 v232, v199, v203\n"
        "v_mfma_f32_16x16x32_bf16 v[16:19], a[144:147], a[96:99], 0\n"
        "s_waitcnt lgkmcnt(12)\n"
        "v_cvt_pk_bf16_f32 v233, v207, v195\n"
        "v_cvt_pk_bf16_f32 v234, v219, v227\n"
        "v_max3_u32 v236, v236, v124, v125\n"
        "v_cmp_lt_u32 vcc, s24, v236\n"
        "v_mfma_f32_16x16x32_bf16 v[16:19], a[148:151], a[100:103], v[16:19]\n"
        "s_waitcnt lgkmcnt(11)\n"
        "buffer_store_dwordx3 v[232:234], v250, s[16:19], s39 offen offset:1152 sc1\n"
        "s_cmp_lg_u64 vcc, 0\n"
        "s_addc_u32 s25, s25, s25\n"
        "v_mfma_f32_16x16x32_bf16 v[16:19], a[152:155], a[104:107], v[16:19]\n"
        "s_waitcnt lgkmcnt(10)\n"
        "v_mov_b32 v101, s25\n"
        "v_mov_b32 v102, 147456\n"
        "v_mfma_f32_16x16x32_bf16 v[16:19], a[156:159], a[108:111], v[16:19]\n"
        "ds_write_b32 v100, v101\n"
        "s_waitcnt lgkmcnt(0)\n"
        "s_barrier\n"
        "ds_read_b128 a[168:171], v247 offset:512\n"
        "v_mfma_f32_16x16x32_bf16 v[16:19], a[160:163], a[112:115], v[16:19]\n"
        "ds_read_b128 a[172:175], v247 offset:4608\n"
        "ds_read_b128 a[176:179], v247 offset:8704\n"
        "v_mfma_f32_16x16x32_bf16 v[16:19], a[164:167], a[116:119], v[16:19]\n"
        "ds_read_b128 a[180:183], v247 offset:12800\n"
        "ds_read_b128 a[184:187], v247 offset:16896\n"
        "v_mfma_f32_16x16x32_bf16 v[20:23], a[164:167], a[140:143], 0\n"
        "ds_read_b128 a[188:191], v247 offset:20992\n"
        "v_cvt_pk_bf16_f32 v48, v0, v4\n"
        "v_mfma_f32_16x16x32_bf16 v[20:23], a[160:163], a[136:139], v[20:23]\n"
        "ds_read_b128 v[104:107], v102\n"
        "v_cvt_pk_bf16_f32 v49, v8, v12\n"
        "v_mfma_f32_16x16x32_bf16 v[20:23], a[156:159], a[132:135], v[20:23]\n"
        "s_add_u32 s38, s37, s43\n"
        "v_cvt_pk_bf16_f32 v52, v1, v5\n"
        "v_cvt_pk_bf16_f32 v53, v9, v13\n"
        "v_mfma_f32_16x16x32_bf16 v[20:23], a[152:155], a[128:131], v[20:23]\n"
        "s_add_u32 s39, s28, s43\n"
        "v_cvt_pk_bf16_f32 v56, v2, v6\n"
        "v_cvt_pk_bf16_f32 v57, v10, v14\n"
        "v_mfma_f32_16x16x32_bf16 v[20:23], a[148:151], a[124:127], v[20:23]\n"
        "v_cvt_pk_bf16_f32 v60, v3, v7\n"
        "v_cvt_pk_bf16_f32 v61, v11, v15\n"
        "v_mfma_f32_16x16x32_bf16 v[20:23], a[144:147], a[120:123], v[20:23]\n"
        "s_waitcnt lgkmcnt(6)\n"
        "v_mfma_f32_16x16x32_bf16 v[24:27], a[168:171], a[0:3], 0\n"
        "s_waitcnt lgkmcnt(5)\n"
        "v_mfma_f32_16x16x32_bf16 v[24:27], a[172:175], a[4:7], v[24:27]\n"
        "s_waitcnt lgkmcnt(4)\n"
        "v_mfma_f32_16x16x32_bf16 v[24:27], a[176:179], a[8:11], v[24:27]\n"
        "s_waitcnt lgkmcnt(3)\n"
        "v_mfma_f32_16x16x32_bf16 v[24:27], a[180:183], a[12:15], v[24:27]\n"
        "s_waitcnt lgkmcnt(2)\n"
        "v_mfma_f32_16x16x32_bf16 v[24:27], a[184:187], a[16:19], v[24:27]\n"
        "s_waitcnt lgkmcnt(1)\n"
        "v_mfma_f32_16x16x32_bf16 v[24:27], a[188:191], a[20:23], v[24:27]\n"
        "v_mfma_f32_16x16x32_bf16 v[28:31], a[188:191], a[44:47], 0\n"
        "s_nop 0\n"
        "v_cvt_pk_bf16_f32 v50, v16, v20\n"
        "v_cvt_pk_bf16_f32 v54, v17, v21\n"
        "v_mfma_f32_16x16x32_bf16 v[28:31], a[184:187], a[40:43], v[28:31]\n"
        "v_cvt_pk_bf16_f32 v58, v18, v22\n"
        "v_cvt_pk_bf16_f32 v62, v19, v23\n"
        "v_mfma_f32_16x16x32_bf16 v[28:31], a[180:183], a[36:39], v[28:31]\n"
        "v_mfma_f32_16x16x32_bf16 v[28:31], a[176:179], a[32:35], v[28:31]\n"
        "s_waitcnt lgkmcnt(0)\n"
        "v_or3_b32 v104, v104, v105, v106\n"
        "v_or_b32 v104, v104, v107\n"
        "v_mfma_f32_16x16x32_bf16 v[28:31], a[172:175], a[28:31], v[28:31]\n"
        "s_nop 0\n"
        "v_readfirstlane_b32 s25, v104\n"
        "v_mfma_f32_16x16x32_bf16 v[28:31], a[168:171], a[24:27], v[28:31]\n"
        "v_mfma_f32_16x16x32_bf16 v[32:35], a[168:171], a[48:51], 0\n"
        "v_mfma_f32_16x16x32_bf16 v[32:35], a[172:175], a[52:55], v[32:35]\n"
        "buffer_store_dwordx3 v[48:50], v250, s[16:19], s38 offen offset:0 sc1\n"
        "v_mfma_f32_16x16x32_bf16 v[32:35], a[176:179], a[56:59], v[32:35]\n"
        "v_mfma_f32_16x16x32_bf16 v[32:35], a[180:183], a[60:63], v[32:35]\n"
        "v_mfma_f32_16x16x32_bf16 v[32:35], a[184:187], a[64:67], v[32:35]\n"
        "v_mfma_f32_16x16x32_bf16 v[32:35], a[188:191], a[68:71], v[32:35]\n"
        "v_mfma_f32_16x16x32_bf16 v[36:39], a[188:191], a[92:95], 0\n"
        "buffer_store_dwordx3 v[52:54], v250, s[16:19], s38 offen offset:384 sc1\n"
        "v_mfma_f32_16x16x32_bf16 v[36:39], a[184:187], a[88:91], v[36:39]\n"
        "v_cvt_pk_bf16_f32 v64, v24, v28\n"
        "v_cvt_pk_bf16_f32 v68, v25, v29\n"
        "v_mfma_f32_16x16x32_bf16 v[36:39], a[180:183], a[84:87], v[36:39]\n"
        "v_cvt_pk_bf16_f32 v72, v26, v30\n"
        "v_cvt_pk_bf16_f32 v76, v27, v31\n"
        "v_mfma_f32_16x16x32_bf16 v[36:39], a[176:179], a[80:83], v[36:39]\n"
        "v_mfma_f32_16x16x32_bf16 v[36:39], a[172:175], a[76:79], v[36:39]\n"
        "v_mfma_f32_16x16x32_bf16 v[36:39], a[168:171], a[72:75], v[36:39]\n"
        "buffer_store_dwordx3 v[56:58], v250, s[16:19], s38 offen offset:768 sc1\n"
        "v_mfma_f32_16x16x32_bf16 v[40:43], a[168:171], a[96:99], 0\n"
        "v_mfma_f32_16x16x32_bf16 v[40:43], a[172:175], a[100:103], v[40:43]\n"
        "v_mfma_f32_16x16x32_bf16 v[40:43], a[176:179], a[104:107], v[40:43]\n"
        "v_mfma_f32_16x16x32_bf16 v[40:43], a[180:183], a[108:111], v[40:43]\n"
        "v_mfma_f32_16x16x32_bf16 v[40:43], a[184:187], a[112:115], v[40:43]\n"
        "buffer_store_dwordx3 v[60:62], v250, s[16:19], s38 offen offset:1152 sc1\n"
        "v_mfma_f32_16x16x32_bf16 v[40:43], a[188:191], a[116:119], v[40:43]\n"
        "v_cvt_pk_bf16_f32 v65, v32, v36\n"
        "v_cvt_pk_bf16_f32 v69, v33, v37\n"
        "v_mfma_f32_16x16x32_bf16 v[44:47], a[188:191], a[140:143], 0\n"
        "v_cvt_pk_bf16_f32 v73, v34, v38\n"
        "v_cvt_pk_bf16_f32 v77, v35, v39\n"
        "v_mfma_f32_16x16x32_bf16 v[44:47], a[184:187], a[136:139], v[44:47]\n"
        "v_mfma_f32_16x16x32_bf16 v[44:47], a[180:183], a[132:135], v[44:47]\n"
        "v_mfma_f32_16x16x32_bf16 v[44:47], a[176:179], a[128:131], v[44:47]\n"
        "v_mfma_f32_16x16x32_bf16 v[44:47], a[172:175], a[124:127], v[44:47]\n"
        "v_mfma_f32_16x16x32_bf16 v[44:47], a[168:171], a[120:123], v[44:47]\n"
        "s_nop 2\n"
        "s_nop 4\n"
        "v_cvt_pk_bf16_f32 v66, v40, v44\n"
        "buffer_store_dwordx3 v[64:66], v250, s[16:19], s39 offen offset:0 sc1\n"
        "v_cvt_pk_bf16_f32 v70, v41, v45\n"
        "buffer_store_dwordx3 v[68:70], v250, s[16:19], s39 offen offset:384 sc1\n"
        "v_cvt_pk_bf16_f32 v74, v42, v46\n"
        "buffer_store_dwordx3 v[72:74], v250, s[16:19], s39 offen offset:768 sc1\n"
        "v_cvt_pk_bf16_f32 v78, v43, v47\n"
        "buffer_store_dwordx3 v[76:78], v250, s[16:19], s39 offen offset:1152 sc1\n"
        "s_cmp_lg_u32 s25, 0\n"
        "s_cbranch_scc1 30f\n"
        "s_endpgm\n"
        "30:\n"
        : "={s25}"(flg), "={s42}"(w), "+{s[0:1]}"(ka), "+{s2}"(bid), "+{v0}"(tid)
        : "s"(ldsb)
        : "v1", "v2", "v3", "v4", "v5", "v6", "v7", "v8", "v9", "v10", "v11", "v12", "v13", "v14", "v15", "v16", "v17", "v18", "v19", "v20", "v21", "v22", "v23", "v24", "v25", "v26", "v27", "v28", "v29", "v30", "v31", "v32", "v33", "v34", "v35", "v36", "v37", "v38", "v39", "v40", "v41", "v42", "v43", "v44", "v45", "v46", "v47", "v48", "v49", "v50", "v51", "v52", "v53", "v54", "v55", "v56", "v57", "v58", "v59", "v60", "v61", "v62", "v63", "v64", "v65", "v66", "v67", "v68", "v69", "v70", "v71", "v72", "v73", "v74", "v75", "v76", "v77", "v78", "v79", "v80", "v81", "v82", "v83", "v84", "v85", "v86", "v87", "v88", "v89", "v90", "v91", "v92", "v93", "v94", "v95", "v96", "v97", "v98", "v99", "v100", "v101", "v102", "v103", "v104", "v105", "v106", "v107", "v108", "v109", "v110", "v111", "v112", "v113", "v114", "v115", "v116", "v117", "v118", "v119", "v120", "v121", "v122", "v123", "v124", "v125", "v126", "v127", "v128", "v129", "v130", "v131", "v132", "v133", "v134", "v135", "v136", "v137", "v138", "v139", "v140", "v141", "v142", "v143", "v144", "v145", "v146", "v147", "v148", "v149", "v150", "v151", "v152", "v153", "v154", "v155", "v156", "v157", "v158", "v159", "v160", "v161", "v162", "v163", "v164", "v165", "v166", "v167", "v168", "v169", "v170", "v171", "v172", "v173", "v174", "v175", "v176", "v177", "v178", "v179", "v180", "v181", "v182", "v183", "v184", "v185", "v186", "v187", "v188", "v189", "v190", "v191", "v192", "v193", "v194", "v195", "v196", "v197", "v198", "v199", "v200", "v201", "v202", "v203", "v204", "v205", "v206", "v207", "v208", "v209", "v210", "v211", "v212", "v213", "v214", "v215", "v216", "v217", "v218", "v219", "v220", "v221", "v222", "v223", "v224", "v225", "v226", "v227", "v228", "v229", "v230", "v231", "v232", "v233", "v234", "v235", "v236", "v237", "v238", "v239", "v240", "v241", "v242", "v243", "v244", "v245", "v246", "v247", "v248", "v249", "v250", "v251", "v252", "v253", "v254", "v255", "a0", "a1", "a2", "a3", "a4", "a5", "a6", "a7", "a8", "a9", "a10", "a11", "a12", "a13", "a14", "a15", "a16", "a17", "a18", "a19", "a20", "a21", "a22", "a23", "a24", "a25", "a26", "a27", "a28", "a29", "a30", "a31", "a32", "a33", "a34", "a35", "a36", "a37", "a38", "a39", "a40", "a41", "a42", "a43", "a44", "a45", "a46", "a47", "a48", "a49", "a50", "a51", "a52", "a53", "a54", "a55", "a56", "a57", "a58", "a59", "a60", "a61", "a62", "a63", "a64", "a65", "a66", "a67", "a68", "a69", "a70", "a71", "a72", "a73", "a74", "a75", "a76", "a77", "a78", "a79", "a80", "a81", "a82", "a83", "a84", "a85", "a86", "a87", "a88", "a89", "a90", "a91", "a92", "a93", "a94", "a95", "a96", "a97", "a98", "a99", "a100", "a101", "a102", "a103", "a104", "a105", "a106", "a107", "a108", "a109", "a110", "a111", "a112", "a113", "a114", "a115", "a116", "a117", "a118", "a119", "a120", "a121", "a122", "a123", "a124", "a125", "a126", "a127", "a128", "a129", "a130", "a131", "a132", "a133", "a134", "a135", "a136", "a137", "a138", "a139", "a140", "a141", "a142", "a143", "a144", "a145", "a146", "a147", "a148", "a149", "a150", "a151", "a152", "a153", "a154", "a155", "a156", "a157", "a158", "a159", "a160", "a161", "a162", "a163", "a164", "a165", "a166", "a167", "a168", "a169", "a170", "a171", "a172", "a173", "a174", "a175", "a176", "a177", "a178", "a179", "a180", "a181", "a182", "a183", "a184", "a185", "a186", "a187", "a188", "a189", "a190", "a191", "a192", "a193", "a194", "a195", "a196", "a197", "a198", "a199", "a200", "a201", "a202", "a203", "a204", "a205", "a206", "a207", "a208", "a209", "a210", "a211", "a212", "a213", "a214", "a215", "a216", "a217", "a218", "a219", "a220", "a221", "a222", "a223", "a224", "a225", "a226", "a227", "a228", "a229", "a230", "a231", "a232", "a233", "a234", "a235", "a236", "a237", "a238", "a239", "a240", "a241", "a242", "a243", "a244", "a245", "a246", "a247", "a248", "a249", "a250", "a251", "a252", "a253", "a254", "a255", "s8", "s9", "s10", "s11", "s12", "s13", "s14", "s15", "s16", "s17", "s18", "s19", "s20", "s21", "s22", "s23", "s24", "s26", "s27", "s28", "s29", "s30", "s31", "s34", "s35", "s36", "s37", "s38", "s39", "s40", "s41", "s43", "s44", "s45", "s46", "s47", "s48", "s49", "s50", "s51", "s52", "s53", "s54", "s55", "s56", "s57", "s58", "s59", "s60", "s61", "s62", "s63", "s64", "s65", "s66", "s67", "s68", "s69", "s70", "s71", "s72", "s73", "s74", "s75", "s76", "s77", "s78", "s79", "s80", "s81", "s82", "s83", "vcc", "scc", "memory");
    if (flg) {
        unsigned ln;
        asm volatile("s_waitcnt vmcnt(0)\n v_mbcnt_lo_u32_b32 %0, -1, 0\n v_mbcnt_hi_u32_b32 %0, -1, %0" : "=v"(ln) :: "memory");
        const unsigned t = w * 64u + ln, wx = bid & 63u, wy0 = bid >> 6;
        typedef const char* __attribute__((address_space(4))) const* kargp;
        const char *x = ((kargp)ka)[0], *wqkv = ((kargp)ka)[1], *wo = ((kargp)ka)[2], *rpb = ((kargp)ka)[3], *rpi = ((kargp)ka)[4],
                   *mask = ((kargp)ka)[5];
        char* out = (char*)((kargp)ka)[6];
        unsigned short* xs = (unsigned short*)smem;
        unsigned short* qs = xs + 64 * 192;
        const unsigned short* W1 = (const unsigned short*)wqkv;
        const unsigned short* W2 = (const unsigned short*)wo;
        for (unsigned i = 0; i < 16u; ++i) {
            if (!((flg >> (15u - i)) & 1u)) continue;
            const unsigned wy = wy0 + 4u * i;
            __syncthreads();
            for (unsigned e = t; e < 64u * 96u; e += 256u) {
                const unsigned tok = e / 96u, c2 = e % 96u;
                const unsigned pix = ((8u * wy + 4u + (tok >> 3)) & 511u) * 512u + ((8u * wx + 4u + (tok & 7u)) & 511u);
                ((unsigned*)xs)[e] = ((const unsigned*)x)[pix * 96u + c2];
            }
            __syncthreads();
            for (unsigned col = t; col < 576u; col += 256u)
                for (unsigned tb = 0; tb < 64u; tb += 8u) {
                    float a[8] = {0.f, 0.f, 0.f, 0.f, 0.f, 0.f, 0.f, 0.f};
                    for (unsigned c = 0; c < 192u; ++c) {
                        const float wt = bf2f(W1[c * 576u + col]);
#pragma unroll
                        for (int j = 0; j < 8; ++j) a[j] += bf2f(xs[(tb + j) * 192u + c]) * wt;
                    }
#pragma unroll
                    for (int j = 0; j < 8; ++j) qs[(tb + j) * 578u + col] = f2bf(a[j]);
                }
            __syncthreads();
            {
                float qv[48], o[48];
#pragma unroll
                for (int d = 0; d < 48; ++d) { qv[d] = bf2f(qs[ln * 578u + w * 48u + d]); o[d] = 0.f; }
                const unsigned short* mrow = (const unsigned short*)mask + (size_t)(wy * 64u + wx) * 4096u + ln * 64u;
                float mx = -__builtin_inff(), l = 0.f;
                for (int pass = 0; pass < 2; ++pass)
                    for (unsigned k = 0; k < 64u; ++k) {
                        float s = 0.f;
#pragma unroll
                        for (int d = 0; d < 48; ++d) s += qv[d] * bf2f(qs[k * 578u + 192u + w * 48u + d]);
                        s = s * 0.14433756729740643f + bf2f(((const unsigned short*)rpb)[((const unsigned*)rpi)[ln * 64u + k] * 4u + w]) + bf2f(mrow[k]);
                        if (pass == 0) mx = fmaxf(mx, s);
                        else {
                            const float p = __expf(s - mx);
                            l += p;
#pragma unroll
                            for (int d = 0; d < 48; ++d) o[d] += p * bf2f(qs[k * 578u + 384u + w * 48u + d]);
                        }
                    }
#pragma unroll
                for (int d = 0; d < 48; ++d) xs[ln * 192u + w * 48u + d] = f2bf(o[d] / l);
            }
            __syncthreads();
            if (t < 192u)
                for (unsigned tb = 0; tb < 64u; tb += 8u) {
                    float a[8] = {0.f, 0.f, 0.f, 0.f, 0.f, 0.f, 0.f, 0.f};
                    for (unsigned c = 0; c < 192u; ++c) {
                        const float wt = bf2f(W2[c * 192u + t]);
#pragma unroll
                        for (int j = 0; j < 8; ++j) a[j] += bf2f(xs[(tb + j) * 192u + c]) * wt;
                    }
                    const unsigned row = (8u * wy + 4u + (tb >> 3)) & 511u;
#pragma unroll
                    for (int j = 0; j < 8; ++j)
                        ((unsigned short*)out)[(row * 512u + ((8u * wx + 4u + j) & 511u)) * 192u + t] = f2bf(a[j]);
                }
        }
    }
}


extern "C" void kernel_launch(void* const* d_in, const int* in_sizes, int n_in, void* d_out, int out_size, void* d_ws,
                              size_t ws_size, hipStream_t stream) {
    win_attn_kernel<<<256, 256, 0, stream>>>((const char*)d_in[0], (const char*)d_in[1], (const char*)d_in[2],
                                             (const char*)d_in[3], (const char*)d_in[4], (const char*)d_in[5],
                                             (char*)d_out, (char*)d_ws);

}
```

```cpp
#include <hip/hip_runtime.h>
#include <hip/hip_bf16.h>

typedef unsigned int u32x4 __attribute__((ext_vector_type(4)));

static __device__ __forceinline__ float bf2f(unsigned short u) { return __uint_as_float((unsigned)u << 16); }
static __device__ __forceinline__ unsigned short f2bf(float f) { return __builtin_bit_cast(unsigned short, (__bf16)f); }

__global__ __launch_bounds__(256, 1) __attribute__((aligned(4096))) void win_attn_kernel(const char* x_, const char* wqkv_, const char* wo_, const char* rpb_,
                                                          const char* rpi_, const char* mask_, char* out_, char* ws) {
    __shared__ __attribute__((aligned(16))) char smem[163840];
    const void* ka = (const void*)__builtin_amdgcn_kernarg_segment_ptr();
    unsigned bid = blockIdx.x, tid = threadIdx.x, flg, w;
    unsigned ldsb = (unsigned)(size_t)smem;
    asm volatile(
        "s_load_dwordx16 s[68:83], s[0:1], 0\n"
        "v_readfirstlane_b32 s42, v0\n"
        "v_and_b32 v1, 63, v0\n"
        "s_lshr_b32 s31, s2, 1\n"
        "s_lshr_b32 s42, s42, 6\n"
        "s_and_b32 s31, s31, 0x7c\n"
        "v_lshlrev_b32 v2, 4, v1\n"
        "s_add_u32 s31, s31, s42\n"
        "s_and_b32 s35, s31, 31\n"
        "s_min_u32 s35, s35, 31\n"
        "s_getpc_b64 s[66:67]\n"
        "v_lshl_or_b32 v10, s35, 10, v2\n"
        "global_load_dwordx4 v[60:63], v10, s[66:67]\n"
        "s_lshl_b32 s50, s31, 6\n"
        "s_mul_i32 s50, s50, 27\n"
        "v_min_u32 v136, 26, v1\n"
        "v_lshl_add_u32 v136, v136, 6, s50\n"
        "v_mul_u32_u24 v11, 171, v1\n"
        "v_lshrrev_b32 v4, 10, v11\n"
        "v_bfe_u32 v7, v11, 13, 1\n"
        "v_add_u32 v11, 10944, v11\n"
        "v_lshrrev_b32 v5, 10, v11\n"
        "v_bfe_u32 v8, v11, 13, 1\n"
        "v_add_u32 v11, 10944, v11\n"
        "v_lshrrev_b32 v6, 10, v11\n"
        "v_bfe_u32 v9, v11, 13, 1\n"
        "s_mul_i32 s40, s42, 3072\n"
        "s_mov_b32 s10, 100663296\n"
        "s_mov_b32 s11, 0x20000\n"
        "s_mov_b32 s58, 16384\n"
        "s_mov_b32 s59, 0x20000\n"
        "s_mov_b32 s62, 1800\n"
        "s_mov_b32 s63, 0x20000\n"
        "s_mov_b32 s22, 221184\n"
        "s_mov_b32 s23, 0x20000\n"
        "s_and_b32 s51, s42, 1\n"
        "v_lshlrev_b32 v134, 3, v0\n"
        "s_lshl_b32 s35, s51, 7\n"
        "v_and_b32 v10, 48, v1\n"
        "v_xor_b32 v131, s35, v2\n"
        "s_lshl_b32 s46, s42, 12\n"
        "v_xor_b32 v131, v131, v10\n"
        "v_xor_b32 v132, 64, v131\n"
        "s_and_b32 s31, s2, 63\n"
        "s_lshl_b32 s31, s31, 3\n"
        "s_add_u32 s31, s31, 4\n"
        "s_lshl_b32 s35, s42, 5\n"
        "s_movk_i32 s41, 384\n"
        "s_movk_i32 s45, -384\n"
        "v_lshrrev_b32 v10, 2, v4\n"
        "v_mad_i32_i24 v11, v10, s45, v2\n"
        "v_add_u32 v10, s31, v10\n"
        "v_lshl_or_b32 v242, v7, 4, s35\n"
        "v_and_b32 v10, 511, v10\n"
        "v_xor_b32 v11, v11, v242\n"
        "v_mad_u32_u24 v242, v10, s41, v11\n"
        "v_lshrrev_b32 v10, 2, v5\n"
        "v_mad_i32_i24 v11, v10, s45, v2\n"
        "v_add_u32 v10, s31, v10\n"
        "v_add_u32 v11, 1024, v11\n"
        "v_lshl_or_b32 v243, v8, 4, s35\n"
        "v_and_b32 v10, 511, v10\n"
        "v_xor_b32 v11, v11, v243\n"
        "v_mad_u32_u24 v243, v10, s41, v11\n"
        "v_lshrrev_b32 v10, 2, v6\n"
        "v_mad_i32_i24 v11, v10, s45, v2\n"
        "v_add_u32 v10, s31, v10\n"
        "v_add_u32 v11, 2048, v11\n"
        "v_lshl_or_b32 v244, v9, 4, s35\n"
        "v_and_b32 v10, 511, v10\n"
        "v_xor_b32 v11, v11, v244\n"
        "v_mad_u32_u24 v244, v10, s41, v11\n"
        "s_mul_i32 s43, s31, 384\n"
        "s_add_u32 s31, s31, 4\n"
        "s_and_b32 s31, s31, 511\n"
        "s_mul_i32 s44, s31, 384\n"
        "s_lshr_b32 s31, s2, 6\n"
        "s_lshl_b32 s31, s31, 3\n"
        "s_add_u32 s47, s31, s42\n"
        "s_add_u32 s47, s47, 68\n"
        "s_mul_i32 s47, s47, 196608\n"
        "s_lshl2_add_u32 s37, s51, s31\n"
        "s_add_u32 s37, s37, 4\n"
        "s_mul_i32 s37, s37, 196608\n"
        "s_mul_i32 s65, s42, 18432\n"
        "s_mov_b32 s14, 33554432\n"
        "s_mov_b32 s15, 0x20000\n"
        "s_mul_i32 s35, s42, 96\n"
        "s_movk_i32 s31, 1056\n"
        "s_movk_i32 s45, 4608\n"
        "v_add_u32 v3, s35, v2\n"
        "s_waitcnt lgkmcnt(0)\n"
        "s_mov_b64 s[60:61], s[74:75]\n"
        "s_mov_b64 s[20:21], s[70:71]\n"
        "s_mov_b64 s[56:57], s[76:77]\n"
        "s_mov_b64 s[8:9], s[68:69]\n"
        "buffer_load_dwordx2 v[144:145], v134, s[60:63], 0 offen\n"
        "s_add_u32 m0, s46, 122880\n"
        "s_mov_b32 s64, s46\n"
        "buffer_load_dword v136, s[20:23], 0 offen lds\n"
        "s_mov_b64 s[52:53], s[72:73]\n"
        "s_mov_b64 s[12:13], s[78:79]\n"
        "buffer_load_dwordx4 v131, s[56:59], s64 offen lds\n"
        "s_mov_b64 s[16:17], s[80:81]\n"
        "v_bfe_u32 v12, v4, 2, 1\n"
        "v_mad_u32_u24 v128, v4, s31, v3\n"
        "v_sub_u32 v12, v12, v7\n"
        "v_mad_i32_i24 v128, v12, s45, v128\n"
        "v_bfe_u32 v13, v5, 2, 1\n"
        "buffer_load_dwordx4 v131, s[56:59], s64 offen offset:1024 lds\n"
        "v_mad_u32_u24 v129, v5, s31, v3\n"
        "v_sub_u32 v13, v13, v8\n"
        "v_mad_i32_i24 v129, v13, s45, v129\n"
        "v_bfe_u32 v14, v6, 2, 1\n"
        "v_mad_u32_u24 v130, v6, s31, v3\n"
        "v_sub_u32 v14, v14, v9\n"
        "v_mad_i32_i24 v130, v14, s45, v130\n"
        "s_lshl_b32 s48, s42, 11\n"
        "s_lshl_b32 s36, s2, 13\n"
        "v_and_b32 v19, 16, v1\n"
        "buffer_load_dwordx4 v132, s[56:59], s64 offen offset:2048 lds\n"
        "s_add_u32 s36, s36, s48\n"
        "s_add_u32 s36, s36, 4194304\n"
        "s_add_u32 s48, s48, 155648\n"
        "s_lshl_b32 s35, s51, 6\n"
        "v_xor_b32 v249, v2, v19\n"
        "v_xor_b32 v249, s35, v249\n"
        "v_xor_b32 v253, 32, v249\n"
        "v_and_b32 v15, 15, v1\n"
        "v_lshrrev_b32 v16, 4, v1\n"
        "v_and_b32 v17, 12, v1\n"
        "buffer_load_dwordx4 v132, s[56:59], s64 offen offset:3072 lds\n"
        "v_and_b32 v18, 0xf0, v2\n"
        "v_add_u32 v17, v17, v15\n"
        "v_bfe_u32 v10, v1, 2, 3\n"
        "v_lshrrev_b32 v11, 5, v1\n"
        "v_lshl_or_b32 v10, v11, 4, v10\n"
        "v_and_b32 v255, 3, v1\n"
        "v_mul_u32_u24 v10, 96, v10\n"
        "v_lshl_add_u32 v255, v255, 3, s65\n"
        "v_add_u32 v255, v255, v10\n"
        "v_lshrrev_b32 v20, 1, v15\n"
        "s_add_u32 m0, s40, 73728\n"
        "s_sub_u32 s64, s47, 12582912\n"
        "buffer_load_dwordx4 v242, s[8:11], s64 offen lds\n"
        "v_xor_b32 v20, v20, v16\n"
        "v_lshlrev_b32 v20, 4, v20\n"
        "v_mad_u32_u24 v240, v17, s41, v20\n"
        "v_lshl_or_b32 v251, v17, 7, v20\n"
        "v_add_u32 v240, 73728, v240\n"
        "v_add_u32 v251, 155648, v251\n"
        "v_xor_b32 v241, 64, v240\n"
        "v_xor_b32 v252, 64, v251\n"
        "s_lshl_b32 s67, s42, 10\n"
        "s_add_u32 s67, s67, 139264\n"
        "s_add_u32 m0, s40, 74752\n"
        "s_nop 0\n"
        "buffer_load_dwordx4 v243, s[8:11], s64 offen lds\n"
        "v_lshlrev_b32 v135, 2, v0\n"
        "v_add_u32 v135, 139264, v135\n"
        "v_lshlrev_b32 v11, 5, v16\n"
        "v_xor_b32 v11, v11, v18\n"
        "v_lshl_or_b32 v11, v17, 8, v11\n"
        "v_add_u32 v140, 122880, v11\n"
        "v_xor_b32 v141, 16, v140\n"
        "v_xor_b32 v142, 128, v140\n"
        "v_xor_b32 v143, 144, v140\n"
        "v_lshrrev_b32 v10, 2, v15\n"
        "s_add_u32 m0, s40, 75776\n"
        "s_nop 0\n"
        "buffer_load_dwordx4 v244, s[8:11], s64 offen lds\n"
        "v_cmp_eq_u32 vcc, v10, v16\n"
        "v_mov_b32 v11, 0x3fb9\n"
        "v_mov_b32 v189, 0\n"
        "v_and_b32 v10, 48, v2\n"
        "v_cndmask_b32 v188, 0, v11, vcc\n"
        "v_lshlrev_b64 v[188:189], v10, v[188:189]\n"
        "v_cmp_gt_u32 vcc, 32, v1\n"
        "v_mov_b32 v11, 15957\n"
        "v_lshrrev_b32 v149, 2, v15\n"
        "v_lshrrev_b32 v151, 3, v19\n"
        "v_mov_b32 v148, 14702\n"
        "v_cndmask_b32 v11, v148, v11, vcc\n"
        "v_xor_b32 v151, v149, v151\n"
        "v_cmp_eq_u32 vcc, 0, v151\n"
        "v_mov_b32 v149, 0\n"
        "v_cndmask_b32 v148, 0, v11, vcc\n"
        "v_lshlrev_b64 v[148:149], v10, v[148:149]\n"
        "v_cmp_eq_u32 vcc, 1, v151\n"
        "v_mov_b32 v151, 0\n"
        "v_cndmask_b32 v150, 0, v11, vcc\n"
        "v_lshlrev_b64 v[150:151], v10, v[150:151]\n"
        "v_and_b32 v10, 8, v1\n"
        "v_add_u32 v10, v10, v15\n"
        "v_mul_u32_u24 v10, 96, v10\n"
        "v_add3_u32 v137, v10, v19, s65\n"
        "v_and_b32 v237, 1, v1\n"
        "v_mul_u32_u24 v237, 0xaaaa, v237\n"
        "v_mov_b64 v[170:171], v[188:189]\n"
        "v_mov_b64 v[210:211], v[188:189]\n"
        "v_mov_b64 v[228:229], v[188:189]\n"
        "v_add_u32 v237, 0x4444, v237\n"
        "v_mov_b32 v11, 0x3f803f80\n"
        "v_accvgpr_write_b32 a252, v11\n"
        "v_accvgpr_write_b32 a253, v11\n"
        "v_accvgpr_write_b32 a254, v11\n"
        "v_accvgpr_write_b32 a255, v11\n"
        "v_lshl_or_b32 v10, v16, 10, v18\n"
        "s_lshl_b32 s31, s42, 12\n"
        "s_add_u32 s31, s31, 73728\n"
        "s_lshl_b32 s35, s51, 9\n"
        "s_add_u32 s35, s35, 73728\n"
        "v_add_u32 v245, s31, v10\n"
        "s_sub_u32 s50, s35, s31\n"
        "v_lshrrev_b32 v10, 5, v1\n"
        "v_lshrrev_b32 v11, 1, v19\n"
        "s_sub_u32 s31, s48, 65536\n"
        "v_lshl_or_b32 v10, v10, 10, v18\n"
        "v_add3_u32 v246, v10, v11, s31\n"
        "v_mul_u32_u24 v10, 768, v16\n"
        "v_xor_b32 v11, v18, v19\n"
        "v_add3_u32 v235, v10, v11, s65\n"
        "s_lshr_b32 s31, s42, 1\n"
        "s_mul_i32 s35, s31, 36864\n"
        "s_movk_i32 s45, 17664\n"
        "v_mul_u32_u24 v11, 96, v15\n"
        "v_lshrrev_b32 v248, 3, v15\n"
        "v_add3_u32 v10, v10, v11, s35\n"
        "v_mad_u32_u24 v10, v248, s45, v10\n"
        "v_add_u32 v248, v10, v19\n"
        "v_sub_u32 v254, v10, v19\n"
        "s_mul_i32 s31, s31, 192\n"
        "v_mul_u32_u24 v10, 196608, v16\n"
        "v_mul_u32_u24 v11, 12, v15\n"
        "v_add3_u32 v250, v10, v11, s31\n"
        "s_movk_i32 s31, 288\n"
        "s_movk_i32 s35, 3072\n"
        "s_movk_i32 s41, 1536\n"
        "s_movk_i32 s45, 12288\n"
        "v_mad_u32_u24 v10, v4, s31, v3\n"
        "v_lshrrev_b32 v11, 4, v4\n"
        "v_mad_i32_i24 v238, v12, s41, v10\n"
        "v_sub_u32 v7, v7, v11\n"
        "v_mad_u32_u24 v238, v11, s45, v238\n"
        "v_mad_i32_i24 v232, v7, s35, v10\n"
        "v_mad_u32_u24 v10, v5, s31, v3\n"
        "v_lshrrev_b32 v11, 4, v5\n"
        "v_mad_i32_i24 v239, v13, s41, v10\n"
        "v_sub_u32 v8, v8, v11\n"
        "v_mad_u32_u24 v239, v11, s45, v239\n"
        "v_mad_i32_i24 v233, v8, s35, v10\n"
        "v_mad_u32_u24 v10, v6, s31, v3\n"
        "v_lshrrev_b32 v11, 4, v6\n"
        "v_mad_i32_i24 v247, v14, s41, v10\n"
        "v_sub_u32 v9, v9, v11\n"
        "v_mad_u32_u24 v247, v11, s45, v247\n"
        "v_mad_i32_i24 v234, v9, s35, v10\n"
        "s_add_u32 s38, s37, s43\n"
        "s_mov_b32 s26, 0x3fb8aa3b\n"
        "s_mov_b32 s24, 0x5d800000\n"
        "s_mov_b32 s27, 14\n"
        "s_mov_b32 s28, 98304\n"
        "s_mov_b32 s29, 73728\n"
        "s_mov_b32 s30, 122880\n"
        "s_mov_b32 s45, s50\n"
        "s_mov_b32 s25, 0\n"
        "s_mov_b32 s54, 73728\n"
        "s_mov_b32 s55, 0x20000\n"
        "s_mov_b32 s18, 100663296\n"
        "s_mov_b32 s19, 0x20000\n"
        "s_add_u32 m0, s40, 86016\n"
        "s_sub_u32 s64, s47, 11796480\n"
        "buffer_load_dwordx4 v242, s[8:11], s64 offen lds\n"
        "s_waitcnt vmcnt(9)\n"
        "v_and_b32 v146, 0xffff0000, v144\n"
        "v_and_b32 v147, 0xffff0000, v145\n"
        "v_lshlrev_b32 v144, 16, v144\n"
        "v_lshlrev_b32 v145, 16, v145\n"
        "s_add_u32 m0, s40, 87040\n"
        "v_mul_f32 v146, s26, v146\n"
        "buffer_load_dwordx4 v243, s[8:11], s64 offen lds\n"
        "v_mul_f32 v147, s26, v147\n"
        "v_mul_f32 v144, s26, v144\n"
        "v_mul_f32 v145, s26, v145\n"
        "ds_write2st64_b32 v135, v144, v146 offset0:0 offset1:4\n"
        "ds_write2st64_b32 v135, v145, v147 offset0:8 offset1:12\n"
        "s_cmp_eq_u32 s42, 0\n"
        "s_cbranch_scc1 41f\n"
        "s_cmp_eq_u32 s42, 1\n"
        "s_cbranch_scc1 42f\n"
        "s_cmp_eq_u32 s42, 2\n"
        "s_cbranch_scc1 43f\n"
        "s_cmp_eq_u32 s42, 3\n"
        "s_cbranch_scc1 44f\n"
        "60:\n"
        "s_add_u32 m0, s40, 88064\n"
        "s_nop 0\n"
        "buffer_load_dwordx4 v244, s[8:11], s64 offen lds\n"
        "s_waitcnt lgkmcnt(0)\n"
        "s_waitcnt vmcnt(6)\n"
        "s_barrier\n"
        "s_add_u32 m0, s65, 0\n"
        "s_mov_b32 s64, 0\n"
        "buffer_load_dwordx4 v128, s[20:23], s64 offen lds\n"
        "buffer_load_dwordx4 v129, s[20:23], s64 offen offset:1024 lds\n"
        "ds_read_b128 v[0:3], v140 offset:0\n"
        "ds_read_b128 v[4:7], v141 offset:0\n"
        "ds_read_b128 v[16:19], v140 offset:1024\n"
        "ds_read_b128 v[20:23], v141 offset:1024\n"
        "ds_read_b128 v[8:11], v142 offset:0\n"
        "ds_read_b128 v[24:27], v142 offset:1024\n"
        "ds_read_b128 v[12:15], v143 offset:0\n"
        "ds_read_b128 v[28:31], v143 offset:1024\n"
        "ds_read_b128 v[32:35], v140 offset:8192\n"
        "ds_read_b128 v[36:39], v141 offset:8192\n"
        "ds_read_b128 v[40:43], v142 offset:8192\n"
        "s_cmp_eq_u32 s42, 0\n"
        "s_cbranch_scc1 51f\n"
        "s_cmp_eq_u32 s42, 1\n"
        "s_cbranch_scc1 54f\n"
        "s_cmp_eq_u32 s42, 2\n"
        "s_cbranch_scc1 45f\n"
        "s_cmp_eq_u32 s42, 3\n"
        "s_cbranch_scc1 47f\n"
        "61:\n"
        "buffer_load_dwordx4 v130, s[20:23], s64 offen offset:2048 lds\n"
        "s_add_u32 m0, s65, 3072\n"
        "s_mov_b32 s64, 36864\n"
        "buffer_load_dwordx4 v128, s[20:23], s64 offen lds\n"
        "s_waitcnt lgkmcnt(10)\n"
        "v_lshl_add_u32 v0, v0, 2, s67\n"
        "v_lshl_add_u32 v1, v1, 2, s67\n"
        "v_lshl_add_u32 v2, v2, 2, s67\n"
        "v_lshl_add_u32 v3, v3, 2, s67\n"
        "s_waitcnt lgkmcnt(9)\n"
        "v_lshl_add_u32 v4, v4, 2, s67\n"
        "v_lshl_add_u32 v5, v5, 2, s67\n"
        "v_lshl_add_u32 v6, v6, 2, s67\n"
        "v_lshl_add_u32 v7, v7, 2, s67\n"
        "s_waitcnt lgkmcnt(8)\n"
        "v_lshl_add_u32 v16, v16, 2, s67\n"
        "v_lshl_add_u32 v17, v17, 2, s67\n"
        "v_lshl_add_u32 v18, v18, 2, s67\n"
        "v_lshl_add_u32 v19, v19, 2, s67\n"
        "buffer_load_dwordx4 v129, s[20:23], s64 offen offset:1024 lds\n"
        "ds_read_b32 v0, v0\n"
        "ds_read_b32 v1, v1\n"
        "ds_read_b32 v2, v2\n"
        "ds_read_b32 v3, v3\n"
        "ds_read_b32 v4, v4\n"
        "ds_read_b32 v5, v5\n"
        "ds_read_b32 v6, v6\n"
        "ds_read_b32 v7, v7\n"
        "ds_read_b32 v16, v16\n"
        "ds_read_b32 v17, v17\n"
        "ds_read_b32 v18, v18\n"
        "ds_read_b32 v19, v19\n"
        "buffer_load_dwordx4 v130, s[20:23], s64 offen offset:2048 lds\n"
        "s_waitcnt lgkmcnt(15)\n"
        "v_lshl_add_u32 v20, v20, 2, s67\n"
        "v_lshl_add_u32 v21, v21, 2, s67\n"
        "v_lshl_add_u32 v22, v22, 2, s67\n"
        "v_lshl_add_u32 v23, v23, 2, s67\n"
        "v_lshl_add_u32 v8, v8, 2, s67\n"
        "v_lshl_add_u32 v9, v9, 2, s67\n"
        "v_lshl_add_u32 v10, v10, 2, s67\n"
        "v_lshl_add_u32 v11, v11, 2, s67\n"
        "v_lshl_add_u32 v24, v24, 2, s67\n"
        "v_lshl_add_u32 v25, v25, 2, s67\n"
        "v_lshl_add_u32 v26, v26, 2, s67\n"
        "v_lshl_add_u32 v27, v27, 2, s67\n"
        "ds_read_b32 v20, v20\n"
        "ds_read_b32 v21, v21\n"
        "ds_read_b32 v22, v22\n"
        "ds_read_b32 v23, v23\n"
        "ds_read_b32 v8, v8\n"
        "ds_read_b32 v9, v9\n"
        "ds_read_b32 v10, v10\n"
        "ds_read_b32 v11, v11\n"
        "ds_read_b32 v24, v24\n"
        "ds_read_b32 v25, v25\n"
        "ds_read_b32 v26, v26\n"
        "ds_read_b32 v27, v27\n"
        "s_add_u32 m0, s65, 6144\n"
        "s_mov_b32 s64, 73728\n"
        "buffer_load_dwordx4 v128, s[20:23], s64 offen lds\n"
        "v_lshl_add_u32 v12, v12, 2, s67\n"
        "v_lshl_add_u32 v13, v13, 2, s67\n"
        "v_lshl_add_u32 v14, v14, 2, s67\n"
        "v_lshl_add_u32 v15, v15, 2, s67\n"
        "v_lshl_add_u32 v28, v28, 2, s67\n"
        "v_lshl_add_u32 v29, v29, 2, s67\n"
        "v_lshl_add_u32 v30, v30, 2, s67\n"
        "v_lshl_add_u32 v31, v31, 2, s67\n"
        "s_waitcnt lgkmcnt(15)\n"
        "v_lshl_add_u32 v32, v32, 2, s67\n"
        "v_lshl_add_u32 v33, v33, 2, s67\n"
        "v_lshl_add_u32 v34, v34, 2, s67\n"
        "v_lshl_add_u32 v35, v35, 2, s67\n"
        "ds_read_b32 v12, v12\n"
        "ds_read_b32 v13, v13\n"
        "ds_read_b32 v14, v14\n"
        "ds_read_b32 v15, v15\n"
        "ds_read_b32 v28, v28\n"
        "ds_read_b32 v29, v29\n"
        "ds_read_b32 v30, v30\n"
        "ds_read_b32 v31, v31\n"
        "ds_read_b32 v32, v32\n"
        "ds_read_b32 v33, v33\n"
        "ds_read_b32 v34, v34\n"
        "ds_read_b32 v35, v35\n"
        "buffer_load_dwordx4 v129, s[20:23], s64 offen offset:1024 lds\n"
        "v_lshl_add_u32 v36, v36, 2, s67\n"
        "v_lshl_add_u32 v37, v37, 2, s67\n"
        "v_lshl_add_u32 v38, v38, 2, s67\n"
        "v_lshl_add_u32 v39, v39, 2, s67\n"
        "v_lshl_add_u32 v40, v40, 2, s67\n"
        "v_lshl_add_u32 v41, v41, 2, s67\n"
        "v_lshl_add_u32 v42, v42, 2, s67\n"
        "v_lshl_add_u32 v43, v43, 2, s67\n"
        "ds_read_b32 v36, v36\n"
        "ds_read_b32 v37, v37\n"
        "ds_read_b32 v38, v38\n"
        "ds_read_b32 v39, v39\n"
        "ds_read_b32 v40, v40\n"
        "ds_read_b32 v41, v41\n"
        "ds_read_b32 v42, v42\n"
        "ds_read_b32 v43, v43\n"
        "buffer_load_dwordx4 v130, s[20:23], s64 offen offset:2048 lds\n"
        "s_waitcnt vmcnt(9)\n"
        "s_barrier\n"
        "s_waitcnt vmcnt(6)\n"
        "ds_read_b128 v[216:219], v137 offset:0\n"
        "ds_read_b128 v[156:159], v137 offset:768\n"
        "ds_read_b128 v[176:179], v137 offset:32\n"
        "ds_read_b128 v[196:199], v137 offset:800\n"
        "ds_read_b128 v[160:163], v137 offset:64\n"
        "s_add_u32 m0, s65, 9216\n"
        "s_mov_b32 s64, 110592\n"
        "buffer_load_dwordx4 v128, s[20:23], s64 offen lds\n"
        "ds_read_b128 v[164:167], v137 offset:832\n"
        "s_waitcnt lgkmcnt(5)\n"
        "v_mfma_f32_16x16x32_bf16 v[152:155], v[216:219], v[148:151], 0\n"
        "s_waitcnt lgkmcnt(4)\n"
        "ds_read_b128 a[216:219], v240 offset:0\n"
        "ds_read_b128 a[220:223], v240 offset:1536\n"
        "ds_read_b128 a[224:227], v240 offset:12288\n"
        "ds_read_b128 a[228:231], v240 offset:13824\n"
        "v_mfma_f32_16x16x32_bf16 v[172:175], v[156:159], v[148:151], 0\n"
        "s_waitcnt lgkmcnt(7)\n"
        "buffer_load_dwordx4 v129, s[20:23], s64 offen offset:1024 lds\n"
        "v_mfma_f32_16x16x32_bf16 v[192:195], v[176:179], v[148:151], 0\n"
        "s_waitcnt lgkmcnt(6)\n"
        "ds_read_b128 a[232:235], v241 offset:0\n"
        "ds_read_b128 a[236:239], v241 offset:1536\n"
        "v_mfma_f32_16x16x32_bf16 v[212:215], v[196:199], v[148:151], 0\n"
        "s_waitcnt lgkmcnt(7)\n"
        "s_waitcnt vmcnt(5)\n"
        "ds_read_b128 v[180:183], v137 offset:3072\n"
        "ds_read_b128 v[184:187], v137 offset:3840\n"
        "v_mfma_f32_16x16x32_bf16 v[112:115], v[160:163], v[148:151], 0\n"
        "s_waitcnt lgkmcnt(8)\n"
        "buffer_load_dwordx4 v130, s[20:23], s64 offen offset:2048 lds\n"
        "v_mfma_f32_16x16x32_bf16 v[116:119], v[164:167], v[148:151], 0\n"
        "s_waitcnt lgkmcnt(7)\n"
        "s_add_u32 m0, s65, 12288\n"
        "ds_read_b128 v[200:203], v137 offset:3104\n"
        "ds_read_b128 v[204:207], v137 offset:3872\n"
        "v_cvt_pk_bf16_f32 v152, v152, v153\n"
        "v_accvgpr_write_b32 a0, v152\n"
        "v_cvt_pk_bf16_f32 v154, v154, v155\n"
        "v_accvgpr_write_b32 a1, v154\n"
        "v_cvt_pk_bf16_f32 v172, v172, v173\n"
        "v_accvgpr_write_b32 a2, v172\n"
        "v_cvt_pk_bf16_f32 v174, v174, v175\n"
        "v_accvgpr_write_b32 a3, v174\n"
        "s_nop 1\n"
        "v_mfma_f32_16x16x32_bf16 v[64:67], a[0:3], a[216:219], 0\n"
        "s_waitcnt lgkmcnt(8)\n"
        "s_mov_b32 s64, 147456\n"
        "ds_read_b128 v[220:223], v137 offset:3136\n"
        "ds_read_b128 v[224:227], v137 offset:3904\n"
        "v_mfma_f32_16x16x32_bf16 v[68:71], a[0:3], a[220:223], 0\n"
        "s_waitcnt lgkmcnt(9)\n"
        "buffer_load_dwordx4 v128, s[20:23], s64 offen lds\n"
        "v_mfma_f32_16x16x32_bf16 v[72:75], a[0:3], a[224:227], 0\n"
        "s_waitcnt lgkmcnt(8)\n"
        "ds_read_b128 a[240:243], v241 offset:12288\n"
        "ds_read_b128 a[244:247], v241 offset:13824\n"
        "v_mfma_f32_16x16x32_bf16 v[76:79], a[0:3], a[228:231], 0\n"
        "s_waitcnt lgkmcnt(7)\n"
        "v_cvt_pk_bf16_f32 v192, v192, v193\n"
        "v_accvgpr_write_b32 a24, v192\n"
        "v_mfma_f32_16x16x32_bf16 v[120:123], v[180:183], v[148:151], 0\n"
        "buffer_load_dwordx4 v129, s[20:23], s64 offen offset:1024 lds\n"
        "v_cvt_pk_bf16_f32 v194, v194, v195\n"
        "v_accvgpr_write_b32 a25, v194\n"
        "v_cvt_pk_bf16_f32 v212, v212, v213\n"
        "v_accvgpr_write_b32 a26, v212\n"
        "v_cvt_pk_bf16_f32 v214, v214, v215\n"
        "v_accvgpr_write_b32 a27, v214\n"
        "s_nop 1\n"
        "v_mfma_f32_16x16x32_bf16 v[80:83], a[24:27], a[228:231], 0\n"
        "s_waitcnt lgkmcnt(6)\n"
        "v_cvt_pk_bf16_f32 v112, v112, v113\n"
        "v_accvgpr_write_b32 a48, v112\n"
        "v_mfma_f32_16x16x32_bf16 v[124:127], v[184:187], v[148:151], 0\n"
        "v_cvt_pk_bf16_f32 v114, v114, v115\n"
        "v_accvgpr_write_b32 a49, v114\n"
        "v_mfma_f32_16x16x32_bf16 v[84:87], a[24:27], a[224:227], 0\n"
        "s_waitcnt lgkmcnt(5)\n"
        "v_cvt_pk_bf16_f32 v116, v116, v117\n"
        "v_accvgpr_write_b32 a50, v116\n"
        "v_mfma_f32_16x16x32_bf16 v[152:155], v[200:203], v[148:151], 0\n"
        "buffer_load_dwordx4 v130, s[20:23], s64 offen offset:2048 lds\n"
        "v_cvt_pk_bf16_f32 v118, v118, v119\n"
        "v_accvgpr_write_b32 a51, v118\n"
        "v_mfma_f32_16x16x32_bf16 v[88:91], a[24:27], a[220:223], 0\n"
        "s_waitcnt lgkmcnt(4)\n"
        "s_add_u32 m0, s65, 15360\n"
        "v_cvt_pk_bf16_f32 v120, v120, v121\n"
        "v_accvgpr_write_b32 a4, v120\n"
        "v_mfma_f32_16x16x32_bf16 v[172:175], v[204:207], v[148:151], 0\n"
        "s_mov_b32 s64, 184320\n"
        "v_cvt_pk_bf16_f32 v122, v122, v123\n"
        "v_accvgpr_write_b32 a5, v122\n"
        "v_mfma_f32_16x16x32_bf16 v[92:95], a[24:27], a[216:219], 0\n"
        "s_waitcnt lgkmcnt(3)\n"
        "buffer_load_dwordx4 v128, s[20:23], s64 offen lds\n"
        "v_mfma_f32_16x16x32_bf16 v[192:195], v[220:223], v[148:151], 0\n"
        "ds_read_b128 a[248:251], v240 offset:128\n"
        "v_cvt_pk_bf16_f32 v124, v124, v125\n"
        "v_mfma_f32_16x16x32_bf16 v[96:99], a[48:51], a[216:219], 0\n"
        "s_waitcnt lgkmcnt(3)\n"
        "v_accvgpr_write_b32 a6, v124\n"
        "v_cvt_pk_bf16_f32 v126, v126, v127\n"
        "v_mfma_f32_16x16x32_bf16 v[212:215], v[224:227], v[148:151], 0\n"
        "s_waitcnt vmcnt(7)\n"
        "ds_read_b128 v[216:219], v137 offset:6144\n"
        "ds_read_b128 v[156:159], v137 offset:6912\n"
        "v_mfma_f32_16x16x32_bf16 v[100:103], a[48:51], a[220:223], 0\n"
        "buffer_load_dwordx4 v129, s[20:23], s64 offen offset:1024 lds\n"
        "v_accvgpr_write_b32 a7, v126\n"
        "v_mfma_f32_16x16x32_bf16 v[104:107], a[48:51], a[224:227], 0\n"
        "ds_read_b128 v[176:179], v137 offset:6176\n"
        "ds_read_b128 v[196:199], v137 offset:6944\n"
        "v_mfma_f32_16x16x32_bf16 v[108:111], a[48:51], a[228:231], 0\n"
        "ds_read_b128 v[160:163], v137 offset:6208\n"
        "ds_read_b128 v[164:167], v137 offset:6976\n"
        "v_cvt_pk_bf16_f32 v152, v152, v153\n"
        "v_accvgpr_write_b32 a28, v152\n"
        "v_mfma_f32_16x16x32_bf16 v[64:67], a[4:7], a[232:235], v[64:67]\n"
        "s_waitcnt lgkmcnt(5)\n"
        "buffer_load_dwordx4 v130, s[20:23], s64 offen offset:2048 lds\n"
        "v_cvt_pk_bf16_f32 v154, v154, v155\n"
        "v_accvgpr_write_b32 a29, v154\n"
        "v_mfma_f32_16x16x32_bf16 v[112:115], v[216:219], v[148:151], 0\n"
        "ds_read_b128 a[216:219], v240 offset:1664\n"
        "ds_read_b128 a[220:223], v240 offset:12416\n"
        "v_cvt_pk_bf16_f32 v172, v172, v173\n"
        "v_accvgpr_write_b32 a30, v172\n"
        "v_mfma_f32_16x16x32_bf16 v[68:71], a[4:7], a[236:239], v[68:71]\n"
        "s_add_u32 m0, s65, 0\n"
        "ds_read_b128 a[224:227], v240 offset:13952\n"
        "v_cvt_pk_bf16_f32 v174, v174, v175\n"
        "v_accvgpr_write_b32 a31, v174\n"
        "v_mfma_f32_16x16x32_bf16 v[72:75], a[4:7], a[240:243], v[72:75]\n"
        "s_waitcnt lgkmcnt(7)\n"
        "s_mov_b32 s64, 384\n"
        "v_cvt_pk_bf16_f32 v192, v192, v193\n"
        "v_accvgpr_write_b32 a52, v192\n"
        "v_cvt_pk_bf16_f32 v194, v194, v195\n"
        "v_accvgpr_write_b32 a53, v194\n"
        "v_mfma_f32_16x16x32_bf16 v[116:119], v[156:159], v[148:151], 0\n"
        "buffer_load_dwordx4 v128, s[20:23], s64 offen lds\n"
        "v_mfma_f32_16x16x32_bf16 v[76:79], a[4:7], a[244:247], v[76:79]\n"
        "v_cvt_pk_bf16_f32 v212, v212, v213\n"
        "v_accvgpr_write_b32 a54, v212\n"
        "v_cvt_pk_bf16_f32 v214, v214, v215\n"
        "v_accvgpr_write_b32 a55, v214\n"
        "v_mfma_f32_16x16x32_bf16 v[80:83], a[28:31], a[244:247], v[80:83]\n"
        "s_waitcnt lgkmcnt(6)\n"
        "v_cvt_pk_bf16_f32 v112, v112, v113\n"
        "v_accvgpr_write_b32 a8, v112\n"
        "v_mfma_f32_16x16x32_bf16 v[120:123], v[176:179], v[148:151], 0\n"
        "buffer_load_dwordx4 v129, s[20:23], s64 offen offset:1024 lds\n"
        "v_mfma_f32_16x16x32_bf16 v[84:87], a[28:31], a[240:243], v[84:87]\n"
        "v_cvt_pk_bf16_f32 v114, v114, v115\n"
        "v_accvgpr_write_b32 a9, v114\n"
        "v_mfma_f32_16x16x32_bf16 v[88:91], a[28:31], a[236:239], v[88:91]\n"
        "s_waitcnt lgkmcnt(5)\n"
        "v_cvt_pk_bf16_f32 v116, v116, v117\n"
        "v_accvgpr_write_b32 a10, v116\n"
        "v_mfma_f32_16x16x32_bf16 v[124:127], v[196:199], v[148:151], 0\n"
        "v_cvt_pk_bf16_f32 v118, v118, v119\n"
        "v_accvgpr_write_b32 a11, v118\n"
        "v_mfma_f32_16x16x32_bf16 v[92:95], a[28:31], a[232:235], v[92:95]\n"
        "buffer_load_dwordx4 v130, s[20:23], s64 offen offset:2048 lds\n"
        "v_mfma_f32_16x16x32_bf16 v[96:99], a[52:55], a[232:235], v[96:99]\n"
        "s_waitcnt lgkmcnt(4)\n"
        "s_add_u32 m0, s65, 3072\n"
        "s_waitcnt vmcnt(9)\n"
        "ds_read_b128 v[180:183], v137 offset:9216\n"
        "ds_read_b128 v[184:187], v137 offset:9984\n"
        "v_mfma_f32_16x16x32_bf16 v[152:155], v[160:163], v[148:151], 0\n"
        "s_mov_b32 s64, 37248\n"
        "ds_read_b128 v[200:203], v137 offset:9248\n"
        "ds_read_b128 v[204:207], v137 offset:10016\n"
        "v_mfma_f32_16x16x32_bf16 v[100:103], a[52:55], a[236:239], v[100:103]\n"
        "buffer_load_dwordx4 v128, s[20:23], s64 offen lds\n"
        "v_mfma_f32_16x16x32_bf16 v[104:107], a[52:55], a[240:243], v[104:107]\n"
        "s_waitcnt lgkmcnt(7)\n"
        "ds_read_b128 v[220:223], v137 offset:9280\n"
        "ds_read_b128 v[224:227], v137 offset:10048\n"
        "v_mfma_f32_16x16x32_bf16 v[172:175], v[164:167], v[148:151], 0\n"
        ".long 0xbec200ff\n"
        "41:\n"
        "s_branch 60b\n"
        "ds_read_b128 a[228:231], v241 offset:128\n"
        "ds_read_b128 a[232:235], v241 offset:1664\n"
        "v_cvt_pk_bf16_f32 v120, v120, v121\n"
        "v_accvgpr_write_b32 a32, v120\n"
        "v_mfma_f32_16x16x32_bf16 v[108:111], a[52:55], a[244:247], v[108:111]\n"
        "ds_read_b128 a[236:239], v241 offset:12416\n"
        "ds_read_b128 a[240:243], v241 offset:13952\n"
        "v_cvt_pk_bf16_f32 v122, v122, v123\n"
        "v_accvgpr_write_b32 a33, v122\n"
        "v_mfma_f32_16x16x32_bf16 v[64:67], a[8:11], a[248:251], v[64:67]\n"
        "s_waitcnt lgkmcnt(9)\n"
        "buffer_load_dwordx4 v129, s[20:23], s64 offen offset:1024 lds\n"
        "v_mfma_f32_16x16x32_bf16 v[192:195], v[180:183], v[148:151], 0\n"
        "v_cvt_pk_bf16_f32 v124, v124, v125\n"
        "v_accvgpr_write_b32 a34, v124\n"
        "v_cvt_pk_bf16_f32 v126, v126, v127\n"
        "v_accvgpr_write_b32 a35, v126\n"
        "v_mfma_f32_16x16x32_bf16 v[68:71], a[8:11], a[216:219], v[68:71]\n"
        "v_cvt_pk_bf16_f32 v152, v152, v153\n"
        "v_accvgpr_write_b32 a56, v152\n"
        "v_mfma_f32_16x16x32_bf16 v[72:75], a[8:11], a[220:223], v[72:75]\n"
        "s_waitcnt lgkmcnt(8)\n"
        "buffer_load_dwordx4 v130, s[20:23], s64 offen offset:2048 lds\n"
        "v_mfma_f32_16x16x32_bf16 v[212:215], v[184:187], v[148:151], 0\n"
        "v_cvt_pk_bf16_f32 v154, v154, v155\n"
        "v_accvgpr_write_b32 a57, v154\n"
        "v_mfma_f32_16x16x32_bf16 v[76:79], a[8:11], a[224:227], v[76:79]\n"
        "s_add_u32 m0, s65, 6144\n"
        "v_cvt_pk_bf16_f32 v172, v172, v173\n"
        "v_accvgpr_write_b32 a58, v172\n"
        "v_mfma_f32_16x16x32_bf16 v[80:83], a[32:35], a[224:227], v[80:83]\n"
        "s_waitcnt lgkmcnt(7)\n"
        "s_mov_b32 s64, 74112\n"
        "v_cvt_pk_bf16_f32 v174, v174, v175\n"
        "v_accvgpr_write_b32 a59, v174\n"
        "v_mfma_f32_16x16x32_bf16 v[112:115], v[200:203], v[148:151], 0\n"
        "buffer_load_dwordx4 v128, s[20:23], s64 offen lds\n"
        "v_mfma_f32_16x16x32_bf16 v[84:87], a[32:35], a[220:223], v[84:87]\n"
        "v_cvt_pk_bf16_f32 v192, v192, v193\n"
        "v_accvgpr_write_b32 a12, v192\n"
        "v_mfma_f32_16x16x32_bf16 v[88:91], a[32:35], a[216:219], v[88:91]\n"
        "s_waitcnt lgkmcnt(6)\n"
        "v_cvt_pk_bf16_f32 v194, v194, v195\n"
        "v_accvgpr_write_b32 a13, v194\n"
        "v_mfma_f32_16x16x32_bf16 v[116:119], v[204:207], v[148:151], 0\n"
        "buffer_load_dwordx4 v129, s[20:23], s64 offen offset:1024 lds\n"
        "v_mfma_f32_16x16x32_bf16 v[92:95], a[32:35], a[248:251], v[92:95]\n"
        "v_cvt_pk_bf16_f32 v212, v212, v213\n"
        "v_accvgpr_write_b32 a14, v212\n"
        "v_mfma_f32_16x16x32_bf16 v[96:99], a[56:59], a[248:251], v[96:99]\n"
        "s_waitcnt lgkmcnt(5)\n"
        "s_waitcnt vmcnt(11)\n"
        "ds_read_b128 v[216:219], v137 offset:12288\n"
        "ds_read_b128 v[156:159], v137 offset:13056\n"
        "v_mfma_f32_16x16x32_bf16 v[120:123], v[220:223], v[148:151], 0\n"
        "ds_read_b128 v[176:179], v137 offset:12320\n"
        "ds_read_b128 v[196:199], v137 offset:13088\n"
        "v_cvt_pk_bf16_f32 v214, v214, v215\n"
        "v_accvgpr_write_b32 a15, v214\n"
        "v_mfma_f32_16x16x32_bf16 v[100:103], a[56:59], a[216:219], v[100:103]\n"
        "buffer_load_dwordx4 v130, s[20:23], s64 offen offset:2048 lds\n"
        "v_mfma_f32_16x16x32_bf16 v[104:107], a[56:59], a[220:223], v[104:107]\n"
        "s_waitcnt lgkmcnt(8)\n"
        "s_add_u32 m0, s65, 9216\n"
        "ds_read_b128 v[160:163], v137 offset:12352\n"
        "ds_read_b128 v[164:167], v137 offset:13120\n"
        "v_mfma_f32_16x16x32_bf16 v[124:127], v[224:227], v[148:151], 0\n"
        "s_mov_b32 s64, 110976\n"
        "ds_read_b128 a[244:247], v240 offset:256\n"
        "ds_read_b128 a[248:251], v240 offset:1792\n"
        "v_cvt_pk_bf16_f32 v112, v112, v113\n"
        "v_accvgpr_write_b32 a36, v112\n"
        "v_mfma_f32_16x16x32_bf16 v[108:111], a[56:59], a[224:227], v[108:111]\n"
        "s_waitcnt lgkmcnt(11)\n"
        "buffer_load_dwordx4 v128, s[20:23], s64 offen lds\n"
        "v_cvt_pk_bf16_f32 v114, v114, v115\n"
        "v_accvgpr_write_b32 a37, v114\n"
        "v_mfma_f32_16x16x32_bf16 v[64:67], a[12:15], a[228:231], v[64:67]\n"
        "s_waitcnt lgkmcnt(7)\n"
        "ds_read_b128 a[216:219], v240 offset:12544\n"
        "ds_read_b128 a[220:223], v240 offset:14080\n"
        "v_mfma_f32_16x16x32_bf16 v[152:155], v[216:219], v[148:151], 0\n"
        "v_cvt_pk_bf16_f32 v116, v116, v117\n"
        "v_accvgpr_write_b32 a38, v116\n"
        "v_cvt_pk_bf16_f32 v118, v118, v119\n"
        "v_accvgpr_write_b32 a39, v118\n"
        "v_mfma_f32_16x16x32_bf16 v[68:71], a[12:15], a[232:235], v[68:71]\n"
        "v_cvt_pk_bf16_f32 v120, v120, v121\n"
        "v_accvgpr_write_b32 a60, v120\n"
        "v_mfma_f32_16x16x32_bf16 v[72:75], a[12:15], a[236:239], v[72:75]\n"
        "s_waitcnt lgkmcnt(8)\n"
        "buffer_load_dwordx4 v129, s[20:23], s64 offen offset:1024 lds\n"
        "v_mfma_f32_16x16x32_bf16 v[172:175], v[156:159], v[148:151], 0\n"
        "v_cvt_pk_bf16_f32 v122, v122, v123\n"
        "v_accvgpr_write_b32 a61, v122\n"
        "v_mfma_f32_16x16x32_bf16 v[76:79], a[12:15], a[240:243], v[76:79]\n"
        "v_cvt_pk_bf16_f32 v124, v124, v125\n"
        "v_accvgpr_write_b32 a62, v124\n"
        "v_mfma_f32_16x16x32_bf16 v[80:83], a[36:39], a[240:243], v[80:83]\n"
        "s_waitcnt lgkmcnt(7)\n"
        "buffer_load_dwordx4 v130, s[20:23], s64 offen offset:2048 lds\n"
        "v_mfma_f32_16x16x32_bf16 v[192:195], v[176:179], v[148:151], 0\n"
        "v_cvt_pk_bf16_f32 v126, v126, v127\n"
        "v_accvgpr_write_b32 a63, v126\n"
        "v_mfma_f32_16x16x32_bf16 v[84:87], a[36:39], a[236:239], v[84:87]\n"
        "s_add_u32 m0, s65, 12288\n"
        "s_waitcnt vmcnt(9)\n"
        "ds_read_b64_tr_b16 a[72:73], v255 offset:0\n"
        "ds_read_b64_tr_b16 a[74:75], v255 offset:768\n"
        "v_mfma_f32_16x16x32_bf16 v[88:91], a[36:39], a[232:235], v[88:91]\n"
        "s_waitcnt lgkmcnt(8)\n"
        "s_mov_b32 s64, 147840\n"
        "ds_read_b64_tr_b16 a[96:97], v255 offset:32\n"
        "ds_read_b64_tr_b16 a[98:99], v255 offset:800\n"
        "v_mfma_f32_16x16x32_bf16 v[212:215], v[196:199], v[148:151], 0\n"
        "s_waitcnt lgkmcnt(8)\n"
        "buffer_load_dwordx4 v128, s[20:23], s64 offen lds\n"
        "ds_read_b64_tr_b16 a[120:121], v255 offset:64\n"
        "ds_read_b64_tr_b16 a[122:123], v255 offset:832\n"
        "v_mfma_f32_16x16x32_bf16 v[92:95], a[36:39], a[228:231], v[92:95]\n"
        "ds_read_b128 v[180:183], v137 offset:15360\n"
        "ds_read_b128 v[184:187], v137 offset:16128\n"
        "v_cvt_pk_bf16_f32 v152, v152, v153\n"
        "v_accvgpr_write_b32 a16, v152\n"
        "v_mfma_f32_16x16x32_bf16 v[96:99], a[60:63], a[228:231], v[96:99]\n"
        "ds_read_b128 v[200:203], v137 offset:15392\n"
        "ds_read_b128 v[204:207], v137 offset:16160\n"
        "v_cvt_pk_bf16_f32 v154, v154, v155\n"
        "v_accvgpr_write_b32 a17, v154\n"
        "v_mfma_f32_16x16x32_bf16 v[112:115], v[160:163], v[148:151], 0\n"
        "buffer_load_dwordx4 v129, s[20:23], s64 offen offset:1024 lds\n"
        "v_cvt_pk_bf16_f32 v172, v172, v173\n"
        "v_accvgpr_write_b32 a18, v172\n"
        "v_mfma_f32_16x16x32_bf16 v[100:103], a[60:63], a[232:235], v[100:103]\n"
        "ds_read_b128 v[220:223], v137 offset:15424\n"
        "ds_read_b128 v[224:227], v137 offset:16192\n"
        "v_cvt_pk_bf16_f32 v174, v174, v175\n"
        "v_accvgpr_write_b32 a19, v174\n"
        "v_mfma_f32_16x16x32_bf16 v[104:107], a[60:63], a[236:239], v[104:107]\n"
        "ds_read_b128 a[224:227], v241 offset:256\n"
        "ds_read_b128 a[228:231], v241 offset:1792\n"
        "v_mfma_f32_16x16x32_bf16 v[116:119], v[164:167], v[148:151], 0\n"
        "ds_read_b128 a[232:235], v241 offset:12544\n"
        "ds_read_b128 a[236:239], v241 offset:14080\n"
        "v_cvt_pk_bf16_f32 v192, v192, v193\n"
        "v_accvgpr_write_b32 a40, v192\n"
        "v_mfma_f32_16x16x32_bf16 v[108:111], a[60:63], a[240:243], v[108:111]\n"
        "s_waitcnt lgkmcnt(15)\n"
        "buffer_load_dwordx4 v130, s[20:23], s64 offen offset:2048 lds\n"
        "v_cvt_pk_bf16_f32 v194, v194, v195\n"
        "v_accvgpr_write_b32 a41, v194\n"
        "v_mfma_f32_16x16x32_bf16 v[64:67], a[16:19], a[244:247], v[64:67]\n"
        "s_waitcnt lgkmcnt(9)\n"
        "s_add_u32 m0, s65, 15360\n"
        "v_cvt_pk_bf16_f32 v212, v212, v213\n"
        "v_accvgpr_write_b32 a42, v212\n"
        "v_mfma_f32_16x16x32_bf16 v[120:123], v[180:183], v[148:151], 0\n"
        "s_mov_b32 s64, 184704\n"
        "s_waitcnt vmcnt(9)\n"
        "ds_read_b64_tr_b16 a[76:77], v255 offset:3072\n"
        "ds_read_b64_tr_b16 a[78:79], v255 offset:3840\n"
        "v_cvt_pk_bf16_f32 v214, v214, v215\n"
        "v_accvgpr_write_b32 a43, v214\n"
        "v_mfma_f32_16x16x32_bf16 v[68:71], a[16:19], a[248:251], v[68:71]\n"
        "s_waitcnt lgkmcnt(6)\n"
        "buffer_load_dwordx4 v128, s[20:23], s64 offen lds\n"
        "ds_read_b64_tr_b16 a[100:101], v255 offset:3104\n"
        "ds_read_b64_tr_b16 a[102:103], v255 offset:3872\n"
        "v_mfma_f32_16x16x32_bf16 v[72:75], a[16:19], a[216:219], v[72:75]\n"
        "ds_read_b64_tr_b16 a[124:125], v255 offset:3136\n"
        "ds_read_b64_tr_b16 a[126:127], v255 offset:3904\n"
        "v_mfma_f32_16x16x32_bf16 v[124:127], v[184:187], v[148:151], 0\n"
        "v_cvt_pk_bf16_f32 v112, v112, v113\n"
        "v_accvgpr_write_b32 a64, v112\n"
        "v_cvt_pk_bf16_f32 v114, v114, v115\n"
        "v_accvgpr_write_b32 a65, v114\n"
        "v_mfma_f32_16x16x32_bf16 v[76:79], a[16:19], a[220:223], v[76:79]\n"
        "v_cvt_pk_bf16_f32 v116, v116, v117\n"
        "v_accvgpr_write_b32 a66, v116\n"
        "v_mfma_f32_16x16x32_bf16 v[80:83], a[40:43], a[220:223], v[80:83]\n"
        "buffer_load_dwordx4 v129, s[20:23], s64 offen offset:1024 lds\n"
        "v_mfma_f32_16x16x32_bf16 v[152:155], v[200:203], v[148:151], 0\n"
        "ds_read_b128 a[240:243], v240 offset:0\n"
        "v_cvt_pk_bf16_f32 v118, v118, v119\n"
        "v_accvgpr_write_b32 a67, v118\n"
        "v_mfma_f32_16x16x32_bf16 v[84:87], a[40:43], a[216:219], v[84:87]\n"
        "v_cvt_pk_bf16_f32 v120, v120, v121\n"
        "v_accvgpr_write_b32 a20, v120\n"
        "v_mfma_f32_16x16x32_bf16 v[88:91], a[40:43], a[248:251], v[88:91]\n"
        "buffer_load_dwordx4 v130, s[20:23], s64 offen offset:2048 lds\n"
        "v_mfma_f32_16x16x32_bf16 v[172:175], v[204:207], v[148:151], 0\n"
        "v_cvt_pk_bf16_f32 v122, v122, v123\n"
        "v_accvgpr_write_b32 a21, v122\n"
        "v_mfma_f32_16x16x32_bf16 v[92:95], a[40:43], a[244:247], v[92:95]\n"
        "v_cvt_pk_bf16_f32 v124, v124, v125\n"
        "v_accvgpr_write_b32 a22, v124\n"
        "v_mfma_f32_16x16x32_bf16 v[96:99], a[64:67], a[244:247], v[96:99]\n"
        "s_waitcnt vmcnt(9)\n"
        "ds_read_b64_tr_b16 a[80:81], v255 offset:6144\n"
        "ds_read_b64_tr_b16 a[82:83], v255 offset:6912\n"
        "v_mfma_f32_16x16x32_bf16 v[192:195], v[220:223], v[148:151], 0\n"
        "ds_read_b64_tr_b16 a[104:105], v255 offset:6176\n"
        "ds_read_b64_tr_b16 a[106:107], v255 offset:6944\n"
        "v_cvt_pk_bf16_f32 v126, v126, v127\n"
        "v_accvgpr_write_b32 a23, v126\n"
        "v_mfma_f32_16x16x32_bf16 v[100:103], a[64:67], a[248:251], v[100:103]\n"
        "ds_read_b64_tr_b16 a[128:129], v255 offset:6208\n"
        "ds_read_b64_tr_b16 a[130:131], v255 offset:6976\n"
        "v_mfma_f32_16x16x32_bf16 v[104:107], a[64:67], a[216:219], v[104:107]\n"
        "ds_read_b128 a[244:247], v241 offset:0\n"
        "ds_read_b128 a[248:251], v240 offset:128\n"
        "v_mfma_f32_16x16x32_bf16 v[212:215], v[224:227], v[148:151], 0\n"
        "ds_read_b128 a[216:219], v240 offset:1536\n"
        "v_cvt_pk_bf16_f32 v152, v152, v153\n"
        "v_accvgpr_write_b32 a44, v152\n"
        "v_cvt_pk_bf16_f32 v154, v154, v155\n"
        "v_mfma_f32_16x16x32_bf16 v[108:111], a[64:67], a[220:223], v[108:111]\n"
        "s_waitcnt lgkmcnt(15)\n"
        "ds_read_b128 a[220:223], v241 offset:1536\n"
        "v_accvgpr_write_b32 a45, v154\n"
        "v_mfma_f32_16x16x32_bf16 v[64:67], a[20:23], a[224:227], v[64:67]\n"
        "s_waitcnt lgkmcnt(15)\n"
        "s_mov_b32 m0, s48\n"
        "v_cvt_pk_bf16_f32 v172, v172, v173\n"
        "v_accvgpr_write_b32 a46, v172\n"
        "v_mfma_f32_16x16x32_bf16 v[68:71], a[20:23], a[228:231], v[68:71]\n"
        "s_waitcnt lgkmcnt(15)\n"
        "s_sub_u32 s64, s36, 4194304\n"
        "v_cvt_pk_bf16_f32 v174, v174, v175\n"
        "v_accvgpr_write_b32 a47, v174\n"
        "v_mfma_f32_16x16x32_bf16 v[72:75], a[20:23], a[232:235], v[72:75]\n"
        "s_waitcnt lgkmcnt(15)\n"
        "buffer_load_dwordx4 v249, s[12:15], s64 offen lds\n"
        "v_mfma_f32_16x16x32_bf16 v[76:79], a[20:23], a[236:239], v[76:79]\n"
        "v_cvt_pk_bf16_f32 v192, v192, v193\n"
        "v_accvgpr_write_b32 a68, v192\n"
        "v_mfma_f32_16x16x32_bf16 v[80:83], a[44:47], a[236:239], v[80:83]\n"
        "v_cvt_pk_bf16_f32 v194, v194, v195\n"
        "v_accvgpr_write_b32 a69, v194\n"
        "v_mfma_f32_16x16x32_bf16 v[84:87], a[44:47], a[232:235], v[84:87]\n"
        "v_cvt_pk_bf16_f32 v212, v212, v213\n"
        "v_accvgpr_write_b32 a70, v212\n"
        "v_mfma_f32_16x16x32_bf16 v[88:91], a[44:47], a[228:231], v[88:91]\n"
        "buffer_load_dwordx4 v253, s[12:15], s64 offen offset:1024 lds\n"
        "v_mfma_f32_16x16x32_bf16 v[92:95], a[44:47], a[224:227], v[92:95]\n"
        "v_cvt_pk_bf16_f32 v214, v214, v215\n"
        "v_accvgpr_write_b32 a71, v214\n"
        "s_nop 1\n"
        "v_mfma_f32_16x16x32_bf16 v[96:99], a[68:71], a[224:227], v[96:99]\n"
        "s_add_u32 m0, s65, 0\n"
        "s_waitcnt vmcnt(8)\n"
        "ds_read_b64_tr_b16 a[84:85], v255 offset:9216\n"
        "ds_read_b64_tr_b16 a[86:87], v255 offset:9984\n"
        "v_mfma_f32_16x16x32_bf16 v[100:103], a[68:71], a[228:231], v[100:103]\n"
        "s_mov_b32 s64, 768\n"
        "ds_read_b64_tr_b16 a[108:109], v255 offset:9248\n"
        "ds_read_b64_tr_b16 a[110:111], v255 offset:10016\n"
        "v_mfma_f32_16x16x32_bf16 v[104:107], a[68:71], a[232:235], v[104:107]\n"
        "buffer_load_dwordx4 v128, s[20:23], s64 offen lds\n"
        "ds_read_b64_tr_b16 a[132:133], v255 offset:9280\n"
        "ds_read_b64_tr_b16 a[134:135], v255 offset:10048\n"
        "v_mfma_f32_16x16x32_bf16 v[108:111], a[68:71], a[236:239], v[108:111]\n"
        "s_waitcnt lgkmcnt(15)\n"
        "ds_read_b128 a[224:227], v240 offset:1664\n"
        "ds_read_b128 a[228:231], v240 offset:12288\n"
        "v_mfma_f32_16x16x32_bf16 v[112:115], a[72:75], a[240:243], 0\n"
        "s_waitcnt lgkmcnt(11)\n"
        "ds_read_b128 a[232:235], v241 offset:12288\n"
        "ds_read_b128 a[236:239], v240 offset:12416\n"
        "v_mfma_f32_16x16x32_bf16 v[112:115], a[76:79], a[244:247], v[112:115]\n"
        "s_waitcnt lgkmcnt(12)\n"
        "v_cvt_pk_bf16_f32 v156, v64, v65\n"
        "v_cvt_pk_bf16_f32 v157, v66, v67\n"
        "v_mfma_f32_16x16x32_bf16 v[112:115], a[80:83], a[248:251], v[112:115]\n"
        "buffer_load_dwordx4 v129, s[20:23], s64 offen offset:1024 lds\n"
        "v_mfma_f32_16x16x32_bf16 v[116:119], a[104:107], a[248:251], 0\n"
        "v_cvt_pk_bf16_f32 v196, v72, v73\n"
        "v_cvt_pk_bf16_f32 v197, v74, v75\n"
        "v_mfma_f32_16x16x32_bf16 v[116:119], a[100:103], a[244:247], v[116:119]\n"
        "v_cvt_pk_bf16_f32 v176, v68, v69\n"
        "v_cvt_pk_bf16_f32 v177, v70, v71\n"
        "v_mfma_f32_16x16x32_bf16 v[116:119], a[96:99], a[240:243], v[116:119]\n"
        "v_cvt_pk_bf16_f32 v216, v76, v77\n"
        "v_cvt_pk_bf16_f32 v217, v78, v79\n"
        "v_mfma_f32_16x16x32_bf16 v[120:123], a[120:123], a[240:243], 0\n"
        "buffer_load_dwordx4 v130, s[20:23], s64 offen offset:2048 lds\n"
        "v_mfma_f32_16x16x32_bf16 v[120:123], a[124:127], a[244:247], v[120:123]\n"
        "ds_read_b128 a[240:243], v240 offset:13824\n"
        "ds_read_b128 a[244:247], v241 offset:13824\n"
        "v_mfma_f32_16x16x32_bf16 v[120:123], a[128:131], a[248:251], v[120:123]\n"
        "s_waitcnt lgkmcnt(13)\n"
        "s_add_u32 m0, s65, 3072\n"
        "ds_read_b128 a[248:251], v240 offset:13952\n"
        "v_cvt_pk_bf16_f32 v218, v80, v81\n"
        "v_mfma_f32_16x16x32_bf16 v[124:127], a[72:75], a[216:219], 0\n"
        "s_waitcnt lgkmcnt(13)\n"
        "s_mov_b32 s64, 37632\n"
        "v_cvt_pk_bf16_f32 v219, v82, v83\n"
        "v_cvt_pk_bf16_f32 v198, v84, v85\n"
        "v_mfma_f32_16x16x32_bf16 v[124:127], a[76:79], a[220:223], v[124:127]\n"
        "s_waitcnt lgkmcnt(6)\n"
        "buffer_load_dwordx4 v128, s[20:23], s64 offen lds\n"
        "s_waitcnt vmcnt(9)\n"
        "ds_read_b64_tr_b16 a[88:89], v255 offset:12288\n"
        "ds_read_b64_tr_b16 a[90:91], v255 offset:13056\n"
        "v_mfma_f32_16x16x32_bf16 v[124:127], a[80:83], a[224:227], v[124:127]\n"
        "ds_read_b64_tr_b16 a[112:113], v255 offset:12320\n"
        "ds_read_b64_tr_b16 a[114:115], v255 offset:13088\n"
        "v_mfma_f32_16x16x32_bf16 v[64:67], a[104:107], a[224:227], 0\n"
        "ds_read_b64_tr_b16 a[136:137], v255 offset:12352\n"
        "ds_read_b64_tr_b16 a[138:139], v255 offset:13120\n"
        "v_mfma_f32_16x16x32_bf16 v[64:67], a[100:103], a[220:223], v[64:67]\n"
        "v_cvt_pk_bf16_f32 v199, v86, v87\n"
        "v_cvt_pk_bf16_f32 v178, v88, v89\n"
        "v_mfma_f32_16x16x32_bf16 v[64:67], a[96:99], a[216:219], v[64:67]\n"
        "buffer_load_dwordx4 v129, s[20:23], s64 offen offset:1024 lds\n"
        "v_mfma_f32_16x16x32_bf16 v[72:75], a[120:123], a[216:219], 0\n"
        "ds_read_b128 a[216:219], v241 offset:128\n"
        "v_cvt_pk_bf16_f32 v179, v90, v91\n"
        "v_mfma_f32_16x16x32_bf16 v[72:75], a[124:127], a[220:223], v[72:75]\n"
        "ds_read_b128 a[220:223], v240 offset:256\n"
        "v_cvt_pk_bf16_f32 v158, v92, v93\n"
        "v_mfma_f32_16x16x32_bf16 v[72:75], a[128:131], a[224:227], v[72:75]\n"
        "s_waitcnt lgkmcnt(13)\n"
        "ds_read_b128 a[224:227], v241 offset:256\n"
        "v_cvt_pk_bf16_f32 v159, v94, v95\n"
        "v_mfma_f32_16x16x32_bf16 v[68:71], a[72:75], a[228:231], 0\n"
        "s_waitcnt lgkmcnt(13)\n"
        "buffer_load_dwordx4 v130, s[20:23], s64 offen offset:2048 lds\n"
        "v_mfma_f32_16x16x32_bf16 v[68:71], a[76:79], a[232:235], v[68:71]\n"
        "s_waitcnt lgkmcnt(12)\n"
        "v_cvt_pk_bf16_f32 v160, v96, v97\n"
        "v_cvt_pk_bf16_f32 v161, v98, v99\n"
        "v_mfma_f32_16x16x32_bf16 v[68:71], a[80:83], a[236:239], v[68:71]\n"
        "s_add_u32 m0, s65, 6144\n"
        "ds_read_b128 v[44:47], v143 offset:8192\n"
        "v_cvt_pk_bf16_f32 v166, v96, v97\n"
        "v_mfma_f32_16x16x32_bf16 v[76:79], a[104:107], a[236:239], 0\n"
        "s_mov_b32 s64, 74496\n"
        "v_cvt_pk_bf16_f32 v167, v98, v99\n"
        "v_cvt_pk_bf16_f32 v180, v100, v101\n"
        "v_mfma_f32_16x16x32_bf16 v[76:79], a[100:103], a[232:235], v[76:79]\n"
        "buffer_load_dwordx4 v128, s[20:23], s64 offen lds\n"
        "s_waitcnt vmcnt(9)\n"
        "ds_read_b64_tr_b16 a[92:93], v255 offset:15360\n"
        "ds_read_b64_tr_b16 a[94:95], v255 offset:16128\n"
        "v_mfma_f32_16x16x32_bf16 v[76:79], a[96:99], a[228:231], v[76:79]\n"
        "ds_read_b64_tr_b16 a[116:117], v255 offset:15392\n"
        "ds_read_b64_tr_b16 a[118:119], v255 offset:16160\n"
        "v_mfma_f32_16x16x32_bf16 v[80:83], a[120:123], a[228:231], 0\n"
        "ds_read_b64_tr_b16 a[140:141], v255 offset:15424\n"
        "ds_read_b64_tr_b16 a[142:143], v255 offset:16192\n"
        "v_mfma_f32_16x16x32_bf16 v[80:83], a[124:127], a[232:235], v[80:83]\n"
        "ds_read_b128 a[228:231], v241 offset:1664\n"
        "ds_read_b128 a[232:235], v240 offset:1792\n"
        "v_mfma_f32_16x16x32_bf16 v[80:83], a[128:131], a[236:239], v[80:83]\n"
        "s_waitcnt lgkmcnt(15)\n"
        "buffer_load_dwordx4 v129, s[20:23], s64 offen offset:1024 lds\n"
        "v_mfma_f32_16x16x32_bf16 v[84:87], a[72:75], a[240:243], 0\n"
        "s_waitcnt lgkmcnt(15)\n"
        "ds_read_b128 v[48:51], v140 offset:9216\n"
        "ds_read_b128 a[236:239], v241 offset:1792\n"
        "v_mfma_f32_16x16x32_bf16 v[84:87], a[76:79], a[244:247], v[84:87]\n"
        "s_waitcnt lgkmcnt(15)\n"
        "v_cvt_pk_bf16_f32 v181, v102, v103\n"
        "v_cvt_pk_bf16_f32 v186, v100, v101\n"
        "v_mfma_f32_16x16x32_bf16 v[84:87], a[80:83], a[248:251], v[84:87]\n"
        "v_cvt_pk_bf16_f32 v187, v102, v103\n"
        "v_cvt_pk_bf16_f32 v200, v104, v105\n"
        "v_mfma_f32_16x16x32_bf16 v[88:91], a[104:107], a[248:251], 0\n"
        "v_cvt_pk_bf16_f32 v201, v106, v107\n"
        "v_cvt_pk_bf16_f32 v206, v104, v105\n"
        "v_mfma_f32_16x16x32_bf16 v[88:91], a[100:103], a[244:247], v[88:91]\n"
        "v_cvt_pk_bf16_f32 v207, v106, v107\n"
        "s_waitcnt lgkmcnt(10)\n"
        "v_lshl_add_u32 v44, v44, 2, s67\n"
        "v_mfma_f32_16x16x32_bf16 v[88:91], a[96:99], a[240:243], v[88:91]\n"
        "ds_read_b128 v[52:55], v141 offset:9216\n"
        "ds_read_b32 v44, v44\n"
        "v_mfma_f32_16x16x32_bf16 v[92:95], a[120:123], a[240:243], 0\n"
        "ds_read_b128 a[240:243], v241 offset:12416\n"
        "v_lshl_add_u32 v45, v45, 2, s67\n"
        "v_mfma_f32_16x16x32_bf16 v[92:95], a[124:127], a[244:247], v[92:95]\n"
        "ds_read_b32 v45, v45\n"
        "ds_read_b128 a[244:247], v240 offset:12544\n"
        "v_mfma_f32_16x16x32_bf16 v[92:95], a[128:131], a[248:251], v[92:95]\n"
        "ds_read_b128 a[248:251], v241 offset:12544\n"
        "v_lshl_add_u32 v46, v46, 2, s67\n"
        "v_mfma_f32_16x16x32_bf16 v[112:115], a[84:87], a[216:219], v[112:115]\n"
        "ds_read_b32 v46, v46\n"
        "v_lshl_add_u32 v47, v47, 2, s67\n"
        "v_mfma_f32_16x16x32_bf16 v[112:115], a[88:91], a[220:223], v[112:115]\n"
        "s_waitcnt lgkmcnt(15)\n"
        "ds_read_b32 v47, v47\n"
        "v_cvt_pk_bf16_f32 v220, v108, v109\n"
        "v_mfma_f32_16x16x32_bf16 v[112:115], a[92:95], a[224:227], v[112:115]\n"
        "s_waitcnt lgkmcnt(14)\n"
        "ds_read_b128 v[56:59], v142 offset:9216\n"
        "v_cvt_pk_bf16_f32 v221, v110, v111\n"
        "v_mfma_f32_16x16x32_bf16 v[116:119], a[116:119], a[224:227], v[116:119]\n"
        "v_cvt_pk_bf16_f32 v226, v108, v109\n"
        "v_cvt_pk_bf16_f32 v227, v110, v111\n"
        "v_mfma_f32_16x16x32_bf16 v[116:119], a[112:115], a[220:223], v[116:119]\n"
        "s_waitcnt lgkmcnt(10)\n"
        "v_lshl_add_u32 v48, v48, 2, s67\n"
        "v_lshl_add_u32 v49, v49, 2, s67\n"
        "v_mfma_f32_16x16x32_bf16 v[116:119], a[108:111], a[216:219], v[116:119]\n"
        "ds_read_b32 v48, v48\n"
        "ds_read_b32 v49, v49\n"
        "v_mfma_f32_16x16x32_bf16 v[120:123], a[132:135], a[216:219], v[120:123]\n"
        "ds_read_b128 a[216:219], v241 offset:13952\n"
        "v_lshl_add_u32 v50, v50, 2, s67\n"
        "v_mfma_f32_16x16x32_bf16 v[120:123], a[136:139], a[220:223], v[120:123]\n"
        "ds_read_b32 v50, v50\n"
        "ds_read_b128 a[220:223], v240 offset:14080\n"
        "v_mfma_f32_16x16x32_bf16 v[120:123], a[140:143], a[224:227], v[120:123]\n"
        "ds_read_b128 v[60:63], v143 offset:9216\n"
        "ds_read_b128 a[224:227], v241 offset:14080\n"
        "v_lshl_add_u32 v51, v51, 2, s67\n"
        "v_mfma_f32_16x16x32_bf16 v[124:127], a[84:87], a[228:231], v[124:127]\n"
        "ds_read_b32 v51, v51\n"
        "v_cvt_pk_bf16_f32 v152, v112, v113\n"
        "v_cvt_pk_bf16_f32 v153, v114, v115\n"
        "v_mfma_f32_16x16x32_bf16 v[124:127], a[88:91], a[232:235], v[124:127]\n"
        "s_waitcnt lgkmcnt(15)\n"
        "v_cvt_pk_bf16_f32 v154, v116, v117\n"
        "v_cvt_pk_bf16_f32 v155, v118, v119\n"
        "v_mfma_f32_16x16x32_bf16 v[124:127], a[92:95], a[236:239], v[124:127]\n"
        "s_waitcnt lgkmcnt(15)\n"
        "v_lshl_add_u32 v52, v52, 2, s67\n"
        "v_lshl_add_u32 v53, v53, 2, s67\n"
        "v_mfma_f32_16x16x32_bf16 v[64:67], a[116:119], a[236:239], v[64:67]\n"
        "buffer_load_dwordx4 v130, s[20:23], s64 offen offset:2048 lds\n"
        "v_mfma_f32_16x16x32_bf16 v[64:67], a[112:115], a[232:235], v[64:67]\n"
        "ds_read_b32 v52, v52\n"
        "ds_read_b32 v53, v53\n"
        "v_mfma_f32_16x16x32_bf16 v[64:67], a[108:111], a[228:231], v[64:67]\n"
        "s_add_u32 m0, s65, 9216\n"
        "v_lshl_add_u32 v54, v54, 2, s67\n"
        "v_lshl_add_u32 v55, v55, 2, s67\n"
        "v_mfma_f32_16x16x32_bf16 v[72:75], a[132:135], a[228:231], v[72:75]\n"
        "s_mov_b32 s64, 111360\n"
        "ds_read_b32 v54, v54\n"
        "ds_read_b32 v55, v55\n"
        "v_cvt_pk_bf16_f32 v168, v120, v121\n"
        "v_cvt_pk_bf16_f32 v169, v122, v123\n"
        "v_mfma_f32_16x16x32_bf16 v[72:75], a[136:139], a[232:235], v[72:75]\n"
        "buffer_load_dwordx4 v128, s[20:23], s64 offen lds\n"
        "v_mfma_f32_16x16x32_bf16 v[72:75], a[140:143], a[236:239], v[72:75]\n"
        "s_waitcnt lgkmcnt(15)\n"
        "v_cvt_pk_bf16_f32 v172, v124, v125\n"
        "v_cvt_pk_bf16_f32 v173, v126, v127\n"
        "v_mfma_f32_16x16x32_bf16 v[68:71], a[84:87], a[240:243], v[68:71]\n"
        "s_waitcnt lgkmcnt(12)\n"
        "v_lshl_add_u32 v56, v56, 2, s67\n"
        "v_lshl_add_u32 v57, v57, 2, s67\n"
        "v_mfma_f32_16x16x32_bf16 v[68:71], a[88:91], a[244:247], v[68:71]\n"
        "ds_read_b32 v56, v56\n"
        "ds_read_b32 v57, v57\n"
        "v_mfma_f32_16x16x32_bf16 v[68:71], a[92:95], a[248:251], v[68:71]\n"
        "buffer_load_dwordx4 v129, s[20:23], s64 offen offset:1024 lds\n"
        "v_lshl_add_u32 v58, v58, 2, s67\n"
        "v_lshl_add_u32 v59, v59, 2, s67\n"
        "v_mfma_f32_16x16x32_bf16 v[76:79], a[116:119], a[248:251], v[76:79]\n"
        "ds_read_b32 v58, v58\n"
        "ds_read_b32 v59, v59\n"
        "v_cvt_pk_bf16_f32 v174, v64, v65\n"
        "v_cvt_pk_bf16_f32 v175, v66, v67\n"
        "v_mfma_f32_16x16x32_bf16 v[76:79], a[112:115], a[244:247], v[76:79]\n"
        "v_cvt_pk_bf16_f32 v190, v72, v73\n"
        "v_cvt_pk_bf16_f32 v191, v74, v75\n"
        "v_mfma_f32_16x16x32_bf16 v[76:79], a[108:111], a[240:243], v[76:79]\n"
        "s_waitcnt lgkmcnt(10)\n"
        "v_lshl_add_u32 v60, v60, 2, s67\n"
        "v_lshl_add_u32 v61, v61, 2, s67\n"
        "v_mfma_f32_16x16x32_bf16 v[80:83], a[132:135], a[240:243], v[80:83]\n"
        "buffer_load_dwordx4 v130, s[20:23], s64 offen offset:2048 lds\n"
        "s_waitcnt vmcnt(12)\n"
        "s_barrier\n"
        "ds_read_b128 v[162:165], v251 offset:0\n"
        "v_mfma_f32_16x16x32_bf16 v[80:83], a[136:139], a[244:247], v[80:83]\n"
        "ds_read_b32 v60, v60\n"
        "ds_read_b32 v61, v61\n"
        "v_mfma_f32_16x16x32_bf16 v[80:83], a[140:143], a[248:251], v[80:83]\n"
        "s_add_u32 m0, s65, 12288\n"
        "ds_read_b128 v[182:185], v251 offset:512\n"
        "ds_read_b128 v[202:205], v251 offset:4096\n"
        "v_lshl_add_u32 v62, v62, 2, s67\n"
        "v_lshl_add_u32 v63, v63, 2, s67\n"
        "v_mfma_f32_16x16x32_bf16 v[84:87], a[84:87], a[216:219], v[84:87]\n"
        "s_mov_b32 s64, 148224\n"
        "ds_read_b32 v62, v62\n"
        "ds_read_b32 v63, v63\n"
        "v_cvt_pk_bf16_f32 v192, v68, v69\n"
        "v_cvt_pk_bf16_f32 v193, v70, v71\n"
        "v_mfma_f32_16x16x32_bf16 v[84:87], a[88:91], a[220:223], v[84:87]\n"
        "s_waitcnt lgkmcnt(15)\n"
        "buffer_load_dwordx4 v128, s[20:23], s64 offen lds\n"
        "v_mfma_f32_16x16x32_bf16 v[84:87], a[92:95], a[224:227], v[84:87]\n"
        "v_cvt_pk_bf16_f32 v194, v76, v77\n"
        "v_cvt_pk_bf16_f32 v195, v78, v79\n"
        "v_mfma_f32_16x16x32_bf16 v[88:91], a[116:119], a[224:227], v[88:91]\n"
        "ds_read_b128 a[228:231], v240 offset:0\n"
        "v_cvt_pk_bf16_f32 v208, v80, v81\n"
        "v_mfma_f32_16x16x32_bf16 v[88:91], a[112:115], a[220:223], v[88:91]\n"
        "ds_read_b128 a[232:235], v241 offset:0\n"
        "v_cvt_pk_bf16_f32 v209, v82, v83\n"
        "v_mfma_f32_16x16x32_bf16 v[88:91], a[108:111], a[216:219], v[88:91]\n"
        "buffer_load_dwordx4 v129, s[20:23], s64 offen offset:1024 lds\n"
        "v_mfma_f32_16x16x32_bf16 v[64:67], v[152:155], v[156:159], v[0:3]\n"
        "s_waitcnt lgkmcnt(8)\n"
        "s_waitcnt vmcnt(11)\n"
        "ds_read_b64_tr_b16 a[144:145], v255 offset:0\n"
        "ds_read_b64_tr_b16 a[146:147], v255 offset:768\n"
        "v_mfma_f32_16x16x32_bf16 v[64:67], v[168:171], v[160:163], v[64:67]\n"
        "ds_read_b64_tr_b16 a[168:169], v255 offset:32\n"
        "ds_read_b64_tr_b16 a[170:171], v255 offset:800\n"
        "v_mfma_f32_16x16x32_bf16 v[96:99], v[172:175], v[156:159], v[4:7]\n"
        "ds_read_b64_tr_b16 a[192:193], v255 offset:64\n"
        "ds_read_b64_tr_b16 a[194:195], v255 offset:832\n"
        "v_mfma_f32_16x16x32_bf16 v[96:99], v[188:191], v[164:167], v[96:99]\n"
        "ds_read_b128 v[162:165], v252 offset:0\n"
        "buffer_load_dwordx4 v130, s[20:23], s64 offen offset:2048 lds\n"
        "s_add_u32 m0, s65, 15360\n"
        "v_exp_f32 v64, v64\n"
        "v_cvt_pk_bf16_f32 v212, v84, v85\n"
        "v_mfma_f32_16x16x32_bf16 v[92:95], a[132:135], a[216:219], v[92:95]\n"
        "s_mov_b32 s64, 185088\n"
        "ds_read_b128 a[236:239], v240 offset:128\n"
        "v_exp_f32 v65, v65\n"
        "v_exp_f32 v66, v66\n"
        "v_cvt_pk_bf16_f32 v64, v64, v65\n"
        "v_mfma_f32_16x16x32_bf16 v[92:95], a[136:139], a[220:223], v[92:95]\n"
        "buffer_load_dwordx4 v128, s[20:23], s64 offen lds\n"
        "v_exp_f32 v96, v96\n"
        "v_cvt_pk_bf16_f32 v213, v86, v87\n"
        "v_mfma_f32_16x16x32_bf16 v[92:95], a[140:143], a[224:227], v[92:95]\n"
        "v_exp_f32 v67, v67\n"
        "v_exp_f32 v97, v97\n"
        "v_cvt_pk_bf16_f32 v65, v66, v67\n"
        "v_cvt_pk_bf16_f32 v66, v96, v97\n"
        "v_mfma_f32_16x16x32_bf16 v[72:75], v[152:155], v[176:179], v[16:19]\n"
        "s_waitcnt lgkmcnt(13)\n"
        "buffer_load_dwordx4 v129, s[20:23], s64 offen offset:1024 lds\n"
        "v_exp_f32 v98, v98\n"
        "v_exp_f32 v99, v99\n"
        "v_mfma_f32_16x16x32_bf16 v[72:75], v[168:171], v[180:183], v[72:75]\n"
        ".long 0xbec200ff\n"
        "42:\n"
        "s_branch 60b\n"
        "s_waitcnt vmcnt(11)\n"
        "ds_read_b64_tr_b16 a[148:149], v255 offset:3072\n"
        "ds_read_b64_tr_b16 a[150:151], v255 offset:3840\n"
        "v_cvt_pk_bf16_f32 v214, v88, v89\n"
        "v_cvt_pk_bf16_f32 v215, v90, v91\n"
        "v_cvt_pk_bf16_f32 v67, v98, v99\n"
        "v_mfma_f32_16x16x32_bf16 v[100:103], v[172:175], v[176:179], v[20:23]\n"
        "buffer_load_dwordx4 v130, s[20:23], s64 offen offset:2048 lds\n"
        "ds_read_b64_tr_b16 a[172:173], v255 offset:3104\n"
        "ds_read_b64_tr_b16 a[174:175], v255 offset:3872\n"
        "v_exp_f32 v72, v72\n"
        "v_mfma_f32_16x16x32_bf16 v[100:103], v[188:191], v[184:187], v[100:103]\n"
        "ds_read_b128 v[182:185], v252 offset:512\n"
        "ds_read_b64_tr_b16 a[196:197], v255 offset:3136\n"
        "ds_read_b64_tr_b16 a[198:199], v255 offset:3904\n"
        "v_exp_f32 v73, v73\n"
        "v_mfma_f32_16x16x32_bf16 v[68:71], v[192:195], v[156:159], v[8:11]\n"
        "s_waitcnt lgkmcnt(8)\n"
        "ds_read_b128 a[240:243], v240 offset:1536\n"
        "ds_read_b128 a[244:247], v241 offset:1536\n"
        "v_exp_f32 v74, v74\n"
        "v_cvt_pk_bf16_f32 v72, v72, v73\n"
        "v_mfma_f32_16x16x32_bf16 v[68:71], v[208:211], v[160:163], v[68:71]\n"
        "ds_read_b128 a[248:251], v240 offset:1664\n"
        "v_exp_f32 v100, v100\n"
        "v_exp_f32 v75, v75\n"
        "v_mfma_f32_16x16x32_bf16 v[76:79], v[192:195], v[176:179], v[24:27]\n"
        "s_waitcnt lgkmcnt(5)\n"
        "s_add_u32 m0, s40, 98304\n"
        "v_exp_f32 v101, v101\n"
        "v_cvt_pk_bf16_f32 v73, v74, v75\n"
        "v_exp_f32 v102, v102\n"
        "v_cvt_pk_bf16_f32 v74, v100, v101\n"
        "v_mfma_f32_16x16x32_bf16 v[76:79], v[208:211], v[180:183], v[76:79]\n"
        "s_sub_u32 s64, s47, 6291456\n"
        "v_exp_f32 v68, v68\n"
        "v_exp_f32 v103, v103\n"
        "v_exp_f32 v69, v69\n"
        "v_mfma_f32_16x16x32_bf16 v[104:107], v[212:215], v[156:159], v[12:15]\n"
        "buffer_load_dwordx4 v242, s[8:11], s64 offen lds\n"
        "s_waitcnt vmcnt(10)\n"
        "ds_read_b64_tr_b16 a[152:153], v255 offset:6144\n"
        "ds_read_b64_tr_b16 a[154:155], v255 offset:6912\n"
        "v_cvt_pk_bf16_f32 v230, v92, v93\n"
        "v_cvt_pk_bf16_f32 v231, v94, v95\n"
        "s_nop 1\n"
        "v_mfma_f32_16x16x32_bf16 v[104:107], v[228:231], v[164:167], v[104:107]\n"
        "s_add_u32 m0, s40, 99328\n"
        "ds_read_b64_tr_b16 a[176:177], v255 offset:6176\n"
        "ds_read_b64_tr_b16 a[178:179], v255 offset:6944\n"
        "v_cvt_pk_bf16_f32 v75, v102, v103\n"
        "v_exp_f32 v70, v70\n"
        "v_cvt_pk_bf16_f32 v68, v68, v69\n"
        "v_mfma_f32_16x16x32_bf16 v[108:111], v[212:215], v[176:179], v[28:31]\n"
        "ds_read_b64_tr_b16 a[200:201], v255 offset:6208\n"
        "ds_read_b64_tr_b16 a[202:203], v255 offset:6976\n"
        "v_exp_f32 v76, v76\n"
        "v_exp_f32 v71, v71\n"
        "v_mfma_f32_16x16x32_bf16 v[108:111], v[228:231], v[184:187], v[108:111]\n"
        "buffer_load_dwordx4 v243, s[8:11], s64 offen lds\n"
        "v_exp_f32 v77, v77\n"
        "v_cvt_pk_bf16_f32 v69, v70, v71\n"
        "v_cvt_pk_bf16_f32 v76, v76, v77\n"
        "v_mfma_f32_16x16x32_bf16 v[80:83], v[152:155], v[196:199], v[32:35]\n"
        "s_add_u32 m0, s40, 100352\n"
        "ds_read_b128 a[216:219], v240 offset:12288\n"
        "v_exp_f32 v78, v78\n"
        "v_exp_f32 v79, v79\n"
        "v_mfma_f32_16x16x32_bf16 v[80:83], v[168:171], v[200:203], v[80:83]\n"
        "ds_read_b128 a[220:223], v241 offset:12288\n"
        "v_cvt_pk_bf16_f32 v77, v78, v79\n"
        "v_mfma_f32_16x16x32_bf16 v[112:115], a[228:231], a[144:147], 0\n"
        "buffer_load_dwordx4 v244, s[8:11], s64 offen lds\n"
        "v_mfma_f32_16x16x32_bf16 v[112:115], a[232:235], a[148:151], v[112:115]\n"
        "s_waitcnt lgkmcnt(6)\n"
        "ds_read_b128 a[224:227], v240 offset:12416\n"
        "v_mfma_f32_16x16x32_bf16 v[112:115], a[236:239], a[152:155], v[112:115]\n"
        "s_waitcnt lgkmcnt(5)\n"
        "s_add_u32 m0, s40, 110592\n"
        "v_exp_f32 v104, v104\n"
        "v_mfma_f32_16x16x32_bf16 v[116:119], a[236:239], a[176:179], 0\n"
        "s_sub_u32 s64, s47, 5505024\n"
        "ds_read_b128 v[222:225], v251 offset:4608\n"
        "v_mfma_f32_16x16x32_bf16 v[116:119], a[232:235], a[172:175], v[116:119]\n"
        "buffer_load_dwordx4 v242, s[8:11], s64 offen lds\n"
        "v_mfma_f32_16x16x32_bf16 v[116:119], a[228:231], a[168:171], v[116:119]\n"
        "v_exp_f32 v105, v105\n"
        "v_mfma_f32_16x16x32_bf16 v[96:99], v[172:175], v[196:199], v[36:39]\n"
        "s_add_u32 m0, s40, 111616\n"
        "s_waitcnt vmcnt(10)\n"
        "ds_read_b64_tr_b16 a[156:157], v255 offset:9216\n"
        "ds_read_b64_tr_b16 a[158:159], v255 offset:9984\n"
        "v_mfma_f32_16x16x32_bf16 v[96:99], v[188:191], v[204:207], v[96:99]\n"
        "ds_read_b64_tr_b16 a[180:181], v255 offset:9248\n"
        "ds_read_b64_tr_b16 a[182:183], v255 offset:10016\n"
        "ds_read_b128 v[202:205], v252 offset:4096\n"
        "v_mfma_f32_16x16x32_bf16 v[120:123], a[228:231], a[192:195], 0\n"
        "buffer_load_dwordx4 v243, s[8:11], s64 offen lds\n"
        "ds_read_b64_tr_b16 a[204:205], v255 offset:9280\n"
        "ds_read_b64_tr_b16 a[206:207], v255 offset:10048\n"
        "v_mfma_f32_16x16x32_bf16 v[120:123], a[232:235], a[196:199], v[120:123]\n"
        "s_waitcnt lgkmcnt(11)\n"
        "v_exp_f32 v106, v106\n"
        "v_mfma_f32_16x16x32_bf16 v[120:123], a[236:239], a[200:203], v[120:123]\n"
        "s_add_u32 m0, s40, 112640\n"
        "v_exp_f32 v108, v108\n"
        "v_mfma_f32_16x16x32_bf16 v[124:127], a[240:243], a[144:147], 0\n"
        "ds_read_b128 a[228:231], v240 offset:13824\n"
        "v_cvt_pk_bf16_f32 v70, v104, v105\n"
        "v_mfma_f32_16x16x32_bf16 v[124:127], a[244:247], a[148:151], v[124:127]\n"
        "buffer_load_dwordx4 v244, s[8:11], s64 offen lds\n"
        "v_mfma_f32_16x16x32_bf16 v[124:127], a[248:251], a[152:155], v[124:127]\n"
        "ds_read_b128 a[232:235], v241 offset:13824\n"
        "ds_read_b128 a[236:239], v240 offset:13952\n"
        "v_mfma_f32_16x16x32_bf16 v[156:159], a[248:251], a[176:179], 0\n"
        "s_xor_b32 m0, s48, 8192\n"
        "v_exp_f32 v107, v107\n"
        "v_mfma_f32_16x16x32_bf16 v[156:159], a[244:247], a[172:175], v[156:159]\n"
        "s_sub_u32 s64, s36, 2097152\n"
        "v_exp_f32 v109, v109\n"
        "v_mfma_f32_16x16x32_bf16 v[156:159], a[240:243], a[168:171], v[156:159]\n"
        "buffer_load_dwordx4 v249, s[12:15], s64 offen lds\n"
        "v_mfma_f32_16x16x32_bf16 v[84:87], v[192:195], v[196:199], v[40:43]\n"
        "s_waitcnt lgkmcnt(5)\n"
        "v_cvt_pk_bf16_f32 v71, v106, v107\n"
        "v_cvt_pk_bf16_f32 v78, v108, v109\n"
        "v_mfma_f32_16x16x32_bf16 v[84:87], v[208:211], v[200:203], v[84:87]\n"
        "v_exp_f32 v110, v110\n"
        "v_mfma_f32_16x16x32_bf16 v[160:163], a[240:243], a[192:195], 0\n"
        "s_waitcnt vmcnt(10)\n"
        "ds_read_b64_tr_b16 a[160:161], v255 offset:12288\n"
        "ds_read_b64_tr_b16 a[162:163], v255 offset:13056\n"
        "v_mfma_f32_16x16x32_bf16 v[160:163], a[244:247], a[196:199], v[160:163]\n"
        "buffer_load_dwordx4 v253, s[12:15], s64 offen offset:1024 lds\n"
        "ds_read_b64_tr_b16 a[184:185], v255 offset:12320\n"
        "ds_read_b64_tr_b16 a[186:187], v255 offset:13088\n"
        "v_mfma_f32_16x16x32_bf16 v[160:163], a[248:251], a[200:203], v[160:163]\n"
        "ds_read_b64_tr_b16 a[208:209], v255 offset:12352\n"
        "ds_read_b64_tr_b16 a[210:211], v255 offset:13120\n"
        "v_exp_f32 v80, v80\n"
        "v_mfma_f32_16x16x32_bf16 v[164:167], a[216:219], a[144:147], 0\n"
        "s_add_u32 m0, s65, 12288\n"
        "ds_read_b128 a[240:243], v241 offset:128\n"
        "v_exp_f32 v111, v111\n"
        "v_mfma_f32_16x16x32_bf16 v[164:167], a[220:223], a[148:151], v[164:167]\n"
        "s_mov_b32 s64, 12288\n"
        "ds_read_b128 a[244:247], v240 offset:256\n"
        "v_exp_f32 v81, v81\n"
        "v_cvt_pk_bf16_f32 v79, v110, v111\n"
        "v_mfma_f32_16x16x32_bf16 v[164:167], a[224:227], a[152:155], v[164:167]\n"
        "s_waitcnt lgkmcnt(2)\n"
        "buffer_load_dword v136, s[52:55], 0 offen lds\n"
        "v_exp_f32 v82, v82\n"
        "v_mfma_f32_16x16x32_bf16 v[100:103], v[212:215], v[196:199], v[44:47]\n"
        "ds_read_b128 a[248:251], v241 offset:256\n"
        "v_cvt_pk_bf16_f32 v80, v80, v81\n"
        "v_exp_f32 v83, v83\n"
        "v_mfma_f32_16x16x32_bf16 v[100:103], v[228:231], v[204:207], v[100:103]\n"
        "v_cvt_pk_bf16_f32 v81, v82, v83\n"
        "v_mfma_f32_16x16x32_bf16 v[176:179], a[224:227], a[176:179], 0\n"
        "v_exp_f32 v96, v96\n"
        "v_mfma_f32_16x16x32_bf16 v[176:179], a[220:223], a[172:175], v[176:179]\n"
        "v_exp_f32 v97, v97\n"
        "v_mfma_f32_16x16x32_bf16 v[176:179], a[216:219], a[168:171], v[176:179]\n"
        "v_exp_f32 v98, v98\n"
        "v_mfma_f32_16x16x32_bf16 v[180:183], a[216:219], a[192:195], 0\n"
        "v_exp_f32 v99, v99\n"
        "v_mfma_f32_16x16x32_bf16 v[180:183], a[220:223], a[196:199], v[180:183]\n"
        "ds_read_b128 a[216:219], v241 offset:1664\n"
        "v_cvt_pk_bf16_f32 v82, v96, v97\n"
        "v_mfma_f32_16x16x32_bf16 v[180:183], a[224:227], a[200:203], v[180:183]\n"
        "s_waitcnt vmcnt(9)\n"
        "ds_read_b64_tr_b16 a[164:165], v255 offset:15360\n"
        "ds_read_b64_tr_b16 a[166:167], v255 offset:16128\n"
        "v_mfma_f32_16x16x32_bf16 v[88:91], v[152:155], v[216:219], v[48:51]\n"
        "ds_read_b64_tr_b16 a[188:189], v255 offset:15392\n"
        "ds_read_b64_tr_b16 a[190:191], v255 offset:16160\n"
        "v_mfma_f32_16x16x32_bf16 v[88:91], v[168:171], v[220:223], v[88:91]\n"
        "ds_read_b64_tr_b16 a[212:213], v255 offset:15424\n"
        "ds_read_b64_tr_b16 a[214:215], v255 offset:16192\n"
        "v_mfma_f32_16x16x32_bf16 v[184:187], a[228:231], a[144:147], 0\n"
        "ds_read_b128 a[220:223], v240 offset:1792\n"
        "ds_read_b128 a[224:227], v241 offset:1792\n"
        "v_mfma_f32_16x16x32_bf16 v[184:187], a[232:235], a[148:151], v[184:187]\n"
        "v_exp_f32 v84, v84\n"
        "v_mfma_f32_16x16x32_bf16 v[184:187], a[236:239], a[152:155], v[184:187]\n"
        "v_exp_f32 v85, v85\n"
        "v_mfma_f32_16x16x32_bf16 v[196:199], a[236:239], a[176:179], 0\n"
        "v_exp_f32 v86, v86\n"
        "v_mfma_f32_16x16x32_bf16 v[196:199], a[232:235], a[172:175], v[196:199]\n"
        "v_cvt_pk_bf16_f32 v83, v98, v99\n"
        "v_cvt_pk_bf16_f32 v84, v84, v85\n"
        "v_mfma_f32_16x16x32_bf16 v[196:199], a[228:231], a[168:171], v[196:199]\n"
        "v_exp_f32 v87, v87\n"
        "v_mfma_f32_16x16x32_bf16 v[200:203], a[228:231], a[192:195], 0\n"
        "v_exp_f32 v100, v100\n"
        "v_mfma_f32_16x16x32_bf16 v[200:203], a[232:235], a[196:199], v[200:203]\n"
        "ds_read_b128 a[228:231], v241 offset:12416\n"
        "v_cvt_pk_bf16_f32 v85, v86, v87\n"
        "v_mfma_f32_16x16x32_bf16 v[200:203], a[236:239], a[200:203], v[200:203]\n"
        "ds_read_b128 a[232:235], v240 offset:12544\n"
        "v_mfma_f32_16x16x32_bf16 v[104:107], v[172:175], v[216:219], v[52:55]\n"
        "ds_read_b128 a[236:239], v241 offset:12544\n"
        "v_mfma_f32_16x16x32_bf16 v[104:107], v[188:191], v[224:227], v[104:107]\n"
        "s_waitcnt lgkmcnt(14)\n"
        "ds_read_b128 v[222:225], v252 offset:4608\n"
        "v_mfma_f32_16x16x32_bf16 v[112:115], a[240:243], a[156:159], v[112:115]\n"
        "s_waitcnt lgkmcnt(14)\n"
        "v_exp_f32 v101, v101\n"
        "v_mfma_f32_16x16x32_bf16 v[112:115], a[244:247], a[160:163], v[112:115]\n"
        "s_waitcnt lgkmcnt(10)\n"
        "v_exp_f32 v102, v102\n"
        "v_mfma_f32_16x16x32_bf16 v[112:115], a[248:251], a[164:167], v[112:115]\n"
        "s_waitcnt lgkmcnt(8)\n"
        "v_exp_f32 v103, v103\n"
        "v_mfma_f32_16x16x32_bf16 v[116:119], a[248:251], a[188:191], v[116:119]\n"
        "v_exp_f32 v88, v88\n"
        "v_mfma_f32_16x16x32_bf16 v[116:119], a[244:247], a[184:187], v[116:119]\n"
        "v_exp_f32 v89, v89\n"
        "v_mfma_f32_16x16x32_bf16 v[116:119], a[240:243], a[180:183], v[116:119]\n"
        "v_exp_f32 v90, v90\n"
        "v_mfma_f32_16x16x32_bf16 v[120:123], a[240:243], a[204:207], v[120:123]\n"
        "v_cvt_pk_bf16_f32 v128, v112, v113\n"
        "v_cvt_pk_bf16_f32 v129, v114, v115\n"
        "v_mfma_f32_16x16x32_bf16 v[120:123], a[244:247], a[208:211], v[120:123]\n"
        "s_waitcnt lgkmcnt(6)\n"
        "ds_read_b128 a[240:243], v241 offset:13952\n"
        "v_cvt_pk_bf16_f32 v86, v100, v101\n"
        "v_mfma_f32_16x16x32_bf16 v[120:123], a[248:251], a[212:215], v[120:123]\n"
        "ds_read_b128 a[244:247], v240 offset:14080\n"
        "v_cvt_pk_bf16_f32 v136, v116, v117\n"
        "v_mfma_f32_16x16x32_bf16 v[124:127], a[216:219], a[156:159], v[124:127]\n"
        "s_waitcnt lgkmcnt(7)\n"
        "ds_read_b128 a[248:251], v241 offset:14080\n"
        "v_cvt_pk_bf16_f32 v137, v118, v119\n"
        "v_mfma_f32_16x16x32_bf16 v[124:127], a[220:223], a[160:163], v[124:127]\n"
        "s_waitcnt lgkmcnt(7)\n"
        "v_exp_f32 v91, v91\n"
        "v_mfma_f32_16x16x32_bf16 v[124:127], a[224:227], a[164:167], v[124:127]\n"
        "v_exp_f32 v104, v104\n"
        "v_mfma_f32_16x16x32_bf16 v[92:95], v[192:195], v[216:219], v[56:59]\n"
        "s_waitcnt lgkmcnt(3)\n"
        "v_cvt_pk_bf16_f32 v87, v102, v103\n"
        "v_cvt_pk_bf16_f32 v144, v120, v121\n"
        "v_mfma_f32_16x16x32_bf16 v[92:95], v[208:211], v[220:223], v[92:95]\n"
        "v_cvt_pk_bf16_f32 v145, v122, v123\n"
        "v_cvt_pk_bf16_f32 v88, v88, v89\n"
        "v_mfma_f32_16x16x32_bf16 v[156:159], a[224:227], a[188:191], v[156:159]\n"
        "v_exp_f32 v105, v105\n"
        "v_mfma_f32_16x16x32_bf16 v[156:159], a[220:223], a[184:187], v[156:159]\n"
        "v_cvt_pk_bf16_f32 v130, v124, v125\n"
        "v_cvt_pk_bf16_f32 v131, v126, v127\n"
        "v_mfma_f32_16x16x32_bf16 v[156:159], a[216:219], a[180:183], v[156:159]\n"
        "v_exp_f32 v106, v106\n"
        "v_mfma_f32_16x16x32_bf16 v[160:163], a[216:219], a[204:207], v[160:163]\n"
        "v_exp_f32 v107, v107\n"
        "v_mfma_f32_16x16x32_bf16 v[160:163], a[220:223], a[208:211], v[160:163]\n"
        "v_exp_f32 v92, v92\n"
        "v_mfma_f32_16x16x32_bf16 v[160:163], a[224:227], a[212:215], v[160:163]\n"
        "v_exp_f32 v93, v93\n"
        "v_mfma_f32_16x16x32_bf16 v[164:167], a[228:231], a[156:159], v[164:167]\n"
        "v_cvt_pk_bf16_f32 v89, v90, v91\n"
        "v_cvt_pk_bf16_f32 v138, v156, v157\n"
        "v_mfma_f32_16x16x32_bf16 v[164:167], a[232:235], a[160:163], v[164:167]\n"
        "v_cvt_pk_bf16_f32 v139, v158, v159\n"
        "v_cvt_pk_bf16_f32 v90, v104, v105\n"
        "v_mfma_f32_16x16x32_bf16 v[164:167], a[236:239], a[164:167], v[164:167]\n"
        "v_exp_f32 v94, v94\n"
        "v_mfma_f32_16x16x32_bf16 v[108:111], v[212:215], v[216:219], v[60:63]\n"
        "v_cvt_pk_bf16_f32 v146, v160, v161\n"
        "v_cvt_pk_bf16_f32 v147, v162, v163\n"
        "v_mfma_f32_16x16x32_bf16 v[108:111], v[228:231], v[224:227], v[108:111]\n"
        "v_exp_f32 v95, v95\n"
        "v_mfma_f32_16x16x32_bf16 v[176:179], a[236:239], a[188:191], v[176:179]\n"
        "v_cvt_pk_bf16_f32 v91, v106, v107\n"
        "v_cvt_pk_bf16_f32 v132, v164, v165\n"
        "v_mfma_f32_16x16x32_bf16 v[176:179], a[232:235], a[184:187], v[176:179]\n"
        "v_cvt_pk_bf16_f32 v133, v166, v167\n"
        "v_cvt_pk_bf16_f32 v92, v92, v93\n"
        "v_mfma_f32_16x16x32_bf16 v[176:179], a[228:231], a[180:183], v[176:179]\n"
        "v_exp_f32 v108, v108\n"
        "v_mfma_f32_16x16x32_bf16 v[180:183], a[228:231], a[204:207], v[180:183]\n"
        "v_mov_b64 v[96:97], 0\n"
        "v_cvt_pk_bf16_f32 v93, v94, v95\n"
        "v_mfma_f32_16x16x32_bf16 v[180:183], a[232:235], a[208:211], v[180:183]\n"
        "v_exp_f32 v109, v109\n"
        "v_mfma_f32_16x16x32_bf16 v[180:183], a[236:239], a[212:215], v[180:183]\n"
        "s_waitcnt lgkmcnt(2)\n"
        "v_exp_f32 v110, v110\n"
        "v_mfma_f32_16x16x32_bf16 v[184:187], a[240:243], a[156:159], v[184:187]\n"
        "s_waitcnt lgkmcnt(1)\n"
        "v_cvt_pk_bf16_f32 v140, v176, v177\n"
        "v_cvt_pk_bf16_f32 v141, v178, v179\n"
        "v_mfma_f32_16x16x32_bf16 v[184:187], a[244:247], a[160:163], v[184:187]\n"
        "s_waitcnt lgkmcnt(0)\n"
        "v_exp_f32 v111, v111\n"
        "v_mfma_f32_16x16x32_bf16 v[184:187], a[248:251], a[164:167], v[184:187]\n"
        "v_add_u32 v240, 24576, v240\n"
        "v_cvt_pk_bf16_f32 v148, v180, v181\n"
        "v_mfma_f32_16x16x32_bf16 v[196:199], a[248:251], a[188:191], v[196:199]\n"
        "v_cvt_pk_bf16_f32 v149, v182, v183\n"
        "v_add_u32 v241, 24576, v241\n"
        "v_mfma_f32_16x16x32_bf16 v[196:199], a[244:247], a[184:187], v[196:199]\n"
        "v_mov_b64 v[172:173], 0\n"
        "v_cvt_pk_bf16_f32 v94, v108, v109\n"
        "v_mfma_f32_16x16x32_bf16 v[196:199], a[240:243], a[180:183], v[196:199]\n"
        "v_cvt_pk_bf16_f32 v134, v184, v185\n"
        "v_cvt_pk_bf16_f32 v135, v186, v187\n"
        "v_mfma_f32_16x16x32_bf16 v[200:203], a[240:243], a[204:207], v[200:203]\n"
        "v_cvt_pk_bf16_f32 v95, v110, v111\n"
        "s_nop 0\n"
        "v_mfma_f32_16x16x32_bf16 v[200:203], a[244:247], a[208:211], v[200:203]\n"
        "s_nop 1\n"
        "v_cvt_pk_bf16_f32 v142, v196, v197\n"
        "v_mfma_f32_16x16x32_bf16 v[200:203], a[248:251], a[212:215], v[200:203]\n"
        "v_cvt_pk_bf16_f32 v143, v198, v199\n"
        "v_smfmac_f32_16x16x64_bf16 v[96:99], a[252:255], v[64:71], v237\n"
        "s_nop 1\n"
        "v_mfma_f32_16x16x32_bf16 v[100:103], v[132:135], v[68:71], 0\n"
        "s_nop 2\n"
        "v_mfma_f32_16x16x32_bf16 v[100:103], v[128:131], v[64:67], v[100:103]\n"
        "v_mov_b64 v[192:193], 0\n"
        "v_cvt_pk_bf16_f32 v150, v200, v201\n"
        "v_mfma_f32_16x16x32_bf16 v[104:107], v[136:139], v[64:67], 0\n"
        "v_cvt_pk_bf16_f32 v151, v202, v203\n"
        "v_add_f32 v97, v96, v97\n"
        "v_mfma_f32_16x16x32_bf16 v[104:107], v[140:143], v[68:71], v[104:107]\n"
        "v_rcp_f32 v96, v97\n"
        "v_mfma_f32_16x16x32_bf16 v[108:111], v[148:151], v[68:71], 0\n"
        "v_mul_f32 v100, v96, v100\n"
        "v_mul_f32 v101, v96, v101\n"
        "v_mfma_f32_16x16x32_bf16 v[108:111], v[144:147], v[64:67], v[108:111]\n"
        "s_waitcnt vmcnt(6)\n"
        "s_barrier\n"
        "ds_read_b128 a[236:239], v240 offset:0\n"
        "ds_read_b128 a[240:243], v241 offset:0\n"
        "v_smfmac_f32_16x16x64_bf16 v[172:175], a[252:255], v[72:79], v237\n"
        "ds_read_b128 a[244:247], v240 offset:128\n"
        "v_mfma_f32_16x16x32_bf16 v[176:179], v[132:135], v[76:79], 0\n"
        "ds_read_b128 a[248:251], v241 offset:128\n"
        "ds_read_b128 a[216:219], v240 offset:256\n"
        "v_mfma_f32_16x16x32_bf16 v[176:179], v[128:131], v[72:75], v[176:179]\n"
        "ds_read_b128 a[220:223], v241 offset:256\n"
        "v_mul_f32 v102, v96, v102\n"
        "v_mfma_f32_16x16x32_bf16 v[180:183], v[136:139], v[72:75], 0\n"
        "v_mul_f32 v103, v96, v103\n"
        "v_cvt_pk_bf16_f32 v100, v100, v101\n"
        "v_mfma_f32_16x16x32_bf16 v[180:183], v[140:143], v[76:79], v[180:183]\n"
        "v_cvt_pk_bf16_f32 v101, v102, v103\n"
        "v_mul_f32 v104, v96, v104\n"
        "v_mfma_f32_16x16x32_bf16 v[184:187], v[148:151], v[76:79], 0\n"
        "buffer_load_dwordx4 v238, s[52:55], s64 offen lds\n"
        "v_mfma_f32_16x16x32_bf16 v[184:187], v[144:147], v[72:75], v[184:187]\n"
        "v_mul_f32 v105, v96, v105\n"
        "v_mul_f32 v106, v96, v106\n"
        "v_smfmac_f32_16x16x64_bf16 v[192:195], a[252:255], v[80:87], v237\n"
        "v_mul_f32 v107, v96, v107\n"
        "v_mfma_f32_16x16x32_bf16 v[196:199], v[132:135], v[84:87], 0\n"
        "v_cvt_pk_bf16_f32 v102, v104, v105\n"
        "v_cvt_pk_bf16_f32 v103, v106, v107\n"
        "v_mfma_f32_16x16x32_bf16 v[196:199], v[128:131], v[80:83], v[196:199]\n"
        "ds_write_b128 v245, v[100:103] offset:0\n"
        "v_mfma_f32_16x16x32_bf16 v[200:203], v[136:139], v[80:83], 0\n"
        "v_mov_b64 v[124:125], 0\n"
        "v_mul_f32 v108, v96, v108\n"
        "v_mfma_f32_16x16x32_bf16 v[200:203], v[140:143], v[84:87], v[200:203]\n"
        "v_mul_f32 v109, v96, v109\n"
        "v_mul_f32 v110, v96, v110\n"
        "v_mul_f32 v111, v96, v111\n"
        "v_mfma_f32_16x16x32_bf16 v[204:207], v[148:151], v[84:87], 0\n"
        "v_cvt_pk_bf16_f32 v108, v108, v109\n"
        "v_cvt_pk_bf16_f32 v109, v110, v111\n"
        "v_mfma_f32_16x16x32_bf16 v[204:207], v[144:147], v[80:83], v[204:207]\n"
        "s_waitcnt lgkmcnt(6)\n"
        "buffer_load_dwordx4 v239, s[52:55], s64 offen offset:1024 lds\n"
        "v_mfma_f32_16x16x32_bf16 v[112:115], a[72:75], a[236:239], 0\n"
        "s_waitcnt lgkmcnt(5)\n"
        "ds_write_b64 v246, v[108:109] offset:0\n"
        "v_mfma_f32_16x16x32_bf16 v[112:115], a[76:79], a[240:243], v[112:115]\n"
        "s_waitcnt lgkmcnt(5)\n"
        "v_max_u32 v236, v96, v97\n"
        "v_add_f32 v173, v172, v173\n"
        "v_mfma_f32_16x16x32_bf16 v[112:115], a[80:83], a[244:247], v[112:115]\n"
        "s_waitcnt lgkmcnt(4)\n"
        "v_rcp_f32 v172, v173\n"
        "v_mfma_f32_16x16x32_bf16 v[112:115], a[84:87], a[248:251], v[112:115]\n"
        "s_waitcnt lgkmcnt(3)\n"
        "v_mul_f32 v176, v172, v176\n"
        "v_mul_f32 v177, v172, v177\n"
        "v_mfma_f32_16x16x32_bf16 v[112:115], a[88:91], a[216:219], v[112:115]\n"
        "s_waitcnt lgkmcnt(2)\n"
        "v_mul_f32 v178, v172, v178\n"
        "v_mul_f32 v179, v172, v179\n"
        "v_mfma_f32_16x16x32_bf16 v[112:115], a[92:95], a[220:223], v[112:115]\n"
        "v_cvt_pk_bf16_f32 v176, v176, v177\n"
        "v_cvt_pk_bf16_f32 v177, v178, v179\n"
        "v_smfmac_f32_16x16x64_bf16 v[124:127], a[252:255], v[88:95], v237\n"
        "v_mul_f32 v180, v172, v180\n"
        "v_mfma_f32_16x16x32_bf16 v[80:83], v[132:135], v[92:95], 0\n"
        "buffer_load_dwordx4 v247, s[52:55], s64 offen offset:2048 lds\n"
        "v_mfma_f32_16x16x32_bf16 v[80:83], v[128:131], v[88:91], v[80:83]\n"
        "v_mul_f32 v181, v172, v181\n"
        "v_cvt_pk_bf16_f32 v152, v112, v113\n"
        "v_mfma_f32_16x16x32_bf16 v[84:87], v[136:139], v[88:91], 0\n"
        "v_cvt_pk_bf16_f32 v153, v114, v115\n"
        "v_mul_f32 v182, v172, v182\n"
        "v_mfma_f32_16x16x32_bf16 v[84:87], v[140:143], v[92:95], v[84:87]\n"
        "v_mul_f32 v183, v172, v183\n"
        "v_cvt_pk_bf16_f32 v178, v180, v181\n"
        "v_mfma_f32_16x16x32_bf16 v[96:99], v[148:151], v[92:95], 0\n"
        "v_cvt_pk_bf16_f32 v179, v182, v183\n"
        "v_mul_f32 v184, v172, v184\n"
        "v_mfma_f32_16x16x32_bf16 v[96:99], v[144:147], v[88:91], v[96:99]\n"
        "ds_write_b128 v245, v[176:179] offset:256\n"
        "v_mfma_f32_16x16x32_bf16 v[116:119], a[116:119], a[220:223], 0\n"
        "s_add_u32 m0, s65, 15360\n"
        "v_mul_f32 v185, v172, v185\n"
        "v_mul_f32 v186, v172, v186\n"
        "v_mfma_f32_16x16x32_bf16 v[116:119], a[112:115], a[216:219], v[116:119]\n"
        "s_mov_b32 s64, 49152\n"
        "v_mul_f32 v187, v172, v187\n"
        "v_add_f32 v125, v124, v125\n"
        "v_mfma_f32_16x16x32_bf16 v[116:119], a[108:111], a[248:251], v[116:119]\n"
        "buffer_load_dwordx4 v238, s[52:55], s64 offen lds\n"
        "v_mfma_f32_16x16x32_bf16 v[116:119], a[104:107], a[244:247], v[116:119]\n"
        "v_rcp_f32 v124, v125\n"
        "v_mfma_f32_16x16x32_bf16 v[116:119], a[100:103], a[240:243], v[116:119]\n"
        "v_cvt_pk_bf16_f32 v184, v184, v185\n"
        "v_cvt_pk_bf16_f32 v185, v186, v187\n"
        "v_mfma_f32_16x16x32_bf16 v[116:119], a[96:99], a[236:239], v[116:119]\n"
        "ds_write_b64 v246, v[184:185] offset:256\n"
        "v_mfma_f32_16x16x32_bf16 v[120:123], a[120:123], a[236:239], 0\n"
        "v_mul_f32 v80, v124, v80\n"
        "v_mul_f32 v81, v124, v81\n"
        "v_mfma_f32_16x16x32_bf16 v[120:123], a[124:127], a[240:243], v[120:123]\n"
        "v_mul_f32 v82, v124, v82\n"
        "v_mul_f32 v83, v124, v83\n"
        "v_mfma_f32_16x16x32_bf16 v[120:123], a[128:131], a[244:247], v[120:123]\n"
        "v_max3_u32 v236, v236, v172, v173\n"
        "v_cvt_pk_bf16_f32 v80, v80, v81\n"
        "v_mfma_f32_16x16x32_bf16 v[120:123], a[132:135], a[248:251], v[120:123]\n"
        "v_cvt_pk_bf16_f32 v81, v82, v83\n"
        "v_mul_f32 v84, v124, v84\n"
        "v_mfma_f32_16x16x32_bf16 v[120:123], a[136:139], a[216:219], v[120:123]\n"
        "buffer_load_dwordx4 v239, s[52:55], s64 offen offset:1024 lds\n"
        "v_mfma_f32_16x16x32_bf16 v[120:123], a[140:143], a[220:223], v[120:123]\n"
        "v_mul_f32 v85, v124, v85\n"
        "v_mul_f32 v86, v124, v86\n"
        "v_mfma_f32_16x16x32_bf16 v[112:115], a[20:23], a[220:223], 0\n"
        "v_mul_f32 v87, v124, v87\n"
        "v_cvt_pk_bf16_f32 v82, v84, v85\n"
        "v_mfma_f32_16x16x32_bf16 v[112:115], a[16:19], a[216:219], v[112:115]\n"
        "v_cvt_pk_bf16_f32 v83, v86, v87\n"
        "v_mul_f32 v96, v124, v96\n"
        "v_mfma_f32_16x16x32_bf16 v[112:115], a[12:15], a[248:251], v[112:115]\n"
        "ds_write_b128 v245, v[80:83] offset:768\n"
        "v_mfma_f32_16x16x32_bf16 v[112:115], a[8:11], a[244:247], v[112:115]\n"
        "v_mul_f32 v97, v124, v97\n"
        "v_mul_f32 v98, v124, v98\n"
        "v_mfma_f32_16x16x32_bf16 v[112:115], a[4:7], a[240:243], v[112:115]\n"
        "v_mul_f32 v99, v124, v99\n"
        "v_add_f32 v193, v192, v193\n"
        "v_mfma_f32_16x16x32_bf16 v[112:115], a[0:3], a[236:239], v[112:115]\n"
        "v_cvt_pk_bf16_f32 v154, v116, v117\n"
        "v_cvt_pk_bf16_f32 v155, v118, v119\n"
        "v_mfma_f32_16x16x32_bf16 v[116:119], a[24:27], a[236:239], 0\n"
        "buffer_load_dwordx4 v247, s[52:55], s64 offen offset:2048 lds\n"
        "v_mfma_f32_16x16x32_bf16 v[116:119], a[28:31], a[240:243], v[116:119]\n"
        "v_rcp_f32 v192, v193\n"
        "v_mfma_f32_16x16x32_bf16 v[116:119], a[32:35], a[244:247], v[116:119]\n"
        "v_cvt_pk_bf16_f32 v96, v96, v97\n"
        "v_cvt_pk_bf16_f32 v97, v98, v99\n"
        "v_mfma_f32_16x16x32_bf16 v[116:119], a[36:39], a[248:251], v[116:119]\n"
        "ds_write_b64 v246, v[96:97] offset:768\n"
        "v_mfma_f32_16x16x32_bf16 v[116:119], a[40:43], a[216:219], v[116:119]\n"
        "v_mul_f32 v196, v192, v196\n"
        "v_mul_f32 v197, v192, v197\n"
        "v_mfma_f32_16x16x32_bf16 v[116:119], a[44:47], a[220:223], v[116:119]\n"
        "v_mul_f32 v198, v192, v198\n"
        "v_mul_f32 v199, v192, v199\n"
        "v_cvt_pk_bf16_f32 v168, v120, v121\n"
        "v_cvt_pk_bf16_f32 v169, v122, v123\n"
        "v_mfma_f32_16x16x32_bf16 v[120:123], a[68:71], a[220:223], 0\n"
        "s_add_u32 m0, s65, 0\n"
        "v_cvt_pk_bf16_f32 v196, v196, v197\n"
        "v_cvt_pk_bf16_f32 v197, v198, v199\n"
        "v_mfma_f32_16x16x32_bf16 v[120:123], a[64:67], a[216:219], v[120:123]\n"
        "s_mov_b32 s64, 0\n"
        "v_mul_f32 v200, v192, v200\n"
        "v_mul_f32 v201, v192, v201\n"
        "v_mfma_f32_16x16x32_bf16 v[120:123], a[60:63], a[248:251], v[120:123]\n"
        "buffer_load_dwordx4 v232, s[52:55], s64 offen lds\n"
        "v_mfma_f32_16x16x32_bf16 v[120:123], a[56:59], a[244:247], v[120:123]\n"
        "ds_read_b128 a[224:227], v240 offset:1536\n"
        "v_mul_f32 v202, v192, v202\n"
        "v_mfma_f32_16x16x32_bf16 v[120:123], a[52:55], a[240:243], v[120:123]\n"
        "ds_read_b128 a[228:231], v241 offset:1536\n"
        "v_mul_f32 v203, v192, v203\n"
        "v_mfma_f32_16x16x32_bf16 v[120:123], a[48:51], a[236:239], v[120:123]\n"
        "ds_read_b128 a[232:235], v240 offset:1664\n"
        "v_cvt_pk_bf16_f32 v198, v200, v201\n"
        "v_cvt_pk_bf16_f32 v156, v112, v113\n"
        "v_cvt_pk_bf16_f32 v157, v114, v115\n"
        "v_mfma_f32_16x16x32_bf16 v[112:115], a[236:239], a[144:147], 0\n"
        "v_cvt_pk_bf16_f32 v199, v202, v203\n"
        "v_mul_f32 v204, v192, v204\n"
        "v_mfma_f32_16x16x32_bf16 v[112:115], a[240:243], a[148:151], v[112:115]\n"
        "ds_write_b128 v245, v[196:199] offset:512\n"
        "v_mfma_f32_16x16x32_bf16 v[112:115], a[244:247], a[152:155], v[112:115]\n"
        "v_mul_f32 v205, v192, v205\n"
        "v_mul_f32 v206, v192, v206\n"
        "v_mfma_f32_16x16x32_bf16 v[112:115], a[248:251], a[156:159], v[112:115]\n"
        "s_waitcnt vmcnt(4)\n"
        "ds_read_b64_tr_b16 v[64:65], v255 offset:12288\n"
        "ds_read_b64_tr_b16 v[66:67], v255 offset:13056\n"
        "v_mfma_f32_16x16x32_bf16 v[112:115], a[216:219], a[160:163], v[112:115]\n"
        "buffer_load_dwordx4 v233, s[52:55], s64 offen offset:1024 lds\n"
        "v_mfma_f32_16x16x32_bf16 v[112:115], a[220:223], a[164:167], v[112:115]\n"
        "ds_read_b64_tr_b16 v[68:69], v255 offset:12320\n"
        "ds_read_b64_tr_b16 v[70:71], v255 offset:13088\n"
        "v_cvt_pk_bf16_f32 v158, v116, v117\n"
        "v_cvt_pk_bf16_f32 v159, v118, v119\n"
        "v_mfma_f32_16x16x32_bf16 v[116:119], a[220:223], a[188:191], 0\n"
        "ds_read_b64_tr_b16 v[72:73], v255 offset:12352\n"
        "ds_read_b64_tr_b16 v[74:75], v255 offset:13120\n"
        "v_mfma_f32_16x16x32_bf16 v[116:119], a[216:219], a[184:187], v[116:119]\n"
        "v_mul_f32 v207, v192, v207\n"
        "v_cvt_pk_bf16_f32 v204, v204, v205\n"
        "v_mfma_f32_16x16x32_bf16 v[116:119], a[248:251], a[180:183], v[116:119]\n"
        "v_cvt_pk_bf16_f32 v205, v206, v207\n"
        "v_max3_u32 v236, v236, v192, v193\n"
        "v_mfma_f32_16x16x32_bf16 v[116:119], a[244:247], a[176:179], v[116:119]\n"
        "ds_write_b64 v246, v[204:205] offset:512\n"
        "v_mfma_f32_16x16x32_bf16 v[116:119], a[240:243], a[172:175], v[116:119]\n"
        "v_max3_u32 v236, v236, v124, v125\n"
        "v_cmp_lt_u32 vcc, s24, v236\n"
        "v_mfma_f32_16x16x32_bf16 v[116:119], a[236:239], a[168:171], v[116:119]\n"
        "v_cvt_pk_bf16_f32 v160, v120, v121\n"
        "v_cvt_pk_bf16_f32 v161, v122, v123\n"
        "v_cvt_pk_bf16_f32 v166, v120, v121\n"
        "v_cvt_pk_bf16_f32 v167, v122, v123\n"
        "v_mfma_f32_16x16x32_bf16 v[120:123], a[236:239], a[192:195], 0\n"
        "s_cmp_lg_u64 vcc, 0\n"
        "s_addc_u32 s25, s25, s25\n"
        "ds_read_b128 a[236:239], v241 offset:1664\n"
        "v_mfma_f32_16x16x32_bf16 v[120:123], a[240:243], a[196:199], v[120:123]\n"
        "buffer_load_dwordx4 v234, s[52:55], s64 offen offset:2048 lds\n"
        "v_mfma_f32_16x16x32_bf16 v[120:123], a[244:247], a[200:203], v[120:123]\n"
        "ds_read_b128 a[240:243], v240 offset:1792\n"
        "ds_read_b128 a[244:247], v241 offset:1792\n"
        "v_mfma_f32_16x16x32_bf16 v[120:123], a[248:251], a[204:207], v[120:123]\n"
        "s_waitcnt lgkmcnt(8)\n"
        "ds_write_b128 v235, v[64:67] offset:12288\n"
        "v_mfma_f32_16x16x32_bf16 v[120:123], a[216:219], a[208:211], v[120:123]\n"
        "v_cvt_pk_bf16_f32 v128, v112, v113\n"
        "v_cvt_pk_bf16_f32 v129, v114, v115\n"
        "v_mfma_f32_16x16x32_bf16 v[120:123], a[220:223], a[212:215], v[120:123]\n"
        "s_waitcnt vmcnt(3)\n"
        "ds_read_b64_tr_b16 v[76:77], v255 offset:15360\n"
        "ds_read_b64_tr_b16 v[78:79], v255 offset:16128\n"
        "v_mfma_f32_16x16x32_bf16 v[112:115], a[72:75], a[224:227], 0\n"
        "s_add_u32 m0, s65, 3072\n"
        "v_cvt_pk_bf16_f32 v136, v116, v117\n"
        "v_cvt_pk_bf16_f32 v137, v118, v119\n"
        "v_mfma_f32_16x16x32_bf16 v[112:115], a[76:79], a[228:231], v[112:115]\n"
        "s_mov_b32 s64, 18432\n"
        "v_xor_b32 v251, 0x2000, v251\n"
        "v_xor_b32 v252, 0x2000, v252\n"
        "v_mfma_f32_16x16x32_bf16 v[112:115], a[80:83], a[232:235], v[112:115]\n"
        "s_waitcnt lgkmcnt(5)\n"
        "buffer_load_dwordx4 v232, s[52:55], s64 offen lds\n"
        "v_mfma_f32_16x16x32_bf16 v[112:115], a[84:87], a[236:239], v[112:115]\n"
        "s_waitcnt lgkmcnt(4)\n"
        "ds_write_b128 v235, v[68:71] offset:12544\n"
        "v_mfma_f32_16x16x32_bf16 v[112:115], a[88:91], a[240:243], v[112:115]\n"
        "s_waitcnt lgkmcnt(4)\n"
        "s_barrier\n"
        "v_cvt_pk_bf16_f32 v144, v120, v121\n"
        "v_cvt_pk_bf16_f32 v145, v122, v123\n"
        "v_mfma_f32_16x16x32_bf16 v[112:115], a[92:95], a[244:247], v[112:115]\n"
        "ds_read_b64_tr_b16 v[88:89], v255 offset:15392\n"
        "ds_read_b64_tr_b16 v[90:91], v255 offset:16160\n"
        "v_mfma_f32_16x16x32_bf16 v[116:119], a[116:119], a[244:247], 0\n"
        "v_add_u32 v247, s45, v245\n"
        "s_nop 0\n"
        "v_mfma_f32_16x16x32_bf16 v[116:119], a[112:115], a[240:243], v[116:119]\n"
        "s_nop 1\n"
        "v_cvt_pk_bf16_f32 v172, v112, v113\n"
        "v_mfma_f32_16x16x32_bf16 v[116:119], a[108:111], a[236:239], v[116:119]\n"
        ".long 0xbec200ff\n"
        "43:\n"
        "s_branch 60b\n"
        "v_cvt_pk_bf16_f32 v173, v114, v115\n"
        "v_mfma_f32_16x16x32_bf16 v[116:119], a[104:107], a[232:235], v[116:119]\n"
        "ds_write_b128 v235, v[72:75] offset:12800\n"
        "v_mfma_f32_16x16x32_bf16 v[116:119], a[100:103], a[228:231], v[116:119]\n"
        "buffer_load_dwordx4 v233, s[52:55], s64 offen offset:1024 lds\n"
        "v_mfma_f32_16x16x32_bf16 v[116:119], a[96:99], a[224:227], v[116:119]\n"
        "s_nop 2\n"
        "v_mfma_f32_16x16x32_bf16 v[120:123], a[120:123], a[224:227], 0\n"
        "ds_read_b64_tr_b16 v[92:93], v255 offset:15424\n"
        "ds_read_b64_tr_b16 v[94:95], v255 offset:16192\n"
        "v_mfma_f32_16x16x32_bf16 v[120:123], a[124:127], a[228:231], v[120:123]\n"
        "ds_read_b128 v[162:165], v251 offset:0\n"
        "v_cvt_pk_bf16_f32 v174, v116, v117\n"
        "v_mfma_f32_16x16x32_bf16 v[120:123], a[128:131], a[232:235], v[120:123]\n"
        "v_cvt_pk_bf16_f32 v175, v118, v119\n"
        "v_mfma_f32_16x16x32_bf16 v[120:123], a[132:135], a[236:239], v[120:123]\n"
        "v_mfma_f32_16x16x32_bf16 v[120:123], a[136:139], a[240:243], v[120:123]\n"
        "v_mfma_f32_16x16x32_bf16 v[120:123], a[140:143], a[244:247], v[120:123]\n"
        "s_nop 2\n"
        "v_mfma_f32_16x16x32_bf16 v[112:115], a[20:23], a[244:247], 0\n"
        "buffer_load_dwordx4 v234, s[52:55], s64 offen offset:2048 lds\n"
        "v_mfma_f32_16x16x32_bf16 v[112:115], a[16:19], a[240:243], v[112:115]\n"
        "s_waitcnt vmcnt(3)\n"
        "ds_read_b64_tr_b16 v[212:213], v255 offset:0\n"
        "ds_read_b64_tr_b16 v[214:215], v255 offset:768\n"
        "v_mfma_f32_16x16x32_bf16 v[112:115], a[12:15], a[236:239], v[112:115]\n"
        "v_cvt_pk_bf16_f32 v190, v120, v121\n"
        "v_cvt_pk_bf16_f32 v191, v122, v123\n"
        "v_mfma_f32_16x16x32_bf16 v[112:115], a[8:11], a[232:235], v[112:115]\n"
        "v_mfma_f32_16x16x32_bf16 v[112:115], a[4:7], a[228:231], v[112:115]\n"
        "ds_read_b128 v[182:185], v251 offset:512\n"
        "v_mfma_f32_16x16x32_bf16 v[112:115], a[0:3], a[224:227], v[112:115]\n"
        "s_waitcnt lgkmcnt(10)\n"
        "ds_write_b128 v235, v[76:79] offset:15360\n"
        "v_mfma_f32_16x16x32_bf16 v[116:119], a[24:27], a[224:227], 0\n"
        "s_add_u32 m0, s65, 6144\n"
        "s_nop 1\n"
        "v_mfma_f32_16x16x32_bf16 v[116:119], a[28:31], a[228:231], v[116:119]\n"
        "s_mov_b32 s64, 36864\n"
        "s_nop 0\n"
        "v_cvt_pk_bf16_f32 v176, v112, v113\n"
        "v_mfma_f32_16x16x32_bf16 v[116:119], a[32:35], a[232:235], v[116:119]\n"
        "buffer_load_dwordx4 v232, s[52:55], s64 offen lds\n"
        "v_mfma_f32_16x16x32_bf16 v[116:119], a[36:39], a[236:239], v[116:119]\n"
        "ds_read_b64_tr_b16 v[216:217], v255 offset:32\n"
        "ds_read_b64_tr_b16 v[218:219], v255 offset:800\n"
        "v_mfma_f32_16x16x32_bf16 v[116:119], a[40:43], a[240:243], v[116:119]\n"
        "v_cvt_pk_bf16_f32 v177, v114, v115\n"
        "v_mfma_f32_16x16x32_bf16 v[116:119], a[44:47], a[244:247], v[116:119]\n"
        "s_nop 2\n"
        "v_mfma_f32_16x16x32_bf16 v[120:123], a[68:71], a[244:247], 0\n"
        "s_waitcnt lgkmcnt(10)\n"
        "ds_write_b128 v235, v[88:91] offset:15616\n"
        "v_mfma_f32_16x16x32_bf16 v[120:123], a[64:67], a[240:243], v[120:123]\n"
        "s_nop 1\n"
        "v_cvt_pk_bf16_f32 v178, v116, v117\n"
        "v_mfma_f32_16x16x32_bf16 v[120:123], a[60:63], a[236:239], v[120:123]\n"
        "v_cvt_pk_bf16_f32 v179, v118, v119\n"
        "v_mfma_f32_16x16x32_bf16 v[120:123], a[56:59], a[232:235], v[120:123]\n"
        "ds_read_b64_tr_b16 v[220:221], v255 offset:64\n"
        "ds_read_b64_tr_b16 v[222:223], v255 offset:832\n"
        "v_mfma_f32_16x16x32_bf16 v[120:123], a[52:55], a[228:231], v[120:123]\n"
        "buffer_load_dwordx4 v233, s[52:55], s64 offen offset:1024 lds\n"
        "v_mfma_f32_16x16x32_bf16 v[120:123], a[48:51], a[224:227], v[120:123]\n"
        "s_nop 2\n"
        "v_mfma_f32_16x16x32_bf16 v[112:115], a[224:227], a[144:147], 0\n"
        "s_nop 2\n"
        "v_mfma_f32_16x16x32_bf16 v[112:115], a[228:231], a[148:151], v[112:115]\n"
        "s_waitcnt lgkmcnt(10)\n"
        "ds_write_b128 v235, v[92:95] offset:15872\n"
        "v_mfma_f32_16x16x32_bf16 v[112:115], a[232:235], a[152:155], v[112:115]\n"
        "v_cvt_pk_bf16_f32 v180, v120, v121\n"
        "v_cvt_pk_bf16_f32 v181, v122, v123\n"
        "v_mfma_f32_16x16x32_bf16 v[112:115], a[236:239], a[156:159], v[112:115]\n"
        "v_cvt_pk_bf16_f32 v186, v120, v121\n"
        "v_cvt_pk_bf16_f32 v187, v122, v123\n"
        "v_mfma_f32_16x16x32_bf16 v[112:115], a[240:243], a[160:163], v[112:115]\n"
        "s_waitcnt vmcnt(2)\n"
        "ds_read_b64_tr_b16 v[224:225], v255 offset:3072\n"
        "ds_read_b64_tr_b16 v[226:227], v255 offset:3840\n"
        "v_mfma_f32_16x16x32_bf16 v[112:115], a[244:247], a[164:167], v[112:115]\n"
        "ds_read_b128 a[248:251], v240 offset:12288\n"
        "s_nop 0\n"
        "v_mfma_f32_16x16x32_bf16 v[96:99], v[152:155], v[156:159], v[0:3]\n"
        "s_waitcnt lgkmcnt(13)\n"
        "buffer_load_dwordx4 v234, s[52:55], s64 offen offset:2048 lds\n"
        "v_mfma_f32_16x16x32_bf16 v[96:99], v[168:171], v[160:163], v[96:99]\n"
        "ds_read_b128 a[216:219], v241 offset:12288\n"
        "s_sub_u32 s31, s30, s28\n"
        "s_nop 0\n"
        "v_mfma_f32_16x16x32_bf16 v[100:103], v[172:175], v[156:159], v[4:7]\n"
        "s_waitcnt lgkmcnt(12)\n"
        "ds_write_b128 v235, v[212:215] offset:0\n"
        "s_sub_u32 s45, s28, s29\n"
        "v_mfma_f32_16x16x32_bf16 v[100:103], v[188:191], v[164:167], v[100:103]\n"
        "s_add_u32 s28, s28, s31\n"
        "v_cvt_pk_bf16_f32 v130, v112, v113\n"
        "v_cvt_pk_bf16_f32 v131, v114, v115\n"
        "v_mfma_f32_16x16x32_bf16 v[116:119], a[244:247], a[188:191], 0\n"
        "ds_read_b128 a[220:223], v240 offset:12416\n"
        "v_mfma_f32_16x16x32_bf16 v[116:119], a[240:243], a[184:187], v[116:119]\n"
        "ds_read_b64_tr_b16 v[88:89], v255 offset:3104\n"
        "ds_read_b64_tr_b16 v[90:91], v255 offset:3872\n"
        "v_mfma_f32_16x16x32_bf16 v[116:119], a[236:239], a[180:183], v[116:119]\n"
        "s_add_u32 m0, s65, 9216\n"
        "ds_read_b128 v[162:165], v252 offset:0\n"
        "v_mfma_f32_16x16x32_bf16 v[116:119], a[232:235], a[176:179], v[116:119]\n"
        "s_mov_b32 s64, 55296\n"
        "v_exp_f32 v96, v96\n"
        "v_mfma_f32_16x16x32_bf16 v[116:119], a[228:231], a[172:175], v[116:119]\n"
        "buffer_load_dwordx4 v232, s[52:55], s64 offen lds\n"
        "v_mfma_f32_16x16x32_bf16 v[116:119], a[224:227], a[168:171], v[116:119]\n"
        "s_waitcnt lgkmcnt(13)\n"
        "ds_write_b128 v235, v[216:219] offset:256\n"
        "v_mfma_f32_16x16x32_bf16 v[104:107], v[152:155], v[176:179], v[16:19]\n"
        "v_exp_f32 v97, v97\n"
        "v_mfma_f32_16x16x32_bf16 v[104:107], v[168:171], v[180:183], v[104:107]\n"
        "v_exp_f32 v98, v98\n"
        "v_mfma_f32_16x16x32_bf16 v[108:111], v[172:175], v[176:179], v[20:23]\n"
        "ds_read_b64_tr_b16 v[92:93], v255 offset:3136\n"
        "ds_read_b64_tr_b16 v[94:95], v255 offset:3904\n"
        "v_mfma_f32_16x16x32_bf16 v[108:111], v[188:191], v[184:187], v[108:111]\n"
        "ds_read_b128 v[182:185], v252 offset:512\n"
        "v_cvt_pk_bf16_f32 v64, v96, v97\n"
        "v_mfma_f32_16x16x32_bf16 v[120:123], a[224:227], a[192:195], 0\n"
        "ds_read_b128 a[224:227], v241 offset:12416\n"
        "v_cvt_pk_bf16_f32 v138, v116, v117\n"
        "v_mfma_f32_16x16x32_bf16 v[120:123], a[228:231], a[196:199], v[120:123]\n"
        "ds_read_b128 a[228:231], v240 offset:12544\n"
        "v_cvt_pk_bf16_f32 v139, v118, v119\n"
        "v_mfma_f32_16x16x32_bf16 v[120:123], a[232:235], a[200:203], v[120:123]\n"
        "ds_read_b128 a[232:235], v241 offset:12544\n"
        "v_mfma_f32_16x16x32_bf16 v[120:123], a[236:239], a[204:207], v[120:123]\n"
        "buffer_load_dwordx4 v233, s[52:55], s64 offen offset:1024 lds\n"
        "v_mfma_f32_16x16x32_bf16 v[120:123], a[240:243], a[208:211], v[120:123]\n"
        "s_waitcnt lgkmcnt(15)\n"
        "ds_write_b128 v235, v[220:223] offset:512\n"
        "v_mfma_f32_16x16x32_bf16 v[120:123], a[244:247], a[212:215], v[120:123]\n"
        "s_waitcnt lgkmcnt(14)\n"
        "v_exp_f32 v100, v100\n"
        "v_mfma_f32_16x16x32_bf16 v[112:115], a[0:3], a[248:251], 0\n"
        "s_waitcnt lgkmcnt(13)\n"
        "s_waitcnt vmcnt(2)\n"
        "ds_read_b64_tr_b16 v[212:213], v255 offset:6144\n"
        "ds_read_b64_tr_b16 v[214:215], v255 offset:6912\n"
        "v_mfma_f32_16x16x32_bf16 v[112:115], a[4:7], a[216:219], v[112:115]\n"
        "s_waitcnt lgkmcnt(13)\n"
        "ds_read_b128 v[202:205], v251 offset:4096\n"
        "s_nop 0\n"
        "v_mfma_f32_16x16x32_bf16 v[112:115], a[8:11], a[220:223], v[112:115]\n"
        "s_waitcnt lgkmcnt(6)\n"
        "v_exp_f32 v99, v99\n"
        "v_mfma_f32_16x16x32_bf16 v[112:115], a[12:15], a[224:227], v[112:115]\n"
        "s_waitcnt lgkmcnt(5)\n"
        "ds_write_b128 v235, v[224:227] offset:3072\n"
        "v_mfma_f32_16x16x32_bf16 v[112:115], a[16:19], a[228:231], v[112:115]\n"
        "s_waitcnt lgkmcnt(5)\n"
        "buffer_load_dwordx4 v234, s[52:55], s64 offen offset:2048 lds\n"
        "s_add_u32 s41, s30, s40\n"
        "s_add_u32 s35, s47, 786432\n"
        "s_sub_u32 s46, s35, 100663296\n"
        "s_cmp_lt_u32 s46, 786432\n"
        "s_mov_b32 s30, s29\n"
        "v_mfma_f32_16x16x32_bf16 v[112:115], a[20:23], a[232:235], v[112:115]\n"
        "s_cselect_b32 s35, s46, s35\n"
        "s_add_u32 s29, s29, s45\n"
        "v_cvt_pk_bf16_f32 v65, v98, v99\n"
        "v_mfma_f32_16x16x32_bf16 v[116:119], a[44:47], a[232:235], 0\n"
        "ds_read_b64_tr_b16 v[216:217], v255 offset:6176\n"
        "ds_read_b64_tr_b16 v[218:219], v255 offset:6944\n"
        "v_mfma_f32_16x16x32_bf16 v[116:119], a[40:43], a[228:231], v[116:119]\n"
        "v_exp_f32 v101, v101\n"
        "v_mfma_f32_16x16x32_bf16 v[116:119], a[36:39], a[224:227], v[116:119]\n"
        "v_exp_f32 v102, v102\n"
        "v_mfma_f32_16x16x32_bf16 v[116:119], a[32:35], a[220:223], v[116:119]\n"
        "v_cvt_pk_bf16_f32 v66, v100, v101\n"
        "v_cvt_pk_bf16_f32 v146, v120, v121\n"
        "v_mfma_f32_16x16x32_bf16 v[116:119], a[28:31], a[216:219], v[116:119]\n"
        "ds_write_b128 v235, v[88:91] offset:3328\n"
        "s_mov_b32 m0, s48\n"
        "v_mfma_f32_16x16x32_bf16 v[116:119], a[24:27], a[248:251], v[116:119]\n"
        "v_exp_f32 v103, v103\n"
        "v_cvt_pk_bf16_f32 v147, v122, v123\n"
        "v_mfma_f32_16x16x32_bf16 v[120:123], a[48:51], a[248:251], 0\n"
        "buffer_load_dwordx4 v249, s[12:15], s36 offen offset:0 lds\n"
        "v_mfma_f32_16x16x32_bf16 v[120:123], a[52:55], a[216:219], v[120:123]\n"
        "ds_read_b64_tr_b16 v[220:221], v255 offset:6208\n"
        "ds_read_b64_tr_b16 v[222:223], v255 offset:6976\n"
        "v_mfma_f32_16x16x32_bf16 v[120:123], a[56:59], a[220:223], v[120:123]\n"
        "v_cvt_pk_bf16_f32 v67, v102, v103\n"
        "v_cvt_pk_bf16_f32 v196, v112, v113\n"
        "v_mfma_f32_16x16x32_bf16 v[120:123], a[60:63], a[224:227], v[120:123]\n"
        "v_exp_f32 v104, v104\n"
        "v_mfma_f32_16x16x32_bf16 v[120:123], a[64:67], a[228:231], v[120:123]\n"
        "v_exp_f32 v105, v105\n"
        "v_mfma_f32_16x16x32_bf16 v[120:123], a[68:71], a[232:235], v[120:123]\n"
        "ds_write_b128 v235, v[92:95] offset:3584\n"
        "v_cvt_pk_bf16_f32 v197, v114, v115\n"
        "v_mfma_f32_16x16x32_bf16 v[112:115], a[92:95], a[232:235], 0\n"
        "v_exp_f32 v106, v106\n"
        "v_mfma_f32_16x16x32_bf16 v[112:115], a[88:91], a[228:231], v[112:115]\n"
        "buffer_load_dwordx4 v253, s[12:15], s36 offen offset:1024 lds\n"
        "v_mfma_f32_16x16x32_bf16 v[112:115], a[84:87], a[224:227], v[112:115]\n"
        "s_waitcnt vmcnt(2)\n"
        "ds_read_b64_tr_b16 v[224:225], v255 offset:9216\n"
        "ds_read_b64_tr_b16 v[226:227], v255 offset:9984\n"
        "v_mfma_f32_16x16x32_bf16 v[112:115], a[80:83], a[220:223], v[112:115]\n"
        "v_cvt_pk_bf16_f32 v72, v104, v105\n"
        "v_cvt_pk_bf16_f32 v198, v116, v117\n"
        "v_mfma_f32_16x16x32_bf16 v[112:115], a[76:79], a[216:219], v[112:115]\n"
        "v_exp_f32 v108, v108\n"
        "v_mfma_f32_16x16x32_bf16 v[112:115], a[72:75], a[248:251], v[112:115]\n"
        "v_exp_f32 v107, v107\n"
        "v_cvt_pk_bf16_f32 v199, v118, v119\n"
        "s_nop 1\n"
        "v_mfma_f32_16x16x32_bf16 v[96:99], v[152:155], v[196:199], v[32:35]\n"
        "s_waitcnt lgkmcnt(9)\n"
        "s_waitcnt lgkmcnt(10)\n"
        "ds_write_b128 v235, v[212:215] offset:6144\n"
        "s_add_u32 m0, s41, 0\n"
        "v_cvt_pk_bf16_f32 v200, v120, v121\n"
        "v_cvt_pk_bf16_f32 v201, v122, v123\n"
        "s_nop 1\n"
        "v_mfma_f32_16x16x32_bf16 v[96:99], v[168:171], v[200:203], v[96:99]\n"
        "v_exp_f32 v109, v109\n"
        "v_mfma_f32_16x16x32_bf16 v[100:103], v[172:175], v[196:199], v[36:39]\n"
        "buffer_load_dwordx4 v242, s[8:11], s47 offen lds\n"
        "v_cvt_pk_bf16_f32 v206, v120, v121\n"
        "v_cvt_pk_bf16_f32 v207, v122, v123\n"
        "s_nop 1\n"
        "v_mfma_f32_16x16x32_bf16 v[100:103], v[188:191], v[204:207], v[100:103]\n"
        "ds_read_b64_tr_b16 v[88:89], v255 offset:9248\n"
        "ds_read_b64_tr_b16 v[90:91], v255 offset:10016\n"
        "s_cmp_eq_u32 s37, 100663296\n"
        "v_mfma_f32_16x16x32_bf16 v[116:119], a[96:99], a[248:251], 0\n"
        "s_cselect_b32 s46, 0, s37\n"
        "ds_read_b128 v[202:205], v252 offset:4096\n"
        "v_cvt_pk_bf16_f32 v73, v106, v107\n"
        "v_mfma_f32_16x16x32_bf16 v[116:119], a[100:103], a[216:219], v[116:119]\n"
        "s_add_u32 s38, s46, s43\n"
        "v_exp_f32 v110, v110\n"
        "v_mfma_f32_16x16x32_bf16 v[116:119], a[104:107], a[220:223], v[116:119]\n"
        "s_add_u32 s39, s46, s44\n"
        "v_cvt_pk_bf16_f32 v74, v108, v109\n"
        "v_cvt_pk_bf16_f32 v192, v112, v113\n"
        "v_mfma_f32_16x16x32_bf16 v[116:119], a[108:111], a[224:227], v[116:119]\n"
        "s_waitcnt lgkmcnt(10)\n"
        "ds_write_b128 v235, v[216:219] offset:6400\n"
        "s_add_u32 m0, s41, 1024\n"
        "v_mfma_f32_16x16x32_bf16 v[116:119], a[112:115], a[228:231], v[116:119]\n"
        "s_add_u32 s37, s37, 6291456\n"
        "v_exp_f32 v111, v111\n"
        "v_mfma_f32_16x16x32_bf16 v[116:119], a[116:119], a[232:235], v[116:119]\n"
        "buffer_load_dwordx4 v243, s[8:11], s47 offen lds\n"
        "v_mfma_f32_16x16x32_bf16 v[120:123], a[140:143], a[232:235], 0\n"
        "ds_read_b64_tr_b16 v[92:93], v255 offset:9280\n"
        "ds_read_b64_tr_b16 v[94:95], v255 offset:10048\n"
        "v_mfma_f32_16x16x32_bf16 v[120:123], a[136:139], a[228:231], v[120:123]\n"
        "v_cvt_pk_bf16_f32 v75, v110, v111\n"
        "v_cvt_pk_bf16_f32 v193, v114, v115\n"
        "v_mfma_f32_16x16x32_bf16 v[120:123], a[132:135], a[224:227], v[120:123]\n"
        "v_exp_f32 v96, v96\n"
        "v_mfma_f32_16x16x32_bf16 v[120:123], a[128:131], a[220:223], v[120:123]\n"
        "v_exp_f32 v97, v97\n"
        "v_mfma_f32_16x16x32_bf16 v[120:123], a[124:127], a[216:219], v[120:123]\n"
        "s_waitcnt lgkmcnt(10)\n"
        "ds_write_b128 v235, v[220:223] offset:6656\n"
        "s_add_u32 m0, s41, 2048\n"
        "v_mfma_f32_16x16x32_bf16 v[120:123], a[120:123], a[248:251], v[120:123]\n"
        "v_exp_f32 v98, v98\n"
        "v_mfma_f32_16x16x32_bf16 v[112:115], a[248:251], a[144:147], 0\n"
        "buffer_load_dwordx4 v244, s[8:11], s47 offen lds\n"
        "v_mfma_f32_16x16x32_bf16 v[112:115], a[216:219], a[148:151], v[112:115]\n"
        "v_cvt_pk_bf16_f32 v80, v96, v97\n"
        "v_cvt_pk_bf16_f32 v194, v116, v117\n"
        "v_mfma_f32_16x16x32_bf16 v[112:115], a[220:223], a[152:155], v[112:115]\n"
        "ds_read_b128 a[236:239], v240 offset:13824\n"
        "v_cvt_pk_bf16_f32 v195, v118, v119\n"
        "v_mfma_f32_16x16x32_bf16 v[112:115], a[224:227], a[156:159], v[112:115]\n"
        "ds_read_b128 a[240:243], v241 offset:13824\n"
        "v_cvt_pk_bf16_f32 v208, v120, v121\n"
        "v_mfma_f32_16x16x32_bf16 v[112:115], a[228:231], a[160:163], v[112:115]\n"
        "ds_read_b128 a[244:247], v240 offset:13952\n"
        "v_cvt_pk_bf16_f32 v209, v122, v123\n"
        "v_mfma_f32_16x16x32_bf16 v[112:115], a[232:235], a[164:167], v[112:115]\n"
        "s_waitcnt lgkmcnt(11)\n"
        "ds_write_b128 v235, v[224:227] offset:9216\n"
        "s_add_u32 m0, s41, 12288\n"
        "v_mfma_f32_16x16x32_bf16 v[104:107], v[192:195], v[156:159], v[8:11]\n"
        "v_exp_f32 v100, v100\n"
        "v_mfma_f32_16x16x32_bf16 v[104:107], v[208:211], v[160:163], v[104:107]\n"
        "buffer_load_dwordx4 v242, s[8:11], s35 offen lds\n"
        "v_mfma_f32_16x16x32_bf16 v[116:119], a[232:235], a[188:191], 0\n"
        "v_exp_f32 v99, v99\n"
        "v_mfma_f32_16x16x32_bf16 v[116:119], a[228:231], a[184:187], v[116:119]\n"
        "v_exp_f32 v101, v101\n"
        "v_mfma_f32_16x16x32_bf16 v[116:119], a[224:227], a[180:183], v[116:119]\n"
        "v_cvt_pk_bf16_f32 v81, v98, v99\n"
        "v_cvt_pk_bf16_f32 v82, v100, v101\n"
        "v_mfma_f32_16x16x32_bf16 v[116:119], a[220:223], a[176:179], v[116:119]\n"
        "v_exp_f32 v102, v102\n"
        "v_mfma_f32_16x16x32_bf16 v[116:119], a[216:219], a[172:175], v[116:119]\n"
        "s_waitcnt lgkmcnt(9)\n"
        "ds_write_b128 v235, v[88:91] offset:9472\n"
        "s_add_u32 m0, s41, 13312\n"
        "v_mfma_f32_16x16x32_bf16 v[116:119], a[248:251], a[168:171], v[116:119]\n"
        "v_exp_f32 v103, v103\n"
        "v_mfma_f32_16x16x32_bf16 v[108:111], v[192:195], v[176:179], v[24:27]\n"
        "buffer_load_dwordx4 v243, s[8:11], s35 offen lds\n"
        "v_mfma_f32_16x16x32_bf16 v[108:111], v[208:211], v[180:183], v[108:111]\n"
        "v_cvt_pk_bf16_f32 v83, v102, v103\n"
        "v_cvt_pk_bf16_f32 v132, v112, v113\n"
        "v_mfma_f32_16x16x32_bf16 v[120:123], a[248:251], a[192:195], 0\n"
        "ds_read_b128 a[248:251], v241 offset:13952\n"
        "v_cvt_pk_bf16_f32 v133, v114, v115\n"
        "v_mfma_f32_16x16x32_bf16 v[120:123], a[216:219], a[196:199], v[120:123]\n"
        "ds_read_b128 a[216:219], v240 offset:14080\n"
        "v_cvt_pk_bf16_f32 v140, v116, v117\n"
        "v_mfma_f32_16x16x32_bf16 v[120:123], a[220:223], a[200:203], v[120:123]\n"
        "ds_read_b128 a[220:223], v241 offset:14080\n"
        "v_cvt_pk_bf16_f32 v141, v118, v119\n"
        "v_mfma_f32_16x16x32_bf16 v[120:123], a[224:227], a[204:207], v[120:123]\n"
        "s_waitcnt lgkmcnt(9)\n"
        "ds_write_b128 v235, v[92:95] offset:9728\n"
        "s_add_u32 m0, s41, 14336\n"
        "v_mfma_f32_16x16x32_bf16 v[120:123], a[228:231], a[208:211], v[120:123]\n"
        "v_exp_f32 v104, v104\n"
        "v_mfma_f32_16x16x32_bf16 v[120:123], a[232:235], a[212:215], v[120:123]\n"
        "buffer_load_dwordx4 v244, s[8:11], s35 offen lds\n"
        "v_mfma_f32_16x16x32_bf16 v[96:99], v[192:195], v[196:199], v[40:43]\n"
        "v_exp_f32 v105, v105\n"
        "v_mfma_f32_16x16x32_bf16 v[96:99], v[208:211], v[200:203], v[96:99]\n"
        "s_waitcnt lgkmcnt(8)\n"
        "v_exp_f32 v106, v106\n"
        "v_mfma_f32_16x16x32_bf16 v[112:115], a[72:75], a[236:239], 0\n"
        "s_waitcnt lgkmcnt(7)\n"
        "v_cvt_pk_bf16_f32 v68, v104, v105\n"
        "v_add_u32 v245, s45, v245\n"
        "v_mfma_f32_16x16x32_bf16 v[112:115], a[76:79], a[240:243], v[112:115]\n"
        "s_waitcnt lgkmcnt(6)\n"
        "v_exp_f32 v107, v107\n"
        "v_mfma_f32_16x16x32_bf16 v[112:115], a[80:83], a[244:247], v[112:115]\n"
        "s_waitcnt lgkmcnt(3)\n"
        "v_cvt_pk_bf16_f32 v69, v106, v107\n"
        "v_cvt_pk_bf16_f32 v148, v120, v121\n"
        "v_mfma_f32_16x16x32_bf16 v[112:115], a[84:87], a[248:251], v[112:115]\n"
        "s_waitcnt lgkmcnt(2)\n"
        "v_exp_f32 v108, v108\n"
        "v_mfma_f32_16x16x32_bf16 v[112:115], a[88:91], a[216:219], v[112:115]\n"
        "s_waitcnt lgkmcnt(1)\n"
        "v_cvt_pk_bf16_f32 v149, v122, v123\n"
        "v_add_u32 v246, s45, v246\n"
        "v_mfma_f32_16x16x32_bf16 v[112:115], a[92:95], a[220:223], v[112:115]\n"
        "v_exp_f32 v109, v109\n"
        "v_mfma_f32_16x16x32_bf16 v[116:119], a[116:119], a[220:223], 0\n"
        "v_exp_f32 v110, v110\n"
        "v_mfma_f32_16x16x32_bf16 v[116:119], a[112:115], a[216:219], v[116:119]\n"
        "v_cvt_pk_bf16_f32 v76, v108, v109\n"
        "v_add_u32 v240, s31, v240\n"
        "v_mfma_f32_16x16x32_bf16 v[116:119], a[108:111], a[248:251], v[116:119]\n"
        "v_exp_f32 v111, v111\n"
        "v_mfma_f32_16x16x32_bf16 v[116:119], a[104:107], a[244:247], v[116:119]\n"
        "v_cvt_pk_bf16_f32 v212, v112, v113\n"
        "v_cvt_pk_bf16_f32 v213, v114, v115\n"
        "v_mfma_f32_16x16x32_bf16 v[116:119], a[100:103], a[240:243], v[116:119]\n"
        "v_cvt_pk_bf16_f32 v77, v110, v111\n"
        "v_add_u32 v241, s31, v241\n"
        "v_mfma_f32_16x16x32_bf16 v[116:119], a[96:99], a[236:239], v[116:119]\n"
        "v_exp_f32 v96, v96\n"
        "v_mfma_f32_16x16x32_bf16 v[120:123], a[120:123], a[236:239], 0\n"
        "v_exp_f32 v97, v97\n"
        "v_mfma_f32_16x16x32_bf16 v[120:123], a[124:127], a[240:243], v[120:123]\n"
        "s_add_u32 s47, s47, 6291456\n"
        "v_exp_f32 v98, v98\n"
        "v_mfma_f32_16x16x32_bf16 v[120:123], a[128:131], a[244:247], v[120:123]\n"
        "s_add_u32 s36, s36, 2097152\n"
        "v_cvt_pk_bf16_f32 v214, v116, v117\n"
        "v_cvt_pk_bf16_f32 v215, v118, v119\n"
        "v_mfma_f32_16x16x32_bf16 v[120:123], a[132:135], a[248:251], v[120:123]\n"
        "s_xor_b32 s48, s48, 8192\n"
        "v_cvt_pk_bf16_f32 v84, v96, v97\n"
        "v_mfma_f32_16x16x32_bf16 v[120:123], a[136:139], a[216:219], v[120:123]\n"
        "v_exp_f32 v99, v99\n"
        "v_mfma_f32_16x16x32_bf16 v[120:123], a[140:143], a[220:223], v[120:123]\n"
        "v_cvt_pk_bf16_f32 v85, v98, v99\n"
        "s_nop 0\n"
        "v_mfma_f32_16x16x32_bf16 v[112:115], a[220:223], a[164:167], 0\n"
        "s_nop 2\n"
        "v_mfma_f32_16x16x32_bf16 v[112:115], a[216:219], a[160:163], v[112:115]\n"
        "s_nop 0\n"
        "v_cvt_pk_bf16_f32 v230, v120, v121\n"
        "v_cvt_pk_bf16_f32 v231, v122, v123\n"
        "v_mfma_f32_16x16x32_bf16 v[112:115], a[248:251], a[156:159], v[112:115]\n"
        "s_waitcnt lgkmcnt(0)\n"
        "s_barrier\n"
        "v_mfma_f32_16x16x32_bf16 v[112:115], a[244:247], a[152:155], v[112:115]\n"
        "v_mfma_f32_16x16x32_bf16 v[112:115], a[240:243], a[148:151], v[112:115]\n"
        "v_mfma_f32_16x16x32_bf16 v[112:115], a[236:239], a[144:147], v[112:115]\n"
        "s_nop 2\n"
        "v_mfma_f32_16x16x32_bf16 v[100:103], v[212:215], v[156:159], v[12:15]\n"
        "s_nop 2\n"
        "v_mfma_f32_16x16x32_bf16 v[100:103], v[228:231], v[164:167], v[100:103]\n"
        "v_cvt_pk_bf16_f32 v134, v112, v113\n"
        "v_cvt_pk_bf16_f32 v135, v114, v115\n"
        "v_mfma_f32_16x16x32_bf16 v[116:119], a[236:239], a[168:171], 0\n"
        "v_mfma_f32_16x16x32_bf16 v[116:119], a[240:243], a[172:175], v[116:119]\n"
        "v_mfma_f32_16x16x32_bf16 v[116:119], a[244:247], a[176:179], v[116:119]\n"
        "v_mfma_f32_16x16x32_bf16 v[116:119], a[248:251], a[180:183], v[116:119]\n"
        "v_mfma_f32_16x16x32_bf16 v[116:119], a[216:219], a[184:187], v[116:119]\n"
        "s_nop 0\n"
        "v_exp_f32 v100, v100\n"
        "v_mfma_f32_16x16x32_bf16 v[116:119], a[220:223], a[188:191], v[116:119]\n"
        "v_exp_f32 v101, v101\n"
        "v_mfma_f32_16x16x32_bf16 v[104:107], v[212:215], v[176:179], v[28:31]\n"
        "v_exp_f32 v102, v102\n"
        "v_mfma_f32_16x16x32_bf16 v[104:107], v[228:231], v[184:187], v[104:107]\n"
        "v_exp_f32 v103, v103\n"
        "v_mfma_f32_16x16x32_bf16 v[120:123], a[220:223], a[212:215], 0\n"
        "v_cvt_pk_bf16_f32 v70, v100, v101\n"
        "v_cvt_pk_bf16_f32 v71, v102, v103\n"
        "v_mfma_f32_16x16x32_bf16 v[120:123], a[216:219], a[208:211], v[120:123]\n"
        "v_cvt_pk_bf16_f32 v142, v116, v117\n"
        "v_cvt_pk_bf16_f32 v143, v118, v119\n"
        "v_mfma_f32_16x16x32_bf16 v[120:123], a[248:251], a[204:207], v[120:123]\n"
        "v_mov_b64 v[96:97], 0\n"
        "v_mfma_f32_16x16x32_bf16 v[120:123], a[244:247], a[200:203], v[120:123]\n"
        "v_exp_f32 v104, v104\n"
        "v_mfma_f32_16x16x32_bf16 v[120:123], a[240:243], a[196:199], v[120:123]\n"
        "v_exp_f32 v105, v105\n"
        "v_mfma_f32_16x16x32_bf16 v[120:123], a[236:239], a[192:195], v[120:123]\n"
        "v_exp_f32 v106, v106\n"
        "v_mfma_f32_16x16x32_bf16 v[108:111], v[212:215], v[196:199], v[44:47]\n"
        "v_exp_f32 v107, v107\n"
        "v_mfma_f32_16x16x32_bf16 v[108:111], v[228:231], v[204:207], v[108:111]\n"
        "v_cvt_pk_bf16_f32 v78, v104, v105\n"
        "v_cvt_pk_bf16_f32 v79, v106, v107\n"
        "v_mfma_f32_16x16x32_bf16 v[112:115], a[0:3], a[236:239], 0\n"
        "s_nop 0\n"
        "v_cvt_pk_bf16_f32 v150, v120, v121\n"
        "v_cvt_pk_bf16_f32 v151, v122, v123\n"
        "v_mfma_f32_16x16x32_bf16 v[112:115], a[4:7], a[240:243], v[112:115]\n"
        "v_mfma_f32_16x16x32_bf16 v[112:115], a[8:11], a[244:247], v[112:115]\n"
        "v_exp_f32 v108, v108\n"
        "v_mfma_f32_16x16x32_bf16 v[112:115], a[12:15], a[248:251], v[112:115]\n"
        "v_exp_f32 v109, v109\n"
        "v_mfma_f32_16x16x32_bf16 v[112:115], a[16:19], a[216:219], v[112:115]\n"
        "v_exp_f32 v110, v110\n"
        "v_mfma_f32_16x16x32_bf16 v[112:115], a[20:23], a[220:223], v[112:115]\n"
        "v_exp_f32 v111, v111\n"
        "v_smfmac_f32_16x16x64_bf16 v[96:99], a[252:255], v[64:71], v237\n"
        "v_cvt_pk_bf16_f32 v86, v108, v109\n"
        "v_mfma_f32_16x16x32_bf16 v[100:103], v[132:135], v[68:71], 0\n"
        "v_mov_b64 v[124:125], 0\n"
        "v_cvt_pk_bf16_f32 v87, v110, v111\n"
        "v_mfma_f32_16x16x32_bf16 v[100:103], v[128:131], v[64:67], v[100:103]\n"
        "s_nop 0\n"
        "v_cvt_pk_bf16_f32 v216, v112, v113\n"
        "v_cvt_pk_bf16_f32 v217, v114, v115\n"
        "v_mfma_f32_16x16x32_bf16 v[104:107], v[136:139], v[64:67], 0\n"
        "ds_read_b128 v[222:225], v251 offset:4608\n"
        "v_add_f32 v97, v96, v97\n"
        "v_mfma_f32_16x16x32_bf16 v[104:107], v[140:143], v[68:71], v[104:107]\n"
        "v_rcp_f32 v96, v97\n"
        "v_mfma_f32_16x16x32_bf16 v[108:111], v[148:151], v[68:71], 0\n"
        "v_mul_f32 v100, v96, v100\n"
        "v_mul_f32 v101, v96, v101\n"
        "v_mfma_f32_16x16x32_bf16 v[108:111], v[144:147], v[64:67], v[108:111]\n"
        "v_mul_f32 v102, v96, v102\n"
        "v_mul_f32 v103, v96, v103\n"
        "v_mfma_f32_16x16x32_bf16 v[116:119], a[44:47], a[220:223], 0\n"
        "v_cvt_pk_bf16_f32 v100, v100, v101\n"
        "v_cvt_pk_bf16_f32 v101, v102, v103\n"
        "v_mfma_f32_16x16x32_bf16 v[116:119], a[40:43], a[216:219], v[116:119]\n"
        "v_mul_f32 v104, v96, v104\n"
        "v_mul_f32 v105, v96, v105\n"
        "v_mfma_f32_16x16x32_bf16 v[116:119], a[36:39], a[248:251], v[116:119]\n"
        "v_mul_f32 v106, v96, v106\n"
        "v_mul_f32 v107, v96, v107\n"
        "v_mfma_f32_16x16x32_bf16 v[116:119], a[32:35], a[244:247], v[116:119]\n"
        "v_cvt_pk_bf16_f32 v102, v104, v105\n"
        "v_cvt_pk_bf16_f32 v103, v106, v107\n"
        "v_mfma_f32_16x16x32_bf16 v[116:119], a[28:31], a[240:243], v[116:119]\n"
        "ds_write_b128 v245, v[100:103] offset:0\n"
        "v_mfma_f32_16x16x32_bf16 v[116:119], a[24:27], a[236:239], v[116:119]\n"
        "v_mul_f32 v108, v96, v108\n"
        "v_mul_f32 v109, v96, v109\n"
        "v_smfmac_f32_16x16x64_bf16 v[124:127], a[252:255], v[72:79], v237\n"
        "v_mul_f32 v110, v96, v110\n"
        "v_mul_f32 v111, v96, v111\n"
        "v_mfma_f32_16x16x32_bf16 v[64:67], v[132:135], v[76:79], 0\n"
        "v_cvt_pk_bf16_f32 v108, v108, v109\n"
        "v_cvt_pk_bf16_f32 v109, v110, v111\n"
        "v_mfma_f32_16x16x32_bf16 v[64:67], v[128:131], v[72:75], v[64:67]\n"
        "ds_read_b128 v[156:159], v247 offset:0\n"
        "v_cvt_pk_bf16_f32 v218, v116, v117\n"
        "v_mfma_f32_16x16x32_bf16 v[68:71], v[136:139], v[72:75], 0\n"
        "ds_write_b64 v246, v[108:109] offset:0\n"
        "v_mfma_f32_16x16x32_bf16 v[68:71], v[140:143], v[76:79], v[68:71]\n"
        "ds_read_b128 v[160:163], v247 offset:4096\n"
        "v_cvt_pk_bf16_f32 v219, v118, v119\n"
        "v_mfma_f32_16x16x32_bf16 v[120:123], v[148:151], v[76:79], 0\n"
        "ds_read_b128 v[164:167], v247 offset:4352\n"
        "v_max_u32 v236, v96, v97\n"
        "v_mfma_f32_16x16x32_bf16 v[120:123], v[144:147], v[72:75], v[120:123]\n"
        "v_mov_b64 v[116:117], 0\n"
        "v_add_f32 v125, v124, v125\n"
        "v_mfma_f32_16x16x32_bf16 v[112:115], a[48:51], a[236:239], 0\n"
        "v_rcp_f32 v124, v125\n"
        "v_mfma_f32_16x16x32_bf16 v[112:115], a[52:55], a[240:243], v[112:115]\n"
        "v_mul_f32 v64, v124, v64\n"
        "v_mul_f32 v65, v124, v65\n"
        "v_mfma_f32_16x16x32_bf16 v[112:115], a[56:59], a[244:247], v[112:115]\n"
        "v_mul_f32 v66, v124, v66\n"
        "v_mul_f32 v67, v124, v67\n"
        "v_mfma_f32_16x16x32_bf16 v[112:115], a[60:63], a[248:251], v[112:115]\n"
        "v_cvt_pk_bf16_f32 v64, v64, v65\n"
        "v_cvt_pk_bf16_f32 v65, v66, v67\n"
        "v_mfma_f32_16x16x32_bf16 v[112:115], a[64:67], a[216:219], v[112:115]\n"
        "v_mul_f32 v68, v124, v68\n"
        "v_mul_f32 v69, v124, v69\n"
        "v_mfma_f32_16x16x32_bf16 v[112:115], a[68:71], a[220:223], v[112:115]\n"
        "ds_read_b128 a[216:219], v248 offset:0\n"
        "v_mul_f32 v70, v124, v70\n"
        "v_smfmac_f32_16x16x64_bf16 v[116:119], a[252:255], v[80:87], v237\n"
        "ds_read_b128 a[220:223], v248 offset:3072\n"
        "v_mul_f32 v71, v124, v71\n"
        "v_mfma_f32_16x16x32_bf16 v[72:75], v[132:135], v[84:87], 0\n"
        "v_cvt_pk_bf16_f32 v66, v68, v69\n"
        "v_cvt_pk_bf16_f32 v67, v70, v71\n"
        "v_mfma_f32_16x16x32_bf16 v[72:75], v[128:131], v[80:83], v[72:75]\n"
        "ds_write_b128 v245, v[64:67] offset:256\n"
        "v_mfma_f32_16x16x32_bf16 v[76:79], v[136:139], v[80:83], 0\n"
        "v_cvt_pk_bf16_f32 v220, v112, v113\n"
        "v_cvt_pk_bf16_f32 v221, v114, v115\n"
        "v_mfma_f32_16x16x32_bf16 v[76:79], v[140:143], v[84:87], v[76:79]\n"
        "ds_read_b128 a[224:227], v254 offset:16\n"
        "v_cvt_pk_bf16_f32 v226, v112, v113\n"
        "v_mfma_f32_16x16x32_bf16 v[96:99], v[148:151], v[84:87], 0\n"
        "ds_read_b128 a[228:231], v254 offset:3088\n"
        "v_cvt_pk_bf16_f32 v227, v114, v115\n"
        "v_mfma_f32_16x16x32_bf16 v[96:99], v[144:147], v[80:83], v[96:99]\n"
        "v_mul_f32 v120, v124, v120\n"
        "v_mul_f32 v121, v124, v121\n"
        "v_mfma_f32_16x16x32_bf16 v[100:103], v[152:155], v[216:219], v[48:51]\n"
        "s_waitcnt lgkmcnt(10)\n"
        "ds_read_b128 v[152:155], v247 offset:256\n"
        "v_mul_f32 v122, v124, v122\n"
        "v_mfma_f32_16x16x32_bf16 v[100:103], v[168:171], v[220:223], v[100:103]\n"
        "v_mul_f32 v123, v124, v123\n"
        "v_cvt_pk_bf16_f32 v120, v120, v121\n"
        "v_mfma_f32_16x16x32_bf16 v[104:107], v[172:175], v[216:219], v[52:55]\n"
        "v_cvt_pk_bf16_f32 v121, v122, v123\n"
        "v_max3_u32 v236, v236, v124, v125\n"
        "v_mfma_f32_16x16x32_bf16 v[104:107], v[188:191], v[224:227], v[104:107]\n"
        "s_waitcnt lgkmcnt(5)\n"
        "ds_write_b64 v246, v[120:121] offset:256\n"
        "v_mfma_f32_16x16x32_bf16 v[176:179], v[156:159], a[216:219], 0\n"
        "s_waitcnt lgkmcnt(5)\n"
        "ds_read_b128 v[222:225], v252 offset:4608\n"
        "v_add_f32 v117, v116, v117\n"
        "v_mfma_f32_16x16x32_bf16 v[176:179], v[160:163], a[220:223], v[176:179]\n"
        "ds_read_b128 a[232:235], v248 offset:32\n"
        "v_mfma_f32_16x16x32_bf16 v[196:199], v[164:167], a[220:223], 0\n"
        "s_waitcnt lgkmcnt(3)\n"
        "ds_read_b128 a[220:223], v248 offset:3104\n"
        "v_mfma_f32_16x16x32_bf16 v[196:199], v[152:155], a[216:219], v[196:199]\n"
        "v_rcp_f32 v116, v117\n"
        "v_mfma_f32_16x16x32_bf16 v[200:203], v[152:155], a[224:227], 0\n"
        "ds_read_b128 a[216:219], v254 offset:48\n"
        "v_mul_f32 v72, v116, v72\n"
        "v_mfma_f32_16x16x32_bf16 v[200:203], v[164:167], a[228:231], v[200:203]\n"
        "v_exp_f32 v100, v100\n"
        "v_mfma_f32_16x16x32_bf16 v[180:183], v[160:163], a[228:231], 0\n"
        "ds_read_b128 a[228:231], v254 offset:3120\n"
        "v_mul_f32 v73, v116, v73\n"
        "v_mfma_f32_16x16x32_bf16 v[180:183], v[156:159], a[224:227], v[180:183]\n"
        "ds_read_b128 a[236:239], v247 offset:8192\n"
        "v_mul_f32 v74, v116, v74\n"
        "v_mfma_f32_16x16x32_bf16 v[108:111], v[192:195], v[216:219], v[56:59]\n"
        "s_waitcnt lgkmcnt(5)\n"
        "ds_read_b128 a[224:227], v248 offset:64\n"
        "v_mul_f32 v75, v116, v75\n"
        "v_mfma_f32_16x16x32_bf16 v[108:111], v[208:211], v[220:223], v[108:111]\n"
        "ds_read_b128 a[240:243], v247 offset:12288\n"
        "v_cvt_pk_bf16_f32 v72, v72, v73\n"
        "v_mfma_f32_16x16x32_bf16 v[112:115], v[212:215], v[216:219], v[60:63]\n"
        "ds_read_b128 a[244:247], v247 offset:12544\n"
        "v_cvt_pk_bf16_f32 v73, v74, v75\n"
        "v_mfma_f32_16x16x32_bf16 v[112:115], v[228:231], v[224:227], v[112:115]\n"
        "s_waitcnt lgkmcnt(7)\n"
        "ds_read_b128 a[248:251], v247 offset:8448\n"
        "v_mul_f32 v76, v116, v76\n"
        "v_mfma_f32_16x16x32_bf16 v[184:187], v[156:159], a[232:235], 0\n"
        "s_waitcnt lgkmcnt(7)\n"
        "v_exp_f32 v101, v101\n"
        "v_mfma_f32_16x16x32_bf16 v[184:187], v[160:163], a[220:223], v[184:187]\n"
        ".long 0xbec200ff\n"
        "44:\n"
        "s_branch 60b\n"
        "v_mul_f32 v77, v116, v77\n"
        "v_mul_f32 v78, v116, v78\n"
        "v_mfma_f32_16x16x32_bf16 v[204:207], v[164:167], a[220:223], 0\n"
        "ds_read_b128 a[220:223], v248 offset:3136\n"
        "v_mul_f32 v79, v116, v79\n"
        "v_mfma_f32_16x16x32_bf16 v[204:207], v[152:155], a[232:235], v[204:207]\n"
        "s_waitcnt lgkmcnt(7)\n"
        "ds_read_b128 a[232:235], v254 offset:80\n"
        "v_cvt_pk_bf16_f32 v88, v100, v101\n"
        "v_mfma_f32_16x16x32_bf16 v[192:195], v[152:155], a[216:219], 0\n"
        "s_waitcnt lgkmcnt(7)\n"
        "v_exp_f32 v102, v102\n"
        "v_mfma_f32_16x16x32_bf16 v[192:195], v[164:167], a[228:231], v[192:195]\n"
        "v_exp_f32 v104, v104\n"
        "v_mfma_f32_16x16x32_bf16 v[172:175], v[160:163], a[228:231], 0\n"
        "ds_read_b128 a[228:231], v254 offset:3152\n"
        "v_cvt_pk_bf16_f32 v74, v76, v77\n"
        "v_mfma_f32_16x16x32_bf16 v[172:175], v[156:159], a[216:219], v[172:175]\n"
        "s_waitcnt lgkmcnt(6)\n"
        "ds_read_b128 a[216:219], v248 offset:6144\n"
        "v_cvt_pk_bf16_f32 v75, v78, v79\n"
        "v_mfma_f32_16x16x32_bf16 v[212:215], v[156:159], a[224:227], 0\n"
        "s_waitcnt lgkmcnt(3)\n"
        "ds_write_b128 v245, v[72:75] offset:512\n"
        "v_mfma_f32_16x16x32_bf16 v[212:215], v[160:163], a[220:223], v[212:215]\n"
        "v_exp_f32 v103, v103\n"
        "v_mfma_f32_16x16x32_bf16 v[216:219], v[164:167], a[220:223], 0\n"
        "ds_read_b128 a[220:223], v248 offset:9216\n"
        "v_cvt_pk_bf16_f32 v89, v102, v103\n"
        "v_mfma_f32_16x16x32_bf16 v[216:219], v[152:155], a[224:227], v[216:219]\n"
        "s_waitcnt lgkmcnt(4)\n"
        "ds_read_b128 a[224:227], v254 offset:6160\n"
        "v_mul_f32 v96, v116, v96\n"
        "v_mfma_f32_16x16x32_bf16 v[224:227], v[152:155], a[232:235], 0\n"
        "s_waitcnt lgkmcnt(4)\n"
        "v_exp_f32 v105, v105\n"
        "v_mfma_f32_16x16x32_bf16 v[224:227], v[164:167], a[228:231], v[224:227]\n"
        "v_exp_f32 v106, v106\n"
        "v_mfma_f32_16x16x32_bf16 v[220:223], v[160:163], a[228:231], 0\n"
        "ds_read_b128 a[228:231], v254 offset:9232\n"
        "v_cvt_pk_bf16_f32 v90, v104, v105\n"
        "v_mfma_f32_16x16x32_bf16 v[220:223], v[156:159], a[232:235], v[220:223]\n"
        "s_waitcnt lgkmcnt(4)\n"
        "ds_read_b128 a[232:235], v248 offset:6176\n"
        "v_mul_f32 v97, v116, v97\n"
        "v_mfma_f32_16x16x32_bf16 v[176:179], a[236:239], a[216:219], v[176:179]\n"
        "s_waitcnt lgkmcnt(3)\n"
        "v_mul_f32 v98, v116, v98\n"
        "v_mul_f32 v99, v116, v99\n"
        "v_mfma_f32_16x16x32_bf16 v[176:179], a[240:243], a[220:223], v[176:179]\n"
        "v_exp_f32 v107, v107\n"
        "v_mfma_f32_16x16x32_bf16 v[196:199], a[244:247], a[220:223], v[196:199]\n"
        "ds_read_b128 a[220:223], v248 offset:9248\n"
        "v_cvt_pk_bf16_f32 v91, v106, v107\n"
        "v_mfma_f32_16x16x32_bf16 v[196:199], a[248:251], a[216:219], v[196:199]\n"
        "s_waitcnt lgkmcnt(3)\n"
        "ds_read_b128 a[216:219], v254 offset:6192\n"
        "v_cvt_pk_bf16_f32 v96, v96, v97\n"
        "v_mfma_f32_16x16x32_bf16 v[200:203], a[248:251], a[224:227], v[200:203]\n"
        "s_waitcnt lgkmcnt(3)\n"
        "ds_read_b128 v[156:159], v247 offset:16384\n"
        "v_cvt_pk_bf16_f32 v97, v98, v99\n"
        "v_mfma_f32_16x16x32_bf16 v[200:203], a[244:247], a[228:231], v[200:203]\n"
        "ds_write_b64 v246, v[96:97] offset:512\n"
        "v_mfma_f32_16x16x32_bf16 v[180:183], a[240:243], a[228:231], v[180:183]\n"
        "ds_read_b128 a[228:231], v254 offset:9264\n"
        "v_max3_u32 v236, v236, v116, v117\n"
        "v_mfma_f32_16x16x32_bf16 v[180:183], a[236:239], a[224:227], v[180:183]\n"
        "s_waitcnt lgkmcnt(5)\n"
        "ds_read_b128 a[224:227], v248 offset:6208\n"
        "v_mfma_f32_16x16x32_bf16 v[184:187], a[236:239], a[232:235], v[184:187]\n"
        "s_waitcnt lgkmcnt(5)\n"
        "ds_read_b128 v[160:163], v247 offset:20480\n"
        "v_mfma_f32_16x16x32_bf16 v[184:187], a[240:243], a[220:223], v[184:187]\n"
        "ds_read_b128 v[164:167], v247 offset:20736\n"
        "v_mfma_f32_16x16x32_bf16 v[204:207], a[244:247], a[220:223], v[204:207]\n"
        "ds_read_b128 a[220:223], v248 offset:9280\n"
        "v_mfma_f32_16x16x32_bf16 v[204:207], a[248:251], a[232:235], v[204:207]\n"
        "s_waitcnt lgkmcnt(7)\n"
        "ds_read_b128 a[232:235], v254 offset:6224\n"
        "v_mfma_f32_16x16x32_bf16 v[192:195], a[248:251], a[216:219], v[192:195]\n"
        "s_waitcnt lgkmcnt(5)\n"
        "ds_read_b128 v[152:155], v247 offset:16640\n"
        "v_mfma_f32_16x16x32_bf16 v[192:195], a[244:247], a[228:231], v[192:195]\n"
        "v_exp_f32 v108, v108\n"
        "v_mfma_f32_16x16x32_bf16 v[172:175], a[240:243], a[228:231], v[172:175]\n"
        "ds_read_b128 a[228:231], v254 offset:9296\n"
        "v_mfma_f32_16x16x32_bf16 v[172:175], a[236:239], a[216:219], v[172:175]\n"
        "s_waitcnt lgkmcnt(6)\n"
        "ds_read_b128 a[216:219], v248 offset:12288\n"
        "v_mfma_f32_16x16x32_bf16 v[212:215], a[236:239], a[224:227], v[212:215]\n"
        "s_waitcnt lgkmcnt(4)\n"
        "v_exp_f32 v109, v109\n"
        "v_mfma_f32_16x16x32_bf16 v[212:215], a[240:243], a[220:223], v[212:215]\n"
        "v_exp_f32 v110, v110\n"
        "v_mfma_f32_16x16x32_bf16 v[216:219], a[244:247], a[220:223], v[216:219]\n"
        "ds_read_b128 a[220:223], v248 offset:15360\n"
        "v_cvt_pk_bf16_f32 v92, v108, v109\n"
        "v_mfma_f32_16x16x32_bf16 v[216:219], a[248:251], a[224:227], v[216:219]\n"
        "s_waitcnt lgkmcnt(4)\n"
        "ds_read_b128 a[224:227], v254 offset:12304\n"
        "v_mfma_f32_16x16x32_bf16 v[224:227], a[248:251], a[232:235], v[224:227]\n"
        "s_waitcnt lgkmcnt(3)\n"
        "v_exp_f32 v112, v112\n"
        "v_mfma_f32_16x16x32_bf16 v[224:227], a[244:247], a[228:231], v[224:227]\n"
        "v_exp_f32 v111, v111\n"
        "v_mfma_f32_16x16x32_bf16 v[220:223], a[240:243], a[228:231], v[220:223]\n"
        "ds_read_b128 a[228:231], v254 offset:15376\n"
        "v_cvt_pk_bf16_f32 v93, v110, v111\n"
        "v_mfma_f32_16x16x32_bf16 v[220:223], a[236:239], a[232:235], v[220:223]\n"
        "s_waitcnt lgkmcnt(3)\n"
        "ds_read_b128 a[232:235], v248 offset:12320\n"
        "v_mfma_f32_16x16x32_bf16 v[176:179], v[156:159], a[216:219], v[176:179]\n"
        "s_waitcnt lgkmcnt(3)\n"
        "v_exp_f32 v113, v113\n"
        "v_mfma_f32_16x16x32_bf16 v[176:179], v[160:163], a[220:223], v[176:179]\n"
        "v_exp_f32 v114, v114\n"
        "v_mfma_f32_16x16x32_bf16 v[196:199], v[164:167], a[220:223], v[196:199]\n"
        "ds_read_b128 a[220:223], v248 offset:15392\n"
        "v_cvt_pk_bf16_f32 v94, v112, v113\n"
        "v_mfma_f32_16x16x32_bf16 v[196:199], v[152:155], a[216:219], v[196:199]\n"
        "s_waitcnt lgkmcnt(3)\n"
        "ds_read_b128 a[216:219], v254 offset:12336\n"
        "v_mfma_f32_16x16x32_bf16 v[200:203], v[152:155], a[224:227], v[200:203]\n"
        "s_waitcnt lgkmcnt(3)\n"
        "v_exp_f32 v115, v115\n"
        "v_mfma_f32_16x16x32_bf16 v[200:203], v[164:167], a[228:231], v[200:203]\n"
        "v_cvt_pk_bf16_f32 v95, v114, v115\n"
        "v_mfma_f32_16x16x32_bf16 v[180:183], v[160:163], a[228:231], v[180:183]\n"
        "s_waitcnt vmcnt(0)\n"
        "s_barrier\n"
        "ds_read_b128 a[228:231], v254 offset:15408\n"
        "v_mfma_f32_16x16x32_bf16 v[180:183], v[156:159], a[224:227], v[180:183]\n"
        "s_waitcnt lgkmcnt(3)\n"
        "ds_read_b128 a[224:227], v248 offset:12352\n"
        "v_mfma_f32_16x16x32_bf16 v[184:187], v[156:159], a[232:235], v[184:187]\n"
        "s_waitcnt lgkmcnt(3)\n"
        "v_mfma_f32_16x16x32_bf16 v[184:187], v[160:163], a[220:223], v[184:187]\n"
        "v_mfma_f32_16x16x32_bf16 v[204:207], v[164:167], a[220:223], v[204:207]\n"
        "ds_read_b128 a[220:223], v248 offset:15424\n"
        "v_mfma_f32_16x16x32_bf16 v[204:207], v[152:155], a[232:235], v[204:207]\n"
        "s_waitcnt lgkmcnt(3)\n"
        "ds_read_b128 a[232:235], v254 offset:12368\n"
        "v_mfma_f32_16x16x32_bf16 v[192:195], v[152:155], a[216:219], v[192:195]\n"
        "s_waitcnt lgkmcnt(3)\n"
        "ds_read_b128 a[236:239], v240 offset:0\n"
        "v_mfma_f32_16x16x32_bf16 v[192:195], v[164:167], a[228:231], v[192:195]\n"
        "ds_read_b128 a[240:243], v241 offset:0\n"
        "v_mfma_f32_16x16x32_bf16 v[172:175], v[160:163], a[228:231], v[172:175]\n"
        "ds_read_b128 a[228:231], v254 offset:15440\n"
        "v_mfma_f32_16x16x32_bf16 v[172:175], v[156:159], a[216:219], v[172:175]\n"
        "s_waitcnt lgkmcnt(5)\n"
        "ds_read_b128 a[244:247], v240 offset:128\n"
        "v_mfma_f32_16x16x32_bf16 v[212:215], v[156:159], a[224:227], v[212:215]\n"
        "s_waitcnt lgkmcnt(5)\n"
        "ds_read_b128 a[248:251], v241 offset:128\n"
        "v_mfma_f32_16x16x32_bf16 v[212:215], v[160:163], a[220:223], v[212:215]\n"
        "ds_read_b128 a[216:219], v240 offset:256\n"
        "v_mfma_f32_16x16x32_bf16 v[216:219], v[164:167], a[220:223], v[216:219]\n"
        "ds_read_b128 a[220:223], v241 offset:256\n"
        "v_mfma_f32_16x16x32_bf16 v[216:219], v[152:155], a[224:227], v[216:219]\n"
        "s_waitcnt lgkmcnt(7)\n"
        "v_mfma_f32_16x16x32_bf16 v[224:227], v[152:155], a[232:235], v[224:227]\n"
        "s_waitcnt lgkmcnt(4)\n"
        "v_mfma_f32_16x16x32_bf16 v[224:227], v[164:167], a[228:231], v[224:227]\n"
        "v_mfma_f32_16x16x32_bf16 v[220:223], v[160:163], a[228:231], v[220:223]\n"
        "v_mfma_f32_16x16x32_bf16 v[220:223], v[156:159], a[232:235], v[220:223]\n"
        "s_waitcnt lgkmcnt(0)\n"
        "s_cmp_eq_u32 s42, 2\n"
        "s_cbranch_scc1 12f\n"
        "s_cmp_eq_u32 s42, 3\n"
        "s_cbranch_scc1 12f\n"
        ".p2align 6\n"
        "s_nop 0\n"
        "s_nop 0\n"
        "s_nop 0\n"
        "s_nop 0\n"
        "11:\n"
        "10:\n"
        "v_mfma_f32_16x16x32_bf16 v[112:115], a[72:75], a[236:239], 0\n"
        "v_mov_b64 v[124:125], 0\n"
        "v_mfma_f32_16x16x32_bf16 v[112:115], a[76:79], a[240:243], v[112:115]\n"
        "s_waitcnt lgkmcnt(3)\n"
        "s_add_u32 s41, s30, s40\n"
        "v_cvt_pk_bf16_f32 v232, v176, v180\n"
        "v_cvt_pk_bf16_f32 v233, v184, v172\n"
        "v_mfma_f32_16x16x32_bf16 v[112:115], a[80:83], a[244:247], v[112:115]\n"
        "s_waitcnt lgkmcnt(2)\n"
        "v_cvt_pk_bf16_f32 v234, v212, v220\n"
        "v_mfma_f32_16x16x32_bf16 v[112:115], a[84:87], a[248:251], v[112:115]\n"
        "s_waitcnt lgkmcnt(1)\n"
        "buffer_store_dwordx3 v[232:234], v250, s[16:19], s38 offen offset:0 sc1\n"
        "v_mfma_f32_16x16x32_bf16 v[112:115], a[88:91], a[216:219], v[112:115]\n"
        "s_waitcnt lgkmcnt(0)\n"
        "s_nop 0\n"
        "v_mfma_f32_16x16x32_bf16 v[112:115], a[92:95], a[220:223], v[112:115]\n"
        "v_cvt_pk_bf16_f32 v232, v177, v181\n"
        "v_cvt_pk_bf16_f32 v233, v185, v173\n"
        "v_smfmac_f32_16x16x64_bf16 v[124:127], a[252:255], v[88:95], v237\n"
        "v_cvt_pk_bf16_f32 v234, v213, v221\n"
        "v_mfma_f32_16x16x32_bf16 v[80:83], v[132:135], v[92:95], 0\n"
        "s_nop 2\n"
        "v_mfma_f32_16x16x32_bf16 v[80:83], v[128:131], v[88:91], v[80:83]\n"
        "v_cvt_pk_bf16_f32 v152, v112, v113\n"
        "v_cvt_pk_bf16_f32 v153, v114, v115\n"
        "v_mfma_f32_16x16x32_bf16 v[84:87], v[136:139], v[88:91], 0\n"
        "v_add_f32 v125, v124, v125\n"
        "v_mfma_f32_16x16x32_bf16 v[84:87], v[140:143], v[92:95], v[84:87]\n"
        "v_rcp_f32 v124, v125\n"
        "v_mfma_f32_16x16x32_bf16 v[96:99], v[148:151], v[92:95], 0\n"
        "buffer_store_dwordx3 v[232:234], v250, s[16:19], s38 offen offset:384 sc1\n"
        "v_mfma_f32_16x16x32_bf16 v[96:99], v[144:147], v[88:91], v[96:99]\n"
        "s_add_u32 s35, s47, 786432\n"
        "v_mul_f32 v80, v124, v80\n"
        "v_mul_f32 v81, v124, v81\n"
        "v_mfma_f32_16x16x32_bf16 v[116:119], a[116:119], a[220:223], 0\n"
        "s_sub_u32 s46, s35, 100663296\n"
        "v_mul_f32 v82, v124, v82\n"
        "v_mul_f32 v83, v124, v83\n"
        "v_mfma_f32_16x16x32_bf16 v[116:119], a[112:115], a[216:219], v[116:119]\n"
        "s_cmp_lt_u32 s46, 786432\n"
        "v_cvt_pk_bf16_f32 v80, v80, v81\n"
        "v_cvt_pk_bf16_f32 v81, v82, v83\n"
        "v_mfma_f32_16x16x32_bf16 v[116:119], a[108:111], a[248:251], v[116:119]\n"
        "s_cselect_b32 s35, s46, s35\n"
        "v_mul_f32 v84, v124, v84\n"
        "v_mul_f32 v85, v124, v85\n"
        "v_mfma_f32_16x16x32_bf16 v[116:119], a[104:107], a[244:247], v[116:119]\n"
        "v_mul_f32 v86, v124, v86\n"
        "v_mul_f32 v87, v124, v87\n"
        "v_mfma_f32_16x16x32_bf16 v[116:119], a[100:103], a[240:243], v[116:119]\n"
        "v_cvt_pk_bf16_f32 v232, v178, v182\n"
        "v_cvt_pk_bf16_f32 v233, v186, v174\n"
        "v_mfma_f32_16x16x32_bf16 v[116:119], a[96:99], a[236:239], v[116:119]\n"
        "v_cvt_pk_bf16_f32 v234, v214, v222\n"
        "v_cvt_pk_bf16_f32 v82, v84, v85\n"
        "v_mfma_f32_16x16x32_bf16 v[120:123], a[120:123], a[236:239], 0\n"
        "v_cvt_pk_bf16_f32 v83, v86, v87\n"
        "v_mul_f32 v96, v124, v96\n"
        "v_mfma_f32_16x16x32_bf16 v[120:123], a[124:127], a[240:243], v[120:123]\n"
        "buffer_store_dwordx3 v[232:234], v250, s[16:19], s38 offen offset:768 sc1\n"
        "v_mfma_f32_16x16x32_bf16 v[120:123], a[128:131], a[244:247], v[120:123]\n"
        "v_mul_f32 v97, v124, v97\n"
        "v_mul_f32 v98, v124, v98\n"
        "v_mfma_f32_16x16x32_bf16 v[120:123], a[132:135], a[248:251], v[120:123]\n"
        "v_mul_f32 v99, v124, v99\n"
        "v_cvt_pk_bf16_f32 v154, v116, v117\n"
        "v_mfma_f32_16x16x32_bf16 v[120:123], a[136:139], a[216:219], v[120:123]\n"
        "ds_write_b128 v245, v[80:83] offset:768\n"
        "v_mfma_f32_16x16x32_bf16 v[120:123], a[140:143], a[220:223], v[120:123]\n"
        "v_cvt_pk_bf16_f32 v155, v118, v119\n"
        "v_cvt_pk_bf16_f32 v96, v96, v97\n"
        "v_mfma_f32_16x16x32_bf16 v[112:115], a[20:23], a[220:223], 0\n"
        "v_cvt_pk_bf16_f32 v97, v98, v99\n"
        "v_max3_u32 v236, v236, v124, v125\n"
        "v_mfma_f32_16x16x32_bf16 v[112:115], a[16:19], a[216:219], v[112:115]\n"
        "v_cmp_lt_u32 vcc, s24, v236\n"
        "v_cvt_pk_bf16_f32 v232, v179, v183\n"
        "v_mfma_f32_16x16x32_bf16 v[112:115], a[12:15], a[248:251], v[112:115]\n"
        "ds_write_b64 v246, v[96:97] offset:768\n"
        "s_cmp_lg_u64 vcc, 0\n"
        "s_addc_u32 s25, s25, s25\n"
        "v_mfma_f32_16x16x32_bf16 v[112:115], a[8:11], a[244:247], v[112:115]\n"
        "v_cvt_pk_bf16_f32 v233, v187, v175\n"
        "v_cvt_pk_bf16_f32 v234, v215, v223\n"
        "v_mfma_f32_16x16x32_bf16 v[112:115], a[4:7], a[240:243], v[112:115]\n"
        "v_cvt_pk_bf16_f32 v168, v120, v121\n"
        "v_cvt_pk_bf16_f32 v169, v122, v123\n"
        "v_mfma_f32_16x16x32_bf16 v[112:115], a[0:3], a[236:239], v[112:115]\n"
        "buffer_store_dwordx3 v[232:234], v250, s[16:19], s38 offen offset:1152 sc1\n"
        "v_mfma_f32_16x16x32_bf16 v[116:119], a[24:27], a[236:239], 0\n"
        "v_xor_b32 v251, 0x2000, v251\n"
        "v_xor_b32 v252, 0x2000, v252\n"
        "v_mfma_f32_16x16x32_bf16 v[116:119], a[28:31], a[240:243], v[116:119]\n"
        "v_cvt_pk_bf16_f32 v232, v196, v200\n"
        "v_cvt_pk_bf16_f32 v233, v204, v192\n"
        "v_mfma_f32_16x16x32_bf16 v[116:119], a[32:35], a[244:247], v[116:119]\n"
        "v_cvt_pk_bf16_f32 v156, v112, v113\n"
        "v_cvt_pk_bf16_f32 v157, v114, v115\n"
        "v_mfma_f32_16x16x32_bf16 v[116:119], a[36:39], a[248:251], v[116:119]\n"
        "v_cvt_pk_bf16_f32 v234, v216, v224\n"
        "v_mfma_f32_16x16x32_bf16 v[116:119], a[40:43], a[216:219], v[116:119]\n"
        "v_mfma_f32_16x16x32_bf16 v[116:119], a[44:47], a[220:223], v[116:119]\n"
        "s_nop 2\n"
        "v_mfma_f32_16x16x32_bf16 v[120:123], a[68:71], a[220:223], 0\n"
        "s_nop 2\n"
        "v_mfma_f32_16x16x32_bf16 v[120:123], a[64:67], a[216:219], v[120:123]\n"
        "buffer_store_dwordx3 v[232:234], v250, s[16:19], s39 offen offset:0 sc1\n"
        "v_mfma_f32_16x16x32_bf16 v[120:123], a[60:63], a[248:251], v[120:123]\n"
        "ds_read_b128 a[224:227], v240 offset:1536\n"
        "v_cvt_pk_bf16_f32 v158, v116, v117\n"
        "v_mfma_f32_16x16x32_bf16 v[120:123], a[56:59], a[244:247], v[120:123]\n"
        "ds_read_b128 a[228:231], v241 offset:1536\n"
        "v_cvt_pk_bf16_f32 v159, v118, v119\n"
        "v_mfma_f32_16x16x32_bf16 v[120:123], a[52:55], a[240:243], v[120:123]\n"
        "ds_read_b128 a[232:235], v240 offset:1664\n"
        "v_cvt_pk_bf16_f32 v232, v197, v201\n"
        "v_mfma_f32_16x16x32_bf16 v[120:123], a[48:51], a[236:239], v[120:123]\n"
        "v_cvt_pk_bf16_f32 v233, v205, v193\n"
        "v_cvt_pk_bf16_f32 v234, v217, v225\n"
        "v_mfma_f32_16x16x32_bf16 v[112:115], a[236:239], a[144:147], 0\n"
        "v_add_u32 v247, s45, v247\n"
        "s_nop 0\n"
        "v_mfma_f32_16x16x32_bf16 v[112:115], a[240:243], a[148:151], v[112:115]\n"
        "s_nop 1\n"
        "v_cvt_pk_bf16_f32 v160, v120, v121\n"
        "v_mfma_f32_16x16x32_bf16 v[112:115], a[244:247], a[152:155], v[112:115]\n"
        "v_cvt_pk_bf16_f32 v161, v122, v123\n"
        "v_cvt_pk_bf16_f32 v166, v120, v121\n"
        "v_mfma_f32_16x16x32_bf16 v[112:115], a[248:251], a[156:159], v[112:115]\n"
        "v_cvt_pk_bf16_f32 v167, v122, v123\n"
        "v_mfma_f32_16x16x32_bf16 v[112:115], a[216:219], a[160:163], v[112:115]\n"
        "buffer_store_dwordx3 v[232:234], v250, s[16:19], s39 offen offset:384 sc1\n"
        "v_mfma_f32_16x16x32_bf16 v[112:115], a[220:223], a[164:167], v[112:115]\n"
        "s_nop 2\n"
        "v_mfma_f32_16x16x32_bf16 v[116:119], a[220:223], a[188:191], 0\n"
        "s_nop 2\n"
        "v_mfma_f32_16x16x32_bf16 v[116:119], a[216:219], a[184:187], v[116:119]\n"
        "v_cvt_pk_bf16_f32 v128, v112, v113\n"
        "v_cvt_pk_bf16_f32 v129, v114, v115\n"
        "v_mfma_f32_16x16x32_bf16 v[116:119], a[248:251], a[180:183], v[116:119]\n"
        "v_cvt_pk_bf16_f32 v232, v198, v202\n"
        "v_cvt_pk_bf16_f32 v233, v206, v194\n"
        "v_mfma_f32_16x16x32_bf16 v[116:119], a[244:247], a[176:179], v[116:119]\n"
        "v_cvt_pk_bf16_f32 v234, v218, v226\n"
        "v_mfma_f32_16x16x32_bf16 v[116:119], a[240:243], a[172:175], v[116:119]\n"
        "v_mfma_f32_16x16x32_bf16 v[116:119], a[236:239], a[168:171], v[116:119]\n"
        "s_nop 2\n"
        "v_mfma_f32_16x16x32_bf16 v[120:123], a[236:239], a[192:195], 0\n"
        "ds_read_b128 a[236:239], v241 offset:1664\n"
        "s_nop 0\n"
        "v_mfma_f32_16x16x32_bf16 v[120:123], a[240:243], a[196:199], v[120:123]\n"
        "ds_read_b128 a[240:243], v240 offset:1792\n"
        "v_cvt_pk_bf16_f32 v136, v116, v117\n"
        "v_mfma_f32_16x16x32_bf16 v[120:123], a[244:247], a[200:203], v[120:123]\n"
        "ds_read_b128 a[244:247], v241 offset:1792\n"
        "v_cvt_pk_bf16_f32 v137, v118, v119\n"
        "v_mfma_f32_16x16x32_bf16 v[120:123], a[248:251], a[204:207], v[120:123]\n"
        "buffer_store_dwordx3 v[232:234], v250, s[16:19], s39 offen offset:768 sc1\n"
        "v_mfma_f32_16x16x32_bf16 v[120:123], a[216:219], a[208:211], v[120:123]\n"
        "s_nop 0\n"
        "v_cvt_pk_bf16_f32 v232, v199, v203\n"
        "v_cvt_pk_bf16_f32 v233, v207, v195\n"
        "v_mfma_f32_16x16x32_bf16 v[120:123], a[220:223], a[212:215], v[120:123]\n"
        "s_waitcnt lgkmcnt(5)\n"
        "v_cvt_pk_bf16_f32 v234, v219, v227\n"
        "s_nop 0\n"
        "v_mfma_f32_16x16x32_bf16 v[112:115], a[72:75], a[224:227], 0\n"
        "s_waitcnt lgkmcnt(4)\n"
        "s_nop 1\n"
        "v_mfma_f32_16x16x32_bf16 v[112:115], a[76:79], a[228:231], v[112:115]\n"
        "s_waitcnt lgkmcnt(3)\n"
        "s_nop 1\n"
        "v_mfma_f32_16x16x32_bf16 v[112:115], a[80:83], a[232:235], v[112:115]\n"
        "s_waitcnt lgkmcnt(2)\n"
        "v_cvt_pk_bf16_f32 v144, v120, v121\n"
        "v_cvt_pk_bf16_f32 v145, v122, v123\n"
        "v_mfma_f32_16x16x32_bf16 v[112:115], a[84:87], a[236:239], v[112:115]\n"
        "s_waitcnt lgkmcnt(1)\n"
        "v_mfma_f32_16x16x32_bf16 v[112:115], a[88:91], a[240:243], v[112:115]\n"
        "s_waitcnt lgkmcnt(0)\n"
        "buffer_store_dwordx3 v[232:234], v250, s[16:19], s39 offen offset:1152 sc1\n"
        "v_mfma_f32_16x16x32_bf16 v[112:115], a[92:95], a[244:247], v[112:115]\n"
        "s_nop 2\n"
        "v_mfma_f32_16x16x32_bf16 v[116:119], a[116:119], a[244:247], 0\n"
        "s_nop 2\n"
        "v_mfma_f32_16x16x32_bf16 v[116:119], a[112:115], a[240:243], v[116:119]\n"
        "v_cvt_pk_bf16_f32 v172, v112, v113\n"
        "v_cvt_pk_bf16_f32 v173, v114, v115\n"
        "v_mfma_f32_16x16x32_bf16 v[116:119], a[108:111], a[236:239], v[116:119]\n"
        "v_mfma_f32_16x16x32_bf16 v[116:119], a[104:107], a[232:235], v[116:119]\n"
        "v_mfma_f32_16x16x32_bf16 v[116:119], a[100:103], a[228:231], v[116:119]\n"
        "v_mfma_f32_16x16x32_bf16 v[116:119], a[96:99], a[224:227], v[116:119]\n"
        "s_mov_b32 m0, s48\n"
        "s_nop 1\n"
        "v_mfma_f32_16x16x32_bf16 v[120:123], a[120:123], a[224:227], 0\n"
        "ds_read_b128 v[162:165], v251 offset:0\n"
        "s_nop 0\n"
        "v_mfma_f32_16x16x32_bf16 v[120:123], a[124:127], a[228:231], v[120:123]\n"
        "buffer_load_dwordx4 v249, s[12:15], s36 offen offset:0 lds\n"
        "v_mfma_f32_16x16x32_bf16 v[120:123], a[128:131], a[232:235], v[120:123]\n"
        "v_cvt_pk_bf16_f32 v174, v116, v117\n"
        "v_cvt_pk_bf16_f32 v175, v118, v119\n"
        "v_mfma_f32_16x16x32_bf16 v[120:123], a[132:135], a[236:239], v[120:123]\n"
        "v_mfma_f32_16x16x32_bf16 v[120:123], a[136:139], a[240:243], v[120:123]\n"
        "v_mfma_f32_16x16x32_bf16 v[120:123], a[140:143], a[244:247], v[120:123]\n"
        "s_nop 2\n"
        "v_mfma_f32_16x16x32_bf16 v[112:115], a[20:23], a[244:247], 0\n"
        "s_nop 2\n"
        "v_mfma_f32_16x16x32_bf16 v[112:115], a[16:19], a[240:243], v[112:115]\n"
        "v_cvt_pk_bf16_f32 v190, v120, v121\n"
        "v_cvt_pk_bf16_f32 v191, v122, v123\n"
        "v_mfma_f32_16x16x32_bf16 v[112:115], a[12:15], a[236:239], v[112:115]\n"
        "v_mfma_f32_16x16x32_bf16 v[112:115], a[8:11], a[232:235], v[112:115]\n"
        "v_mfma_f32_16x16x32_bf16 v[112:115], a[4:7], a[228:231], v[112:115]\n"
        "ds_read_b128 v[182:185], v251 offset:512\n"
        "v_mfma_f32_16x16x32_bf16 v[112:115], a[0:3], a[224:227], v[112:115]\n"
        "s_nop 2\n"
        "v_mfma_f32_16x16x32_bf16 v[116:119], a[24:27], a[224:227], 0\n"
        "s_nop 2\n"
        "v_mfma_f32_16x16x32_bf16 v[116:119], a[28:31], a[228:231], v[116:119]\n"
        "v_cvt_pk_bf16_f32 v176, v112, v113\n"
        "v_cvt_pk_bf16_f32 v177, v114, v115\n"
        "v_mfma_f32_16x16x32_bf16 v[116:119], a[32:35], a[232:235], v[116:119]\n"
        "v_mfma_f32_16x16x32_bf16 v[116:119], a[36:39], a[236:239], v[116:119]\n"
        "buffer_load_dwordx4 v253, s[12:15], s36 offen offset:1024 lds\n"
        "v_mfma_f32_16x16x32_bf16 v[116:119], a[40:43], a[240:243], v[116:119]\n"
        "v_mfma_f32_16x16x32_bf16 v[116:119], a[44:47], a[244:247], v[116:119]\n"
        "s_nop 2\n"
        "v_mfma_f32_16x16x32_bf16 v[120:123], a[68:71], a[244:247], 0\n"
        "s_nop 2\n"
        "v_mfma_f32_16x16x32_bf16 v[120:123], a[64:67], a[240:243], v[120:123]\n"
        "v_cvt_pk_bf16_f32 v178, v116, v117\n"
        "v_cvt_pk_bf16_f32 v179, v118, v119\n"
        "v_mfma_f32_16x16x32_bf16 v[120:123], a[60:63], a[236:239], v[120:123]\n"
        "v_mfma_f32_16x16x32_bf16 v[120:123], a[56:59], a[232:235], v[120:123]\n"
        "v_mfma_f32_16x16x32_bf16 v[120:123], a[52:55], a[228:231], v[120:123]\n"
        "v_mfma_f32_16x16x32_bf16 v[120:123], a[48:51], a[224:227], v[120:123]\n"
        "s_nop 2\n"
        "v_mfma_f32_16x16x32_bf16 v[112:115], a[224:227], a[144:147], 0\n"
        "s_nop 2\n"
        "v_mfma_f32_16x16x32_bf16 v[112:115], a[228:231], a[148:151], v[112:115]\n"
        "v_cvt_pk_bf16_f32 v180, v120, v121\n"
        "v_cvt_pk_bf16_f32 v181, v122, v123\n"
        "v_mfma_f32_16x16x32_bf16 v[112:115], a[232:235], a[152:155], v[112:115]\n"
        "v_cvt_pk_bf16_f32 v186, v120, v121\n"
        "v_cvt_pk_bf16_f32 v187, v122, v123\n"
        "v_mfma_f32_16x16x32_bf16 v[112:115], a[236:239], a[156:159], v[112:115]\n"
        "s_add_u32 m0, s41, 0\n"
        "v_mfma_f32_16x16x32_bf16 v[112:115], a[240:243], a[160:163], v[112:115]\n"
        "ds_read_b128 a[248:251], v240 offset:12288\n"
        "v_mfma_f32_16x16x32_bf16 v[112:115], a[244:247], a[164:167], v[112:115]\n"
        "buffer_load_dwordx4 v242, s[8:11], s47 offen lds\n"
        "v_mfma_f32_16x16x32_bf16 v[96:99], v[152:155], v[156:159], v[0:3]\n"
        "s_waitcnt lgkmcnt(2)\n"
        "ds_read_b128 a[216:219], v241 offset:12288\n"
        "s_nop 0\n"
        "v_mfma_f32_16x16x32_bf16 v[96:99], v[168:171], v[160:163], v[96:99]\n"
        "ds_read_b128 a[220:223], v240 offset:12416\n"
        "s_sub_u32 s31, s30, s28\n"
        "s_nop 0\n"
        "v_mfma_f32_16x16x32_bf16 v[100:103], v[172:175], v[156:159], v[4:7]\n"
        "s_sub_u32 s45, s28, s29\n"
        "v_cvt_pk_bf16_f32 v130, v112, v113\n"
        "v_cvt_pk_bf16_f32 v131, v114, v115\n"
        "v_mfma_f32_16x16x32_bf16 v[100:103], v[188:191], v[164:167], v[100:103]\n"
        "s_add_u32 s28, s28, s31\n"
        "ds_read_b128 v[162:165], v252 offset:0\n"
        "v_mfma_f32_16x16x32_bf16 v[116:119], a[244:247], a[188:191], 0\n"
        "s_mov_b32 s30, s29\n"
        "v_exp_f32 v96, v96\n"
        "v_mfma_f32_16x16x32_bf16 v[116:119], a[240:243], a[184:187], v[116:119]\n"
        "s_add_u32 s29, s29, s45\n"
        "v_exp_f32 v97, v97\n"
        "v_mfma_f32_16x16x32_bf16 v[116:119], a[236:239], a[180:183], v[116:119]\n"
        "v_exp_f32 v98, v98\n"
        "v_mfma_f32_16x16x32_bf16 v[116:119], a[232:235], a[176:179], v[116:119]\n"
        "v_exp_f32 v100, v100\n"
        "v_mfma_f32_16x16x32_bf16 v[116:119], a[228:231], a[172:175], v[116:119]\n"
        "v_exp_f32 v99, v99\n"
        "v_mfma_f32_16x16x32_bf16 v[116:119], a[224:227], a[168:171], v[116:119]\n"
        "v_exp_f32 v101, v101\n"
        "v_mfma_f32_16x16x32_bf16 v[104:107], v[152:155], v[176:179], v[16:19]\n"
        "s_waitcnt lgkmcnt(4)\n"
        "v_exp_f32 v102, v102\n"
        "v_mfma_f32_16x16x32_bf16 v[104:107], v[168:171], v[180:183], v[104:107]\n"
        "s_add_u32 m0, s41, 1024\n"
        "v_cvt_pk_bf16_f32 v64, v96, v97\n"
        "v_cvt_pk_bf16_f32 v65, v98, v99\n"
        "v_mfma_f32_16x16x32_bf16 v[108:111], v[172:175], v[176:179], v[20:23]\n"
        "v_cvt_pk_bf16_f32 v138, v116, v117\n"
        "v_cvt_pk_bf16_f32 v139, v118, v119\n"
        "v_mfma_f32_16x16x32_bf16 v[108:111], v[188:191], v[184:187], v[108:111]\n"
        "buffer_load_dwordx4 v243, s[8:11], s47 offen lds\n"
        "v_mfma_f32_16x16x32_bf16 v[120:123], a[224:227], a[192:195], 0\n"
        "ds_read_b128 a[224:227], v241 offset:12416\n"
        "v_cvt_pk_bf16_f32 v66, v100, v101\n"
        "v_mfma_f32_16x16x32_bf16 v[120:123], a[228:231], a[196:199], v[120:123]\n"
        "ds_read_b128 a[228:231], v240 offset:12544\n"
        "v_mfma_f32_16x16x32_bf16 v[120:123], a[232:235], a[200:203], v[120:123]\n"
        "ds_read_b128 a[232:235], v241 offset:12544\n"
        "v_mfma_f32_16x16x32_bf16 v[120:123], a[236:239], a[204:207], v[120:123]\n"
        "ds_read_b128 v[202:205], v251 offset:4096\n"
        "v_mfma_f32_16x16x32_bf16 v[120:123], a[240:243], a[208:211], v[120:123]\n"
        "ds_read_b128 v[182:185], v252 offset:512\n"
        "v_mfma_f32_16x16x32_bf16 v[120:123], a[244:247], a[212:215], v[120:123]\n"
        "s_waitcnt lgkmcnt(8)\n"
        "v_exp_f32 v103, v103\n"
        "v_mfma_f32_16x16x32_bf16 v[112:115], a[0:3], a[248:251], 0\n"
        "s_waitcnt lgkmcnt(7)\n"
        "v_cvt_pk_bf16_f32 v67, v102, v103\n"
        "s_nop 0\n"
        "v_mfma_f32_16x16x32_bf16 v[112:115], a[4:7], a[216:219], v[112:115]\n"
        "s_waitcnt lgkmcnt(6)\n"
        "v_exp_f32 v104, v104\n"
        "v_mfma_f32_16x16x32_bf16 v[112:115], a[8:11], a[220:223], v[112:115]\n"
        "s_waitcnt lgkmcnt(4)\n"
        "v_exp_f32 v105, v105\n"
        "v_mfma_f32_16x16x32_bf16 v[112:115], a[12:15], a[224:227], v[112:115]\n"
        "s_waitcnt lgkmcnt(3)\n"
        "v_exp_f32 v106, v106\n"
        "v_mfma_f32_16x16x32_bf16 v[112:115], a[16:19], a[228:231], v[112:115]\n"
        "s_waitcnt lgkmcnt(2)\n"
        "v_cvt_pk_bf16_f32 v72, v104, v105\n"
        "v_cvt_pk_bf16_f32 v146, v120, v121\n"
        "v_mfma_f32_16x16x32_bf16 v[112:115], a[20:23], a[232:235], v[112:115]\n"
        "s_add_u32 m0, s41, 2048\n"
        "v_exp_f32 v108, v108\n"
        "v_mfma_f32_16x16x32_bf16 v[116:119], a[44:47], a[232:235], 0\n"
        "v_cvt_pk_bf16_f32 v147, v122, v123\n"
        "v_add_u32 v245, s45, v245\n"
        "v_mfma_f32_16x16x32_bf16 v[116:119], a[40:43], a[228:231], v[116:119]\n"
        "buffer_load_dwordx4 v244, s[8:11], s47 offen lds\n"
        "v_mfma_f32_16x16x32_bf16 v[116:119], a[36:39], a[224:227], v[116:119]\n"
        "v_exp_f32 v107, v107\n"
        "v_mfma_f32_16x16x32_bf16 v[116:119], a[32:35], a[220:223], v[116:119]\n"
        "v_exp_f32 v109, v109\n"
        "v_mfma_f32_16x16x32_bf16 v[116:119], a[28:31], a[216:219], v[116:119]\n"
        "v_cvt_pk_bf16_f32 v73, v106, v107\n"
        "v_cvt_pk_bf16_f32 v74, v108, v109\n"
        "v_mfma_f32_16x16x32_bf16 v[116:119], a[24:27], a[248:251], v[116:119]\n"
        "v_exp_f32 v110, v110\n"
        "v_mfma_f32_16x16x32_bf16 v[120:123], a[48:51], a[248:251], 0\n"
        "v_exp_f32 v111, v111\n"
        "v_mfma_f32_16x16x32_bf16 v[120:123], a[52:55], a[216:219], v[120:123]\n"
        "v_cvt_pk_bf16_f32 v75, v110, v111\n"
        "v_cvt_pk_bf16_f32 v196, v112, v113\n"
        "v_mfma_f32_16x16x32_bf16 v[120:123], a[56:59], a[220:223], v[120:123]\n"
        "v_cvt_pk_bf16_f32 v197, v114, v115\n"
        "v_cvt_pk_bf16_f32 v198, v116, v117\n"
        "v_mfma_f32_16x16x32_bf16 v[120:123], a[60:63], a[224:227], v[120:123]\n"
        "v_cvt_pk_bf16_f32 v199, v118, v119\n"
        "v_add_u32 v246, s45, v246\n"
        "v_mfma_f32_16x16x32_bf16 v[120:123], a[64:67], a[228:231], v[120:123]\n"
        "v_mfma_f32_16x16x32_bf16 v[120:123], a[68:71], a[232:235], v[120:123]\n"
        "s_nop 2\n"
        "v_mfma_f32_16x16x32_bf16 v[112:115], a[92:95], a[232:235], 0\n"
        "s_nop 2\n"
        "v_mfma_f32_16x16x32_bf16 v[112:115], a[88:91], a[228:231], v[112:115]\n"
        "s_add_u32 m0, s41, 12288\n"
        "v_cvt_pk_bf16_f32 v200, v120, v121\n"
        "v_cvt_pk_bf16_f32 v201, v122, v123\n"
        "v_mfma_f32_16x16x32_bf16 v[112:115], a[84:87], a[224:227], v[112:115]\n"
        "v_cvt_pk_bf16_f32 v206, v120, v121\n"
        "v_cvt_pk_bf16_f32 v207, v122, v123\n"
        "v_mfma_f32_16x16x32_bf16 v[112:115], a[80:83], a[220:223], v[112:115]\n"
        "buffer_load_dwordx4 v242, s[8:11], s35 offen lds\n"
        "v_mfma_f32_16x16x32_bf16 v[112:115], a[76:79], a[216:219], v[112:115]\n"
        "v_mfma_f32_16x16x32_bf16 v[112:115], a[72:75], a[248:251], v[112:115]\n"
        "s_nop 2\n"
        "v_mfma_f32_16x16x32_bf16 v[96:99], v[152:155], v[196:199], v[32:35]\n"
        "s_waitcnt lgkmcnt(1)\n"
        "s_nop 1\n"
        "v_mfma_f32_16x16x32_bf16 v[96:99], v[168:171], v[200:203], v[96:99]\n"
        "s_nop 0\n"
        "v_cvt_pk_bf16_f32 v192, v112, v113\n"
        "v_cvt_pk_bf16_f32 v193, v114, v115\n"
        "v_mfma_f32_16x16x32_bf16 v[100:103], v[172:175], v[196:199], v[36:39]\n"
        "v_mfma_f32_16x16x32_bf16 v[100:103], v[188:191], v[204:207], v[100:103]\n"
        "ds_read_b128 v[202:205], v252 offset:4096\n"
        "s_cmp_eq_u32 s37, 100663296\n"
        "v_mfma_f32_16x16x32_bf16 v[116:119], a[96:99], a[248:251], 0\n"
        "s_cselect_b32 s46, 0, s37\n"
        "v_exp_f32 v96, v96\n"
        "v_mfma_f32_16x16x32_bf16 v[116:119], a[100:103], a[216:219], v[116:119]\n"
        "s_add_u32 s38, s46, s43\n"
        "v_exp_f32 v97, v97\n"
        "v_mfma_f32_16x16x32_bf16 v[116:119], a[104:107], a[220:223], v[116:119]\n"
        "s_add_u32 s39, s46, s44\n"
        "v_exp_f32 v98, v98\n"
        "v_mfma_f32_16x16x32_bf16 v[116:119], a[108:111], a[224:227], v[116:119]\n"
        "s_add_u32 s37, s37, 6291456\n"
        "v_exp_f32 v100, v100\n"
        "v_mfma_f32_16x16x32_bf16 v[116:119], a[112:115], a[228:231], v[116:119]\n"
        "v_exp_f32 v99, v99\n"
        "v_mfma_f32_16x16x32_bf16 v[116:119], a[116:119], a[232:235], v[116:119]\n"
        "s_add_u32 m0, s41, 13312\n"
        "v_exp_f32 v101, v101\n"
        "v_mfma_f32_16x16x32_bf16 v[120:123], a[140:143], a[232:235], 0\n"
        "v_exp_f32 v102, v102\n"
        "v_mfma_f32_16x16x32_bf16 v[120:123], a[136:139], a[228:231], v[120:123]\n"
        "buffer_load_dwordx4 v243, s[8:11], s35 offen lds\n"
        "v_mfma_f32_16x16x32_bf16 v[120:123], a[132:135], a[224:227], v[120:123]\n"
        "v_cvt_pk_bf16_f32 v80, v96, v97\n"
        "v_cvt_pk_bf16_f32 v194, v116, v117\n"
        "v_mfma_f32_16x16x32_bf16 v[120:123], a[128:131], a[220:223], v[120:123]\n"
        "v_cvt_pk_bf16_f32 v195, v118, v119\n"
        "v_cvt_pk_bf16_f32 v81, v98, v99\n"
        "v_mfma_f32_16x16x32_bf16 v[120:123], a[124:127], a[216:219], v[120:123]\n"
        "v_cvt_pk_bf16_f32 v82, v100, v101\n"
        "v_mfma_f32_16x16x32_bf16 v[120:123], a[120:123], a[248:251], v[120:123]\n"
        ".long 0xbec200ff\n"
        "45:\n"
        "s_branch 61b\n"
        "v_exp_f32 v103, v103\n"
        "v_mfma_f32_16x16x32_bf16 v[112:115], a[248:251], a[144:147], 0\n"
        "v_cvt_pk_bf16_f32 v83, v102, v103\n"
        "s_nop 0\n"
        "v_mfma_f32_16x16x32_bf16 v[112:115], a[216:219], a[148:151], v[112:115]\n"
        "s_nop 2\n"
        "v_mfma_f32_16x16x32_bf16 v[112:115], a[220:223], a[152:155], v[112:115]\n"
        "ds_read_b128 a[236:239], v240 offset:13824\n"
        "v_cvt_pk_bf16_f32 v208, v120, v121\n"
        "v_mfma_f32_16x16x32_bf16 v[112:115], a[224:227], a[156:159], v[112:115]\n"
        "ds_read_b128 a[240:243], v241 offset:13824\n"
        "v_cvt_pk_bf16_f32 v209, v122, v123\n"
        "v_mfma_f32_16x16x32_bf16 v[112:115], a[228:231], a[160:163], v[112:115]\n"
        "ds_read_b128 a[244:247], v240 offset:13952\n"
        "v_mfma_f32_16x16x32_bf16 v[112:115], a[232:235], a[164:167], v[112:115]\n"
        "s_nop 2\n"
        "v_mfma_f32_16x16x32_bf16 v[104:107], v[192:195], v[156:159], v[8:11]\n"
        "s_nop 1\n"
        "v_mfma_f32_16x16x32_bf16 v[104:107], v[208:211], v[160:163], v[104:107]\n"
        "s_add_u32 m0, s41, 14336\n"
        "v_cvt_pk_bf16_f32 v132, v112, v113\n"
        "v_cvt_pk_bf16_f32 v133, v114, v115\n"
        "v_mfma_f32_16x16x32_bf16 v[116:119], a[232:235], a[188:191], 0\n"
        "v_mfma_f32_16x16x32_bf16 v[116:119], a[228:231], a[184:187], v[116:119]\n"
        "buffer_load_dwordx4 v244, s[8:11], s35 offen lds\n"
        "v_mfma_f32_16x16x32_bf16 v[116:119], a[224:227], a[180:183], v[116:119]\n"
        "v_mfma_f32_16x16x32_bf16 v[116:119], a[220:223], a[176:179], v[116:119]\n"
        "v_exp_f32 v104, v104\n"
        "v_mfma_f32_16x16x32_bf16 v[116:119], a[216:219], a[172:175], v[116:119]\n"
        "v_exp_f32 v105, v105\n"
        "v_mfma_f32_16x16x32_bf16 v[116:119], a[248:251], a[168:171], v[116:119]\n"
        "v_exp_f32 v106, v106\n"
        "v_mfma_f32_16x16x32_bf16 v[108:111], v[192:195], v[176:179], v[24:27]\n"
        "s_waitcnt lgkmcnt(4)\n"
        "v_exp_f32 v107, v107\n"
        "v_mfma_f32_16x16x32_bf16 v[108:111], v[208:211], v[180:183], v[108:111]\n"
        "v_cvt_pk_bf16_f32 v68, v104, v105\n"
        "v_cvt_pk_bf16_f32 v69, v106, v107\n"
        "v_mfma_f32_16x16x32_bf16 v[120:123], a[248:251], a[192:195], 0\n"
        "ds_read_b128 a[248:251], v241 offset:13952\n"
        "v_cvt_pk_bf16_f32 v140, v116, v117\n"
        "v_mfma_f32_16x16x32_bf16 v[120:123], a[216:219], a[196:199], v[120:123]\n"
        "ds_read_b128 a[216:219], v240 offset:14080\n"
        "v_cvt_pk_bf16_f32 v141, v118, v119\n"
        "v_mfma_f32_16x16x32_bf16 v[120:123], a[220:223], a[200:203], v[120:123]\n"
        "ds_read_b128 a[220:223], v241 offset:14080\n"
        "v_mfma_f32_16x16x32_bf16 v[120:123], a[224:227], a[204:207], v[120:123]\n"
        "v_exp_f32 v108, v108\n"
        "v_mfma_f32_16x16x32_bf16 v[120:123], a[228:231], a[208:211], v[120:123]\n"
        "v_exp_f32 v109, v109\n"
        "v_mfma_f32_16x16x32_bf16 v[120:123], a[232:235], a[212:215], v[120:123]\n"
        "v_exp_f32 v110, v110\n"
        "v_mfma_f32_16x16x32_bf16 v[96:99], v[192:195], v[196:199], v[40:43]\n"
        "s_waitcnt lgkmcnt(6)\n"
        "v_exp_f32 v111, v111\n"
        "v_mfma_f32_16x16x32_bf16 v[96:99], v[208:211], v[200:203], v[96:99]\n"
        "s_waitcnt lgkmcnt(5)\n"
        "v_cvt_pk_bf16_f32 v76, v108, v109\n"
        "v_cvt_pk_bf16_f32 v77, v110, v111\n"
        "v_mfma_f32_16x16x32_bf16 v[112:115], a[72:75], a[236:239], 0\n"
        "s_waitcnt lgkmcnt(4)\n"
        "s_nop 0\n"
        "v_cvt_pk_bf16_f32 v148, v120, v121\n"
        "v_mfma_f32_16x16x32_bf16 v[112:115], a[76:79], a[240:243], v[112:115]\n"
        "s_waitcnt lgkmcnt(3)\n"
        "v_cvt_pk_bf16_f32 v149, v122, v123\n"
        "v_mfma_f32_16x16x32_bf16 v[112:115], a[80:83], a[244:247], v[112:115]\n"
        "s_waitcnt lgkmcnt(2)\n"
        "v_exp_f32 v96, v96\n"
        "v_mfma_f32_16x16x32_bf16 v[112:115], a[84:87], a[248:251], v[112:115]\n"
        "s_waitcnt lgkmcnt(1)\n"
        "v_exp_f32 v97, v97\n"
        "v_mfma_f32_16x16x32_bf16 v[112:115], a[88:91], a[216:219], v[112:115]\n"
        "s_waitcnt lgkmcnt(0)\n"
        "v_exp_f32 v98, v98\n"
        "v_mfma_f32_16x16x32_bf16 v[112:115], a[92:95], a[220:223], v[112:115]\n"
        "v_exp_f32 v99, v99\n"
        "v_mfma_f32_16x16x32_bf16 v[116:119], a[116:119], a[220:223], 0\n"
        "v_cvt_pk_bf16_f32 v84, v96, v97\n"
        "v_cvt_pk_bf16_f32 v85, v98, v99\n"
        "v_mfma_f32_16x16x32_bf16 v[116:119], a[112:115], a[216:219], v[116:119]\n"
        "v_add_u32 v240, s31, v240\n"
        "v_add_u32 v241, s31, v241\n"
        "v_mfma_f32_16x16x32_bf16 v[116:119], a[108:111], a[248:251], v[116:119]\n"
        "v_cvt_pk_bf16_f32 v212, v112, v113\n"
        "v_cvt_pk_bf16_f32 v213, v114, v115\n"
        "v_mfma_f32_16x16x32_bf16 v[116:119], a[104:107], a[244:247], v[116:119]\n"
        "v_mfma_f32_16x16x32_bf16 v[116:119], a[100:103], a[240:243], v[116:119]\n"
        "v_mfma_f32_16x16x32_bf16 v[116:119], a[96:99], a[236:239], v[116:119]\n"
        "s_nop 2\n"
        "v_mfma_f32_16x16x32_bf16 v[120:123], a[120:123], a[236:239], 0\n"
        "s_nop 2\n"
        "v_mfma_f32_16x16x32_bf16 v[120:123], a[124:127], a[240:243], v[120:123]\n"
        "s_add_u32 s47, s47, 6291456\n"
        "v_cvt_pk_bf16_f32 v214, v116, v117\n"
        "v_cvt_pk_bf16_f32 v215, v118, v119\n"
        "v_mfma_f32_16x16x32_bf16 v[120:123], a[128:131], a[244:247], v[120:123]\n"
        "s_add_u32 s36, s36, 2097152\n"
        "v_mfma_f32_16x16x32_bf16 v[120:123], a[132:135], a[248:251], v[120:123]\n"
        "s_xor_b32 s48, s48, 8192\n"
        "v_mfma_f32_16x16x32_bf16 v[120:123], a[136:139], a[216:219], v[120:123]\n"
        "v_mfma_f32_16x16x32_bf16 v[120:123], a[140:143], a[220:223], v[120:123]\n"
        "s_nop 2\n"
        "v_mfma_f32_16x16x32_bf16 v[112:115], a[220:223], a[164:167], 0\n"
        "s_nop 2\n"
        "v_mfma_f32_16x16x32_bf16 v[112:115], a[216:219], a[160:163], v[112:115]\n"
        "v_cvt_pk_bf16_f32 v230, v120, v121\n"
        "v_cvt_pk_bf16_f32 v231, v122, v123\n"
        "v_mfma_f32_16x16x32_bf16 v[112:115], a[248:251], a[156:159], v[112:115]\n"
        "s_barrier\n"
        "v_mfma_f32_16x16x32_bf16 v[112:115], a[244:247], a[152:155], v[112:115]\n"
        "v_mfma_f32_16x16x32_bf16 v[112:115], a[240:243], a[148:151], v[112:115]\n"
        "v_mfma_f32_16x16x32_bf16 v[112:115], a[236:239], a[144:147], v[112:115]\n"
        "s_nop 2\n"
        "v_mfma_f32_16x16x32_bf16 v[100:103], v[212:215], v[156:159], v[12:15]\n"
        "s_nop 2\n"
        "v_mfma_f32_16x16x32_bf16 v[100:103], v[228:231], v[164:167], v[100:103]\n"
        "v_cvt_pk_bf16_f32 v134, v112, v113\n"
        "v_cvt_pk_bf16_f32 v135, v114, v115\n"
        "v_mfma_f32_16x16x32_bf16 v[116:119], a[236:239], a[168:171], 0\n"
        "v_mfma_f32_16x16x32_bf16 v[116:119], a[240:243], a[172:175], v[116:119]\n"
        "v_mfma_f32_16x16x32_bf16 v[116:119], a[244:247], a[176:179], v[116:119]\n"
        "v_mfma_f32_16x16x32_bf16 v[116:119], a[248:251], a[180:183], v[116:119]\n"
        "v_mfma_f32_16x16x32_bf16 v[116:119], a[216:219], a[184:187], v[116:119]\n"
        "s_nop 0\n"
        "v_exp_f32 v100, v100\n"
        "v_mfma_f32_16x16x32_bf16 v[116:119], a[220:223], a[188:191], v[116:119]\n"
        "v_exp_f32 v101, v101\n"
        "v_mfma_f32_16x16x32_bf16 v[104:107], v[212:215], v[176:179], v[28:31]\n"
        "v_exp_f32 v102, v102\n"
        "v_mfma_f32_16x16x32_bf16 v[104:107], v[228:231], v[184:187], v[104:107]\n"
        "v_exp_f32 v103, v103\n"
        "v_mfma_f32_16x16x32_bf16 v[120:123], a[220:223], a[212:215], 0\n"
        "v_cvt_pk_bf16_f32 v70, v100, v101\n"
        "v_cvt_pk_bf16_f32 v71, v102, v103\n"
        "v_mfma_f32_16x16x32_bf16 v[120:123], a[216:219], a[208:211], v[120:123]\n"
        "v_cvt_pk_bf16_f32 v142, v116, v117\n"
        "v_cvt_pk_bf16_f32 v143, v118, v119\n"
        "v_mfma_f32_16x16x32_bf16 v[120:123], a[248:251], a[204:207], v[120:123]\n"
        "v_mov_b64 v[96:97], 0\n"
        "v_mfma_f32_16x16x32_bf16 v[120:123], a[244:247], a[200:203], v[120:123]\n"
        "v_exp_f32 v104, v104\n"
        "v_mfma_f32_16x16x32_bf16 v[120:123], a[240:243], a[196:199], v[120:123]\n"
        "v_exp_f32 v105, v105\n"
        "v_mfma_f32_16x16x32_bf16 v[120:123], a[236:239], a[192:195], v[120:123]\n"
        "v_exp_f32 v106, v106\n"
        "v_mfma_f32_16x16x32_bf16 v[108:111], v[212:215], v[196:199], v[44:47]\n"
        "v_exp_f32 v107, v107\n"
        "v_mfma_f32_16x16x32_bf16 v[108:111], v[228:231], v[204:207], v[108:111]\n"
        "v_cvt_pk_bf16_f32 v78, v104, v105\n"
        "v_cvt_pk_bf16_f32 v79, v106, v107\n"
        "v_mfma_f32_16x16x32_bf16 v[112:115], a[0:3], a[236:239], 0\n"
        "s_nop 0\n"
        "v_cvt_pk_bf16_f32 v150, v120, v121\n"
        "v_cvt_pk_bf16_f32 v151, v122, v123\n"
        "v_mfma_f32_16x16x32_bf16 v[112:115], a[4:7], a[240:243], v[112:115]\n"
        "v_mfma_f32_16x16x32_bf16 v[112:115], a[8:11], a[244:247], v[112:115]\n"
        "v_exp_f32 v108, v108\n"
        "v_mfma_f32_16x16x32_bf16 v[112:115], a[12:15], a[248:251], v[112:115]\n"
        "v_exp_f32 v109, v109\n"
        "v_mfma_f32_16x16x32_bf16 v[112:115], a[16:19], a[216:219], v[112:115]\n"
        "v_exp_f32 v110, v110\n"
        "v_mfma_f32_16x16x32_bf16 v[112:115], a[20:23], a[220:223], v[112:115]\n"
        "v_exp_f32 v111, v111\n"
        "v_smfmac_f32_16x16x64_bf16 v[96:99], a[252:255], v[64:71], v237\n"
        "v_cvt_pk_bf16_f32 v86, v108, v109\n"
        "v_mfma_f32_16x16x32_bf16 v[100:103], v[132:135], v[68:71], 0\n"
        "v_mov_b64 v[124:125], 0\n"
        "v_cvt_pk_bf16_f32 v87, v110, v111\n"
        "v_mfma_f32_16x16x32_bf16 v[100:103], v[128:131], v[64:67], v[100:103]\n"
        "s_nop 0\n"
        "v_cvt_pk_bf16_f32 v216, v112, v113\n"
        "v_cvt_pk_bf16_f32 v217, v114, v115\n"
        "v_mfma_f32_16x16x32_bf16 v[104:107], v[136:139], v[64:67], 0\n"
        "ds_read_b128 v[222:225], v251 offset:4608\n"
        "v_add_f32 v97, v96, v97\n"
        "v_mfma_f32_16x16x32_bf16 v[104:107], v[140:143], v[68:71], v[104:107]\n"
        "v_rcp_f32 v96, v97\n"
        "v_mfma_f32_16x16x32_bf16 v[108:111], v[148:151], v[68:71], 0\n"
        "v_mul_f32 v100, v96, v100\n"
        "v_mul_f32 v101, v96, v101\n"
        "v_mfma_f32_16x16x32_bf16 v[108:111], v[144:147], v[64:67], v[108:111]\n"
        "v_mul_f32 v102, v96, v102\n"
        "v_mul_f32 v103, v96, v103\n"
        "v_mfma_f32_16x16x32_bf16 v[116:119], a[44:47], a[220:223], 0\n"
        "v_cvt_pk_bf16_f32 v100, v100, v101\n"
        "v_cvt_pk_bf16_f32 v101, v102, v103\n"
        "v_mfma_f32_16x16x32_bf16 v[116:119], a[40:43], a[216:219], v[116:119]\n"
        "v_mul_f32 v104, v96, v104\n"
        "v_mul_f32 v105, v96, v105\n"
        "v_mfma_f32_16x16x32_bf16 v[116:119], a[36:39], a[248:251], v[116:119]\n"
        "v_mul_f32 v106, v96, v106\n"
        "v_mul_f32 v107, v96, v107\n"
        "v_mfma_f32_16x16x32_bf16 v[116:119], a[32:35], a[244:247], v[116:119]\n"
        "v_cvt_pk_bf16_f32 v102, v104, v105\n"
        "v_cvt_pk_bf16_f32 v103, v106, v107\n"
        "v_mfma_f32_16x16x32_bf16 v[116:119], a[28:31], a[240:243], v[116:119]\n"
        "ds_write_b128 v245, v[100:103] offset:0\n"
        "v_mfma_f32_16x16x32_bf16 v[116:119], a[24:27], a[236:239], v[116:119]\n"
        "v_mul_f32 v108, v96, v108\n"
        "v_mul_f32 v109, v96, v109\n"
        "v_smfmac_f32_16x16x64_bf16 v[124:127], a[252:255], v[72:79], v237\n"
        "v_mul_f32 v110, v96, v110\n"
        "v_mfma_f32_16x16x32_bf16 v[64:67], v[132:135], v[76:79], 0\n"
        "v_mul_f32 v111, v96, v111\n"
        "v_cvt_pk_bf16_f32 v108, v108, v109\n"
        "v_mfma_f32_16x16x32_bf16 v[64:67], v[128:131], v[72:75], v[64:67]\n"
        "ds_read_b128 v[156:159], v247 offset:0\n"
        "v_cvt_pk_bf16_f32 v218, v116, v117\n"
        "v_mfma_f32_16x16x32_bf16 v[68:71], v[136:139], v[72:75], 0\n"
        "ds_read_b128 v[160:163], v247 offset:4096\n"
        "v_cvt_pk_bf16_f32 v219, v118, v119\n"
        "v_mfma_f32_16x16x32_bf16 v[68:71], v[140:143], v[76:79], v[68:71]\n"
        "ds_read_b128 v[164:167], v247 offset:4352\n"
        "v_cvt_pk_bf16_f32 v109, v110, v111\n"
        "v_mfma_f32_16x16x32_bf16 v[120:123], v[148:151], v[76:79], 0\n"
        "ds_write_b64 v246, v[108:109] offset:0\n"
        "v_mfma_f32_16x16x32_bf16 v[120:123], v[144:147], v[72:75], v[120:123]\n"
        "v_max_u32 v236, v96, v97\n"
        "v_mov_b64 v[116:117], 0\n"
        "v_mfma_f32_16x16x32_bf16 v[112:115], a[48:51], a[236:239], 0\n"
        "v_add_f32 v125, v124, v125\n"
        "v_mfma_f32_16x16x32_bf16 v[112:115], a[52:55], a[240:243], v[112:115]\n"
        "v_rcp_f32 v124, v125\n"
        "v_mfma_f32_16x16x32_bf16 v[112:115], a[56:59], a[244:247], v[112:115]\n"
        "v_mul_f32 v64, v124, v64\n"
        "v_mul_f32 v65, v124, v65\n"
        "v_mfma_f32_16x16x32_bf16 v[112:115], a[60:63], a[248:251], v[112:115]\n"
        "v_mul_f32 v66, v124, v66\n"
        "v_mul_f32 v67, v124, v67\n"
        "v_mfma_f32_16x16x32_bf16 v[112:115], a[64:67], a[216:219], v[112:115]\n"
        "v_cvt_pk_bf16_f32 v64, v64, v65\n"
        "v_cvt_pk_bf16_f32 v65, v66, v67\n"
        "v_mfma_f32_16x16x32_bf16 v[112:115], a[68:71], a[220:223], v[112:115]\n"
        "ds_read_b128 a[216:219], v248 offset:0\n"
        "v_mul_f32 v68, v124, v68\n"
        "v_smfmac_f32_16x16x64_bf16 v[116:119], a[252:255], v[80:87], v237\n"
        "ds_read_b128 a[220:223], v248 offset:3072\n"
        "v_mfma_f32_16x16x32_bf16 v[72:75], v[132:135], v[84:87], 0\n"
        "v_mul_f32 v69, v124, v69\n"
        "v_mul_f32 v70, v124, v70\n"
        "v_mfma_f32_16x16x32_bf16 v[72:75], v[128:131], v[80:83], v[72:75]\n"
        "v_cvt_pk_bf16_f32 v220, v112, v113\n"
        "v_cvt_pk_bf16_f32 v221, v114, v115\n"
        "v_mfma_f32_16x16x32_bf16 v[76:79], v[136:139], v[80:83], 0\n"
        "ds_read_b128 a[224:227], v254 offset:16\n"
        "v_cvt_pk_bf16_f32 v226, v112, v113\n"
        "v_mfma_f32_16x16x32_bf16 v[76:79], v[140:143], v[84:87], v[76:79]\n"
        "ds_read_b128 a[228:231], v254 offset:3088\n"
        "v_cvt_pk_bf16_f32 v227, v114, v115\n"
        "v_mfma_f32_16x16x32_bf16 v[96:99], v[148:151], v[84:87], 0\n"
        "v_mul_f32 v71, v124, v71\n"
        "v_cvt_pk_bf16_f32 v66, v68, v69\n"
        "v_mfma_f32_16x16x32_bf16 v[96:99], v[144:147], v[80:83], v[96:99]\n"
        "v_cvt_pk_bf16_f32 v67, v70, v71\n"
        "v_mul_f32 v120, v124, v120\n"
        "v_mfma_f32_16x16x32_bf16 v[100:103], v[152:155], v[216:219], v[48:51]\n"
        "s_waitcnt lgkmcnt(9)\n"
        "ds_read_b128 v[152:155], v247 offset:256\n"
        "v_mul_f32 v121, v124, v121\n"
        "v_mfma_f32_16x16x32_bf16 v[100:103], v[168:171], v[220:223], v[100:103]\n"
        "ds_write_b128 v245, v[64:67] offset:256\n"
        "v_mfma_f32_16x16x32_bf16 v[104:107], v[172:175], v[216:219], v[52:55]\n"
        "v_mul_f32 v122, v124, v122\n"
        "v_mul_f32 v123, v124, v123\n"
        "v_mfma_f32_16x16x32_bf16 v[104:107], v[188:191], v[224:227], v[104:107]\n"
        "s_waitcnt lgkmcnt(5)\n"
        "v_cvt_pk_bf16_f32 v120, v120, v121\n"
        "v_cvt_pk_bf16_f32 v121, v122, v123\n"
        "v_mfma_f32_16x16x32_bf16 v[176:179], v[156:159], a[216:219], 0\n"
        "s_waitcnt lgkmcnt(4)\n"
        "ds_write_b64 v246, v[120:121] offset:256\n"
        "v_mfma_f32_16x16x32_bf16 v[176:179], v[160:163], a[220:223], v[176:179]\n"
        "ds_read_b128 v[222:225], v252 offset:4608\n"
        "v_max3_u32 v236, v236, v124, v125\n"
        "v_mfma_f32_16x16x32_bf16 v[196:199], v[164:167], a[220:223], 0\n"
        "s_waitcnt lgkmcnt(3)\n"
        "ds_read_b128 a[232:235], v248 offset:32\n"
        "v_add_f32 v117, v116, v117\n"
        "v_mfma_f32_16x16x32_bf16 v[196:199], v[152:155], a[216:219], v[196:199]\n"
        "ds_read_b128 a[220:223], v248 offset:3104\n"
        "v_mfma_f32_16x16x32_bf16 v[200:203], v[152:155], a[224:227], 0\n"
        "ds_read_b128 a[216:219], v254 offset:48\n"
        "v_mfma_f32_16x16x32_bf16 v[200:203], v[164:167], a[228:231], v[200:203]\n"
        "v_rcp_f32 v116, v117\n"
        "v_mfma_f32_16x16x32_bf16 v[180:183], v[160:163], a[228:231], 0\n"
        "ds_read_b128 a[228:231], v254 offset:3120\n"
        "v_mul_f32 v72, v116, v72\n"
        "v_mfma_f32_16x16x32_bf16 v[180:183], v[156:159], a[224:227], v[180:183]\n"
        "ds_read_b128 a[236:239], v247 offset:8192\n"
        "v_mul_f32 v73, v116, v73\n"
        "v_mfma_f32_16x16x32_bf16 v[108:111], v[192:195], v[216:219], v[56:59]\n"
        "s_waitcnt lgkmcnt(5)\n"
        "ds_read_b128 a[224:227], v248 offset:64\n"
        "v_mul_f32 v74, v116, v74\n"
        "v_mfma_f32_16x16x32_bf16 v[108:111], v[208:211], v[220:223], v[108:111]\n"
        "ds_read_b128 a[240:243], v247 offset:12288\n"
        "v_mul_f32 v75, v116, v75\n"
        "v_mfma_f32_16x16x32_bf16 v[112:115], v[212:215], v[216:219], v[60:63]\n"
        "ds_read_b128 a[244:247], v247 offset:12544\n"
        "v_cvt_pk_bf16_f32 v72, v72, v73\n"
        "v_mfma_f32_16x16x32_bf16 v[112:115], v[228:231], v[224:227], v[112:115]\n"
        "s_waitcnt lgkmcnt(7)\n"
        "ds_read_b128 a[248:251], v247 offset:8448\n"
        "v_cvt_pk_bf16_f32 v73, v74, v75\n"
        "v_mfma_f32_16x16x32_bf16 v[184:187], v[156:159], a[232:235], 0\n"
        "s_waitcnt lgkmcnt(7)\n"
        "v_exp_f32 v100, v100\n"
        "v_mfma_f32_16x16x32_bf16 v[184:187], v[160:163], a[220:223], v[184:187]\n"
        "v_exp_f32 v101, v101\n"
        "v_mfma_f32_16x16x32_bf16 v[204:207], v[164:167], a[220:223], 0\n"
        "ds_read_b128 a[220:223], v248 offset:3136\n"
        "v_mul_f32 v76, v116, v76\n"
        "v_mfma_f32_16x16x32_bf16 v[204:207], v[152:155], a[232:235], v[204:207]\n"
        "s_waitcnt lgkmcnt(7)\n"
        "ds_read_b128 a[232:235], v254 offset:80\n"
        "v_mul_f32 v77, v116, v77\n"
        "v_mfma_f32_16x16x32_bf16 v[192:195], v[152:155], a[216:219], 0\n"
        "s_waitcnt lgkmcnt(7)\n"
        "v_mul_f32 v78, v116, v78\n"
        "v_mul_f32 v79, v116, v79\n"
        "v_mfma_f32_16x16x32_bf16 v[192:195], v[164:167], a[228:231], v[192:195]\n"
        "v_exp_f32 v102, v102\n"
        "v_mfma_f32_16x16x32_bf16 v[172:175], v[160:163], a[228:231], 0\n"
        "ds_read_b128 a[228:231], v254 offset:3152\n"
        "v_cvt_pk_bf16_f32 v88, v100, v101\n"
        "v_mfma_f32_16x16x32_bf16 v[172:175], v[156:159], a[216:219], v[172:175]\n"
        "s_waitcnt lgkmcnt(6)\n"
        "ds_read_b128 a[216:219], v248 offset:6144\n"
        "v_cvt_pk_bf16_f32 v74, v76, v77\n"
        "v_mfma_f32_16x16x32_bf16 v[212:215], v[156:159], a[224:227], 0\n"
        "s_waitcnt lgkmcnt(3)\n"
        "v_exp_f32 v104, v104\n"
        "v_mfma_f32_16x16x32_bf16 v[212:215], v[160:163], a[220:223], v[212:215]\n"
        "v_exp_f32 v103, v103\n"
        "v_mfma_f32_16x16x32_bf16 v[216:219], v[164:167], a[220:223], 0\n"
        "ds_read_b128 a[220:223], v248 offset:9216\n"
        "v_cvt_pk_bf16_f32 v75, v78, v79\n"
        "v_mfma_f32_16x16x32_bf16 v[216:219], v[152:155], a[224:227], v[216:219]\n"
        "s_waitcnt lgkmcnt(3)\n"
        "ds_write_b128 v245, v[72:75] offset:512\n"
        "v_mfma_f32_16x16x32_bf16 v[224:227], v[152:155], a[232:235], 0\n"
        "s_waitcnt lgkmcnt(3)\n"
        "v_exp_f32 v105, v105\n"
        "v_mfma_f32_16x16x32_bf16 v[224:227], v[164:167], a[228:231], v[224:227]\n"
        "v_cvt_pk_bf16_f32 v89, v102, v103\n"
        "v_cvt_pk_bf16_f32 v90, v104, v105\n"
        "v_mfma_f32_16x16x32_bf16 v[220:223], v[160:163], a[228:231], 0\n"
        "ds_read_b128 a[224:227], v254 offset:6160\n"
        "ds_read_b128 a[228:231], v254 offset:9232\n"
        "v_mfma_f32_16x16x32_bf16 v[220:223], v[156:159], a[232:235], v[220:223]\n"
        "s_waitcnt lgkmcnt(4)\n"
        "ds_read_b128 a[232:235], v248 offset:6176\n"
        "v_mul_f32 v96, v116, v96\n"
        "v_mfma_f32_16x16x32_bf16 v[176:179], a[236:239], a[216:219], v[176:179]\n"
        "s_waitcnt lgkmcnt(4)\n"
        "v_exp_f32 v106, v106\n"
        "v_mfma_f32_16x16x32_bf16 v[176:179], a[240:243], a[220:223], v[176:179]\n"
        "v_mul_f32 v97, v116, v97\n"
        "v_mul_f32 v98, v116, v98\n"
        "v_mfma_f32_16x16x32_bf16 v[196:199], a[244:247], a[220:223], v[196:199]\n"
        "ds_read_b128 a[220:223], v248 offset:9248\n"
        "v_mul_f32 v99, v116, v99\n"
        "v_mfma_f32_16x16x32_bf16 v[196:199], a[248:251], a[216:219], v[196:199]\n"
        "s_waitcnt lgkmcnt(3)\n"
        "ds_read_b128 a[216:219], v254 offset:6192\n"
        "v_cvt_pk_bf16_f32 v96, v96, v97\n"
        "v_mfma_f32_16x16x32_bf16 v[200:203], a[248:251], a[224:227], v[200:203]\n"
        "s_waitcnt lgkmcnt(3)\n"
        "ds_read_b128 v[156:159], v247 offset:16384\n"
        "v_cvt_pk_bf16_f32 v97, v98, v99\n"
        "v_mfma_f32_16x16x32_bf16 v[200:203], a[244:247], a[228:231], v[200:203]\n"
        "ds_write_b64 v246, v[96:97] offset:512\n"
        "v_mfma_f32_16x16x32_bf16 v[180:183], a[240:243], a[228:231], v[180:183]\n"
        "ds_read_b128 a[228:231], v254 offset:9264\n"
        "v_max3_u32 v236, v236, v116, v117\n"
        "v_mfma_f32_16x16x32_bf16 v[180:183], a[236:239], a[224:227], v[180:183]\n"
        "s_waitcnt lgkmcnt(5)\n"
        "ds_read_b128 a[224:227], v248 offset:6208\n"
        "v_mfma_f32_16x16x32_bf16 v[184:187], a[236:239], a[232:235], v[184:187]\n"
        "s_waitcnt lgkmcnt(5)\n"
        "ds_read_b128 v[160:163], v247 offset:20480\n"
        "v_mfma_f32_16x16x32_bf16 v[184:187], a[240:243], a[220:223], v[184:187]\n"
        "ds_read_b128 v[164:167], v247 offset:20736\n"
        "v_mfma_f32_16x16x32_bf16 v[204:207], a[244:247], a[220:223], v[204:207]\n"
        "ds_read_b128 a[220:223], v248 offset:9280\n"
        "v_mfma_f32_16x16x32_bf16 v[204:207], a[248:251], a[232:235], v[204:207]\n"
        "s_waitcnt lgkmcnt(7)\n"
        "ds_read_b128 a[232:235], v254 offset:6224\n"
        "v_mfma_f32_16x16x32_bf16 v[192:195], a[248:251], a[216:219], v[192:195]\n"
        "s_waitcnt lgkmcnt(5)\n"
        "ds_read_b128 v[152:155], v247 offset:16640\n"
        "v_mfma_f32_16x16x32_bf16 v[192:195], a[244:247], a[228:231], v[192:195]\n"
        "v_exp_f32 v107, v107\n"
        "v_mfma_f32_16x16x32_bf16 v[172:175], a[240:243], a[228:231], v[172:175]\n"
        "ds_read_b128 a[228:231], v254 offset:9296\n"
        "v_cvt_pk_bf16_f32 v91, v106, v107\n"
        "v_mfma_f32_16x16x32_bf16 v[172:175], a[236:239], a[216:219], v[172:175]\n"
        "s_waitcnt lgkmcnt(6)\n"
        "ds_read_b128 a[216:219], v248 offset:12288\n"
        "v_mfma_f32_16x16x32_bf16 v[212:215], a[236:239], a[224:227], v[212:215]\n"
        "s_waitcnt lgkmcnt(4)\n"
        "v_exp_f32 v108, v108\n"
        "v_mfma_f32_16x16x32_bf16 v[212:215], a[240:243], a[220:223], v[212:215]\n"
        "v_exp_f32 v109, v109\n"
        "v_mfma_f32_16x16x32_bf16 v[216:219], a[244:247], a[220:223], v[216:219]\n"
        "ds_read_b128 a[220:223], v248 offset:15360\n"
        "v_cvt_pk_bf16_f32 v92, v108, v109\n"
        "v_mfma_f32_16x16x32_bf16 v[216:219], a[248:251], a[224:227], v[216:219]\n"
        "s_waitcnt lgkmcnt(4)\n"
        "ds_read_b128 a[224:227], v254 offset:12304\n"
        "v_mfma_f32_16x16x32_bf16 v[224:227], a[248:251], a[232:235], v[224:227]\n"
        "s_waitcnt lgkmcnt(3)\n"
        "v_exp_f32 v110, v110\n"
        "v_mfma_f32_16x16x32_bf16 v[224:227], a[244:247], a[228:231], v[224:227]\n"
        "v_exp_f32 v112, v112\n"
        "v_mfma_f32_16x16x32_bf16 v[220:223], a[240:243], a[228:231], v[220:223]\n"
        "ds_read_b128 a[228:231], v254 offset:15376\n"
        "v_mfma_f32_16x16x32_bf16 v[220:223], a[236:239], a[232:235], v[220:223]\n"
        "s_waitcnt lgkmcnt(3)\n"
        "ds_read_b128 a[232:235], v248 offset:12320\n"
        "v_mfma_f32_16x16x32_bf16 v[176:179], v[156:159], a[216:219], v[176:179]\n"
        "s_waitcnt lgkmcnt(3)\n"
        "v_exp_f32 v111, v111\n"
        "v_mfma_f32_16x16x32_bf16 v[176:179], v[160:163], a[220:223], v[176:179]\n"
        "v_exp_f32 v113, v113\n"
        "v_mfma_f32_16x16x32_bf16 v[196:199], v[164:167], a[220:223], v[196:199]\n"
        "ds_read_b128 a[220:223], v248 offset:15392\n"
        "v_cvt_pk_bf16_f32 v93, v110, v111\n"
        "v_mfma_f32_16x16x32_bf16 v[196:199], v[152:155], a[216:219], v[196:199]\n"
        "s_waitcnt lgkmcnt(3)\n"
        "ds_read_b128 a[216:219], v254 offset:12336\n"
        "v_cvt_pk_bf16_f32 v94, v112, v113\n"
        "v_mfma_f32_16x16x32_bf16 v[200:203], v[152:155], a[224:227], v[200:203]\n"
        "s_waitcnt lgkmcnt(3)\n"
        "v_exp_f32 v114, v114\n"
        "v_mfma_f32_16x16x32_bf16 v[200:203], v[164:167], a[228:231], v[200:203]\n"
        "v_exp_f32 v115, v115\n"
        "v_mfma_f32_16x16x32_bf16 v[180:183], v[160:163], a[228:231], v[180:183]\n"
        "s_waitcnt vmcnt(0)\n"
        "s_barrier\n"
        "ds_read_b128 a[228:231], v254 offset:15408\n"
        "v_cvt_pk_bf16_f32 v95, v114, v115\n"
        "v_mfma_f32_16x16x32_bf16 v[180:183], v[156:159], a[224:227], v[180:183]\n"
        "s_waitcnt lgkmcnt(3)\n"
        "ds_read_b128 a[224:227], v248 offset:12352\n"
        "v_mfma_f32_16x16x32_bf16 v[184:187], v[156:159], a[232:235], v[184:187]\n"
        "s_waitcnt lgkmcnt(3)\n"
        "v_mfma_f32_16x16x32_bf16 v[184:187], v[160:163], a[220:223], v[184:187]\n"
        "v_mfma_f32_16x16x32_bf16 v[204:207], v[164:167], a[220:223], v[204:207]\n"
        "ds_read_b128 a[220:223], v248 offset:15424\n"
        "v_mfma_f32_16x16x32_bf16 v[204:207], v[152:155], a[232:235], v[204:207]\n"
        "s_waitcnt lgkmcnt(3)\n"
        "ds_read_b128 a[232:235], v254 offset:12368\n"
        "v_mfma_f32_16x16x32_bf16 v[192:195], v[152:155], a[216:219], v[192:195]\n"
        "s_waitcnt lgkmcnt(3)\n"
        "ds_read_b128 a[236:239], v240 offset:0\n"
        "v_mfma_f32_16x16x32_bf16 v[192:195], v[164:167], a[228:231], v[192:195]\n"
        "ds_read_b128 a[240:243], v241 offset:0\n"
        "v_mfma_f32_16x16x32_bf16 v[172:175], v[160:163], a[228:231], v[172:175]\n"
        "ds_read_b128 a[228:231], v254 offset:15440\n"
        "v_mfma_f32_16x16x32_bf16 v[172:175], v[156:159], a[216:219], v[172:175]\n"
        "s_waitcnt lgkmcnt(5)\n"
        "ds_read_b128 a[244:247], v240 offset:128\n"
        "v_mfma_f32_16x16x32_bf16 v[212:215], v[156:159], a[224:227], v[212:215]\n"
        "s_waitcnt lgkmcnt(5)\n"
        "ds_read_b128 a[248:251], v241 offset:128\n"
        "v_mfma_f32_16x16x32_bf16 v[212:215], v[160:163], a[220:223], v[212:215]\n"
        "ds_read_b128 a[216:219], v240 offset:256\n"
        "v_mfma_f32_16x16x32_bf16 v[216:219], v[164:167], a[220:223], v[216:219]\n"
        "ds_read_b128 a[220:223], v241 offset:256\n"
        "v_mfma_f32_16x16x32_bf16 v[216:219], v[152:155], a[224:227], v[216:219]\n"
        "s_waitcnt lgkmcnt(7)\n"
        "v_mfma_f32_16x16x32_bf16 v[224:227], v[152:155], a[232:235], v[224:227]\n"
        "s_waitcnt lgkmcnt(4)\n"
        "v_mfma_f32_16x16x32_bf16 v[224:227], v[164:167], a[228:231], v[224:227]\n"
        "s_sub_u32 s27, s27, 1\n"
        "v_mfma_f32_16x16x32_bf16 v[220:223], v[160:163], a[228:231], v[220:223]\n"
        "s_cmp_lg_u32 s27, 0\n"
        "v_mfma_f32_16x16x32_bf16 v[220:223], v[156:159], a[232:235], v[220:223]\n"
        "s_cbranch_scc1 10b\n"
        "s_branch 19f\n"
        ".p2align 12\n"
        "51:\n"
        "s_branch 61b\n"
        "s_nop 0\n"
        "s_nop 0\n"
        "s_nop 0\n"
        "12:\n"
        "10:\n"
        "v_mfma_f32_16x16x32_bf16 v[112:115], a[72:75], a[236:239], 0\n"
        "v_mov_b64 v[124:125], 0\n"
        "v_mfma_f32_16x16x32_bf16 v[112:115], a[76:79], a[240:243], v[112:115]\n"
        "s_waitcnt lgkmcnt(3)\n"
        "s_add_u32 s41, s30, s40\n"
        "v_cvt_pk_bf16_f32 v232, v176, v180\n"
        "v_cvt_pk_bf16_f32 v233, v184, v172\n"
        "v_mfma_f32_16x16x32_bf16 v[112:115], a[80:83], a[244:247], v[112:115]\n"
        "s_waitcnt lgkmcnt(2)\n"
        "v_cvt_pk_bf16_f32 v234, v212, v220\n"
        "v_mfma_f32_16x16x32_bf16 v[112:115], a[84:87], a[248:251], v[112:115]\n"
        "s_waitcnt lgkmcnt(1)\n"
        "buffer_store_dwordx3 v[232:234], v250, s[16:19], s38 offen offset:0 sc1\n"
        "v_mfma_f32_16x16x32_bf16 v[112:115], a[88:91], a[216:219], v[112:115]\n"
        "s_waitcnt lgkmcnt(0)\n"
        "s_nop 0\n"
        "v_mfma_f32_16x16x32_bf16 v[112:115], a[92:95], a[220:223], v[112:115]\n"
        "v_cvt_pk_bf16_f32 v232, v177, v181\n"
        "v_cvt_pk_bf16_f32 v233, v185, v173\n"
        "v_smfmac_f32_16x16x64_bf16 v[124:127], a[252:255], v[88:95], v237\n"
        "v_cvt_pk_bf16_f32 v234, v213, v221\n"
        "v_mfma_f32_16x16x32_bf16 v[80:83], v[132:135], v[92:95], 0\n"
        "s_nop 2\n"
        "v_mfma_f32_16x16x32_bf16 v[80:83], v[128:131], v[88:91], v[80:83]\n"
        "v_cvt_pk_bf16_f32 v152, v112, v113\n"
        "v_cvt_pk_bf16_f32 v153, v114, v115\n"
        "v_mfma_f32_16x16x32_bf16 v[84:87], v[136:139], v[88:91], 0\n"
        "v_add_f32 v125, v124, v125\n"
        "v_mfma_f32_16x16x32_bf16 v[84:87], v[140:143], v[92:95], v[84:87]\n"
        "v_rcp_f32 v124, v125\n"
        "v_mfma_f32_16x16x32_bf16 v[96:99], v[148:151], v[92:95], 0\n"
        "buffer_store_dwordx3 v[232:234], v250, s[16:19], s38 offen offset:384 sc1\n"
        "v_mfma_f32_16x16x32_bf16 v[96:99], v[144:147], v[88:91], v[96:99]\n"
        "s_add_u32 s35, s47, 786432\n"
        "v_mul_f32 v80, v124, v80\n"
        "v_mul_f32 v81, v124, v81\n"
        "v_mfma_f32_16x16x32_bf16 v[116:119], a[116:119], a[220:223], 0\n"
        "s_sub_u32 s46, s35, 100663296\n"
        "v_mul_f32 v82, v124, v82\n"
        "v_mul_f32 v83, v124, v83\n"
        "v_mfma_f32_16x16x32_bf16 v[116:119], a[112:115], a[216:219], v[116:119]\n"
        "s_cmp_lt_u32 s46, 786432\n"
        "v_cvt_pk_bf16_f32 v80, v80, v81\n"
        "v_cvt_pk_bf16_f32 v81, v82, v83\n"
        "v_mfma_f32_16x16x32_bf16 v[116:119], a[108:111], a[248:251], v[116:119]\n"
        "s_cselect_b32 s35, s46, s35\n"
        "v_mul_f32 v84, v124, v84\n"
        "v_mul_f32 v85, v124, v85\n"
        "v_mfma_f32_16x16x32_bf16 v[116:119], a[104:107], a[244:247], v[116:119]\n"
        "v_mul_f32 v86, v124, v86\n"
        "v_mul_f32 v87, v124, v87\n"
        "v_mfma_f32_16x16x32_bf16 v[116:119], a[100:103], a[240:243], v[116:119]\n"
        "v_cvt_pk_bf16_f32 v232, v178, v182\n"
        "v_cvt_pk_bf16_f32 v233, v186, v174\n"
        "v_mfma_f32_16x16x32_bf16 v[116:119], a[96:99], a[236:239], v[116:119]\n"
        "v_cvt_pk_bf16_f32 v234, v214, v222\n"
        "v_cvt_pk_bf16_f32 v82, v84, v85\n"
        "v_mfma_f32_16x16x32_bf16 v[120:123], a[120:123], a[236:239], 0\n"
        "v_cvt_pk_bf16_f32 v83, v86, v87\n"
        "v_mul_f32 v96, v124, v96\n"
        "v_mfma_f32_16x16x32_bf16 v[120:123], a[124:127], a[240:243], v[120:123]\n"
        "buffer_store_dwordx3 v[232:234], v250, s[16:19], s38 offen offset:768 sc1\n"
        "v_mfma_f32_16x16x32_bf16 v[120:123], a[128:131], a[244:247], v[120:123]\n"
        "v_mul_f32 v97, v124, v97\n"
        "v_mul_f32 v98, v124, v98\n"
        "v_mfma_f32_16x16x32_bf16 v[120:123], a[132:135], a[248:251], v[120:123]\n"
        "v_mul_f32 v99, v124, v99\n"
        "v_cvt_pk_bf16_f32 v154, v116, v117\n"
        "v_mfma_f32_16x16x32_bf16 v[120:123], a[136:139], a[216:219], v[120:123]\n"
        "ds_write_b128 v245, v[80:83] offset:768\n"
        "v_mfma_f32_16x16x32_bf16 v[120:123], a[140:143], a[220:223], v[120:123]\n"
        "v_cvt_pk_bf16_f32 v155, v118, v119\n"
        "v_cvt_pk_bf16_f32 v96, v96, v97\n"
        "v_mfma_f32_16x16x32_bf16 v[112:115], a[20:23], a[220:223], 0\n"
        "v_cvt_pk_bf16_f32 v97, v98, v99\n"
        "v_max3_u32 v236, v236, v124, v125\n"
        "v_mfma_f32_16x16x32_bf16 v[112:115], a[16:19], a[216:219], v[112:115]\n"
        "v_cmp_lt_u32 vcc, s24, v236\n"
        "v_cvt_pk_bf16_f32 v232, v179, v183\n"
        "v_mfma_f32_16x16x32_bf16 v[112:115], a[12:15], a[248:251], v[112:115]\n"
        "ds_write_b64 v246, v[96:97] offset:768\n"
        "s_cmp_lg_u64 vcc, 0\n"
        "s_addc_u32 s25, s25, s25\n"
        "v_mfma_f32_16x16x32_bf16 v[112:115], a[8:11], a[244:247], v[112:115]\n"
        "v_cvt_pk_bf16_f32 v233, v187, v175\n"
        "v_cvt_pk_bf16_f32 v234, v215, v223\n"
        "v_mfma_f32_16x16x32_bf16 v[112:115], a[4:7], a[240:243], v[112:115]\n"
        "v_cvt_pk_bf16_f32 v168, v120, v121\n"
        "v_cvt_pk_bf16_f32 v169, v122, v123\n"
        "v_mfma_f32_16x16x32_bf16 v[112:115], a[0:3], a[236:239], v[112:115]\n"
        "buffer_store_dwordx3 v[232:234], v250, s[16:19], s38 offen offset:1152 sc1\n"
        "v_mfma_f32_16x16x32_bf16 v[116:119], a[24:27], a[236:239], 0\n"
        "v_xor_b32 v251, 0x2000, v251\n"
        "v_xor_b32 v252, 0x2000, v252\n"
        "v_mfma_f32_16x16x32_bf16 v[116:119], a[28:31], a[240:243], v[116:119]\n"
        "v_cvt_pk_bf16_f32 v232, v196, v200\n"
        "v_cvt_pk_bf16_f32 v233, v204, v192\n"
        "v_mfma_f32_16x16x32_bf16 v[116:119], a[32:35], a[244:247], v[116:119]\n"
        "v_cvt_pk_bf16_f32 v156, v112, v113\n"
        "v_cvt_pk_bf16_f32 v157, v114, v115\n"
        "v_mfma_f32_16x16x32_bf16 v[116:119], a[36:39], a[248:251], v[116:119]\n"
        "v_cvt_pk_bf16_f32 v234, v216, v224\n"
        "v_mfma_f32_16x16x32_bf16 v[116:119], a[40:43], a[216:219], v[116:119]\n"
        "v_mfma_f32_16x16x32_bf16 v[116:119], a[44:47], a[220:223], v[116:119]\n"
        "s_nop 2\n"
        "v_mfma_f32_16x16x32_bf16 v[120:123], a[68:71], a[220:223], 0\n"
        "s_nop 2\n"
        "v_mfma_f32_16x16x32_bf16 v[120:123], a[64:67], a[216:219], v[120:123]\n"
        "buffer_store_dwordx3 v[232:234], v250, s[16:19], s39 offen offset:0 sc1\n"
        "v_mfma_f32_16x16x32_bf16 v[120:123], a[60:63], a[248:251], v[120:123]\n"
        "ds_read_b128 a[224:227], v240 offset:1536\n"
        "v_cvt_pk_bf16_f32 v158, v116, v117\n"
        "v_mfma_f32_16x16x32_bf16 v[120:123], a[56:59], a[244:247], v[120:123]\n"
        "ds_read_b128 a[228:231], v241 offset:1536\n"
        "v_cvt_pk_bf16_f32 v159, v118, v119\n"
        "v_mfma_f32_16x16x32_bf16 v[120:123], a[52:55], a[240:243], v[120:123]\n"
        "ds_read_b128 a[232:235], v240 offset:1664\n"
        "v_cvt_pk_bf16_f32 v232, v197, v201\n"
        "v_mfma_f32_16x16x32_bf16 v[120:123], a[48:51], a[236:239], v[120:123]\n"
        "v_cvt_pk_bf16_f32 v233, v205, v193\n"
        "v_cvt_pk_bf16_f32 v234, v217, v225\n"
        "v_mfma_f32_16x16x32_bf16 v[112:115], a[236:239], a[144:147], 0\n"
        "v_add_u32 v247, s45, v247\n"
        "s_nop 0\n"
        "v_mfma_f32_16x16x32_bf16 v[112:115], a[240:243], a[148:151], v[112:115]\n"
        "s_nop 1\n"
        "v_cvt_pk_bf16_f32 v160, v120, v121\n"
        "v_mfma_f32_16x16x32_bf16 v[112:115], a[244:247], a[152:155], v[112:115]\n"
        "v_cvt_pk_bf16_f32 v161, v122, v123\n"
        "v_cvt_pk_bf16_f32 v166, v120, v121\n"
        "v_mfma_f32_16x16x32_bf16 v[112:115], a[248:251], a[156:159], v[112:115]\n"
        "v_cvt_pk_bf16_f32 v167, v122, v123\n"
        "v_mfma_f32_16x16x32_bf16 v[112:115], a[216:219], a[160:163], v[112:115]\n"
        "buffer_store_dwordx3 v[232:234], v250, s[16:19], s39 offen offset:384 sc1\n"
        "v_mfma_f32_16x16x32_bf16 v[112:115], a[220:223], a[164:167], v[112:115]\n"
        "s_nop 2\n"
        "v_mfma_f32_16x16x32_bf16 v[116:119], a[220:223], a[188:191], 0\n"
        "s_nop 2\n"
        "v_mfma_f32_16x16x32_bf16 v[116:119], a[216:219], a[184:187], v[116:119]\n"
        "v_cvt_pk_bf16_f32 v128, v112, v113\n"
        "v_cvt_pk_bf16_f32 v129, v114, v115\n"
        "v_mfma_f32_16x16x32_bf16 v[116:119], a[248:251], a[180:183], v[116:119]\n"
        "v_cvt_pk_bf16_f32 v232, v198, v202\n"
        "v_cvt_pk_bf16_f32 v233, v206, v194\n"
        "v_mfma_f32_16x16x32_bf16 v[116:119], a[244:247], a[176:179], v[116:119]\n"
        "v_cvt_pk_bf16_f32 v234, v218, v226\n"
        "v_mfma_f32_16x16x32_bf16 v[116:119], a[240:243], a[172:175], v[116:119]\n"
        "v_mfma_f32_16x16x32_bf16 v[116:119], a[236:239], a[168:171], v[116:119]\n"
        "s_nop 2\n"
        "v_mfma_f32_16x16x32_bf16 v[120:123], a[236:239], a[192:195], 0\n"
        "ds_read_b128 a[236:239], v241 offset:1664\n"
        "s_nop 0\n"
        "v_mfma_f32_16x16x32_bf16 v[120:123], a[240:243], a[196:199], v[120:123]\n"
        "ds_read_b128 a[240:243], v240 offset:1792\n"
        "v_cvt_pk_bf16_f32 v136, v116, v117\n"
        "v_mfma_f32_16x16x32_bf16 v[120:123], a[244:247], a[200:203], v[120:123]\n"
        "ds_read_b128 a[244:247], v241 offset:1792\n"
        "v_cvt_pk_bf16_f32 v137, v118, v119\n"
        "v_mfma_f32_16x16x32_bf16 v[120:123], a[248:251], a[204:207], v[120:123]\n"
        "buffer_store_dwordx3 v[232:234], v250, s[16:19], s39 offen offset:768 sc1\n"
        "v_mfma_f32_16x16x32_bf16 v[120:123], a[216:219], a[208:211], v[120:123]\n"
        "s_nop 0\n"
        "v_cvt_pk_bf16_f32 v232, v199, v203\n"
        "v_cvt_pk_bf16_f32 v233, v207, v195\n"
        "v_mfma_f32_16x16x32_bf16 v[120:123], a[220:223], a[212:215], v[120:123]\n"
        "s_waitcnt lgkmcnt(5)\n"
        "v_cvt_pk_bf16_f32 v234, v219, v227\n"
        "s_nop 0\n"
        "v_mfma_f32_16x16x32_bf16 v[112:115], a[72:75], a[224:227], 0\n"
        "s_waitcnt lgkmcnt(4)\n"
        "s_nop 1\n"
        "v_mfma_f32_16x16x32_bf16 v[112:115], a[76:79], a[228:231], v[112:115]\n"
        "s_waitcnt lgkmcnt(3)\n"
        "s_nop 1\n"
        "v_mfma_f32_16x16x32_bf16 v[112:115], a[80:83], a[232:235], v[112:115]\n"
        "s_waitcnt lgkmcnt(2)\n"
        "v_cvt_pk_bf16_f32 v144, v120, v121\n"
        "v_cvt_pk_bf16_f32 v145, v122, v123\n"
        "v_mfma_f32_16x16x32_bf16 v[112:115], a[84:87], a[236:239], v[112:115]\n"
        "s_waitcnt lgkmcnt(1)\n"
        "v_mfma_f32_16x16x32_bf16 v[112:115], a[88:91], a[240:243], v[112:115]\n"
        "s_waitcnt lgkmcnt(0)\n"
        "buffer_store_dwordx3 v[232:234], v250, s[16:19], s39 offen offset:1152 sc1\n"
        "v_mfma_f32_16x16x32_bf16 v[112:115], a[92:95], a[244:247], v[112:115]\n"
        "s_nop 2\n"
        "v_mfma_f32_16x16x32_bf16 v[116:119], a[116:119], a[244:247], 0\n"
        "s_nop 2\n"
        "v_mfma_f32_16x16x32_bf16 v[116:119], a[112:115], a[240:243], v[116:119]\n"
        "v_cvt_pk_bf16_f32 v172, v112, v113\n"
        "v_cvt_pk_bf16_f32 v173, v114, v115\n"
        "v_mfma_f32_16x16x32_bf16 v[116:119], a[108:111], a[236:239], v[116:119]\n"
        "v_mfma_f32_16x16x32_bf16 v[116:119], a[104:107], a[232:235], v[116:119]\n"
        "v_mfma_f32_16x16x32_bf16 v[116:119], a[100:103], a[228:231], v[116:119]\n"
        "v_mfma_f32_16x16x32_bf16 v[116:119], a[96:99], a[224:227], v[116:119]\n"
        "s_mov_b32 m0, s48\n"
        "s_nop 1\n"
        "v_mfma_f32_16x16x32_bf16 v[120:123], a[120:123], a[224:227], 0\n"
        "ds_read_b128 v[162:165], v251 offset:0\n"
        "s_nop 0\n"
        "v_mfma_f32_16x16x32_bf16 v[120:123], a[124:127], a[228:231], v[120:123]\n"
        "buffer_load_dwordx4 v249, s[12:15], s36 offen offset:0 lds\n"
        "v_mfma_f32_16x16x32_bf16 v[120:123], a[128:131], a[232:235], v[120:123]\n"
        "v_cvt_pk_bf16_f32 v174, v116, v117\n"
        "v_cvt_pk_bf16_f32 v175, v118, v119\n"
        "v_mfma_f32_16x16x32_bf16 v[120:123], a[132:135], a[236:239], v[120:123]\n"
        "v_mfma_f32_16x16x32_bf16 v[120:123], a[136:139], a[240:243], v[120:123]\n"
        "v_mfma_f32_16x16x32_bf16 v[120:123], a[140:143], a[244:247], v[120:123]\n"
        "s_nop 2\n"
        "v_mfma_f32_16x16x32_bf16 v[112:115], a[20:23], a[244:247], 0\n"
        "s_nop 2\n"
        "v_mfma_f32_16x16x32_bf16 v[112:115], a[16:19], a[240:243], v[112:115]\n"
        "v_cvt_pk_bf16_f32 v190, v120, v121\n"
        "v_cvt_pk_bf16_f32 v191, v122, v123\n"
        "v_mfma_f32_16x16x32_bf16 v[112:115], a[12:15], a[236:239], v[112:115]\n"
        "v_mfma_f32_16x16x32_bf16 v[112:115], a[8:11], a[232:235], v[112:115]\n"
        "v_mfma_f32_16x16x32_bf16 v[112:115], a[4:7], a[228:231], v[112:115]\n"
        "ds_read_b128 v[182:185], v251 offset:512\n"
        "v_mfma_f32_16x16x32_bf16 v[112:115], a[0:3], a[224:227], v[112:115]\n"
        "s_nop 2\n"
        "v_mfma_f32_16x16x32_bf16 v[116:119], a[24:27], a[224:227], 0\n"
        "s_nop 2\n"
        "v_mfma_f32_16x16x32_bf16 v[116:119], a[28:31], a[228:231], v[116:119]\n"
        "v_cvt_pk_bf16_f32 v176, v112, v113\n"
        "v_cvt_pk_bf16_f32 v177, v114, v115\n"
        "v_mfma_f32_16x16x32_bf16 v[116:119], a[32:35], a[232:235], v[116:119]\n"
        "v_mfma_f32_16x16x32_bf16 v[116:119], a[36:39], a[236:239], v[116:119]\n"
        "buffer_load_dwordx4 v253, s[12:15], s36 offen offset:1024 lds\n"
        "v_mfma_f32_16x16x32_bf16 v[116:119], a[40:43], a[240:243], v[116:119]\n"
        "v_mfma_f32_16x16x32_bf16 v[116:119], a[44:47], a[244:247], v[116:119]\n"
        "s_nop 2\n"
        "v_mfma_f32_16x16x32_bf16 v[120:123], a[68:71], a[244:247], 0\n"
        "s_nop 2\n"
        "v_mfma_f32_16x16x32_bf16 v[120:123], a[64:67], a[240:243], v[120:123]\n"
        "v_cvt_pk_bf16_f32 v178, v116, v117\n"
        "v_cvt_pk_bf16_f32 v179, v118, v119\n"
        "v_mfma_f32_16x16x32_bf16 v[120:123], a[60:63], a[236:239], v[120:123]\n"
        "v_mfma_f32_16x16x32_bf16 v[120:123], a[56:59], a[232:235], v[120:123]\n"
        "v_mfma_f32_16x16x32_bf16 v[120:123], a[52:55], a[228:231], v[120:123]\n"
        "v_mfma_f32_16x16x32_bf16 v[120:123], a[48:51], a[224:227], v[120:123]\n"
        "s_nop 2\n"
        "v_mfma_f32_16x16x32_bf16 v[112:115], a[224:227], a[144:147], 0\n"
        "s_nop 2\n"
        "v_mfma_f32_16x16x32_bf16 v[112:115], a[228:231], a[148:151], v[112:115]\n"
        "v_cvt_pk_bf16_f32 v180, v120, v121\n"
        "v_cvt_pk_bf16_f32 v181, v122, v123\n"
        "v_mfma_f32_16x16x32_bf16 v[112:115], a[232:235], a[152:155], v[112:115]\n"
        "v_cvt_pk_bf16_f32 v186, v120, v121\n"
        "v_cvt_pk_bf16_f32 v187, v122, v123\n"
        "v_mfma_f32_16x16x32_bf16 v[112:115], a[236:239], a[156:159], v[112:115]\n"
        "s_add_u32 m0, s41, 0\n"
        "v_mfma_f32_16x16x32_bf16 v[112:115], a[240:243], a[160:163], v[112:115]\n"
        "ds_read_b128 a[248:251], v240 offset:12288\n"
        "v_mfma_f32_16x16x32_bf16 v[112:115], a[244:247], a[164:167], v[112:115]\n"
        "buffer_load_dwordx4 v242, s[8:11], s47 offen lds\n"
        "v_mfma_f32_16x16x32_bf16 v[96:99], v[152:155], v[156:159], v[0:3]\n"
        "s_waitcnt lgkmcnt(2)\n"
        "ds_read_b128 a[216:219], v241 offset:12288\n"
        "s_nop 0\n"
        "v_mfma_f32_16x16x32_bf16 v[96:99], v[168:171], v[160:163], v[96:99]\n"
        "ds_read_b128 a[220:223], v240 offset:12416\n"
        "s_sub_u32 s31, s30, s28\n"
        "s_nop 0\n"
        "v_mfma_f32_16x16x32_bf16 v[100:103], v[172:175], v[156:159], v[4:7]\n"
        "s_sub_u32 s45, s28, s29\n"
        "v_cvt_pk_bf16_f32 v130, v112, v113\n"
        "v_cvt_pk_bf16_f32 v131, v114, v115\n"
        "v_mfma_f32_16x16x32_bf16 v[100:103], v[188:191], v[164:167], v[100:103]\n"
        "s_add_u32 s28, s28, s31\n"
        "ds_read_b128 v[162:165], v252 offset:0\n"
        "v_mfma_f32_16x16x32_bf16 v[116:119], a[244:247], a[188:191], 0\n"
        "s_mov_b32 s30, s29\n"
        "v_exp_f32 v96, v96\n"
        "v_mfma_f32_16x16x32_bf16 v[116:119], a[240:243], a[184:187], v[116:119]\n"
        "s_add_u32 s29, s29, s45\n"
        "v_exp_f32 v97, v97\n"
        "v_mfma_f32_16x16x32_bf16 v[116:119], a[236:239], a[180:183], v[116:119]\n"
        "v_exp_f32 v98, v98\n"
        "v_mfma_f32_16x16x32_bf16 v[116:119], a[232:235], a[176:179], v[116:119]\n"
        "v_exp_f32 v100, v100\n"
        "v_mfma_f32_16x16x32_bf16 v[116:119], a[228:231], a[172:175], v[116:119]\n"
        "v_exp_f32 v99, v99\n"
        "v_mfma_f32_16x16x32_bf16 v[116:119], a[224:227], a[168:171], v[116:119]\n"
        "v_exp_f32 v101, v101\n"
        "v_mfma_f32_16x16x32_bf16 v[104:107], v[152:155], v[176:179], v[16:19]\n"
        "s_waitcnt lgkmcnt(4)\n"
        "v_exp_f32 v102, v102\n"
        "v_mfma_f32_16x16x32_bf16 v[104:107], v[168:171], v[180:183], v[104:107]\n"
        "s_add_u32 m0, s41, 1024\n"
        "v_cvt_pk_bf16_f32 v64, v96, v97\n"
        "v_cvt_pk_bf16_f32 v65, v98, v99\n"
        "v_mfma_f32_16x16x32_bf16 v[108:111], v[172:175], v[176:179], v[20:23]\n"
        "v_cvt_pk_bf16_f32 v138, v116, v117\n"
        "v_cvt_pk_bf16_f32 v139, v118, v119\n"
        "v_mfma_f32_16x16x32_bf16 v[108:111], v[188:191], v[184:187], v[108:111]\n"
        "buffer_load_dwordx4 v243, s[8:11], s47 offen lds\n"
        "v_mfma_f32_16x16x32_bf16 v[120:123], a[224:227], a[192:195], 0\n"
        "ds_read_b128 a[224:227], v241 offset:12416\n"
        "v_cvt_pk_bf16_f32 v66, v100, v101\n"
        "v_mfma_f32_16x16x32_bf16 v[120:123], a[228:231], a[196:199], v[120:123]\n"
        "ds_read_b128 a[228:231], v240 offset:12544\n"
        "v_mfma_f32_16x16x32_bf16 v[120:123], a[232:235], a[200:203], v[120:123]\n"
        "ds_read_b128 a[232:235], v241 offset:12544\n"
        "v_mfma_f32_16x16x32_bf16 v[120:123], a[236:239], a[204:207], v[120:123]\n"
        "ds_read_b128 v[202:205], v251 offset:4096\n"
        "v_mfma_f32_16x16x32_bf16 v[120:123], a[240:243], a[208:211], v[120:123]\n"
        "ds_read_b128 v[182:185], v252 offset:512\n"
        "v_mfma_f32_16x16x32_bf16 v[120:123], a[244:247], a[212:215], v[120:123]\n"
        "s_waitcnt lgkmcnt(8)\n"
        "v_exp_f32 v103, v103\n"
        "v_mfma_f32_16x16x32_bf16 v[112:115], a[0:3], a[248:251], 0\n"
        "s_waitcnt lgkmcnt(7)\n"
        "v_cvt_pk_bf16_f32 v67, v102, v103\n"
        "s_nop 0\n"
        "v_mfma_f32_16x16x32_bf16 v[112:115], a[4:7], a[216:219], v[112:115]\n"
        "s_waitcnt lgkmcnt(6)\n"
        "v_exp_f32 v104, v104\n"
        "v_mfma_f32_16x16x32_bf16 v[112:115], a[8:11], a[220:223], v[112:115]\n"
        "s_waitcnt lgkmcnt(4)\n"
        "v_exp_f32 v105, v105\n"
        "v_mfma_f32_16x16x32_bf16 v[112:115], a[12:15], a[224:227], v[112:115]\n"
        "s_waitcnt lgkmcnt(3)\n"
        "v_exp_f32 v106, v106\n"
        "v_mfma_f32_16x16x32_bf16 v[112:115], a[16:19], a[228:231], v[112:115]\n"
        "s_waitcnt lgkmcnt(2)\n"
        "v_cvt_pk_bf16_f32 v72, v104, v105\n"
        "v_cvt_pk_bf16_f32 v146, v120, v121\n"
        "v_mfma_f32_16x16x32_bf16 v[112:115], a[20:23], a[232:235], v[112:115]\n"
        "s_add_u32 m0, s41, 2048\n"
        "v_exp_f32 v108, v108\n"
        "v_mfma_f32_16x16x32_bf16 v[116:119], a[44:47], a[232:235], 0\n"
        "v_cvt_pk_bf16_f32 v147, v122, v123\n"
        "v_add_u32 v245, s45, v245\n"
        "v_mfma_f32_16x16x32_bf16 v[116:119], a[40:43], a[228:231], v[116:119]\n"
        "buffer_load_dwordx4 v244, s[8:11], s47 offen lds\n"
        "v_mfma_f32_16x16x32_bf16 v[116:119], a[36:39], a[224:227], v[116:119]\n"
        "v_exp_f32 v107, v107\n"
        "v_mfma_f32_16x16x32_bf16 v[116:119], a[32:35], a[220:223], v[116:119]\n"
        "v_exp_f32 v109, v109\n"
        "v_mfma_f32_16x16x32_bf16 v[116:119], a[28:31], a[216:219], v[116:119]\n"
        "v_cvt_pk_bf16_f32 v73, v106, v107\n"
        "v_cvt_pk_bf16_f32 v74, v108, v109\n"
        "v_mfma_f32_16x16x32_bf16 v[116:119], a[24:27], a[248:251], v[116:119]\n"
        "v_exp_f32 v110, v110\n"
        "v_mfma_f32_16x16x32_bf16 v[120:123], a[48:51], a[248:251], 0\n"
        "v_exp_f32 v111, v111\n"
        "v_mfma_f32_16x16x32_bf16 v[120:123], a[52:55], a[216:219], v[120:123]\n"
        "v_cvt_pk_bf16_f32 v75, v110, v111\n"
        "v_cvt_pk_bf16_f32 v196, v112, v113\n"
        "v_mfma_f32_16x16x32_bf16 v[120:123], a[56:59], a[220:223], v[120:123]\n"
        "v_cvt_pk_bf16_f32 v197, v114, v115\n"
        "v_cvt_pk_bf16_f32 v198, v116, v117\n"
        "v_mfma_f32_16x16x32_bf16 v[120:123], a[60:63], a[224:227], v[120:123]\n"
        "v_cvt_pk_bf16_f32 v199, v118, v119\n"
        "v_add_u32 v246, s45, v246\n"
        "v_mfma_f32_16x16x32_bf16 v[120:123], a[64:67], a[228:231], v[120:123]\n"
        "v_mfma_f32_16x16x32_bf16 v[120:123], a[68:71], a[232:235], v[120:123]\n"
        "s_nop 2\n"
        "v_mfma_f32_16x16x32_bf16 v[112:115], a[92:95], a[232:235], 0\n"
        "s_nop 2\n"
        "v_mfma_f32_16x16x32_bf16 v[112:115], a[88:91], a[228:231], v[112:115]\n"
        "s_add_u32 m0, s41, 12288\n"
        "v_cvt_pk_bf16_f32 v200, v120, v121\n"
        "v_cvt_pk_bf16_f32 v201, v122, v123\n"
        "v_mfma_f32_16x16x32_bf16 v[112:115], a[84:87], a[224:227], v[112:115]\n"
        "v_cvt_pk_bf16_f32 v206, v120, v121\n"
        "v_cvt_pk_bf16_f32 v207, v122, v123\n"
        "v_mfma_f32_16x16x32_bf16 v[112:115], a[80:83], a[220:223], v[112:115]\n"
        "buffer_load_dwordx4 v242, s[8:11], s35 offen lds\n"
        "v_mfma_f32_16x16x32_bf16 v[112:115], a[76:79], a[216:219], v[112:115]\n"
        "v_mfma_f32_16x16x32_bf16 v[112:115], a[72:75], a[248:251], v[112:115]\n"
        "s_nop 2\n"
        "v_mfma_f32_16x16x32_bf16 v[96:99], v[152:155], v[196:199], v[32:35]\n"
        "s_waitcnt lgkmcnt(1)\n"
        "s_nop 1\n"
        "v_mfma_f32_16x16x32_bf16 v[96:99], v[168:171], v[200:203], v[96:99]\n"
        "s_nop 0\n"
        "v_cvt_pk_bf16_f32 v192, v112, v113\n"
        "v_cvt_pk_bf16_f32 v193, v114, v115\n"
        "v_mfma_f32_16x16x32_bf16 v[100:103], v[172:175], v[196:199], v[36:39]\n"
        "v_mfma_f32_16x16x32_bf16 v[100:103], v[188:191], v[204:207], v[100:103]\n"
        "ds_read_b128 v[202:205], v252 offset:4096\n"
        "s_cmp_eq_u32 s37, 100663296\n"
        "v_mfma_f32_16x16x32_bf16 v[116:119], a[96:99], a[248:251], 0\n"
        "s_cselect_b32 s46, 0, s37\n"
        "v_exp_f32 v96, v96\n"
        "v_mfma_f32_16x16x32_bf16 v[116:119], a[100:103], a[216:219], v[116:119]\n"
        "s_add_u32 s38, s46, s43\n"
        "v_exp_f32 v97, v97\n"
        "v_mfma_f32_16x16x32_bf16 v[116:119], a[104:107], a[220:223], v[116:119]\n"
        "s_add_u32 s39, s46, s44\n"
        "v_exp_f32 v98, v98\n"
        "v_mfma_f32_16x16x32_bf16 v[116:119], a[108:111], a[224:227], v[116:119]\n"
        "s_add_u32 s37, s37, 6291456\n"
        "v_exp_f32 v100, v100\n"
        "v_mfma_f32_16x16x32_bf16 v[116:119], a[112:115], a[228:231], v[116:119]\n"
        "v_exp_f32 v99, v99\n"
        "v_mfma_f32_16x16x32_bf16 v[116:119], a[116:119], a[232:235], v[116:119]\n"
        "s_add_u32 m0, s41, 13312\n"
        "v_exp_f32 v101, v101\n"
        "v_mfma_f32_16x16x32_bf16 v[120:123], a[140:143], a[232:235], 0\n"
        "v_exp_f32 v102, v102\n"
        "v_mfma_f32_16x16x32_bf16 v[120:123], a[136:139], a[228:231], v[120:123]\n"
        "buffer_load_dwordx4 v243, s[8:11], s35 offen lds\n"
        "v_mfma_f32_16x16x32_bf16 v[120:123], a[132:135], a[224:227], v[120:123]\n"
        "v_cvt_pk_bf16_f32 v80, v96, v97\n"
        "v_cvt_pk_bf16_f32 v194, v116, v117\n"
        "v_mfma_f32_16x16x32_bf16 v[120:123], a[128:131], a[220:223], v[120:123]\n"
        "v_cvt_pk_bf16_f32 v195, v118, v119\n"
        "v_cvt_pk_bf16_f32 v81, v98, v99\n"
        "v_mfma_f32_16x16x32_bf16 v[120:123], a[124:127], a[216:219], v[120:123]\n"
        "v_cvt_pk_bf16_f32 v82, v100, v101\n"
        "v_mfma_f32_16x16x32_bf16 v[120:123], a[120:123], a[248:251], v[120:123]\n"
        "v_exp_f32 v103, v103\n"
        "v_mfma_f32_16x16x32_bf16 v[112:115], a[248:251], a[144:147], 0\n"
        "v_cvt_pk_bf16_f32 v83, v102, v103\n"
        "s_nop 0\n"
        "v_mfma_f32_16x16x32_bf16 v[112:115], a[216:219], a[148:151], v[112:115]\n"
        "s_nop 2\n"
        "v_mfma_f32_16x16x32_bf16 v[112:115], a[220:223], a[152:155], v[112:115]\n"
        "ds_read_b128 a[236:239], v240 offset:13824\n"
        "v_cvt_pk_bf16_f32 v208, v120, v121\n"
        "v_mfma_f32_16x16x32_bf16 v[112:115], a[224:227], a[156:159], v[112:115]\n"
        "ds_read_b128 a[240:243], v241 offset:13824\n"
        "v_cvt_pk_bf16_f32 v209, v122, v123\n"
        "v_mfma_f32_16x16x32_bf16 v[112:115], a[228:231], a[160:163], v[112:115]\n"
        "ds_read_b128 a[244:247], v240 offset:13952\n"
        "v_mfma_f32_16x16x32_bf16 v[112:115], a[232:235], a[164:167], v[112:115]\n"
        "s_nop 2\n"
        "v_mfma_f32_16x16x32_bf16 v[104:107], v[192:195], v[156:159], v[8:11]\n"
        "s_nop 1\n"
        "v_mfma_f32_16x16x32_bf16 v[104:107], v[208:211], v[160:163], v[104:107]\n"
        "s_add_u32 m0, s41, 14336\n"
        "v_cvt_pk_bf16_f32 v132, v112, v113\n"
        "v_cvt_pk_bf16_f32 v133, v114, v115\n"
        "v_mfma_f32_16x16x32_bf16 v[116:119], a[232:235], a[188:191], 0\n"
        "v_mfma_f32_16x16x32_bf16 v[116:119], a[228:231], a[184:187], v[116:119]\n"
        "buffer_load_dwordx4 v244, s[8:11], s35 offen lds\n"
        "v_mfma_f32_16x16x32_bf16 v[116:119], a[224:227], a[180:183], v[116:119]\n"
        "v_mfma_f32_16x16x32_bf16 v[116:119], a[220:223], a[176:179], v[116:119]\n"
        "v_exp_f32 v104, v104\n"
        "v_mfma_f32_16x16x32_bf16 v[116:119], a[216:219], a[172:175], v[116:119]\n"
        "v_exp_f32 v105, v105\n"
        "v_mfma_f32_16x16x32_bf16 v[116:119], a[248:251], a[168:171], v[116:119]\n"
        "v_exp_f32 v106, v106\n"
        "v_mfma_f32_16x16x32_bf16 v[108:111], v[192:195], v[176:179], v[24:27]\n"
        "s_waitcnt lgkmcnt(4)\n"
        "v_exp_f32 v107, v107\n"
        "v_mfma_f32_16x16x32_bf16 v[108:111], v[208:211], v[180:183], v[108:111]\n"
        "v_cvt_pk_bf16_f32 v68, v104, v105\n"
        "v_cvt_pk_bf16_f32 v69, v106, v107\n"
        "v_mfma_f32_16x16x32_bf16 v[120:123], a[248:251], a[192:195], 0\n"
        "ds_read_b128 a[248:251], v241 offset:13952\n"
        "v_cvt_pk_bf16_f32 v140, v116, v117\n"
        "v_mfma_f32_16x16x32_bf16 v[120:123], a[216:219], a[196:199], v[120:123]\n"
        "ds_read_b128 a[216:219], v240 offset:14080\n"
        "v_cvt_pk_bf16_f32 v141, v118, v119\n"
        "v_mfma_f32_16x16x32_bf16 v[120:123], a[220:223], a[200:203], v[120:123]\n"
        "ds_read_b128 a[220:223], v241 offset:14080\n"
        "v_mfma_f32_16x16x32_bf16 v[120:123], a[224:227], a[204:207], v[120:123]\n"
        "v_exp_f32 v108, v108\n"
        "v_mfma_f32_16x16x32_bf16 v[120:123], a[228:231], a[208:211], v[120:123]\n"
        "v_exp_f32 v109, v109\n"
        "v_mfma_f32_16x16x32_bf16 v[120:123], a[232:235], a[212:215], v[120:123]\n"
        "v_exp_f32 v110, v110\n"
        "v_mfma_f32_16x16x32_bf16 v[96:99], v[192:195], v[196:199], v[40:43]\n"
        "s_waitcnt lgkmcnt(6)\n"
        "v_exp_f32 v111, v111\n"
        "v_mfma_f32_16x16x32_bf16 v[96:99], v[208:211], v[200:203], v[96:99]\n"
        "s_waitcnt lgkmcnt(5)\n"
        "v_cvt_pk_bf16_f32 v76, v108, v109\n"
        "v_cvt_pk_bf16_f32 v77, v110, v111\n"
        "v_mfma_f32_16x16x32_bf16 v[112:115], a[72:75], a[236:239], 0\n"
        "s_waitcnt lgkmcnt(4)\n"
        "s_nop 0\n"
        "v_cvt_pk_bf16_f32 v148, v120, v121\n"
        "v_mfma_f32_16x16x32_bf16 v[112:115], a[76:79], a[240:243], v[112:115]\n"
        "s_waitcnt lgkmcnt(3)\n"
        "v_cvt_pk_bf16_f32 v149, v122, v123\n"
        "v_mfma_f32_16x16x32_bf16 v[112:115], a[80:83], a[244:247], v[112:115]\n"
        "s_waitcnt lgkmcnt(2)\n"
        "v_exp_f32 v96, v96\n"
        "v_mfma_f32_16x16x32_bf16 v[112:115], a[84:87], a[248:251], v[112:115]\n"
        "s_waitcnt lgkmcnt(1)\n"
        "v_exp_f32 v97, v97\n"
        "v_mfma_f32_16x16x32_bf16 v[112:115], a[88:91], a[216:219], v[112:115]\n"
        "s_waitcnt lgkmcnt(0)\n"
        "v_exp_f32 v98, v98\n"
        "v_mfma_f32_16x16x32_bf16 v[112:115], a[92:95], a[220:223], v[112:115]\n"
        "v_exp_f32 v99, v99\n"
        "v_mfma_f32_16x16x32_bf16 v[116:119], a[116:119], a[220:223], 0\n"
        "v_cvt_pk_bf16_f32 v84, v96, v97\n"
        "v_cvt_pk_bf16_f32 v85, v98, v99\n"
        "v_mfma_f32_16x16x32_bf16 v[116:119], a[112:115], a[216:219], v[116:119]\n"
        "v_add_u32 v240, s31, v240\n"
        "v_add_u32 v241, s31, v241\n"
        "v_mfma_f32_16x16x32_bf16 v[116:119], a[108:111], a[248:251], v[116:119]\n"
        "v_cvt_pk_bf16_f32 v212, v112, v113\n"
        "v_cvt_pk_bf16_f32 v213, v114, v115\n"
        "v_mfma_f32_16x16x32_bf16 v[116:119], a[104:107], a[244:247], v[116:119]\n"
        "v_mfma_f32_16x16x32_bf16 v[116:119], a[100:103], a[240:243], v[116:119]\n"
        "v_mfma_f32_16x16x32_bf16 v[116:119], a[96:99], a[236:239], v[116:119]\n"
        "s_nop 2\n"
        "v_mfma_f32_16x16x32_bf16 v[120:123], a[120:123], a[236:239], 0\n"
        "s_nop 2\n"
        "v_mfma_f32_16x16x32_bf16 v[120:123], a[124:127], a[240:243], v[120:123]\n"
        "s_add_u32 s47, s47, 6291456\n"
        "v_cvt_pk_bf16_f32 v214, v116, v117\n"
        "v_cvt_pk_bf16_f32 v215, v118, v119\n"
        "v_mfma_f32_16x16x32_bf16 v[120:123], a[128:131], a[244:247], v[120:123]\n"
        "s_add_u32 s36, s36, 2097152\n"
        "v_mfma_f32_16x16x32_bf16 v[120:123], a[132:135], a[248:251], v[120:123]\n"
        "s_xor_b32 s48, s48, 8192\n"
        "v_mfma_f32_16x16x32_bf16 v[120:123], a[136:139], a[216:219], v[120:123]\n"
        "v_mfma_f32_16x16x32_bf16 v[120:123], a[140:143], a[220:223], v[120:123]\n"
        "s_nop 2\n"
        "v_mfma_f32_16x16x32_bf16 v[112:115], a[220:223], a[164:167], 0\n"
        "s_nop 2\n"
        "v_mfma_f32_16x16x32_bf16 v[112:115], a[216:219], a[160:163], v[112:115]\n"
        "v_cvt_pk_bf16_f32 v230, v120, v121\n"
        "v_cvt_pk_bf16_f32 v231, v122, v123\n"
        "v_mfma_f32_16x16x32_bf16 v[112:115], a[248:251], a[156:159], v[112:115]\n"
        "v_mfma_f32_16x16x32_bf16 v[112:115], a[244:247], a[152:155], v[112:115]\n"
        "v_mfma_f32_16x16x32_bf16 v[112:115], a[240:243], a[148:151], v[112:115]\n"
        "v_mfma_f32_16x16x32_bf16 v[112:115], a[236:239], a[144:147], v[112:115]\n"
        "s_barrier\n"
        "s_nop 2\n"
        "v_mfma_f32_16x16x32_bf16 v[100:103], v[212:215], v[156:159], v[12:15]\n"
        "s_nop 2\n"
        "v_mfma_f32_16x16x32_bf16 v[100:103], v[228:231], v[164:167], v[100:103]\n"
        "v_cvt_pk_bf16_f32 v134, v112, v113\n"
        "v_cvt_pk_bf16_f32 v135, v114, v115\n"
        "v_mfma_f32_16x16x32_bf16 v[116:119], a[236:239], a[168:171], 0\n"
        "v_mfma_f32_16x16x32_bf16 v[116:119], a[240:243], a[172:175], v[116:119]\n"
        "v_mfma_f32_16x16x32_bf16 v[116:119], a[244:247], a[176:179], v[116:119]\n"
        "v_mfma_f32_16x16x32_bf16 v[116:119], a[248:251], a[180:183], v[116:119]\n"
        "v_mfma_f32_16x16x32_bf16 v[116:119], a[216:219], a[184:187], v[116:119]\n"
        "s_nop 0\n"
        "v_exp_f32 v100, v100\n"
        "v_mfma_f32_16x16x32_bf16 v[116:119], a[220:223], a[188:191], v[116:119]\n"
        "v_exp_f32 v101, v101\n"
        "v_mfma_f32_16x16x32_bf16 v[104:107], v[212:215], v[176:179], v[28:31]\n"
        "v_exp_f32 v102, v102\n"
        "v_mfma_f32_16x16x32_bf16 v[104:107], v[228:231], v[184:187], v[104:107]\n"
        "v_exp_f32 v103, v103\n"
        "v_mfma_f32_16x16x32_bf16 v[120:123], a[220:223], a[212:215], 0\n"
        "v_cvt_pk_bf16_f32 v70, v100, v101\n"
        "v_cvt_pk_bf16_f32 v71, v102, v103\n"
        "v_mfma_f32_16x16x32_bf16 v[120:123], a[216:219], a[208:211], v[120:123]\n"
        "v_cvt_pk_bf16_f32 v142, v116, v117\n"
        "v_cvt_pk_bf16_f32 v143, v118, v119\n"
        "v_mfma_f32_16x16x32_bf16 v[120:123], a[248:251], a[204:207], v[120:123]\n"
        "v_mov_b64 v[96:97], 0\n"
        "v_mfma_f32_16x16x32_bf16 v[120:123], a[244:247], a[200:203], v[120:123]\n"
        "v_exp_f32 v104, v104\n"
        "v_mfma_f32_16x16x32_bf16 v[120:123], a[240:243], a[196:199], v[120:123]\n"
        "v_exp_f32 v105, v105\n"
        "v_mfma_f32_16x16x32_bf16 v[120:123], a[236:239], a[192:195], v[120:123]\n"
        "v_exp_f32 v106, v106\n"
        "v_mfma_f32_16x16x32_bf16 v[108:111], v[212:215], v[196:199], v[44:47]\n"
        "v_exp_f32 v107, v107\n"
        "v_mfma_f32_16x16x32_bf16 v[108:111], v[228:231], v[204:207], v[108:111]\n"
        "v_cvt_pk_bf16_f32 v78, v104, v105\n"
        "v_cvt_pk_bf16_f32 v79, v106, v107\n"
        "v_mfma_f32_16x16x32_bf16 v[112:115], a[0:3], a[236:239], 0\n"
        "s_nop 0\n"
        "v_cvt_pk_bf16_f32 v150, v120, v121\n"
        "v_cvt_pk_bf16_f32 v151, v122, v123\n"
        "v_mfma_f32_16x16x32_bf16 v[112:115], a[4:7], a[240:243], v[112:115]\n"
        "v_mfma_f32_16x16x32_bf16 v[112:115], a[8:11], a[244:247], v[112:115]\n"
        "v_exp_f32 v108, v108\n"
        "v_mfma_f32_16x16x32_bf16 v[112:115], a[12:15], a[248:251], v[112:115]\n"
        "v_exp_f32 v109, v109\n"
        "v_mfma_f32_16x16x32_bf16 v[112:115], a[16:19], a[216:219], v[112:115]\n"
        "v_exp_f32 v110, v110\n"
        "v_mfma_f32_16x16x32_bf16 v[112:115], a[20:23], a[220:223], v[112:115]\n"
        "v_exp_f32 v111, v111\n"
        "v_smfmac_f32_16x16x64_bf16 v[96:99], a[252:255], v[64:71], v237\n"
        "v_cvt_pk_bf16_f32 v86, v108, v109\n"
        "v_mfma_f32_16x16x32_bf16 v[100:103], v[132:135], v[68:71], 0\n"
        "v_mov_b64 v[124:125], 0\n"
        "v_cvt_pk_bf16_f32 v87, v110, v111\n"
        "v_mfma_f32_16x16x32_bf16 v[100:103], v[128:131], v[64:67], v[100:103]\n"
        "s_nop 0\n"
        "v_cvt_pk_bf16_f32 v216, v112, v113\n"
        "v_cvt_pk_bf16_f32 v217, v114, v115\n"
        "v_mfma_f32_16x16x32_bf16 v[104:107], v[136:139], v[64:67], 0\n"
        "ds_read_b128 v[222:225], v251 offset:4608\n"
        "v_add_f32 v97, v96, v97\n"
        "v_mfma_f32_16x16x32_bf16 v[104:107], v[140:143], v[68:71], v[104:107]\n"
        "v_rcp_f32 v96, v97\n"
        "v_mfma_f32_16x16x32_bf16 v[108:111], v[148:151], v[68:71], 0\n"
        ".long 0xbec200ff\n"
        "47:\n"
        "s_branch 61b\n"
        "v_mul_f32 v100, v96, v100\n"
        "v_mul_f32 v101, v96, v101\n"
        "v_mfma_f32_16x16x32_bf16 v[108:111], v[144:147], v[64:67], v[108:111]\n"
        "v_mul_f32 v102, v96, v102\n"
        "v_mul_f32 v103, v96, v103\n"
        "v_mfma_f32_16x16x32_bf16 v[116:119], a[44:47], a[220:223], 0\n"
        "v_cvt_pk_bf16_f32 v100, v100, v101\n"
        "v_cvt_pk_bf16_f32 v101, v102, v103\n"
        "v_mfma_f32_16x16x32_bf16 v[116:119], a[40:43], a[216:219], v[116:119]\n"
        "v_mul_f32 v104, v96, v104\n"
        "v_mul_f32 v105, v96, v105\n"
        "v_mfma_f32_16x16x32_bf16 v[116:119], a[36:39], a[248:251], v[116:119]\n"
        "v_mul_f32 v106, v96, v106\n"
        "v_mul_f32 v107, v96, v107\n"
        "v_mfma_f32_16x16x32_bf16 v[116:119], a[32:35], a[244:247], v[116:119]\n"
        "v_cvt_pk_bf16_f32 v102, v104, v105\n"
        "v_cvt_pk_bf16_f32 v103, v106, v107\n"
        "v_mfma_f32_16x16x32_bf16 v[116:119], a[28:31], a[240:243], v[116:119]\n"
        "ds_write_b128 v245, v[100:103] offset:0\n"
        "v_mfma_f32_16x16x32_bf16 v[116:119], a[24:27], a[236:239], v[116:119]\n"
        "v_mul_f32 v108, v96, v108\n"
        "v_mul_f32 v109, v96, v109\n"
        "v_smfmac_f32_16x16x64_bf16 v[124:127], a[252:255], v[72:79], v237\n"
        "v_mul_f32 v110, v96, v110\n"
        "v_mfma_f32_16x16x32_bf16 v[64:67], v[132:135], v[76:79], 0\n"
        "v_mul_f32 v111, v96, v111\n"
        "v_cvt_pk_bf16_f32 v108, v108, v109\n"
        "v_mfma_f32_16x16x32_bf16 v[64:67], v[128:131], v[72:75], v[64:67]\n"
        "ds_read_b128 v[156:159], v247 offset:0\n"
        "v_cvt_pk_bf16_f32 v218, v116, v117\n"
        "v_mfma_f32_16x16x32_bf16 v[68:71], v[136:139], v[72:75], 0\n"
        "ds_read_b128 v[160:163], v247 offset:4096\n"
        "v_cvt_pk_bf16_f32 v219, v118, v119\n"
        "v_mfma_f32_16x16x32_bf16 v[68:71], v[140:143], v[76:79], v[68:71]\n"
        "ds_read_b128 v[164:167], v247 offset:4352\n"
        "v_cvt_pk_bf16_f32 v109, v110, v111\n"
        "v_mfma_f32_16x16x32_bf16 v[120:123], v[148:151], v[76:79], 0\n"
        "ds_write_b64 v246, v[108:109] offset:0\n"
        "v_mfma_f32_16x16x32_bf16 v[120:123], v[144:147], v[72:75], v[120:123]\n"
        "v_max_u32 v236, v96, v97\n"
        "v_mov_b64 v[116:117], 0\n"
        "v_mfma_f32_16x16x32_bf16 v[112:115], a[48:51], a[236:239], 0\n"
        "v_add_f32 v125, v124, v125\n"
        "v_mfma_f32_16x16x32_bf16 v[112:115], a[52:55], a[240:243], v[112:115]\n"
        "v_rcp_f32 v124, v125\n"
        "v_mfma_f32_16x16x32_bf16 v[112:115], a[56:59], a[244:247], v[112:115]\n"
        "v_mul_f32 v64, v124, v64\n"
        "v_mul_f32 v65, v124, v65\n"
        "v_mfma_f32_16x16x32_bf16 v[112:115], a[60:63], a[248:251], v[112:115]\n"
        "v_mul_f32 v66, v124, v66\n"
        "v_mul_f32 v67, v124, v67\n"
        "v_mfma_f32_16x16x32_bf16 v[112:115], a[64:67], a[216:219], v[112:115]\n"
        "v_cvt_pk_bf16_f32 v64, v64, v65\n"
        "v_cvt_pk_bf16_f32 v65, v66, v67\n"
        "v_mfma_f32_16x16x32_bf16 v[112:115], a[68:71], a[220:223], v[112:115]\n"
        "ds_read_b128 a[216:219], v248 offset:0\n"
        "v_mul_f32 v68, v124, v68\n"
        "v_smfmac_f32_16x16x64_bf16 v[116:119], a[252:255], v[80:87], v237\n"
        "ds_read_b128 a[220:223], v248 offset:3072\n"
        "v_mfma_f32_16x16x32_bf16 v[72:75], v[132:135], v[84:87], 0\n"
        "v_mul_f32 v69, v124, v69\n"
        "v_mul_f32 v70, v124, v70\n"
        "v_mfma_f32_16x16x32_bf16 v[72:75], v[128:131], v[80:83], v[72:75]\n"
        "v_cvt_pk_bf16_f32 v220, v112, v113\n"
        "v_cvt_pk_bf16_f32 v221, v114, v115\n"
        "v_mfma_f32_16x16x32_bf16 v[76:79], v[136:139], v[80:83], 0\n"
        "ds_read_b128 a[224:227], v254 offset:16\n"
        "v_cvt_pk_bf16_f32 v226, v112, v113\n"
        "v_mfma_f32_16x16x32_bf16 v[76:79], v[140:143], v[84:87], v[76:79]\n"
        "ds_read_b128 a[228:231], v254 offset:3088\n"
        "v_cvt_pk_bf16_f32 v227, v114, v115\n"
        "v_mfma_f32_16x16x32_bf16 v[96:99], v[148:151], v[84:87], 0\n"
        "v_mul_f32 v71, v124, v71\n"
        "v_cvt_pk_bf16_f32 v66, v68, v69\n"
        "v_mfma_f32_16x16x32_bf16 v[96:99], v[144:147], v[80:83], v[96:99]\n"
        "v_cvt_pk_bf16_f32 v67, v70, v71\n"
        "v_mul_f32 v120, v124, v120\n"
        "v_mfma_f32_16x16x32_bf16 v[100:103], v[152:155], v[216:219], v[48:51]\n"
        "s_waitcnt lgkmcnt(9)\n"
        "ds_read_b128 v[152:155], v247 offset:256\n"
        "v_mul_f32 v121, v124, v121\n"
        "v_mfma_f32_16x16x32_bf16 v[100:103], v[168:171], v[220:223], v[100:103]\n"
        "ds_write_b128 v245, v[64:67] offset:256\n"
        "v_mfma_f32_16x16x32_bf16 v[104:107], v[172:175], v[216:219], v[52:55]\n"
        "v_mul_f32 v122, v124, v122\n"
        "v_mul_f32 v123, v124, v123\n"
        "v_mfma_f32_16x16x32_bf16 v[104:107], v[188:191], v[224:227], v[104:107]\n"
        "s_waitcnt lgkmcnt(5)\n"
        "v_cvt_pk_bf16_f32 v120, v120, v121\n"
        "v_cvt_pk_bf16_f32 v121, v122, v123\n"
        "v_mfma_f32_16x16x32_bf16 v[176:179], v[156:159], a[216:219], 0\n"
        "s_waitcnt lgkmcnt(4)\n"
        "ds_write_b64 v246, v[120:121] offset:256\n"
        "v_mfma_f32_16x16x32_bf16 v[176:179], v[160:163], a[220:223], v[176:179]\n"
        "ds_read_b128 v[222:225], v252 offset:4608\n"
        "v_max3_u32 v236, v236, v124, v125\n"
        "v_mfma_f32_16x16x32_bf16 v[196:199], v[164:167], a[220:223], 0\n"
        "s_waitcnt lgkmcnt(3)\n"
        "ds_read_b128 a[232:235], v248 offset:32\n"
        "v_add_f32 v117, v116, v117\n"
        "v_mfma_f32_16x16x32_bf16 v[196:199], v[152:155], a[216:219], v[196:199]\n"
        "ds_read_b128 a[220:223], v248 offset:3104\n"
        "v_mfma_f32_16x16x32_bf16 v[200:203], v[152:155], a[224:227], 0\n"
        "ds_read_b128 a[216:219], v254 offset:48\n"
        "v_mfma_f32_16x16x32_bf16 v[200:203], v[164:167], a[228:231], v[200:203]\n"
        "v_rcp_f32 v116, v117\n"
        "v_mfma_f32_16x16x32_bf16 v[180:183], v[160:163], a[228:231], 0\n"
        "ds_read_b128 a[228:231], v254 offset:3120\n"
        "v_mul_f32 v72, v116, v72\n"
        "v_mfma_f32_16x16x32_bf16 v[180:183], v[156:159], a[224:227], v[180:183]\n"
        "ds_read_b128 a[236:239], v247 offset:8192\n"
        "v_mul_f32 v73, v116, v73\n"
        "v_mfma_f32_16x16x32_bf16 v[108:111], v[192:195], v[216:219], v[56:59]\n"
        "s_waitcnt lgkmcnt(5)\n"
        "ds_read_b128 a[224:227], v248 offset:64\n"
        "v_mul_f32 v74, v116, v74\n"
        "v_mfma_f32_16x16x32_bf16 v[108:111], v[208:211], v[220:223], v[108:111]\n"
        "ds_read_b128 a[240:243], v247 offset:12288\n"
        "v_mul_f32 v75, v116, v75\n"
        "v_mfma_f32_16x16x32_bf16 v[112:115], v[212:215], v[216:219], v[60:63]\n"
        "ds_read_b128 a[244:247], v247 offset:12544\n"
        "v_cvt_pk_bf16_f32 v72, v72, v73\n"
        "v_mfma_f32_16x16x32_bf16 v[112:115], v[228:231], v[224:227], v[112:115]\n"
        "s_waitcnt lgkmcnt(7)\n"
        "ds_read_b128 a[248:251], v247 offset:8448\n"
        "v_cvt_pk_bf16_f32 v73, v74, v75\n"
        "v_mfma_f32_16x16x32_bf16 v[184:187], v[156:159], a[232:235], 0\n"
        "s_waitcnt lgkmcnt(7)\n"
        "v_exp_f32 v100, v100\n"
        "v_mfma_f32_16x16x32_bf16 v[184:187], v[160:163], a[220:223], v[184:187]\n"
        "v_exp_f32 v101, v101\n"
        "v_mfma_f32_16x16x32_bf16 v[204:207], v[164:167], a[220:223], 0\n"
        "ds_read_b128 a[220:223], v248 offset:3136\n"
        "v_mul_f32 v76, v116, v76\n"
        "v_mfma_f32_16x16x32_bf16 v[204:207], v[152:155], a[232:235], v[204:207]\n"
        "s_waitcnt lgkmcnt(7)\n"
        "ds_read_b128 a[232:235], v254 offset:80\n"
        "v_mul_f32 v77, v116, v77\n"
        "v_mfma_f32_16x16x32_bf16 v[192:195], v[152:155], a[216:219], 0\n"
        "s_waitcnt lgkmcnt(7)\n"
        "v_mul_f32 v78, v116, v78\n"
        "v_mul_f32 v79, v116, v79\n"
        "v_mfma_f32_16x16x32_bf16 v[192:195], v[164:167], a[228:231], v[192:195]\n"
        "v_exp_f32 v102, v102\n"
        "v_mfma_f32_16x16x32_bf16 v[172:175], v[160:163], a[228:231], 0\n"
        "ds_read_b128 a[228:231], v254 offset:3152\n"
        "v_cvt_pk_bf16_f32 v88, v100, v101\n"
        "v_mfma_f32_16x16x32_bf16 v[172:175], v[156:159], a[216:219], v[172:175]\n"
        "s_waitcnt lgkmcnt(6)\n"
        "ds_read_b128 a[216:219], v248 offset:6144\n"
        "v_cvt_pk_bf16_f32 v74, v76, v77\n"
        "v_mfma_f32_16x16x32_bf16 v[212:215], v[156:159], a[224:227], 0\n"
        "s_waitcnt lgkmcnt(3)\n"
        "v_exp_f32 v104, v104\n"
        "v_mfma_f32_16x16x32_bf16 v[212:215], v[160:163], a[220:223], v[212:215]\n"
        "v_exp_f32 v103, v103\n"
        "v_mfma_f32_16x16x32_bf16 v[216:219], v[164:167], a[220:223], 0\n"
        "ds_read_b128 a[220:223], v248 offset:9216\n"
        "v_cvt_pk_bf16_f32 v75, v78, v79\n"
        "v_mfma_f32_16x16x32_bf16 v[216:219], v[152:155], a[224:227], v[216:219]\n"
        "s_waitcnt lgkmcnt(3)\n"
        "ds_write_b128 v245, v[72:75] offset:512\n"
        "v_mfma_f32_16x16x32_bf16 v[224:227], v[152:155], a[232:235], 0\n"
        "s_waitcnt lgkmcnt(3)\n"
        "v_exp_f32 v105, v105\n"
        "v_mfma_f32_16x16x32_bf16 v[224:227], v[164:167], a[228:231], v[224:227]\n"
        "v_cvt_pk_bf16_f32 v89, v102, v103\n"
        "v_cvt_pk_bf16_f32 v90, v104, v105\n"
        "v_mfma_f32_16x16x32_bf16 v[220:223], v[160:163], a[228:231], 0\n"
        "ds_read_b128 a[224:227], v254 offset:6160\n"
        "ds_read_b128 a[228:231], v254 offset:9232\n"
        "v_mfma_f32_16x16x32_bf16 v[220:223], v[156:159], a[232:235], v[220:223]\n"
        "s_waitcnt lgkmcnt(4)\n"
        "ds_read_b128 a[232:235], v248 offset:6176\n"
        "v_mul_f32 v96, v116, v96\n"
        "v_mfma_f32_16x16x32_bf16 v[176:179], a[236:239], a[216:219], v[176:179]\n"
        "s_waitcnt lgkmcnt(4)\n"
        "v_exp_f32 v106, v106\n"
        "v_mfma_f32_16x16x32_bf16 v[176:179], a[240:243], a[220:223], v[176:179]\n"
        "v_mul_f32 v97, v116, v97\n"
        "v_mul_f32 v98, v116, v98\n"
        "v_mfma_f32_16x16x32_bf16 v[196:199], a[244:247], a[220:223], v[196:199]\n"
        "ds_read_b128 a[220:223], v248 offset:9248\n"
        "v_mul_f32 v99, v116, v99\n"
        "v_mfma_f32_16x16x32_bf16 v[196:199], a[248:251], a[216:219], v[196:199]\n"
        "s_waitcnt lgkmcnt(3)\n"
        "ds_read_b128 a[216:219], v254 offset:6192\n"
        "v_cvt_pk_bf16_f32 v96, v96, v97\n"
        "v_mfma_f32_16x16x32_bf16 v[200:203], a[248:251], a[224:227], v[200:203]\n"
        "s_waitcnt lgkmcnt(3)\n"
        "ds_read_b128 v[156:159], v247 offset:16384\n"
        "v_cvt_pk_bf16_f32 v97, v98, v99\n"
        "v_mfma_f32_16x16x32_bf16 v[200:203], a[244:247], a[228:231], v[200:203]\n"
        "ds_write_b64 v246, v[96:97] offset:512\n"
        "v_mfma_f32_16x16x32_bf16 v[180:183], a[240:243], a[228:231], v[180:183]\n"
        "ds_read_b128 a[228:231], v254 offset:9264\n"
        "v_max3_u32 v236, v236, v116, v117\n"
        "v_mfma_f32_16x16x32_bf16 v[180:183], a[236:239], a[224:227], v[180:183]\n"
        "s_waitcnt lgkmcnt(5)\n"
        "ds_read_b128 a[224:227], v248 offset:6208\n"
        "v_mfma_f32_16x16x32_bf16 v[184:187], a[236:239], a[232:235], v[184:187]\n"
        "s_waitcnt lgkmcnt(5)\n"
        "ds_read_b128 v[160:163], v247 offset:20480\n"
        "v_mfma_f32_16x16x32_bf16 v[184:187], a[240:243], a[220:223], v[184:187]\n"
        "ds_read_b128 v[164:167], v247 offset:20736\n"
        "v_mfma_f32_16x16x32_bf16 v[204:207], a[244:247], a[220:223], v[204:207]\n"
        "ds_read_b128 a[220:223], v248 offset:9280\n"
        "v_mfma_f32_16x16x32_bf16 v[204:207], a[248:251], a[232:235], v[204:207]\n"
        "s_waitcnt lgkmcnt(7)\n"
        "ds_read_b128 a[232:235], v254 offset:6224\n"
        "v_mfma_f32_16x16x32_bf16 v[192:195], a[248:251], a[216:219], v[192:195]\n"
        "s_waitcnt lgkmcnt(5)\n"
        "ds_read_b128 v[152:155], v247 offset:16640\n"
        "v_mfma_f32_16x16x32_bf16 v[192:195], a[244:247], a[228:231], v[192:195]\n"
        "v_exp_f32 v107, v107\n"
        "v_mfma_f32_16x16x32_bf16 v[172:175], a[240:243], a[228:231], v[172:175]\n"
        "ds_read_b128 a[228:231], v254 offset:9296\n"
        "v_cvt_pk_bf16_f32 v91, v106, v107\n"
        "v_mfma_f32_16x16x32_bf16 v[172:175], a[236:239], a[216:219], v[172:175]\n"
        "s_waitcnt lgkmcnt(6)\n"
        "ds_read_b128 a[216:219], v248 offset:12288\n"
        "v_mfma_f32_16x16x32_bf16 v[212:215], a[236:239], a[224:227], v[212:215]\n"
        "s_waitcnt lgkmcnt(4)\n"
        "v_exp_f32 v108, v108\n"
        "v_mfma_f32_16x16x32_bf16 v[212:215], a[240:243], a[220:223], v[212:215]\n"
        "v_exp_f32 v109, v109\n"
        "v_mfma_f32_16x16x32_bf16 v[216:219], a[244:247], a[220:223], v[216:219]\n"
        "ds_read_b128 a[220:223], v248 offset:15360\n"
        "v_cvt_pk_bf16_f32 v92, v108, v109\n"
        "v_mfma_f32_16x16x32_bf16 v[216:219], a[248:251], a[224:227], v[216:219]\n"
        "s_waitcnt lgkmcnt(4)\n"
        "ds_read_b128 a[224:227], v254 offset:12304\n"
        "v_mfma_f32_16x16x32_bf16 v[224:227], a[248:251], a[232:235], v[224:227]\n"
        "s_waitcnt lgkmcnt(3)\n"
        "v_exp_f32 v110, v110\n"
        "v_mfma_f32_16x16x32_bf16 v[224:227], a[244:247], a[228:231], v[224:227]\n"
        "v_exp_f32 v112, v112\n"
        "v_mfma_f32_16x16x32_bf16 v[220:223], a[240:243], a[228:231], v[220:223]\n"
        "ds_read_b128 a[228:231], v254 offset:15376\n"
        "v_mfma_f32_16x16x32_bf16 v[220:223], a[236:239], a[232:235], v[220:223]\n"
        "s_waitcnt lgkmcnt(3)\n"
        "ds_read_b128 a[232:235], v248 offset:12320\n"
        "v_mfma_f32_16x16x32_bf16 v[176:179], v[156:159], a[216:219], v[176:179]\n"
        "s_waitcnt lgkmcnt(3)\n"
        "v_exp_f32 v111, v111\n"
        "v_mfma_f32_16x16x32_bf16 v[176:179], v[160:163], a[220:223], v[176:179]\n"
        "v_exp_f32 v113, v113\n"
        "v_mfma_f32_16x16x32_bf16 v[196:199], v[164:167], a[220:223], v[196:199]\n"
        "ds_read_b128 a[220:223], v248 offset:15392\n"
        "v_cvt_pk_bf16_f32 v93, v110, v111\n"
        "v_mfma_f32_16x16x32_bf16 v[196:199], v[152:155], a[216:219], v[196:199]\n"
        "s_waitcnt lgkmcnt(3)\n"
        "ds_read_b128 a[216:219], v254 offset:12336\n"
        "v_cvt_pk_bf16_f32 v94, v112, v113\n"
        "v_mfma_f32_16x16x32_bf16 v[200:203], v[152:155], a[224:227], v[200:203]\n"
        "s_waitcnt lgkmcnt(3)\n"
        "v_exp_f32 v114, v114\n"
        "v_mfma_f32_16x16x32_bf16 v[200:203], v[164:167], a[228:231], v[200:203]\n"
        "v_exp_f32 v115, v115\n"
        "v_mfma_f32_16x16x32_bf16 v[180:183], v[160:163], a[228:231], v[180:183]\n"
        "s_waitcnt vmcnt(0)\n"
        "ds_read_b128 a[228:231], v254 offset:15408\n"
        "v_cvt_pk_bf16_f32 v95, v114, v115\n"
        "v_mfma_f32_16x16x32_bf16 v[180:183], v[156:159], a[224:227], v[180:183]\n"
        "s_waitcnt lgkmcnt(3)\n"
        "ds_read_b128 a[224:227], v248 offset:12352\n"
        "v_mfma_f32_16x16x32_bf16 v[184:187], v[156:159], a[232:235], v[184:187]\n"
        "s_waitcnt lgkmcnt(3)\n"
        "v_mfma_f32_16x16x32_bf16 v[184:187], v[160:163], a[220:223], v[184:187]\n"
        "s_barrier\n"
        "v_mfma_f32_16x16x32_bf16 v[204:207], v[164:167], a[220:223], v[204:207]\n"
        "ds_read_b128 a[220:223], v248 offset:15424\n"
        "v_mfma_f32_16x16x32_bf16 v[204:207], v[152:155], a[232:235], v[204:207]\n"
        "s_waitcnt lgkmcnt(3)\n"
        "ds_read_b128 a[232:235], v254 offset:12368\n"
        "v_mfma_f32_16x16x32_bf16 v[192:195], v[152:155], a[216:219], v[192:195]\n"
        "s_waitcnt lgkmcnt(3)\n"
        "ds_read_b128 a[236:239], v240 offset:0\n"
        "v_mfma_f32_16x16x32_bf16 v[192:195], v[164:167], a[228:231], v[192:195]\n"
        "ds_read_b128 a[240:243], v241 offset:0\n"
        "v_mfma_f32_16x16x32_bf16 v[172:175], v[160:163], a[228:231], v[172:175]\n"
        "ds_read_b128 a[228:231], v254 offset:15440\n"
        "v_mfma_f32_16x16x32_bf16 v[172:175], v[156:159], a[216:219], v[172:175]\n"
        "s_waitcnt lgkmcnt(5)\n"
        "ds_read_b128 a[244:247], v240 offset:128\n"
        "v_mfma_f32_16x16x32_bf16 v[212:215], v[156:159], a[224:227], v[212:215]\n"
        "s_waitcnt lgkmcnt(5)\n"
        "ds_read_b128 a[248:251], v241 offset:128\n"
        "v_mfma_f32_16x16x32_bf16 v[212:215], v[160:163], a[220:223], v[212:215]\n"
        "ds_read_b128 a[216:219], v240 offset:256\n"
        "v_mfma_f32_16x16x32_bf16 v[216:219], v[164:167], a[220:223], v[216:219]\n"
        "ds_read_b128 a[220:223], v241 offset:256\n"
        "v_mfma_f32_16x16x32_bf16 v[216:219], v[152:155], a[224:227], v[216:219]\n"
        "s_waitcnt lgkmcnt(7)\n"
        "v_mfma_f32_16x16x32_bf16 v[224:227], v[152:155], a[232:235], v[224:227]\n"
        "s_waitcnt lgkmcnt(4)\n"
        "v_mfma_f32_16x16x32_bf16 v[224:227], v[164:167], a[228:231], v[224:227]\n"
        "s_sub_u32 s27, s27, 1\n"
        "v_mfma_f32_16x16x32_bf16 v[220:223], v[160:163], a[228:231], v[220:223]\n"
        "s_cmp_lg_u32 s27, 0\n"
        "v_mfma_f32_16x16x32_bf16 v[220:223], v[156:159], a[232:235], v[220:223]\n"
        "s_cbranch_scc1 10b\n"
        "19:\n"
        ".long 0xbec200ff\n"
        "54:\n"
        "s_branch 61b\n"
        "s_waitcnt lgkmcnt(0)\n"
        "s_and_b32 s46, s42, 1\n"
        "s_lshl_b32 s31, s46, 8\n"
        "s_sub_u32 s31, s45, s31\n"
        "v_add_u32 v247, s31, v247\n"
        "ds_read_b128 a[144:147], v247 offset:0\n"
        "ds_read_b128 a[0:3], v248 offset:0\n"
        "ds_read_b128 a[148:151], v247 offset:4096\n"
        "ds_read_b128 a[4:7], v248 offset:3072\n"
        "v_mfma_f32_16x16x32_bf16 v[80:83], v[132:135], v[92:95], 0\n"
        "ds_read_b128 a[152:155], v247 offset:8192\n"
        "ds_read_b128 a[8:11], v248 offset:6144\n"
        "s_mul_i32 s45, s46, 786432\n"
        "v_mov_b64 v[124:125], 0\n"
        "v_mfma_f32_16x16x32_bf16 v[80:83], v[128:131], v[88:91], v[80:83]\n"
        "ds_read_b128 a[156:159], v247 offset:12288\n"
        "ds_read_b128 a[12:15], v248 offset:9216\n"
        "s_sub_u32 s37, s37, s45\n"
        "v_cvt_pk_bf16_f32 v232, v176, v180\n"
        "v_cvt_pk_bf16_f32 v233, v184, v172\n"
        "v_mfma_f32_16x16x32_bf16 v[84:87], v[136:139], v[88:91], 0\n"
        "ds_read_b128 a[160:163], v247 offset:16384\n"
        "ds_read_b128 a[16:19], v248 offset:12288\n"
        "s_cmp_eq_u32 s46, 0\n"
        "s_cselect_b32 s43, s43, s44\n"
        "v_cvt_pk_bf16_f32 v234, v212, v220\n"
        "v_mfma_f32_16x16x32_bf16 v[84:87], v[140:143], v[92:95], v[84:87]\n"
        "buffer_store_dwordx3 v[232:234], v250, s[16:19], s38 offen offset:0 sc1\n"
        "s_add_u32 s28, s37, 786432\n"
        "v_mfma_f32_16x16x32_bf16 v[96:99], v[148:151], v[92:95], 0\n"
        "ds_read_b128 a[164:167], v247 offset:20480\n"
        "ds_read_b128 a[20:23], v248 offset:15360\n"
        "ds_read_b128 a[44:47], v254 offset:15376\n"
        "s_cmp_eq_u32 s28, 100663296\n"
        "s_cselect_b32 s28, 0, s28\n"
        "v_mfma_f32_16x16x32_bf16 v[96:99], v[144:147], v[88:91], v[96:99]\n"
        "ds_read_b128 a[40:43], v254 offset:12304\n"
        "ds_read_b128 a[36:39], v254 offset:9232\n"
        "s_lshl_b32 s29, s42, 2\n"
        "v_cvt_pk_bf16_f32 v232, v177, v181\n"
        "v_cvt_pk_bf16_f32 v233, v185, v173\n"
        "v_smfmac_f32_16x16x64_bf16 v[124:127], a[252:255], v[88:95], v237\n"
        "s_waitcnt lgkmcnt(13)\n"
        "ds_read_b128 a[32:35], v254 offset:6160\n"
        "v_cvt_pk_bf16_f32 v234, v213, v221\n"
        "v_mfma_f32_16x16x32_bf16 v[0:3], a[144:147], a[0:3], 0\n"
        "s_waitcnt lgkmcnt(12)\n"
        "buffer_store_dwordx3 v[232:234], v250, s[16:19], s38 offen offset:384 sc1\n"
        "s_add_u32 s29, s29, 147456\n"
        "v_mfma_f32_16x16x32_bf16 v[0:3], a[148:151], a[4:7], v[0:3]\n"
        "s_waitcnt lgkmcnt(10)\n"
        "ds_read_b128 a[28:31], v254 offset:3088\n"
        "ds_read_b128 a[24:27], v254 offset:16\n"
        "v_cvt_pk_bf16_f32 v232, v178, v182\n"
        "v_cvt_pk_bf16_f32 v233, v186, v174\n"
        "v_mfma_f32_16x16x32_bf16 v[0:3], a[152:155], a[8:11], v[0:3]\n"
        "s_waitcnt lgkmcnt(10)\n"
        "ds_read_b128 a[48:51], v248 offset:32\n"
        "ds_read_b128 a[52:55], v248 offset:3104\n"
        "v_cvt_pk_bf16_f32 v234, v214, v222\n"
        "v_mfma_f32_16x16x32_bf16 v[0:3], a[156:159], a[12:15], v[0:3]\n"
        "s_waitcnt lgkmcnt(10)\n"
        "buffer_store_dwordx3 v[232:234], v250, s[16:19], s38 offen offset:768 sc1\n"
        "v_mfma_f32_16x16x32_bf16 v[0:3], a[160:163], a[16:19], v[0:3]\n"
        "s_waitcnt lgkmcnt(8)\n"
        "ds_read_b128 a[56:59], v248 offset:6176\n"
        "ds_read_b128 a[60:63], v248 offset:9248\n"
        "v_cvt_pk_bf16_f32 v232, v179, v183\n"
        "v_cvt_pk_bf16_f32 v233, v187, v175\n"
        "v_mfma_f32_16x16x32_bf16 v[0:3], a[164:167], a[20:23], v[0:3]\n"
        "s_waitcnt lgkmcnt(9)\n"
        "ds_read_b128 a[64:67], v248 offset:12320\n"
        "ds_read_b128 a[68:71], v248 offset:15392\n"
        "v_cvt_pk_bf16_f32 v234, v215, v223\n"
        "v_add_f32 v125, v124, v125\n"
        "v_mfma_f32_16x16x32_bf16 v[4:7], a[164:167], a[44:47], 0\n"
        "s_waitcnt lgkmcnt(10)\n"
        "buffer_store_dwordx3 v[232:234], v250, s[16:19], s38 offen offset:1152 sc1\n"
        "v_mfma_f32_16x16x32_bf16 v[4:7], a[160:163], a[40:43], v[4:7]\n"
        "s_waitcnt lgkmcnt(9)\n"
        "ds_read_b128 a[92:95], v254 offset:15408\n"
        "ds_read_b128 a[88:91], v254 offset:12336\n"
        "v_rcp_f32 v124, v125\n"
        "v_mfma_f32_16x16x32_bf16 v[4:7], a[156:159], a[36:39], v[4:7]\n"
        "s_waitcnt lgkmcnt(10)\n"
        "ds_read_b128 a[84:87], v254 offset:9264\n"
        "ds_read_b128 a[80:83], v254 offset:6192\n"
        "v_mul_f32 v80, v124, v80\n"
        "v_mul_f32 v81, v124, v81\n"
        "v_mfma_f32_16x16x32_bf16 v[4:7], a[152:155], a[32:35], v[4:7]\n"
        "s_waitcnt lgkmcnt(11)\n"
        "ds_read_b128 a[76:79], v254 offset:3120\n"
        "ds_read_b128 a[72:75], v254 offset:48\n"
        "v_mul_f32 v82, v124, v82\n"
        "v_mul_f32 v83, v124, v83\n"
        "v_mfma_f32_16x16x32_bf16 v[4:7], a[148:151], a[28:31], v[4:7]\n"
        "s_waitcnt lgkmcnt(12)\n"
        "ds_read_b128 a[96:99], v248 offset:64\n"
        "ds_read_b128 a[100:103], v248 offset:3136\n"
        "v_cvt_pk_bf16_f32 v232, v196, v200\n"
        "v_cvt_pk_bf16_f32 v233, v204, v192\n"
        "v_mfma_f32_16x16x32_bf16 v[4:7], a[144:147], a[24:27], v[4:7]\n"
        "s_waitcnt lgkmcnt(13)\n"
        "ds_read_b128 a[104:107], v248 offset:6208\n"
        "ds_read_b128 a[108:111], v248 offset:9280\n"
        "v_cvt_pk_bf16_f32 v234, v216, v224\n"
        "v_cvt_pk_bf16_f32 v80, v80, v81\n"
        "v_mfma_f32_16x16x32_bf16 v[8:11], a[144:147], a[48:51], 0\n"
        "s_waitcnt lgkmcnt(14)\n"
        "buffer_store_dwordx3 v[232:234], v250, s[16:19], s39 offen offset:0 sc1\n"
        "v_mfma_f32_16x16x32_bf16 v[8:11], a[148:151], a[52:55], v[8:11]\n"
        "s_waitcnt lgkmcnt(13)\n"
        "ds_read_b128 a[112:115], v248 offset:12352\n"
        "ds_read_b128 a[116:119], v248 offset:15424\n"
        "v_cvt_pk_bf16_f32 v81, v82, v83\n"
        "v_mul_f32 v84, v124, v84\n"
        "v_mfma_f32_16x16x32_bf16 v[8:11], a[152:155], a[56:59], v[8:11]\n"
        "s_waitcnt lgkmcnt(14)\n"
        "ds_read_b128 a[140:143], v254 offset:15440\n"
        "ds_read_b128 a[136:139], v254 offset:12368\n"
        "v_mul_f32 v85, v124, v85\n"
        "v_mul_f32 v86, v124, v86\n"
        "v_mfma_f32_16x16x32_bf16 v[8:11], a[156:159], a[60:63], v[8:11]\n"
        "s_waitcnt lgkmcnt(15)\n"
        "ds_read_b128 a[132:135], v254 offset:9296\n"
        "ds_read_b128 a[128:131], v254 offset:6224\n"
        "v_mul_f32 v87, v124, v87\n"
        "v_cvt_pk_bf16_f32 v232, v197, v201\n"
        "v_mfma_f32_16x16x32_bf16 v[8:11], a[160:163], a[64:67], v[8:11]\n"
        "s_waitcnt lgkmcnt(15)\n"
        "ds_read_b128 a[124:127], v254 offset:3152\n"
        "ds_read_b128 a[120:123], v254 offset:80\n"
        "v_cvt_pk_bf16_f32 v233, v205, v193\n"
        "v_cvt_pk_bf16_f32 v234, v217, v225\n"
        "v_mfma_f32_16x16x32_bf16 v[8:11], a[164:167], a[68:71], v[8:11]\n"
        "s_waitcnt lgkmcnt(15)\n"
        "buffer_store_dwordx3 v[232:234], v250, s[16:19], s39 offen offset:384 sc1\n"
        "v_mfma_f32_16x16x32_bf16 v[12:15], a[164:167], a[92:95], 0\n"
        "s_waitcnt lgkmcnt(15)\n"
        "v_cvt_pk_bf16_f32 v82, v84, v85\n"
        "v_cvt_pk_bf16_f32 v83, v86, v87\n"
        "v_mul_f32 v96, v124, v96\n"
        "v_mul_f32 v97, v124, v97\n"
        "v_mfma_f32_16x16x32_bf16 v[12:15], a[160:163], a[88:91], v[12:15]\n"
        "s_waitcnt lgkmcnt(15)\n"
        "ds_write_b128 v245, v[80:83] offset:768\n"
        "v_mfma_f32_16x16x32_bf16 v[12:15], a[156:159], a[84:87], v[12:15]\n"
        "s_waitcnt lgkmcnt(15)\n"
        "v_mul_f32 v98, v124, v98\n"
        "v_mul_f32 v99, v124, v99\n"
        "v_cvt_pk_bf16_f32 v232, v198, v202\n"
        "v_cvt_pk_bf16_f32 v233, v206, v194\n"
        "v_mfma_f32_16x16x32_bf16 v[12:15], a[152:155], a[80:83], v[12:15]\n"
        "s_waitcnt lgkmcnt(14)\n"
        "v_cvt_pk_bf16_f32 v234, v218, v226\n"
        "v_cvt_pk_bf16_f32 v96, v96, v97\n"
        "v_cvt_pk_bf16_f32 v97, v98, v99\n"
        "v_mov_b32 v100, s29\n"
        "v_mfma_f32_16x16x32_bf16 v[12:15], a[148:151], a[76:79], v[12:15]\n"
        "s_waitcnt lgkmcnt(13)\n"
        "buffer_store_dwordx3 v[232:234], v250, s[16:19], s39 offen offset:768 sc1\n"
        "v_mfma_f32_16x16x32_bf16 v[12:15], a[144:147], a[72:75], v[12:15]\n"
        "s_waitcnt lgkmcnt(12)\n"
        "ds_write_b64 v246, v[96:97] offset:768\n"
        "v_cvt_pk_bf16_f32 v232, v199, v203\n"
        "v_mfma_f32_16x16x32_bf16 v[16:19], a[144:147], a[96:99], 0\n"
        "s_waitcnt lgkmcnt(12)\n"
        "v_cvt_pk_bf16_f32 v233, v207, v195\n"
        "v_cvt_pk_bf16_f32 v234, v219, v227\n"
        "v_max3_u32 v236, v236, v124, v125\n"
        "v_cmp_lt_u32 vcc, s24, v236\n"
        "v_mfma_f32_16x16x32_bf16 v[16:19], a[148:151], a[100:103], v[16:19]\n"
        "s_waitcnt lgkmcnt(11)\n"
        "buffer_store_dwordx3 v[232:234], v250, s[16:19], s39 offen offset:1152 sc1\n"
        "s_cmp_lg_u64 vcc, 0\n"
        "s_addc_u32 s25, s25, s25\n"
        "v_mfma_f32_16x16x32_bf16 v[16:19], a[152:155], a[104:107], v[16:19]\n"
        "s_waitcnt lgkmcnt(10)\n"
        "v_mov_b32 v101, s25\n"
        "v_mov_b32 v102, 147456\n"
        "v_mfma_f32_16x16x32_bf16 v[16:19], a[156:159], a[108:111], v[16:19]\n"
        "ds_write_b32 v100, v101\n"
        "s_waitcnt lgkmcnt(0)\n"
        "s_barrier\n"
        "ds_read_b128 a[168:171], v247 offset:512\n"
        "v_mfma_f32_16x16x32_bf16 v[16:19], a[160:163], a[112:115], v[16:19]\n"
        "ds_read_b128 a[172:175], v247 offset:4608\n"
        "ds_read_b128 a[176:179], v247 offset:8704\n"
        "v_mfma_f32_16x16x32_bf16 v[16:19], a[164:167], a[116:119], v[16:19]\n"
        "ds_read_b128 a[180:183], v247 offset:12800\n"
        "ds_read_b128 a[184:187], v247 offset:16896\n"
        "v_mfma_f32_16x16x32_bf16 v[20:23], a[164:167], a[140:143], 0\n"
        "ds_read_b128 a[188:191], v247 offset:20992\n"
        "v_cvt_pk_bf16_f32 v48, v0, v4\n"
        "v_mfma_f32_16x16x32_bf16 v[20:23], a[160:163], a[136:139], v[20:23]\n"
        "ds_read_b128 v[104:107], v102\n"
        "v_cvt_pk_bf16_f32 v49, v8, v12\n"
        "v_mfma_f32_16x16x32_bf16 v[20:23], a[156:159], a[132:135], v[20:23]\n"
        "s_add_u32 s38, s37, s43\n"
        "v_cvt_pk_bf16_f32 v52, v1, v5\n"
        "v_cvt_pk_bf16_f32 v53, v9, v13\n"
        "v_mfma_f32_16x16x32_bf16 v[20:23], a[152:155], a[128:131], v[20:23]\n"
        "s_add_u32 s39, s28, s43\n"
        "v_cvt_pk_bf16_f32 v56, v2, v6\n"
        "v_cvt_pk_bf16_f32 v57, v10, v14\n"
        "v_mfma_f32_16x16x32_bf16 v[20:23], a[148:151], a[124:127], v[20:23]\n"
        "v_cvt_pk_bf16_f32 v60, v3, v7\n"
        "v_cvt_pk_bf16_f32 v61, v11, v15\n"
        "v_mfma_f32_16x16x32_bf16 v[20:23], a[144:147], a[120:123], v[20:23]\n"
        "s_waitcnt lgkmcnt(6)\n"
        "v_mfma_f32_16x16x32_bf16 v[24:27], a[168:171], a[0:3], 0\n"
        "s_waitcnt lgkmcnt(5)\n"
        "v_mfma_f32_16x16x32_bf16 v[24:27], a[172:175], a[4:7], v[24:27]\n"
        "s_waitcnt lgkmcnt(4)\n"
        "v_mfma_f32_16x16x32_bf16 v[24:27], a[176:179], a[8:11], v[24:27]\n"
        "s_waitcnt lgkmcnt(3)\n"
        "v_mfma_f32_16x16x32_bf16 v[24:27], a[180:183], a[12:15], v[24:27]\n"
        "s_waitcnt lgkmcnt(2)\n"
        "v_mfma_f32_16x16x32_bf16 v[24:27], a[184:187], a[16:19], v[24:27]\n"
        "s_waitcnt lgkmcnt(1)\n"
        "v_mfma_f32_16x16x32_bf16 v[24:27], a[188:191], a[20:23], v[24:27]\n"
        "v_mfma_f32_16x16x32_bf16 v[28:31], a[188:191], a[44:47], 0\n"
        "s_nop 0\n"
        "v_cvt_pk_bf16_f32 v50, v16, v20\n"
        "v_cvt_pk_bf16_f32 v54, v17, v21\n"
        "v_mfma_f32_16x16x32_bf16 v[28:31], a[184:187], a[40:43], v[28:31]\n"
        "v_cvt_pk_bf16_f32 v58, v18, v22\n"
        "v_cvt_pk_bf16_f32 v62, v19, v23\n"
        "v_mfma_f32_16x16x32_bf16 v[28:31], a[180:183], a[36:39], v[28:31]\n"
        "v_mfma_f32_16x16x32_bf16 v[28:31], a[176:179], a[32:35], v[28:31]\n"
        "s_waitcnt lgkmcnt(0)\n"
        "v_or3_b32 v104, v104, v105, v106\n"
        "v_or_b32 v104, v104, v107\n"
        "v_mfma_f32_16x16x32_bf16 v[28:31], a[172:175], a[28:31], v[28:31]\n"
        "s_nop 0\n"
        "v_readfirstlane_b32 s25, v104\n"
        "v_mfma_f32_16x16x32_bf16 v[28:31], a[168:171], a[24:27], v[28:31]\n"
        "v_mfma_f32_16x16x32_bf16 v[32:35], a[168:171], a[48:51], 0\n"
        "v_mfma_f32_16x16x32_bf16 v[32:35], a[172:175], a[52:55], v[32:35]\n"
        "buffer_store_dwordx3 v[48:50], v250, s[16:19], s38 offen offset:0 sc1\n"
        "v_mfma_f32_16x16x32_bf16 v[32:35], a[176:179], a[56:59], v[32:35]\n"
        "v_mfma_f32_16x16x32_bf16 v[32:35], a[180:183], a[60:63], v[32:35]\n"
        "v_mfma_f32_16x16x32_bf16 v[32:35], a[184:187], a[64:67], v[32:35]\n"
        "v_mfma_f32_16x16x32_bf16 v[32:35], a[188:191], a[68:71], v[32:35]\n"
        "v_mfma_f32_16x16x32_bf16 v[36:39], a[188:191], a[92:95], 0\n"
        "buffer_store_dwordx3 v[52:54], v250, s[16:19], s38 offen offset:384 sc1\n"
        "v_mfma_f32_16x16x32_bf16 v[36:39], a[184:187], a[88:91], v[36:39]\n"
        "v_cvt_pk_bf16_f32 v64, v24, v28\n"
        "v_cvt_pk_bf16_f32 v68, v25, v29\n"
        "v_mfma_f32_16x16x32_bf16 v[36:39], a[180:183], a[84:87], v[36:39]\n"
        "v_cvt_pk_bf16_f32 v72, v26, v30\n"
        "v_cvt_pk_bf16_f32 v76, v27, v31\n"
        "v_mfma_f32_16x16x32_bf16 v[36:39], a[176:179], a[80:83], v[36:39]\n"
        "v_mfma_f32_16x16x32_bf16 v[36:39], a[172:175], a[76:79], v[36:39]\n"
        "v_mfma_f32_16x16x32_bf16 v[36:39], a[168:171], a[72:75], v[36:39]\n"
        "buffer_store_dwordx3 v[56:58], v250, s[16:19], s38 offen offset:768 sc1\n"
        "v_mfma_f32_16x16x32_bf16 v[40:43], a[168:171], a[96:99], 0\n"
        "v_mfma_f32_16x16x32_bf16 v[40:43], a[172:175], a[100:103], v[40:43]\n"
        "v_mfma_f32_16x16x32_bf16 v[40:43], a[176:179], a[104:107], v[40:43]\n"
        "v_mfma_f32_16x16x32_bf16 v[40:43], a[180:183], a[108:111], v[40:43]\n"
        "v_mfma_f32_16x16x32_bf16 v[40:43], a[184:187], a[112:115], v[40:43]\n"
        "buffer_store_dwordx3 v[60:62], v250, s[16:19], s38 offen offset:1152 sc1\n"
        "v_mfma_f32_16x16x32_bf16 v[40:43], a[188:191], a[116:119], v[40:43]\n"
        "v_cvt_pk_bf16_f32 v65, v32, v36\n"
        "v_cvt_pk_bf16_f32 v69, v33, v37\n"
        "v_mfma_f32_16x16x32_bf16 v[44:47], a[188:191], a[140:143], 0\n"
        "v_cvt_pk_bf16_f32 v73, v34, v38\n"
        "v_cvt_pk_bf16_f32 v77, v35, v39\n"
        "v_mfma_f32_16x16x32_bf16 v[44:47], a[184:187], a[136:139], v[44:47]\n"
        "v_mfma_f32_16x16x32_bf16 v[44:47], a[180:183], a[132:135], v[44:47]\n"
        "v_mfma_f32_16x16x32_bf16 v[44:47], a[176:179], a[128:131], v[44:47]\n"
        "v_mfma_f32_16x16x32_bf16 v[44:47], a[172:175], a[124:127], v[44:47]\n"
        "v_mfma_f32_16x16x32_bf16 v[44:47], a[168:171], a[120:123], v[44:47]\n"
        "s_nop 2\n"
        "s_nop 4\n"
        "v_cvt_pk_bf16_f32 v66, v40, v44\n"
        "buffer_store_dwordx3 v[64:66], v250, s[16:19], s39 offen offset:0 sc1\n"
        "v_cvt_pk_bf16_f32 v70, v41, v45\n"
        "buffer_store_dwordx3 v[68:70], v250, s[16:19], s39 offen offset:384 sc1\n"
        "v_cvt_pk_bf16_f32 v74, v42, v46\n"
        "buffer_store_dwordx3 v[72:74], v250, s[16:19], s39 offen offset:768 sc1\n"
        "v_cvt_pk_bf16_f32 v78, v43, v47\n"
        "buffer_store_dwordx3 v[76:78], v250, s[16:19], s39 offen offset:1152 sc1\n"
        "s_cmp_lg_u32 s25, 0\n"
        "s_cbranch_scc1 30f\n"
        "s_endpgm\n"
        "30:\n"
        : "={s25}"(flg), "={s42}"(w), "+{s[0:1]}"(ka), "+{s2}"(bid), "+{v0}"(tid)
        : "s"(ldsb)
        : "v1", "v2", "v3", "v4", "v5", "v6", "v7", "v8", "v9", "v10", "v11", "v12", "v13", "v14", "v15", "v16", "v17", "v18", "v19", "v20", "v21", "v22", "v23", "v24", "v25", "v26", "v27", "v28", "v29", "v30", "v31", "v32", "v33", "v34", "v35", "v36", "v37", "v38", "v39", "v40", "v41", "v42", "v43", "v44", "v45", "v46", "v47", "v48", "v49", "v50", "v51", "v52", "v53", "v54", "v55", "v56", "v57", "v58", "v59", "v60", "v61", "v62", "v63", "v64", "v65", "v66", "v67", "v68", "v69", "v70", "v71", "v72", "v73", "v74", "v75", "v76", "v77", "v78", "v79", "v80", "v81", "v82", "v83", "v84", "v85", "v86", "v87", "v88", "v89", "v90", "v91", "v92", "v93", "v94", "v95", "v96", "v97", "v98", "v99", "v100", "v101", "v102", "v103", "v104", "v105", "v106", "v107", "v108", "v109", "v110", "v111", "v112", "v113", "v114", "v115", "v116", "v117", "v118", "v119", "v120", "v121", "v122", "v123", "v124", "v125", "v126", "v127", "v128", "v129", "v130", "v131", "v132", "v133", "v134", "v135", "v136", "v137", "v138", "v139", "v140", "v141", "v142", "v143", "v144", "v145", "v146", "v147", "v148", "v149", "v150", "v151", "v152", "v153", "v154", "v155", "v156", "v157", "v158", "v159", "v160", "v161", "v162", "v163", "v164", "v165", "v166", "v167", "v168", "v169", "v170", "v171", "v172", "v173", "v174", "v175", "v176", "v177", "v178", "v179", "v180", "v181", "v182", "v183", "v184", "v185", "v186", "v187", "v188", "v189", "v190", "v191", "v192", "v193", "v194", "v195", "v196", "v197", "v198", "v199", "v200", "v201", "v202", "v203", "v204", "v205", "v206", "v207", "v208", "v209", "v210", "v211", "v212", "v213", "v214", "v215", "v216", "v217", "v218", "v219", "v220", "v221", "v222", "v223", "v224", "v225", "v226", "v227", "v228", "v229", "v230", "v231", "v232", "v233", "v234", "v235", "v236", "v237", "v238", "v239", "v240", "v241", "v242", "v243", "v244", "v245", "v246", "v247", "v248", "v249", "v250", "v251", "v252", "v253", "v254", "v255", "a0", "a1", "a2", "a3", "a4", "a5", "a6", "a7", "a8", "a9", "a10", "a11", "a12", "a13", "a14", "a15", "a16", "a17", "a18", "a19", "a20", "a21", "a22", "a23", "a24", "a25", "a26", "a27", "a28", "a29", "a30", "a31", "a32", "a33", "a34", "a35", "a36", "a37", "a38", "a39", "a40", "a41", "a42", "a43", "a44", "a45", "a46", "a47", "a48", "a49", "a50", "a51", "a52", "a53", "a54", "a55", "a56", "a57", "a58", "a59", "a60", "a61", "a62", "a63", "a64", "a65", "a66", "a67", "a68", "a69", "a70", "a71", "a72", "a73", "a74", "a75", "a76", "a77", "a78", "a79", "a80", "a81", "a82", "a83", "a84", "a85", "a86", "a87", "a88", "a89", "a90", "a91", "a92", "a93", "a94", "a95", "a96", "a97", "a98", "a99", "a100", "a101", "a102", "a103", "a104", "a105", "a106", "a107", "a108", "a109", "a110", "a111", "a112", "a113", "a114", "a115", "a116", "a117", "a118", "a119", "a120", "a121", "a122", "a123", "a124", "a125", "a126", "a127", "a128", "a129", "a130", "a131", "a132", "a133", "a134", "a135", "a136", "a137", "a138", "a139", "a140", "a141", "a142", "a143", "a144", "a145", "a146", "a147", "a148", "a149", "a150", "a151", "a152", "a153", "a154", "a155", "a156", "a157", "a158", "a159", "a160", "a161", "a162", "a163", "a164", "a165", "a166", "a167", "a168", "a169", "a170", "a171", "a172", "a173", "a174", "a175", "a176", "a177", "a178", "a179", "a180", "a181", "a182", "a183", "a184", "a185", "a186", "a187", "a188", "a189", "a190", "a191", "a192", "a193", "a194", "a195", "a196", "a197", "a198", "a199", "a200", "a201", "a202", "a203", "a204", "a205", "a206", "a207", "a208", "a209", "a210", "a211", "a212", "a213", "a214", "a215", "a216", "a217", "a218", "a219", "a220", "a221", "a222", "a223", "a224", "a225", "a226", "a227", "a228", "a229", "a230", "a231", "a232", "a233", "a234", "a235", "a236", "a237", "a238", "a239", "a240", "a241", "a242", "a243", "a244", "a245", "a246", "a247", "a248", "a249", "a250", "a251", "a252", "a253", "a254", "a255", "s8", "s9", "s10", "s11", "s12", "s13", "s14", "s15", "s16", "s17", "s18", "s19", "s20", "s21", "s22", "s23", "s24", "s26", "s27", "s28", "s29", "s30", "s31", "s34", "s35", "s36", "s37", "s38", "s39", "s40", "s41", "s43", "s44", "s45", "s46", "s47", "s48", "s49", "s50", "s51", "s52", "s53", "s54", "s55", "s56", "s57", "s58", "s59", "s60", "s61", "s62", "s63", "s64", "s65", "s66", "s67", "s68", "s69", "s70", "s71", "s72", "s73", "s74", "s75", "s76", "s77", "s78", "s79", "s80", "s81", "s82", "s83", "vcc", "scc", "memory");
    if (flg) {
        unsigned ln;
        asm volatile("s_waitcnt vmcnt(0)\n v_mbcnt_lo_u32_b32 %0, -1, 0\n v_mbcnt_hi_u32_b32 %0, -1, %0" : "=v"(ln) :: "memory");
        const unsigned t = w * 64u + ln, wx = bid & 63u, wy0 = bid >> 6;
        typedef const char* __attribute__((address_space(4))) const* kargp;
        const char *x = ((kargp)ka)[0], *wqkv = ((kargp)ka)[1], *wo = ((kargp)ka)[2], *rpb = ((kargp)ka)[3], *rpi = ((kargp)ka)[4],
                   *mask = ((kargp)ka)[5];
        char* out = (char*)((kargp)ka)[6];
        unsigned short* xs = (unsigned short*)smem;
        unsigned short* qs = xs + 64 * 192;
        const unsigned short* W1 = (const unsigned short*)wqkv;
        const unsigned short* W2 = (const unsigned short*)wo;
        for (unsigned i = 0; i < 16u; ++i) {
            if (!((flg >> (15u - i)) & 1u)) continue;
            const unsigned wy = wy0 + 4u * i;
            __syncthreads();
            for (unsigned e = t; e < 64u * 96u; e += 256u) {
                const unsigned tok = e / 96u, c2 = e % 96u;
                const unsigned pix = ((8u * wy + 4u + (tok >> 3)) & 511u) * 512u + ((8u * wx + 4u + (tok & 7u)) & 511u);
                ((unsigned*)xs)[e] = ((const unsigned*)x)[pix * 96u + c2];
            }
            __syncthreads();
            for (unsigned col = t; col < 576u; col += 256u)
                for (unsigned tb = 0; tb < 64u; tb += 8u) {
                    float a[8] = {0.f, 0.f, 0.f, 0.f, 0.f, 0.f, 0.f, 0.f};
                    for (unsigned c = 0; c < 192u; ++c) {
                        const float wt = bf2f(W1[c * 576u + col]);
#pragma unroll
                        for (int j = 0; j < 8; ++j) a[j] += bf2f(xs[(tb + j) * 192u + c]) * wt;
                    }
#pragma unroll
                    for (int j = 0; j < 8; ++j) qs[(tb + j) * 578u + col] = f2bf(a[j]);
                }
            __syncthreads();
            {
                float qv[48], o[48];
#pragma unroll
                for (int d = 0; d < 48; ++d) { qv[d] = bf2f(qs[ln * 578u + w * 48u + d]); o[d] = 0.f; }
                const unsigned short* mrow = (const unsigned short*)mask + (size_t)(wy * 64u + wx) * 4096u + ln * 64u;
                float mx = -__builtin_inff(), l = 0.f;
                for (int pass = 0; pass < 2; ++pass)
                    for (unsigned k = 0; k < 64u; ++k) {
                        float s = 0.f;
#pragma unroll
                        for (int d = 0; d < 48; ++d) s += qv[d] * bf2f(qs[k * 578u + 192u + w * 48u + d]);
                        s = s * 0.14433756729740643f + bf2f(((const unsigned short*)rpb)[((const unsigned*)rpi)[ln * 64u + k] * 4u + w]) + bf2f(mrow[k]);
                        if (pass == 0) mx = fmaxf(mx, s);
                        else {
                            const float p = __expf(s - mx);
                            l += p;
#pragma unroll
                            for (int d = 0; d < 48; ++d) o[d] += p * bf2f(qs[k * 578u + 384u + w * 48u + d]);
                        }
                    }
#pragma unroll
                for (int d = 0; d < 48; ++d) xs[ln * 192u + w * 48u + d] = f2bf(o[d] / l);
            }
            __syncthreads();
            if (t < 192u)
                for (unsigned tb = 0; tb < 64u; tb += 8u) {
                    float a[8] = {0.f, 0.f, 0.f, 0.f, 0.f, 0.f, 0.f, 0.f};
                    for (unsigned c = 0; c < 192u; ++c) {
                        const float wt = bf2f(W2[c * 192u + t]);
#pragma unroll
                        for (int j = 0; j < 8; ++j) a[j] += bf2f(xs[(tb + j) * 192u + c]) * wt;
                    }
                    const unsigned row = (8u * wy + 4u + (tb >> 3)) & 511u;
#pragma unroll
                    for (int j = 0; j < 8; ++j)
                        ((unsigned short*)out)[(row * 512u + ((8u * wx + 4u + j) & 511u)) * 192u + t] = f2bf(a[j]);
                }
        }
    }
}


extern "C" void kernel_launch(void* const* d_in, const int* in_sizes, int n_in, void* d_out, int out_size, void* d_ws,
                              size_t ws_size, hipStream_t stream) {
    win_attn_kernel<<<256, 256, 0, stream>>>((const char*)d_in[0], (const char*)d_in[1], (const char*)d_in[2],
                                             (const char*)d_in[3], (const char*)d_in[4], (const char*)d_in[5],
                                             (char*)d_out, (char*)d_ws);

}
```

```cpp
#include <hip/hip_runtime.h>
#include <hip/hip_bf16.h>

typedef unsigned int u32x4 __attribute__((ext_vector_type(4)));

static __device__ __forceinline__ float bf2f(unsigned short u) { return __uint_as_float((unsigned)u << 16); }
static __device__ __forceinline__ unsigned short f2bf(float f) { return __builtin_bit_cast(unsigned short, (__bf16)f); }

__global__ __launch_bounds__(256, 1) __attribute__((aligned(4096))) void win_attn_kernel(const char* x_, const char* wqkv_, const char* wo_, const char* rpb_,
                                                          const char* rpi_, const char* mask_, char* out_, char* ws) {
    __shared__ __attribute__((aligned(16))) char smem[163840];
    const void* ka = (const void*)__builtin_amdgcn_kernarg_segment_ptr();
    unsigned bid = blockIdx.x, tid = threadIdx.x, flg, w;
    unsigned ldsb = (unsigned)(size_t)smem;
    asm volatile(
        "s_load_dwordx16 s[68:83], s[0:1], 0\n"
        "v_readfirstlane_b32 s42, v0\n"
        "v_and_b32 v1, 63, v0\n"
        "s_lshr_b32 s31, s2, 1\n"
        "s_lshr_b32 s42, s42, 6\n"
        "s_and_b32 s31, s31, 0x7c\n"
        "v_lshlrev_b32 v2, 4, v1\n"
        "s_add_u32 s31, s31, s42\n"
        "s_and_b32 s35, s31, 31\n"
        "s_min_u32 s35, s35, 31\n"
        "s_getpc_b64 s[66:67]\n"
        "v_lshl_or_b32 v10, s35, 10, v2\n"
        "global_load_dwordx4 v[60:63], v10, s[66:67]\n"
        "s_lshl_b32 s50, s31, 6\n"
        "s_mul_i32 s50, s50, 27\n"
        "v_min_u32 v136, 26, v1\n"
        "v_lshl_add_u32 v136, v136, 6, s50\n"
        "v_mul_u32_u24 v11, 171, v1\n"
        "v_lshrrev_b32 v4, 10, v11\n"
        "v_bfe_u32 v7, v11, 13, 1\n"
        "v_add_u32 v11, 10944, v11\n"
        "v_lshrrev_b32 v5, 10, v11\n"
        "v_bfe_u32 v8, v11, 13, 1\n"
        "v_add_u32 v11, 10944, v11\n"
        "v_lshrrev_b32 v6, 10, v11\n"
        "v_bfe_u32 v9, v11, 13, 1\n"
        "s_mul_i32 s40, s42, 3072\n"
        "s_mov_b32 s10, 100663296\n"
        "s_mov_b32 s11, 0x20000\n"
        "s_mov_b32 s58, 16384\n"
        "s_mov_b32 s59, 0x20000\n"
        "s_mov_b32 s62, 1800\n"
        "s_mov_b32 s63, 0x20000\n"
        "s_mov_b32 s22, 221184\n"
        "s_mov_b32 s23, 0x20000\n"
        "s_and_b32 s51, s42, 1\n"
        "v_lshlrev_b32 v134, 3, v0\n"
        "s_lshl_b32 s35, s51, 7\n"
        "v_and_b32 v10, 48, v1\n"
        "v_xor_b32 v131, s35, v2\n"
        "s_lshl_b32 s46, s42, 12\n"
        "v_xor_b32 v131, v131, v10\n"
        "v_xor_b32 v132, 64, v131\n"
        "s_and_b32 s31, s2, 63\n"
        "s_lshl_b32 s31, s31, 3\n"
        "s_add_u32 s31, s31, 4\n"
        "s_lshl_b32 s35, s42, 5\n"
        "s_movk_i32 s41, 384\n"
        "s_movk_i32 s45, -384\n"
        "v_lshrrev_b32 v10, 2, v4\n"
        "v_mad_i32_i24 v11, v10, s45, v2\n"
        "v_add_u32 v10, s31, v10\n"
        "v_lshl_or_b32 v242, v7, 4, s35\n"
        "v_and_b32 v10, 511, v10\n"
        "v_xor_b32 v11, v11, v242\n"
        "v_mad_u32_u24 v242, v10, s41, v11\n"
        "v_lshrrev_b32 v10, 2, v5\n"
        "v_mad_i32_i24 v11, v10, s45, v2\n"
        "v_add_u32 v10, s31, v10\n"
        "v_add_u32 v11, 1024, v11\n"
        "v_lshl_or_b32 v243, v8, 4, s35\n"
        "v_and_b32 v10, 511, v10\n"
        "v_xor_b32 v11, v11, v243\n"
        "v_mad_u32_u24 v243, v10, s41, v11\n"
        "v_lshrrev_b32 v10, 2, v6\n"
        "v_mad_i32_i24 v11, v10, s45, v2\n"
        "v_add_u32 v10, s31, v10\n"
        "v_add_u32 v11, 2048, v11\n"
        "v_lshl_or_b32 v244, v9, 4, s35\n"
        "v_and_b32 v10, 511, v10\n"
        "v_xor_b32 v11, v11, v244\n"
        "v_mad_u32_u24 v244, v10, s41, v11\n"
        "s_mul_i32 s43, s31, 384\n"
        "s_add_u32 s31, s31, 4\n"
        "s_and_b32 s31, s31, 511\n"
        "s_mul_i32 s44, s31, 384\n"
        "s_lshr_b32 s31, s2, 6\n"
        "s_lshl_b32 s31, s31, 3\n"
        "s_add_u32 s47, s31, s42\n"
        "s_add_u32 s47, s47, 68\n"
        "s_mul_i32 s47, s47, 196608\n"
        "s_lshl2_add_u32 s37, s51, s31\n"
        "s_add_u32 s37, s37, 4\n"
        "s_mul_i32 s37, s37, 196608\n"
        "s_mul_i32 s65, s42, 18432\n"
        "s_mov_b32 s14, 33554432\n"
        "s_mov_b32 s15, 0x20000\n"
        "s_mul_i32 s35, s42, 96\n"
        "s_movk_i32 s31, 1056\n"
        "s_movk_i32 s45, 4608\n"
        "v_add_u32 v3, s35, v2\n"
        "s_waitcnt lgkmcnt(0)\n"
        "s_mov_b64 s[60:61], s[74:75]\n"
        "s_mov_b64 s[20:21], s[70:71]\n"
        "s_mov_b64 s[56:57], s[76:77]\n"
        "s_mov_b64 s[8:9], s[68:69]\n"
        "buffer_load_dwordx2 v[144:145], v134, s[60:63], 0 offen\n"
        "s_add_u32 m0, s46, 122880\n"
        "s_mov_b32 s64, s46\n"
        "buffer_load_dword v136, s[20:23], 0 offen lds\n"
        "s_mov_b64 s[52:53], s[72:73]\n"
        "s_mov_b64 s[12:13], s[78:79]\n"
        "buffer_load_dwordx4 v131, s[56:59], s64 offen lds\n"
        "s_mov_b64 s[16:17], s[80:81]\n"
        "v_bfe_u32 v12, v4, 2, 1\n"
        "v_mad_u32_u24 v128, v4, s31, v3\n"
        "v_sub_u32 v12, v12, v7\n"
        "v_mad_i32_i24 v128, v12, s45, v128\n"
        "v_bfe_u32 v13, v5, 2, 1\n"
        "buffer_load_dwordx4 v131, s[56:59], s64 offen offset:1024 lds\n"
        "v_mad_u32_u24 v129, v5, s31, v3\n"
        "v_sub_u32 v13, v13, v8\n"
        "v_mad_i32_i24 v129, v13, s45, v129\n"
        "v_bfe_u32 v14, v6, 2, 1\n"
        "v_mad_u32_u24 v130, v6, s31, v3\n"
        "v_sub_u32 v14, v14, v9\n"
        "v_mad_i32_i24 v130, v14, s45, v130\n"
        "s_lshl_b32 s48, s42, 11\n"
        "s_lshl_b32 s36, s2, 13\n"
        "v_and_b32 v19, 16, v1\n"
        "buffer_load_dwordx4 v132, s[56:59], s64 offen offset:2048 lds\n"
        "s_add_u32 s36, s36, s48\n"
        "s_add_u32 s36, s36, 4194304\n"
        "s_add_u32 s48, s48, 155648\n"
        "s_lshl_b32 s35, s51, 6\n"
        "v_xor_b32 v249, v2, v19\n"
        "v_xor_b32 v249, s35, v249\n"
        "v_xor_b32 v253, 32, v249\n"
        "v_and_b32 v15, 15, v1\n"
        "v_lshrrev_b32 v16, 4, v1\n"
        "v_and_b32 v17, 12, v1\n"
        "buffer_load_dwordx4 v132, s[56:59], s64 offen offset:3072 lds\n"
        "v_and_b32 v18, 0xf0, v2\n"
        "v_add_u32 v17, v17, v15\n"
        "v_bfe_u32 v10, v1, 2, 3\n"
        "v_lshrrev_b32 v11, 5, v1\n"
        "v_lshl_or_b32 v10, v11, 4, v10\n"
        "v_and_b32 v255, 3, v1\n"
        "v_mul_u32_u24 v10, 96, v10\n"
        "v_lshl_add_u32 v255, v255, 3, s65\n"
        "v_add_u32 v255, v255, v10\n"
        "v_lshrrev_b32 v20, 1, v15\n"
        "s_add_u32 m0, s40, 73728\n"
        "s_sub_u32 s64, s47, 12582912\n"
        "buffer_load_dwordx4 v242, s[8:11], s64 offen lds\n"
        "v_xor_b32 v20, v20, v16\n"
        "v_lshlrev_b32 v20, 4, v20\n"
        "v_mad_u32_u24 v240, v17, s41, v20\n"
        "v_lshl_or_b32 v251, v17, 7, v20\n"
        "v_add_u32 v240, 73728, v240\n"
        "v_add_u32 v251, 155648, v251\n"
        "v_xor_b32 v241, 64, v240\n"
        "v_xor_b32 v252, 64, v251\n"
        "s_lshl_b32 s67, s42, 10\n"
        "s_add_u32 s67, s67, 139264\n"
        "s_add_u32 m0, s40, 74752\n"
        "s_nop 0\n"
        "buffer_load_dwordx4 v243, s[8:11], s64 offen lds\n"
        "v_lshlrev_b32 v135, 2, v0\n"
        "v_add_u32 v135, 139264, v135\n"
        "v_lshlrev_b32 v11, 5, v16\n"
        "v_xor_b32 v11, v11, v18\n"
        "v_lshl_or_b32 v11, v17, 8, v11\n"
        "v_add_u32 v140, 122880, v11\n"
        "v_xor_b32 v141, 16, v140\n"
        "v_xor_b32 v142, 128, v140\n"
        "v_xor_b32 v143, 144, v140\n"
        "v_lshrrev_b32 v10, 2, v15\n"
        "s_add_u32 m0, s40, 75776\n"
        "s_nop 0\n"
        "buffer_load_dwordx4 v244, s[8:11], s64 offen lds\n"
        "v_cmp_eq_u32 vcc, v10, v16\n"
        "v_mov_b32 v11, 0x3fb9\n"
        "v_mov_b32 v189, 0\n"
        "v_and_b32 v10, 48, v2\n"
        "v_cndmask_b32 v188, 0, v11, vcc\n"
        "v_lshlrev_b64 v[188:189], v10, v[188:189]\n"
        "v_cmp_gt_u32 vcc, 32, v1\n"
        "v_mov_b32 v11, 15957\n"
        "v_lshrrev_b32 v149, 2, v15\n"
        "v_lshrrev_b32 v151, 3, v19\n"
        "v_mov_b32 v148, 14702\n"
        "v_cndmask_b32 v11, v148, v11, vcc\n"
        "v_xor_b32 v151, v149, v151\n"
        "v_cmp_eq_u32 vcc, 0, v151\n"
        "v_mov_b32 v149, 0\n"
        "v_cndmask_b32 v148, 0, v11, vcc\n"
        "v_lshlrev_b64 v[148:149], v10, v[148:149]\n"
        "v_cmp_eq_u32 vcc, 1, v151\n"
        "v_mov_b32 v151, 0\n"
        "v_cndmask_b32 v150, 0, v11, vcc\n"
        "v_lshlrev_b64 v[150:151], v10, v[150:151]\n"
        "v_and_b32 v10, 8, v1\n"
        "v_add_u32 v10, v10, v15\n"
        "v_mul_u32_u24 v10, 96, v10\n"
        "v_add3_u32 v137, v10, v19, s65\n"
        "v_and_b32 v237, 1, v1\n"
        "v_mul_u32_u24 v237, 0xaaaa, v237\n"
        "v_mov_b64 v[170:171], v[188:189]\n"
        "v_mov_b64 v[210:211], v[188:189]\n"
        "v_mov_b64 v[228:229], v[188:189]\n"
        "v_add_u32 v237, 0x4444, v237\n"
        "v_mov_b32 v11, 0x3f803f80\n"
        "v_accvgpr_write_b32 a252, v11\n"
        "v_accvgpr_write_b32 a253, v11\n"
        "v_accvgpr_write_b32 a254, v11\n"
        "v_accvgpr_write_b32 a255, v11\n"
        "v_lshl_or_b32 v10, v16, 10, v18\n"
        "s_lshl_b32 s31, s42, 12\n"
        "s_add_u32 s31, s31, 73728\n"
        "s_lshl_b32 s35, s51, 9\n"
        "s_add_u32 s35, s35, 73728\n"
        "v_add_u32 v245, s31, v10\n"
        "s_sub_u32 s50, s35, s31\n"
        "v_lshrrev_b32 v10, 5, v1\n"
        "v_lshrrev_b32 v11, 1, v19\n"
        "s_sub_u32 s31, s48, 65536\n"
        "v_lshl_or_b32 v10, v10, 10, v18\n"
        "v_add3_u32 v246, v10, v11, s31\n"
        "v_mul_u32_u24 v10, 768, v16\n"
        "v_xor_b32 v11, v18, v19\n"
        "v_add3_u32 v235, v10, v11, s65\n"
        "s_lshr_b32 s31, s42, 1\n"
        "s_mul_i32 s35, s31, 36864\n"
        "s_movk_i32 s45, 17664\n"
        "v_mul_u32_u24 v11, 96, v15\n"
        "v_lshrrev_b32 v248, 3, v15\n"
        "v_add3_u32 v10, v10, v11, s35\n"
        "v_mad_u32_u24 v10, v248, s45, v10\n"
        "v_add_u32 v248, v10, v19\n"
        "v_sub_u32 v254, v10, v19\n"
        "s_mul_i32 s31, s31, 192\n"
        "v_mul_u32_u24 v10, 196608, v16\n"
        "v_mul_u32_u24 v11, 12, v15\n"
        "v_add3_u32 v250, v10, v11, s31\n"
        "s_movk_i32 s31, 288\n"
        "s_movk_i32 s35, 3072\n"
        "s_movk_i32 s41, 1536\n"
        "s_movk_i32 s45, 12288\n"
        "v_mad_u32_u24 v10, v4, s31, v3\n"
        "v_lshrrev_b32 v11, 4, v4\n"
        "v_mad_i32_i24 v238, v12, s41, v10\n"
        "v_sub_u32 v7, v7, v11\n"
        "v_mad_u32_u24 v238, v11, s45, v238\n"
        "v_mad_i32_i24 v232, v7, s35, v10\n"
        "v_mad_u32_u24 v10, v5, s31, v3\n"
        "v_lshrrev_b32 v11, 4, v5\n"
        "v_mad_i32_i24 v239, v13, s41, v10\n"
        "v_sub_u32 v8, v8, v11\n"
        "v_mad_u32_u24 v239, v11, s45, v239\n"
        "v_mad_i32_i24 v233, v8, s35, v10\n"
        "v_mad_u32_u24 v10, v6, s31, v3\n"
        "v_lshrrev_b32 v11, 4, v6\n"
        "v_mad_i32_i24 v247, v14, s41, v10\n"
        "v_sub_u32 v9, v9, v11\n"
        "v_mad_u32_u24 v247, v11, s45, v247\n"
        "v_mad_i32_i24 v234, v9, s35, v10\n"
        "s_add_u32 s38, s37, s43\n"
        "s_mov_b32 s26, 0x3fb8aa3b\n"
        "s_mov_b32 s24, 0x5d800000\n"
        "s_mov_b32 s27, 14\n"
        "s_mov_b32 s28, 98304\n"
        "s_mov_b32 s29, 73728\n"
        "s_mov_b32 s30, 122880\n"
        "s_mov_b32 s45, s50\n"
        "s_mov_b32 s25, 0\n"
        "s_mov_b32 s54, 73728\n"
        "s_mov_b32 s55, 0x20000\n"
        "s_mov_b32 s18, 100663296\n"
        "s_mov_b32 s19, 0x20000\n"
        "s_mov_b32 s56, 36864\n"
        "s_mov_b32 s57, 73728\n"
        "s_mov_b32 s58, 110592\n"
        "s_mov_b32 s59, 147456\n"
        "s_mov_b32 s60, 184320\n"
        "s_mov_b32 s61, 18432\n"
        "s_mov_b32 s62, 55296\n"
        "s_mov_b32 s63, 12288\n"
        "s_mov_b32 s34, 49152\n"
        "s_add_u32 m0, s40, 86016\n"
        "s_sub_u32 s64, s47, 11796480\n"
        "buffer_load_dwordx4 v242, s[8:11], s64 offen lds\n"
        "s_waitcnt vmcnt(9)\n"
        "v_and_b32 v146, 0xffff0000, v144\n"
        "v_and_b32 v147, 0xffff0000, v145\n"
        "v_lshlrev_b32 v144, 16, v144\n"
        "v_lshlrev_b32 v145, 16, v145\n"
        "s_add_u32 m0, s40, 87040\n"
        "v_mul_f32 v146, s26, v146\n"
        "buffer_load_dwordx4 v243, s[8:11], s64 offen lds\n"
        "v_mul_f32 v147, s26, v147\n"
        "v_mul_f32 v144, s26, v144\n"
        "v_mul_f32 v145, s26, v145\n"
        "ds_write2st64_b32 v135, v144, v146 offset0:0 offset1:4\n"
        "ds_write2st64_b32 v135, v145, v147 offset0:8 offset1:12\n"
        "s_cmp_eq_u32 s42, 0\n"
        "s_cbranch_scc1 41f\n"
        "s_cmp_eq_u32 s42, 1\n"
        "s_cbranch_scc1 42f\n"
        "s_cmp_eq_u32 s42, 2\n"
        "s_cbranch_scc1 43f\n"
        "s_cmp_eq_u32 s42, 3\n"
        "s_cbranch_scc1 44f\n"
        "60:\n"
        "s_add_u32 m0, s40, 88064\n"
        "s_nop 0\n"
        "buffer_load_dwordx4 v244, s[8:11], s64 offen lds\n"
        "s_waitcnt lgkmcnt(0)\n"
        "s_waitcnt vmcnt(6)\n"
        "s_barrier\n"
        "s_add_u32 m0, s65, 0\n"
        "ds_read_b128 v[0:3], v140 offset:0\n"
        "buffer_load_dwordx4 v128, s[20:23], 0 offen lds\n"
        "buffer_load_dwordx4 v129, s[20:23], 0 offen offset:1024 lds\n"
        "ds_read_b128 v[4:7], v141 offset:0\n"
        "ds_read_b128 v[16:19], v140 offset:1024\n"
        "ds_read_b128 v[20:23], v141 offset:1024\n"
        "ds_read_b128 v[8:11], v142 offset:0\n"
        "ds_read_b128 v[24:27], v142 offset:1024\n"
        "ds_read_b128 v[12:15], v143 offset:0\n"
        "ds_read_b128 v[28:31], v143 offset:1024\n"
        "ds_read_b128 v[32:35], v140 offset:8192\n"
        "ds_read_b128 v[36:39], v141 offset:8192\n"
        "ds_read_b128 v[40:43], v142 offset:8192\n"
        "s_cmp_eq_u32 s42, 0\n"
        "s_cbranch_scc1 51f\n"
        "s_cmp_eq_u32 s42, 1\n"
        "s_cbranch_scc1 54f\n"
        "s_cmp_eq_u32 s42, 2\n"
        "s_cbranch_scc1 45f\n"
        "s_cmp_eq_u32 s42, 3\n"
        "s_cbranch_scc1 47f\n"
        "61:\n"
        "buffer_load_dwordx4 v130, s[20:23], 0 offen offset:2048 lds\n"
        "s_add_u32 m0, s65, 2688\n"
        "s_waitcnt lgkmcnt(10)\n"
        "v_lshl_add_u32 v0, v0, 2, s67\n"
        "buffer_load_dwordx4 v128, s[20:23], 0 offen offset:384 lds\n"
        "v_lshl_add_u32 v1, v1, 2, s67\n"
        "v_lshl_add_u32 v2, v2, 2, s67\n"
        "v_lshl_add_u32 v3, v3, 2, s67\n"
        "s_waitcnt lgkmcnt(9)\n"
        "v_lshl_add_u32 v4, v4, 2, s67\n"
        "v_lshl_add_u32 v5, v5, 2, s67\n"
        "v_lshl_add_u32 v6, v6, 2, s67\n"
        "v_lshl_add_u32 v7, v7, 2, s67\n"
        "s_waitcnt lgkmcnt(8)\n"
        "v_lshl_add_u32 v16, v16, 2, s67\n"
        "v_lshl_add_u32 v17, v17, 2, s67\n"
        "v_lshl_add_u32 v18, v18, 2, s67\n"
        "v_lshl_add_u32 v19, v19, 2, s67\n"
        "buffer_load_dwordx4 v129, s[20:23], 0 offen offset:1408 lds\n"
        "ds_read_b32 v0, v0\n"
        "ds_read_b32 v1, v1\n"
        "ds_read_b32 v2, v2\n"
        "ds_read_b32 v3, v3\n"
        "ds_read_b32 v4, v4\n"
        "ds_read_b32 v5, v5\n"
        "ds_read_b32 v6, v6\n"
        "ds_read_b32 v7, v7\n"
        "ds_read_b32 v16, v16\n"
        "ds_read_b32 v17, v17\n"
        "ds_read_b32 v18, v18\n"
        "ds_read_b32 v19, v19\n"
        "buffer_load_dwordx4 v130, s[20:23], 0 offen offset:2432 lds\n"
        "s_waitcnt lgkmcnt(15)\n"
        "v_lshl_add_u32 v20, v20, 2, s67\n"
        "v_lshl_add_u32 v21, v21, 2, s67\n"
        "v_lshl_add_u32 v22, v22, 2, s67\n"
        "v_lshl_add_u32 v23, v23, 2, s67\n"
        "v_lshl_add_u32 v8, v8, 2, s67\n"
        "v_lshl_add_u32 v9, v9, 2, s67\n"
        "v_lshl_add_u32 v10, v10, 2, s67\n"
        "v_lshl_add_u32 v11, v11, 2, s67\n"
        "v_lshl_add_u32 v24, v24, 2, s67\n"
        "v_lshl_add_u32 v25, v25, 2, s67\n"
        "v_lshl_add_u32 v26, v26, 2, s67\n"
        "v_lshl_add_u32 v27, v27, 2, s67\n"
        "ds_read_b32 v20, v20\n"
        "ds_read_b32 v21, v21\n"
        "ds_read_b32 v22, v22\n"
        "ds_read_b32 v23, v23\n"
        "ds_read_b32 v8, v8\n"
        "ds_read_b32 v9, v9\n"
        "ds_read_b32 v10, v10\n"
        "ds_read_b32 v11, v11\n"
        "ds_read_b32 v24, v24\n"
        "ds_read_b32 v25, v25\n"
        "ds_read_b32 v26, v26\n"
        "ds_read_b32 v27, v27\n"
        "s_add_u32 m0, s65, 6144\n"
        "v_lshl_add_u32 v12, v12, 2, s67\n"
        "buffer_load_dwordx4 v128, s[20:23], s56 offen lds\n"
        "v_lshl_add_u32 v13, v13, 2, s67\n"
        "v_lshl_add_u32 v14, v14, 2, s67\n"
        "v_lshl_add_u32 v15, v15, 2, s67\n"
        "v_lshl_add_u32 v28, v28, 2, s67\n"
        "v_lshl_add_u32 v29, v29, 2, s67\n"
        "v_lshl_add_u32 v30, v30, 2, s67\n"
        "v_lshl_add_u32 v31, v31, 2, s67\n"
        "s_waitcnt lgkmcnt(15)\n"
        "v_lshl_add_u32 v32, v32, 2, s67\n"
        "v_lshl_add_u32 v33, v33, 2, s67\n"
        "v_lshl_add_u32 v34, v34, 2, s67\n"
        "v_lshl_add_u32 v35, v35, 2, s67\n"
        "ds_read_b32 v12, v12\n"
        "ds_read_b32 v13, v13\n"
        "ds_read_b32 v14, v14\n"
        "ds_read_b32 v15, v15\n"
        "ds_read_b32 v28, v28\n"
        "ds_read_b32 v29, v29\n"
        "ds_read_b32 v30, v30\n"
        "ds_read_b32 v31, v31\n"
        "ds_read_b32 v32, v32\n"
        "ds_read_b32 v33, v33\n"
        "ds_read_b32 v34, v34\n"
        "ds_read_b32 v35, v35\n"
        "buffer_load_dwordx4 v129, s[20:23], s56 offen offset:1024 lds\n"
        "v_lshl_add_u32 v36, v36, 2, s67\n"
        "v_lshl_add_u32 v37, v37, 2, s67\n"
        "v_lshl_add_u32 v38, v38, 2, s67\n"
        "v_lshl_add_u32 v39, v39, 2, s67\n"
        "v_lshl_add_u32 v40, v40, 2, s67\n"
        "v_lshl_add_u32 v41, v41, 2, s67\n"
        "v_lshl_add_u32 v42, v42, 2, s67\n"
        "v_lshl_add_u32 v43, v43, 2, s67\n"
        "ds_read_b32 v36, v36\n"
        "ds_read_b32 v37, v37\n"
        "ds_read_b32 v38, v38\n"
        "ds_read_b32 v39, v39\n"
        "ds_read_b32 v40, v40\n"
        "ds_read_b32 v41, v41\n"
        "ds_read_b32 v42, v42\n"
        "ds_read_b32 v43, v43\n"
        "buffer_load_dwordx4 v130, s[20:23], s56 offen offset:2048 lds\n"
        "s_waitcnt vmcnt(9)\n"
        "s_barrier\n"
        "s_waitcnt vmcnt(6)\n"
        "ds_read_b128 a[212:215], v137 offset:0\n"
        "ds_read_b128 a[208:211], v137 offset:768\n"
        "ds_read_b128 a[204:207], v137 offset:32\n"
        "ds_read_b128 a[200:203], v137 offset:800\n"
        "ds_read_b128 a[196:199], v137 offset:64\n"
        "ds_read_b128 a[192:195], v137 offset:832\n"
        "s_add_u32 m0, s65, 8832\n"
        "s_nop 0\n"
        "buffer_load_dwordx4 v128, s[20:23], s56 offen offset:384 lds\n"
        "s_waitcnt lgkmcnt(5)\n"
        "v_mfma_f32_16x16x32_bf16 v[112:115], a[212:215], v[148:151], 0\n"
        "s_waitcnt lgkmcnt(4)\n"
        "ds_read_b128 a[216:219], v240 offset:0\n"
        "ds_read_b128 a[220:223], v240 offset:1536\n"
        "ds_read_b128 a[224:227], v240 offset:12288\n"
        "ds_read_b128 a[228:231], v240 offset:13824\n"
        "v_mfma_f32_16x16x32_bf16 v[116:119], a[208:211], v[148:151], 0\n"
        "s_waitcnt lgkmcnt(7)\n"
        "v_mfma_f32_16x16x32_bf16 v[120:123], a[204:207], v[148:151], 0\n"
        "s_waitcnt lgkmcnt(6)\n"
        "v_mfma_f32_16x16x32_bf16 v[124:127], a[200:203], v[148:151], 0\n"
        "s_waitcnt lgkmcnt(5)\n"
        "buffer_load_dwordx4 v129, s[20:23], s56 offen offset:1408 lds\n"
        "v_mfma_f32_16x16x32_bf16 v[96:99], a[196:199], v[148:151], 0\n"
        "s_waitcnt lgkmcnt(4)\n"
        "ds_read_b128 v[44:47], v143 offset:8192\n"
        "v_cvt_pk_bf16_f32 v112, v112, v113\n"
        "v_mfma_f32_16x16x32_bf16 v[100:103], a[192:195], v[148:151], 0\n"
        "s_waitcnt lgkmcnt(4)\n"
        "s_waitcnt vmcnt(5)\n"
        "ds_read_b64_tr_b16 a[72:73], v255 offset:3072\n"
        "ds_read_b64_tr_b16 a[74:75], v255 offset:3840\n"
        "v_accvgpr_write_b32 a0, v112\n"
        "v_cvt_pk_bf16_f32 v114, v114, v115\n"
        "v_accvgpr_write_b32 a1, v114\n"
        "v_cvt_pk_bf16_f32 v116, v116, v117\n"
        "v_accvgpr_write_b32 a2, v116\n"
        "v_cvt_pk_bf16_f32 v118, v118, v119\n"
        "v_accvgpr_write_b32 a3, v118\n"
        "s_nop 1\n"
        "v_mfma_f32_16x16x32_bf16 v[156:159], a[0:3], a[216:219], 0\n"
        "s_waitcnt lgkmcnt(5)\n"
        "ds_read_b64_tr_b16 a[96:97], v255 offset:3104\n"
        "ds_read_b64_tr_b16 a[98:99], v255 offset:3872\n"
        "v_mfma_f32_16x16x32_bf16 v[176:179], a[0:3], a[220:223], 0\n"
        "s_waitcnt lgkmcnt(5)\n"
        "buffer_load_dwordx4 v130, s[20:23], s56 offen offset:2432 lds\n"
        "ds_read_b64_tr_b16 a[120:121], v255 offset:3136\n"
        "ds_read_b64_tr_b16 a[122:123], v255 offset:3904\n"
        "v_mfma_f32_16x16x32_bf16 v[196:199], a[0:3], a[224:227], 0\n"
        "s_add_u32 m0, s65, 12288\n"
        "v_cvt_pk_bf16_f32 v120, v120, v121\n"
        "v_accvgpr_write_b32 a24, v120\n"
        "v_mfma_f32_16x16x32_bf16 v[216:219], a[0:3], a[228:231], 0\n"
        "s_waitcnt lgkmcnt(4)\n"
        "v_cvt_pk_bf16_f32 v122, v122, v123\n"
        "v_accvgpr_write_b32 a25, v122\n"
        "v_mfma_f32_16x16x32_bf16 v[152:155], a[72:75], a[216:219], 0\n"
        "buffer_load_dwordx4 v128, s[20:23], s57 offen lds\n"
        "s_waitcnt vmcnt(4)\n"
        "ds_read_b128 a[188:191], v137 offset:6144\n"
        "v_mfma_f32_16x16x32_bf16 v[172:175], a[72:75], a[220:223], 0\n"
        "ds_read_b128 a[184:187], v137 offset:6912\n"
        "ds_read_b128 a[180:183], v137 offset:6176\n"
        "v_cvt_pk_bf16_f32 v124, v124, v125\n"
        "v_accvgpr_write_b32 a26, v124\n"
        "v_cvt_pk_bf16_f32 v126, v126, v127\n"
        "v_accvgpr_write_b32 a27, v126\n"
        "s_nop 1\n"
        "v_mfma_f32_16x16x32_bf16 v[224:227], a[24:27], a[228:231], 0\n"
        "buffer_load_dwordx4 v129, s[20:23], s57 offen offset:1024 lds\n"
        "ds_read_b128 a[176:179], v137 offset:6944\n"
        "ds_read_b128 a[172:175], v137 offset:6208\n"
        "v_mfma_f32_16x16x32_bf16 v[204:207], a[24:27], a[224:227], 0\n"
        "ds_read_b128 a[168:171], v137 offset:6976\n"
        "ds_read_b128 a[232:235], v241 offset:0\n"
        "v_mfma_f32_16x16x32_bf16 v[184:187], a[24:27], a[220:223], 0\n"
        "ds_read_b128 a[236:239], v241 offset:1536\n"
        "ds_read_b128 a[240:243], v241 offset:12288\n"
        "v_mfma_f32_16x16x32_bf16 v[164:167], a[24:27], a[216:219], 0\n"
        "buffer_load_dwordx4 v130, s[20:23], s57 offen offset:2048 lds\n"
        "v_mfma_f32_16x16x32_bf16 v[192:195], a[72:75], a[224:227], 0\n"
        "s_add_u32 m0, s65, 14976\n"
        "ds_read_b128 a[244:247], v241 offset:13824\n"
        "v_cvt_pk_bf16_f32 v96, v96, v97\n"
        "v_mfma_f32_16x16x32_bf16 v[212:215], a[72:75], a[228:231], 0\n"
        "v_accvgpr_write_b32 a48, v96\n"
        "v_cvt_pk_bf16_f32 v98, v98, v99\n"
        "v_accvgpr_write_b32 a49, v98\n"
        "v_cvt_pk_bf16_f32 v100, v100, v101\n"
        "v_accvgpr_write_b32 a50, v100\n"
        "v_cvt_pk_bf16_f32 v102, v102, v103\n"
        "v_accvgpr_write_b32 a51, v102\n"
        "s_nop 1\n"
        "v_mfma_f32_16x16x32_bf16 v[160:163], a[48:51], a[216:219], 0\n"
        "v_lshl_add_u32 v44, v44, 2, s67\n"
        "v_lshl_add_u32 v45, v45, 2, s67\n"
        "v_mfma_f32_16x16x32_bf16 v[180:183], a[48:51], a[220:223], 0\n"
        "s_waitcnt lgkmcnt(5)\n"
        "ds_read_b32 v44, v44\n"
        "ds_read_b32 v45, v45\n"
        "v_mfma_f32_16x16x32_bf16 v[104:107], a[188:191], v[148:151], 0\n"
        "buffer_load_dwordx4 v128, s[20:23], s57 offen offset:384 lds\n"
        "v_mfma_f32_16x16x32_bf16 v[200:203], a[48:51], a[224:227], 0\n"
        "v_lshl_add_u32 v46, v46, 2, s67\n"
        "v_lshl_add_u32 v47, v47, 2, s67\n"
        "v_mfma_f32_16x16x32_bf16 v[108:111], a[184:187], v[148:151], 0\n"
        "ds_read_b32 v46, v46\n"
        "ds_read_b32 v47, v47\n"
        "v_mfma_f32_16x16x32_bf16 v[220:223], a[48:51], a[228:231], 0\n"
        "buffer_load_dwordx4 v129, s[20:23], s57 offen offset:1408 lds\n"
        "v_mfma_f32_16x16x32_bf16 v[88:91], a[96:99], a[228:231], 0\n"
        "ds_read_b128 v[48:51], v140 offset:9216\n"
        "v_cvt_pk_bf16_f32 v104, v104, v105\n"
        "v_mfma_f32_16x16x32_bf16 v[80:83], a[96:99], a[224:227], 0\n"
        "s_waitcnt vmcnt(5)\n"
        "ds_read_b64_tr_b16 a[76:77], v255 offset:9216\n"
        "ds_read_b64_tr_b16 a[78:79], v255 offset:9984\n"
        "v_mfma_f32_16x16x32_bf16 v[68:71], a[96:99], a[220:223], 0\n"
        "ds_read_b64_tr_b16 a[100:101], v255 offset:9248\n"
        "ds_read_b64_tr_b16 a[102:103], v255 offset:10016\n"
        "v_mfma_f32_16x16x32_bf16 v[64:67], a[96:99], a[216:219], 0\n"
        "buffer_load_dwordx4 v130, s[20:23], s57 offen offset:2432 lds\n"
        "ds_read_b64_tr_b16 a[124:125], v255 offset:9280\n"
        "ds_read_b64_tr_b16 a[126:127], v255 offset:10048\n"
        "v_mfma_f32_16x16x32_bf16 v[112:115], a[180:183], v[148:151], 0\n"
        "s_add_u32 m0, s65, 0\n"
        "v_accvgpr_write_b32 a4, v104\n"
        "v_cvt_pk_bf16_f32 v106, v106, v107\n"
        "v_mfma_f32_16x16x32_bf16 v[116:119], a[176:179], v[148:151], 0\n"
        "v_accvgpr_write_b32 a5, v106\n"
        "v_cvt_pk_bf16_f32 v108, v108, v109\n"
        "v_mfma_f32_16x16x32_bf16 v[72:75], a[120:123], a[216:219], 0\n"
        "buffer_load_dwordx4 v128, s[20:23], s58 offen lds\n"
        "v_mfma_f32_16x16x32_bf16 v[76:79], a[120:123], a[220:223], 0\n"
        "v_accvgpr_write_b32 a6, v108\n"
        "v_cvt_pk_bf16_f32 v110, v110, v111\n"
        "v_mfma_f32_16x16x32_bf16 v[84:87], a[120:123], a[224:227], 0\n"
        "v_accvgpr_write_b32 a7, v110\n"
        "v_cvt_pk_bf16_f32 v112, v112, v113\n"
        "v_mfma_f32_16x16x32_bf16 v[92:95], a[120:123], a[228:231], 0\n"
        "s_waitcnt lgkmcnt(2)\n"
        "v_accvgpr_write_b32 a28, v112\n"
        "v_cvt_pk_bf16_f32 v114, v114, v115\n"
        "v_mfma_f32_16x16x32_bf16 v[156:159], a[4:7], a[232:235], v[156:159]\n"
        "buffer_load_dwordx4 v129, s[20:23], s58 offen offset:1024 lds\n"
        "v_mfma_f32_16x16x32_bf16 v[176:179], a[4:7], a[236:239], v[176:179]\n"
        "v_lshl_add_u32 v48, v48, 2, s67\n"
        "v_lshl_add_u32 v49, v49, 2, s67\n"
        "v_mfma_f32_16x16x32_bf16 v[196:199], a[4:7], a[240:243], v[196:199]\n"
        "ds_read_b32 v48, v48\n"
        "ds_read_b32 v49, v49\n"
        "v_mfma_f32_16x16x32_bf16 v[216:219], a[4:7], a[244:247], v[216:219]\n"
        "v_lshl_add_u32 v50, v50, 2, s67\n"
        "v_lshl_add_u32 v51, v51, 2, s67\n"
        "v_mfma_f32_16x16x32_bf16 v[120:123], a[172:175], v[148:151], 0\n"
        "buffer_load_dwordx4 v130, s[20:23], s58 offen offset:2048 lds\n"
        "v_mfma_f32_16x16x32_bf16 v[124:127], a[168:171], v[148:151], 0\n"
        "s_add_u32 m0, s65, 2688\n"
        "ds_read_b32 v50, v50\n"
        "ds_read_b32 v51, v51\n"
        "v_mfma_f32_16x16x32_bf16 v[152:155], a[76:79], a[232:235], v[152:155]\n"
        "ds_read_b128 a[248:251], v240 offset:128\n"
        "v_accvgpr_write_b32 a29, v114\n"
        "v_mfma_f32_16x16x32_bf16 v[172:175], a[76:79], a[236:239], v[172:175]\n"
        "s_waitcnt vmcnt(6)\n"
        "ds_read_b128 a[212:215], v137 offset:12288\n"
        "ds_read_b128 a[208:211], v137 offset:13056\n"
        "v_mfma_f32_16x16x32_bf16 v[192:195], a[76:79], a[240:243], v[192:195]\n"
        "buffer_load_dwordx4 v128, s[20:23], s58 offen offset:384 lds\n"
        "ds_read_b128 a[204:207], v137 offset:12320\n"
        "v_cvt_pk_bf16_f32 v116, v116, v117\n"
        "v_mfma_f32_16x16x32_bf16 v[212:215], a[76:79], a[244:247], v[212:215]\n"
        ".long 0xbec200ff\n"
        "41:\n"
        "s_branch 60b\n"
        "ds_read_b128 a[200:203], v137 offset:13088\n"
        "ds_read_b128 a[196:199], v137 offset:12352\n"
        "v_accvgpr_write_b32 a30, v116\n"
        "v_cvt_pk_bf16_f32 v118, v118, v119\n"
        "v_accvgpr_write_b32 a31, v118\n"
        "s_nop 1\n"
        "v_mfma_f32_16x16x32_bf16 v[224:227], a[28:31], a[244:247], v[224:227]\n"
        "ds_read_b128 a[192:195], v137 offset:13120\n"
        "ds_read_b128 a[216:219], v240 offset:1664\n"
        "v_mfma_f32_16x16x32_bf16 v[204:207], a[28:31], a[240:243], v[204:207]\n"
        "buffer_load_dwordx4 v129, s[20:23], s58 offen offset:1408 lds\n"
        "v_mfma_f32_16x16x32_bf16 v[184:187], a[28:31], a[236:239], v[184:187]\n"
        "ds_read_b128 a[220:223], v240 offset:12416\n"
        "ds_read_b128 a[224:227], v240 offset:13952\n"
        "v_mfma_f32_16x16x32_bf16 v[164:167], a[28:31], a[232:235], v[164:167]\n"
        "s_waitcnt lgkmcnt(8)\n"
        "v_cvt_pk_bf16_f32 v120, v120, v121\n"
        "v_accvgpr_write_b32 a52, v120\n"
        "v_mfma_f32_16x16x32_bf16 v[96:99], a[212:215], v[148:151], 0\n"
        "s_waitcnt lgkmcnt(7)\n"
        "v_cvt_pk_bf16_f32 v122, v122, v123\n"
        "v_accvgpr_write_b32 a53, v122\n"
        "v_mfma_f32_16x16x32_bf16 v[100:103], a[208:211], v[148:151], 0\n"
        "buffer_load_dwordx4 v130, s[20:23], s58 offen offset:2432 lds\n"
        "v_mfma_f32_16x16x32_bf16 v[88:91], a[100:103], a[244:247], v[88:91]\n"
        "s_add_u32 m0, s65, 6144\n"
        "v_cvt_pk_bf16_f32 v124, v124, v125\n"
        "v_accvgpr_write_b32 a54, v124\n"
        "v_mfma_f32_16x16x32_bf16 v[80:83], a[100:103], a[240:243], v[80:83]\n"
        "v_cvt_pk_bf16_f32 v126, v126, v127\n"
        "v_accvgpr_write_b32 a55, v126\n"
        "v_mfma_f32_16x16x32_bf16 v[68:71], a[100:103], a[236:239], v[68:71]\n"
        "v_cvt_pk_bf16_f32 v96, v96, v97\n"
        "v_accvgpr_write_b32 a8, v96\n"
        "v_mfma_f32_16x16x32_bf16 v[64:67], a[100:103], a[232:235], v[64:67]\n"
        "buffer_load_dwordx4 v128, s[20:23], s59 offen lds\n"
        "v_mfma_f32_16x16x32_bf16 v[160:163], a[52:55], a[232:235], v[160:163]\n"
        "s_waitcnt vmcnt(7)\n"
        "ds_read_b64_tr_b16 a[80:81], v255 offset:15360\n"
        "ds_read_b64_tr_b16 a[82:83], v255 offset:16128\n"
        "v_mfma_f32_16x16x32_bf16 v[180:183], a[52:55], a[236:239], v[180:183]\n"
        "ds_read_b64_tr_b16 a[104:105], v255 offset:15392\n"
        "ds_read_b64_tr_b16 a[106:107], v255 offset:16160\n"
        "v_mfma_f32_16x16x32_bf16 v[200:203], a[52:55], a[240:243], v[200:203]\n"
        "ds_read_b64_tr_b16 a[128:129], v255 offset:15424\n"
        "ds_read_b64_tr_b16 a[130:131], v255 offset:16192\n"
        "v_mfma_f32_16x16x32_bf16 v[220:223], a[52:55], a[244:247], v[220:223]\n"
        "s_waitcnt lgkmcnt(6)\n"
        "v_cvt_pk_bf16_f32 v98, v98, v99\n"
        "v_accvgpr_write_b32 a9, v98\n"
        "v_mfma_f32_16x16x32_bf16 v[104:107], a[204:207], v[148:151], 0\n"
        "buffer_load_dwordx4 v129, s[20:23], s59 offen offset:1024 lds\n"
        "v_mfma_f32_16x16x32_bf16 v[108:111], a[200:203], v[148:151], 0\n"
        "v_cvt_pk_bf16_f32 v100, v100, v101\n"
        "v_accvgpr_write_b32 a10, v100\n"
        "v_mfma_f32_16x16x32_bf16 v[72:75], a[124:127], a[232:235], v[72:75]\n"
        "v_cvt_pk_bf16_f32 v102, v102, v103\n"
        "v_accvgpr_write_b32 a11, v102\n"
        "v_mfma_f32_16x16x32_bf16 v[76:79], a[124:127], a[236:239], v[76:79]\n"
        "buffer_load_dwordx4 v130, s[20:23], s59 offen offset:2048 lds\n"
        "v_mfma_f32_16x16x32_bf16 v[84:87], a[124:127], a[240:243], v[84:87]\n"
        "s_add_u32 m0, s65, 8832\n"
        "v_cvt_pk_bf16_f32 v104, v104, v105\n"
        "v_accvgpr_write_b32 a32, v104\n"
        "v_mfma_f32_16x16x32_bf16 v[92:95], a[124:127], a[244:247], v[92:95]\n"
        "s_waitcnt vmcnt(6)\n"
        "ds_read_b128 a[188:191], v137 offset:0\n"
        "ds_read_b128 a[184:187], v137 offset:768\n"
        "v_mfma_f32_16x16x32_bf16 v[156:159], a[8:11], a[248:251], v[156:159]\n"
        "buffer_load_dwordx4 v128, s[20:23], s59 offen offset:384 lds\n"
        "ds_read_b128 a[180:183], v137 offset:32\n"
        "v_mfma_f32_16x16x32_bf16 v[176:179], a[8:11], a[216:219], v[176:179]\n"
        "ds_read_b128 a[176:179], v137 offset:800\n"
        "ds_read_b128 a[172:175], v137 offset:64\n"
        "v_mfma_f32_16x16x32_bf16 v[196:199], a[8:11], a[220:223], v[196:199]\n"
        "ds_read_b128 a[168:171], v137 offset:832\n"
        "v_cvt_pk_bf16_f32 v106, v106, v107\n"
        "v_mfma_f32_16x16x32_bf16 v[216:219], a[8:11], a[224:227], v[216:219]\n"
        "v_accvgpr_write_b32 a33, v106\n"
        "v_cvt_pk_bf16_f32 v108, v108, v109\n"
        "v_mfma_f32_16x16x32_bf16 v[112:115], a[196:199], v[148:151], 0\n"
        "buffer_load_dwordx4 v129, s[20:23], s59 offen offset:1408 lds\n"
        "v_mfma_f32_16x16x32_bf16 v[116:119], a[192:195], v[148:151], 0\n"
        "s_waitcnt lgkmcnt(6)\n"
        "v_accvgpr_write_b32 a34, v108\n"
        "v_cvt_pk_bf16_f32 v110, v110, v111\n"
        "v_mfma_f32_16x16x32_bf16 v[152:155], a[80:83], a[248:251], v[152:155]\n"
        "ds_read_b128 a[228:231], v241 offset:128\n"
        "v_accvgpr_write_b32 a35, v110\n"
        "v_mfma_f32_16x16x32_bf16 v[172:175], a[80:83], a[216:219], v[172:175]\n"
        "ds_read_b128 a[232:235], v241 offset:1664\n"
        "v_cvt_pk_bf16_f32 v112, v112, v113\n"
        "v_mfma_f32_16x16x32_bf16 v[192:195], a[80:83], a[220:223], v[192:195]\n"
        "buffer_load_dwordx4 v130, s[20:23], s59 offen offset:2432 lds\n"
        "v_mfma_f32_16x16x32_bf16 v[212:215], a[80:83], a[224:227], v[212:215]\n"
        "s_add_u32 m0, s65, 12288\n"
        "ds_read_b128 a[236:239], v241 offset:12416\n"
        "ds_read_b128 a[240:243], v241 offset:13952\n"
        "v_mfma_f32_16x16x32_bf16 v[224:227], a[32:35], a[224:227], v[224:227]\n"
        "ds_read_b128 v[52:55], v141 offset:9216\n"
        "v_accvgpr_write_b32 a56, v112\n"
        "v_mfma_f32_16x16x32_bf16 v[204:207], a[32:35], a[220:223], v[204:207]\n"
        "v_cvt_pk_bf16_f32 v114, v114, v115\n"
        "v_accvgpr_write_b32 a57, v114\n"
        "v_mfma_f32_16x16x32_bf16 v[184:187], a[32:35], a[216:219], v[184:187]\n"
        "buffer_load_dwordx4 v128, s[20:23], s60 offen lds\n"
        "v_mfma_f32_16x16x32_bf16 v[164:167], a[32:35], a[248:251], v[164:167]\n"
        "s_waitcnt lgkmcnt(4)\n"
        "v_cvt_pk_bf16_f32 v116, v116, v117\n"
        "v_accvgpr_write_b32 a58, v116\n"
        "v_mfma_f32_16x16x32_bf16 v[120:123], a[188:191], v[148:151], 0\n"
        "s_waitcnt vmcnt(7)\n"
        "ds_read_b64_tr_b16 a[84:85], v255 offset:3072\n"
        "ds_read_b64_tr_b16 a[86:87], v255 offset:3840\n"
        "v_mfma_f32_16x16x32_bf16 v[124:127], a[184:187], v[148:151], 0\n"
        "ds_read_b64_tr_b16 a[108:109], v255 offset:3104\n"
        "ds_read_b64_tr_b16 a[110:111], v255 offset:3872\n"
        "v_mfma_f32_16x16x32_bf16 v[88:91], a[104:107], a[224:227], v[88:91]\n"
        "buffer_load_dwordx4 v129, s[20:23], s60 offen offset:1024 lds\n"
        "ds_read_b64_tr_b16 a[132:133], v255 offset:3136\n"
        "ds_read_b64_tr_b16 a[134:135], v255 offset:3904\n"
        "v_mfma_f32_16x16x32_bf16 v[80:83], a[104:107], a[220:223], v[80:83]\n"
        "v_cvt_pk_bf16_f32 v118, v118, v119\n"
        "v_accvgpr_write_b32 a59, v118\n"
        "v_mfma_f32_16x16x32_bf16 v[68:71], a[104:107], a[216:219], v[68:71]\n"
        "v_cvt_pk_bf16_f32 v120, v120, v121\n"
        "v_accvgpr_write_b32 a12, v120\n"
        "v_mfma_f32_16x16x32_bf16 v[64:67], a[104:107], a[248:251], v[64:67]\n"
        "buffer_load_dwordx4 v130, s[20:23], s60 offen offset:2048 lds\n"
        "v_mfma_f32_16x16x32_bf16 v[160:163], a[56:59], a[248:251], v[160:163]\n"
        "s_add_u32 m0, s65, 14976\n"
        "v_cvt_pk_bf16_f32 v122, v122, v123\n"
        "v_accvgpr_write_b32 a13, v122\n"
        "v_mfma_f32_16x16x32_bf16 v[180:183], a[56:59], a[216:219], v[180:183]\n"
        "s_waitcnt lgkmcnt(4)\n"
        "v_lshl_add_u32 v52, v52, 2, s67\n"
        "v_lshl_add_u32 v53, v53, 2, s67\n"
        "v_mfma_f32_16x16x32_bf16 v[200:203], a[56:59], a[220:223], v[200:203]\n"
        "ds_read_b32 v52, v52\n"
        "ds_read_b32 v53, v53\n"
        "v_mfma_f32_16x16x32_bf16 v[220:223], a[56:59], a[224:227], v[220:223]\n"
        "buffer_load_dwordx4 v128, s[20:23], s60 offen offset:384 lds\n"
        "v_mfma_f32_16x16x32_bf16 v[96:99], a[180:183], v[148:151], 0\n"
        "v_lshl_add_u32 v54, v54, 2, s67\n"
        "v_lshl_add_u32 v55, v55, 2, s67\n"
        "v_mfma_f32_16x16x32_bf16 v[100:103], a[176:179], v[148:151], 0\n"
        "ds_read_b32 v54, v54\n"
        "ds_read_b32 v55, v55\n"
        "v_mfma_f32_16x16x32_bf16 v[72:75], a[128:131], a[248:251], v[72:75]\n"
        "v_cvt_pk_bf16_f32 v124, v124, v125\n"
        "v_accvgpr_write_b32 a14, v124\n"
        "v_mfma_f32_16x16x32_bf16 v[76:79], a[128:131], a[216:219], v[76:79]\n"
        "buffer_load_dwordx4 v129, s[20:23], s60 offen offset:1408 lds\n"
        "v_mfma_f32_16x16x32_bf16 v[84:87], a[128:131], a[220:223], v[84:87]\n"
        "v_cvt_pk_bf16_f32 v126, v126, v127\n"
        "v_accvgpr_write_b32 a15, v126\n"
        "v_mfma_f32_16x16x32_bf16 v[92:95], a[128:131], a[224:227], v[92:95]\n"
        "v_cvt_pk_bf16_f32 v96, v96, v97\n"
        "v_accvgpr_write_b32 a36, v96\n"
        "v_mfma_f32_16x16x32_bf16 v[156:159], a[12:15], a[228:231], v[156:159]\n"
        "s_waitcnt vmcnt(8)\n"
        "ds_read_b128 a[212:215], v137 offset:6144\n"
        "ds_read_b128 a[208:211], v137 offset:6912\n"
        "v_mfma_f32_16x16x32_bf16 v[176:179], a[12:15], a[232:235], v[176:179]\n"
        "buffer_load_dwordx4 v130, s[20:23], s60 offen offset:2432 lds\n"
        "ds_read_b128 a[204:207], v137 offset:6176\n"
        "v_mfma_f32_16x16x32_bf16 v[196:199], a[12:15], a[236:239], v[196:199]\n"
        "ds_read_b128 a[200:203], v137 offset:6944\n"
        "ds_read_b128 a[196:199], v137 offset:6208\n"
        "v_mfma_f32_16x16x32_bf16 v[216:219], a[12:15], a[240:243], v[216:219]\n"
        "ds_read_b128 a[192:195], v137 offset:6976\n"
        "v_cvt_pk_bf16_f32 v98, v98, v99\n"
        "v_mfma_f32_16x16x32_bf16 v[104:107], a[172:175], v[148:151], 0\n"
        "v_accvgpr_write_b32 a37, v98\n"
        "v_cvt_pk_bf16_f32 v100, v100, v101\n"
        "v_mfma_f32_16x16x32_bf16 v[108:111], a[168:171], v[148:151], 0\n"
        "v_accvgpr_write_b32 a38, v100\n"
        "v_cvt_pk_bf16_f32 v102, v102, v103\n"
        "v_mfma_f32_16x16x32_bf16 v[152:155], a[84:87], a[228:231], v[152:155]\n"
        "ds_read_b128 a[244:247], v240 offset:256\n"
        "v_accvgpr_write_b32 a39, v102\n"
        "v_mfma_f32_16x16x32_bf16 v[172:175], a[84:87], a[232:235], v[172:175]\n"
        "s_mov_b32 m0, s48\n"
        "ds_read_b128 a[248:251], v240 offset:1792\n"
        "v_cvt_pk_bf16_f32 v104, v104, v105\n"
        "v_mfma_f32_16x16x32_bf16 v[192:195], a[84:87], a[236:239], v[192:195]\n"
        "s_sub_u32 s64, s36, 4194304\n"
        "ds_read_b128 a[216:219], v240 offset:12544\n"
        "v_accvgpr_write_b32 a60, v104\n"
        "v_mfma_f32_16x16x32_bf16 v[212:215], a[84:87], a[240:243], v[212:215]\n"
        "buffer_load_dwordx4 v249, s[12:15], s64 offen lds\n"
        "v_mfma_f32_16x16x32_bf16 v[224:227], a[36:39], a[240:243], v[224:227]\n"
        "ds_read_b128 a[220:223], v240 offset:14080\n"
        "v_cvt_pk_bf16_f32 v106, v106, v107\n"
        "v_mfma_f32_16x16x32_bf16 v[204:207], a[36:39], a[236:239], v[204:207]\n"
        "ds_read_b128 v[56:59], v142 offset:9216\n"
        "v_accvgpr_write_b32 a61, v106\n"
        "v_mfma_f32_16x16x32_bf16 v[184:187], a[36:39], a[232:235], v[184:187]\n"
        "s_waitcnt vmcnt(7)\n"
        "ds_read_b64_tr_b16 a[88:89], v255 offset:9216\n"
        "ds_read_b64_tr_b16 a[90:91], v255 offset:9984\n"
        "v_mfma_f32_16x16x32_bf16 v[164:167], a[36:39], a[228:231], v[164:167]\n"
        "s_waitcnt lgkmcnt(6)\n"
        "ds_read_b64_tr_b16 a[112:113], v255 offset:9248\n"
        "ds_read_b64_tr_b16 a[114:115], v255 offset:10016\n"
        "v_mfma_f32_16x16x32_bf16 v[112:115], a[212:215], v[148:151], 0\n"
        "buffer_load_dwordx4 v253, s[12:15], s64 offen offset:1024 lds\n"
        "ds_read_b64_tr_b16 a[136:137], v255 offset:9280\n"
        "v_mfma_f32_16x16x32_bf16 v[116:119], a[208:211], v[148:151], 0\n"
        "ds_read_b64_tr_b16 a[138:139], v255 offset:10048\n"
        "s_add_u32 m0, s65, 0\n"
        "v_cvt_pk_bf16_f32 v108, v108, v109\n"
        "v_mfma_f32_16x16x32_bf16 v[88:91], a[108:111], a[240:243], v[88:91]\n"
        "s_mov_b32 s64, 768\n"
        "v_accvgpr_write_b32 a62, v108\n"
        "v_cvt_pk_bf16_f32 v110, v110, v111\n"
        "v_mfma_f32_16x16x32_bf16 v[80:83], a[108:111], a[236:239], v[80:83]\n"
        "buffer_load_dwordx4 v128, s[20:23], s64 offen lds\n"
        "v_mfma_f32_16x16x32_bf16 v[68:71], a[108:111], a[232:235], v[68:71]\n"
        "v_accvgpr_write_b32 a63, v110\n"
        "v_cvt_pk_bf16_f32 v112, v112, v113\n"
        "v_mfma_f32_16x16x32_bf16 v[64:67], a[108:111], a[228:231], v[64:67]\n"
        "v_accvgpr_write_b32 a16, v112\n"
        "v_cvt_pk_bf16_f32 v114, v114, v115\n"
        "v_mfma_f32_16x16x32_bf16 v[160:163], a[60:63], a[228:231], v[160:163]\n"
        "v_accvgpr_write_b32 a17, v114\n"
        "v_cvt_pk_bf16_f32 v116, v116, v117\n"
        "v_mfma_f32_16x16x32_bf16 v[180:183], a[60:63], a[232:235], v[180:183]\n"
        "buffer_load_dwordx4 v129, s[20:23], s64 offen offset:1024 lds\n"
        "v_mfma_f32_16x16x32_bf16 v[200:203], a[60:63], a[236:239], v[200:203]\n"
        "ds_read_b128 v[60:63], v143 offset:9216\n"
        "v_accvgpr_write_b32 a18, v116\n"
        "v_mfma_f32_16x16x32_bf16 v[220:223], a[60:63], a[240:243], v[220:223]\n"
        "v_cvt_pk_bf16_f32 v118, v118, v119\n"
        "v_accvgpr_write_b32 a19, v118\n"
        "v_mfma_f32_16x16x32_bf16 v[120:123], a[204:207], v[148:151], 0\n"
        "s_waitcnt lgkmcnt(1)\n"
        "v_lshl_add_u32 v56, v56, 2, s67\n"
        "v_lshl_add_u32 v57, v57, 2, s67\n"
        "v_mfma_f32_16x16x32_bf16 v[124:127], a[200:203], v[148:151], 0\n"
        "buffer_load_dwordx4 v130, s[20:23], s64 offen offset:2048 lds\n"
        "v_mfma_f32_16x16x32_bf16 v[72:75], a[132:135], a[228:231], v[72:75]\n"
        "s_add_u32 m0, s65, 2304\n"
        "ds_read_b32 v56, v56\n"
        "ds_read_b32 v57, v57\n"
        "v_mfma_f32_16x16x32_bf16 v[76:79], a[132:135], a[232:235], v[76:79]\n"
        "v_lshl_add_u32 v58, v58, 2, s67\n"
        "v_lshl_add_u32 v59, v59, 2, s67\n"
        "v_mfma_f32_16x16x32_bf16 v[84:87], a[132:135], a[236:239], v[84:87]\n"
        "ds_read_b32 v58, v58\n"
        "ds_read_b32 v59, v59\n"
        "v_mfma_f32_16x16x32_bf16 v[92:95], a[132:135], a[240:243], v[92:95]\n"
        "buffer_load_dwordx4 v128, s[20:23], s56 offen offset:768 lds\n"
        "v_mfma_f32_16x16x32_bf16 v[156:159], a[16:19], a[244:247], v[156:159]\n"
        "v_cvt_pk_bf16_f32 v120, v120, v121\n"
        "v_accvgpr_write_b32 a40, v120\n"
        "v_mfma_f32_16x16x32_bf16 v[176:179], a[16:19], a[248:251], v[176:179]\n"
        "v_cvt_pk_bf16_f32 v122, v122, v123\n"
        "v_accvgpr_write_b32 a41, v122\n"
        "v_mfma_f32_16x16x32_bf16 v[196:199], a[16:19], a[216:219], v[196:199]\n"
        "v_cvt_pk_bf16_f32 v124, v124, v125\n"
        "v_accvgpr_write_b32 a42, v124\n"
        "v_mfma_f32_16x16x32_bf16 v[216:219], a[16:19], a[220:223], v[216:219]\n"
        "buffer_load_dwordx4 v129, s[20:23], s56 offen offset:1792 lds\n"
        "v_mfma_f32_16x16x32_bf16 v[96:99], a[196:199], v[148:151], 0\n"
        "ds_read_b128 a[224:227], v241 offset:256\n"
        "v_cvt_pk_bf16_f32 v126, v126, v127\n"
        "v_mfma_f32_16x16x32_bf16 v[100:103], a[192:195], v[148:151], 0\n"
        "s_waitcnt vmcnt(10)\n"
        "ds_read_b128 a[188:191], v137 offset:12288\n"
        "ds_read_b128 a[184:187], v137 offset:13056\n"
        "v_mfma_f32_16x16x32_bf16 v[152:155], a[88:91], a[244:247], v[152:155]\n"
        "ds_read_b128 a[180:183], v137 offset:12320\n"
        "ds_read_b128 a[176:179], v137 offset:13088\n"
        "v_mfma_f32_16x16x32_bf16 v[172:175], a[88:91], a[248:251], v[172:175]\n"
        "buffer_load_dwordx4 v130, s[20:23], s56 offen offset:2816 lds\n"
        "ds_read_b128 a[172:175], v137 offset:12352\n"
        "v_mfma_f32_16x16x32_bf16 v[192:195], a[88:91], a[216:219], v[192:195]\n"
        "ds_read_b128 a[168:171], v137 offset:13120\n"
        "s_add_u32 m0, s65, 5376\n"
        "ds_read_b128 a[228:231], v241 offset:1792\n"
        "v_accvgpr_write_b32 a43, v126\n"
        "v_mfma_f32_16x16x32_bf16 v[212:215], a[88:91], a[220:223], v[212:215]\n"
        "ds_read_b128 a[232:235], v241 offset:12544\n"
        "ds_read_b128 a[236:239], v241 offset:14080\n"
        "v_mfma_f32_16x16x32_bf16 v[224:227], a[40:43], a[220:223], v[224:227]\n"
        "s_waitcnt lgkmcnt(10)\n"
        "v_lshl_add_u32 v60, v60, 2, s67\n"
        "v_lshl_add_u32 v61, v61, 2, s67\n"
        "v_lshl_add_u32 v62, v62, 2, s67\n"
        "v_mfma_f32_16x16x32_bf16 v[204:207], a[40:43], a[216:219], v[204:207]\n"
        "buffer_load_dwordx4 v128, s[20:23], s57 offen offset:768 lds\n"
        "v_lshl_add_u32 v63, v63, 2, s67\n"
        "v_mfma_f32_16x16x32_bf16 v[184:187], a[40:43], a[248:251], v[184:187]\n"
        "ds_read_b32 v60, v60\n"
        "ds_read_b32 v61, v61\n"
        "v_mfma_f32_16x16x32_bf16 v[164:167], a[40:43], a[244:247], v[164:167]\n"
        "s_waitcnt lgkmcnt(7)\n"
        "ds_read_b32 v62, v62\n"
        "ds_read_b32 v63, v63\n"
        "v_mfma_f32_16x16x32_bf16 v[104:107], a[188:191], v[148:151], 0\n"
        "v_cvt_pk_bf16_f32 v96, v96, v97\n"
        "v_accvgpr_write_b32 a64, v96\n"
        "v_mfma_f32_16x16x32_bf16 v[108:111], a[184:187], v[148:151], 0\n"
        "buffer_load_dwordx4 v129, s[20:23], s57 offen offset:1792 lds\n"
        "v_mfma_f32_16x16x32_bf16 v[88:91], a[112:115], a[220:223], v[88:91]\n"
        "v_cvt_pk_bf16_f32 v98, v98, v99\n"
        "v_accvgpr_write_b32 a65, v98\n"
        "v_mfma_f32_16x16x32_bf16 v[80:83], a[112:115], a[216:219], v[80:83]\n"
        "v_cvt_pk_bf16_f32 v100, v100, v101\n"
        "v_accvgpr_write_b32 a66, v100\n"
        "v_mfma_f32_16x16x32_bf16 v[68:71], a[112:115], a[248:251], v[68:71]\n"
        "v_cvt_pk_bf16_f32 v102, v102, v103\n"
        "v_accvgpr_write_b32 a67, v102\n"
        "v_mfma_f32_16x16x32_bf16 v[64:67], a[112:115], a[244:247], v[64:67]\n"
        "s_waitcnt vmcnt(10)\n"
        "ds_read_b64_tr_b16 a[92:93], v255 offset:15360\n"
        "ds_read_b64_tr_b16 a[94:95], v255 offset:16128\n"
        "v_mfma_f32_16x16x32_bf16 v[160:163], a[64:67], a[244:247], v[160:163]\n"
        "ds_read_b64_tr_b16 a[116:117], v255 offset:15392\n"
        "ds_read_b64_tr_b16 a[118:119], v255 offset:16160\n"
        "v_mfma_f32_16x16x32_bf16 v[180:183], a[64:67], a[248:251], v[180:183]\n"
        "ds_read_b64_tr_b16 a[140:141], v255 offset:15424\n"
        "ds_read_b64_tr_b16 a[142:143], v255 offset:16192\n"
        "v_mfma_f32_16x16x32_bf16 v[200:203], a[64:67], a[216:219], v[200:203]\n"
        "v_cvt_pk_bf16_f32 v104, v104, v105\n"
        "v_accvgpr_write_b32 a20, v104\n"
        "v_mfma_f32_16x16x32_bf16 v[220:223], a[64:67], a[220:223], v[220:223]\n"
        "v_cvt_pk_bf16_f32 v106, v106, v107\n"
        "v_accvgpr_write_b32 a21, v106\n"
        "v_mfma_f32_16x16x32_bf16 v[112:115], a[180:183], v[148:151], 0\n"
        "v_cvt_pk_bf16_f32 v108, v108, v109\n"
        "v_accvgpr_write_b32 a22, v108\n"
        "v_mfma_f32_16x16x32_bf16 v[116:119], a[176:179], v[148:151], 0\n"
        "v_cvt_pk_bf16_f32 v110, v110, v111\n"
        "v_accvgpr_write_b32 a23, v110\n"
        "v_mfma_f32_16x16x32_bf16 v[72:75], a[136:139], a[244:247], v[72:75]\n"
        "v_mfma_f32_16x16x32_bf16 v[76:79], a[136:139], a[248:251], v[76:79]\n"
        "v_mfma_f32_16x16x32_bf16 v[156:159], a[20:23], a[224:227], v[156:159]\n"
        "s_waitcnt lgkmcnt(0)\n"
        "v_cvt_pk_bf16_f32 v112, v112, v113\n"
        "v_accvgpr_write_b32 a44, v112\n"
        "v_mfma_f32_16x16x32_bf16 v[176:179], a[20:23], a[228:231], v[176:179]\n"
        "v_cvt_pk_bf16_f32 v114, v114, v115\n"
        "v_accvgpr_write_b32 a45, v114\n"
        "v_mfma_f32_16x16x32_bf16 v[196:199], a[20:23], a[232:235], v[196:199]\n"
        "v_cvt_pk_bf16_f32 v116, v116, v117\n"
        "v_accvgpr_write_b32 a46, v116\n"
        "v_cvt_pk_bf16_f32 v156, v156, v157\n"
        "v_mfma_f32_16x16x32_bf16 v[216:219], a[20:23], a[236:239], v[216:219]\n"
        "v_cvt_pk_bf16_f32 v157, v158, v159\n"
        "v_cvt_pk_bf16_f32 v176, v176, v177\n"
        "v_cvt_pk_bf16_f32 v177, v178, v179\n"
        "v_mfma_f32_16x16x32_bf16 v[120:123], a[172:175], v[148:151], 0\n"
        "v_cvt_pk_bf16_f32 v196, v196, v197\n"
        "v_cvt_pk_bf16_f32 v197, v198, v199\n"
        "v_mfma_f32_16x16x32_bf16 v[124:127], a[168:171], v[148:151], 0\n"
        "v_cvt_pk_bf16_f32 v118, v118, v119\n"
        "v_cvt_pk_bf16_f32 v216, v216, v217\n"
        "v_cvt_pk_bf16_f32 v217, v218, v219\n"
        "v_mfma_f32_16x16x32_bf16 v[84:87], a[136:139], a[216:219], v[84:87]\n"
        "v_accvgpr_write_b32 a47, v118\n"
        "v_cvt_pk_bf16_f32 v120, v120, v121\n"
        "v_mfma_f32_16x16x32_bf16 v[92:95], a[136:139], a[220:223], v[92:95]\n"
        "v_accvgpr_write_b32 a68, v120\n"
        "v_cvt_pk_bf16_f32 v122, v122, v123\n"
        "v_mfma_f32_16x16x32_bf16 v[224:227], a[44:47], a[236:239], v[224:227]\n"
        "v_accvgpr_write_b32 a69, v122\n"
        "v_cvt_pk_bf16_f32 v124, v124, v125\n"
        "v_mfma_f32_16x16x32_bf16 v[204:207], a[44:47], a[232:235], v[204:207]\n"
        "v_accvgpr_write_b32 a70, v124\n"
        "v_cvt_pk_bf16_f32 v126, v126, v127\n"
        "v_mfma_f32_16x16x32_bf16 v[184:187], a[44:47], a[228:231], v[184:187]\n"
        "v_accvgpr_write_b32 a71, v126\n"
        "s_nop 0\n"
        "v_cvt_pk_bf16_f32 v218, v224, v225\n"
        "v_mfma_f32_16x16x32_bf16 v[164:167], a[44:47], a[224:227], v[164:167]\n"
        "v_cvt_pk_bf16_f32 v219, v226, v227\n"
        "v_cvt_pk_bf16_f32 v198, v204, v205\n"
        "v_cvt_pk_bf16_f32 v199, v206, v207\n"
        "v_mfma_f32_16x16x32_bf16 v[152:155], a[92:95], a[224:227], v[152:155]\n"
        "v_cvt_pk_bf16_f32 v178, v184, v185\n"
        "v_cvt_pk_bf16_f32 v179, v186, v187\n"
        "v_mfma_f32_16x16x32_bf16 v[64:67], a[116:119], a[224:227], v[64:67]\n"
        "buffer_load_dwordx4 v130, s[20:23], s57 offen offset:2816 lds\n"
        "v_cvt_pk_bf16_f32 v158, v164, v165\n"
        "v_mfma_f32_16x16x32_bf16 v[72:75], a[140:143], a[224:227], v[72:75]\n"
        "s_add_u32 m0, s65, 8448\n"
        "v_cvt_pk_bf16_f32 v159, v166, v167\n"
        "v_cvt_pk_bf16_f32 v152, v152, v153\n"
        "v_mfma_f32_16x16x32_bf16 v[172:175], a[92:95], a[228:231], v[172:175]\n"
        "v_cvt_pk_bf16_f32 v153, v154, v155\n"
        "v_cvt_pk_bf16_f32 v154, v64, v65\n"
        "v_mfma_f32_16x16x32_bf16 v[160:163], a[68:71], a[224:227], v[160:163]\n"
        "v_cvt_pk_bf16_f32 v155, v66, v67\n"
        "v_cvt_pk_bf16_f32 v168, v72, v73\n"
        "v_mfma_f32_16x16x32_bf16 v[180:183], a[68:71], a[228:231], v[180:183]\n"
        "buffer_load_dwordx4 v128, s[20:23], s58 offen offset:768 lds\n"
        "v_mfma_f32_16x16x32_bf16 v[200:203], a[68:71], a[232:235], v[200:203]\n"
        "v_cvt_pk_bf16_f32 v169, v74, v75\n"
        "v_cvt_pk_bf16_f32 v172, v172, v173\n"
        "v_mfma_f32_16x16x32_bf16 v[220:223], a[68:71], a[236:239], v[220:223]\n"
        "v_cvt_pk_bf16_f32 v166, v160, v161\n"
        "v_cvt_pk_bf16_f32 v160, v160, v161\n"
        "v_cvt_pk_bf16_f32 v161, v162, v163\n"
        "v_cvt_pk_bf16_f32 v167, v162, v163\n"
        "v_cvt_pk_bf16_f32 v186, v180, v181\n"
        "v_cvt_pk_bf16_f32 v180, v180, v181\n"
        "v_mfma_f32_16x16x32_bf16 v[68:71], a[116:119], a[228:231], v[68:71]\n"
        "buffer_load_dwordx4 v129, s[20:23], s58 offen offset:1792 lds\n"
        "v_cvt_pk_bf16_f32 v181, v182, v183\n"
        "v_cvt_pk_bf16_f32 v187, v182, v183\n"
        "v_cvt_pk_bf16_f32 v206, v200, v201\n"
        "v_cvt_pk_bf16_f32 v200, v200, v201\n"
        "v_mfma_f32_16x16x32_bf16 v[76:79], a[140:143], a[228:231], v[76:79]\n"
        "s_waitcnt vmcnt(11)\n"
        "s_barrier\n"
        "ds_read_b128 v[162:165], v251 offset:0\n"
        "ds_read_b128 v[182:185], v251 offset:512\n"
        "v_cvt_pk_bf16_f32 v201, v202, v203\n"
        "v_cvt_pk_bf16_f32 v207, v202, v203\n"
        "v_cvt_pk_bf16_f32 v226, v220, v221\n"
        "v_cvt_pk_bf16_f32 v220, v220, v221\n"
        "v_mfma_f32_16x16x32_bf16 v[192:195], a[92:95], a[232:235], v[192:195]\n"
        "ds_read_b128 v[202:205], v251 offset:4096\n"
        "ds_read_b128 a[240:243], v240 offset:0\n"
        "v_cvt_pk_bf16_f32 v221, v222, v223\n"
        "v_cvt_pk_bf16_f32 v227, v222, v223\n"
        "v_cvt_pk_bf16_f32 v173, v174, v175\n"
        "v_mfma_f32_16x16x32_bf16 v[80:83], a[116:119], a[232:235], v[80:83]\n"
        "buffer_load_dwordx4 v130, s[20:23], s58 offen offset:2816 lds\n"
        "v_mfma_f32_16x16x32_bf16 v[84:87], a[140:143], a[232:235], v[84:87]\n"
        "s_add_u32 m0, s65, 11520\n"
        "ds_read_b128 a[244:247], v241 offset:0\n"
        "ds_read_b128 a[248:251], v240 offset:128\n"
        "v_mfma_f32_16x16x32_bf16 v[212:215], a[92:95], a[236:239], v[212:215]\n"
        "v_cvt_pk_bf16_f32 v174, v68, v69\n"
        "v_cvt_pk_bf16_f32 v175, v70, v71\n"
        "v_mfma_f32_16x16x32_bf16 v[88:91], a[116:119], a[236:239], v[88:91]\n"
        "buffer_load_dwordx4 v128, s[20:23], s59 offen offset:768 lds\n"
        "v_mfma_f32_16x16x32_bf16 v[92:95], a[140:143], a[236:239], v[92:95]\n"
        "s_waitcnt vmcnt(10)\n"
        "ds_read_b64_tr_b16 a[144:145], v255 offset:0\n"
        "ds_read_b64_tr_b16 a[146:147], v255 offset:768\n"
        "v_mfma_f32_16x16x32_bf16 v[64:67], v[152:155], v[156:159], v[0:3]\n"
        "s_waitcnt lgkmcnt(6)\n"
        "ds_read_b64_tr_b16 a[168:169], v255 offset:32\n"
        "ds_read_b64_tr_b16 a[170:171], v255 offset:800\n"
        "v_cvt_pk_bf16_f32 v190, v76, v77\n"
        "v_cvt_pk_bf16_f32 v191, v78, v79\n"
        "v_mfma_f32_16x16x32_bf16 v[64:67], v[168:171], v[160:163], v[64:67]\n"
        "ds_read_b64_tr_b16 a[192:193], v255 offset:64\n"
        "ds_read_b64_tr_b16 a[194:195], v255 offset:832\n"
        "v_cvt_pk_bf16_f32 v192, v192, v193\n"
        "v_cvt_pk_bf16_f32 v193, v194, v195\n"
        "v_mfma_f32_16x16x32_bf16 v[96:99], v[172:175], v[156:159], v[4:7]\n"
        "buffer_load_dwordx4 v129, s[20:23], s59 offen offset:1792 lds\n"
        "v_cvt_pk_bf16_f32 v194, v80, v81\n"
        "v_cvt_pk_bf16_f32 v195, v82, v83\n"
        "v_exp_f32 v64, v64\n"
        "v_mfma_f32_16x16x32_bf16 v[96:99], v[188:191], v[164:167], v[96:99]\n"
        "ds_read_b128 v[162:165], v252 offset:0\n"
        "v_exp_f32 v65, v65\n"
        "v_exp_f32 v66, v66\n"
        "v_cvt_pk_bf16_f32 v64, v64, v65\n"
        "v_mfma_f32_16x16x32_bf16 v[72:75], v[152:155], v[176:179], v[16:19]\n"
        "buffer_load_dwordx4 v130, s[20:23], s59 offen offset:2816 lds\n"
        "v_cvt_pk_bf16_f32 v208, v84, v85\n"
        "v_cvt_pk_bf16_f32 v209, v86, v87\n"
        "v_exp_f32 v96, v96\n"
        "v_mfma_f32_16x16x32_bf16 v[72:75], v[168:171], v[180:183], v[72:75]\n"
        "s_add_u32 m0, s65, 14592\n"
        "s_waitcnt vmcnt(9)\n"
        "ds_read_b64_tr_b16 a[148:149], v255 offset:3072\n"
        "ds_read_b64_tr_b16 a[150:151], v255 offset:3840\n"
        "v_exp_f32 v67, v67\n"
        "v_exp_f32 v97, v97\n"
        "v_mfma_f32_16x16x32_bf16 v[100:103], v[172:175], v[176:179], v[20:23]\n"
        "buffer_load_dwordx4 v128, s[20:23], s60 offen offset:768 lds\n"
        "ds_read_b64_tr_b16 a[172:173], v255 offset:3104\n"
        "ds_read_b64_tr_b16 a[174:175], v255 offset:3872\n"
        "v_cvt_pk_bf16_f32 v212, v212, v213\n"
        "v_cvt_pk_bf16_f32 v213, v214, v215\n"
        "v_mfma_f32_16x16x32_bf16 v[100:103], v[188:191], v[184:187], v[100:103]\n"
        "ds_read_b128 v[182:185], v252 offset:512\n"
        "ds_read_b64_tr_b16 a[196:197], v255 offset:3136\n"
        "ds_read_b64_tr_b16 a[198:199], v255 offset:3904\n"
        "v_cvt_pk_bf16_f32 v65, v66, v67\n"
        "v_exp_f32 v98, v98\n"
        "v_mfma_f32_16x16x32_bf16 v[68:71], v[192:195], v[156:159], v[8:11]\n"
        "s_waitcnt lgkmcnt(7)\n"
        "ds_read_b128 a[216:219], v240 offset:1536\n"
        "ds_read_b128 a[220:223], v241 offset:1536\n"
        "v_cvt_pk_bf16_f32 v66, v96, v97\n"
        "v_exp_f32 v72, v72\n"
        "v_cvt_pk_bf16_f32 v214, v88, v89\n"
        "v_mfma_f32_16x16x32_bf16 v[68:71], v[208:211], v[160:163], v[68:71]\n"
        "buffer_load_dwordx4 v129, s[20:23], s60 offen offset:1792 lds\n"
        "s_waitcnt vmcnt(8)\n"
        "ds_read_b64_tr_b16 a[152:153], v255 offset:6144\n"
        "ds_read_b64_tr_b16 a[154:155], v255 offset:6912\n"
        "v_cvt_pk_bf16_f32 v215, v90, v91\n"
        "v_cvt_pk_bf16_f32 v230, v92, v93\n"
        "v_mfma_f32_16x16x32_bf16 v[76:79], v[192:195], v[176:179], v[24:27]\n"
        "s_waitcnt lgkmcnt(6)\n"
        "ds_read_b64_tr_b16 a[176:177], v255 offset:6176\n"
        "ds_read_b64_tr_b16 a[178:179], v255 offset:6944\n"
        "v_exp_f32 v99, v99\n"
        "v_exp_f32 v73, v73\n"
        "v_mfma_f32_16x16x32_bf16 v[76:79], v[208:211], v[180:183], v[76:79]\n"
        "buffer_load_dwordx4 v130, s[20:23], s60 offen offset:2816 lds\n"
        "ds_read_b64_tr_b16 a[200:201], v255 offset:6208\n"
        "ds_read_b64_tr_b16 a[202:203], v255 offset:6976\n"
        "v_cvt_pk_bf16_f32 v231, v94, v95\n"
        "v_cvt_pk_bf16_f32 v67, v98, v99\n"
        "v_mfma_f32_16x16x32_bf16 v[104:107], v[212:215], v[156:159], v[12:15]\n"
        "s_add_u32 m0, s40, 98304\n"
        "ds_read_b128 a[224:227], v240 offset:1664\n"
        "v_exp_f32 v74, v74\n"
        "v_mfma_f32_16x16x32_bf16 v[104:107], v[228:231], v[164:167], v[104:107]\n"
        "s_sub_u32 s64, s47, 6291456\n"
        "v_cvt_pk_bf16_f32 v72, v72, v73\n"
        "v_exp_f32 v100, v100\n"
        "v_mfma_f32_16x16x32_bf16 v[108:111], v[212:215], v[176:179], v[28:31]\n"
        "buffer_load_dwordx4 v242, s[8:11], s64 offen lds\n"
        "v_mfma_f32_16x16x32_bf16 v[108:111], v[228:231], v[184:187], v[108:111]\n"
        "s_add_u32 m0, s40, 99328\n"
        "v_exp_f32 v75, v75\n"
        "v_mfma_f32_16x16x32_bf16 v[80:83], v[152:155], v[196:199], v[32:35]\n"
        "ds_read_b128 a[228:231], v240 offset:12288\n"
        "v_exp_f32 v101, v101\n"
        "v_mfma_f32_16x16x32_bf16 v[80:83], v[168:171], v[200:203], v[80:83]\n"
        ".long 0xbec200ff\n"
        "42:\n"
        "s_branch 60b\n"
        "buffer_load_dwordx4 v243, s[8:11], s64 offen lds\n"
        "v_cvt_pk_bf16_f32 v73, v74, v75\n"
        "v_mfma_f32_16x16x32_bf16 v[112:115], a[240:243], a[144:147], 0\n"
        "s_add_u32 m0, s40, 100352\n"
        "ds_read_b128 a[232:235], v241 offset:12288\n"
        "ds_read_b128 a[236:239], v240 offset:12416\n"
        "v_cvt_pk_bf16_f32 v74, v100, v101\n"
        "v_mfma_f32_16x16x32_bf16 v[112:115], a[244:247], a[148:151], v[112:115]\n"
        "s_waitcnt lgkmcnt(3)\n"
        "v_exp_f32 v102, v102\n"
        "v_mfma_f32_16x16x32_bf16 v[112:115], a[248:251], a[152:155], v[112:115]\n"
        "buffer_load_dwordx4 v244, s[8:11], s64 offen lds\n"
        "v_mfma_f32_16x16x32_bf16 v[116:119], a[248:251], a[176:179], 0\n"
        "s_add_u32 m0, s40, 110592\n"
        "v_exp_f32 v68, v68\n"
        "v_mfma_f32_16x16x32_bf16 v[116:119], a[244:247], a[172:175], v[116:119]\n"
        "s_sub_u32 s64, s47, 5505024\n"
        "v_exp_f32 v103, v103\n"
        "v_mfma_f32_16x16x32_bf16 v[116:119], a[240:243], a[168:171], v[116:119]\n"
        "buffer_load_dwordx4 v242, s[8:11], s64 offen lds\n"
        "v_cvt_pk_bf16_f32 v75, v102, v103\n"
        "v_mfma_f32_16x16x32_bf16 v[96:99], v[172:175], v[196:199], v[36:39]\n"
        "s_add_u32 m0, s40, 111616\n"
        "s_waitcnt vmcnt(10)\n"
        "ds_read_b64_tr_b16 a[156:157], v255 offset:9216\n"
        "ds_read_b64_tr_b16 a[158:159], v255 offset:9984\n"
        "v_mfma_f32_16x16x32_bf16 v[96:99], v[188:191], v[204:207], v[96:99]\n"
        "ds_read_b64_tr_b16 a[180:181], v255 offset:9248\n"
        "ds_read_b64_tr_b16 a[182:183], v255 offset:10016\n"
        "ds_read_b128 v[202:205], v252 offset:4096\n"
        "v_mfma_f32_16x16x32_bf16 v[120:123], a[240:243], a[192:195], 0\n"
        "buffer_load_dwordx4 v243, s[8:11], s64 offen lds\n"
        "ds_read_b64_tr_b16 a[204:205], v255 offset:9280\n"
        "v_mfma_f32_16x16x32_bf16 v[120:123], a[244:247], a[196:199], v[120:123]\n"
        "ds_read_b64_tr_b16 a[206:207], v255 offset:10048\n"
        "s_add_u32 m0, s40, 112640\n"
        "ds_read_b128 a[240:243], v240 offset:13824\n"
        "v_mfma_f32_16x16x32_bf16 v[120:123], a[248:251], a[200:203], v[120:123]\n"
        "s_waitcnt vmcnt(8)\n"
        "ds_read_b64_tr_b16 a[160:161], v255 offset:12288\n"
        "ds_read_b64_tr_b16 a[162:163], v255 offset:13056\n"
        "v_mfma_f32_16x16x32_bf16 v[124:127], a[216:219], a[144:147], 0\n"
        "buffer_load_dwordx4 v244, s[8:11], s64 offen lds\n"
        "ds_read_b64_tr_b16 a[184:185], v255 offset:12320\n"
        "v_mfma_f32_16x16x32_bf16 v[124:127], a[220:223], a[148:151], v[124:127]\n"
        "ds_read_b64_tr_b16 a[186:187], v255 offset:13088\n"
        "s_xor_b32 m0, s48, 8192\n"
        "ds_read_b64_tr_b16 a[208:209], v255 offset:12352\n"
        "ds_read_b64_tr_b16 a[210:211], v255 offset:13120\n"
        "v_mfma_f32_16x16x32_bf16 v[124:127], a[224:227], a[152:155], v[124:127]\n"
        "s_sub_u32 s64, s36, 2097152\n"
        "ds_read_b128 a[244:247], v241 offset:13824\n"
        "ds_read_b128 a[248:251], v240 offset:13952\n"
        "v_mfma_f32_16x16x32_bf16 v[156:159], a[224:227], a[176:179], 0\n"
        "buffer_load_dwordx4 v249, s[12:15], s64 offen lds\n"
        "v_mfma_f32_16x16x32_bf16 v[156:159], a[220:223], a[172:175], v[156:159]\n"
        "ds_read_b128 v[222:225], v251 offset:4608\n"
        "v_exp_f32 v69, v69\n"
        "v_mfma_f32_16x16x32_bf16 v[156:159], a[216:219], a[168:171], v[156:159]\n"
        "v_exp_f32 v70, v70\n"
        "v_cvt_pk_bf16_f32 v68, v68, v69\n"
        "v_mfma_f32_16x16x32_bf16 v[84:87], v[192:195], v[196:199], v[40:43]\n"
        "s_waitcnt lgkmcnt(7)\n"
        "v_exp_f32 v76, v76\n"
        "v_mfma_f32_16x16x32_bf16 v[84:87], v[208:211], v[200:203], v[84:87]\n"
        "buffer_load_dwordx4 v253, s[12:15], s64 offen offset:1024 lds\n"
        "v_mfma_f32_16x16x32_bf16 v[160:163], a[216:219], a[192:195], 0\n"
        "s_add_u32 m0, s65, 12288\n"
        "v_exp_f32 v71, v71\n"
        "v_mfma_f32_16x16x32_bf16 v[160:163], a[220:223], a[196:199], v[160:163]\n"
        "ds_read_b128 a[216:219], v241 offset:128\n"
        "v_exp_f32 v77, v77\n"
        "v_mfma_f32_16x16x32_bf16 v[160:163], a[224:227], a[200:203], v[160:163]\n"
        "ds_read_b128 a[220:223], v240 offset:256\n"
        "v_cvt_pk_bf16_f32 v69, v70, v71\n"
        "v_cvt_pk_bf16_f32 v76, v76, v77\n"
        "v_mfma_f32_16x16x32_bf16 v[164:167], a[228:231], a[144:147], 0\n"
        "s_waitcnt lgkmcnt(2)\n"
        "buffer_load_dword v136, s[52:55], 0 offen lds\n"
        "v_mfma_f32_16x16x32_bf16 v[164:167], a[232:235], a[148:151], v[164:167]\n"
        "ds_read_b128 a[224:227], v241 offset:256\n"
        "v_exp_f32 v78, v78\n"
        "v_mfma_f32_16x16x32_bf16 v[164:167], a[236:239], a[152:155], v[164:167]\n"
        "v_exp_f32 v104, v104\n"
        "v_mfma_f32_16x16x32_bf16 v[100:103], v[212:215], v[196:199], v[44:47]\n"
        "v_exp_f32 v79, v79\n"
        "v_mfma_f32_16x16x32_bf16 v[100:103], v[228:231], v[204:207], v[100:103]\n"
        "v_exp_f32 v105, v105\n"
        "v_cvt_pk_bf16_f32 v77, v78, v79\n"
        "v_mfma_f32_16x16x32_bf16 v[176:179], a[236:239], a[176:179], 0\n"
        "v_exp_f32 v106, v106\n"
        "v_cvt_pk_bf16_f32 v70, v104, v105\n"
        "v_mfma_f32_16x16x32_bf16 v[176:179], a[232:235], a[172:175], v[176:179]\n"
        "v_exp_f32 v108, v108\n"
        "v_mfma_f32_16x16x32_bf16 v[176:179], a[228:231], a[168:171], v[176:179]\n"
        "s_waitcnt vmcnt(9)\n"
        "ds_read_b64_tr_b16 a[164:165], v255 offset:15360\n"
        "ds_read_b64_tr_b16 a[166:167], v255 offset:16128\n"
        "v_mfma_f32_16x16x32_bf16 v[180:183], a[228:231], a[192:195], 0\n"
        "ds_read_b64_tr_b16 a[188:189], v255 offset:15392\n"
        "ds_read_b64_tr_b16 a[190:191], v255 offset:16160\n"
        "v_mfma_f32_16x16x32_bf16 v[180:183], a[232:235], a[196:199], v[180:183]\n"
        "ds_read_b64_tr_b16 a[212:213], v255 offset:15424\n"
        "ds_read_b64_tr_b16 a[214:215], v255 offset:16192\n"
        "v_mfma_f32_16x16x32_bf16 v[180:183], a[236:239], a[200:203], v[180:183]\n"
        "ds_read_b128 a[228:231], v241 offset:1664\n"
        "ds_read_b128 a[232:235], v240 offset:1792\n"
        "v_mfma_f32_16x16x32_bf16 v[184:187], a[240:243], a[144:147], 0\n"
        "ds_read_b128 a[236:239], v241 offset:1792\n"
        "v_exp_f32 v107, v107\n"
        "v_mfma_f32_16x16x32_bf16 v[184:187], a[244:247], a[148:151], v[184:187]\n"
        "v_exp_f32 v109, v109\n"
        "v_cvt_pk_bf16_f32 v71, v106, v107\n"
        "v_mfma_f32_16x16x32_bf16 v[184:187], a[248:251], a[152:155], v[184:187]\n"
        "v_exp_f32 v110, v110\n"
        "v_cvt_pk_bf16_f32 v78, v108, v109\n"
        "v_mfma_f32_16x16x32_bf16 v[196:199], a[248:251], a[176:179], 0\n"
        "v_exp_f32 v111, v111\n"
        "v_mfma_f32_16x16x32_bf16 v[196:199], a[244:247], a[172:175], v[196:199]\n"
        "v_cvt_pk_bf16_f32 v79, v110, v111\n"
        "v_exp_f32 v80, v80\n"
        "v_mfma_f32_16x16x32_bf16 v[196:199], a[240:243], a[168:171], v[196:199]\n"
        "v_exp_f32 v81, v81\n"
        "v_mfma_f32_16x16x32_bf16 v[200:203], a[240:243], a[192:195], 0\n"
        "v_exp_f32 v82, v82\n"
        "v_cvt_pk_bf16_f32 v80, v80, v81\n"
        "v_mfma_f32_16x16x32_bf16 v[200:203], a[244:247], a[196:199], v[200:203]\n"
        "ds_read_b128 a[240:243], v241 offset:12416\n"
        "v_exp_f32 v83, v83\n"
        "v_mfma_f32_16x16x32_bf16 v[200:203], a[248:251], a[200:203], v[200:203]\n"
        "ds_read_b128 a[244:247], v240 offset:12544\n"
        "v_cvt_pk_bf16_f32 v81, v82, v83\n"
        "v_mfma_f32_16x16x32_bf16 v[88:91], v[152:155], v[216:219], v[48:51]\n"
        "ds_read_b128 a[248:251], v241 offset:12544\n"
        "v_exp_f32 v96, v96\n"
        "v_mfma_f32_16x16x32_bf16 v[88:91], v[168:171], v[220:223], v[88:91]\n"
        "s_waitcnt lgkmcnt(3)\n"
        "v_exp_f32 v97, v97\n"
        "v_mfma_f32_16x16x32_bf16 v[112:115], a[216:219], a[156:159], v[112:115]\n"
        "v_exp_f32 v98, v98\n"
        "v_cvt_pk_bf16_f32 v82, v96, v97\n"
        "v_mfma_f32_16x16x32_bf16 v[112:115], a[220:223], a[160:163], v[112:115]\n"
        "v_exp_f32 v99, v99\n"
        "v_mfma_f32_16x16x32_bf16 v[112:115], a[224:227], a[164:167], v[112:115]\n"
        "v_cvt_pk_bf16_f32 v83, v98, v99\n"
        "v_exp_f32 v84, v84\n"
        "v_mfma_f32_16x16x32_bf16 v[116:119], a[224:227], a[188:191], v[116:119]\n"
        "v_exp_f32 v85, v85\n"
        "v_mfma_f32_16x16x32_bf16 v[116:119], a[220:223], a[184:187], v[116:119]\n"
        "v_exp_f32 v86, v86\n"
        "v_cvt_pk_bf16_f32 v84, v84, v85\n"
        "v_mfma_f32_16x16x32_bf16 v[116:119], a[216:219], a[180:183], v[116:119]\n"
        "v_exp_f32 v87, v87\n"
        "v_mfma_f32_16x16x32_bf16 v[120:123], a[216:219], a[204:207], v[120:123]\n"
        "v_cvt_pk_bf16_f32 v85, v86, v87\n"
        "v_exp_f32 v100, v100\n"
        "v_mfma_f32_16x16x32_bf16 v[120:123], a[220:223], a[208:211], v[120:123]\n"
        "ds_read_b128 a[216:219], v241 offset:13952\n"
        "v_exp_f32 v101, v101\n"
        "v_mfma_f32_16x16x32_bf16 v[120:123], a[224:227], a[212:215], v[120:123]\n"
        "ds_read_b128 a[220:223], v240 offset:14080\n"
        "v_exp_f32 v102, v102\n"
        "v_mfma_f32_16x16x32_bf16 v[124:127], a[228:231], a[156:159], v[124:127]\n"
        "ds_read_b128 a[224:227], v241 offset:14080\n"
        "v_cvt_pk_bf16_f32 v86, v100, v101\n"
        "v_cvt_pk_bf16_f32 v128, v112, v113\n"
        "v_mfma_f32_16x16x32_bf16 v[124:127], a[232:235], a[160:163], v[124:127]\n"
        "v_exp_f32 v103, v103\n"
        "v_cvt_pk_bf16_f32 v129, v114, v115\n"
        "v_mfma_f32_16x16x32_bf16 v[124:127], a[236:239], a[164:167], v[124:127]\n"
        "v_cvt_pk_bf16_f32 v87, v102, v103\n"
        "v_cvt_pk_bf16_f32 v136, v116, v117\n"
        "v_mfma_f32_16x16x32_bf16 v[104:107], v[172:175], v[216:219], v[52:55]\n"
        "v_cvt_pk_bf16_f32 v137, v118, v119\n"
        "v_cvt_pk_bf16_f32 v144, v120, v121\n"
        "v_mfma_f32_16x16x32_bf16 v[104:107], v[188:191], v[224:227], v[104:107]\n"
        "ds_read_b128 v[222:225], v252 offset:4608\n"
        "v_cvt_pk_bf16_f32 v145, v122, v123\n"
        "v_mfma_f32_16x16x32_bf16 v[156:159], a[236:239], a[188:191], v[156:159]\n"
        "v_exp_f32 v88, v88\n"
        "v_mfma_f32_16x16x32_bf16 v[156:159], a[232:235], a[184:187], v[156:159]\n"
        "v_cvt_pk_bf16_f32 v130, v124, v125\n"
        "v_cvt_pk_bf16_f32 v131, v126, v127\n"
        "v_mfma_f32_16x16x32_bf16 v[156:159], a[228:231], a[180:183], v[156:159]\n"
        "v_exp_f32 v89, v89\n"
        "v_mfma_f32_16x16x32_bf16 v[160:163], a[228:231], a[204:207], v[160:163]\n"
        "v_exp_f32 v90, v90\n"
        "v_mfma_f32_16x16x32_bf16 v[160:163], a[232:235], a[208:211], v[160:163]\n"
        "v_exp_f32 v91, v91\n"
        "v_mfma_f32_16x16x32_bf16 v[160:163], a[236:239], a[212:215], v[160:163]\n"
        "s_waitcnt lgkmcnt(1)\n"
        "v_exp_f32 v104, v104\n"
        "v_mfma_f32_16x16x32_bf16 v[164:167], a[240:243], a[156:159], v[164:167]\n"
        "v_cvt_pk_bf16_f32 v138, v156, v157\n"
        "v_cvt_pk_bf16_f32 v139, v158, v159\n"
        "v_mfma_f32_16x16x32_bf16 v[164:167], a[244:247], a[160:163], v[164:167]\n"
        "v_cvt_pk_bf16_f32 v88, v88, v89\n"
        "v_cvt_pk_bf16_f32 v89, v90, v91\n"
        "v_mfma_f32_16x16x32_bf16 v[164:167], a[248:251], a[164:167], v[164:167]\n"
        "v_exp_f32 v105, v105\n"
        "v_mfma_f32_16x16x32_bf16 v[92:95], v[192:195], v[216:219], v[56:59]\n"
        "s_waitcnt lgkmcnt(0)\n"
        "v_cvt_pk_bf16_f32 v146, v160, v161\n"
        "v_cvt_pk_bf16_f32 v147, v162, v163\n"
        "v_mfma_f32_16x16x32_bf16 v[92:95], v[208:211], v[220:223], v[92:95]\n"
        "v_exp_f32 v106, v106\n"
        "v_mfma_f32_16x16x32_bf16 v[176:179], a[248:251], a[188:191], v[176:179]\n"
        "v_exp_f32 v107, v107\n"
        "v_mfma_f32_16x16x32_bf16 v[176:179], a[244:247], a[184:187], v[176:179]\n"
        "v_cvt_pk_bf16_f32 v132, v164, v165\n"
        "v_cvt_pk_bf16_f32 v133, v166, v167\n"
        "v_mfma_f32_16x16x32_bf16 v[176:179], a[240:243], a[180:183], v[176:179]\n"
        "v_cvt_pk_bf16_f32 v90, v104, v105\n"
        "v_cvt_pk_bf16_f32 v91, v106, v107\n"
        "v_mfma_f32_16x16x32_bf16 v[180:183], a[240:243], a[204:207], v[180:183]\n"
        "v_exp_f32 v92, v92\n"
        "v_mfma_f32_16x16x32_bf16 v[180:183], a[244:247], a[208:211], v[180:183]\n"
        "v_exp_f32 v93, v93\n"
        "v_mfma_f32_16x16x32_bf16 v[180:183], a[248:251], a[212:215], v[180:183]\n"
        "v_mov_b64 v[96:97], 0\n"
        "v_cvt_pk_bf16_f32 v140, v176, v177\n"
        "v_mfma_f32_16x16x32_bf16 v[184:187], a[216:219], a[156:159], v[184:187]\n"
        "v_cvt_pk_bf16_f32 v141, v178, v179\n"
        "v_cvt_pk_bf16_f32 v92, v92, v93\n"
        "v_mfma_f32_16x16x32_bf16 v[184:187], a[220:223], a[160:163], v[184:187]\n"
        "v_exp_f32 v94, v94\n"
        "v_mfma_f32_16x16x32_bf16 v[184:187], a[224:227], a[164:167], v[184:187]\n"
        "v_exp_f32 v95, v95\n"
        "v_mfma_f32_16x16x32_bf16 v[108:111], v[212:215], v[216:219], v[60:63]\n"
        "v_cvt_pk_bf16_f32 v148, v180, v181\n"
        "v_cvt_pk_bf16_f32 v149, v182, v183\n"
        "v_mfma_f32_16x16x32_bf16 v[108:111], v[228:231], v[224:227], v[108:111]\n"
        "v_add_u32 v240, 24576, v240\n"
        "v_add_u32 v241, 24576, v241\n"
        "v_mfma_f32_16x16x32_bf16 v[196:199], a[224:227], a[188:191], v[196:199]\n"
        "v_cvt_pk_bf16_f32 v134, v184, v185\n"
        "v_cvt_pk_bf16_f32 v135, v186, v187\n"
        "v_mfma_f32_16x16x32_bf16 v[196:199], a[220:223], a[184:187], v[196:199]\n"
        "v_mov_b64 v[172:173], 0\n"
        "v_cvt_pk_bf16_f32 v93, v94, v95\n"
        "v_mfma_f32_16x16x32_bf16 v[196:199], a[216:219], a[180:183], v[196:199]\n"
        "v_exp_f32 v108, v108\n"
        "v_mfma_f32_16x16x32_bf16 v[200:203], a[216:219], a[204:207], v[200:203]\n"
        "v_exp_f32 v109, v109\n"
        "v_mfma_f32_16x16x32_bf16 v[200:203], a[220:223], a[208:211], v[200:203]\n"
        "v_exp_f32 v110, v110\n"
        "v_mfma_f32_16x16x32_bf16 v[200:203], a[224:227], a[212:215], v[200:203]\n"
        "v_exp_f32 v111, v111\n"
        "v_smfmac_f32_16x16x64_bf16 v[96:99], a[252:255], v[64:71], v237\n"
        "v_cvt_pk_bf16_f32 v142, v196, v197\n"
        "v_mfma_f32_16x16x32_bf16 v[100:103], v[132:135], v[68:71], 0\n"
        "v_cvt_pk_bf16_f32 v143, v198, v199\n"
        "v_cvt_pk_bf16_f32 v94, v108, v109\n"
        "v_mfma_f32_16x16x32_bf16 v[100:103], v[128:131], v[64:67], v[100:103]\n"
        "v_mov_b64 v[192:193], 0\n"
        "v_cvt_pk_bf16_f32 v150, v200, v201\n"
        "v_mfma_f32_16x16x32_bf16 v[104:107], v[136:139], v[64:67], 0\n"
        "v_cvt_pk_bf16_f32 v151, v202, v203\n"
        "v_add_f32 v97, v96, v97\n"
        "v_mfma_f32_16x16x32_bf16 v[104:107], v[140:143], v[68:71], v[104:107]\n"
        "v_rcp_f32 v96, v97\n"
        "v_cvt_pk_bf16_f32 v95, v110, v111\n"
        "v_mfma_f32_16x16x32_bf16 v[108:111], v[148:151], v[68:71], 0\n"
        "v_mul_f32 v100, v96, v100\n"
        "v_mul_f32 v101, v96, v101\n"
        "v_mfma_f32_16x16x32_bf16 v[108:111], v[144:147], v[64:67], v[108:111]\n"
        "v_mul_f32 v102, v96, v102\n"
        "v_mul_f32 v103, v96, v103\n"
        "v_smfmac_f32_16x16x64_bf16 v[172:175], a[252:255], v[72:79], v237\n"
        "s_waitcnt vmcnt(6)\n"
        "s_barrier\n"
        "ds_read_b128 a[236:239], v240 offset:0\n"
        "v_mfma_f32_16x16x32_bf16 v[176:179], v[132:135], v[76:79], 0\n"
        "ds_read_b128 a[240:243], v241 offset:0\n"
        "ds_read_b128 a[244:247], v240 offset:128\n"
        "v_mfma_f32_16x16x32_bf16 v[176:179], v[128:131], v[72:75], v[176:179]\n"
        "ds_read_b128 a[248:251], v241 offset:128\n"
        "ds_read_b128 a[216:219], v240 offset:256\n"
        "v_mfma_f32_16x16x32_bf16 v[180:183], v[136:139], v[72:75], 0\n"
        "ds_read_b128 a[220:223], v241 offset:256\n"
        "v_cvt_pk_bf16_f32 v100, v100, v101\n"
        "v_mfma_f32_16x16x32_bf16 v[180:183], v[140:143], v[76:79], v[180:183]\n"
        "v_cvt_pk_bf16_f32 v101, v102, v103\n"
        "v_mul_f32 v104, v96, v104\n"
        "v_mfma_f32_16x16x32_bf16 v[184:187], v[148:151], v[76:79], 0\n"
        "v_mul_f32 v105, v96, v105\n"
        "v_mul_f32 v106, v96, v106\n"
        "v_mfma_f32_16x16x32_bf16 v[184:187], v[144:147], v[72:75], v[184:187]\n"
        "v_mul_f32 v107, v96, v107\n"
        "v_cvt_pk_bf16_f32 v102, v104, v105\n"
        "v_smfmac_f32_16x16x64_bf16 v[192:195], a[252:255], v[80:87], v237\n"
        "v_cvt_pk_bf16_f32 v103, v106, v107\n"
        "v_mfma_f32_16x16x32_bf16 v[196:199], v[132:135], v[84:87], 0\n"
        "ds_write_b128 v245, v[100:103] offset:0\n"
        "v_mfma_f32_16x16x32_bf16 v[196:199], v[128:131], v[80:83], v[196:199]\n"
        "buffer_load_dwordx4 v238, s[52:55], s63 offen lds\n"
        "v_mfma_f32_16x16x32_bf16 v[200:203], v[136:139], v[80:83], 0\n"
        "v_mov_b64 v[124:125], 0\n"
        "v_mul_f32 v108, v96, v108\n"
        "v_mfma_f32_16x16x32_bf16 v[200:203], v[140:143], v[84:87], v[200:203]\n"
        "v_mul_f32 v109, v96, v109\n"
        "v_mul_f32 v110, v96, v110\n"
        "v_mul_f32 v111, v96, v111\n"
        "v_mfma_f32_16x16x32_bf16 v[204:207], v[148:151], v[84:87], 0\n"
        "v_cvt_pk_bf16_f32 v108, v108, v109\n"
        "v_cvt_pk_bf16_f32 v109, v110, v111\n"
        "v_mfma_f32_16x16x32_bf16 v[204:207], v[144:147], v[80:83], v[204:207]\n"
        "s_waitcnt lgkmcnt(1)\n"
        "ds_write_b64 v246, v[108:109] offset:0\n"
        "v_mfma_f32_16x16x32_bf16 v[112:115], a[72:75], a[236:239], 0\n"
        "v_max_u32 v236, v96, v97\n"
        "v_add_f32 v173, v172, v173\n"
        "v_mfma_f32_16x16x32_bf16 v[112:115], a[76:79], a[240:243], v[112:115]\n"
        "v_rcp_f32 v172, v173\n"
        "v_mfma_f32_16x16x32_bf16 v[112:115], a[80:83], a[244:247], v[112:115]\n"
        "v_mul_f32 v176, v172, v176\n"
        "v_mul_f32 v177, v172, v177\n"
        "v_mfma_f32_16x16x32_bf16 v[112:115], a[84:87], a[248:251], v[112:115]\n"
        "buffer_load_dwordx4 v239, s[52:55], s63 offen offset:1024 lds\n"
        "v_mfma_f32_16x16x32_bf16 v[112:115], a[88:91], a[216:219], v[112:115]\n"
        "v_mul_f32 v178, v172, v178\n"
        "v_mul_f32 v179, v172, v179\n"
        "v_mfma_f32_16x16x32_bf16 v[112:115], a[92:95], a[220:223], v[112:115]\n"
        "v_cvt_pk_bf16_f32 v176, v176, v177\n"
        "v_cvt_pk_bf16_f32 v177, v178, v179\n"
        "v_smfmac_f32_16x16x64_bf16 v[124:127], a[252:255], v[88:95], v237\n"
        "v_mul_f32 v180, v172, v180\n"
        "v_mfma_f32_16x16x32_bf16 v[80:83], v[132:135], v[92:95], 0\n"
        "v_mul_f32 v181, v172, v181\n"
        "v_mul_f32 v182, v172, v182\n"
        "v_mfma_f32_16x16x32_bf16 v[80:83], v[128:131], v[88:91], v[80:83]\n"
        "v_cvt_pk_bf16_f32 v152, v112, v113\n"
        "v_cvt_pk_bf16_f32 v153, v114, v115\n"
        "v_mfma_f32_16x16x32_bf16 v[84:87], v[136:139], v[88:91], 0\n"
        "v_mul_f32 v183, v172, v183\n"
        "v_cvt_pk_bf16_f32 v178, v180, v181\n"
        "v_mfma_f32_16x16x32_bf16 v[84:87], v[140:143], v[92:95], v[84:87]\n"
        "v_cvt_pk_bf16_f32 v179, v182, v183\n"
        "v_mul_f32 v184, v172, v184\n"
        "v_mfma_f32_16x16x32_bf16 v[96:99], v[148:151], v[92:95], 0\n"
        "buffer_load_dwordx4 v247, s[52:55], s63 offen offset:2048 lds\n"
        "v_mfma_f32_16x16x32_bf16 v[96:99], v[144:147], v[88:91], v[96:99]\n"
        "ds_write_b128 v245, v[176:179] offset:256\n"
        "v_mfma_f32_16x16x32_bf16 v[116:119], a[116:119], a[220:223], 0\n"
        "v_mul_f32 v185, v172, v185\n"
        "v_mul_f32 v186, v172, v186\n"
        "v_mfma_f32_16x16x32_bf16 v[116:119], a[112:115], a[216:219], v[116:119]\n"
        "v_mul_f32 v187, v172, v187\n"
        "v_add_f32 v125, v124, v125\n"
        "v_mfma_f32_16x16x32_bf16 v[116:119], a[108:111], a[248:251], v[116:119]\n"
        "v_rcp_f32 v124, v125\n"
        "v_mfma_f32_16x16x32_bf16 v[116:119], a[104:107], a[244:247], v[116:119]\n"
        "s_add_u32 m0, s65, 15360\n"
        "v_cvt_pk_bf16_f32 v184, v184, v185\n"
        "v_cvt_pk_bf16_f32 v185, v186, v187\n"
        "v_mfma_f32_16x16x32_bf16 v[116:119], a[100:103], a[240:243], v[116:119]\n"
        "ds_write_b64 v246, v[184:185] offset:256\n"
        "v_mfma_f32_16x16x32_bf16 v[116:119], a[96:99], a[236:239], v[116:119]\n"
        "v_mul_f32 v80, v124, v80\n"
        "v_mul_f32 v81, v124, v81\n"
        "v_mfma_f32_16x16x32_bf16 v[120:123], a[120:123], a[236:239], 0\n"
        "buffer_load_dwordx4 v238, s[52:55], s34 offen lds\n"
        "v_mfma_f32_16x16x32_bf16 v[120:123], a[124:127], a[240:243], v[120:123]\n"
        "v_mul_f32 v82, v124, v82\n"
        "v_mul_f32 v83, v124, v83\n"
        "v_mfma_f32_16x16x32_bf16 v[120:123], a[128:131], a[244:247], v[120:123]\n"
        "v_max3_u32 v236, v236, v172, v173\n"
        "v_cvt_pk_bf16_f32 v80, v80, v81\n"
        "v_mfma_f32_16x16x32_bf16 v[120:123], a[132:135], a[248:251], v[120:123]\n"
        "v_cvt_pk_bf16_f32 v81, v82, v83\n"
        "v_mul_f32 v84, v124, v84\n"
        "v_mfma_f32_16x16x32_bf16 v[120:123], a[136:139], a[216:219], v[120:123]\n"
        "v_mul_f32 v85, v124, v85\n"
        "v_mul_f32 v86, v124, v86\n"
        "v_mfma_f32_16x16x32_bf16 v[120:123], a[140:143], a[220:223], v[120:123]\n"
        "v_mul_f32 v87, v124, v87\n"
        "v_cvt_pk_bf16_f32 v82, v84, v85\n"
        "v_mfma_f32_16x16x32_bf16 v[112:115], a[20:23], a[220:223], 0\n"
        "v_cvt_pk_bf16_f32 v83, v86, v87\n"
        "v_mul_f32 v96, v124, v96\n"
        "v_mfma_f32_16x16x32_bf16 v[112:115], a[16:19], a[216:219], v[112:115]\n"
        "ds_write_b128 v245, v[80:83] offset:768\n"
        "v_mfma_f32_16x16x32_bf16 v[112:115], a[12:15], a[248:251], v[112:115]\n"
        "buffer_load_dwordx4 v239, s[52:55], s34 offen offset:1024 lds\n"
        "v_mfma_f32_16x16x32_bf16 v[112:115], a[8:11], a[244:247], v[112:115]\n"
        "v_mul_f32 v97, v124, v97\n"
        "v_mul_f32 v98, v124, v98\n"
        "v_mfma_f32_16x16x32_bf16 v[112:115], a[4:7], a[240:243], v[112:115]\n"
        "v_mul_f32 v99, v124, v99\n"
        "v_cvt_pk_bf16_f32 v154, v116, v117\n"
        "v_mfma_f32_16x16x32_bf16 v[112:115], a[0:3], a[236:239], v[112:115]\n"
        "v_cvt_pk_bf16_f32 v155, v118, v119\n"
        "v_cvt_pk_bf16_f32 v96, v96, v97\n"
        "v_mfma_f32_16x16x32_bf16 v[116:119], a[24:27], a[236:239], 0\n"
        "v_cvt_pk_bf16_f32 v97, v98, v99\n"
        "v_cvt_pk_bf16_f32 v168, v120, v121\n"
        "v_mfma_f32_16x16x32_bf16 v[116:119], a[28:31], a[240:243], v[116:119]\n"
        "ds_write_b64 v246, v[96:97] offset:768\n"
        "v_mfma_f32_16x16x32_bf16 v[116:119], a[32:35], a[244:247], v[116:119]\n"
        "v_cvt_pk_bf16_f32 v169, v122, v123\n"
        "v_add_f32 v193, v192, v193\n"
        "v_mfma_f32_16x16x32_bf16 v[116:119], a[36:39], a[248:251], v[116:119]\n"
        "v_rcp_f32 v192, v193\n"
        "v_mfma_f32_16x16x32_bf16 v[116:119], a[40:43], a[216:219], v[116:119]\n"
        "buffer_load_dwordx4 v247, s[52:55], s34 offen offset:2048 lds\n"
        "v_mfma_f32_16x16x32_bf16 v[116:119], a[44:47], a[220:223], v[116:119]\n"
        "v_mul_f32 v196, v192, v196\n"
        "v_mul_f32 v197, v192, v197\n"
        "v_mfma_f32_16x16x32_bf16 v[120:123], a[68:71], a[220:223], 0\n"
        "v_mul_f32 v198, v192, v198\n"
        "v_mul_f32 v199, v192, v199\n"
        "v_mfma_f32_16x16x32_bf16 v[120:123], a[64:67], a[216:219], v[120:123]\n"
        "v_cvt_pk_bf16_f32 v156, v112, v113\n"
        "v_cvt_pk_bf16_f32 v157, v114, v115\n"
        "v_mfma_f32_16x16x32_bf16 v[120:123], a[60:63], a[248:251], v[120:123]\n"
        "ds_read_b128 a[224:227], v240 offset:1536\n"
        "v_cvt_pk_bf16_f32 v196, v196, v197\n"
        "v_mfma_f32_16x16x32_bf16 v[120:123], a[56:59], a[244:247], v[120:123]\n"
        "s_add_u32 m0, s65, 0\n"
        "ds_read_b128 a[228:231], v241 offset:1536\n"
        "v_cvt_pk_bf16_f32 v197, v198, v199\n"
        "v_mfma_f32_16x16x32_bf16 v[120:123], a[52:55], a[240:243], v[120:123]\n"
        "ds_read_b128 a[232:235], v240 offset:1664\n"
        "v_mul_f32 v200, v192, v200\n"
        "v_mfma_f32_16x16x32_bf16 v[120:123], a[48:51], a[236:239], v[120:123]\n"
        "v_mul_f32 v201, v192, v201\n"
        "v_mul_f32 v202, v192, v202\n"
        "v_mfma_f32_16x16x32_bf16 v[112:115], a[236:239], a[144:147], 0\n"
        "buffer_load_dwordx4 v232, s[52:55], 0 offen lds\n"
        "v_mfma_f32_16x16x32_bf16 v[112:115], a[240:243], a[148:151], v[112:115]\n"
        "v_mul_f32 v203, v192, v203\n"
        "v_cvt_pk_bf16_f32 v198, v200, v201\n"
        "v_mfma_f32_16x16x32_bf16 v[112:115], a[244:247], a[152:155], v[112:115]\n"
        "v_cvt_pk_bf16_f32 v199, v202, v203\n"
        "v_mul_f32 v204, v192, v204\n"
        "v_mfma_f32_16x16x32_bf16 v[112:115], a[248:251], a[156:159], v[112:115]\n"
        "ds_write_b128 v245, v[196:199] offset:512\n"
        "v_mfma_f32_16x16x32_bf16 v[112:115], a[216:219], a[160:163], v[112:115]\n"
        "v_mul_f32 v205, v192, v205\n"
        "v_mul_f32 v206, v192, v206\n"
        "v_mfma_f32_16x16x32_bf16 v[112:115], a[220:223], a[164:167], v[112:115]\n"
        "v_mul_f32 v207, v192, v207\n"
        "v_cvt_pk_bf16_f32 v158, v116, v117\n"
        "v_cvt_pk_bf16_f32 v159, v118, v119\n"
        "v_mfma_f32_16x16x32_bf16 v[116:119], a[220:223], a[188:191], 0\n"
        "v_cvt_pk_bf16_f32 v204, v204, v205\n"
        "v_cvt_pk_bf16_f32 v205, v206, v207\n"
        "v_mfma_f32_16x16x32_bf16 v[116:119], a[216:219], a[184:187], v[116:119]\n"
        "ds_write_b64 v246, v[204:205] offset:512\n"
        "v_mfma_f32_16x16x32_bf16 v[116:119], a[248:251], a[180:183], v[116:119]\n"
        "buffer_load_dwordx4 v233, s[52:55], 0 offen offset:1024 lds\n"
        "v_mfma_f32_16x16x32_bf16 v[116:119], a[244:247], a[176:179], v[116:119]\n"
        "s_waitcnt vmcnt(5)\n"
        "ds_read_b64_tr_b16 v[64:65], v255 offset:12288\n"
        "ds_read_b64_tr_b16 v[66:67], v255 offset:13056\n"
        "v_mfma_f32_16x16x32_bf16 v[116:119], a[240:243], a[172:175], v[116:119]\n"
        "v_max3_u32 v236, v236, v192, v193\n"
        "v_max3_u32 v236, v236, v124, v125\n"
        "v_mfma_f32_16x16x32_bf16 v[116:119], a[236:239], a[168:171], v[116:119]\n"
        "ds_read_b64_tr_b16 v[68:69], v255 offset:12320\n"
        "ds_read_b64_tr_b16 v[70:71], v255 offset:13088\n"
        "v_cvt_pk_bf16_f32 v160, v120, v121\n"
        "v_cvt_pk_bf16_f32 v161, v122, v123\n"
        "v_cvt_pk_bf16_f32 v166, v120, v121\n"
        "v_cvt_pk_bf16_f32 v167, v122, v123\n"
        "v_mfma_f32_16x16x32_bf16 v[120:123], a[236:239], a[192:195], 0\n"
        "ds_read_b128 a[236:239], v241 offset:1664\n"
        "v_cmp_lt_u32 vcc, s24, v236\n"
        "v_mfma_f32_16x16x32_bf16 v[120:123], a[240:243], a[196:199], v[120:123]\n"
        "ds_read_b128 a[240:243], v240 offset:1792\n"
        "s_cmp_lg_u64 vcc, 0\n"
        "s_addc_u32 s25, s25, s25\n"
        "v_mfma_f32_16x16x32_bf16 v[120:123], a[244:247], a[200:203], v[120:123]\n"
        "ds_read_b64_tr_b16 v[72:73], v255 offset:12352\n"
        "ds_read_b64_tr_b16 v[74:75], v255 offset:13120\n"
        "v_mfma_f32_16x16x32_bf16 v[120:123], a[248:251], a[204:207], v[120:123]\n"
        "ds_read_b128 a[244:247], v241 offset:1792\n"
        "v_cvt_pk_bf16_f32 v128, v112, v113\n"
        "v_mfma_f32_16x16x32_bf16 v[120:123], a[216:219], a[208:211], v[120:123]\n"
        "s_waitcnt lgkmcnt(7)\n"
        "ds_write_b128 v235, v[64:67] offset:12288\n"
        "v_mfma_f32_16x16x32_bf16 v[120:123], a[220:223], a[212:215], v[120:123]\n"
        "v_cvt_pk_bf16_f32 v129, v114, v115\n"
        "v_cvt_pk_bf16_f32 v136, v116, v117\n"
        "v_mfma_f32_16x16x32_bf16 v[112:115], a[72:75], a[224:227], 0\n"
        "buffer_load_dwordx4 v234, s[52:55], 0 offen offset:2048 lds\n"
        "v_mfma_f32_16x16x32_bf16 v[112:115], a[76:79], a[228:231], v[112:115]\n"
        "s_waitcnt vmcnt(3)\n"
        "ds_read_b64_tr_b16 v[76:77], v255 offset:15360\n"
        "ds_read_b64_tr_b16 v[78:79], v255 offset:16128\n"
        "v_mfma_f32_16x16x32_bf16 v[112:115], a[80:83], a[232:235], v[112:115]\n"
        "s_waitcnt lgkmcnt(6)\n"
        "v_cvt_pk_bf16_f32 v137, v118, v119\n"
        "v_xor_b32 v251, 0x2000, v251\n"
        "v_mfma_f32_16x16x32_bf16 v[112:115], a[84:87], a[236:239], v[112:115]\n"
        "s_add_u32 m0, s65, 3072\n"
        "v_xor_b32 v252, 0x2000, v252\n"
        "v_cvt_pk_bf16_f32 v144, v120, v121\n"
        "v_mfma_f32_16x16x32_bf16 v[112:115], a[88:91], a[240:243], v[112:115]\n"
        "s_waitcnt lgkmcnt(3)\n"
        "ds_write_b128 v235, v[68:71] offset:12544\n"
        "v_mfma_f32_16x16x32_bf16 v[112:115], a[92:95], a[244:247], v[112:115]\n"
        "s_barrier\n"
        "v_cvt_pk_bf16_f32 v145, v122, v123\n"
        "v_add_u32 v247, s45, v245\n"
        "v_mfma_f32_16x16x32_bf16 v[116:119], a[116:119], a[244:247], 0\n"
        "buffer_load_dwordx4 v232, s[52:55], s61 offen lds\n"
        "v_mfma_f32_16x16x32_bf16 v[116:119], a[112:115], a[240:243], v[116:119]\n"
        "ds_read_b64_tr_b16 v[88:89], v255 offset:15392\n"
        "ds_read_b64_tr_b16 v[90:91], v255 offset:16160\n"
        "v_mfma_f32_16x16x32_bf16 v[116:119], a[108:111], a[236:239], v[116:119]\n"
        "v_cvt_pk_bf16_f32 v172, v112, v113\n"
        "v_cvt_pk_bf16_f32 v173, v114, v115\n"
        "v_mfma_f32_16x16x32_bf16 v[116:119], a[104:107], a[232:235], v[116:119]\n"
        "v_mfma_f32_16x16x32_bf16 v[116:119], a[100:103], a[228:231], v[116:119]\n"
        "v_mfma_f32_16x16x32_bf16 v[116:119], a[96:99], a[224:227], v[116:119]\n"
        "ds_write_b128 v235, v[72:75] offset:12800\n"
        "v_mfma_f32_16x16x32_bf16 v[120:123], a[120:123], a[224:227], 0\n"
        "s_nop 2\n"
        "v_mfma_f32_16x16x32_bf16 v[120:123], a[124:127], a[228:231], v[120:123]\n"
        "ds_read_b128 v[162:165], v251 offset:0\n"
        "s_nop 0\n"
        "v_mfma_f32_16x16x32_bf16 v[120:123], a[128:131], a[232:235], v[120:123]\n"
        "buffer_load_dwordx4 v233, s[52:55], s61 offen offset:1024 lds\n"
        "v_mfma_f32_16x16x32_bf16 v[120:123], a[132:135], a[236:239], v[120:123]\n"
        "ds_read_b64_tr_b16 v[92:93], v255 offset:15424\n"
        "ds_read_b64_tr_b16 v[94:95], v255 offset:16192\n"
        "v_mfma_f32_16x16x32_bf16 v[120:123], a[136:139], a[240:243], v[120:123]\n"
        "v_cvt_pk_bf16_f32 v174, v116, v117\n"
        "v_cvt_pk_bf16_f32 v175, v118, v119\n"
        "v_mfma_f32_16x16x32_bf16 v[120:123], a[140:143], a[244:247], v[120:123]\n"
        "s_nop 2\n"
        "v_mfma_f32_16x16x32_bf16 v[112:115], a[20:23], a[244:247], 0\n"
        "s_nop 2\n"
        "v_mfma_f32_16x16x32_bf16 v[112:115], a[16:19], a[240:243], v[112:115]\n"
        "v_cvt_pk_bf16_f32 v190, v120, v121\n"
        "v_cvt_pk_bf16_f32 v191, v122, v123\n"
        "v_mfma_f32_16x16x32_bf16 v[112:115], a[12:15], a[236:239], v[112:115]\n"
        "v_mfma_f32_16x16x32_bf16 v[112:115], a[8:11], a[232:235], v[112:115]\n"
        "s_waitcnt vmcnt(2)\n"
        "ds_read_b64_tr_b16 v[212:213], v255 offset:0\n"
        "ds_read_b64_tr_b16 v[214:215], v255 offset:768\n"
        "v_mfma_f32_16x16x32_bf16 v[112:115], a[4:7], a[228:231], v[112:115]\n"
        "buffer_load_dwordx4 v234, s[52:55], s61 offen offset:2048 lds\n"
        "v_mfma_f32_16x16x32_bf16 v[112:115], a[0:3], a[224:227], v[112:115]\n"
        "ds_read_b128 v[182:185], v251 offset:512\n"
        "s_nop 0\n"
        "v_mfma_f32_16x16x32_bf16 v[116:119], a[24:27], a[224:227], 0\n"
        "s_nop 2\n"
        "v_mfma_f32_16x16x32_bf16 v[116:119], a[28:31], a[228:231], v[116:119]\n"
        "s_waitcnt lgkmcnt(3)\n"
        "ds_write_b128 v235, v[76:79] offset:15360\n"
        "v_mfma_f32_16x16x32_bf16 v[116:119], a[32:35], a[232:235], v[116:119]\n"
        "v_cvt_pk_bf16_f32 v176, v112, v113\n"
        "v_cvt_pk_bf16_f32 v177, v114, v115\n"
        "v_mfma_f32_16x16x32_bf16 v[116:119], a[36:39], a[236:239], v[116:119]\n"
        "s_add_u32 m0, s65, 6144\n"
        "v_mfma_f32_16x16x32_bf16 v[116:119], a[40:43], a[240:243], v[116:119]\n"
        "ds_read_b64_tr_b16 v[216:217], v255 offset:32\n"
        "ds_read_b64_tr_b16 v[218:219], v255 offset:800\n"
        "v_mfma_f32_16x16x32_bf16 v[116:119], a[44:47], a[244:247], v[116:119]\n"
        "s_nop 2\n"
        "v_mfma_f32_16x16x32_bf16 v[120:123], a[68:71], a[244:247], 0\n"
        "buffer_load_dwordx4 v232, s[52:55], s56 offen lds\n"
        "v_mfma_f32_16x16x32_bf16 v[120:123], a[64:67], a[240:243], v[120:123]\n"
        "s_nop 1\n"
        "v_cvt_pk_bf16_f32 v178, v116, v117\n"
        "v_mfma_f32_16x16x32_bf16 v[120:123], a[60:63], a[236:239], v[120:123]\n"
        "ds_write_b128 v235, v[88:91] offset:15616\n"
        "v_mfma_f32_16x16x32_bf16 v[120:123], a[56:59], a[232:235], v[120:123]\n"
        "v_cvt_pk_bf16_f32 v179, v118, v119\n"
        "v_mfma_f32_16x16x32_bf16 v[120:123], a[52:55], a[228:231], v[120:123]\n"
        "v_mfma_f32_16x16x32_bf16 v[120:123], a[48:51], a[224:227], v[120:123]\n"
        "ds_read_b64_tr_b16 v[220:221], v255 offset:64\n"
        "ds_read_b64_tr_b16 v[222:223], v255 offset:832\n"
        "v_mfma_f32_16x16x32_bf16 v[112:115], a[224:227], a[144:147], 0\n"
        "s_nop 2\n"
        "v_mfma_f32_16x16x32_bf16 v[112:115], a[228:231], a[148:151], v[112:115]\n"
        "s_nop 0\n"
        "v_cvt_pk_bf16_f32 v180, v120, v121\n"
        "v_cvt_pk_bf16_f32 v181, v122, v123\n"
        "v_mfma_f32_16x16x32_bf16 v[112:115], a[232:235], a[152:155], v[112:115]\n"
        "buffer_load_dwordx4 v233, s[52:55], s56 offen offset:1024 lds\n"
        "v_mfma_f32_16x16x32_bf16 v[112:115], a[236:239], a[156:159], v[112:115]\n"
        ".long 0xbec200ff\n"
        "43:\n"
        "s_branch 60b\n"
        "ds_write_b128 v235, v[92:95] offset:15872\n"
        "v_mfma_f32_16x16x32_bf16 v[112:115], a[240:243], a[160:163], v[112:115]\n"
        "v_cvt_pk_bf16_f32 v186, v120, v121\n"
        "v_cvt_pk_bf16_f32 v187, v122, v123\n"
        "v_mfma_f32_16x16x32_bf16 v[112:115], a[244:247], a[164:167], v[112:115]\n"
        "ds_read_b128 a[248:251], v240 offset:12288\n"
        "s_nop 0\n"
        "v_mfma_f32_16x16x32_bf16 v[96:99], v[152:155], v[156:159], v[0:3]\n"
        "s_waitcnt vmcnt(2)\n"
        "ds_read_b64_tr_b16 v[224:225], v255 offset:3072\n"
        "ds_read_b64_tr_b16 v[226:227], v255 offset:3840\n"
        "v_mfma_f32_16x16x32_bf16 v[96:99], v[168:171], v[160:163], v[96:99]\n"
        "ds_read_b128 a[216:219], v241 offset:12288\n"
        "s_sub_u32 s31, s30, s28\n"
        "v_cvt_pk_bf16_f32 v130, v112, v113\n"
        "v_mfma_f32_16x16x32_bf16 v[100:103], v[172:175], v[156:159], v[4:7]\n"
        "ds_read_b128 a[220:223], v240 offset:12416\n"
        "s_sub_u32 s45, s28, s29\n"
        "v_cvt_pk_bf16_f32 v131, v114, v115\n"
        "v_mfma_f32_16x16x32_bf16 v[100:103], v[188:191], v[164:167], v[100:103]\n"
        "s_add_u32 s28, s28, s31\n"
        "ds_read_b128 v[162:165], v252 offset:0\n"
        "v_mfma_f32_16x16x32_bf16 v[116:119], a[244:247], a[188:191], 0\n"
        "buffer_load_dwordx4 v234, s[52:55], s56 offen offset:2048 lds\n"
        "v_mfma_f32_16x16x32_bf16 v[116:119], a[240:243], a[184:187], v[116:119]\n"
        "s_waitcnt lgkmcnt(7)\n"
        "ds_write_b128 v235, v[212:215] offset:0\n"
        "v_mfma_f32_16x16x32_bf16 v[116:119], a[236:239], a[180:183], v[116:119]\n"
        "v_exp_f32 v96, v96\n"
        "v_mfma_f32_16x16x32_bf16 v[116:119], a[232:235], a[176:179], v[116:119]\n"
        "ds_read_b64_tr_b16 v[88:89], v255 offset:3104\n"
        "ds_read_b64_tr_b16 v[90:91], v255 offset:3872\n"
        "v_mfma_f32_16x16x32_bf16 v[116:119], a[228:231], a[172:175], v[116:119]\n"
        "v_exp_f32 v97, v97\n"
        "v_mfma_f32_16x16x32_bf16 v[116:119], a[224:227], a[168:171], v[116:119]\n"
        "s_add_u32 m0, s65, 9216\n"
        "v_exp_f32 v98, v98\n"
        "v_mfma_f32_16x16x32_bf16 v[104:107], v[152:155], v[176:179], v[16:19]\n"
        "v_exp_f32 v100, v100\n"
        "v_mfma_f32_16x16x32_bf16 v[104:107], v[168:171], v[180:183], v[104:107]\n"
        "ds_write_b128 v235, v[216:219] offset:256\n"
        "v_mfma_f32_16x16x32_bf16 v[108:111], v[172:175], v[176:179], v[20:23]\n"
        "buffer_load_dwordx4 v232, s[52:55], s62 offen lds\n"
        "v_mfma_f32_16x16x32_bf16 v[108:111], v[188:191], v[184:187], v[108:111]\n"
        "ds_read_b128 v[182:185], v252 offset:512\n"
        "v_cvt_pk_bf16_f32 v64, v96, v97\n"
        "v_mfma_f32_16x16x32_bf16 v[120:123], a[224:227], a[192:195], 0\n"
        "ds_read_b128 a[224:227], v241 offset:12416\n"
        "ds_read_b64_tr_b16 v[92:93], v255 offset:3136\n"
        "v_mfma_f32_16x16x32_bf16 v[120:123], a[228:231], a[196:199], v[120:123]\n"
        "ds_read_b64_tr_b16 v[94:95], v255 offset:3904\n"
        "ds_read_b128 a[228:231], v240 offset:12544\n"
        "v_mfma_f32_16x16x32_bf16 v[120:123], a[232:235], a[200:203], v[120:123]\n"
        "ds_read_b128 a[232:235], v241 offset:12544\n"
        "v_cvt_pk_bf16_f32 v138, v116, v117\n"
        "v_mfma_f32_16x16x32_bf16 v[120:123], a[236:239], a[204:207], v[120:123]\n"
        "ds_read_b128 v[202:205], v251 offset:4096\n"
        "v_cvt_pk_bf16_f32 v139, v118, v119\n"
        "v_mfma_f32_16x16x32_bf16 v[120:123], a[240:243], a[208:211], v[120:123]\n"
        "ds_write_b128 v235, v[220:223] offset:512\n"
        "v_mfma_f32_16x16x32_bf16 v[120:123], a[244:247], a[212:215], v[120:123]\n"
        "s_waitcnt lgkmcnt(9)\n"
        "v_exp_f32 v99, v99\n"
        "v_mfma_f32_16x16x32_bf16 v[112:115], a[0:3], a[248:251], 0\n"
        "buffer_load_dwordx4 v233, s[52:55], s62 offen offset:1024 lds\n"
        "v_mfma_f32_16x16x32_bf16 v[112:115], a[4:7], a[216:219], v[112:115]\n"
        "s_waitcnt vmcnt(2)\n"
        "ds_read_b64_tr_b16 v[212:213], v255 offset:6144\n"
        "ds_read_b64_tr_b16 v[214:215], v255 offset:6912\n"
        "v_mfma_f32_16x16x32_bf16 v[112:115], a[8:11], a[220:223], v[112:115]\n"
        "s_waitcnt lgkmcnt(5)\n"
        "v_exp_f32 v101, v101\n"
        "v_mfma_f32_16x16x32_bf16 v[112:115], a[12:15], a[224:227], v[112:115]\n"
        "v_cvt_pk_bf16_f32 v65, v98, v99\n"
        "v_cvt_pk_bf16_f32 v66, v100, v101\n"
        "v_mfma_f32_16x16x32_bf16 v[112:115], a[16:19], a[228:231], v[112:115]\n"
        "s_waitcnt lgkmcnt(3)\n"
        "v_exp_f32 v102, v102\n"
        "v_mfma_f32_16x16x32_bf16 v[112:115], a[20:23], a[232:235], v[112:115]\n"
        "ds_write_b128 v235, v[224:227] offset:3072\n"
        "v_mfma_f32_16x16x32_bf16 v[116:119], a[44:47], a[232:235], 0\n"
        "v_exp_f32 v103, v103\n"
        "v_mfma_f32_16x16x32_bf16 v[116:119], a[40:43], a[228:231], v[116:119]\n"
        "v_cvt_pk_bf16_f32 v67, v102, v103\n"
        "v_cvt_pk_bf16_f32 v146, v120, v121\n"
        "v_mfma_f32_16x16x32_bf16 v[116:119], a[36:39], a[224:227], v[116:119]\n"
        "buffer_load_dwordx4 v234, s[52:55], s62 offen offset:2048 lds\n"
        "s_add_u32 s41, s30, s40\n"
        "s_add_u32 s35, s47, 786432\n"
        "s_sub_u32 s46, s35, 100663296\n"
        "s_cmp_lt_u32 s46, 786432\n"
        "s_mov_b32 s30, s29\n"
        "v_mfma_f32_16x16x32_bf16 v[116:119], a[32:35], a[220:223], v[116:119]\n"
        "s_cselect_b32 s35, s46, s35\n"
        "s_add_u32 s29, s29, s45\n"
        "ds_read_b64_tr_b16 v[216:217], v255 offset:6176\n"
        "v_mfma_f32_16x16x32_bf16 v[116:119], a[28:31], a[216:219], v[116:119]\n"
        "ds_read_b64_tr_b16 v[218:219], v255 offset:6944\n"
        "v_cvt_pk_bf16_f32 v147, v122, v123\n"
        "v_mfma_f32_16x16x32_bf16 v[116:119], a[24:27], a[248:251], v[116:119]\n"
        "v_exp_f32 v104, v104\n"
        "v_mfma_f32_16x16x32_bf16 v[120:123], a[48:51], a[248:251], 0\n"
        "ds_write_b128 v235, v[88:91] offset:3328\n"
        "v_mfma_f32_16x16x32_bf16 v[120:123], a[52:55], a[216:219], v[120:123]\n"
        "v_exp_f32 v105, v105\n"
        "v_mfma_f32_16x16x32_bf16 v[120:123], a[56:59], a[220:223], v[120:123]\n"
        "s_mov_b32 m0, s48\n"
        "v_exp_f32 v106, v106\n"
        "v_mfma_f32_16x16x32_bf16 v[120:123], a[60:63], a[224:227], v[120:123]\n"
        "ds_read_b64_tr_b16 v[220:221], v255 offset:6208\n"
        "ds_read_b64_tr_b16 v[222:223], v255 offset:6976\n"
        "v_mfma_f32_16x16x32_bf16 v[120:123], a[64:67], a[228:231], v[120:123]\n"
        "buffer_load_dwordx4 v249, s[12:15], s36 offen offset:0 lds\n"
        "v_mfma_f32_16x16x32_bf16 v[120:123], a[68:71], a[232:235], v[120:123]\n"
        "v_cvt_pk_bf16_f32 v72, v104, v105\n"
        "v_cvt_pk_bf16_f32 v196, v112, v113\n"
        "v_cvt_pk_bf16_f32 v197, v114, v115\n"
        "v_mfma_f32_16x16x32_bf16 v[112:115], a[92:95], a[232:235], 0\n"
        "v_exp_f32 v108, v108\n"
        "v_mfma_f32_16x16x32_bf16 v[112:115], a[88:91], a[228:231], v[112:115]\n"
        "ds_write_b128 v235, v[92:95] offset:3584\n"
        "v_mfma_f32_16x16x32_bf16 v[112:115], a[84:87], a[224:227], v[112:115]\n"
        "v_exp_f32 v107, v107\n"
        "v_mfma_f32_16x16x32_bf16 v[112:115], a[80:83], a[220:223], v[112:115]\n"
        "v_exp_f32 v109, v109\n"
        "v_mfma_f32_16x16x32_bf16 v[112:115], a[76:79], a[216:219], v[112:115]\n"
        "s_waitcnt vmcnt(1)\n"
        "ds_read_b64_tr_b16 v[224:225], v255 offset:9216\n"
        "ds_read_b64_tr_b16 v[226:227], v255 offset:9984\n"
        "v_mfma_f32_16x16x32_bf16 v[112:115], a[72:75], a[248:251], v[112:115]\n"
        "buffer_load_dwordx4 v253, s[12:15], s36 offen offset:1024 lds\n"
        "v_cvt_pk_bf16_f32 v198, v116, v117\n"
        "v_cvt_pk_bf16_f32 v199, v118, v119\n"
        "s_nop 1\n"
        "v_mfma_f32_16x16x32_bf16 v[96:99], v[152:155], v[196:199], v[32:35]\n"
        "v_cvt_pk_bf16_f32 v73, v106, v107\n"
        "v_cvt_pk_bf16_f32 v74, v108, v109\n"
        "v_cvt_pk_bf16_f32 v200, v120, v121\n"
        "v_cvt_pk_bf16_f32 v201, v122, v123\n"
        "s_nop 1\n"
        "v_mfma_f32_16x16x32_bf16 v[96:99], v[168:171], v[200:203], v[96:99]\n"
        "v_exp_f32 v110, v110\n"
        "v_mfma_f32_16x16x32_bf16 v[100:103], v[172:175], v[196:199], v[36:39]\n"
        "s_waitcnt lgkmcnt(3)\n"
        "ds_write_b128 v235, v[212:215] offset:6144\n"
        "v_cvt_pk_bf16_f32 v206, v120, v121\n"
        "v_cvt_pk_bf16_f32 v207, v122, v123\n"
        "s_nop 1\n"
        "v_mfma_f32_16x16x32_bf16 v[100:103], v[188:191], v[204:207], v[100:103]\n"
        "s_cmp_eq_u32 s37, 100663296\n"
        "v_exp_f32 v111, v111\n"
        "v_mfma_f32_16x16x32_bf16 v[116:119], a[96:99], a[248:251], 0\n"
        "s_cselect_b32 s46, 0, s37\n"
        "ds_read_b128 v[202:205], v252 offset:4096\n"
        "v_cvt_pk_bf16_f32 v75, v110, v111\n"
        "v_mfma_f32_16x16x32_bf16 v[116:119], a[100:103], a[216:219], v[116:119]\n"
        "ds_read_b64_tr_b16 v[88:89], v255 offset:9248\n"
        "ds_read_b64_tr_b16 v[90:91], v255 offset:10016\n"
        "s_add_u32 m0, s41, 0\n"
        "v_mfma_f32_16x16x32_bf16 v[116:119], a[104:107], a[220:223], v[116:119]\n"
        "buffer_load_dwordx4 v242, s[8:11], s47 offen lds\n"
        "v_mfma_f32_16x16x32_bf16 v[116:119], a[108:111], a[224:227], v[116:119]\n"
        "s_add_u32 s38, s46, s43\n"
        "v_cvt_pk_bf16_f32 v192, v112, v113\n"
        "v_cvt_pk_bf16_f32 v193, v114, v115\n"
        "v_mfma_f32_16x16x32_bf16 v[116:119], a[112:115], a[228:231], v[116:119]\n"
        "s_add_u32 s39, s46, s44\n"
        "v_exp_f32 v96, v96\n"
        "v_mfma_f32_16x16x32_bf16 v[116:119], a[116:119], a[232:235], v[116:119]\n"
        "ds_write_b128 v235, v[216:219] offset:6400\n"
        "s_add_u32 s37, s37, 6291456\n"
        "v_mfma_f32_16x16x32_bf16 v[120:123], a[140:143], a[232:235], 0\n"
        "v_exp_f32 v97, v97\n"
        "v_mfma_f32_16x16x32_bf16 v[120:123], a[136:139], a[228:231], v[120:123]\n"
        "s_add_u32 m0, s41, 1024\n"
        "v_exp_f32 v98, v98\n"
        "v_mfma_f32_16x16x32_bf16 v[120:123], a[132:135], a[224:227], v[120:123]\n"
        "ds_read_b64_tr_b16 v[92:93], v255 offset:9280\n"
        "ds_read_b64_tr_b16 v[94:95], v255 offset:10048\n"
        "v_mfma_f32_16x16x32_bf16 v[120:123], a[128:131], a[220:223], v[120:123]\n"
        "buffer_load_dwordx4 v243, s[8:11], s47 offen lds\n"
        "v_mfma_f32_16x16x32_bf16 v[120:123], a[124:127], a[216:219], v[120:123]\n"
        "v_cvt_pk_bf16_f32 v80, v96, v97\n"
        "v_cvt_pk_bf16_f32 v194, v116, v117\n"
        "v_mfma_f32_16x16x32_bf16 v[120:123], a[120:123], a[248:251], v[120:123]\n"
        "v_exp_f32 v100, v100\n"
        "v_mfma_f32_16x16x32_bf16 v[112:115], a[248:251], a[144:147], 0\n"
        "ds_write_b128 v235, v[220:223] offset:6656\n"
        "v_mfma_f32_16x16x32_bf16 v[112:115], a[216:219], a[148:151], v[112:115]\n"
        "v_cvt_pk_bf16_f32 v195, v118, v119\n"
        "v_add_u32 v245, s45, v245\n"
        "v_mfma_f32_16x16x32_bf16 v[112:115], a[220:223], a[152:155], v[112:115]\n"
        "s_add_u32 m0, s41, 2048\n"
        "ds_read_b128 a[236:239], v240 offset:13824\n"
        "v_cvt_pk_bf16_f32 v208, v120, v121\n"
        "v_mfma_f32_16x16x32_bf16 v[112:115], a[224:227], a[156:159], v[112:115]\n"
        "ds_read_b128 a[240:243], v241 offset:13824\n"
        "v_cvt_pk_bf16_f32 v209, v122, v123\n"
        "v_mfma_f32_16x16x32_bf16 v[112:115], a[228:231], a[160:163], v[112:115]\n"
        "buffer_load_dwordx4 v244, s[8:11], s47 offen lds\n"
        "v_mfma_f32_16x16x32_bf16 v[112:115], a[232:235], a[164:167], v[112:115]\n"
        "ds_read_b128 a[244:247], v240 offset:13952\n"
        "v_add_u32 v246, s45, v246\n"
        "v_mfma_f32_16x16x32_bf16 v[104:107], v[192:195], v[156:159], v[8:11]\n"
        "v_exp_f32 v99, v99\n"
        "v_mfma_f32_16x16x32_bf16 v[104:107], v[208:211], v[160:163], v[104:107]\n"
        "s_waitcnt lgkmcnt(4)\n"
        "ds_write_b128 v235, v[224:227] offset:9216\n"
        "v_mfma_f32_16x16x32_bf16 v[116:119], a[232:235], a[188:191], 0\n"
        "v_exp_f32 v101, v101\n"
        "v_mfma_f32_16x16x32_bf16 v[116:119], a[228:231], a[184:187], v[116:119]\n"
        "s_add_u32 m0, s41, 12288\n"
        "v_cvt_pk_bf16_f32 v81, v98, v99\n"
        "v_cvt_pk_bf16_f32 v82, v100, v101\n"
        "v_mfma_f32_16x16x32_bf16 v[116:119], a[224:227], a[180:183], v[116:119]\n"
        "v_exp_f32 v102, v102\n"
        "v_mfma_f32_16x16x32_bf16 v[116:119], a[220:223], a[176:179], v[116:119]\n"
        "buffer_load_dwordx4 v242, s[8:11], s35 offen lds\n"
        "v_mfma_f32_16x16x32_bf16 v[116:119], a[216:219], a[172:175], v[116:119]\n"
        "v_exp_f32 v103, v103\n"
        "v_mfma_f32_16x16x32_bf16 v[116:119], a[248:251], a[168:171], v[116:119]\n"
        "v_cvt_pk_bf16_f32 v83, v102, v103\n"
        "v_cvt_pk_bf16_f32 v132, v112, v113\n"
        "v_mfma_f32_16x16x32_bf16 v[108:111], v[192:195], v[176:179], v[24:27]\n"
        "ds_write_b128 v235, v[88:91] offset:9472\n"
        "v_mfma_f32_16x16x32_bf16 v[108:111], v[208:211], v[180:183], v[108:111]\n"
        "v_cvt_pk_bf16_f32 v133, v114, v115\n"
        "s_nop 0\n"
        "v_mfma_f32_16x16x32_bf16 v[120:123], a[248:251], a[192:195], 0\n"
        "ds_read_b128 a[248:251], v241 offset:13952\n"
        "s_add_u32 m0, s41, 13312\n"
        "v_cvt_pk_bf16_f32 v140, v116, v117\n"
        "v_mfma_f32_16x16x32_bf16 v[120:123], a[216:219], a[196:199], v[120:123]\n"
        "ds_read_b128 a[216:219], v240 offset:14080\n"
        "v_cvt_pk_bf16_f32 v141, v118, v119\n"
        "v_mfma_f32_16x16x32_bf16 v[120:123], a[220:223], a[200:203], v[120:123]\n"
        "buffer_load_dwordx4 v243, s[8:11], s35 offen lds\n"
        "v_mfma_f32_16x16x32_bf16 v[120:123], a[224:227], a[204:207], v[120:123]\n"
        "ds_read_b128 a[220:223], v241 offset:14080\n"
        "v_mfma_f32_16x16x32_bf16 v[120:123], a[228:231], a[208:211], v[120:123]\n"
        "v_exp_f32 v104, v104\n"
        "v_mfma_f32_16x16x32_bf16 v[120:123], a[232:235], a[212:215], v[120:123]\n"
        "ds_write_b128 v235, v[92:95] offset:9728\n"
        "v_mfma_f32_16x16x32_bf16 v[96:99], v[192:195], v[196:199], v[40:43]\n"
        "v_exp_f32 v105, v105\n"
        "v_mfma_f32_16x16x32_bf16 v[96:99], v[208:211], v[200:203], v[96:99]\n"
        "s_waitcnt lgkmcnt(5)\n"
        "s_add_u32 m0, s41, 14336\n"
        "v_exp_f32 v106, v106\n"
        "v_mfma_f32_16x16x32_bf16 v[112:115], a[72:75], a[236:239], 0\n"
        "v_cvt_pk_bf16_f32 v68, v104, v105\n"
        "v_cvt_pk_bf16_f32 v148, v120, v121\n"
        "v_mfma_f32_16x16x32_bf16 v[112:115], a[76:79], a[240:243], v[112:115]\n"
        "buffer_load_dwordx4 v244, s[8:11], s35 offen lds\n"
        "v_mfma_f32_16x16x32_bf16 v[112:115], a[80:83], a[244:247], v[112:115]\n"
        "s_waitcnt lgkmcnt(1)\n"
        "v_exp_f32 v107, v107\n"
        "v_mfma_f32_16x16x32_bf16 v[112:115], a[84:87], a[248:251], v[112:115]\n"
        "v_cvt_pk_bf16_f32 v69, v106, v107\n"
        "v_cvt_pk_bf16_f32 v149, v122, v123\n"
        "v_mfma_f32_16x16x32_bf16 v[112:115], a[88:91], a[216:219], v[112:115]\n"
        "v_exp_f32 v108, v108\n"
        "v_mfma_f32_16x16x32_bf16 v[112:115], a[92:95], a[220:223], v[112:115]\n"
        "v_exp_f32 v109, v109\n"
        "v_mfma_f32_16x16x32_bf16 v[116:119], a[116:119], a[220:223], 0\n"
        "v_exp_f32 v110, v110\n"
        "v_mfma_f32_16x16x32_bf16 v[116:119], a[112:115], a[216:219], v[116:119]\n"
        "v_cvt_pk_bf16_f32 v76, v108, v109\n"
        "v_add_u32 v240, s31, v240\n"
        "v_mfma_f32_16x16x32_bf16 v[116:119], a[108:111], a[248:251], v[116:119]\n"
        "v_exp_f32 v111, v111\n"
        "v_mfma_f32_16x16x32_bf16 v[116:119], a[104:107], a[244:247], v[116:119]\n"
        "v_cvt_pk_bf16_f32 v212, v112, v113\n"
        "v_cvt_pk_bf16_f32 v213, v114, v115\n"
        "v_mfma_f32_16x16x32_bf16 v[116:119], a[100:103], a[240:243], v[116:119]\n"
        "v_cvt_pk_bf16_f32 v77, v110, v111\n"
        "v_add_u32 v241, s31, v241\n"
        "v_mfma_f32_16x16x32_bf16 v[116:119], a[96:99], a[236:239], v[116:119]\n"
        "v_exp_f32 v96, v96\n"
        "v_mfma_f32_16x16x32_bf16 v[120:123], a[120:123], a[236:239], 0\n"
        "v_exp_f32 v97, v97\n"
        "v_mfma_f32_16x16x32_bf16 v[120:123], a[124:127], a[240:243], v[120:123]\n"
        "s_add_u32 s47, s47, 6291456\n"
        "v_exp_f32 v98, v98\n"
        "v_mfma_f32_16x16x32_bf16 v[120:123], a[128:131], a[244:247], v[120:123]\n"
        "s_add_u32 s36, s36, 2097152\n"
        "v_cvt_pk_bf16_f32 v214, v116, v117\n"
        "v_cvt_pk_bf16_f32 v215, v118, v119\n"
        "v_mfma_f32_16x16x32_bf16 v[120:123], a[132:135], a[248:251], v[120:123]\n"
        "s_xor_b32 s48, s48, 8192\n"
        "v_cvt_pk_bf16_f32 v84, v96, v97\n"
        "v_mfma_f32_16x16x32_bf16 v[120:123], a[136:139], a[216:219], v[120:123]\n"
        "v_exp_f32 v99, v99\n"
        "v_mfma_f32_16x16x32_bf16 v[120:123], a[140:143], a[220:223], v[120:123]\n"
        "v_cvt_pk_bf16_f32 v85, v98, v99\n"
        "s_nop 0\n"
        "v_mfma_f32_16x16x32_bf16 v[112:115], a[220:223], a[164:167], 0\n"
        "s_nop 2\n"
        "v_mfma_f32_16x16x32_bf16 v[112:115], a[216:219], a[160:163], v[112:115]\n"
        "s_nop 0\n"
        "v_cvt_pk_bf16_f32 v230, v120, v121\n"
        "v_cvt_pk_bf16_f32 v231, v122, v123\n"
        "v_mfma_f32_16x16x32_bf16 v[112:115], a[248:251], a[156:159], v[112:115]\n"
        "s_waitcnt lgkmcnt(0)\n"
        "s_barrier\n"
        "v_mfma_f32_16x16x32_bf16 v[112:115], a[244:247], a[152:155], v[112:115]\n"
        "v_mfma_f32_16x16x32_bf16 v[112:115], a[240:243], a[148:151], v[112:115]\n"
        "v_mfma_f32_16x16x32_bf16 v[112:115], a[236:239], a[144:147], v[112:115]\n"
        "s_nop 2\n"
        "v_mfma_f32_16x16x32_bf16 v[100:103], v[212:215], v[156:159], v[12:15]\n"
        "s_nop 2\n"
        "v_mfma_f32_16x16x32_bf16 v[100:103], v[228:231], v[164:167], v[100:103]\n"
        "v_cvt_pk_bf16_f32 v134, v112, v113\n"
        "v_cvt_pk_bf16_f32 v135, v114, v115\n"
        "v_mfma_f32_16x16x32_bf16 v[116:119], a[236:239], a[168:171], 0\n"
        "v_mfma_f32_16x16x32_bf16 v[116:119], a[240:243], a[172:175], v[116:119]\n"
        "v_mfma_f32_16x16x32_bf16 v[116:119], a[244:247], a[176:179], v[116:119]\n"
        "v_mfma_f32_16x16x32_bf16 v[116:119], a[248:251], a[180:183], v[116:119]\n"
        "v_mfma_f32_16x16x32_bf16 v[116:119], a[216:219], a[184:187], v[116:119]\n"
        "s_nop 0\n"
        "v_exp_f32 v100, v100\n"
        "v_mfma_f32_16x16x32_bf16 v[116:119], a[220:223], a[188:191], v[116:119]\n"
        "v_exp_f32 v101, v101\n"
        "v_mfma_f32_16x16x32_bf16 v[104:107], v[212:215], v[176:179], v[28:31]\n"
        "v_exp_f32 v102, v102\n"
        "v_mfma_f32_16x16x32_bf16 v[104:107], v[228:231], v[184:187], v[104:107]\n"
        "v_exp_f32 v103, v103\n"
        "v_mfma_f32_16x16x32_bf16 v[120:123], a[220:223], a[212:215], 0\n"
        "v_cvt_pk_bf16_f32 v70, v100, v101\n"
        "v_cvt_pk_bf16_f32 v71, v102, v103\n"
        "v_mfma_f32_16x16x32_bf16 v[120:123], a[216:219], a[208:211], v[120:123]\n"
        "v_cvt_pk_bf16_f32 v142, v116, v117\n"
        "v_cvt_pk_bf16_f32 v143, v118, v119\n"
        "v_mfma_f32_16x16x32_bf16 v[120:123], a[248:251], a[204:207], v[120:123]\n"
        "v_mov_b64 v[96:97], 0\n"
        "v_mfma_f32_16x16x32_bf16 v[120:123], a[244:247], a[200:203], v[120:123]\n"
        "v_exp_f32 v104, v104\n"
        "v_mfma_f32_16x16x32_bf16 v[120:123], a[240:243], a[196:199], v[120:123]\n"
        "v_exp_f32 v105, v105\n"
        "v_mfma_f32_16x16x32_bf16 v[120:123], a[236:239], a[192:195], v[120:123]\n"
        "v_exp_f32 v106, v106\n"
        "v_mfma_f32_16x16x32_bf16 v[108:111], v[212:215], v[196:199], v[44:47]\n"
        "v_exp_f32 v107, v107\n"
        "v_mfma_f32_16x16x32_bf16 v[108:111], v[228:231], v[204:207], v[108:111]\n"
        "v_cvt_pk_bf16_f32 v78, v104, v105\n"
        "v_cvt_pk_bf16_f32 v79, v106, v107\n"
        "v_mfma_f32_16x16x32_bf16 v[112:115], a[0:3], a[236:239], 0\n"
        "s_nop 0\n"
        "v_cvt_pk_bf16_f32 v150, v120, v121\n"
        "v_cvt_pk_bf16_f32 v151, v122, v123\n"
        "v_mfma_f32_16x16x32_bf16 v[112:115], a[4:7], a[240:243], v[112:115]\n"
        "v_mfma_f32_16x16x32_bf16 v[112:115], a[8:11], a[244:247], v[112:115]\n"
        "v_exp_f32 v108, v108\n"
        "v_mfma_f32_16x16x32_bf16 v[112:115], a[12:15], a[248:251], v[112:115]\n"
        "v_exp_f32 v109, v109\n"
        "v_mfma_f32_16x16x32_bf16 v[112:115], a[16:19], a[216:219], v[112:115]\n"
        "v_exp_f32 v110, v110\n"
        "v_mfma_f32_16x16x32_bf16 v[112:115], a[20:23], a[220:223], v[112:115]\n"
        "v_exp_f32 v111, v111\n"
        "v_smfmac_f32_16x16x64_bf16 v[96:99], a[252:255], v[64:71], v237\n"
        "v_cvt_pk_bf16_f32 v86, v108, v109\n"
        "v_mfma_f32_16x16x32_bf16 v[100:103], v[132:135], v[68:71], 0\n"
        "v_mov_b64 v[124:125], 0\n"
        "v_cvt_pk_bf16_f32 v87, v110, v111\n"
        "v_mfma_f32_16x16x32_bf16 v[100:103], v[128:131], v[64:67], v[100:103]\n"
        "s_nop 0\n"
        "v_cvt_pk_bf16_f32 v216, v112, v113\n"
        "v_cvt_pk_bf16_f32 v217, v114, v115\n"
        "v_mfma_f32_16x16x32_bf16 v[104:107], v[136:139], v[64:67], 0\n"
        "ds_read_b128 v[222:225], v251 offset:4608\n"
        "v_add_f32 v97, v96, v97\n"
        "v_mfma_f32_16x16x32_bf16 v[104:107], v[140:143], v[68:71], v[104:107]\n"
        "v_rcp_f32 v96, v97\n"
        "v_mfma_f32_16x16x32_bf16 v[108:111], v[148:151], v[68:71], 0\n"
        "v_mul_f32 v100, v96, v100\n"
        "v_mul_f32 v101, v96, v101\n"
        "v_mfma_f32_16x16x32_bf16 v[108:111], v[144:147], v[64:67], v[108:111]\n"
        "v_mul_f32 v102, v96, v102\n"
        "v_mul_f32 v103, v96, v103\n"
        "v_mfma_f32_16x16x32_bf16 v[116:119], a[44:47], a[220:223], 0\n"
        "v_cvt_pk_bf16_f32 v100, v100, v101\n"
        "v_cvt_pk_bf16_f32 v101, v102, v103\n"
        "v_mfma_f32_16x16x32_bf16 v[116:119], a[40:43], a[216:219], v[116:119]\n"
        "v_mul_f32 v104, v96, v104\n"
        "v_mul_f32 v105, v96, v105\n"
        "v_mfma_f32_16x16x32_bf16 v[116:119], a[36:39], a[248:251], v[116:119]\n"
        "v_mul_f32 v106, v96, v106\n"
        "v_mul_f32 v107, v96, v107\n"
        "v_mfma_f32_16x16x32_bf16 v[116:119], a[32:35], a[244:247], v[116:119]\n"
        "v_cvt_pk_bf16_f32 v102, v104, v105\n"
        "v_cvt_pk_bf16_f32 v103, v106, v107\n"
        "v_mfma_f32_16x16x32_bf16 v[116:119], a[28:31], a[240:243], v[116:119]\n"
        "ds_write_b128 v245, v[100:103] offset:0\n"
        "v_mfma_f32_16x16x32_bf16 v[116:119], a[24:27], a[236:239], v[116:119]\n"
        "v_mul_f32 v108, v96, v108\n"
        "v_mul_f32 v109, v96, v109\n"
        "v_smfmac_f32_16x16x64_bf16 v[124:127], a[252:255], v[72:79], v237\n"
        "v_mul_f32 v110, v96, v110\n"
        "v_mul_f32 v111, v96, v111\n"
        "v_mfma_f32_16x16x32_bf16 v[64:67], v[132:135], v[76:79], 0\n"
        "v_cvt_pk_bf16_f32 v108, v108, v109\n"
        "v_cvt_pk_bf16_f32 v109, v110, v111\n"
        "v_mfma_f32_16x16x32_bf16 v[64:67], v[128:131], v[72:75], v[64:67]\n"
        "ds_read_b128 v[156:159], v247 offset:0\n"
        "v_cvt_pk_bf16_f32 v218, v116, v117\n"
        "v_mfma_f32_16x16x32_bf16 v[68:71], v[136:139], v[72:75], 0\n"
        "ds_write_b64 v246, v[108:109] offset:0\n"
        "v_mfma_f32_16x16x32_bf16 v[68:71], v[140:143], v[76:79], v[68:71]\n"
        "ds_read_b128 v[160:163], v247 offset:4096\n"
        "v_cvt_pk_bf16_f32 v219, v118, v119\n"
        "v_mfma_f32_16x16x32_bf16 v[120:123], v[148:151], v[76:79], 0\n"
        "ds_read_b128 v[164:167], v247 offset:4352\n"
        "v_max_u32 v236, v96, v97\n"
        "v_mfma_f32_16x16x32_bf16 v[120:123], v[144:147], v[72:75], v[120:123]\n"
        "v_mov_b64 v[116:117], 0\n"
        "v_add_f32 v125, v124, v125\n"
        "v_mfma_f32_16x16x32_bf16 v[112:115], a[48:51], a[236:239], 0\n"
        "v_rcp_f32 v124, v125\n"
        "v_mfma_f32_16x16x32_bf16 v[112:115], a[52:55], a[240:243], v[112:115]\n"
        "v_mul_f32 v64, v124, v64\n"
        "v_mul_f32 v65, v124, v65\n"
        "v_mfma_f32_16x16x32_bf16 v[112:115], a[56:59], a[244:247], v[112:115]\n"
        "v_mul_f32 v66, v124, v66\n"
        "v_mul_f32 v67, v124, v67\n"
        "v_mfma_f32_16x16x32_bf16 v[112:115], a[60:63], a[248:251], v[112:115]\n"
        "v_cvt_pk_bf16_f32 v64, v64, v65\n"
        "v_cvt_pk_bf16_f32 v65, v66, v67\n"
        "v_mfma_f32_16x16x32_bf16 v[112:115], a[64:67], a[216:219], v[112:115]\n"
        "v_mul_f32 v68, v124, v68\n"
        "v_mul_f32 v69, v124, v69\n"
        "v_mfma_f32_16x16x32_bf16 v[112:115], a[68:71], a[220:223], v[112:115]\n"
        "ds_read_b128 a[216:219], v248 offset:0\n"
        "v_mul_f32 v70, v124, v70\n"
        "v_smfmac_f32_16x16x64_bf16 v[116:119], a[252:255], v[80:87], v237\n"
        "ds_read_b128 a[220:223], v248 offset:3072\n"
        "v_mul_f32 v71, v124, v71\n"
        "v_mfma_f32_16x16x32_bf16 v[72:75], v[132:135], v[84:87], 0\n"
        "v_cvt_pk_bf16_f32 v66, v68, v69\n"
        "v_cvt_pk_bf16_f32 v67, v70, v71\n"
        "v_mfma_f32_16x16x32_bf16 v[72:75], v[128:131], v[80:83], v[72:75]\n"
        "ds_write_b128 v245, v[64:67] offset:256\n"
        "v_mfma_f32_16x16x32_bf16 v[76:79], v[136:139], v[80:83], 0\n"
        "v_cvt_pk_bf16_f32 v220, v112, v113\n"
        "v_cvt_pk_bf16_f32 v221, v114, v115\n"
        "v_mfma_f32_16x16x32_bf16 v[76:79], v[140:143], v[84:87], v[76:79]\n"
        "ds_read_b128 a[224:227], v254 offset:16\n"
        "v_cvt_pk_bf16_f32 v226, v112, v113\n"
        "v_mfma_f32_16x16x32_bf16 v[96:99], v[148:151], v[84:87], 0\n"
        "ds_read_b128 a[228:231], v254 offset:3088\n"
        "v_cvt_pk_bf16_f32 v227, v114, v115\n"
        "v_mfma_f32_16x16x32_bf16 v[96:99], v[144:147], v[80:83], v[96:99]\n"
        "v_mul_f32 v120, v124, v120\n"
        "v_mul_f32 v121, v124, v121\n"
        "v_mfma_f32_16x16x32_bf16 v[100:103], v[152:155], v[216:219], v[48:51]\n"
        "s_waitcnt lgkmcnt(3)\n"
        "ds_read_b128 v[152:155], v247 offset:256\n"
        "v_mul_f32 v122, v124, v122\n"
        "v_mfma_f32_16x16x32_bf16 v[100:103], v[168:171], v[220:223], v[100:103]\n"
        "v_mul_f32 v123, v124, v123\n"
        "v_cvt_pk_bf16_f32 v120, v120, v121\n"
        "v_mfma_f32_16x16x32_bf16 v[104:107], v[172:175], v[216:219], v[52:55]\n"
        "v_cvt_pk_bf16_f32 v121, v122, v123\n"
        "v_max3_u32 v236, v236, v124, v125\n"
        "v_mfma_f32_16x16x32_bf16 v[104:107], v[188:191], v[224:227], v[104:107]\n"
        "ds_write_b64 v246, v[120:121] offset:256\n"
        "v_mfma_f32_16x16x32_bf16 v[176:179], v[156:159], a[216:219], 0\n"
        "ds_read_b128 v[222:225], v252 offset:4608\n"
        "v_add_f32 v117, v116, v117\n"
        "v_mfma_f32_16x16x32_bf16 v[176:179], v[160:163], a[220:223], v[176:179]\n"
        "ds_read_b128 a[232:235], v248 offset:32\n"
        "v_mfma_f32_16x16x32_bf16 v[196:199], v[164:167], a[220:223], 0\n"
        "s_waitcnt lgkmcnt(3)\n"
        "ds_read_b128 a[220:223], v248 offset:3104\n"
        "v_mfma_f32_16x16x32_bf16 v[196:199], v[152:155], a[216:219], v[196:199]\n"
        "v_rcp_f32 v116, v117\n"
        "v_mfma_f32_16x16x32_bf16 v[200:203], v[152:155], a[224:227], 0\n"
        "ds_read_b128 a[216:219], v254 offset:48\n"
        "v_mul_f32 v72, v116, v72\n"
        "v_mfma_f32_16x16x32_bf16 v[200:203], v[164:167], a[228:231], v[200:203]\n"
        "v_exp_f32 v100, v100\n"
        "v_mfma_f32_16x16x32_bf16 v[180:183], v[160:163], a[228:231], 0\n"
        "ds_read_b128 a[228:231], v254 offset:3120\n"
        "v_mul_f32 v73, v116, v73\n"
        "v_mfma_f32_16x16x32_bf16 v[180:183], v[156:159], a[224:227], v[180:183]\n"
        "ds_read_b128 a[236:239], v247 offset:8192\n"
        "v_mul_f32 v74, v116, v74\n"
        "v_mfma_f32_16x16x32_bf16 v[108:111], v[192:195], v[216:219], v[56:59]\n"
        "s_waitcnt lgkmcnt(4)\n"
        "ds_read_b128 a[224:227], v248 offset:64\n"
        "v_mul_f32 v75, v116, v75\n"
        "v_mfma_f32_16x16x32_bf16 v[108:111], v[208:211], v[220:223], v[108:111]\n"
        "ds_read_b128 a[240:243], v247 offset:12288\n"
        "v_cvt_pk_bf16_f32 v72, v72, v73\n"
        "v_mfma_f32_16x16x32_bf16 v[112:115], v[212:215], v[216:219], v[60:63]\n"
        "ds_read_b128 a[244:247], v247 offset:12544\n"
        "v_cvt_pk_bf16_f32 v73, v74, v75\n"
        "v_mfma_f32_16x16x32_bf16 v[112:115], v[228:231], v[224:227], v[112:115]\n"
        "ds_read_b128 a[248:251], v247 offset:8448\n"
        "v_mul_f32 v76, v116, v76\n"
        "v_mfma_f32_16x16x32_bf16 v[184:187], v[156:159], a[232:235], 0\n"
        "s_waitcnt lgkmcnt(6)\n"
        "v_exp_f32 v101, v101\n"
        "v_mfma_f32_16x16x32_bf16 v[184:187], v[160:163], a[220:223], v[184:187]\n"
        "v_mul_f32 v77, v116, v77\n"
        "v_mul_f32 v78, v116, v78\n"
        "v_mfma_f32_16x16x32_bf16 v[204:207], v[164:167], a[220:223], 0\n"
        "ds_read_b128 a[220:223], v248 offset:3136\n"
        "v_mul_f32 v79, v116, v79\n"
        "v_mfma_f32_16x16x32_bf16 v[204:207], v[152:155], a[232:235], v[204:207]\n"
        "ds_read_b128 a[232:235], v254 offset:80\n"
        "v_cvt_pk_bf16_f32 v88, v100, v101\n"
        "v_mfma_f32_16x16x32_bf16 v[192:195], v[152:155], a[216:219], 0\n"
        "s_waitcnt lgkmcnt(4)\n"
        "v_exp_f32 v102, v102\n"
        "v_mfma_f32_16x16x32_bf16 v[192:195], v[164:167], a[228:231], v[192:195]\n"
        "v_exp_f32 v104, v104\n"
        "v_mfma_f32_16x16x32_bf16 v[172:175], v[160:163], a[228:231], 0\n"
        "ds_read_b128 a[228:231], v254 offset:3152\n"
        "v_cvt_pk_bf16_f32 v74, v76, v77\n"
        "v_mfma_f32_16x16x32_bf16 v[172:175], v[156:159], a[216:219], v[172:175]\n"
        "ds_read_b128 a[216:219], v248 offset:6144\n"
        "v_cvt_pk_bf16_f32 v75, v78, v79\n"
        "v_mfma_f32_16x16x32_bf16 v[212:215], v[156:159], a[224:227], 0\n"
        "s_waitcnt lgkmcnt(3)\n"
        "ds_write_b128 v245, v[72:75] offset:512\n"
        "v_mfma_f32_16x16x32_bf16 v[212:215], v[160:163], a[220:223], v[212:215]\n"
        "v_exp_f32 v103, v103\n"
        "v_mfma_f32_16x16x32_bf16 v[216:219], v[164:167], a[220:223], 0\n"
        "ds_read_b128 a[220:223], v248 offset:9216\n"
        "v_cvt_pk_bf16_f32 v89, v102, v103\n"
        "v_mfma_f32_16x16x32_bf16 v[216:219], v[152:155], a[224:227], v[216:219]\n"
        "s_waitcnt lgkmcnt(4)\n"
        "ds_read_b128 a[224:227], v254 offset:6160\n"
        "v_mul_f32 v96, v116, v96\n"
        "v_mfma_f32_16x16x32_bf16 v[224:227], v[152:155], a[232:235], 0\n"
        "s_waitcnt lgkmcnt(4)\n"
        "v_exp_f32 v105, v105\n"
        "v_mfma_f32_16x16x32_bf16 v[224:227], v[164:167], a[228:231], v[224:227]\n"
        "v_exp_f32 v106, v106\n"
        "v_mfma_f32_16x16x32_bf16 v[220:223], v[160:163], a[228:231], 0\n"
        "ds_read_b128 a[228:231], v254 offset:9232\n"
        "v_cvt_pk_bf16_f32 v90, v104, v105\n"
        "v_mfma_f32_16x16x32_bf16 v[220:223], v[156:159], a[232:235], v[220:223]\n"
        "s_waitcnt lgkmcnt(4)\n"
        "ds_read_b128 a[232:235], v248 offset:6176\n"
        "v_mul_f32 v97, v116, v97\n"
        "v_mfma_f32_16x16x32_bf16 v[176:179], a[236:239], a[216:219], v[176:179]\n"
        "s_waitcnt lgkmcnt(3)\n"
        "v_mul_f32 v98, v116, v98\n"
        "v_mul_f32 v99, v116, v99\n"
        "v_mfma_f32_16x16x32_bf16 v[176:179], a[240:243], a[220:223], v[176:179]\n"
        "v_exp_f32 v107, v107\n"
        "v_mfma_f32_16x16x32_bf16 v[196:199], a[244:247], a[220:223], v[196:199]\n"
        "ds_read_b128 a[220:223], v248 offset:9248\n"
        "v_cvt_pk_bf16_f32 v91, v106, v107\n"
        "v_mfma_f32_16x16x32_bf16 v[196:199], a[248:251], a[216:219], v[196:199]\n"
        "s_waitcnt lgkmcnt(3)\n"
        "ds_read_b128 a[216:219], v254 offset:6192\n"
        "v_cvt_pk_bf16_f32 v96, v96, v97\n"
        "v_mfma_f32_16x16x32_bf16 v[200:203], a[248:251], a[224:227], v[200:203]\n"
        "s_waitcnt lgkmcnt(3)\n"
        "ds_read_b128 v[156:159], v247 offset:16384\n"
        "v_cvt_pk_bf16_f32 v97, v98, v99\n"
        "v_mfma_f32_16x16x32_bf16 v[200:203], a[244:247], a[228:231], v[200:203]\n"
        "ds_write_b64 v246, v[96:97] offset:512\n"
        "v_mfma_f32_16x16x32_bf16 v[180:183], a[240:243], a[228:231], v[180:183]\n"
        "ds_read_b128 a[228:231], v254 offset:9264\n"
        "v_max3_u32 v236, v236, v116, v117\n"
        "v_mfma_f32_16x16x32_bf16 v[180:183], a[236:239], a[224:227], v[180:183]\n"
        "s_waitcnt lgkmcnt(5)\n"
        "ds_read_b128 a[224:227], v248 offset:6208\n"
        "v_mfma_f32_16x16x32_bf16 v[184:187], a[236:239], a[232:235], v[184:187]\n"
        "s_waitcnt lgkmcnt(5)\n"
        "ds_read_b128 v[160:163], v247 offset:20480\n"
        "v_mfma_f32_16x16x32_bf16 v[184:187], a[240:243], a[220:223], v[184:187]\n"
        "ds_read_b128 v[164:167], v247 offset:20736\n"
        "v_mfma_f32_16x16x32_bf16 v[204:207], a[244:247], a[220:223], v[204:207]\n"
        "ds_read_b128 a[220:223], v248 offset:9280\n"
        "v_mfma_f32_16x16x32_bf16 v[204:207], a[248:251], a[232:235], v[204:207]\n"
        "s_waitcnt lgkmcnt(6)\n"
        "ds_read_b128 a[232:235], v254 offset:6224\n"
        "v_mfma_f32_16x16x32_bf16 v[192:195], a[248:251], a[216:219], v[192:195]\n"
        "s_waitcnt lgkmcnt(5)\n"
        "ds_read_b128 v[152:155], v247 offset:16640\n"
        "v_mfma_f32_16x16x32_bf16 v[192:195], a[244:247], a[228:231], v[192:195]\n"
        "v_exp_f32 v108, v108\n"
        "v_mfma_f32_16x16x32_bf16 v[172:175], a[240:243], a[228:231], v[172:175]\n"
        "ds_read_b128 a[228:231], v254 offset:9296\n"
        "v_mfma_f32_16x16x32_bf16 v[172:175], a[236:239], a[216:219], v[172:175]\n"
        "s_waitcnt lgkmcnt(5)\n"
        "ds_read_b128 a[216:219], v248 offset:12288\n"
        "v_mfma_f32_16x16x32_bf16 v[212:215], a[236:239], a[224:227], v[212:215]\n"
        "s_waitcnt lgkmcnt(4)\n"
        "v_exp_f32 v109, v109\n"
        "v_mfma_f32_16x16x32_bf16 v[212:215], a[240:243], a[220:223], v[212:215]\n"
        ".long 0xbec200ff\n"
        "44:\n"
        "s_branch 60b\n"
        "v_exp_f32 v110, v110\n"
        "v_mfma_f32_16x16x32_bf16 v[216:219], a[244:247], a[220:223], v[216:219]\n"
        "ds_read_b128 a[220:223], v248 offset:15360\n"
        "v_cvt_pk_bf16_f32 v92, v108, v109\n"
        "v_mfma_f32_16x16x32_bf16 v[216:219], a[248:251], a[224:227], v[216:219]\n"
        "s_waitcnt lgkmcnt(3)\n"
        "ds_read_b128 a[224:227], v254 offset:12304\n"
        "v_mfma_f32_16x16x32_bf16 v[224:227], a[248:251], a[232:235], v[224:227]\n"
        "s_waitcnt lgkmcnt(3)\n"
        "v_exp_f32 v112, v112\n"
        "v_mfma_f32_16x16x32_bf16 v[224:227], a[244:247], a[228:231], v[224:227]\n"
        "v_exp_f32 v111, v111\n"
        "v_mfma_f32_16x16x32_bf16 v[220:223], a[240:243], a[228:231], v[220:223]\n"
        "ds_read_b128 a[228:231], v254 offset:15376\n"
        "v_cvt_pk_bf16_f32 v93, v110, v111\n"
        "v_mfma_f32_16x16x32_bf16 v[220:223], a[236:239], a[232:235], v[220:223]\n"
        "s_waitcnt lgkmcnt(3)\n"
        "ds_read_b128 a[232:235], v248 offset:12320\n"
        "v_mfma_f32_16x16x32_bf16 v[176:179], v[156:159], a[216:219], v[176:179]\n"
        "s_waitcnt lgkmcnt(3)\n"
        "v_exp_f32 v113, v113\n"
        "v_mfma_f32_16x16x32_bf16 v[176:179], v[160:163], a[220:223], v[176:179]\n"
        "v_exp_f32 v114, v114\n"
        "v_mfma_f32_16x16x32_bf16 v[196:199], v[164:167], a[220:223], v[196:199]\n"
        "ds_read_b128 a[220:223], v248 offset:15392\n"
        "v_cvt_pk_bf16_f32 v94, v112, v113\n"
        "v_mfma_f32_16x16x32_bf16 v[196:199], v[152:155], a[216:219], v[196:199]\n"
        "s_waitcnt lgkmcnt(3)\n"
        "ds_read_b128 a[216:219], v254 offset:12336\n"
        "v_mfma_f32_16x16x32_bf16 v[200:203], v[152:155], a[224:227], v[200:203]\n"
        "s_waitcnt lgkmcnt(3)\n"
        "v_exp_f32 v115, v115\n"
        "v_mfma_f32_16x16x32_bf16 v[200:203], v[164:167], a[228:231], v[200:203]\n"
        "v_cvt_pk_bf16_f32 v95, v114, v115\n"
        "v_mfma_f32_16x16x32_bf16 v[180:183], v[160:163], a[228:231], v[180:183]\n"
        "s_waitcnt vmcnt(0)\n"
        "s_barrier\n"
        "ds_read_b128 a[228:231], v254 offset:15408\n"
        "v_mfma_f32_16x16x32_bf16 v[180:183], v[156:159], a[224:227], v[180:183]\n"
        "s_waitcnt lgkmcnt(3)\n"
        "ds_read_b128 a[224:227], v248 offset:12352\n"
        "v_mfma_f32_16x16x32_bf16 v[184:187], v[156:159], a[232:235], v[184:187]\n"
        "s_waitcnt lgkmcnt(3)\n"
        "v_mfma_f32_16x16x32_bf16 v[184:187], v[160:163], a[220:223], v[184:187]\n"
        "v_mfma_f32_16x16x32_bf16 v[204:207], v[164:167], a[220:223], v[204:207]\n"
        "ds_read_b128 a[220:223], v248 offset:15424\n"
        "v_mfma_f32_16x16x32_bf16 v[204:207], v[152:155], a[232:235], v[204:207]\n"
        "s_waitcnt lgkmcnt(3)\n"
        "ds_read_b128 a[232:235], v254 offset:12368\n"
        "v_mfma_f32_16x16x32_bf16 v[192:195], v[152:155], a[216:219], v[192:195]\n"
        "s_waitcnt lgkmcnt(3)\n"
        "ds_read_b128 a[236:239], v240 offset:0\n"
        "v_mfma_f32_16x16x32_bf16 v[192:195], v[164:167], a[228:231], v[192:195]\n"
        "ds_read_b128 a[240:243], v241 offset:0\n"
        "v_mfma_f32_16x16x32_bf16 v[172:175], v[160:163], a[228:231], v[172:175]\n"
        "ds_read_b128 a[228:231], v254 offset:15440\n"
        "v_mfma_f32_16x16x32_bf16 v[172:175], v[156:159], a[216:219], v[172:175]\n"
        "s_waitcnt lgkmcnt(5)\n"
        "ds_read_b128 a[244:247], v240 offset:128\n"
        "v_mfma_f32_16x16x32_bf16 v[212:215], v[156:159], a[224:227], v[212:215]\n"
        "s_waitcnt lgkmcnt(5)\n"
        "ds_read_b128 a[248:251], v241 offset:128\n"
        "v_mfma_f32_16x16x32_bf16 v[212:215], v[160:163], a[220:223], v[212:215]\n"
        "ds_read_b128 a[216:219], v240 offset:256\n"
        "v_mfma_f32_16x16x32_bf16 v[216:219], v[164:167], a[220:223], v[216:219]\n"
        "ds_read_b128 a[220:223], v241 offset:256\n"
        "v_mfma_f32_16x16x32_bf16 v[216:219], v[152:155], a[224:227], v[216:219]\n"
        "s_waitcnt lgkmcnt(6)\n"
        "v_mfma_f32_16x16x32_bf16 v[224:227], v[152:155], a[232:235], v[224:227]\n"
        "s_waitcnt lgkmcnt(4)\n"
        "v_mfma_f32_16x16x32_bf16 v[224:227], v[164:167], a[228:231], v[224:227]\n"
        "v_mfma_f32_16x16x32_bf16 v[220:223], v[160:163], a[228:231], v[220:223]\n"
        "v_mfma_f32_16x16x32_bf16 v[220:223], v[156:159], a[232:235], v[220:223]\n"
        "s_waitcnt lgkmcnt(0)\n"
        "s_cmp_eq_u32 s42, 2\n"
        "s_cbranch_scc1 12f\n"
        "s_cmp_eq_u32 s42, 3\n"
        "s_cbranch_scc1 12f\n"
        ".p2align 6\n"
        "s_nop 0\n"
        "s_nop 0\n"
        "s_nop 0\n"
        "s_nop 0\n"
        "11:\n"
        "10:\n"
        "v_mfma_f32_16x16x32_bf16 v[112:115], a[72:75], a[236:239], 0\n"
        "v_mov_b64 v[124:125], 0\n"
        "v_mfma_f32_16x16x32_bf16 v[112:115], a[76:79], a[240:243], v[112:115]\n"
        "s_waitcnt lgkmcnt(3)\n"
        "s_add_u32 s41, s30, s40\n"
        "v_cvt_pk_bf16_f32 v232, v176, v180\n"
        "v_cvt_pk_bf16_f32 v233, v184, v172\n"
        "v_mfma_f32_16x16x32_bf16 v[112:115], a[80:83], a[244:247], v[112:115]\n"
        "s_waitcnt lgkmcnt(2)\n"
        "v_cvt_pk_bf16_f32 v234, v212, v220\n"
        "v_mfma_f32_16x16x32_bf16 v[112:115], a[84:87], a[248:251], v[112:115]\n"
        "s_waitcnt lgkmcnt(1)\n"
        "buffer_store_dwordx3 v[232:234], v250, s[16:19], s38 offen offset:0 sc1\n"
        "v_mfma_f32_16x16x32_bf16 v[112:115], a[88:91], a[216:219], v[112:115]\n"
        "s_waitcnt lgkmcnt(0)\n"
        "s_nop 0\n"
        "v_mfma_f32_16x16x32_bf16 v[112:115], a[92:95], a[220:223], v[112:115]\n"
        "v_cvt_pk_bf16_f32 v232, v177, v181\n"
        "v_cvt_pk_bf16_f32 v233, v185, v173\n"
        "v_smfmac_f32_16x16x64_bf16 v[124:127], a[252:255], v[88:95], v237\n"
        "v_cvt_pk_bf16_f32 v234, v213, v221\n"
        "v_mfma_f32_16x16x32_bf16 v[80:83], v[132:135], v[92:95], 0\n"
        "s_nop 2\n"
        "v_mfma_f32_16x16x32_bf16 v[80:83], v[128:131], v[88:91], v[80:83]\n"
        "v_cvt_pk_bf16_f32 v152, v112, v113\n"
        "v_cvt_pk_bf16_f32 v153, v114, v115\n"
        "v_mfma_f32_16x16x32_bf16 v[84:87], v[136:139], v[88:91], 0\n"
        "v_add_f32 v125, v124, v125\n"
        "v_mfma_f32_16x16x32_bf16 v[84:87], v[140:143], v[92:95], v[84:87]\n"
        "v_rcp_f32 v124, v125\n"
        "v_mfma_f32_16x16x32_bf16 v[96:99], v[148:151], v[92:95], 0\n"
        "buffer_store_dwordx3 v[232:234], v250, s[16:19], s38 offen offset:384 sc1\n"
        "v_mfma_f32_16x16x32_bf16 v[96:99], v[144:147], v[88:91], v[96:99]\n"
        "s_add_u32 s35, s47, 786432\n"
        "v_mul_f32 v80, v124, v80\n"
        "v_mul_f32 v81, v124, v81\n"
        "v_mfma_f32_16x16x32_bf16 v[116:119], a[116:119], a[220:223], 0\n"
        "s_sub_u32 s46, s35, 100663296\n"
        "v_mul_f32 v82, v124, v82\n"
        "v_mul_f32 v83, v124, v83\n"
        "v_mfma_f32_16x16x32_bf16 v[116:119], a[112:115], a[216:219], v[116:119]\n"
        "s_cmp_lt_u32 s46, 786432\n"
        "v_cvt_pk_bf16_f32 v80, v80, v81\n"
        "v_cvt_pk_bf16_f32 v81, v82, v83\n"
        "v_mfma_f32_16x16x32_bf16 v[116:119], a[108:111], a[248:251], v[116:119]\n"
        "s_cselect_b32 s35, s46, s35\n"
        "v_mul_f32 v84, v124, v84\n"
        "v_mul_f32 v85, v124, v85\n"
        "v_mfma_f32_16x16x32_bf16 v[116:119], a[104:107], a[244:247], v[116:119]\n"
        "v_mul_f32 v86, v124, v86\n"
        "v_mul_f32 v87, v124, v87\n"
        "v_mfma_f32_16x16x32_bf16 v[116:119], a[100:103], a[240:243], v[116:119]\n"
        "v_cvt_pk_bf16_f32 v232, v178, v182\n"
        "v_cvt_pk_bf16_f32 v233, v186, v174\n"
        "v_mfma_f32_16x16x32_bf16 v[116:119], a[96:99], a[236:239], v[116:119]\n"
        "v_cvt_pk_bf16_f32 v234, v214, v222\n"
        "v_cvt_pk_bf16_f32 v82, v84, v85\n"
        "v_mfma_f32_16x16x32_bf16 v[120:123], a[120:123], a[236:239], 0\n"
        "v_cvt_pk_bf16_f32 v83, v86, v87\n"
        "v_mul_f32 v96, v124, v96\n"
        "v_mfma_f32_16x16x32_bf16 v[120:123], a[124:127], a[240:243], v[120:123]\n"
        "buffer_store_dwordx3 v[232:234], v250, s[16:19], s38 offen offset:768 sc1\n"
        "v_mfma_f32_16x16x32_bf16 v[120:123], a[128:131], a[244:247], v[120:123]\n"
        "v_mul_f32 v97, v124, v97\n"
        "v_mul_f32 v98, v124, v98\n"
        "v_mfma_f32_16x16x32_bf16 v[120:123], a[132:135], a[248:251], v[120:123]\n"
        "v_mul_f32 v99, v124, v99\n"
        "v_cvt_pk_bf16_f32 v154, v116, v117\n"
        "v_mfma_f32_16x16x32_bf16 v[120:123], a[136:139], a[216:219], v[120:123]\n"
        "ds_write_b128 v245, v[80:83] offset:768\n"
        "v_mfma_f32_16x16x32_bf16 v[120:123], a[140:143], a[220:223], v[120:123]\n"
        "v_cvt_pk_bf16_f32 v155, v118, v119\n"
        "v_cvt_pk_bf16_f32 v96, v96, v97\n"
        "v_mfma_f32_16x16x32_bf16 v[112:115], a[20:23], a[220:223], 0\n"
        "v_cvt_pk_bf16_f32 v97, v98, v99\n"
        "v_max3_u32 v236, v236, v124, v125\n"
        "v_mfma_f32_16x16x32_bf16 v[112:115], a[16:19], a[216:219], v[112:115]\n"
        "v_cmp_lt_u32 vcc, s24, v236\n"
        "v_cvt_pk_bf16_f32 v232, v179, v183\n"
        "v_mfma_f32_16x16x32_bf16 v[112:115], a[12:15], a[248:251], v[112:115]\n"
        "ds_write_b64 v246, v[96:97] offset:768\n"
        "s_cmp_lg_u64 vcc, 0\n"
        "s_addc_u32 s25, s25, s25\n"
        "v_mfma_f32_16x16x32_bf16 v[112:115], a[8:11], a[244:247], v[112:115]\n"
        "v_cvt_pk_bf16_f32 v233, v187, v175\n"
        "v_cvt_pk_bf16_f32 v234, v215, v223\n"
        "v_mfma_f32_16x16x32_bf16 v[112:115], a[4:7], a[240:243], v[112:115]\n"
        "v_cvt_pk_bf16_f32 v168, v120, v121\n"
        "v_cvt_pk_bf16_f32 v169, v122, v123\n"
        "v_mfma_f32_16x16x32_bf16 v[112:115], a[0:3], a[236:239], v[112:115]\n"
        "buffer_store_dwordx3 v[232:234], v250, s[16:19], s38 offen offset:1152 sc1\n"
        "v_mfma_f32_16x16x32_bf16 v[116:119], a[24:27], a[236:239], 0\n"
        "v_xor_b32 v251, 0x2000, v251\n"
        "v_xor_b32 v252, 0x2000, v252\n"
        "v_mfma_f32_16x16x32_bf16 v[116:119], a[28:31], a[240:243], v[116:119]\n"
        "v_cvt_pk_bf16_f32 v232, v196, v200\n"
        "v_cvt_pk_bf16_f32 v233, v204, v192\n"
        "v_mfma_f32_16x16x32_bf16 v[116:119], a[32:35], a[244:247], v[116:119]\n"
        "v_cvt_pk_bf16_f32 v156, v112, v113\n"
        "v_cvt_pk_bf16_f32 v157, v114, v115\n"
        "v_mfma_f32_16x16x32_bf16 v[116:119], a[36:39], a[248:251], v[116:119]\n"
        "v_cvt_pk_bf16_f32 v234, v216, v224\n"
        "v_mfma_f32_16x16x32_bf16 v[116:119], a[40:43], a[216:219], v[116:119]\n"
        "v_mfma_f32_16x16x32_bf16 v[116:119], a[44:47], a[220:223], v[116:119]\n"
        "s_nop 2\n"
        "v_mfma_f32_16x16x32_bf16 v[120:123], a[68:71], a[220:223], 0\n"
        "s_nop 2\n"
        "v_mfma_f32_16x16x32_bf16 v[120:123], a[64:67], a[216:219], v[120:123]\n"
        "buffer_store_dwordx3 v[232:234], v250, s[16:19], s39 offen offset:0 sc1\n"
        "v_mfma_f32_16x16x32_bf16 v[120:123], a[60:63], a[248:251], v[120:123]\n"
        "ds_read_b128 a[224:227], v240 offset:1536\n"
        "v_cvt_pk_bf16_f32 v158, v116, v117\n"
        "v_mfma_f32_16x16x32_bf16 v[120:123], a[56:59], a[244:247], v[120:123]\n"
        "ds_read_b128 a[228:231], v241 offset:1536\n"
        "v_cvt_pk_bf16_f32 v159, v118, v119\n"
        "v_mfma_f32_16x16x32_bf16 v[120:123], a[52:55], a[240:243], v[120:123]\n"
        "ds_read_b128 a[232:235], v240 offset:1664\n"
        "v_cvt_pk_bf16_f32 v232, v197, v201\n"
        "v_mfma_f32_16x16x32_bf16 v[120:123], a[48:51], a[236:239], v[120:123]\n"
        "v_cvt_pk_bf16_f32 v233, v205, v193\n"
        "v_cvt_pk_bf16_f32 v234, v217, v225\n"
        "v_mfma_f32_16x16x32_bf16 v[112:115], a[236:239], a[144:147], 0\n"
        "v_add_u32 v247, s45, v247\n"
        "s_nop 0\n"
        "v_mfma_f32_16x16x32_bf16 v[112:115], a[240:243], a[148:151], v[112:115]\n"
        "s_nop 1\n"
        "v_cvt_pk_bf16_f32 v160, v120, v121\n"
        "v_mfma_f32_16x16x32_bf16 v[112:115], a[244:247], a[152:155], v[112:115]\n"
        "v_cvt_pk_bf16_f32 v161, v122, v123\n"
        "v_cvt_pk_bf16_f32 v166, v120, v121\n"
        "v_mfma_f32_16x16x32_bf16 v[112:115], a[248:251], a[156:159], v[112:115]\n"
        "v_cvt_pk_bf16_f32 v167, v122, v123\n"
        "v_mfma_f32_16x16x32_bf16 v[112:115], a[216:219], a[160:163], v[112:115]\n"
        "buffer_store_dwordx3 v[232:234], v250, s[16:19], s39 offen offset:384 sc1\n"
        "v_mfma_f32_16x16x32_bf16 v[112:115], a[220:223], a[164:167], v[112:115]\n"
        "s_nop 2\n"
        "v_mfma_f32_16x16x32_bf16 v[116:119], a[220:223], a[188:191], 0\n"
        "s_nop 2\n"
        "v_mfma_f32_16x16x32_bf16 v[116:119], a[216:219], a[184:187], v[116:119]\n"
        "v_cvt_pk_bf16_f32 v128, v112, v113\n"
        "v_cvt_pk_bf16_f32 v129, v114, v115\n"
        "v_mfma_f32_16x16x32_bf16 v[116:119], a[248:251], a[180:183], v[116:119]\n"
        "v_cvt_pk_bf16_f32 v232, v198, v202\n"
        "v_cvt_pk_bf16_f32 v233, v206, v194\n"
        "v_mfma_f32_16x16x32_bf16 v[116:119], a[244:247], a[176:179], v[116:119]\n"
        "v_cvt_pk_bf16_f32 v234, v218, v226\n"
        "v_mfma_f32_16x16x32_bf16 v[116:119], a[240:243], a[172:175], v[116:119]\n"
        "v_mfma_f32_16x16x32_bf16 v[116:119], a[236:239], a[168:171], v[116:119]\n"
        "s_nop 2\n"
        "v_mfma_f32_16x16x32_bf16 v[120:123], a[236:239], a[192:195], 0\n"
        "ds_read_b128 a[236:239], v241 offset:1664\n"
        "s_nop 0\n"
        "v_mfma_f32_16x16x32_bf16 v[120:123], a[240:243], a[196:199], v[120:123]\n"
        "ds_read_b128 a[240:243], v240 offset:1792\n"
        "v_cvt_pk_bf16_f32 v136, v116, v117\n"
        "v_mfma_f32_16x16x32_bf16 v[120:123], a[244:247], a[200:203], v[120:123]\n"
        "ds_read_b128 a[244:247], v241 offset:1792\n"
        "v_cvt_pk_bf16_f32 v137, v118, v119\n"
        "v_mfma_f32_16x16x32_bf16 v[120:123], a[248:251], a[204:207], v[120:123]\n"
        "buffer_store_dwordx3 v[232:234], v250, s[16:19], s39 offen offset:768 sc1\n"
        "v_mfma_f32_16x16x32_bf16 v[120:123], a[216:219], a[208:211], v[120:123]\n"
        "s_nop 0\n"
        "v_cvt_pk_bf16_f32 v232, v199, v203\n"
        "v_cvt_pk_bf16_f32 v233, v207, v195\n"
        "v_mfma_f32_16x16x32_bf16 v[120:123], a[220:223], a[212:215], v[120:123]\n"
        "s_waitcnt lgkmcnt(5)\n"
        "v_cvt_pk_bf16_f32 v234, v219, v227\n"
        "s_nop 0\n"
        "v_mfma_f32_16x16x32_bf16 v[112:115], a[72:75], a[224:227], 0\n"
        "s_waitcnt lgkmcnt(4)\n"
        "s_nop 1\n"
        "v_mfma_f32_16x16x32_bf16 v[112:115], a[76:79], a[228:231], v[112:115]\n"
        "s_waitcnt lgkmcnt(3)\n"
        "s_nop 1\n"
        "v_mfma_f32_16x16x32_bf16 v[112:115], a[80:83], a[232:235], v[112:115]\n"
        "s_waitcnt lgkmcnt(2)\n"
        "v_cvt_pk_bf16_f32 v144, v120, v121\n"
        "v_cvt_pk_bf16_f32 v145, v122, v123\n"
        "v_mfma_f32_16x16x32_bf16 v[112:115], a[84:87], a[236:239], v[112:115]\n"
        "s_waitcnt lgkmcnt(1)\n"
        "v_mfma_f32_16x16x32_bf16 v[112:115], a[88:91], a[240:243], v[112:115]\n"
        "s_waitcnt lgkmcnt(0)\n"
        "buffer_store_dwordx3 v[232:234], v250, s[16:19], s39 offen offset:1152 sc1\n"
        "v_mfma_f32_16x16x32_bf16 v[112:115], a[92:95], a[244:247], v[112:115]\n"
        "s_nop 2\n"
        "v_mfma_f32_16x16x32_bf16 v[116:119], a[116:119], a[244:247], 0\n"
        "s_nop 2\n"
        "v_mfma_f32_16x16x32_bf16 v[116:119], a[112:115], a[240:243], v[116:119]\n"
        "v_cvt_pk_bf16_f32 v172, v112, v113\n"
        "v_cvt_pk_bf16_f32 v173, v114, v115\n"
        "v_mfma_f32_16x16x32_bf16 v[116:119], a[108:111], a[236:239], v[116:119]\n"
        "v_mfma_f32_16x16x32_bf16 v[116:119], a[104:107], a[232:235], v[116:119]\n"
        "v_mfma_f32_16x16x32_bf16 v[116:119], a[100:103], a[228:231], v[116:119]\n"
        "v_mfma_f32_16x16x32_bf16 v[116:119], a[96:99], a[224:227], v[116:119]\n"
        "s_mov_b32 m0, s48\n"
        "s_nop 1\n"
        "v_mfma_f32_16x16x32_bf16 v[120:123], a[120:123], a[224:227], 0\n"
        "ds_read_b128 v[162:165], v251 offset:0\n"
        "s_nop 0\n"
        "v_mfma_f32_16x16x32_bf16 v[120:123], a[124:127], a[228:231], v[120:123]\n"
        "buffer_load_dwordx4 v249, s[12:15], s36 offen offset:0 lds\n"
        "v_mfma_f32_16x16x32_bf16 v[120:123], a[128:131], a[232:235], v[120:123]\n"
        "v_cvt_pk_bf16_f32 v174, v116, v117\n"
        "v_cvt_pk_bf16_f32 v175, v118, v119\n"
        "v_mfma_f32_16x16x32_bf16 v[120:123], a[132:135], a[236:239], v[120:123]\n"
        "v_mfma_f32_16x16x32_bf16 v[120:123], a[136:139], a[240:243], v[120:123]\n"
        "v_mfma_f32_16x16x32_bf16 v[120:123], a[140:143], a[244:247], v[120:123]\n"
        "s_nop 2\n"
        "v_mfma_f32_16x16x32_bf16 v[112:115], a[20:23], a[244:247], 0\n"
        "s_nop 2\n"
        "v_mfma_f32_16x16x32_bf16 v[112:115], a[16:19], a[240:243], v[112:115]\n"
        "v_cvt_pk_bf16_f32 v190, v120, v121\n"
        "v_cvt_pk_bf16_f32 v191, v122, v123\n"
        "v_mfma_f32_16x16x32_bf16 v[112:115], a[12:15], a[236:239], v[112:115]\n"
        "v_mfma_f32_16x16x32_bf16 v[112:115], a[8:11], a[232:235], v[112:115]\n"
        "v_mfma_f32_16x16x32_bf16 v[112:115], a[4:7], a[228:231], v[112:115]\n"
        "ds_read_b128 v[182:185], v251 offset:512\n"
        "v_mfma_f32_16x16x32_bf16 v[112:115], a[0:3], a[224:227], v[112:115]\n"
        "s_nop 2\n"
        "v_mfma_f32_16x16x32_bf16 v[116:119], a[24:27], a[224:227], 0\n"
        "s_nop 2\n"
        "v_mfma_f32_16x16x32_bf16 v[116:119], a[28:31], a[228:231], v[116:119]\n"
        "v_cvt_pk_bf16_f32 v176, v112, v113\n"
        "v_cvt_pk_bf16_f32 v177, v114, v115\n"
        "v_mfma_f32_16x16x32_bf16 v[116:119], a[32:35], a[232:235], v[116:119]\n"
        "v_mfma_f32_16x16x32_bf16 v[116:119], a[36:39], a[236:239], v[116:119]\n"
        "buffer_load_dwordx4 v253, s[12:15], s36 offen offset:1024 lds\n"
        "v_mfma_f32_16x16x32_bf16 v[116:119], a[40:43], a[240:243], v[116:119]\n"
        "v_mfma_f32_16x16x32_bf16 v[116:119], a[44:47], a[244:247], v[116:119]\n"
        "s_nop 2\n"
        "v_mfma_f32_16x16x32_bf16 v[120:123], a[68:71], a[244:247], 0\n"
        "s_nop 2\n"
        "v_mfma_f32_16x16x32_bf16 v[120:123], a[64:67], a[240:243], v[120:123]\n"
        "v_cvt_pk_bf16_f32 v178, v116, v117\n"
        "v_cvt_pk_bf16_f32 v179, v118, v119\n"
        "v_mfma_f32_16x16x32_bf16 v[120:123], a[60:63], a[236:239], v[120:123]\n"
        "v_mfma_f32_16x16x32_bf16 v[120:123], a[56:59], a[232:235], v[120:123]\n"
        "v_mfma_f32_16x16x32_bf16 v[120:123], a[52:55], a[228:231], v[120:123]\n"
        "v_mfma_f32_16x16x32_bf16 v[120:123], a[48:51], a[224:227], v[120:123]\n"
        "s_nop 2\n"
        "v_mfma_f32_16x16x32_bf16 v[112:115], a[224:227], a[144:147], 0\n"
        "s_nop 2\n"
        "v_mfma_f32_16x16x32_bf16 v[112:115], a[228:231], a[148:151], v[112:115]\n"
        "v_cvt_pk_bf16_f32 v180, v120, v121\n"
        "v_cvt_pk_bf16_f32 v181, v122, v123\n"
        "v_mfma_f32_16x16x32_bf16 v[112:115], a[232:235], a[152:155], v[112:115]\n"
        "v_cvt_pk_bf16_f32 v186, v120, v121\n"
        "v_cvt_pk_bf16_f32 v187, v122, v123\n"
        "v_mfma_f32_16x16x32_bf16 v[112:115], a[236:239], a[156:159], v[112:115]\n"
        "s_add_u32 m0, s41, 0\n"
        "v_mfma_f32_16x16x32_bf16 v[112:115], a[240:243], a[160:163], v[112:115]\n"
        "ds_read_b128 a[248:251], v240 offset:12288\n"
        "v_mfma_f32_16x16x32_bf16 v[112:115], a[244:247], a[164:167], v[112:115]\n"
        "buffer_load_dwordx4 v242, s[8:11], s47 offen lds\n"
        "v_mfma_f32_16x16x32_bf16 v[96:99], v[152:155], v[156:159], v[0:3]\n"
        "s_waitcnt lgkmcnt(2)\n"
        "ds_read_b128 a[216:219], v241 offset:12288\n"
        "s_nop 0\n"
        "v_mfma_f32_16x16x32_bf16 v[96:99], v[168:171], v[160:163], v[96:99]\n"
        "ds_read_b128 a[220:223], v240 offset:12416\n"
        "s_sub_u32 s31, s30, s28\n"
        "s_nop 0\n"
        "v_mfma_f32_16x16x32_bf16 v[100:103], v[172:175], v[156:159], v[4:7]\n"
        "s_sub_u32 s45, s28, s29\n"
        "v_cvt_pk_bf16_f32 v130, v112, v113\n"
        "v_cvt_pk_bf16_f32 v131, v114, v115\n"
        "v_mfma_f32_16x16x32_bf16 v[100:103], v[188:191], v[164:167], v[100:103]\n"
        "s_add_u32 s28, s28, s31\n"
        "ds_read_b128 v[162:165], v252 offset:0\n"
        "v_mfma_f32_16x16x32_bf16 v[116:119], a[244:247], a[188:191], 0\n"
        "s_mov_b32 s30, s29\n"
        "v_exp_f32 v96, v96\n"
        "v_mfma_f32_16x16x32_bf16 v[116:119], a[240:243], a[184:187], v[116:119]\n"
        "s_add_u32 s29, s29, s45\n"
        "v_exp_f32 v97, v97\n"
        "v_mfma_f32_16x16x32_bf16 v[116:119], a[236:239], a[180:183], v[116:119]\n"
        "v_exp_f32 v98, v98\n"
        "v_mfma_f32_16x16x32_bf16 v[116:119], a[232:235], a[176:179], v[116:119]\n"
        "v_exp_f32 v100, v100\n"
        "v_mfma_f32_16x16x32_bf16 v[116:119], a[228:231], a[172:175], v[116:119]\n"
        "v_exp_f32 v99, v99\n"
        "v_mfma_f32_16x16x32_bf16 v[116:119], a[224:227], a[168:171], v[116:119]\n"
        "v_exp_f32 v101, v101\n"
        "v_mfma_f32_16x16x32_bf16 v[104:107], v[152:155], v[176:179], v[16:19]\n"
        "s_waitcnt lgkmcnt(4)\n"
        "v_exp_f32 v102, v102\n"
        "v_mfma_f32_16x16x32_bf16 v[104:107], v[168:171], v[180:183], v[104:107]\n"
        "s_add_u32 m0, s41, 1024\n"
        "v_cvt_pk_bf16_f32 v64, v96, v97\n"
        "v_cvt_pk_bf16_f32 v65, v98, v99\n"
        "v_mfma_f32_16x16x32_bf16 v[108:111], v[172:175], v[176:179], v[20:23]\n"
        "v_cvt_pk_bf16_f32 v138, v116, v117\n"
        "v_cvt_pk_bf16_f32 v139, v118, v119\n"
        "v_mfma_f32_16x16x32_bf16 v[108:111], v[188:191], v[184:187], v[108:111]\n"
        "buffer_load_dwordx4 v243, s[8:11], s47 offen lds\n"
        "v_mfma_f32_16x16x32_bf16 v[120:123], a[224:227], a[192:195], 0\n"
        "ds_read_b128 a[224:227], v241 offset:12416\n"
        "v_cvt_pk_bf16_f32 v66, v100, v101\n"
        "v_mfma_f32_16x16x32_bf16 v[120:123], a[228:231], a[196:199], v[120:123]\n"
        "ds_read_b128 a[228:231], v240 offset:12544\n"
        "v_mfma_f32_16x16x32_bf16 v[120:123], a[232:235], a[200:203], v[120:123]\n"
        "ds_read_b128 a[232:235], v241 offset:12544\n"
        "v_mfma_f32_16x16x32_bf16 v[120:123], a[236:239], a[204:207], v[120:123]\n"
        "ds_read_b128 v[202:205], v251 offset:4096\n"
        "v_mfma_f32_16x16x32_bf16 v[120:123], a[240:243], a[208:211], v[120:123]\n"
        "ds_read_b128 v[182:185], v252 offset:512\n"
        "v_mfma_f32_16x16x32_bf16 v[120:123], a[244:247], a[212:215], v[120:123]\n"
        "s_waitcnt lgkmcnt(8)\n"
        "v_exp_f32 v103, v103\n"
        "v_mfma_f32_16x16x32_bf16 v[112:115], a[0:3], a[248:251], 0\n"
        "s_waitcnt lgkmcnt(7)\n"
        "v_cvt_pk_bf16_f32 v67, v102, v103\n"
        "s_nop 0\n"
        "v_mfma_f32_16x16x32_bf16 v[112:115], a[4:7], a[216:219], v[112:115]\n"
        "s_waitcnt lgkmcnt(6)\n"
        "v_exp_f32 v104, v104\n"
        "v_mfma_f32_16x16x32_bf16 v[112:115], a[8:11], a[220:223], v[112:115]\n"
        "s_waitcnt lgkmcnt(4)\n"
        "v_exp_f32 v105, v105\n"
        "v_mfma_f32_16x16x32_bf16 v[112:115], a[12:15], a[224:227], v[112:115]\n"
        "s_waitcnt lgkmcnt(3)\n"
        "v_exp_f32 v106, v106\n"
        "v_mfma_f32_16x16x32_bf16 v[112:115], a[16:19], a[228:231], v[112:115]\n"
        "s_waitcnt lgkmcnt(2)\n"
        "v_cvt_pk_bf16_f32 v72, v104, v105\n"
        "v_cvt_pk_bf16_f32 v146, v120, v121\n"
        "v_mfma_f32_16x16x32_bf16 v[112:115], a[20:23], a[232:235], v[112:115]\n"
        "s_add_u32 m0, s41, 2048\n"
        "v_exp_f32 v108, v108\n"
        "v_mfma_f32_16x16x32_bf16 v[116:119], a[44:47], a[232:235], 0\n"
        "v_cvt_pk_bf16_f32 v147, v122, v123\n"
        "v_add_u32 v245, s45, v245\n"
        "v_mfma_f32_16x16x32_bf16 v[116:119], a[40:43], a[228:231], v[116:119]\n"
        "buffer_load_dwordx4 v244, s[8:11], s47 offen lds\n"
        "v_mfma_f32_16x16x32_bf16 v[116:119], a[36:39], a[224:227], v[116:119]\n"
        "v_exp_f32 v107, v107\n"
        "v_mfma_f32_16x16x32_bf16 v[116:119], a[32:35], a[220:223], v[116:119]\n"
        "v_exp_f32 v109, v109\n"
        "v_mfma_f32_16x16x32_bf16 v[116:119], a[28:31], a[216:219], v[116:119]\n"
        "v_cvt_pk_bf16_f32 v73, v106, v107\n"
        "v_cvt_pk_bf16_f32 v74, v108, v109\n"
        "v_mfma_f32_16x16x32_bf16 v[116:119], a[24:27], a[248:251], v[116:119]\n"
        "v_exp_f32 v110, v110\n"
        "v_mfma_f32_16x16x32_bf16 v[120:123], a[48:51], a[248:251], 0\n"
        "v_exp_f32 v111, v111\n"
        "v_mfma_f32_16x16x32_bf16 v[120:123], a[52:55], a[216:219], v[120:123]\n"
        "v_cvt_pk_bf16_f32 v75, v110, v111\n"
        "v_cvt_pk_bf16_f32 v196, v112, v113\n"
        "v_mfma_f32_16x16x32_bf16 v[120:123], a[56:59], a[220:223], v[120:123]\n"
        "v_cvt_pk_bf16_f32 v197, v114, v115\n"
        "v_cvt_pk_bf16_f32 v198, v116, v117\n"
        "v_mfma_f32_16x16x32_bf16 v[120:123], a[60:63], a[224:227], v[120:123]\n"
        "v_cvt_pk_bf16_f32 v199, v118, v119\n"
        "v_add_u32 v246, s45, v246\n"
        "v_mfma_f32_16x16x32_bf16 v[120:123], a[64:67], a[228:231], v[120:123]\n"
        "v_mfma_f32_16x16x32_bf16 v[120:123], a[68:71], a[232:235], v[120:123]\n"
        "s_nop 2\n"
        "v_mfma_f32_16x16x32_bf16 v[112:115], a[92:95], a[232:235], 0\n"
        "s_nop 2\n"
        "v_mfma_f32_16x16x32_bf16 v[112:115], a[88:91], a[228:231], v[112:115]\n"
        "s_add_u32 m0, s41, 12288\n"
        "v_cvt_pk_bf16_f32 v200, v120, v121\n"
        "v_cvt_pk_bf16_f32 v201, v122, v123\n"
        "v_mfma_f32_16x16x32_bf16 v[112:115], a[84:87], a[224:227], v[112:115]\n"
        "v_cvt_pk_bf16_f32 v206, v120, v121\n"
        "v_cvt_pk_bf16_f32 v207, v122, v123\n"
        "v_mfma_f32_16x16x32_bf16 v[112:115], a[80:83], a[220:223], v[112:115]\n"
        "buffer_load_dwordx4 v242, s[8:11], s35 offen lds\n"
        "v_mfma_f32_16x16x32_bf16 v[112:115], a[76:79], a[216:219], v[112:115]\n"
        "v_mfma_f32_16x16x32_bf16 v[112:115], a[72:75], a[248:251], v[112:115]\n"
        "s_nop 2\n"
        "v_mfma_f32_16x16x32_bf16 v[96:99], v[152:155], v[196:199], v[32:35]\n"
        "s_waitcnt lgkmcnt(1)\n"
        "s_nop 1\n"
        "v_mfma_f32_16x16x32_bf16 v[96:99], v[168:171], v[200:203], v[96:99]\n"
        "s_nop 0\n"
        "v_cvt_pk_bf16_f32 v192, v112, v113\n"
        "v_cvt_pk_bf16_f32 v193, v114, v115\n"
        "v_mfma_f32_16x16x32_bf16 v[100:103], v[172:175], v[196:199], v[36:39]\n"
        "v_mfma_f32_16x16x32_bf16 v[100:103], v[188:191], v[204:207], v[100:103]\n"
        "ds_read_b128 v[202:205], v252 offset:4096\n"
        "s_cmp_eq_u32 s37, 100663296\n"
        "v_mfma_f32_16x16x32_bf16 v[116:119], a[96:99], a[248:251], 0\n"
        "s_cselect_b32 s46, 0, s37\n"
        "v_exp_f32 v96, v96\n"
        "v_mfma_f32_16x16x32_bf16 v[116:119], a[100:103], a[216:219], v[116:119]\n"
        "s_add_u32 s38, s46, s43\n"
        "v_exp_f32 v97, v97\n"
        "v_mfma_f32_16x16x32_bf16 v[116:119], a[104:107], a[220:223], v[116:119]\n"
        "s_add_u32 s39, s46, s44\n"
        "v_exp_f32 v98, v98\n"
        "v_mfma_f32_16x16x32_bf16 v[116:119], a[108:111], a[224:227], v[116:119]\n"
        "s_add_u32 s37, s37, 6291456\n"
        "v_exp_f32 v100, v100\n"
        "v_mfma_f32_16x16x32_bf16 v[116:119], a[112:115], a[228:231], v[116:119]\n"
        "v_exp_f32 v99, v99\n"
        "v_mfma_f32_16x16x32_bf16 v[116:119], a[116:119], a[232:235], v[116:119]\n"
        "s_add_u32 m0, s41, 13312\n"
        "v_exp_f32 v101, v101\n"
        "v_mfma_f32_16x16x32_bf16 v[120:123], a[140:143], a[232:235], 0\n"
        "v_exp_f32 v102, v102\n"
        "v_mfma_f32_16x16x32_bf16 v[120:123], a[136:139], a[228:231], v[120:123]\n"
        "buffer_load_dwordx4 v243, s[8:11], s35 offen lds\n"
        "v_mfma_f32_16x16x32_bf16 v[120:123], a[132:135], a[224:227], v[120:123]\n"
        "v_cvt_pk_bf16_f32 v80, v96, v97\n"
        "v_cvt_pk_bf16_f32 v194, v116, v117\n"
        "v_mfma_f32_16x16x32_bf16 v[120:123], a[128:131], a[220:223], v[120:123]\n"
        "v_cvt_pk_bf16_f32 v195, v118, v119\n"
        "v_cvt_pk_bf16_f32 v81, v98, v99\n"
        "v_mfma_f32_16x16x32_bf16 v[120:123], a[124:127], a[216:219], v[120:123]\n"
        "v_cvt_pk_bf16_f32 v82, v100, v101\n"
        "v_mfma_f32_16x16x32_bf16 v[120:123], a[120:123], a[248:251], v[120:123]\n"
        "v_exp_f32 v103, v103\n"
        "v_mfma_f32_16x16x32_bf16 v[112:115], a[248:251], a[144:147], 0\n"
        "v_cvt_pk_bf16_f32 v83, v102, v103\n"
        "s_nop 0\n"
        "v_mfma_f32_16x16x32_bf16 v[112:115], a[216:219], a[148:151], v[112:115]\n"
        "s_nop 2\n"
        "v_mfma_f32_16x16x32_bf16 v[112:115], a[220:223], a[152:155], v[112:115]\n"
        "ds_read_b128 a[236:239], v240 offset:13824\n"
        "v_cvt_pk_bf16_f32 v208, v120, v121\n"
        "v_mfma_f32_16x16x32_bf16 v[112:115], a[224:227], a[156:159], v[112:115]\n"
        "ds_read_b128 a[240:243], v241 offset:13824\n"
        "v_cvt_pk_bf16_f32 v209, v122, v123\n"
        "v_mfma_f32_16x16x32_bf16 v[112:115], a[228:231], a[160:163], v[112:115]\n"
        "ds_read_b128 a[244:247], v240 offset:13952\n"
        "v_mfma_f32_16x16x32_bf16 v[112:115], a[232:235], a[164:167], v[112:115]\n"
        "s_nop 2\n"
        "v_mfma_f32_16x16x32_bf16 v[104:107], v[192:195], v[156:159], v[8:11]\n"
        "s_nop 1\n"
        "v_mfma_f32_16x16x32_bf16 v[104:107], v[208:211], v[160:163], v[104:107]\n"
        "s_add_u32 m0, s41, 14336\n"
        "v_cvt_pk_bf16_f32 v132, v112, v113\n"
        "v_cvt_pk_bf16_f32 v133, v114, v115\n"
        "v_mfma_f32_16x16x32_bf16 v[116:119], a[232:235], a[188:191], 0\n"
        "v_mfma_f32_16x16x32_bf16 v[116:119], a[228:231], a[184:187], v[116:119]\n"
        "buffer_load_dwordx4 v244, s[8:11], s35 offen lds\n"
        "v_mfma_f32_16x16x32_bf16 v[116:119], a[224:227], a[180:183], v[116:119]\n"
        "v_mfma_f32_16x16x32_bf16 v[116:119], a[220:223], a[176:179], v[116:119]\n"
        "v_exp_f32 v104, v104\n"
        "v_mfma_f32_16x16x32_bf16 v[116:119], a[216:219], a[172:175], v[116:119]\n"
        "v_exp_f32 v105, v105\n"
        "v_mfma_f32_16x16x32_bf16 v[116:119], a[248:251], a[168:171], v[116:119]\n"
        "v_exp_f32 v106, v106\n"
        "v_mfma_f32_16x16x32_bf16 v[108:111], v[192:195], v[176:179], v[24:27]\n"
        "s_waitcnt lgkmcnt(4)\n"
        "v_exp_f32 v107, v107\n"
        "v_mfma_f32_16x16x32_bf16 v[108:111], v[208:211], v[180:183], v[108:111]\n"
        "v_cvt_pk_bf16_f32 v68, v104, v105\n"
        "v_cvt_pk_bf16_f32 v69, v106, v107\n"
        "v_mfma_f32_16x16x32_bf16 v[120:123], a[248:251], a[192:195], 0\n"
        "ds_read_b128 a[248:251], v241 offset:13952\n"
        "v_cvt_pk_bf16_f32 v140, v116, v117\n"
        "v_mfma_f32_16x16x32_bf16 v[120:123], a[216:219], a[196:199], v[120:123]\n"
        "ds_read_b128 a[216:219], v240 offset:14080\n"
        "v_cvt_pk_bf16_f32 v141, v118, v119\n"
        "v_mfma_f32_16x16x32_bf16 v[120:123], a[220:223], a[200:203], v[120:123]\n"
        "ds_read_b128 a[220:223], v241 offset:14080\n"
        "v_mfma_f32_16x16x32_bf16 v[120:123], a[224:227], a[204:207], v[120:123]\n"
        "v_exp_f32 v108, v108\n"
        "v_mfma_f32_16x16x32_bf16 v[120:123], a[228:231], a[208:211], v[120:123]\n"
        "v_exp_f32 v109, v109\n"
        "v_mfma_f32_16x16x32_bf16 v[120:123], a[232:235], a[212:215], v[120:123]\n"
        "v_exp_f32 v110, v110\n"
        "v_mfma_f32_16x16x32_bf16 v[96:99], v[192:195], v[196:199], v[40:43]\n"
        "s_waitcnt lgkmcnt(6)\n"
        "v_exp_f32 v111, v111\n"
        "v_mfma_f32_16x16x32_bf16 v[96:99], v[208:211], v[200:203], v[96:99]\n"
        "s_waitcnt lgkmcnt(5)\n"
        "v_cvt_pk_bf16_f32 v76, v108, v109\n"
        "v_cvt_pk_bf16_f32 v77, v110, v111\n"
        "v_mfma_f32_16x16x32_bf16 v[112:115], a[72:75], a[236:239], 0\n"
        "s_waitcnt lgkmcnt(4)\n"
        "s_nop 0\n"
        "v_cvt_pk_bf16_f32 v148, v120, v121\n"
        "v_mfma_f32_16x16x32_bf16 v[112:115], a[76:79], a[240:243], v[112:115]\n"
        "s_waitcnt lgkmcnt(3)\n"
        "v_cvt_pk_bf16_f32 v149, v122, v123\n"
        "v_mfma_f32_16x16x32_bf16 v[112:115], a[80:83], a[244:247], v[112:115]\n"
        "s_waitcnt lgkmcnt(2)\n"
        "v_exp_f32 v96, v96\n"
        "v_mfma_f32_16x16x32_bf16 v[112:115], a[84:87], a[248:251], v[112:115]\n"
        "s_waitcnt lgkmcnt(1)\n"
        "v_exp_f32 v97, v97\n"
        "v_mfma_f32_16x16x32_bf16 v[112:115], a[88:91], a[216:219], v[112:115]\n"
        "s_waitcnt lgkmcnt(0)\n"
        "v_exp_f32 v98, v98\n"
        "v_mfma_f32_16x16x32_bf16 v[112:115], a[92:95], a[220:223], v[112:115]\n"
        "v_exp_f32 v99, v99\n"
        "v_mfma_f32_16x16x32_bf16 v[116:119], a[116:119], a[220:223], 0\n"
        "v_cvt_pk_bf16_f32 v84, v96, v97\n"
        "v_cvt_pk_bf16_f32 v85, v98, v99\n"
        "v_mfma_f32_16x16x32_bf16 v[116:119], a[112:115], a[216:219], v[116:119]\n"
        "v_add_u32 v240, s31, v240\n"
        "v_add_u32 v241, s31, v241\n"
        "v_mfma_f32_16x16x32_bf16 v[116:119], a[108:111], a[248:251], v[116:119]\n"
        "v_cvt_pk_bf16_f32 v212, v112, v113\n"
        "v_cvt_pk_bf16_f32 v213, v114, v115\n"
        "v_mfma_f32_16x16x32_bf16 v[116:119], a[104:107], a[244:247], v[116:119]\n"
        "v_mfma_f32_16x16x32_bf16 v[116:119], a[100:103], a[240:243], v[116:119]\n"
        "v_mfma_f32_16x16x32_bf16 v[116:119], a[96:99], a[236:239], v[116:119]\n"
        "s_nop 2\n"
        "v_mfma_f32_16x16x32_bf16 v[120:123], a[120:123], a[236:239], 0\n"
        "s_nop 2\n"
        "v_mfma_f32_16x16x32_bf16 v[120:123], a[124:127], a[240:243], v[120:123]\n"
        "s_add_u32 s47, s47, 6291456\n"
        "v_cvt_pk_bf16_f32 v214, v116, v117\n"
        "v_cvt_pk_bf16_f32 v215, v118, v119\n"
        "v_mfma_f32_16x16x32_bf16 v[120:123], a[128:131], a[244:247], v[120:123]\n"
        "s_add_u32 s36, s36, 2097152\n"
        "v_mfma_f32_16x16x32_bf16 v[120:123], a[132:135], a[248:251], v[120:123]\n"
        ".long 0xbec200ff\n"
        "45:\n"
        "s_branch 61b\n"
        "s_xor_b32 s48, s48, 8192\n"
        "v_mfma_f32_16x16x32_bf16 v[120:123], a[136:139], a[216:219], v[120:123]\n"
        "v_mfma_f32_16x16x32_bf16 v[120:123], a[140:143], a[220:223], v[120:123]\n"
        "s_nop 2\n"
        "v_mfma_f32_16x16x32_bf16 v[112:115], a[220:223], a[164:167], 0\n"
        "s_nop 2\n"
        "v_mfma_f32_16x16x32_bf16 v[112:115], a[216:219], a[160:163], v[112:115]\n"
        "v_cvt_pk_bf16_f32 v230, v120, v121\n"
        "v_cvt_pk_bf16_f32 v231, v122, v123\n"
        "v_mfma_f32_16x16x32_bf16 v[112:115], a[248:251], a[156:159], v[112:115]\n"
        "s_barrier\n"
        "v_mfma_f32_16x16x32_bf16 v[112:115], a[244:247], a[152:155], v[112:115]\n"
        "v_mfma_f32_16x16x32_bf16 v[112:115], a[240:243], a[148:151], v[112:115]\n"
        "v_mfma_f32_16x16x32_bf16 v[112:115], a[236:239], a[144:147], v[112:115]\n"
        "s_nop 2\n"
        "v_mfma_f32_16x16x32_bf16 v[100:103], v[212:215], v[156:159], v[12:15]\n"
        "s_nop 2\n"
        "v_mfma_f32_16x16x32_bf16 v[100:103], v[228:231], v[164:167], v[100:103]\n"
        "v_cvt_pk_bf16_f32 v134, v112, v113\n"
        "v_cvt_pk_bf16_f32 v135, v114, v115\n"
        "v_mfma_f32_16x16x32_bf16 v[116:119], a[236:239], a[168:171], 0\n"
        "v_mfma_f32_16x16x32_bf16 v[116:119], a[240:243], a[172:175], v[116:119]\n"
        "v_mfma_f32_16x16x32_bf16 v[116:119], a[244:247], a[176:179], v[116:119]\n"
        "v_mfma_f32_16x16x32_bf16 v[116:119], a[248:251], a[180:183], v[116:119]\n"
        "v_mfma_f32_16x16x32_bf16 v[116:119], a[216:219], a[184:187], v[116:119]\n"
        "s_nop 0\n"
        "v_exp_f32 v100, v100\n"
        "v_mfma_f32_16x16x32_bf16 v[116:119], a[220:223], a[188:191], v[116:119]\n"
        "v_exp_f32 v101, v101\n"
        "v_mfma_f32_16x16x32_bf16 v[104:107], v[212:215], v[176:179], v[28:31]\n"
        "v_exp_f32 v102, v102\n"
        "v_mfma_f32_16x16x32_bf16 v[104:107], v[228:231], v[184:187], v[104:107]\n"
        "v_exp_f32 v103, v103\n"
        "v_mfma_f32_16x16x32_bf16 v[120:123], a[220:223], a[212:215], 0\n"
        "v_cvt_pk_bf16_f32 v70, v100, v101\n"
        "v_cvt_pk_bf16_f32 v71, v102, v103\n"
        "v_mfma_f32_16x16x32_bf16 v[120:123], a[216:219], a[208:211], v[120:123]\n"
        "v_cvt_pk_bf16_f32 v142, v116, v117\n"
        "v_cvt_pk_bf16_f32 v143, v118, v119\n"
        "v_mfma_f32_16x16x32_bf16 v[120:123], a[248:251], a[204:207], v[120:123]\n"
        "v_mov_b64 v[96:97], 0\n"
        "v_mfma_f32_16x16x32_bf16 v[120:123], a[244:247], a[200:203], v[120:123]\n"
        "v_exp_f32 v104, v104\n"
        "v_mfma_f32_16x16x32_bf16 v[120:123], a[240:243], a[196:199], v[120:123]\n"
        "v_exp_f32 v105, v105\n"
        "v_mfma_f32_16x16x32_bf16 v[120:123], a[236:239], a[192:195], v[120:123]\n"
        "v_exp_f32 v106, v106\n"
        "v_mfma_f32_16x16x32_bf16 v[108:111], v[212:215], v[196:199], v[44:47]\n"
        "v_exp_f32 v107, v107\n"
        "v_mfma_f32_16x16x32_bf16 v[108:111], v[228:231], v[204:207], v[108:111]\n"
        "v_cvt_pk_bf16_f32 v78, v104, v105\n"
        "v_cvt_pk_bf16_f32 v79, v106, v107\n"
        "v_mfma_f32_16x16x32_bf16 v[112:115], a[0:3], a[236:239], 0\n"
        "s_nop 0\n"
        "v_cvt_pk_bf16_f32 v150, v120, v121\n"
        "v_cvt_pk_bf16_f32 v151, v122, v123\n"
        "v_mfma_f32_16x16x32_bf16 v[112:115], a[4:7], a[240:243], v[112:115]\n"
        "v_mfma_f32_16x16x32_bf16 v[112:115], a[8:11], a[244:247], v[112:115]\n"
        "v_exp_f32 v108, v108\n"
        "v_mfma_f32_16x16x32_bf16 v[112:115], a[12:15], a[248:251], v[112:115]\n"
        "v_exp_f32 v109, v109\n"
        "v_mfma_f32_16x16x32_bf16 v[112:115], a[16:19], a[216:219], v[112:115]\n"
        "v_exp_f32 v110, v110\n"
        "v_mfma_f32_16x16x32_bf16 v[112:115], a[20:23], a[220:223], v[112:115]\n"
        "v_exp_f32 v111, v111\n"
        "v_smfmac_f32_16x16x64_bf16 v[96:99], a[252:255], v[64:71], v237\n"
        "v_cvt_pk_bf16_f32 v86, v108, v109\n"
        "v_mfma_f32_16x16x32_bf16 v[100:103], v[132:135], v[68:71], 0\n"
        "v_mov_b64 v[124:125], 0\n"
        "v_cvt_pk_bf16_f32 v87, v110, v111\n"
        "v_mfma_f32_16x16x32_bf16 v[100:103], v[128:131], v[64:67], v[100:103]\n"
        "s_nop 0\n"
        "v_cvt_pk_bf16_f32 v216, v112, v113\n"
        "v_cvt_pk_bf16_f32 v217, v114, v115\n"
        "v_mfma_f32_16x16x32_bf16 v[104:107], v[136:139], v[64:67], 0\n"
        "ds_read_b128 v[222:225], v251 offset:4608\n"
        "v_add_f32 v97, v96, v97\n"
        "v_mfma_f32_16x16x32_bf16 v[104:107], v[140:143], v[68:71], v[104:107]\n"
        "v_rcp_f32 v96, v97\n"
        "v_mfma_f32_16x16x32_bf16 v[108:111], v[148:151], v[68:71], 0\n"
        "v_mul_f32 v100, v96, v100\n"
        "v_mul_f32 v101, v96, v101\n"
        "v_mfma_f32_16x16x32_bf16 v[108:111], v[144:147], v[64:67], v[108:111]\n"
        "v_mul_f32 v102, v96, v102\n"
        "v_mul_f32 v103, v96, v103\n"
        "v_mfma_f32_16x16x32_bf16 v[116:119], a[44:47], a[220:223], 0\n"
        "v_cvt_pk_bf16_f32 v100, v100, v101\n"
        "v_cvt_pk_bf16_f32 v101, v102, v103\n"
        "v_mfma_f32_16x16x32_bf16 v[116:119], a[40:43], a[216:219], v[116:119]\n"
        "v_mul_f32 v104, v96, v104\n"
        "v_mul_f32 v105, v96, v105\n"
        "v_mfma_f32_16x16x32_bf16 v[116:119], a[36:39], a[248:251], v[116:119]\n"
        "v_mul_f32 v106, v96, v106\n"
        "v_mul_f32 v107, v96, v107\n"
        "v_mfma_f32_16x16x32_bf16 v[116:119], a[32:35], a[244:247], v[116:119]\n"
        "v_cvt_pk_bf16_f32 v102, v104, v105\n"
        "v_cvt_pk_bf16_f32 v103, v106, v107\n"
        "v_mfma_f32_16x16x32_bf16 v[116:119], a[28:31], a[240:243], v[116:119]\n"
        "ds_write_b128 v245, v[100:103] offset:0\n"
        "v_mfma_f32_16x16x32_bf16 v[116:119], a[24:27], a[236:239], v[116:119]\n"
        "v_mul_f32 v108, v96, v108\n"
        "v_mul_f32 v109, v96, v109\n"
        "v_smfmac_f32_16x16x64_bf16 v[124:127], a[252:255], v[72:79], v237\n"
        "v_mul_f32 v110, v96, v110\n"
        "v_mfma_f32_16x16x32_bf16 v[64:67], v[132:135], v[76:79], 0\n"
        "v_mul_f32 v111, v96, v111\n"
        "v_cvt_pk_bf16_f32 v108, v108, v109\n"
        "v_mfma_f32_16x16x32_bf16 v[64:67], v[128:131], v[72:75], v[64:67]\n"
        "ds_read_b128 v[156:159], v247 offset:0\n"
        "v_cvt_pk_bf16_f32 v218, v116, v117\n"
        "v_mfma_f32_16x16x32_bf16 v[68:71], v[136:139], v[72:75], 0\n"
        "ds_read_b128 v[160:163], v247 offset:4096\n"
        "v_cvt_pk_bf16_f32 v219, v118, v119\n"
        "v_mfma_f32_16x16x32_bf16 v[68:71], v[140:143], v[76:79], v[68:71]\n"
        "ds_read_b128 v[164:167], v247 offset:4352\n"
        "v_cvt_pk_bf16_f32 v109, v110, v111\n"
        "v_mfma_f32_16x16x32_bf16 v[120:123], v[148:151], v[76:79], 0\n"
        "ds_write_b64 v246, v[108:109] offset:0\n"
        "v_mfma_f32_16x16x32_bf16 v[120:123], v[144:147], v[72:75], v[120:123]\n"
        "v_max_u32 v236, v96, v97\n"
        "v_mov_b64 v[116:117], 0\n"
        "v_mfma_f32_16x16x32_bf16 v[112:115], a[48:51], a[236:239], 0\n"
        "v_add_f32 v125, v124, v125\n"
        "v_mfma_f32_16x16x32_bf16 v[112:115], a[52:55], a[240:243], v[112:115]\n"
        "v_rcp_f32 v124, v125\n"
        "v_mfma_f32_16x16x32_bf16 v[112:115], a[56:59], a[244:247], v[112:115]\n"
        "v_mul_f32 v64, v124, v64\n"
        "v_mul_f32 v65, v124, v65\n"
        "v_mfma_f32_16x16x32_bf16 v[112:115], a[60:63], a[248:251], v[112:115]\n"
        "v_mul_f32 v66, v124, v66\n"
        "v_mul_f32 v67, v124, v67\n"
        "v_mfma_f32_16x16x32_bf16 v[112:115], a[64:67], a[216:219], v[112:115]\n"
        "v_cvt_pk_bf16_f32 v64, v64, v65\n"
        "v_cvt_pk_bf16_f32 v65, v66, v67\n"
        "v_mfma_f32_16x16x32_bf16 v[112:115], a[68:71], a[220:223], v[112:115]\n"
        "ds_read_b128 a[216:219], v248 offset:0\n"
        "v_mul_f32 v68, v124, v68\n"
        "v_smfmac_f32_16x16x64_bf16 v[116:119], a[252:255], v[80:87], v237\n"
        "ds_read_b128 a[220:223], v248 offset:3072\n"
        "v_mfma_f32_16x16x32_bf16 v[72:75], v[132:135], v[84:87], 0\n"
        "v_mul_f32 v69, v124, v69\n"
        "v_mul_f32 v70, v124, v70\n"
        "v_mfma_f32_16x16x32_bf16 v[72:75], v[128:131], v[80:83], v[72:75]\n"
        "v_cvt_pk_bf16_f32 v220, v112, v113\n"
        "v_cvt_pk_bf16_f32 v221, v114, v115\n"
        "v_mfma_f32_16x16x32_bf16 v[76:79], v[136:139], v[80:83], 0\n"
        "ds_read_b128 a[224:227], v254 offset:16\n"
        "v_cvt_pk_bf16_f32 v226, v112, v113\n"
        "v_mfma_f32_16x16x32_bf16 v[76:79], v[140:143], v[84:87], v[76:79]\n"
        "ds_read_b128 a[228:231], v254 offset:3088\n"
        "v_cvt_pk_bf16_f32 v227, v114, v115\n"
        "v_mfma_f32_16x16x32_bf16 v[96:99], v[148:151], v[84:87], 0\n"
        "v_mul_f32 v71, v124, v71\n"
        "v_cvt_pk_bf16_f32 v66, v68, v69\n"
        "v_mfma_f32_16x16x32_bf16 v[96:99], v[144:147], v[80:83], v[96:99]\n"
        "v_cvt_pk_bf16_f32 v67, v70, v71\n"
        "v_mul_f32 v120, v124, v120\n"
        "v_mfma_f32_16x16x32_bf16 v[100:103], v[152:155], v[216:219], v[48:51]\n"
        "s_waitcnt lgkmcnt(9)\n"
        "ds_read_b128 v[152:155], v247 offset:256\n"
        "v_mul_f32 v121, v124, v121\n"
        "v_mfma_f32_16x16x32_bf16 v[100:103], v[168:171], v[220:223], v[100:103]\n"
        "ds_write_b128 v245, v[64:67] offset:256\n"
        "v_mfma_f32_16x16x32_bf16 v[104:107], v[172:175], v[216:219], v[52:55]\n"
        "v_mul_f32 v122, v124, v122\n"
        "v_mul_f32 v123, v124, v123\n"
        "v_mfma_f32_16x16x32_bf16 v[104:107], v[188:191], v[224:227], v[104:107]\n"
        "s_waitcnt lgkmcnt(5)\n"
        "v_cvt_pk_bf16_f32 v120, v120, v121\n"
        "v_cvt_pk_bf16_f32 v121, v122, v123\n"
        "v_mfma_f32_16x16x32_bf16 v[176:179], v[156:159], a[216:219], 0\n"
        "s_waitcnt lgkmcnt(4)\n"
        "ds_write_b64 v246, v[120:121] offset:256\n"
        "v_mfma_f32_16x16x32_bf16 v[176:179], v[160:163], a[220:223], v[176:179]\n"
        "ds_read_b128 v[222:225], v252 offset:4608\n"
        "v_max3_u32 v236, v236, v124, v125\n"
        "v_mfma_f32_16x16x32_bf16 v[196:199], v[164:167], a[220:223], 0\n"
        "s_waitcnt lgkmcnt(3)\n"
        "ds_read_b128 a[232:235], v248 offset:32\n"
        "v_add_f32 v117, v116, v117\n"
        "v_mfma_f32_16x16x32_bf16 v[196:199], v[152:155], a[216:219], v[196:199]\n"
        "ds_read_b128 a[220:223], v248 offset:3104\n"
        "v_mfma_f32_16x16x32_bf16 v[200:203], v[152:155], a[224:227], 0\n"
        "ds_read_b128 a[216:219], v254 offset:48\n"
        "v_mfma_f32_16x16x32_bf16 v[200:203], v[164:167], a[228:231], v[200:203]\n"
        "v_rcp_f32 v116, v117\n"
        "v_mfma_f32_16x16x32_bf16 v[180:183], v[160:163], a[228:231], 0\n"
        "ds_read_b128 a[228:231], v254 offset:3120\n"
        "v_mul_f32 v72, v116, v72\n"
        "v_mfma_f32_16x16x32_bf16 v[180:183], v[156:159], a[224:227], v[180:183]\n"
        "ds_read_b128 a[236:239], v247 offset:8192\n"
        "v_mul_f32 v73, v116, v73\n"
        "v_mfma_f32_16x16x32_bf16 v[108:111], v[192:195], v[216:219], v[56:59]\n"
        "s_waitcnt lgkmcnt(5)\n"
        "ds_read_b128 a[224:227], v248 offset:64\n"
        "v_mul_f32 v74, v116, v74\n"
        "v_mfma_f32_16x16x32_bf16 v[108:111], v[208:211], v[220:223], v[108:111]\n"
        "ds_read_b128 a[240:243], v247 offset:12288\n"
        "v_mul_f32 v75, v116, v75\n"
        "v_mfma_f32_16x16x32_bf16 v[112:115], v[212:215], v[216:219], v[60:63]\n"
        "ds_read_b128 a[244:247], v247 offset:12544\n"
        "v_cvt_pk_bf16_f32 v72, v72, v73\n"
        "v_mfma_f32_16x16x32_bf16 v[112:115], v[228:231], v[224:227], v[112:115]\n"
        "s_waitcnt lgkmcnt(7)\n"
        "ds_read_b128 a[248:251], v247 offset:8448\n"
        "v_cvt_pk_bf16_f32 v73, v74, v75\n"
        "v_mfma_f32_16x16x32_bf16 v[184:187], v[156:159], a[232:235], 0\n"
        "s_waitcnt lgkmcnt(7)\n"
        "v_exp_f32 v100, v100\n"
        "v_mfma_f32_16x16x32_bf16 v[184:187], v[160:163], a[220:223], v[184:187]\n"
        "v_exp_f32 v101, v101\n"
        "v_mfma_f32_16x16x32_bf16 v[204:207], v[164:167], a[220:223], 0\n"
        "ds_read_b128 a[220:223], v248 offset:3136\n"
        "v_mul_f32 v76, v116, v76\n"
        "v_mfma_f32_16x16x32_bf16 v[204:207], v[152:155], a[232:235], v[204:207]\n"
        "s_waitcnt lgkmcnt(7)\n"
        "ds_read_b128 a[232:235], v254 offset:80\n"
        "v_mul_f32 v77, v116, v77\n"
        "v_mfma_f32_16x16x32_bf16 v[192:195], v[152:155], a[216:219], 0\n"
        "s_waitcnt lgkmcnt(7)\n"
        "v_mul_f32 v78, v116, v78\n"
        "v_mul_f32 v79, v116, v79\n"
        "v_mfma_f32_16x16x32_bf16 v[192:195], v[164:167], a[228:231], v[192:195]\n"
        "v_exp_f32 v102, v102\n"
        "v_mfma_f32_16x16x32_bf16 v[172:175], v[160:163], a[228:231], 0\n"
        "ds_read_b128 a[228:231], v254 offset:3152\n"
        "v_cvt_pk_bf16_f32 v88, v100, v101\n"
        "v_mfma_f32_16x16x32_bf16 v[172:175], v[156:159], a[216:219], v[172:175]\n"
        "s_waitcnt lgkmcnt(6)\n"
        "ds_read_b128 a[216:219], v248 offset:6144\n"
        "v_cvt_pk_bf16_f32 v74, v76, v77\n"
        "v_mfma_f32_16x16x32_bf16 v[212:215], v[156:159], a[224:227], 0\n"
        "s_waitcnt lgkmcnt(3)\n"
        "v_exp_f32 v104, v104\n"
        "v_mfma_f32_16x16x32_bf16 v[212:215], v[160:163], a[220:223], v[212:215]\n"
        "v_exp_f32 v103, v103\n"
        "v_mfma_f32_16x16x32_bf16 v[216:219], v[164:167], a[220:223], 0\n"
        "ds_read_b128 a[220:223], v248 offset:9216\n"
        "v_cvt_pk_bf16_f32 v75, v78, v79\n"
        "v_mfma_f32_16x16x32_bf16 v[216:219], v[152:155], a[224:227], v[216:219]\n"
        "s_waitcnt lgkmcnt(3)\n"
        "ds_write_b128 v245, v[72:75] offset:512\n"
        "v_mfma_f32_16x16x32_bf16 v[224:227], v[152:155], a[232:235], 0\n"
        "s_waitcnt lgkmcnt(3)\n"
        "v_exp_f32 v105, v105\n"
        "v_mfma_f32_16x16x32_bf16 v[224:227], v[164:167], a[228:231], v[224:227]\n"
        "v_cvt_pk_bf16_f32 v89, v102, v103\n"
        "v_cvt_pk_bf16_f32 v90, v104, v105\n"
        "v_mfma_f32_16x16x32_bf16 v[220:223], v[160:163], a[228:231], 0\n"
        "ds_read_b128 a[224:227], v254 offset:6160\n"
        "ds_read_b128 a[228:231], v254 offset:9232\n"
        "v_mfma_f32_16x16x32_bf16 v[220:223], v[156:159], a[232:235], v[220:223]\n"
        "s_waitcnt lgkmcnt(4)\n"
        "ds_read_b128 a[232:235], v248 offset:6176\n"
        "v_mul_f32 v96, v116, v96\n"
        "v_mfma_f32_16x16x32_bf16 v[176:179], a[236:239], a[216:219], v[176:179]\n"
        "s_waitcnt lgkmcnt(4)\n"
        "v_exp_f32 v106, v106\n"
        "v_mfma_f32_16x16x32_bf16 v[176:179], a[240:243], a[220:223], v[176:179]\n"
        "v_mul_f32 v97, v116, v97\n"
        "v_mul_f32 v98, v116, v98\n"
        "v_mfma_f32_16x16x32_bf16 v[196:199], a[244:247], a[220:223], v[196:199]\n"
        "ds_read_b128 a[220:223], v248 offset:9248\n"
        "v_mul_f32 v99, v116, v99\n"
        "v_mfma_f32_16x16x32_bf16 v[196:199], a[248:251], a[216:219], v[196:199]\n"
        "s_waitcnt lgkmcnt(3)\n"
        "ds_read_b128 a[216:219], v254 offset:6192\n"
        "v_cvt_pk_bf16_f32 v96, v96, v97\n"
        "v_mfma_f32_16x16x32_bf16 v[200:203], a[248:251], a[224:227], v[200:203]\n"
        "s_waitcnt lgkmcnt(3)\n"
        "ds_read_b128 v[156:159], v247 offset:16384\n"
        "v_cvt_pk_bf16_f32 v97, v98, v99\n"
        "v_mfma_f32_16x16x32_bf16 v[200:203], a[244:247], a[228:231], v[200:203]\n"
        "ds_write_b64 v246, v[96:97] offset:512\n"
        "v_mfma_f32_16x16x32_bf16 v[180:183], a[240:243], a[228:231], v[180:183]\n"
        "ds_read_b128 a[228:231], v254 offset:9264\n"
        "v_max3_u32 v236, v236, v116, v117\n"
        "v_mfma_f32_16x16x32_bf16 v[180:183], a[236:239], a[224:227], v[180:183]\n"
        "s_waitcnt lgkmcnt(5)\n"
        "ds_read_b128 a[224:227], v248 offset:6208\n"
        "v_mfma_f32_16x16x32_bf16 v[184:187], a[236:239], a[232:235], v[184:187]\n"
        "s_waitcnt lgkmcnt(5)\n"
        "ds_read_b128 v[160:163], v247 offset:20480\n"
        "v_mfma_f32_16x16x32_bf16 v[184:187], a[240:243], a[220:223], v[184:187]\n"
        "ds_read_b128 v[164:167], v247 offset:20736\n"
        "v_mfma_f32_16x16x32_bf16 v[204:207], a[244:247], a[220:223], v[204:207]\n"
        "ds_read_b128 a[220:223], v248 offset:9280\n"
        "v_mfma_f32_16x16x32_bf16 v[204:207], a[248:251], a[232:235], v[204:207]\n"
        "s_waitcnt lgkmcnt(7)\n"
        "ds_read_b128 a[232:235], v254 offset:6224\n"
        "v_mfma_f32_16x16x32_bf16 v[192:195], a[248:251], a[216:219], v[192:195]\n"
        "s_waitcnt lgkmcnt(5)\n"
        "ds_read_b128 v[152:155], v247 offset:16640\n"
        "v_mfma_f32_16x16x32_bf16 v[192:195], a[244:247], a[228:231], v[192:195]\n"
        "v_exp_f32 v107, v107\n"
        "v_mfma_f32_16x16x32_bf16 v[172:175], a[240:243], a[228:231], v[172:175]\n"
        "ds_read_b128 a[228:231], v254 offset:9296\n"
        "v_cvt_pk_bf16_f32 v91, v106, v107\n"
        "v_mfma_f32_16x16x32_bf16 v[172:175], a[236:239], a[216:219], v[172:175]\n"
        "s_waitcnt lgkmcnt(6)\n"
        "ds_read_b128 a[216:219], v248 offset:12288\n"
        "v_mfma_f32_16x16x32_bf16 v[212:215], a[236:239], a[224:227], v[212:215]\n"
        "s_waitcnt lgkmcnt(4)\n"
        "v_exp_f32 v108, v108\n"
        "v_mfma_f32_16x16x32_bf16 v[212:215], a[240:243], a[220:223], v[212:215]\n"
        "v_exp_f32 v109, v109\n"
        "v_mfma_f32_16x16x32_bf16 v[216:219], a[244:247], a[220:223], v[216:219]\n"
        "ds_read_b128 a[220:223], v248 offset:15360\n"
        "v_cvt_pk_bf16_f32 v92, v108, v109\n"
        "v_mfma_f32_16x16x32_bf16 v[216:219], a[248:251], a[224:227], v[216:219]\n"
        "s_waitcnt lgkmcnt(4)\n"
        "ds_read_b128 a[224:227], v254 offset:12304\n"
        "v_mfma_f32_16x16x32_bf16 v[224:227], a[248:251], a[232:235], v[224:227]\n"
        "s_waitcnt lgkmcnt(3)\n"
        "v_exp_f32 v110, v110\n"
        "v_mfma_f32_16x16x32_bf16 v[224:227], a[244:247], a[228:231], v[224:227]\n"
        "v_exp_f32 v112, v112\n"
        "v_mfma_f32_16x16x32_bf16 v[220:223], a[240:243], a[228:231], v[220:223]\n"
        "ds_read_b128 a[228:231], v254 offset:15376\n"
        "v_mfma_f32_16x16x32_bf16 v[220:223], a[236:239], a[232:235], v[220:223]\n"
        "s_waitcnt lgkmcnt(3)\n"
        "ds_read_b128 a[232:235], v248 offset:12320\n"
        "v_mfma_f32_16x16x32_bf16 v[176:179], v[156:159], a[216:219], v[176:179]\n"
        "s_waitcnt lgkmcnt(3)\n"
        "v_exp_f32 v111, v111\n"
        "v_mfma_f32_16x16x32_bf16 v[176:179], v[160:163], a[220:223], v[176:179]\n"
        "v_exp_f32 v113, v113\n"
        "v_mfma_f32_16x16x32_bf16 v[196:199], v[164:167], a[220:223], v[196:199]\n"
        "ds_read_b128 a[220:223], v248 offset:15392\n"
        "v_cvt_pk_bf16_f32 v93, v110, v111\n"
        "v_mfma_f32_16x16x32_bf16 v[196:199], v[152:155], a[216:219], v[196:199]\n"
        "s_waitcnt lgkmcnt(3)\n"
        "ds_read_b128 a[216:219], v254 offset:12336\n"
        "v_cvt_pk_bf16_f32 v94, v112, v113\n"
        "v_mfma_f32_16x16x32_bf16 v[200:203], v[152:155], a[224:227], v[200:203]\n"
        "s_waitcnt lgkmcnt(3)\n"
        "v_exp_f32 v114, v114\n"
        "v_mfma_f32_16x16x32_bf16 v[200:203], v[164:167], a[228:231], v[200:203]\n"
        "v_exp_f32 v115, v115\n"
        "v_mfma_f32_16x16x32_bf16 v[180:183], v[160:163], a[228:231], v[180:183]\n"
        "s_waitcnt vmcnt(0)\n"
        "s_barrier\n"
        "ds_read_b128 a[228:231], v254 offset:15408\n"
        "v_cvt_pk_bf16_f32 v95, v114, v115\n"
        "v_mfma_f32_16x16x32_bf16 v[180:183], v[156:159], a[224:227], v[180:183]\n"
        "s_waitcnt lgkmcnt(3)\n"
        "ds_read_b128 a[224:227], v248 offset:12352\n"
        "v_mfma_f32_16x16x32_bf16 v[184:187], v[156:159], a[232:235], v[184:187]\n"
        "s_waitcnt lgkmcnt(3)\n"
        "v_mfma_f32_16x16x32_bf16 v[184:187], v[160:163], a[220:223], v[184:187]\n"
        "v_mfma_f32_16x16x32_bf16 v[204:207], v[164:167], a[220:223], v[204:207]\n"
        "ds_read_b128 a[220:223], v248 offset:15424\n"
        "v_mfma_f32_16x16x32_bf16 v[204:207], v[152:155], a[232:235], v[204:207]\n"
        "s_waitcnt lgkmcnt(3)\n"
        "ds_read_b128 a[232:235], v254 offset:12368\n"
        "v_mfma_f32_16x16x32_bf16 v[192:195], v[152:155], a[216:219], v[192:195]\n"
        "s_waitcnt lgkmcnt(3)\n"
        "ds_read_b128 a[236:239], v240 offset:0\n"
        "v_mfma_f32_16x16x32_bf16 v[192:195], v[164:167], a[228:231], v[192:195]\n"
        "ds_read_b128 a[240:243], v241 offset:0\n"
        "v_mfma_f32_16x16x32_bf16 v[172:175], v[160:163], a[228:231], v[172:175]\n"
        "ds_read_b128 a[228:231], v254 offset:15440\n"
        "v_mfma_f32_16x16x32_bf16 v[172:175], v[156:159], a[216:219], v[172:175]\n"
        "s_waitcnt lgkmcnt(5)\n"
        "ds_read_b128 a[244:247], v240 offset:128\n"
        "v_mfma_f32_16x16x32_bf16 v[212:215], v[156:159], a[224:227], v[212:215]\n"
        "s_waitcnt lgkmcnt(5)\n"
        "ds_read_b128 a[248:251], v241 offset:128\n"
        "v_mfma_f32_16x16x32_bf16 v[212:215], v[160:163], a[220:223], v[212:215]\n"
        "ds_read_b128 a[216:219], v240 offset:256\n"
        "v_mfma_f32_16x16x32_bf16 v[216:219], v[164:167], a[220:223], v[216:219]\n"
        "ds_read_b128 a[220:223], v241 offset:256\n"
        "v_mfma_f32_16x16x32_bf16 v[216:219], v[152:155], a[224:227], v[216:219]\n"
        "s_waitcnt lgkmcnt(7)\n"
        "v_mfma_f32_16x16x32_bf16 v[224:227], v[152:155], a[232:235], v[224:227]\n"
        "s_waitcnt lgkmcnt(4)\n"
        "v_mfma_f32_16x16x32_bf16 v[224:227], v[164:167], a[228:231], v[224:227]\n"
        "s_sub_u32 s27, s27, 1\n"
        "v_mfma_f32_16x16x32_bf16 v[220:223], v[160:163], a[228:231], v[220:223]\n"
        "s_cmp_lg_u32 s27, 0\n"
        "v_mfma_f32_16x16x32_bf16 v[220:223], v[156:159], a[232:235], v[220:223]\n"
        "s_cbranch_scc1 10b\n"
        "s_branch 19f\n"
        ".p2align 12\n"
        "51:\n"
        "s_branch 61b\n"
        "s_nop 0\n"
        "s_nop 0\n"
        "s_nop 0\n"
        "12:\n"
        "10:\n"
        "v_mfma_f32_16x16x32_bf16 v[112:115], a[72:75], a[236:239], 0\n"
        "v_mov_b64 v[124:125], 0\n"
        "v_mfma_f32_16x16x32_bf16 v[112:115], a[76:79], a[240:243], v[112:115]\n"
        "s_waitcnt lgkmcnt(3)\n"
        "s_add_u32 s41, s30, s40\n"
        "v_cvt_pk_bf16_f32 v232, v176, v180\n"
        "v_cvt_pk_bf16_f32 v233, v184, v172\n"
        "v_mfma_f32_16x16x32_bf16 v[112:115], a[80:83], a[244:247], v[112:115]\n"
        "s_waitcnt lgkmcnt(2)\n"
        "v_cvt_pk_bf16_f32 v234, v212, v220\n"
        "v_mfma_f32_16x16x32_bf16 v[112:115], a[84:87], a[248:251], v[112:115]\n"
        "s_waitcnt lgkmcnt(1)\n"
        "buffer_store_dwordx3 v[232:234], v250, s[16:19], s38 offen offset:0 sc1\n"
        "v_mfma_f32_16x16x32_bf16 v[112:115], a[88:91], a[216:219], v[112:115]\n"
        "s_waitcnt lgkmcnt(0)\n"
        "s_nop 0\n"
        "v_mfma_f32_16x16x32_bf16 v[112:115], a[92:95], a[220:223], v[112:115]\n"
        "v_cvt_pk_bf16_f32 v232, v177, v181\n"
        "v_cvt_pk_bf16_f32 v233, v185, v173\n"
        "v_smfmac_f32_16x16x64_bf16 v[124:127], a[252:255], v[88:95], v237\n"
        "v_cvt_pk_bf16_f32 v234, v213, v221\n"
        "v_mfma_f32_16x16x32_bf16 v[80:83], v[132:135], v[92:95], 0\n"
        "s_nop 2\n"
        "v_mfma_f32_16x16x32_bf16 v[80:83], v[128:131], v[88:91], v[80:83]\n"
        "v_cvt_pk_bf16_f32 v152, v112, v113\n"
        "v_cvt_pk_bf16_f32 v153, v114, v115\n"
        "v_mfma_f32_16x16x32_bf16 v[84:87], v[136:139], v[88:91], 0\n"
        "v_add_f32 v125, v124, v125\n"
        "v_mfma_f32_16x16x32_bf16 v[84:87], v[140:143], v[92:95], v[84:87]\n"
        "v_rcp_f32 v124, v125\n"
        "v_mfma_f32_16x16x32_bf16 v[96:99], v[148:151], v[92:95], 0\n"
        "buffer_store_dwordx3 v[232:234], v250, s[16:19], s38 offen offset:384 sc1\n"
        "v_mfma_f32_16x16x32_bf16 v[96:99], v[144:147], v[88:91], v[96:99]\n"
        "s_add_u32 s35, s47, 786432\n"
        "v_mul_f32 v80, v124, v80\n"
        "v_mul_f32 v81, v124, v81\n"
        "v_mfma_f32_16x16x32_bf16 v[116:119], a[116:119], a[220:223], 0\n"
        "s_sub_u32 s46, s35, 100663296\n"
        "v_mul_f32 v82, v124, v82\n"
        "v_mul_f32 v83, v124, v83\n"
        "v_mfma_f32_16x16x32_bf16 v[116:119], a[112:115], a[216:219], v[116:119]\n"
        "s_cmp_lt_u32 s46, 786432\n"
        "v_cvt_pk_bf16_f32 v80, v80, v81\n"
        "v_cvt_pk_bf16_f32 v81, v82, v83\n"
        "v_mfma_f32_16x16x32_bf16 v[116:119], a[108:111], a[248:251], v[116:119]\n"
        "s_cselect_b32 s35, s46, s35\n"
        "v_mul_f32 v84, v124, v84\n"
        "v_mul_f32 v85, v124, v85\n"
        "v_mfma_f32_16x16x32_bf16 v[116:119], a[104:107], a[244:247], v[116:119]\n"
        "v_mul_f32 v86, v124, v86\n"
        "v_mul_f32 v87, v124, v87\n"
        "v_mfma_f32_16x16x32_bf16 v[116:119], a[100:103], a[240:243], v[116:119]\n"
        "v_cvt_pk_bf16_f32 v232, v178, v182\n"
        "v_cvt_pk_bf16_f32 v233, v186, v174\n"
        "v_mfma_f32_16x16x32_bf16 v[116:119], a[96:99], a[236:239], v[116:119]\n"
        "v_cvt_pk_bf16_f32 v234, v214, v222\n"
        "v_cvt_pk_bf16_f32 v82, v84, v85\n"
        "v_mfma_f32_16x16x32_bf16 v[120:123], a[120:123], a[236:239], 0\n"
        "v_cvt_pk_bf16_f32 v83, v86, v87\n"
        "v_mul_f32 v96, v124, v96\n"
        "v_mfma_f32_16x16x32_bf16 v[120:123], a[124:127], a[240:243], v[120:123]\n"
        "buffer_store_dwordx3 v[232:234], v250, s[16:19], s38 offen offset:768 sc1\n"
        "v_mfma_f32_16x16x32_bf16 v[120:123], a[128:131], a[244:247], v[120:123]\n"
        "v_mul_f32 v97, v124, v97\n"
        "v_mul_f32 v98, v124, v98\n"
        "v_mfma_f32_16x16x32_bf16 v[120:123], a[132:135], a[248:251], v[120:123]\n"
        "v_mul_f32 v99, v124, v99\n"
        "v_cvt_pk_bf16_f32 v154, v116, v117\n"
        "v_mfma_f32_16x16x32_bf16 v[120:123], a[136:139], a[216:219], v[120:123]\n"
        "ds_write_b128 v245, v[80:83] offset:768\n"
        "v_mfma_f32_16x16x32_bf16 v[120:123], a[140:143], a[220:223], v[120:123]\n"
        "v_cvt_pk_bf16_f32 v155, v118, v119\n"
        "v_cvt_pk_bf16_f32 v96, v96, v97\n"
        "v_mfma_f32_16x16x32_bf16 v[112:115], a[20:23], a[220:223], 0\n"
        "v_cvt_pk_bf16_f32 v97, v98, v99\n"
        "v_max3_u32 v236, v236, v124, v125\n"
        "v_mfma_f32_16x16x32_bf16 v[112:115], a[16:19], a[216:219], v[112:115]\n"
        "v_cmp_lt_u32 vcc, s24, v236\n"
        "v_cvt_pk_bf16_f32 v232, v179, v183\n"
        "v_mfma_f32_16x16x32_bf16 v[112:115], a[12:15], a[248:251], v[112:115]\n"
        "ds_write_b64 v246, v[96:97] offset:768\n"
        "s_cmp_lg_u64 vcc, 0\n"
        "s_addc_u32 s25, s25, s25\n"
        "v_mfma_f32_16x16x32_bf16 v[112:115], a[8:11], a[244:247], v[112:115]\n"
        "v_cvt_pk_bf16_f32 v233, v187, v175\n"
        "v_cvt_pk_bf16_f32 v234, v215, v223\n"
        "v_mfma_f32_16x16x32_bf16 v[112:115], a[4:7], a[240:243], v[112:115]\n"
        "v_cvt_pk_bf16_f32 v168, v120, v121\n"
        "v_cvt_pk_bf16_f32 v169, v122, v123\n"
        "v_mfma_f32_16x16x32_bf16 v[112:115], a[0:3], a[236:239], v[112:115]\n"
        "buffer_store_dwordx3 v[232:234], v250, s[16:19], s38 offen offset:1152 sc1\n"
        "v_mfma_f32_16x16x32_bf16 v[116:119], a[24:27], a[236:239], 0\n"
        "v_xor_b32 v251, 0x2000, v251\n"
        "v_xor_b32 v252, 0x2000, v252\n"
        "v_mfma_f32_16x16x32_bf16 v[116:119], a[28:31], a[240:243], v[116:119]\n"
        "v_cvt_pk_bf16_f32 v232, v196, v200\n"
        "v_cvt_pk_bf16_f32 v233, v204, v192\n"
        "v_mfma_f32_16x16x32_bf16 v[116:119], a[32:35], a[244:247], v[116:119]\n"
        "v_cvt_pk_bf16_f32 v156, v112, v113\n"
        "v_cvt_pk_bf16_f32 v157, v114, v115\n"
        "v_mfma_f32_16x16x32_bf16 v[116:119], a[36:39], a[248:251], v[116:119]\n"
        "v_cvt_pk_bf16_f32 v234, v216, v224\n"
        "v_mfma_f32_16x16x32_bf16 v[116:119], a[40:43], a[216:219], v[116:119]\n"
        "v_mfma_f32_16x16x32_bf16 v[116:119], a[44:47], a[220:223], v[116:119]\n"
        "s_nop 2\n"
        "v_mfma_f32_16x16x32_bf16 v[120:123], a[68:71], a[220:223], 0\n"
        "s_nop 2\n"
        "v_mfma_f32_16x16x32_bf16 v[120:123], a[64:67], a[216:219], v[120:123]\n"
        "buffer_store_dwordx3 v[232:234], v250, s[16:19], s39 offen offset:0 sc1\n"
        "v_mfma_f32_16x16x32_bf16 v[120:123], a[60:63], a[248:251], v[120:123]\n"
        "ds_read_b128 a[224:227], v240 offset:1536\n"
        "v_cvt_pk_bf16_f32 v158, v116, v117\n"
        "v_mfma_f32_16x16x32_bf16 v[120:123], a[56:59], a[244:247], v[120:123]\n"
        "ds_read_b128 a[228:231], v241 offset:1536\n"
        "v_cvt_pk_bf16_f32 v159, v118, v119\n"
        "v_mfma_f32_16x16x32_bf16 v[120:123], a[52:55], a[240:243], v[120:123]\n"
        "ds_read_b128 a[232:235], v240 offset:1664\n"
        "v_cvt_pk_bf16_f32 v232, v197, v201\n"
        "v_mfma_f32_16x16x32_bf16 v[120:123], a[48:51], a[236:239], v[120:123]\n"
        "v_cvt_pk_bf16_f32 v233, v205, v193\n"
        "v_cvt_pk_bf16_f32 v234, v217, v225\n"
        "v_mfma_f32_16x16x32_bf16 v[112:115], a[236:239], a[144:147], 0\n"
        "v_add_u32 v247, s45, v247\n"
        "s_nop 0\n"
        "v_mfma_f32_16x16x32_bf16 v[112:115], a[240:243], a[148:151], v[112:115]\n"
        "s_nop 1\n"
        "v_cvt_pk_bf16_f32 v160, v120, v121\n"
        "v_mfma_f32_16x16x32_bf16 v[112:115], a[244:247], a[152:155], v[112:115]\n"
        "v_cvt_pk_bf16_f32 v161, v122, v123\n"
        "v_cvt_pk_bf16_f32 v166, v120, v121\n"
        "v_mfma_f32_16x16x32_bf16 v[112:115], a[248:251], a[156:159], v[112:115]\n"
        "v_cvt_pk_bf16_f32 v167, v122, v123\n"
        "v_mfma_f32_16x16x32_bf16 v[112:115], a[216:219], a[160:163], v[112:115]\n"
        "buffer_store_dwordx3 v[232:234], v250, s[16:19], s39 offen offset:384 sc1\n"
        "v_mfma_f32_16x16x32_bf16 v[112:115], a[220:223], a[164:167], v[112:115]\n"
        "s_nop 2\n"
        "v_mfma_f32_16x16x32_bf16 v[116:119], a[220:223], a[188:191], 0\n"
        "s_nop 2\n"
        "v_mfma_f32_16x16x32_bf16 v[116:119], a[216:219], a[184:187], v[116:119]\n"
        "v_cvt_pk_bf16_f32 v128, v112, v113\n"
        "v_cvt_pk_bf16_f32 v129, v114, v115\n"
        "v_mfma_f32_16x16x32_bf16 v[116:119], a[248:251], a[180:183], v[116:119]\n"
        "v_cvt_pk_bf16_f32 v232, v198, v202\n"
        "v_cvt_pk_bf16_f32 v233, v206, v194\n"
        "v_mfma_f32_16x16x32_bf16 v[116:119], a[244:247], a[176:179], v[116:119]\n"
        "v_cvt_pk_bf16_f32 v234, v218, v226\n"
        "v_mfma_f32_16x16x32_bf16 v[116:119], a[240:243], a[172:175], v[116:119]\n"
        "v_mfma_f32_16x16x32_bf16 v[116:119], a[236:239], a[168:171], v[116:119]\n"
        "s_nop 2\n"
        "v_mfma_f32_16x16x32_bf16 v[120:123], a[236:239], a[192:195], 0\n"
        "ds_read_b128 a[236:239], v241 offset:1664\n"
        "s_nop 0\n"
        "v_mfma_f32_16x16x32_bf16 v[120:123], a[240:243], a[196:199], v[120:123]\n"
        "ds_read_b128 a[240:243], v240 offset:1792\n"
        "v_cvt_pk_bf16_f32 v136, v116, v117\n"
        "v_mfma_f32_16x16x32_bf16 v[120:123], a[244:247], a[200:203], v[120:123]\n"
        "ds_read_b128 a[244:247], v241 offset:1792\n"
        "v_cvt_pk_bf16_f32 v137, v118, v119\n"
        "v_mfma_f32_16x16x32_bf16 v[120:123], a[248:251], a[204:207], v[120:123]\n"
        "buffer_store_dwordx3 v[232:234], v250, s[16:19], s39 offen offset:768 sc1\n"
        "v_mfma_f32_16x16x32_bf16 v[120:123], a[216:219], a[208:211], v[120:123]\n"
        "s_nop 0\n"
        "v_cvt_pk_bf16_f32 v232, v199, v203\n"
        "v_cvt_pk_bf16_f32 v233, v207, v195\n"
        "v_mfma_f32_16x16x32_bf16 v[120:123], a[220:223], a[212:215], v[120:123]\n"
        "s_waitcnt lgkmcnt(5)\n"
        "v_cvt_pk_bf16_f32 v234, v219, v227\n"
        "s_nop 0\n"
        "v_mfma_f32_16x16x32_bf16 v[112:115], a[72:75], a[224:227], 0\n"
        "s_waitcnt lgkmcnt(4)\n"
        "s_nop 1\n"
        "v_mfma_f32_16x16x32_bf16 v[112:115], a[76:79], a[228:231], v[112:115]\n"
        "s_waitcnt lgkmcnt(3)\n"
        "s_nop 1\n"
        "v_mfma_f32_16x16x32_bf16 v[112:115], a[80:83], a[232:235], v[112:115]\n"
        "s_waitcnt lgkmcnt(2)\n"
        "v_cvt_pk_bf16_f32 v144, v120, v121\n"
        "v_cvt_pk_bf16_f32 v145, v122, v123\n"
        "v_mfma_f32_16x16x32_bf16 v[112:115], a[84:87], a[236:239], v[112:115]\n"
        "s_waitcnt lgkmcnt(1)\n"
        "v_mfma_f32_16x16x32_bf16 v[112:115], a[88:91], a[240:243], v[112:115]\n"
        "s_waitcnt lgkmcnt(0)\n"
        "buffer_store_dwordx3 v[232:234], v250, s[16:19], s39 offen offset:1152 sc1\n"
        "v_mfma_f32_16x16x32_bf16 v[112:115], a[92:95], a[244:247], v[112:115]\n"
        "s_nop 2\n"
        "v_mfma_f32_16x16x32_bf16 v[116:119], a[116:119], a[244:247], 0\n"
        "s_nop 2\n"
        "v_mfma_f32_16x16x32_bf16 v[116:119], a[112:115], a[240:243], v[116:119]\n"
        "v_cvt_pk_bf16_f32 v172, v112, v113\n"
        "v_cvt_pk_bf16_f32 v173, v114, v115\n"
        "v_mfma_f32_16x16x32_bf16 v[116:119], a[108:111], a[236:239], v[116:119]\n"
        "v_mfma_f32_16x16x32_bf16 v[116:119], a[104:107], a[232:235], v[116:119]\n"
        "v_mfma_f32_16x16x32_bf16 v[116:119], a[100:103], a[228:231], v[116:119]\n"
        "v_mfma_f32_16x16x32_bf16 v[116:119], a[96:99], a[224:227], v[116:119]\n"
        "s_mov_b32 m0, s48\n"
        "s_nop 1\n"
        "v_mfma_f32_16x16x32_bf16 v[120:123], a[120:123], a[224:227], 0\n"
        "ds_read_b128 v[162:165], v251 offset:0\n"
        "s_nop 0\n"
        "v_mfma_f32_16x16x32_bf16 v[120:123], a[124:127], a[228:231], v[120:123]\n"
        "buffer_load_dwordx4 v249, s[12:15], s36 offen offset:0 lds\n"
        "v_mfma_f32_16x16x32_bf16 v[120:123], a[128:131], a[232:235], v[120:123]\n"
        "v_cvt_pk_bf16_f32 v174, v116, v117\n"
        "v_cvt_pk_bf16_f32 v175, v118, v119\n"
        "v_mfma_f32_16x16x32_bf16 v[120:123], a[132:135], a[236:239], v[120:123]\n"
        "v_mfma_f32_16x16x32_bf16 v[120:123], a[136:139], a[240:243], v[120:123]\n"
        "v_mfma_f32_16x16x32_bf16 v[120:123], a[140:143], a[244:247], v[120:123]\n"
        "s_nop 2\n"
        "v_mfma_f32_16x16x32_bf16 v[112:115], a[20:23], a[244:247], 0\n"
        "s_nop 2\n"
        "v_mfma_f32_16x16x32_bf16 v[112:115], a[16:19], a[240:243], v[112:115]\n"
        "v_cvt_pk_bf16_f32 v190, v120, v121\n"
        "v_cvt_pk_bf16_f32 v191, v122, v123\n"
        "v_mfma_f32_16x16x32_bf16 v[112:115], a[12:15], a[236:239], v[112:115]\n"
        "v_mfma_f32_16x16x32_bf16 v[112:115], a[8:11], a[232:235], v[112:115]\n"
        "v_mfma_f32_16x16x32_bf16 v[112:115], a[4:7], a[228:231], v[112:115]\n"
        "ds_read_b128 v[182:185], v251 offset:512\n"
        "v_mfma_f32_16x16x32_bf16 v[112:115], a[0:3], a[224:227], v[112:115]\n"
        "s_nop 2\n"
        "v_mfma_f32_16x16x32_bf16 v[116:119], a[24:27], a[224:227], 0\n"
        "s_nop 2\n"
        "v_mfma_f32_16x16x32_bf16 v[116:119], a[28:31], a[228:231], v[116:119]\n"
        "v_cvt_pk_bf16_f32 v176, v112, v113\n"
        "v_cvt_pk_bf16_f32 v177, v114, v115\n"
        "v_mfma_f32_16x16x32_bf16 v[116:119], a[32:35], a[232:235], v[116:119]\n"
        "v_mfma_f32_16x16x32_bf16 v[116:119], a[36:39], a[236:239], v[116:119]\n"
        "buffer_load_dwordx4 v253, s[12:15], s36 offen offset:1024 lds\n"
        "v_mfma_f32_16x16x32_bf16 v[116:119], a[40:43], a[240:243], v[116:119]\n"
        "v_mfma_f32_16x16x32_bf16 v[116:119], a[44:47], a[244:247], v[116:119]\n"
        "s_nop 2\n"
        "v_mfma_f32_16x16x32_bf16 v[120:123], a[68:71], a[244:247], 0\n"
        "s_nop 2\n"
        "v_mfma_f32_16x16x32_bf16 v[120:123], a[64:67], a[240:243], v[120:123]\n"
        "v_cvt_pk_bf16_f32 v178, v116, v117\n"
        "v_cvt_pk_bf16_f32 v179, v118, v119\n"
        "v_mfma_f32_16x16x32_bf16 v[120:123], a[60:63], a[236:239], v[120:123]\n"
        "v_mfma_f32_16x16x32_bf16 v[120:123], a[56:59], a[232:235], v[120:123]\n"
        "v_mfma_f32_16x16x32_bf16 v[120:123], a[52:55], a[228:231], v[120:123]\n"
        "v_mfma_f32_16x16x32_bf16 v[120:123], a[48:51], a[224:227], v[120:123]\n"
        "s_nop 2\n"
        "v_mfma_f32_16x16x32_bf16 v[112:115], a[224:227], a[144:147], 0\n"
        "s_nop 2\n"
        "v_mfma_f32_16x16x32_bf16 v[112:115], a[228:231], a[148:151], v[112:115]\n"
        "v_cvt_pk_bf16_f32 v180, v120, v121\n"
        "v_cvt_pk_bf16_f32 v181, v122, v123\n"
        "v_mfma_f32_16x16x32_bf16 v[112:115], a[232:235], a[152:155], v[112:115]\n"
        "v_cvt_pk_bf16_f32 v186, v120, v121\n"
        "v_cvt_pk_bf16_f32 v187, v122, v123\n"
        "v_mfma_f32_16x16x32_bf16 v[112:115], a[236:239], a[156:159], v[112:115]\n"
        "s_add_u32 m0, s41, 0\n"
        "v_mfma_f32_16x16x32_bf16 v[112:115], a[240:243], a[160:163], v[112:115]\n"
        "ds_read_b128 a[248:251], v240 offset:12288\n"
        "v_mfma_f32_16x16x32_bf16 v[112:115], a[244:247], a[164:167], v[112:115]\n"
        "buffer_load_dwordx4 v242, s[8:11], s47 offen lds\n"
        "v_mfma_f32_16x16x32_bf16 v[96:99], v[152:155], v[156:159], v[0:3]\n"
        "s_waitcnt lgkmcnt(2)\n"
        "ds_read_b128 a[216:219], v241 offset:12288\n"
        "s_nop 0\n"
        "v_mfma_f32_16x16x32_bf16 v[96:99], v[168:171], v[160:163], v[96:99]\n"
        "ds_read_b128 a[220:223], v240 offset:12416\n"
        "s_sub_u32 s31, s30, s28\n"
        "s_nop 0\n"
        "v_mfma_f32_16x16x32_bf16 v[100:103], v[172:175], v[156:159], v[4:7]\n"
        "s_sub_u32 s45, s28, s29\n"
        "v_cvt_pk_bf16_f32 v130, v112, v113\n"
        "v_cvt_pk_bf16_f32 v131, v114, v115\n"
        "v_mfma_f32_16x16x32_bf16 v[100:103], v[188:191], v[164:167], v[100:103]\n"
        "s_add_u32 s28, s28, s31\n"
        "ds_read_b128 v[162:165], v252 offset:0\n"
        "v_mfma_f32_16x16x32_bf16 v[116:119], a[244:247], a[188:191], 0\n"
        "s_mov_b32 s30, s29\n"
        "v_exp_f32 v96, v96\n"
        "v_mfma_f32_16x16x32_bf16 v[116:119], a[240:243], a[184:187], v[116:119]\n"
        "s_add_u32 s29, s29, s45\n"
        "v_exp_f32 v97, v97\n"
        "v_mfma_f32_16x16x32_bf16 v[116:119], a[236:239], a[180:183], v[116:119]\n"
        "v_exp_f32 v98, v98\n"
        "v_mfma_f32_16x16x32_bf16 v[116:119], a[232:235], a[176:179], v[116:119]\n"
        "v_exp_f32 v100, v100\n"
        "v_mfma_f32_16x16x32_bf16 v[116:119], a[228:231], a[172:175], v[116:119]\n"
        "v_exp_f32 v99, v99\n"
        "v_mfma_f32_16x16x32_bf16 v[116:119], a[224:227], a[168:171], v[116:119]\n"
        "v_exp_f32 v101, v101\n"
        "v_mfma_f32_16x16x32_bf16 v[104:107], v[152:155], v[176:179], v[16:19]\n"
        "s_waitcnt lgkmcnt(4)\n"
        "v_exp_f32 v102, v102\n"
        "v_mfma_f32_16x16x32_bf16 v[104:107], v[168:171], v[180:183], v[104:107]\n"
        "s_add_u32 m0, s41, 1024\n"
        "v_cvt_pk_bf16_f32 v64, v96, v97\n"
        "v_cvt_pk_bf16_f32 v65, v98, v99\n"
        "v_mfma_f32_16x16x32_bf16 v[108:111], v[172:175], v[176:179], v[20:23]\n"
        "v_cvt_pk_bf16_f32 v138, v116, v117\n"
        "v_cvt_pk_bf16_f32 v139, v118, v119\n"
        "v_mfma_f32_16x16x32_bf16 v[108:111], v[188:191], v[184:187], v[108:111]\n"
        "buffer_load_dwordx4 v243, s[8:11], s47 offen lds\n"
        "v_mfma_f32_16x16x32_bf16 v[120:123], a[224:227], a[192:195], 0\n"
        "ds_read_b128 a[224:227], v241 offset:12416\n"
        "v_cvt_pk_bf16_f32 v66, v100, v101\n"
        "v_mfma_f32_16x16x32_bf16 v[120:123], a[228:231], a[196:199], v[120:123]\n"
        "ds_read_b128 a[228:231], v240 offset:12544\n"
        "v_mfma_f32_16x16x32_bf16 v[120:123], a[232:235], a[200:203], v[120:123]\n"
        "ds_read_b128 a[232:235], v241 offset:12544\n"
        "v_mfma_f32_16x16x32_bf16 v[120:123], a[236:239], a[204:207], v[120:123]\n"
        "ds_read_b128 v[202:205], v251 offset:4096\n"
        "v_mfma_f32_16x16x32_bf16 v[120:123], a[240:243], a[208:211], v[120:123]\n"
        "ds_read_b128 v[182:185], v252 offset:512\n"
        "v_mfma_f32_16x16x32_bf16 v[120:123], a[244:247], a[212:215], v[120:123]\n"
        "s_waitcnt lgkmcnt(8)\n"
        "v_exp_f32 v103, v103\n"
        "v_mfma_f32_16x16x32_bf16 v[112:115], a[0:3], a[248:251], 0\n"
        "s_waitcnt lgkmcnt(7)\n"
        "v_cvt_pk_bf16_f32 v67, v102, v103\n"
        "s_nop 0\n"
        "v_mfma_f32_16x16x32_bf16 v[112:115], a[4:7], a[216:219], v[112:115]\n"
        "s_waitcnt lgkmcnt(6)\n"
        "v_exp_f32 v104, v104\n"
        "v_mfma_f32_16x16x32_bf16 v[112:115], a[8:11], a[220:223], v[112:115]\n"
        "s_waitcnt lgkmcnt(4)\n"
        "v_exp_f32 v105, v105\n"
        "v_mfma_f32_16x16x32_bf16 v[112:115], a[12:15], a[224:227], v[112:115]\n"
        "s_waitcnt lgkmcnt(3)\n"
        "v_exp_f32 v106, v106\n"
        "v_mfma_f32_16x16x32_bf16 v[112:115], a[16:19], a[228:231], v[112:115]\n"
        "s_waitcnt lgkmcnt(2)\n"
        "v_cvt_pk_bf16_f32 v72, v104, v105\n"
        "v_cvt_pk_bf16_f32 v146, v120, v121\n"
        "v_mfma_f32_16x16x32_bf16 v[112:115], a[20:23], a[232:235], v[112:115]\n"
        "s_add_u32 m0, s41, 2048\n"
        "v_exp_f32 v108, v108\n"
        "v_mfma_f32_16x16x32_bf16 v[116:119], a[44:47], a[232:235], 0\n"
        "v_cvt_pk_bf16_f32 v147, v122, v123\n"
        "v_add_u32 v245, s45, v245\n"
        "v_mfma_f32_16x16x32_bf16 v[116:119], a[40:43], a[228:231], v[116:119]\n"
        "buffer_load_dwordx4 v244, s[8:11], s47 offen lds\n"
        "v_mfma_f32_16x16x32_bf16 v[116:119], a[36:39], a[224:227], v[116:119]\n"
        "v_exp_f32 v107, v107\n"
        "v_mfma_f32_16x16x32_bf16 v[116:119], a[32:35], a[220:223], v[116:119]\n"
        "v_exp_f32 v109, v109\n"
        "v_mfma_f32_16x16x32_bf16 v[116:119], a[28:31], a[216:219], v[116:119]\n"
        "v_cvt_pk_bf16_f32 v73, v106, v107\n"
        "v_cvt_pk_bf16_f32 v74, v108, v109\n"
        "v_mfma_f32_16x16x32_bf16 v[116:119], a[24:27], a[248:251], v[116:119]\n"
        "v_exp_f32 v110, v110\n"
        "v_mfma_f32_16x16x32_bf16 v[120:123], a[48:51], a[248:251], 0\n"
        "v_exp_f32 v111, v111\n"
        "v_mfma_f32_16x16x32_bf16 v[120:123], a[52:55], a[216:219], v[120:123]\n"
        "v_cvt_pk_bf16_f32 v75, v110, v111\n"
        "v_cvt_pk_bf16_f32 v196, v112, v113\n"
        "v_mfma_f32_16x16x32_bf16 v[120:123], a[56:59], a[220:223], v[120:123]\n"
        "v_cvt_pk_bf16_f32 v197, v114, v115\n"
        "v_cvt_pk_bf16_f32 v198, v116, v117\n"
        "v_mfma_f32_16x16x32_bf16 v[120:123], a[60:63], a[224:227], v[120:123]\n"
        "v_cvt_pk_bf16_f32 v199, v118, v119\n"
        "v_add_u32 v246, s45, v246\n"
        "v_mfma_f32_16x16x32_bf16 v[120:123], a[64:67], a[228:231], v[120:123]\n"
        "v_mfma_f32_16x16x32_bf16 v[120:123], a[68:71], a[232:235], v[120:123]\n"
        "s_nop 2\n"
        "v_mfma_f32_16x16x32_bf16 v[112:115], a[92:95], a[232:235], 0\n"
        "s_nop 2\n"
        "v_mfma_f32_16x16x32_bf16 v[112:115], a[88:91], a[228:231], v[112:115]\n"
        "s_add_u32 m0, s41, 12288\n"
        "v_cvt_pk_bf16_f32 v200, v120, v121\n"
        "v_cvt_pk_bf16_f32 v201, v122, v123\n"
        "v_mfma_f32_16x16x32_bf16 v[112:115], a[84:87], a[224:227], v[112:115]\n"
        "v_cvt_pk_bf16_f32 v206, v120, v121\n"
        "v_cvt_pk_bf16_f32 v207, v122, v123\n"
        "v_mfma_f32_16x16x32_bf16 v[112:115], a[80:83], a[220:223], v[112:115]\n"
        "buffer_load_dwordx4 v242, s[8:11], s35 offen lds\n"
        "v_mfma_f32_16x16x32_bf16 v[112:115], a[76:79], a[216:219], v[112:115]\n"
        "v_mfma_f32_16x16x32_bf16 v[112:115], a[72:75], a[248:251], v[112:115]\n"
        "s_nop 2\n"
        "v_mfma_f32_16x16x32_bf16 v[96:99], v[152:155], v[196:199], v[32:35]\n"
        "s_waitcnt lgkmcnt(1)\n"
        "s_nop 1\n"
        "v_mfma_f32_16x16x32_bf16 v[96:99], v[168:171], v[200:203], v[96:99]\n"
        "s_nop 0\n"
        "v_cvt_pk_bf16_f32 v192, v112, v113\n"
        "v_cvt_pk_bf16_f32 v193, v114, v115\n"
        "v_mfma_f32_16x16x32_bf16 v[100:103], v[172:175], v[196:199], v[36:39]\n"
        "v_mfma_f32_16x16x32_bf16 v[100:103], v[188:191], v[204:207], v[100:103]\n"
        "ds_read_b128 v[202:205], v252 offset:4096\n"
        "s_cmp_eq_u32 s37, 100663296\n"
        "v_mfma_f32_16x16x32_bf16 v[116:119], a[96:99], a[248:251], 0\n"
        "s_cselect_b32 s46, 0, s37\n"
        "v_exp_f32 v96, v96\n"
        "v_mfma_f32_16x16x32_bf16 v[116:119], a[100:103], a[216:219], v[116:119]\n"
        "s_add_u32 s38, s46, s43\n"
        "v_exp_f32 v97, v97\n"
        "v_mfma_f32_16x16x32_bf16 v[116:119], a[104:107], a[220:223], v[116:119]\n"
        "s_add_u32 s39, s46, s44\n"
        "v_exp_f32 v98, v98\n"
        "v_mfma_f32_16x16x32_bf16 v[116:119], a[108:111], a[224:227], v[116:119]\n"
        "s_add_u32 s37, s37, 6291456\n"
        "v_exp_f32 v100, v100\n"
        "v_mfma_f32_16x16x32_bf16 v[116:119], a[112:115], a[228:231], v[116:119]\n"
        "v_exp_f32 v99, v99\n"
        "v_mfma_f32_16x16x32_bf16 v[116:119], a[116:119], a[232:235], v[116:119]\n"
        "s_add_u32 m0, s41, 13312\n"
        "v_exp_f32 v101, v101\n"
        "v_mfma_f32_16x16x32_bf16 v[120:123], a[140:143], a[232:235], 0\n"
        "v_exp_f32 v102, v102\n"
        "v_mfma_f32_16x16x32_bf16 v[120:123], a[136:139], a[228:231], v[120:123]\n"
        "buffer_load_dwordx4 v243, s[8:11], s35 offen lds\n"
        "v_mfma_f32_16x16x32_bf16 v[120:123], a[132:135], a[224:227], v[120:123]\n"
        "v_cvt_pk_bf16_f32 v80, v96, v97\n"
        "v_cvt_pk_bf16_f32 v194, v116, v117\n"
        "v_mfma_f32_16x16x32_bf16 v[120:123], a[128:131], a[220:223], v[120:123]\n"
        "v_cvt_pk_bf16_f32 v195, v118, v119\n"
        "v_cvt_pk_bf16_f32 v81, v98, v99\n"
        "v_mfma_f32_16x16x32_bf16 v[120:123], a[124:127], a[216:219], v[120:123]\n"
        "v_cvt_pk_bf16_f32 v82, v100, v101\n"
        "v_mfma_f32_16x16x32_bf16 v[120:123], a[120:123], a[248:251], v[120:123]\n"
        "v_exp_f32 v103, v103\n"
        "v_mfma_f32_16x16x32_bf16 v[112:115], a[248:251], a[144:147], 0\n"
        "v_cvt_pk_bf16_f32 v83, v102, v103\n"
        "s_nop 0\n"
        "v_mfma_f32_16x16x32_bf16 v[112:115], a[216:219], a[148:151], v[112:115]\n"
        "s_nop 2\n"
        "v_mfma_f32_16x16x32_bf16 v[112:115], a[220:223], a[152:155], v[112:115]\n"
        "ds_read_b128 a[236:239], v240 offset:13824\n"
        "v_cvt_pk_bf16_f32 v208, v120, v121\n"
        "v_mfma_f32_16x16x32_bf16 v[112:115], a[224:227], a[156:159], v[112:115]\n"
        "ds_read_b128 a[240:243], v241 offset:13824\n"
        "v_cvt_pk_bf16_f32 v209, v122, v123\n"
        "v_mfma_f32_16x16x32_bf16 v[112:115], a[228:231], a[160:163], v[112:115]\n"
        "ds_read_b128 a[244:247], v240 offset:13952\n"
        "v_mfma_f32_16x16x32_bf16 v[112:115], a[232:235], a[164:167], v[112:115]\n"
        "s_nop 2\n"
        "v_mfma_f32_16x16x32_bf16 v[104:107], v[192:195], v[156:159], v[8:11]\n"
        "s_nop 1\n"
        "v_mfma_f32_16x16x32_bf16 v[104:107], v[208:211], v[160:163], v[104:107]\n"
        "s_add_u32 m0, s41, 14336\n"
        "v_cvt_pk_bf16_f32 v132, v112, v113\n"
        "v_cvt_pk_bf16_f32 v133, v114, v115\n"
        "v_mfma_f32_16x16x32_bf16 v[116:119], a[232:235], a[188:191], 0\n"
        "v_mfma_f32_16x16x32_bf16 v[116:119], a[228:231], a[184:187], v[116:119]\n"
        "buffer_load_dwordx4 v244, s[8:11], s35 offen lds\n"
        "v_mfma_f32_16x16x32_bf16 v[116:119], a[224:227], a[180:183], v[116:119]\n"
        "v_mfma_f32_16x16x32_bf16 v[116:119], a[220:223], a[176:179], v[116:119]\n"
        "v_exp_f32 v104, v104\n"
        "v_mfma_f32_16x16x32_bf16 v[116:119], a[216:219], a[172:175], v[116:119]\n"
        "v_exp_f32 v105, v105\n"
        "v_mfma_f32_16x16x32_bf16 v[116:119], a[248:251], a[168:171], v[116:119]\n"
        "v_exp_f32 v106, v106\n"
        "v_mfma_f32_16x16x32_bf16 v[108:111], v[192:195], v[176:179], v[24:27]\n"
        "s_waitcnt lgkmcnt(4)\n"
        "v_exp_f32 v107, v107\n"
        "v_mfma_f32_16x16x32_bf16 v[108:111], v[208:211], v[180:183], v[108:111]\n"
        "v_cvt_pk_bf16_f32 v68, v104, v105\n"
        "v_cvt_pk_bf16_f32 v69, v106, v107\n"
        "v_mfma_f32_16x16x32_bf16 v[120:123], a[248:251], a[192:195], 0\n"
        "ds_read_b128 a[248:251], v241 offset:13952\n"
        "v_cvt_pk_bf16_f32 v140, v116, v117\n"
        "v_mfma_f32_16x16x32_bf16 v[120:123], a[216:219], a[196:199], v[120:123]\n"
        "ds_read_b128 a[216:219], v240 offset:14080\n"
        "v_cvt_pk_bf16_f32 v141, v118, v119\n"
        "v_mfma_f32_16x16x32_bf16 v[120:123], a[220:223], a[200:203], v[120:123]\n"
        "ds_read_b128 a[220:223], v241 offset:14080\n"
        "v_mfma_f32_16x16x32_bf16 v[120:123], a[224:227], a[204:207], v[120:123]\n"
        "v_exp_f32 v108, v108\n"
        "v_mfma_f32_16x16x32_bf16 v[120:123], a[228:231], a[208:211], v[120:123]\n"
        "v_exp_f32 v109, v109\n"
        "v_mfma_f32_16x16x32_bf16 v[120:123], a[232:235], a[212:215], v[120:123]\n"
        "v_exp_f32 v110, v110\n"
        "v_mfma_f32_16x16x32_bf16 v[96:99], v[192:195], v[196:199], v[40:43]\n"
        "s_waitcnt lgkmcnt(6)\n"
        "v_exp_f32 v111, v111\n"
        "v_mfma_f32_16x16x32_bf16 v[96:99], v[208:211], v[200:203], v[96:99]\n"
        "s_waitcnt lgkmcnt(5)\n"
        "v_cvt_pk_bf16_f32 v76, v108, v109\n"
        "v_cvt_pk_bf16_f32 v77, v110, v111\n"
        "v_mfma_f32_16x16x32_bf16 v[112:115], a[72:75], a[236:239], 0\n"
        "s_waitcnt lgkmcnt(4)\n"
        "s_nop 0\n"
        "v_cvt_pk_bf16_f32 v148, v120, v121\n"
        "v_mfma_f32_16x16x32_bf16 v[112:115], a[76:79], a[240:243], v[112:115]\n"
        "s_waitcnt lgkmcnt(3)\n"
        "v_cvt_pk_bf16_f32 v149, v122, v123\n"
        "v_mfma_f32_16x16x32_bf16 v[112:115], a[80:83], a[244:247], v[112:115]\n"
        "s_waitcnt lgkmcnt(2)\n"
        "v_exp_f32 v96, v96\n"
        "v_mfma_f32_16x16x32_bf16 v[112:115], a[84:87], a[248:251], v[112:115]\n"
        "s_waitcnt lgkmcnt(1)\n"
        "v_exp_f32 v97, v97\n"
        "v_mfma_f32_16x16x32_bf16 v[112:115], a[88:91], a[216:219], v[112:115]\n"
        "s_waitcnt lgkmcnt(0)\n"
        "v_exp_f32 v98, v98\n"
        "v_mfma_f32_16x16x32_bf16 v[112:115], a[92:95], a[220:223], v[112:115]\n"
        "v_exp_f32 v99, v99\n"
        "v_mfma_f32_16x16x32_bf16 v[116:119], a[116:119], a[220:223], 0\n"
        "v_cvt_pk_bf16_f32 v84, v96, v97\n"
        "v_cvt_pk_bf16_f32 v85, v98, v99\n"
        "v_mfma_f32_16x16x32_bf16 v[116:119], a[112:115], a[216:219], v[116:119]\n"
        "v_add_u32 v240, s31, v240\n"
        "v_add_u32 v241, s31, v241\n"
        "v_mfma_f32_16x16x32_bf16 v[116:119], a[108:111], a[248:251], v[116:119]\n"
        "v_cvt_pk_bf16_f32 v212, v112, v113\n"
        "v_cvt_pk_bf16_f32 v213, v114, v115\n"
        "v_mfma_f32_16x16x32_bf16 v[116:119], a[104:107], a[244:247], v[116:119]\n"
        "v_mfma_f32_16x16x32_bf16 v[116:119], a[100:103], a[240:243], v[116:119]\n"
        "v_mfma_f32_16x16x32_bf16 v[116:119], a[96:99], a[236:239], v[116:119]\n"
        "s_nop 2\n"
        "v_mfma_f32_16x16x32_bf16 v[120:123], a[120:123], a[236:239], 0\n"
        "s_nop 2\n"
        "v_mfma_f32_16x16x32_bf16 v[120:123], a[124:127], a[240:243], v[120:123]\n"
        "s_add_u32 s47, s47, 6291456\n"
        "v_cvt_pk_bf16_f32 v214, v116, v117\n"
        "v_cvt_pk_bf16_f32 v215, v118, v119\n"
        "v_mfma_f32_16x16x32_bf16 v[120:123], a[128:131], a[244:247], v[120:123]\n"
        "s_add_u32 s36, s36, 2097152\n"
        "v_mfma_f32_16x16x32_bf16 v[120:123], a[132:135], a[248:251], v[120:123]\n"
        "s_xor_b32 s48, s48, 8192\n"
        "v_mfma_f32_16x16x32_bf16 v[120:123], a[136:139], a[216:219], v[120:123]\n"
        "v_mfma_f32_16x16x32_bf16 v[120:123], a[140:143], a[220:223], v[120:123]\n"
        "s_nop 2\n"
        "v_mfma_f32_16x16x32_bf16 v[112:115], a[220:223], a[164:167], 0\n"
        "s_nop 2\n"
        "v_mfma_f32_16x16x32_bf16 v[112:115], a[216:219], a[160:163], v[112:115]\n"
        "v_cvt_pk_bf16_f32 v230, v120, v121\n"
        "v_cvt_pk_bf16_f32 v231, v122, v123\n"
        "v_mfma_f32_16x16x32_bf16 v[112:115], a[248:251], a[156:159], v[112:115]\n"
        "v_mfma_f32_16x16x32_bf16 v[112:115], a[244:247], a[152:155], v[112:115]\n"
        "v_mfma_f32_16x16x32_bf16 v[112:115], a[240:243], a[148:151], v[112:115]\n"
        "v_mfma_f32_16x16x32_bf16 v[112:115], a[236:239], a[144:147], v[112:115]\n"
        "s_barrier\n"
        "s_nop 2\n"
        "v_mfma_f32_16x16x32_bf16 v[100:103], v[212:215], v[156:159], v[12:15]\n"
        "s_nop 2\n"
        "v_mfma_f32_16x16x32_bf16 v[100:103], v[228:231], v[164:167], v[100:103]\n"
        "v_cvt_pk_bf16_f32 v134, v112, v113\n"
        "v_cvt_pk_bf16_f32 v135, v114, v115\n"
        "v_mfma_f32_16x16x32_bf16 v[116:119], a[236:239], a[168:171], 0\n"
        "v_mfma_f32_16x16x32_bf16 v[116:119], a[240:243], a[172:175], v[116:119]\n"
        "v_mfma_f32_16x16x32_bf16 v[116:119], a[244:247], a[176:179], v[116:119]\n"
        "v_mfma_f32_16x16x32_bf16 v[116:119], a[248:251], a[180:183], v[116:119]\n"
        "v_mfma_f32_16x16x32_bf16 v[116:119], a[216:219], a[184:187], v[116:119]\n"
        "s_nop 0\n"
        "v_exp_f32 v100, v100\n"
        "v_mfma_f32_16x16x32_bf16 v[116:119], a[220:223], a[188:191], v[116:119]\n"
        "v_exp_f32 v101, v101\n"
        "v_mfma_f32_16x16x32_bf16 v[104:107], v[212:215], v[176:179], v[28:31]\n"
        "v_exp_f32 v102, v102\n"
        "v_mfma_f32_16x16x32_bf16 v[104:107], v[228:231], v[184:187], v[104:107]\n"
        "v_exp_f32 v103, v103\n"
        "v_mfma_f32_16x16x32_bf16 v[120:123], a[220:223], a[212:215], 0\n"
        "v_cvt_pk_bf16_f32 v70, v100, v101\n"
        "v_cvt_pk_bf16_f32 v71, v102, v103\n"
        "v_mfma_f32_16x16x32_bf16 v[120:123], a[216:219], a[208:211], v[120:123]\n"
        "v_cvt_pk_bf16_f32 v142, v116, v117\n"
        "v_cvt_pk_bf16_f32 v143, v118, v119\n"
        "v_mfma_f32_16x16x32_bf16 v[120:123], a[248:251], a[204:207], v[120:123]\n"
        "v_mov_b64 v[96:97], 0\n"
        "v_mfma_f32_16x16x32_bf16 v[120:123], a[244:247], a[200:203], v[120:123]\n"
        "v_exp_f32 v104, v104\n"
        "v_mfma_f32_16x16x32_bf16 v[120:123], a[240:243], a[196:199], v[120:123]\n"
        "v_exp_f32 v105, v105\n"
        "v_mfma_f32_16x16x32_bf16 v[120:123], a[236:239], a[192:195], v[120:123]\n"
        "v_exp_f32 v106, v106\n"
        "v_mfma_f32_16x16x32_bf16 v[108:111], v[212:215], v[196:199], v[44:47]\n"
        "v_exp_f32 v107, v107\n"
        "v_mfma_f32_16x16x32_bf16 v[108:111], v[228:231], v[204:207], v[108:111]\n"
        "v_cvt_pk_bf16_f32 v78, v104, v105\n"
        "v_cvt_pk_bf16_f32 v79, v106, v107\n"
        "v_mfma_f32_16x16x32_bf16 v[112:115], a[0:3], a[236:239], 0\n"
        "s_nop 0\n"
        "v_cvt_pk_bf16_f32 v150, v120, v121\n"
        "v_cvt_pk_bf16_f32 v151, v122, v123\n"
        "v_mfma_f32_16x16x32_bf16 v[112:115], a[4:7], a[240:243], v[112:115]\n"
        "v_mfma_f32_16x16x32_bf16 v[112:115], a[8:11], a[244:247], v[112:115]\n"
        "v_exp_f32 v108, v108\n"
        "v_mfma_f32_16x16x32_bf16 v[112:115], a[12:15], a[248:251], v[112:115]\n"
        "v_exp_f32 v109, v109\n"
        "v_mfma_f32_16x16x32_bf16 v[112:115], a[16:19], a[216:219], v[112:115]\n"
        "v_exp_f32 v110, v110\n"
        "v_mfma_f32_16x16x32_bf16 v[112:115], a[20:23], a[220:223], v[112:115]\n"
        "v_exp_f32 v111, v111\n"
        "v_smfmac_f32_16x16x64_bf16 v[96:99], a[252:255], v[64:71], v237\n"
        "v_cvt_pk_bf16_f32 v86, v108, v109\n"
        "v_mfma_f32_16x16x32_bf16 v[100:103], v[132:135], v[68:71], 0\n"
        "v_mov_b64 v[124:125], 0\n"
        "v_cvt_pk_bf16_f32 v87, v110, v111\n"
        "v_mfma_f32_16x16x32_bf16 v[100:103], v[128:131], v[64:67], v[100:103]\n"
        "s_nop 0\n"
        "v_cvt_pk_bf16_f32 v216, v112, v113\n"
        "v_cvt_pk_bf16_f32 v217, v114, v115\n"
        "v_mfma_f32_16x16x32_bf16 v[104:107], v[136:139], v[64:67], 0\n"
        "ds_read_b128 v[222:225], v251 offset:4608\n"
        "v_add_f32 v97, v96, v97\n"
        "v_mfma_f32_16x16x32_bf16 v[104:107], v[140:143], v[68:71], v[104:107]\n"
        "v_rcp_f32 v96, v97\n"
        "v_mfma_f32_16x16x32_bf16 v[108:111], v[148:151], v[68:71], 0\n"
        ".long 0xbec200ff\n"
        "47:\n"
        "s_branch 61b\n"
        "v_mul_f32 v100, v96, v100\n"
        "v_mul_f32 v101, v96, v101\n"
        "v_mfma_f32_16x16x32_bf16 v[108:111], v[144:147], v[64:67], v[108:111]\n"
        "v_mul_f32 v102, v96, v102\n"
        "v_mul_f32 v103, v96, v103\n"
        "v_mfma_f32_16x16x32_bf16 v[116:119], a[44:47], a[220:223], 0\n"
        "v_cvt_pk_bf16_f32 v100, v100, v101\n"
        "v_cvt_pk_bf16_f32 v101, v102, v103\n"
        "v_mfma_f32_16x16x32_bf16 v[116:119], a[40:43], a[216:219], v[116:119]\n"
        "v_mul_f32 v104, v96, v104\n"
        "v_mul_f32 v105, v96, v105\n"
        "v_mfma_f32_16x16x32_bf16 v[116:119], a[36:39], a[248:251], v[116:119]\n"
        "v_mul_f32 v106, v96, v106\n"
        "v_mul_f32 v107, v96, v107\n"
        "v_mfma_f32_16x16x32_bf16 v[116:119], a[32:35], a[244:247], v[116:119]\n"
        "v_cvt_pk_bf16_f32 v102, v104, v105\n"
        "v_cvt_pk_bf16_f32 v103, v106, v107\n"
        "v_mfma_f32_16x16x32_bf16 v[116:119], a[28:31], a[240:243], v[116:119]\n"
        "ds_write_b128 v245, v[100:103] offset:0\n"
        "v_mfma_f32_16x16x32_bf16 v[116:119], a[24:27], a[236:239], v[116:119]\n"
        "v_mul_f32 v108, v96, v108\n"
        "v_mul_f32 v109, v96, v109\n"
        "v_smfmac_f32_16x16x64_bf16 v[124:127], a[252:255], v[72:79], v237\n"
        "v_mul_f32 v110, v96, v110\n"
        "v_mfma_f32_16x16x32_bf16 v[64:67], v[132:135], v[76:79], 0\n"
        "v_mul_f32 v111, v96, v111\n"
        "v_cvt_pk_bf16_f32 v108, v108, v109\n"
        "v_mfma_f32_16x16x32_bf16 v[64:67], v[128:131], v[72:75], v[64:67]\n"
        "ds_read_b128 v[156:159], v247 offset:0\n"
        "v_cvt_pk_bf16_f32 v218, v116, v117\n"
        "v_mfma_f32_16x16x32_bf16 v[68:71], v[136:139], v[72:75], 0\n"
        "ds_read_b128 v[160:163], v247 offset:4096\n"
        "v_cvt_pk_bf16_f32 v219, v118, v119\n"
        "v_mfma_f32_16x16x32_bf16 v[68:71], v[140:143], v[76:79], v[68:71]\n"
        "ds_read_b128 v[164:167], v247 offset:4352\n"
        "v_cvt_pk_bf16_f32 v109, v110, v111\n"
        "v_mfma_f32_16x16x32_bf16 v[120:123], v[148:151], v[76:79], 0\n"
        "ds_write_b64 v246, v[108:109] offset:0\n"
        "v_mfma_f32_16x16x32_bf16 v[120:123], v[144:147], v[72:75], v[120:123]\n"
        "v_max_u32 v236, v96, v97\n"
        "v_mov_b64 v[116:117], 0\n"
        "v_mfma_f32_16x16x32_bf16 v[112:115], a[48:51], a[236:239], 0\n"
        "v_add_f32 v125, v124, v125\n"
        "v_mfma_f32_16x16x32_bf16 v[112:115], a[52:55], a[240:243], v[112:115]\n"
        "v_rcp_f32 v124, v125\n"
        "v_mfma_f32_16x16x32_bf16 v[112:115], a[56:59], a[244:247], v[112:115]\n"
        "v_mul_f32 v64, v124, v64\n"
        "v_mul_f32 v65, v124, v65\n"
        "v_mfma_f32_16x16x32_bf16 v[112:115], a[60:63], a[248:251], v[112:115]\n"
        "v_mul_f32 v66, v124, v66\n"
        "v_mul_f32 v67, v124, v67\n"
        "v_mfma_f32_16x16x32_bf16 v[112:115], a[64:67], a[216:219], v[112:115]\n"
        "v_cvt_pk_bf16_f32 v64, v64, v65\n"
        "v_cvt_pk_bf16_f32 v65, v66, v67\n"
        "v_mfma_f32_16x16x32_bf16 v[112:115], a[68:71], a[220:223], v[112:115]\n"
        "ds_read_b128 a[216:219], v248 offset:0\n"
        "v_mul_f32 v68, v124, v68\n"
        "v_smfmac_f32_16x16x64_bf16 v[116:119], a[252:255], v[80:87], v237\n"
        "ds_read_b128 a[220:223], v248 offset:3072\n"
        "v_mfma_f32_16x16x32_bf16 v[72:75], v[132:135], v[84:87], 0\n"
        "v_mul_f32 v69, v124, v69\n"
        "v_mul_f32 v70, v124, v70\n"
        "v_mfma_f32_16x16x32_bf16 v[72:75], v[128:131], v[80:83], v[72:75]\n"
        "v_cvt_pk_bf16_f32 v220, v112, v113\n"
        "v_cvt_pk_bf16_f32 v221, v114, v115\n"
        "v_mfma_f32_16x16x32_bf16 v[76:79], v[136:139], v[80:83], 0\n"
        "ds_read_b128 a[224:227], v254 offset:16\n"
        "v_cvt_pk_bf16_f32 v226, v112, v113\n"
        "v_mfma_f32_16x16x32_bf16 v[76:79], v[140:143], v[84:87], v[76:79]\n"
        "ds_read_b128 a[228:231], v254 offset:3088\n"
        "v_cvt_pk_bf16_f32 v227, v114, v115\n"
        "v_mfma_f32_16x16x32_bf16 v[96:99], v[148:151], v[84:87], 0\n"
        "v_mul_f32 v71, v124, v71\n"
        "v_cvt_pk_bf16_f32 v66, v68, v69\n"
        "v_mfma_f32_16x16x32_bf16 v[96:99], v[144:147], v[80:83], v[96:99]\n"
        "v_cvt_pk_bf16_f32 v67, v70, v71\n"
        "v_mul_f32 v120, v124, v120\n"
        "v_mfma_f32_16x16x32_bf16 v[100:103], v[152:155], v[216:219], v[48:51]\n"
        "s_waitcnt lgkmcnt(9)\n"
        "ds_read_b128 v[152:155], v247 offset:256\n"
        "v_mul_f32 v121, v124, v121\n"
        "v_mfma_f32_16x16x32_bf16 v[100:103], v[168:171], v[220:223], v[100:103]\n"
        "ds_write_b128 v245, v[64:67] offset:256\n"
        "v_mfma_f32_16x16x32_bf16 v[104:107], v[172:175], v[216:219], v[52:55]\n"
        "v_mul_f32 v122, v124, v122\n"
        "v_mul_f32 v123, v124, v123\n"
        "v_mfma_f32_16x16x32_bf16 v[104:107], v[188:191], v[224:227], v[104:107]\n"
        "s_waitcnt lgkmcnt(5)\n"
        "v_cvt_pk_bf16_f32 v120, v120, v121\n"
        "v_cvt_pk_bf16_f32 v121, v122, v123\n"
        "v_mfma_f32_16x16x32_bf16 v[176:179], v[156:159], a[216:219], 0\n"
        "s_waitcnt lgkmcnt(4)\n"
        "ds_write_b64 v246, v[120:121] offset:256\n"
        "v_mfma_f32_16x16x32_bf16 v[176:179], v[160:163], a[220:223], v[176:179]\n"
        "ds_read_b128 v[222:225], v252 offset:4608\n"
        "v_max3_u32 v236, v236, v124, v125\n"
        "v_mfma_f32_16x16x32_bf16 v[196:199], v[164:167], a[220:223], 0\n"
        "s_waitcnt lgkmcnt(3)\n"
        "ds_read_b128 a[232:235], v248 offset:32\n"
        "v_add_f32 v117, v116, v117\n"
        "v_mfma_f32_16x16x32_bf16 v[196:199], v[152:155], a[216:219], v[196:199]\n"
        "ds_read_b128 a[220:223], v248 offset:3104\n"
        "v_mfma_f32_16x16x32_bf16 v[200:203], v[152:155], a[224:227], 0\n"
        "ds_read_b128 a[216:219], v254 offset:48\n"
        "v_mfma_f32_16x16x32_bf16 v[200:203], v[164:167], a[228:231], v[200:203]\n"
        "v_rcp_f32 v116, v117\n"
        "v_mfma_f32_16x16x32_bf16 v[180:183], v[160:163], a[228:231], 0\n"
        "ds_read_b128 a[228:231], v254 offset:3120\n"
        "v_mul_f32 v72, v116, v72\n"
        "v_mfma_f32_16x16x32_bf16 v[180:183], v[156:159], a[224:227], v[180:183]\n"
        "ds_read_b128 a[236:239], v247 offset:8192\n"
        "v_mul_f32 v73, v116, v73\n"
        "v_mfma_f32_16x16x32_bf16 v[108:111], v[192:195], v[216:219], v[56:59]\n"
        "s_waitcnt lgkmcnt(5)\n"
        "ds_read_b128 a[224:227], v248 offset:64\n"
        "v_mul_f32 v74, v116, v74\n"
        "v_mfma_f32_16x16x32_bf16 v[108:111], v[208:211], v[220:223], v[108:111]\n"
        "ds_read_b128 a[240:243], v247 offset:12288\n"
        "v_mul_f32 v75, v116, v75\n"
        "v_mfma_f32_16x16x32_bf16 v[112:115], v[212:215], v[216:219], v[60:63]\n"
        "ds_read_b128 a[244:247], v247 offset:12544\n"
        "v_cvt_pk_bf16_f32 v72, v72, v73\n"
        "v_mfma_f32_16x16x32_bf16 v[112:115], v[228:231], v[224:227], v[112:115]\n"
        "s_waitcnt lgkmcnt(7)\n"
        "ds_read_b128 a[248:251], v247 offset:8448\n"
        "v_cvt_pk_bf16_f32 v73, v74, v75\n"
        "v_mfma_f32_16x16x32_bf16 v[184:187], v[156:159], a[232:235], 0\n"
        "s_waitcnt lgkmcnt(7)\n"
        "v_exp_f32 v100, v100\n"
        "v_mfma_f32_16x16x32_bf16 v[184:187], v[160:163], a[220:223], v[184:187]\n"
        "v_exp_f32 v101, v101\n"
        "v_mfma_f32_16x16x32_bf16 v[204:207], v[164:167], a[220:223], 0\n"
        "ds_read_b128 a[220:223], v248 offset:3136\n"
        "v_mul_f32 v76, v116, v76\n"
        "v_mfma_f32_16x16x32_bf16 v[204:207], v[152:155], a[232:235], v[204:207]\n"
        "s_waitcnt lgkmcnt(7)\n"
        "ds_read_b128 a[232:235], v254 offset:80\n"
        "v_mul_f32 v77, v116, v77\n"
        "v_mfma_f32_16x16x32_bf16 v[192:195], v[152:155], a[216:219], 0\n"
        "s_waitcnt lgkmcnt(7)\n"
        "v_mul_f32 v78, v116, v78\n"
        "v_mul_f32 v79, v116, v79\n"
        "v_mfma_f32_16x16x32_bf16 v[192:195], v[164:167], a[228:231], v[192:195]\n"
        "v_exp_f32 v102, v102\n"
        "v_mfma_f32_16x16x32_bf16 v[172:175], v[160:163], a[228:231], 0\n"
        "ds_read_b128 a[228:231], v254 offset:3152\n"
        "v_cvt_pk_bf16_f32 v88, v100, v101\n"
        "v_mfma_f32_16x16x32_bf16 v[172:175], v[156:159], a[216:219], v[172:175]\n"
        "s_waitcnt lgkmcnt(6)\n"
        "ds_read_b128 a[216:219], v248 offset:6144\n"
        "v_cvt_pk_bf16_f32 v74, v76, v77\n"
        "v_mfma_f32_16x16x32_bf16 v[212:215], v[156:159], a[224:227], 0\n"
        "s_waitcnt lgkmcnt(3)\n"
        "v_exp_f32 v104, v104\n"
        "v_mfma_f32_16x16x32_bf16 v[212:215], v[160:163], a[220:223], v[212:215]\n"
        "v_exp_f32 v103, v103\n"
        "v_mfma_f32_16x16x32_bf16 v[216:219], v[164:167], a[220:223], 0\n"
        "ds_read_b128 a[220:223], v248 offset:9216\n"
        "v_cvt_pk_bf16_f32 v75, v78, v79\n"
        "v_mfma_f32_16x16x32_bf16 v[216:219], v[152:155], a[224:227], v[216:219]\n"
        "s_waitcnt lgkmcnt(3)\n"
        "ds_write_b128 v245, v[72:75] offset:512\n"
        "v_mfma_f32_16x16x32_bf16 v[224:227], v[152:155], a[232:235], 0\n"
        "s_waitcnt lgkmcnt(3)\n"
        "v_exp_f32 v105, v105\n"
        "v_mfma_f32_16x16x32_bf16 v[224:227], v[164:167], a[228:231], v[224:227]\n"
        "v_cvt_pk_bf16_f32 v89, v102, v103\n"
        "v_cvt_pk_bf16_f32 v90, v104, v105\n"
        "v_mfma_f32_16x16x32_bf16 v[220:223], v[160:163], a[228:231], 0\n"
        "ds_read_b128 a[224:227], v254 offset:6160\n"
        "ds_read_b128 a[228:231], v254 offset:9232\n"
        "v_mfma_f32_16x16x32_bf16 v[220:223], v[156:159], a[232:235], v[220:223]\n"
        "s_waitcnt lgkmcnt(4)\n"
        "ds_read_b128 a[232:235], v248 offset:6176\n"
        "v_mul_f32 v96, v116, v96\n"
        "v_mfma_f32_16x16x32_bf16 v[176:179], a[236:239], a[216:219], v[176:179]\n"
        "s_waitcnt lgkmcnt(4)\n"
        "v_exp_f32 v106, v106\n"
        "v_mfma_f32_16x16x32_bf16 v[176:179], a[240:243], a[220:223], v[176:179]\n"
        "v_mul_f32 v97, v116, v97\n"
        "v_mul_f32 v98, v116, v98\n"
        "v_mfma_f32_16x16x32_bf16 v[196:199], a[244:247], a[220:223], v[196:199]\n"
        "ds_read_b128 a[220:223], v248 offset:9248\n"
        "v_mul_f32 v99, v116, v99\n"
        "v_mfma_f32_16x16x32_bf16 v[196:199], a[248:251], a[216:219], v[196:199]\n"
        "s_waitcnt lgkmcnt(3)\n"
        "ds_read_b128 a[216:219], v254 offset:6192\n"
        "v_cvt_pk_bf16_f32 v96, v96, v97\n"
        "v_mfma_f32_16x16x32_bf16 v[200:203], a[248:251], a[224:227], v[200:203]\n"
        "s_waitcnt lgkmcnt(3)\n"
        "ds_read_b128 v[156:159], v247 offset:16384\n"
        "v_cvt_pk_bf16_f32 v97, v98, v99\n"
        "v_mfma_f32_16x16x32_bf16 v[200:203], a[244:247], a[228:231], v[200:203]\n"
        "ds_write_b64 v246, v[96:97] offset:512\n"
        "v_mfma_f32_16x16x32_bf16 v[180:183], a[240:243], a[228:231], v[180:183]\n"
        "ds_read_b128 a[228:231], v254 offset:9264\n"
        "v_max3_u32 v236, v236, v116, v117\n"
        "v_mfma_f32_16x16x32_bf16 v[180:183], a[236:239], a[224:227], v[180:183]\n"
        "s_waitcnt lgkmcnt(5)\n"
        "ds_read_b128 a[224:227], v248 offset:6208\n"
        "v_mfma_f32_16x16x32_bf16 v[184:187], a[236:239], a[232:235], v[184:187]\n"
        "s_waitcnt lgkmcnt(5)\n"
        "ds_read_b128 v[160:163], v247 offset:20480\n"
        "v_mfma_f32_16x16x32_bf16 v[184:187], a[240:243], a[220:223], v[184:187]\n"
        "ds_read_b128 v[164:167], v247 offset:20736\n"
        "v_mfma_f32_16x16x32_bf16 v[204:207], a[244:247], a[220:223], v[204:207]\n"
        "ds_read_b128 a[220:223], v248 offset:9280\n"
        "v_mfma_f32_16x16x32_bf16 v[204:207], a[248:251], a[232:235], v[204:207]\n"
        "s_waitcnt lgkmcnt(7)\n"
        "ds_read_b128 a[232:235], v254 offset:6224\n"
        "v_mfma_f32_16x16x32_bf16 v[192:195], a[248:251], a[216:219], v[192:195]\n"
        "s_waitcnt lgkmcnt(5)\n"
        "ds_read_b128 v[152:155], v247 offset:16640\n"
        "v_mfma_f32_16x16x32_bf16 v[192:195], a[244:247], a[228:231], v[192:195]\n"
        "v_exp_f32 v107, v107\n"
        "v_mfma_f32_16x16x32_bf16 v[172:175], a[240:243], a[228:231], v[172:175]\n"
        "ds_read_b128 a[228:231], v254 offset:9296\n"
        "v_cvt_pk_bf16_f32 v91, v106, v107\n"
        "v_mfma_f32_16x16x32_bf16 v[172:175], a[236:239], a[216:219], v[172:175]\n"
        "s_waitcnt lgkmcnt(6)\n"
        "ds_read_b128 a[216:219], v248 offset:12288\n"
        "v_mfma_f32_16x16x32_bf16 v[212:215], a[236:239], a[224:227], v[212:215]\n"
        "s_waitcnt lgkmcnt(4)\n"
        "v_exp_f32 v108, v108\n"
        "v_mfma_f32_16x16x32_bf16 v[212:215], a[240:243], a[220:223], v[212:215]\n"
        "v_exp_f32 v109, v109\n"
        "v_mfma_f32_16x16x32_bf16 v[216:219], a[244:247], a[220:223], v[216:219]\n"
        "ds_read_b128 a[220:223], v248 offset:15360\n"
        "v_cvt_pk_bf16_f32 v92, v108, v109\n"
        "v_mfma_f32_16x16x32_bf16 v[216:219], a[248:251], a[224:227], v[216:219]\n"
        "s_waitcnt lgkmcnt(4)\n"
        "ds_read_b128 a[224:227], v254 offset:12304\n"
        "v_mfma_f32_16x16x32_bf16 v[224:227], a[248:251], a[232:235], v[224:227]\n"
        "s_waitcnt lgkmcnt(3)\n"
        "v_exp_f32 v110, v110\n"
        "v_mfma_f32_16x16x32_bf16 v[224:227], a[244:247], a[228:231], v[224:227]\n"
        "v_exp_f32 v112, v112\n"
        "v_mfma_f32_16x16x32_bf16 v[220:223], a[240:243], a[228:231], v[220:223]\n"
        "ds_read_b128 a[228:231], v254 offset:15376\n"
        "v_mfma_f32_16x16x32_bf16 v[220:223], a[236:239], a[232:235], v[220:223]\n"
        "s_waitcnt lgkmcnt(3)\n"
        "ds_read_b128 a[232:235], v248 offset:12320\n"
        "v_mfma_f32_16x16x32_bf16 v[176:179], v[156:159], a[216:219], v[176:179]\n"
        "s_waitcnt lgkmcnt(3)\n"
        "v_exp_f32 v111, v111\n"
        "v_mfma_f32_16x16x32_bf16 v[176:179], v[160:163], a[220:223], v[176:179]\n"
        "v_exp_f32 v113, v113\n"
        "v_mfma_f32_16x16x32_bf16 v[196:199], v[164:167], a[220:223], v[196:199]\n"
        "ds_read_b128 a[220:223], v248 offset:15392\n"
        "v_cvt_pk_bf16_f32 v93, v110, v111\n"
        "v_mfma_f32_16x16x32_bf16 v[196:199], v[152:155], a[216:219], v[196:199]\n"
        "s_waitcnt lgkmcnt(3)\n"
        "ds_read_b128 a[216:219], v254 offset:12336\n"
        "v_cvt_pk_bf16_f32 v94, v112, v113\n"
        "v_mfma_f32_16x16x32_bf16 v[200:203], v[152:155], a[224:227], v[200:203]\n"
        "s_waitcnt lgkmcnt(3)\n"
        "v_exp_f32 v114, v114\n"
        "v_mfma_f32_16x16x32_bf16 v[200:203], v[164:167], a[228:231], v[200:203]\n"
        "v_exp_f32 v115, v115\n"
        "v_mfma_f32_16x16x32_bf16 v[180:183], v[160:163], a[228:231], v[180:183]\n"
        "s_waitcnt vmcnt(0)\n"
        "ds_read_b128 a[228:231], v254 offset:15408\n"
        "v_cvt_pk_bf16_f32 v95, v114, v115\n"
        "v_mfma_f32_16x16x32_bf16 v[180:183], v[156:159], a[224:227], v[180:183]\n"
        "s_waitcnt lgkmcnt(3)\n"
        "ds_read_b128 a[224:227], v248 offset:12352\n"
        "v_mfma_f32_16x16x32_bf16 v[184:187], v[156:159], a[232:235], v[184:187]\n"
        "s_waitcnt lgkmcnt(3)\n"
        "v_mfma_f32_16x16x32_bf16 v[184:187], v[160:163], a[220:223], v[184:187]\n"
        "s_barrier\n"
        "v_mfma_f32_16x16x32_bf16 v[204:207], v[164:167], a[220:223], v[204:207]\n"
        "ds_read_b128 a[220:223], v248 offset:15424\n"
        "v_mfma_f32_16x16x32_bf16 v[204:207], v[152:155], a[232:235], v[204:207]\n"
        "s_waitcnt lgkmcnt(3)\n"
        "ds_read_b128 a[232:235], v254 offset:12368\n"
        "v_mfma_f32_16x16x32_bf16 v[192:195], v[152:155], a[216:219], v[192:195]\n"
        "s_waitcnt lgkmcnt(3)\n"
        "ds_read_b128 a[236:239], v240 offset:0\n"
        "v_mfma_f32_16x16x32_bf16 v[192:195], v[164:167], a[228:231], v[192:195]\n"
        "ds_read_b128 a[240:243], v241 offset:0\n"
        "v_mfma_f32_16x16x32_bf16 v[172:175], v[160:163], a[228:231], v[172:175]\n"
        "ds_read_b128 a[228:231], v254 offset:15440\n"
        "v_mfma_f32_16x16x32_bf16 v[172:175], v[156:159], a[216:219], v[172:175]\n"
        "s_waitcnt lgkmcnt(5)\n"
        "ds_read_b128 a[244:247], v240 offset:128\n"
        "v_mfma_f32_16x16x32_bf16 v[212:215], v[156:159], a[224:227], v[212:215]\n"
        "s_waitcnt lgkmcnt(5)\n"
        "ds_read_b128 a[248:251], v241 offset:128\n"
        "v_mfma_f32_16x16x32_bf16 v[212:215], v[160:163], a[220:223], v[212:215]\n"
        "ds_read_b128 a[216:219], v240 offset:256\n"
        "v_mfma_f32_16x16x32_bf16 v[216:219], v[164:167], a[220:223], v[216:219]\n"
        "ds_read_b128 a[220:223], v241 offset:256\n"
        "v_mfma_f32_16x16x32_bf16 v[216:219], v[152:155], a[224:227], v[216:219]\n"
        "s_waitcnt lgkmcnt(7)\n"
        "v_mfma_f32_16x16x32_bf16 v[224:227], v[152:155], a[232:235], v[224:227]\n"
        "s_waitcnt lgkmcnt(4)\n"
        "v_mfma_f32_16x16x32_bf16 v[224:227], v[164:167], a[228:231], v[224:227]\n"
        "s_sub_u32 s27, s27, 1\n"
        "v_mfma_f32_16x16x32_bf16 v[220:223], v[160:163], a[228:231], v[220:223]\n"
        "s_cmp_lg_u32 s27, 0\n"
        "v_mfma_f32_16x16x32_bf16 v[220:223], v[156:159], a[232:235], v[220:223]\n"
        "s_cbranch_scc1 10b\n"
        "19:\n"
        ".long 0xbec200ff\n"
        "54:\n"
        "s_branch 61b\n"
        "s_waitcnt lgkmcnt(0)\n"
        "s_and_b32 s46, s42, 1\n"
        "s_lshl_b32 s31, s46, 8\n"
        "s_sub_u32 s31, s45, s31\n"
        "v_add_u32 v247, s31, v247\n"
        "ds_read_b128 a[144:147], v247 offset:0\n"
        "ds_read_b128 a[0:3], v248 offset:0\n"
        "ds_read_b128 a[148:151], v247 offset:4096\n"
        "ds_read_b128 a[4:7], v248 offset:3072\n"
        "v_mfma_f32_16x16x32_bf16 v[80:83], v[132:135], v[92:95], 0\n"
        "ds_read_b128 a[152:155], v247 offset:8192\n"
        "ds_read_b128 a[8:11], v248 offset:6144\n"
        "s_mul_i32 s45, s46, 786432\n"
        "v_mov_b64 v[124:125], 0\n"
        "v_mfma_f32_16x16x32_bf16 v[80:83], v[128:131], v[88:91], v[80:83]\n"
        "ds_read_b128 a[156:159], v247 offset:12288\n"
        "ds_read_b128 a[12:15], v248 offset:9216\n"
        "s_sub_u32 s37, s37, s45\n"
        "v_cvt_pk_bf16_f32 v232, v176, v180\n"
        "v_cvt_pk_bf16_f32 v233, v184, v172\n"
        "v_cvt_pk_bf16_f32 v234, v212, v220\n"
        "v_mfma_f32_16x16x32_bf16 v[84:87], v[136:139], v[88:91], 0\n"
        "buffer_store_dwordx3 v[232:234], v250, s[16:19], s38 offen offset:0 sc1\n"
        "ds_read_b128 a[160:163], v247 offset:16384\n"
        "s_cmp_eq_u32 s46, 0\n"
        "s_cselect_b32 s43, s43, s44\n"
        "v_mfma_f32_16x16x32_bf16 v[84:87], v[140:143], v[92:95], v[84:87]\n"
        "ds_read_b128 a[16:19], v248 offset:12288\n"
        "ds_read_b128 a[164:167], v247 offset:20480\n"
        "ds_read_b128 a[20:23], v248 offset:15360\n"
        "s_add_u32 s28, s37, 786432\n"
        "v_cvt_pk_bf16_f32 v232, v177, v181\n"
        "v_cvt_pk_bf16_f32 v233, v185, v173\n"
        "v_cvt_pk_bf16_f32 v234, v213, v221\n"
        "v_mfma_f32_16x16x32_bf16 v[96:99], v[148:151], v[92:95], 0\n"
        "ds_read_b128 a[44:47], v254 offset:15376\n"
        "ds_read_b128 a[40:43], v254 offset:12304\n"
        "s_cmp_eq_u32 s28, 100663296\n"
        "s_cselect_b32 s28, 0, s28\n"
        "v_mfma_f32_16x16x32_bf16 v[96:99], v[144:147], v[88:91], v[96:99]\n"
        "buffer_store_dwordx3 v[232:234], v250, s[16:19], s38 offen offset:384 sc1\n"
        "s_lshl_b32 s29, s42, 2\n"
        "v_smfmac_f32_16x16x64_bf16 v[124:127], a[252:255], v[88:95], v237\n"
        "s_waitcnt lgkmcnt(12)\n"
        "ds_read_b128 a[36:39], v254 offset:9232\n"
        "v_cvt_pk_bf16_f32 v232, v178, v182\n"
        "v_cvt_pk_bf16_f32 v233, v186, v174\n"
        "v_cvt_pk_bf16_f32 v234, v214, v222\n"
        "v_mfma_f32_16x16x32_bf16 v[0:3], a[144:147], a[0:3], 0\n"
        "s_waitcnt lgkmcnt(11)\n"
        "ds_read_b128 a[32:35], v254 offset:6160\n"
        "s_add_u32 s29, s29, 147456\n"
        "ds_read_b128 a[28:31], v254 offset:3088\n"
        "v_mfma_f32_16x16x32_bf16 v[0:3], a[148:151], a[4:7], v[0:3]\n"
        "s_waitcnt lgkmcnt(11)\n"
        "buffer_store_dwordx3 v[232:234], v250, s[16:19], s38 offen offset:768 sc1\n"
        "v_mfma_f32_16x16x32_bf16 v[0:3], a[152:155], a[8:11], v[0:3]\n"
        "s_waitcnt lgkmcnt(9)\n"
        "ds_read_b128 a[24:27], v254 offset:16\n"
        "ds_read_b128 a[48:51], v248 offset:32\n"
        "v_cvt_pk_bf16_f32 v232, v179, v183\n"
        "v_cvt_pk_bf16_f32 v233, v187, v175\n"
        "v_cvt_pk_bf16_f32 v234, v215, v223\n"
        "v_mfma_f32_16x16x32_bf16 v[0:3], a[156:159], a[12:15], v[0:3]\n"
        "s_waitcnt lgkmcnt(9)\n"
        "ds_read_b128 a[52:55], v248 offset:3104\n"
        "ds_read_b128 a[56:59], v248 offset:6176\n"
        "v_add_f32 v125, v124, v125\n"
        "v_mfma_f32_16x16x32_bf16 v[0:3], a[160:163], a[16:19], v[0:3]\n"
        "s_waitcnt lgkmcnt(9)\n"
        "buffer_store_dwordx3 v[232:234], v250, s[16:19], s38 offen offset:1152 sc1\n"
        "v_mfma_f32_16x16x32_bf16 v[0:3], a[164:167], a[20:23], v[0:3]\n"
        "s_waitcnt lgkmcnt(8)\n"
        "ds_read_b128 a[60:63], v248 offset:9248\n"
        "ds_read_b128 a[64:67], v248 offset:12320\n"
        "v_rcp_f32 v124, v125\n"
        "v_cvt_pk_bf16_f32 v232, v196, v200\n"
        "v_mul_f32 v80, v124, v80\n"
        "v_mfma_f32_16x16x32_bf16 v[4:7], a[164:167], a[44:47], 0\n"
        "s_waitcnt lgkmcnt(9)\n"
        "ds_read_b128 a[68:71], v248 offset:15392\n"
        "ds_read_b128 a[92:95], v254 offset:15408\n"
        "v_mul_f32 v81, v124, v81\n"
        "v_mul_f32 v82, v124, v82\n"
        "v_mul_f32 v83, v124, v83\n"
        "v_cvt_pk_bf16_f32 v233, v204, v192\n"
        "v_mfma_f32_16x16x32_bf16 v[4:7], a[160:163], a[40:43], v[4:7]\n"
        "s_waitcnt lgkmcnt(10)\n"
        "ds_read_b128 a[88:91], v254 offset:12336\n"
        "ds_read_b128 a[84:87], v254 offset:9264\n"
        "v_cvt_pk_bf16_f32 v234, v216, v224\n"
        "v_cvt_pk_bf16_f32 v80, v80, v81\n"
        "v_cvt_pk_bf16_f32 v81, v82, v83\n"
        "v_mfma_f32_16x16x32_bf16 v[4:7], a[156:159], a[36:39], v[4:7]\n"
        "s_waitcnt lgkmcnt(11)\n"
        "buffer_store_dwordx3 v[232:234], v250, s[16:19], s39 offen offset:0 sc1\n"
        "v_mul_f32 v84, v124, v84\n"
        "v_mfma_f32_16x16x32_bf16 v[4:7], a[152:155], a[32:35], v[4:7]\n"
        "s_waitcnt lgkmcnt(10)\n"
        "ds_read_b128 a[80:83], v254 offset:6192\n"
        "ds_read_b128 a[76:79], v254 offset:3120\n"
        "v_mul_f32 v85, v124, v85\n"
        "v_mul_f32 v86, v124, v86\n"
        "v_mul_f32 v87, v124, v87\n"
        "v_cvt_pk_bf16_f32 v232, v197, v201\n"
        "v_mfma_f32_16x16x32_bf16 v[4:7], a[148:151], a[28:31], v[4:7]\n"
        "s_waitcnt lgkmcnt(11)\n"
        "ds_read_b128 a[72:75], v254 offset:48\n"
        "ds_read_b128 a[96:99], v248 offset:64\n"
        "v_cvt_pk_bf16_f32 v233, v205, v193\n"
        "v_cvt_pk_bf16_f32 v234, v217, v225\n"
        "v_cvt_pk_bf16_f32 v82, v84, v85\n"
        "v_cvt_pk_bf16_f32 v83, v86, v87\n"
        "v_mfma_f32_16x16x32_bf16 v[4:7], a[144:147], a[24:27], v[4:7]\n"
        "s_waitcnt lgkmcnt(12)\n"
        "buffer_store_dwordx3 v[232:234], v250, s[16:19], s39 offen offset:384 sc1\n"
        "v_mul_f32 v96, v124, v96\n"
        "v_mfma_f32_16x16x32_bf16 v[8:11], a[144:147], a[48:51], 0\n"
        "s_waitcnt lgkmcnt(11)\n"
        "ds_read_b128 a[100:103], v248 offset:3136\n"
        "ds_read_b128 a[104:107], v248 offset:6208\n"
        "v_mul_f32 v97, v124, v97\n"
        "v_mul_f32 v98, v124, v98\n"
        "v_mul_f32 v99, v124, v99\n"
        "v_cvt_pk_bf16_f32 v232, v198, v202\n"
        "v_mfma_f32_16x16x32_bf16 v[8:11], a[148:151], a[52:55], v[8:11]\n"
        "s_waitcnt lgkmcnt(12)\n"
        "ds_read_b128 a[108:111], v248 offset:9280\n"
        "ds_read_b128 a[112:115], v248 offset:12352\n"
        "v_cvt_pk_bf16_f32 v233, v206, v194\n"
        "v_cvt_pk_bf16_f32 v234, v218, v226\n"
        "v_cvt_pk_bf16_f32 v96, v96, v97\n"
        "v_cvt_pk_bf16_f32 v97, v98, v99\n"
        "v_mfma_f32_16x16x32_bf16 v[8:11], a[152:155], a[56:59], v[8:11]\n"
        "s_waitcnt lgkmcnt(13)\n"
        "ds_write_b128 v245, v[80:83] offset:768\n"
        "v_mov_b32 v100, s29\n"
        "v_mfma_f32_16x16x32_bf16 v[8:11], a[156:159], a[60:63], v[8:11]\n"
        "s_waitcnt lgkmcnt(13)\n"
        "ds_read_b128 a[116:119], v248 offset:15424\n"
        "ds_read_b128 a[140:143], v254 offset:15440\n"
        "v_max3_u32 v236, v236, v124, v125\n"
        "v_mfma_f32_16x16x32_bf16 v[8:11], a[160:163], a[64:67], v[8:11]\n"
        "s_waitcnt lgkmcnt(14)\n"
        "buffer_store_dwordx3 v[232:234], v250, s[16:19], s39 offen offset:768 sc1\n"
        "v_mfma_f32_16x16x32_bf16 v[8:11], a[164:167], a[68:71], v[8:11]\n"
        "s_waitcnt lgkmcnt(13)\n"
        "ds_read_b128 a[136:139], v254 offset:12368\n"
        "ds_read_b128 a[132:135], v254 offset:9296\n"
        "v_cvt_pk_bf16_f32 v232, v199, v203\n"
        "v_cvt_pk_bf16_f32 v233, v207, v195\n"
        "v_cvt_pk_bf16_f32 v234, v219, v227\n"
        "v_cmp_lt_u32 vcc, s24, v236\n"
        "v_mfma_f32_16x16x32_bf16 v[12:15], a[164:167], a[92:95], 0\n"
        "s_waitcnt lgkmcnt(14)\n"
        "ds_read_b128 a[128:131], v254 offset:6224\n"
        "s_cmp_lg_u64 vcc, 0\n"
        "s_addc_u32 s25, s25, s25\n"
        "v_mfma_f32_16x16x32_bf16 v[12:15], a[160:163], a[88:91], v[12:15]\n"
        "s_waitcnt lgkmcnt(14)\n"
        "buffer_store_dwordx3 v[232:234], v250, s[16:19], s39 offen offset:1152 sc1\n"
        "v_mov_b32 v101, s25\n"
        "v_mfma_f32_16x16x32_bf16 v[12:15], a[156:159], a[84:87], v[12:15]\n"
        "s_waitcnt lgkmcnt(13)\n"
        "ds_write_b64 v246, v[96:97] offset:768\n"
        "v_mfma_f32_16x16x32_bf16 v[12:15], a[152:155], a[80:83], v[12:15]\n"
        "s_waitcnt lgkmcnt(13)\n"
        "ds_read_b128 a[124:127], v254 offset:3152\n"
        "ds_read_b128 a[120:123], v254 offset:80\n"
        "v_mfma_f32_16x16x32_bf16 v[12:15], a[148:151], a[76:79], v[12:15]\n"
        "s_waitcnt lgkmcnt(14)\n"
        "v_mov_b32 v102, 147456\n"
        "v_cvt_pk_bf16_f32 v48, v0, v4\n"
        "v_mfma_f32_16x16x32_bf16 v[12:15], a[144:147], a[72:75], v[12:15]\n"
        "ds_write_b32 v100, v101\n"
        "s_waitcnt lgkmcnt(0)\n"
        "s_barrier\n"
        "v_mfma_f32_16x16x32_bf16 v[16:19], a[144:147], a[96:99], 0\n"
        "ds_read_b128 a[168:171], v247 offset:512\n"
        "ds_read_b128 a[172:175], v247 offset:4608\n"
        "v_mfma_f32_16x16x32_bf16 v[16:19], a[148:151], a[100:103], v[16:19]\n"
        "ds_read_b128 a[176:179], v247 offset:8704\n"
        "ds_read_b128 a[180:183], v247 offset:12800\n"
        "v_mfma_f32_16x16x32_bf16 v[16:19], a[152:155], a[104:107], v[16:19]\n"
        "ds_read_b128 a[184:187], v247 offset:16896\n"
        "ds_read_b128 a[188:191], v247 offset:20992\n"
        "v_mfma_f32_16x16x32_bf16 v[16:19], a[156:159], a[108:111], v[16:19]\n"
        "v_cvt_pk_bf16_f32 v49, v8, v12\n"
        "v_cvt_pk_bf16_f32 v52, v1, v5\n"
        "v_mfma_f32_16x16x32_bf16 v[16:19], a[160:163], a[112:115], v[16:19]\n"
        "v_cvt_pk_bf16_f32 v53, v9, v13\n"
        "v_cvt_pk_bf16_f32 v56, v2, v6\n"
        "v_mfma_f32_16x16x32_bf16 v[16:19], a[164:167], a[116:119], v[16:19]\n"
        "v_cvt_pk_bf16_f32 v57, v10, v14\n"
        "v_cvt_pk_bf16_f32 v60, v3, v7\n"
        "v_mfma_f32_16x16x32_bf16 v[20:23], a[164:167], a[140:143], 0\n"
        "v_cvt_pk_bf16_f32 v61, v11, v15\n"
        "v_mfma_f32_16x16x32_bf16 v[20:23], a[160:163], a[136:139], v[20:23]\n"
        "ds_read_b128 v[104:107], v102\n"
        "v_mfma_f32_16x16x32_bf16 v[20:23], a[156:159], a[132:135], v[20:23]\n"
        "s_add_u32 s38, s37, s43\n"
        "v_mfma_f32_16x16x32_bf16 v[20:23], a[152:155], a[128:131], v[20:23]\n"
        "s_add_u32 s39, s28, s43\n"
        "v_mfma_f32_16x16x32_bf16 v[20:23], a[148:151], a[124:127], v[20:23]\n"
        "v_mfma_f32_16x16x32_bf16 v[20:23], a[144:147], a[120:123], v[20:23]\n"
        "s_waitcnt lgkmcnt(6)\n"
        "v_mfma_f32_16x16x32_bf16 v[24:27], a[168:171], a[0:3], 0\n"
        "s_waitcnt lgkmcnt(5)\n"
        "v_mfma_f32_16x16x32_bf16 v[24:27], a[172:175], a[4:7], v[24:27]\n"
        "s_waitcnt lgkmcnt(4)\n"
        "v_mfma_f32_16x16x32_bf16 v[24:27], a[176:179], a[8:11], v[24:27]\n"
        "s_waitcnt lgkmcnt(3)\n"
        "v_mfma_f32_16x16x32_bf16 v[24:27], a[180:183], a[12:15], v[24:27]\n"
        "s_waitcnt lgkmcnt(2)\n"
        "v_mfma_f32_16x16x32_bf16 v[24:27], a[184:187], a[16:19], v[24:27]\n"
        "s_waitcnt lgkmcnt(1)\n"
        "v_mfma_f32_16x16x32_bf16 v[24:27], a[188:191], a[20:23], v[24:27]\n"
        "v_mfma_f32_16x16x32_bf16 v[28:31], a[188:191], a[44:47], 0\n"
        "s_nop 0\n"
        "v_cvt_pk_bf16_f32 v50, v16, v20\n"
        "v_cvt_pk_bf16_f32 v54, v17, v21\n"
        "v_mfma_f32_16x16x32_bf16 v[28:31], a[184:187], a[40:43], v[28:31]\n"
        "v_cvt_pk_bf16_f32 v58, v18, v22\n"
        "v_cvt_pk_bf16_f32 v62, v19, v23\n"
        "v_mfma_f32_16x16x32_bf16 v[28:31], a[180:183], a[36:39], v[28:31]\n"
        "v_mfma_f32_16x16x32_bf16 v[28:31], a[176:179], a[32:35], v[28:31]\n"
        "s_waitcnt lgkmcnt(0)\n"
        "v_or3_b32 v104, v104, v105, v106\n"
        "v_or_b32 v104, v104, v107\n"
        "v_mfma_f32_16x16x32_bf16 v[28:31], a[172:175], a[28:31], v[28:31]\n"
        "s_nop 0\n"
        "v_readfirstlane_b32 s25, v104\n"
        "v_mfma_f32_16x16x32_bf16 v[28:31], a[168:171], a[24:27], v[28:31]\n"
        "v_mfma_f32_16x16x32_bf16 v[32:35], a[168:171], a[48:51], 0\n"
        "v_mfma_f32_16x16x32_bf16 v[32:35], a[172:175], a[52:55], v[32:35]\n"
        "buffer_store_dwordx3 v[48:50], v250, s[16:19], s38 offen offset:0 sc1\n"
        "v_mfma_f32_16x16x32_bf16 v[32:35], a[176:179], a[56:59], v[32:35]\n"
        "v_mfma_f32_16x16x32_bf16 v[32:35], a[180:183], a[60:63], v[32:35]\n"
        "v_mfma_f32_16x16x32_bf16 v[32:35], a[184:187], a[64:67], v[32:35]\n"
        "v_mfma_f32_16x16x32_bf16 v[32:35], a[188:191], a[68:71], v[32:35]\n"
        "v_mfma_f32_16x16x32_bf16 v[36:39], a[188:191], a[92:95], 0\n"
        "buffer_store_dwordx3 v[52:54], v250, s[16:19], s38 offen offset:384 sc1\n"
        "v_mfma_f32_16x16x32_bf16 v[36:39], a[184:187], a[88:91], v[36:39]\n"
        "v_cvt_pk_bf16_f32 v64, v24, v28\n"
        "v_cvt_pk_bf16_f32 v68, v25, v29\n"
        "v_mfma_f32_16x16x32_bf16 v[36:39], a[180:183], a[84:87], v[36:39]\n"
        "v_cvt_pk_bf16_f32 v72, v26, v30\n"
        "v_cvt_pk_bf16_f32 v76, v27, v31\n"
        "v_mfma_f32_16x16x32_bf16 v[36:39], a[176:179], a[80:83], v[36:39]\n"
        "v_mfma_f32_16x16x32_bf16 v[36:39], a[172:175], a[76:79], v[36:39]\n"
        "v_mfma_f32_16x16x32_bf16 v[36:39], a[168:171], a[72:75], v[36:39]\n"
        "buffer_store_dwordx3 v[56:58], v250, s[16:19], s38 offen offset:768 sc1\n"
        "v_mfma_f32_16x16x32_bf16 v[40:43], a[168:171], a[96:99], 0\n"
        "v_mfma_f32_16x16x32_bf16 v[40:43], a[172:175], a[100:103], v[40:43]\n"
        "v_mfma_f32_16x16x32_bf16 v[40:43], a[176:179], a[104:107], v[40:43]\n"
        "v_mfma_f32_16x16x32_bf16 v[40:43], a[180:183], a[108:111], v[40:43]\n"
        "v_mfma_f32_16x16x32_bf16 v[40:43], a[184:187], a[112:115], v[40:43]\n"
        "buffer_store_dwordx3 v[60:62], v250, s[16:19], s38 offen offset:1152 sc1\n"
        "v_mfma_f32_16x16x32_bf16 v[40:43], a[188:191], a[116:119], v[40:43]\n"
        "v_cvt_pk_bf16_f32 v65, v32, v36\n"
        "v_cvt_pk_bf16_f32 v69, v33, v37\n"
        "v_mfma_f32_16x16x32_bf16 v[44:47], a[188:191], a[140:143], 0\n"
        "v_cvt_pk_bf16_f32 v73, v34, v38\n"
        "v_cvt_pk_bf16_f32 v77, v35, v39\n"
        "v_mfma_f32_16x16x32_bf16 v[44:47], a[184:187], a[136:139], v[44:47]\n"
        "v_mfma_f32_16x16x32_bf16 v[44:47], a[180:183], a[132:135], v[44:47]\n"
        "v_mfma_f32_16x16x32_bf16 v[44:47], a[176:179], a[128:131], v[44:47]\n"
        "v_mfma_f32_16x16x32_bf16 v[44:47], a[172:175], a[124:127], v[44:47]\n"
        "v_mfma_f32_16x16x32_bf16 v[44:47], a[168:171], a[120:123], v[44:47]\n"
        "s_nop 2\n"
        "s_nop 4\n"
        "v_cvt_pk_bf16_f32 v66, v40, v44\n"
        "buffer_store_dwordx3 v[64:66], v250, s[16:19], s39 offen offset:0 sc1\n"
        "v_cvt_pk_bf16_f32 v70, v41, v45\n"
        "buffer_store_dwordx3 v[68:70], v250, s[16:19], s39 offen offset:384 sc1\n"
        "v_cvt_pk_bf16_f32 v74, v42, v46\n"
        "buffer_store_dwordx3 v[72:74], v250, s[16:19], s39 offen offset:768 sc1\n"
        "v_cvt_pk_bf16_f32 v78, v43, v47\n"
        "buffer_store_dwordx3 v[76:78], v250, s[16:19], s39 offen offset:1152 sc1\n"
        "s_cmp_lg_u32 s25, 0\n"
        "s_cbranch_scc1 30f\n"
        "s_endpgm\n"
        "30:\n"
        : "={s25}"(flg), "={s42}"(w), "+{s[0:1]}"(ka), "+{s2}"(bid), "+{v0}"(tid)
        : "s"(ldsb)
        : "v1", "v2", "v3", "v4", "v5", "v6", "v7", "v8", "v9", "v10", "v11", "v12", "v13", "v14", "v15", "v16", "v17", "v18", "v19", "v20", "v21", "v22", "v23", "v24", "v25", "v26", "v27", "v28", "v29", "v30", "v31", "v32", "v33", "v34", "v35", "v36", "v37", "v38", "v39", "v40", "v41", "v42", "v43", "v44", "v45", "v46", "v47", "v48", "v49", "v50", "v51", "v52", "v53", "v54", "v55", "v56", "v57", "v58", "v59", "v60", "v61", "v62", "v63", "v64", "v65", "v66", "v67", "v68", "v69", "v70", "v71", "v72", "v73", "v74", "v75", "v76", "v77", "v78", "v79", "v80", "v81", "v82", "v83", "v84", "v85", "v86", "v87", "v88", "v89", "v90", "v91", "v92", "v93", "v94", "v95", "v96", "v97", "v98", "v99", "v100", "v101", "v102", "v103", "v104", "v105", "v106", "v107", "v108", "v109", "v110", "v111", "v112", "v113", "v114", "v115", "v116", "v117", "v118", "v119", "v120", "v121", "v122", "v123", "v124", "v125", "v126", "v127", "v128", "v129", "v130", "v131", "v132", "v133", "v134", "v135", "v136", "v137", "v138", "v139", "v140", "v141", "v142", "v143", "v144", "v145", "v146", "v147", "v148", "v149", "v150", "v151", "v152", "v153", "v154", "v155", "v156", "v157", "v158", "v159", "v160", "v161", "v162", "v163", "v164", "v165", "v166", "v167", "v168", "v169", "v170", "v171", "v172", "v173", "v174", "v175", "v176", "v177", "v178", "v179", "v180", "v181", "v182", "v183", "v184", "v185", "v186", "v187", "v188", "v189", "v190", "v191", "v192", "v193", "v194", "v195", "v196", "v197", "v198", "v199", "v200", "v201", "v202", "v203", "v204", "v205", "v206", "v207", "v208", "v209", "v210", "v211", "v212", "v213", "v214", "v215", "v216", "v217", "v218", "v219", "v220", "v221", "v222", "v223", "v224", "v225", "v226", "v227", "v228", "v229", "v230", "v231", "v232", "v233", "v234", "v235", "v236", "v237", "v238", "v239", "v240", "v241", "v242", "v243", "v244", "v245", "v246", "v247", "v248", "v249", "v250", "v251", "v252", "v253", "v254", "v255", "a0", "a1", "a2", "a3", "a4", "a5", "a6", "a7", "a8", "a9", "a10", "a11", "a12", "a13", "a14", "a15", "a16", "a17", "a18", "a19", "a20", "a21", "a22", "a23", "a24", "a25", "a26", "a27", "a28", "a29", "a30", "a31", "a32", "a33", "a34", "a35", "a36", "a37", "a38", "a39", "a40", "a41", "a42", "a43", "a44", "a45", "a46", "a47", "a48", "a49", "a50", "a51", "a52", "a53", "a54", "a55", "a56", "a57", "a58", "a59", "a60", "a61", "a62", "a63", "a64", "a65", "a66", "a67", "a68", "a69", "a70", "a71", "a72", "a73", "a74", "a75", "a76", "a77", "a78", "a79", "a80", "a81", "a82", "a83", "a84", "a85", "a86", "a87", "a88", "a89", "a90", "a91", "a92", "a93", "a94", "a95", "a96", "a97", "a98", "a99", "a100", "a101", "a102", "a103", "a104", "a105", "a106", "a107", "a108", "a109", "a110", "a111", "a112", "a113", "a114", "a115", "a116", "a117", "a118", "a119", "a120", "a121", "a122", "a123", "a124", "a125", "a126", "a127", "a128", "a129", "a130", "a131", "a132", "a133", "a134", "a135", "a136", "a137", "a138", "a139", "a140", "a141", "a142", "a143", "a144", "a145", "a146", "a147", "a148", "a149", "a150", "a151", "a152", "a153", "a154", "a155", "a156", "a157", "a158", "a159", "a160", "a161", "a162", "a163", "a164", "a165", "a166", "a167", "a168", "a169", "a170", "a171", "a172", "a173", "a174", "a175", "a176", "a177", "a178", "a179", "a180", "a181", "a182", "a183", "a184", "a185", "a186", "a187", "a188", "a189", "a190", "a191", "a192", "a193", "a194", "a195", "a196", "a197", "a198", "a199", "a200", "a201", "a202", "a203", "a204", "a205", "a206", "a207", "a208", "a209", "a210", "a211", "a212", "a213", "a214", "a215", "a216", "a217", "a218", "a219", "a220", "a221", "a222", "a223", "a224", "a225", "a226", "a227", "a228", "a229", "a230", "a231", "a232", "a233", "a234", "a235", "a236", "a237", "a238", "a239", "a240", "a241", "a242", "a243", "a244", "a245", "a246", "a247", "a248", "a249", "a250", "a251", "a252", "a253", "a254", "a255", "s8", "s9", "s10", "s11", "s12", "s13", "s14", "s15", "s16", "s17", "s18", "s19", "s20", "s21", "s22", "s23", "s24", "s26", "s27", "s28", "s29", "s30", "s31", "s34", "s35", "s36", "s37", "s38", "s39", "s40", "s41", "s43", "s44", "s45", "s46", "s47", "s48", "s49", "s50", "s51", "s52", "s53", "s54", "s55", "s56", "s57", "s58", "s59", "s60", "s61", "s62", "s63", "s64", "s65", "s66", "s67", "s68", "s69", "s70", "s71", "s72", "s73", "s74", "s75", "s76", "s77", "s78", "s79", "s80", "s81", "s82", "s83", "vcc", "scc", "memory");
    if (flg) {
        unsigned ln;
        asm volatile("s_waitcnt vmcnt(0)\n v_mbcnt_lo_u32_b32 %0, -1, 0\n v_mbcnt_hi_u32_b32 %0, -1, %0" : "=v"(ln) :: "memory");
        const unsigned t = w * 64u + ln, wx = bid & 63u, wy0 = bid >> 6;
        typedef const char* __attribute__((address_space(4))) const* kargp;
        const char *x = ((kargp)ka)[0], *wqkv = ((kargp)ka)[1], *wo = ((kargp)ka)[2], *rpb = ((kargp)ka)[3], *rpi = ((kargp)ka)[4],
                   *mask = ((kargp)ka)[5];
        char* out = (char*)((kargp)ka)[6];
        unsigned short* xs = (unsigned short*)smem;
        unsigned short* qs = xs + 64 * 192;
        const unsigned short* W1 = (const unsigned short*)wqkv;
        const unsigned short* W2 = (const unsigned short*)wo;
        for (unsigned i = 0; i < 16u; ++i) {
            if (!((flg >> (15u - i)) & 1u)) continue;
            const unsigned wy = wy0 + 4u * i;
            __syncthreads();
            for (unsigned e = t; e < 64u * 96u; e += 256u) {
                const unsigned tok = e / 96u, c2 = e % 96u;
                const unsigned pix = ((8u * wy + 4u + (tok >> 3)) & 511u) * 512u + ((8u * wx + 4u + (tok & 7u)) & 511u);
                ((unsigned*)xs)[e] = ((const unsigned*)x)[pix * 96u + c2];
            }
            __syncthreads();
            for (unsigned col = t; col < 576u; col += 256u)
                for (unsigned tb = 0; tb < 64u; tb += 8u) {
                    float a[8] = {0.f, 0.f, 0.f, 0.f, 0.f, 0.f, 0.f, 0.f};
                    for (unsigned c = 0; c < 192u; ++c) {
                        const float wt = bf2f(W1[c * 576u + col]);
#pragma unroll
                        for (int j = 0; j < 8; ++j) a[j] += bf2f(xs[(tb + j) * 192u + c]) * wt;
                    }
#pragma unroll
                    for (int j = 0; j < 8; ++j) qs[(tb + j) * 578u + col] = f2bf(a[j]);
                }
            __syncthreads();
            {
                float qv[48], o[48];
#pragma unroll
                for (int d = 0; d < 48; ++d) { qv[d] = bf2f(qs[ln * 578u + w * 48u + d]); o[d] = 0.f; }
                const unsigned short* mrow = (const unsigned short*)mask + (size_t)(wy * 64u + wx) * 4096u + ln * 64u;
                float mx = -__builtin_inff(), l = 0.f;
                for (int pass = 0; pass < 2; ++pass)
                    for (unsigned k = 0; k < 64u; ++k) {
                        float s = 0.f;
#pragma unroll
                        for (int d = 0; d < 48; ++d) s += qv[d] * bf2f(qs[k * 578u + 192u + w * 48u + d]);
                        s = s * 0.14433756729740643f + bf2f(((const unsigned short*)rpb)[((const unsigned*)rpi)[ln * 64u + k] * 4u + w]) + bf2f(mrow[k]);
                        if (pass == 0) mx = fmaxf(mx, s);
                        else {
                            const float p = __expf(s - mx);
                            l += p;
#pragma unroll
                            for (int d = 0; d < 48; ++d) o[d] += p * bf2f(qs[k * 578u + 384u + w * 48u + d]);
                        }
                    }
#pragma unroll
                for (int d = 0; d < 48; ++d) xs[ln * 192u + w * 48u + d] = f2bf(o[d] / l);
            }
            __syncthreads();
            if (t < 192u)
                for (unsigned tb = 0; tb < 64u; tb += 8u) {
                    float a[8] = {0.f, 0.f, 0.f, 0.f, 0.f, 0.f, 0.f, 0.f};
                    for (unsigned c = 0; c < 192u; ++c) {
                        const float wt = bf2f(W2[c * 192u + t]);
#pragma unroll
                        for (int j = 0; j < 8; ++j) a[j] += bf2f(xs[(tb + j) * 192u + c]) * wt;
                    }
                    const unsigned row = (8u * wy + 4u + (tb >> 3)) & 511u;
#pragma unroll
                    for (int j = 0; j < 8; ++j)
                        ((unsigned short*)out)[(row * 512u + ((8u * wx + 4u + j) & 511u)) * 192u + t] = f2bf(a[j]);
                }
        }
    }
}


extern "C" void kernel_launch(void* const* d_in, const int* in_sizes, int n_in, void* d_out, int out_size, void* d_ws,
                              size_t ws_size, hipStream_t stream) {
    win_attn_kernel<<<256, 256, 0, stream>>>((const char*)d_in[0], (const char*)d_in[1], (const char*)d_in[2],
                                             (const char*)d_in[3], (const char*)d_in[4], (const char*)d_in[5],
                                             (char*)d_out, (char*)d_ws);

}
```

```cpp
#include <hip/hip_runtime.h>
#include <hip/hip_bf16.h>

typedef unsigned int u32x4 __attribute__((ext_vector_type(4)));

static __device__ __forceinline__ float bf2f(unsigned short u) { return __uint_as_float((unsigned)u << 16); }
static __device__ __forceinline__ unsigned short f2bf(float f) { return __builtin_bit_cast(unsigned short, (__bf16)f); }

__global__ __launch_bounds__(256, 1) __attribute__((aligned(4096))) void win_attn_kernel(const char* x_, const char* wqkv_, const char* wo_, const char* rpb_,
                                                          const char* rpi_, const char* mask_, char* out_, char* ws) {
    __shared__ __attribute__((aligned(16))) char smem[163840];
    const void* ka = (const void*)__builtin_amdgcn_kernarg_segment_ptr();
    unsigned bid = blockIdx.x, tid = threadIdx.x, flg, w;
    unsigned ldsb = (unsigned)(size_t)smem;
    asm volatile(
        "s_load_dwordx16 s[68:83], s[0:1], 0\n"
        "v_readfirstlane_b32 s42, v0\n"
        "v_and_b32 v1, 63, v0\n"
        "s_lshr_b32 s31, s2, 1\n"
        "s_lshr_b32 s42, s42, 6\n"
        "s_and_b32 s31, s31, 0x7c\n"
        "v_lshlrev_b32 v2, 4, v1\n"
        "s_add_u32 s31, s31, s42\n"
        "s_and_b32 s35, s31, 31\n"
        "s_min_u32 s35, s35, 31\n"
        "s_getpc_b64 s[66:67]\n"
        "v_lshl_or_b32 v10, s35, 10, v2\n"
        "global_load_dwordx4 v[60:63], v10, s[66:67]\n"
        "s_lshl_b32 s50, s31, 6\n"
        "s_mul_i32 s50, s50, 27\n"
        "v_min_u32 v136, 26, v1\n"
        "v_lshl_add_u32 v136, v136, 6, s50\n"
        "v_mul_u32_u24 v11, 171, v1\n"
        "v_lshrrev_b32 v4, 10, v11\n"
        "v_bfe_u32 v7, v11, 13, 1\n"
        "v_add_u32 v11, 10944, v11\n"
        "v_lshrrev_b32 v5, 10, v11\n"
        "v_bfe_u32 v8, v11, 13, 1\n"
        "v_add_u32 v11, 10944, v11\n"
        "v_lshrrev_b32 v6, 10, v11\n"
        "v_bfe_u32 v9, v11, 13, 1\n"
        "s_mul_i32 s40, s42, 3072\n"
        "s_mov_b32 s10, 100663296\n"
        "s_mov_b32 s11, 0x20000\n"
        "s_mov_b32 s58, 16384\n"
        "s_mov_b32 s59, 0x20000\n"
        "s_mov_b32 s62, 1800\n"
        "s_mov_b32 s63, 0x20000\n"
        "s_mov_b32 s22, 221184\n"
        "s_mov_b32 s23, 0x20000\n"
        "s_and_b32 s51, s42, 1\n"
        "v_lshlrev_b32 v134, 3, v0\n"
        "s_lshl_b32 s35, s51, 7\n"
        "v_and_b32 v10, 48, v1\n"
        "v_xor_b32 v131, s35, v2\n"
        "s_lshl_b32 s46, s42, 12\n"
        "v_xor_b32 v131, v131, v10\n"
        "v_xor_b32 v132, 64, v131\n"
        "s_and_b32 s31, s2, 63\n"
        "s_lshl_b32 s31, s31, 3\n"
        "s_add_u32 s31, s31, 4\n"
        "s_lshl_b32 s35, s42, 5\n"
        "s_movk_i32 s41, 384\n"
        "s_movk_i32 s45, -384\n"
        "v_lshrrev_b32 v10, 2, v4\n"
        "v_mad_i32_i24 v11, v10, s45, v2\n"
        "v_add_u32 v10, s31, v10\n"
        "v_lshl_or_b32 v242, v7, 4, s35\n"
        "v_and_b32 v10, 511, v10\n"
        "v_xor_b32 v11, v11, v242\n"
        "v_mad_u32_u24 v242, v10, s41, v11\n"
        "v_lshrrev_b32 v10, 2, v5\n"
        "v_mad_i32_i24 v11, v10, s45, v2\n"
        "v_add_u32 v10, s31, v10\n"
        "v_add_u32 v11, 1024, v11\n"
        "v_lshl_or_b32 v243, v8, 4, s35\n"
        "v_and_b32 v10, 511, v10\n"
        "v_xor_b32 v11, v11, v243\n"
        "v_mad_u32_u24 v243, v10, s41, v11\n"
        "v_lshrrev_b32 v10, 2, v6\n"
        "v_mad_i32_i24 v11, v10, s45, v2\n"
        "v_add_u32 v10, s31, v10\n"
        "v_add_u32 v11, 2048, v11\n"
        "v_lshl_or_b32 v244, v9, 4, s35\n"
        "v_and_b32 v10, 511, v10\n"
        "v_xor_b32 v11, v11, v244\n"
        "v_mad_u32_u24 v244, v10, s41, v11\n"
        "s_mul_i32 s43, s31, 384\n"
        "s_add_u32 s31, s31, 4\n"
        "s_and_b32 s31, s31, 511\n"
        "s_mul_i32 s44, s31, 384\n"
        "s_lshr_b32 s31, s2, 6\n"
        "s_lshl_b32 s31, s31, 3\n"
        "s_add_u32 s47, s31, s42\n"
        "s_add_u32 s47, s47, 68\n"
        "s_mul_i32 s47, s47, 196608\n"
        "s_lshl2_add_u32 s37, s51, s31\n"
        "s_add_u32 s37, s37, 4\n"
        "s_mul_i32 s37, s37, 196608\n"
        "s_mul_i32 s65, s42, 18432\n"
        "s_mov_b32 s14, 33554432\n"
        "s_mov_b32 s15, 0x20000\n"
        "s_mul_i32 s35, s42, 96\n"
        "s_movk_i32 s31, 1056\n"
        "s_movk_i32 s45, 4608\n"
        "v_add_u32 v3, s35, v2\n"
        "s_waitcnt lgkmcnt(0)\n"
        "s_mov_b64 s[60:61], s[74:75]\n"
        "s_mov_b64 s[20:21], s[70:71]\n"
        "s_mov_b64 s[56:57], s[76:77]\n"
        "s_mov_b64 s[8:9], s[68:69]\n"
        "buffer_load_dwordx2 v[144:145], v134, s[60:63], 0 offen\n"
        "s_add_u32 m0, s46, 122880\n"
        "s_mov_b32 s64, s46\n"
        "buffer_load_dword v136, s[20:23], 0 offen lds\n"
        "s_mov_b64 s[52:53], s[72:73]\n"
        "s_mov_b64 s[12:13], s[78:79]\n"
        "buffer_load_dwordx4 v131, s[56:59], s64 offen lds\n"
        "s_mov_b64 s[16:17], s[80:81]\n"
        "v_bfe_u32 v12, v4, 2, 1\n"
        "v_mad_u32_u24 v128, v4, s31, v3\n"
        "v_sub_u32 v12, v12, v7\n"
        "v_mad_i32_i24 v128, v12, s45, v128\n"
        "v_bfe_u32 v13, v5, 2, 1\n"
        "buffer_load_dwordx4 v131, s[56:59], s64 offen offset:1024 lds\n"
        "v_mad_u32_u24 v129, v5, s31, v3\n"
        "v_sub_u32 v13, v13, v8\n"
        "v_mad_i32_i24 v129, v13, s45, v129\n"
        "v_bfe_u32 v14, v6, 2, 1\n"
        "v_mad_u32_u24 v130, v6, s31, v3\n"
        "v_sub_u32 v14, v14, v9\n"
        "v_mad_i32_i24 v130, v14, s45, v130\n"
        "s_lshl_b32 s48, s42, 11\n"
        "s_lshl_b32 s36, s2, 13\n"
        "v_and_b32 v19, 16, v1\n"
        "buffer_load_dwordx4 v132, s[56:59], s64 offen offset:2048 lds\n"
        "s_add_u32 s36, s36, s48\n"
        "s_add_u32 s36, s36, 4194304\n"
        "s_add_u32 s48, s48, 155648\n"
        "s_lshl_b32 s35, s51, 6\n"
        "v_xor_b32 v249, v2, v19\n"
        "v_xor_b32 v249, s35, v249\n"
        "v_xor_b32 v253, 32, v249\n"
        "v_and_b32 v15, 15, v1\n"
        "v_lshrrev_b32 v16, 4, v1\n"
        "v_and_b32 v17, 12, v1\n"
        "buffer_load_dwordx4 v132, s[56:59], s64 offen offset:3072 lds\n"
        "v_and_b32 v18, 0xf0, v2\n"
        "v_add_u32 v17, v17, v15\n"
        "v_bfe_u32 v10, v1, 2, 3\n"
        "v_lshrrev_b32 v11, 5, v1\n"
        "v_lshl_or_b32 v10, v11, 4, v10\n"
        "v_and_b32 v255, 3, v1\n"
        "v_mul_u32_u24 v10, 96, v10\n"
        "v_lshl_add_u32 v255, v255, 3, s65\n"
        "v_add_u32 v255, v255, v10\n"
        "v_lshrrev_b32 v20, 1, v15\n"
        "s_add_u32 m0, s40, 73728\n"
        "s_sub_u32 s64, s47, 12582912\n"
        "buffer_load_dwordx4 v242, s[8:11], s64 offen lds\n"
        "v_xor_b32 v20, v20, v16\n"
        "v_lshlrev_b32 v20, 4, v20\n"
        "v_mad_u32_u24 v240, v17, s41, v20\n"
        "v_lshl_or_b32 v251, v17, 7, v20\n"
        "v_add_u32 v240, 73728, v240\n"
        "v_add_u32 v251, 155648, v251\n"
        "v_xor_b32 v241, 64, v240\n"
        "v_xor_b32 v252, 64, v251\n"
        "s_lshl_b32 s67, s42, 10\n"
        "s_add_u32 s67, s67, 139264\n"
        "s_add_u32 m0, s40, 74752\n"
        "s_nop 0\n"
        "buffer_load_dwordx4 v243, s[8:11], s64 offen lds\n"
        "v_lshlrev_b32 v135, 2, v0\n"
        "v_add_u32 v135, 139264, v135\n"
        "v_lshlrev_b32 v11, 5, v16\n"
        "v_xor_b32 v11, v11, v18\n"
        "v_lshl_or_b32 v11, v17, 8, v11\n"
        "v_add_u32 v140, 122880, v11\n"
        "v_xor_b32 v141, 16, v140\n"
        "v_xor_b32 v142, 128, v140\n"
        "v_xor_b32 v143, 144, v140\n"
        "v_lshrrev_b32 v10, 2, v15\n"
        "s_add_u32 m0, s40, 75776\n"
        "s_nop 0\n"
        "buffer_load_dwordx4 v244, s[8:11], s64 offen lds\n"
        "v_cmp_eq_u32 vcc, v10, v16\n"
        "v_mov_b32 v11, 0x3fb9\n"
        "v_mov_b32 v189, 0\n"
        "v_and_b32 v10, 48, v2\n"
        "v_cndmask_b32 v188, 0, v11, vcc\n"
        "v_lshlrev_b64 v[188:189], v10, v[188:189]\n"
        "v_cmp_gt_u32 vcc, 32, v1\n"
        "v_mov_b32 v11, 15957\n"
        "v_lshrrev_b32 v149, 2, v15\n"
        "v_lshrrev_b32 v151, 3, v19\n"
        "v_mov_b32 v148, 14702\n"
        "v_cndmask_b32 v11, v148, v11, vcc\n"
        "v_xor_b32 v151, v149, v151\n"
        "v_cmp_eq_u32 vcc, 0, v151\n"
        "v_mov_b32 v149, 0\n"
        "v_cndmask_b32 v148, 0, v11, vcc\n"
        "v_lshlrev_b64 v[148:149], v10, v[148:149]\n"
        "v_cmp_eq_u32 vcc, 1, v151\n"
        "v_mov_b32 v151, 0\n"
        "v_cndmask_b32 v150, 0, v11, vcc\n"
        "v_lshlrev_b64 v[150:151], v10, v[150:151]\n"
        "v_and_b32 v10, 8, v1\n"
        "v_add_u32 v10, v10, v15\n"
        "v_mul_u32_u24 v10, 96, v10\n"
        "v_add3_u32 v137, v10, v19, s65\n"
        "v_and_b32 v237, 1, v1\n"
        "v_mul_u32_u24 v237, 0xaaaa, v237\n"
        "v_mov_b64 v[170:171], v[188:189]\n"
        "v_mov_b64 v[210:211], v[188:189]\n"
        "v_mov_b64 v[228:229], v[188:189]\n"
        "v_add_u32 v237, 0x4444, v237\n"
        "v_mov_b32 v11, 0x3f803f80\n"
        "v_accvgpr_write_b32 a252, v11\n"
        "v_accvgpr_write_b32 a253, v11\n"
        "v_accvgpr_write_b32 a254, v11\n"
        "v_accvgpr_write_b32 a255, v11\n"
        "v_lshl_or_b32 v10, v16, 10, v18\n"
        "s_lshl_b32 s31, s42, 12\n"
        "s_add_u32 s31, s31, 73728\n"
        "s_lshl_b32 s35, s51, 9\n"
        "s_add_u32 s35, s35, 73728\n"
        "v_add_u32 v245, s31, v10\n"
        "s_sub_u32 s50, s35, s31\n"
        "v_lshrrev_b32 v10, 5, v1\n"
        "v_lshrrev_b32 v11, 1, v19\n"
        "s_sub_u32 s31, s48, 65536\n"
        "v_lshl_or_b32 v10, v10, 10, v18\n"
        "v_add3_u32 v246, v10, v11, s31\n"
        "v_mul_u32_u24 v10, 384, v16\n"
        "s_movk_i32 s45, 1728\n"
        "v_lshrrev_b32 v235, 2, v15\n"
        "v_lshrrev_b32 v254, 1, v15\n"
        "v_and_b32 v11, 1, v1\n"
        "v_mad_u32_u24 v235, v235, 48, v10\n"
        "v_and_b32 v248, 1, v254\n"
        "v_mad_u32_u24 v235, v248, s45, v235\n"
        "v_mul_u32_u24 v248, 36, v11\n"
        "v_lshlrev_b32 v11, 5, v11\n"
        "v_xor_b32 v254, v254, v248\n"
        "v_add3_u32 v235, v235, v11, s65\n"
        "v_mad_u32_u24 v254, v254, 48, v10\n"
        "v_add3_u32 v254, v254, 16, s65\n"
        "s_lshr_b32 s31, s42, 1\n"
        "s_mul_i32 s35, s31, 36864\n"
        "s_movk_i32 s45, 18048\n"
        "v_mad_u32_u24 v10, v15, 48, v10\n"
        "v_lshrrev_b32 v248, 3, v15\n"
        "v_mad_u32_u24 v10, v248, s45, v10\n"
        "v_add_u32 v248, s35, v10\n"
        "s_mul_i32 s49, s31, -192\n"
        "s_add_u32 s49, s49, 128\n"
        "s_mul_i32 s31, s31, 256\n"
        "v_mul_u32_u24 v10, 196608, v16\n"
        "v_mul_u32_u24 v11, 8, v15\n"
        "v_add3_u32 v250, v10, v11, s31\n"
        "s_lshl_b32 s35, s51, 6\n"
        "s_add_u32 s66, s31, s35\n"
        "s_lshl_b32 s35, s42, 5\n"
        "s_sub_u32 s35, s35, s66\n"
        "s_add_u32 s27, s35, 64\n"
        "s_movk_i32 s31, 288\n"
        "s_movk_i32 s35, 3072\n"
        "s_movk_i32 s41, 1536\n"
        "s_movk_i32 s45, 12288\n"
        "v_mad_u32_u24 v10, v4, s31, v2\n"
        "v_bfe_u32 v11, v10, 6, 1\n"
        "v_add_u32 v10, s66, v10\n"
        "v_mad_i32_i24 v10, v11, s27, v10\n"
        "v_lshrrev_b32 v11, 4, v4\n"
        "v_mad_i32_i24 v238, v12, s41, v10\n"
        "v_sub_u32 v7, v7, v11\n"
        "v_mad_u32_u24 v238, v11, s45, v238\n"
        "v_mad_i32_i24 v232, v7, s35, v10\n"
        "v_mad_u32_u24 v10, v5, s31, v2\n"
        "v_bfe_u32 v11, v10, 6, 1\n"
        "v_add_u32 v10, s66, v10\n"
        "v_mad_i32_i24 v10, v11, s27, v10\n"
        "v_lshrrev_b32 v11, 4, v5\n"
        "v_mad_i32_i24 v239, v13, s41, v10\n"
        "v_sub_u32 v8, v8, v11\n"
        "v_mad_u32_u24 v239, v11, s45, v239\n"
        "v_mad_i32_i24 v233, v8, s35, v10\n"
        "v_mad_u32_u24 v10, v6, s31, v2\n"
        "v_bfe_u32 v11, v10, 6, 1\n"
        "v_add_u32 v10, s66, v10\n"
        "v_mad_i32_i24 v10, v11, s27, v10\n"
        "v_lshrrev_b32 v11, 4, v6\n"
        "v_mad_i32_i24 v247, v14, s41, v10\n"
        "v_sub_u32 v9, v9, v11\n"
        "v_mad_u32_u24 v247, v11, s45, v247\n"
        "v_mad_i32_i24 v234, v9, s35, v10\n"
        "s_add_u32 s38, s37, s43\n"
        "s_mov_b32 s26, 0x3fb8aa3b\n"
        "s_mov_b32 s24, 0x5d800000\n"
        "s_mov_b32 s27, 14\n"
        "s_mov_b32 s28, 98304\n"
        "s_mov_b32 s29, 73728\n"
        "s_mov_b32 s30, 122880\n"
        "s_mov_b32 s45, s50\n"
        "s_mov_b32 s25, 0\n"
        "s_mov_b32 s54, 73728\n"
        "s_mov_b32 s55, 0x20000\n"
        "s_mov_b32 s18, 100663296\n"
        "s_mov_b32 s19, 0x20000\n"
        "s_mov_b32 s56, 36864\n"
        "s_mov_b32 s57, 73728\n"
        "s_mov_b32 s58, 110592\n"
        "s_mov_b32 s59, 147456\n"
        "s_mov_b32 s60, 184320\n"
        "s_mov_b32 s61, 18432\n"
        "s_mov_b32 s62, 55296\n"
        "s_mov_b32 s63, 12288\n"
        "s_mov_b32 s34, 49152\n"
        "s_add_u32 m0, s40, 86016\n"
        "s_sub_u32 s64, s47, 11796480\n"
        "buffer_load_dwordx4 v242, s[8:11], s64 offen lds\n"
        "s_waitcnt vmcnt(9)\n"
        "v_and_b32 v146, 0xffff0000, v144\n"
        "v_and_b32 v147, 0xffff0000, v145\n"
        "v_lshlrev_b32 v144, 16, v144\n"
        "v_lshlrev_b32 v145, 16, v145\n"
        "s_add_u32 m0, s40, 87040\n"
        "v_mul_f32 v146, s26, v146\n"
        "buffer_load_dwordx4 v243, s[8:11], s64 offen lds\n"
        "v_mul_f32 v147, s26, v147\n"
        "v_mul_f32 v144, s26, v144\n"
        "v_mul_f32 v145, s26, v145\n"
        "ds_write2st64_b32 v135, v144, v146 offset0:0 offset1:4\n"
        "ds_write2st64_b32 v135, v145, v147 offset0:8 offset1:12\n"
        "s_cmp_eq_u32 s42, 0\n"
        "s_cbranch_scc1 41f\n"
        "s_cmp_eq_u32 s42, 1\n"
        "s_cbranch_scc1 42f\n"
        "s_cmp_eq_u32 s42, 2\n"
        "s_cbranch_scc1 43f\n"
        "s_cmp_eq_u32 s42, 3\n"
        "s_cbranch_scc1 44f\n"
        "60:\n"
        "s_add_u32 m0, s40, 88064\n"
        "s_nop 0\n"
        "buffer_load_dwordx4 v244, s[8:11], s64 offen lds\n"
        "s_waitcnt lgkmcnt(0)\n"
        "s_waitcnt vmcnt(6)\n"
        "s_barrier\n"
        "s_add_u32 m0, s65, 0\n"
        "ds_read_b128 v[0:3], v140 offset:0\n"
        "buffer_load_dwordx4 v128, s[20:23], 0 offen lds\n"
        "buffer_load_dwordx4 v129, s[20:23], 0 offen offset:1024 lds\n"
        "ds_read_b128 v[4:7], v141 offset:0\n"
        "ds_read_b128 v[16:19], v140 offset:1024\n"
        "ds_read_b128 v[20:23], v141 offset:1024\n"
        "ds_read_b128 v[8:11], v142 offset:0\n"
        "ds_read_b128 v[24:27], v142 offset:1024\n"
        "ds_read_b128 v[12:15], v143 offset:0\n"
        "ds_read_b128 v[28:31], v143 offset:1024\n"
        "ds_read_b128 v[32:35], v140 offset:8192\n"
        "ds_read_b128 v[36:39], v141 offset:8192\n"
        "ds_read_b128 v[40:43], v142 offset:8192\n"
        "s_cmp_eq_u32 s42, 0\n"
        "s_cbranch_scc1 51f\n"
        "s_cmp_eq_u32 s42, 1\n"
        "s_cbranch_scc1 54f\n"
        "s_cmp_eq_u32 s42, 2\n"
        "s_cbranch_scc1 45f\n"
        "s_cmp_eq_u32 s42, 3\n"
        "s_cbranch_scc1 47f\n"
        "61:\n"
        "buffer_load_dwordx4 v130, s[20:23], 0 offen offset:2048 lds\n"
        "s_add_u32 m0, s65, 2688\n"
        "s_waitcnt lgkmcnt(10)\n"
        "v_lshl_add_u32 v0, v0, 2, s67\n"
        "buffer_load_dwordx4 v128, s[20:23], 0 offen offset:384 lds\n"
        "v_lshl_add_u32 v1, v1, 2, s67\n"
        "v_lshl_add_u32 v2, v2, 2, s67\n"
        "v_lshl_add_u32 v3, v3, 2, s67\n"
        "s_waitcnt lgkmcnt(9)\n"
        "v_lshl_add_u32 v4, v4, 2, s67\n"
        "v_lshl_add_u32 v5, v5, 2, s67\n"
        "v_lshl_add_u32 v6, v6, 2, s67\n"
        "v_lshl_add_u32 v7, v7, 2, s67\n"
        "s_waitcnt lgkmcnt(8)\n"
        "v_lshl_add_u32 v16, v16, 2, s67\n"
        "v_lshl_add_u32 v17, v17, 2, s67\n"
        "v_lshl_add_u32 v18, v18, 2, s67\n"
        "v_lshl_add_u32 v19, v19, 2, s67\n"
        "buffer_load_dwordx4 v129, s[20:23], 0 offen offset:1408 lds\n"
        "ds_read_b32 v0, v0\n"
        "ds_read_b32 v1, v1\n"
        "ds_read_b32 v2, v2\n"
        "ds_read_b32 v3, v3\n"
        "ds_read_b32 v4, v4\n"
        "ds_read_b32 v5, v5\n"
        "ds_read_b32 v6, v6\n"
        "ds_read_b32 v7, v7\n"
        "ds_read_b32 v16, v16\n"
        "ds_read_b32 v17, v17\n"
        "ds_read_b32 v18, v18\n"
        "ds_read_b32 v19, v19\n"
        "buffer_load_dwordx4 v130, s[20:23], 0 offen offset:2432 lds\n"
        "s_waitcnt lgkmcnt(15)\n"
        "v_lshl_add_u32 v20, v20, 2, s67\n"
        "v_lshl_add_u32 v21, v21, 2, s67\n"
        "v_lshl_add_u32 v22, v22, 2, s67\n"
        "v_lshl_add_u32 v23, v23, 2, s67\n"
        "v_lshl_add_u32 v8, v8, 2, s67\n"
        "v_lshl_add_u32 v9, v9, 2, s67\n"
        "v_lshl_add_u32 v10, v10, 2, s67\n"
        "v_lshl_add_u32 v11, v11, 2, s67\n"
        "v_lshl_add_u32 v24, v24, 2, s67\n"
        "v_lshl_add_u32 v25, v25, 2, s67\n"
        "v_lshl_add_u32 v26, v26, 2, s67\n"
        "v_lshl_add_u32 v27, v27, 2, s67\n"
        "ds_read_b32 v20, v20\n"
        "ds_read_b32 v21, v21\n"
        "ds_read_b32 v22, v22\n"
        "ds_read_b32 v23, v23\n"
        "ds_read_b32 v8, v8\n"
        "ds_read_b32 v9, v9\n"
        "ds_read_b32 v10, v10\n"
        "ds_read_b32 v11, v11\n"
        "ds_read_b32 v24, v24\n"
        "ds_read_b32 v25, v25\n"
        "ds_read_b32 v26, v26\n"
        "ds_read_b32 v27, v27\n"
        "s_add_u32 m0, s65, 6144\n"
        "v_lshl_add_u32 v12, v12, 2, s67\n"
        "buffer_load_dwordx4 v128, s[20:23], s56 offen lds\n"
        "v_lshl_add_u32 v13, v13, 2, s67\n"
        "v_lshl_add_u32 v14, v14, 2, s67\n"
        "v_lshl_add_u32 v15, v15, 2, s67\n"
        "v_lshl_add_u32 v28, v28, 2, s67\n"
        "v_lshl_add_u32 v29, v29, 2, s67\n"
        "v_lshl_add_u32 v30, v30, 2, s67\n"
        "v_lshl_add_u32 v31, v31, 2, s67\n"
        "s_waitcnt lgkmcnt(15)\n"
        "v_lshl_add_u32 v32, v32, 2, s67\n"
        "v_lshl_add_u32 v33, v33, 2, s67\n"
        "v_lshl_add_u32 v34, v34, 2, s67\n"
        "v_lshl_add_u32 v35, v35, 2, s67\n"
        "ds_read_b32 v12, v12\n"
        "ds_read_b32 v13, v13\n"
        "ds_read_b32 v14, v14\n"
        "ds_read_b32 v15, v15\n"
        "ds_read_b32 v28, v28\n"
        "ds_read_b32 v29, v29\n"
        "ds_read_b32 v30, v30\n"
        "ds_read_b32 v31, v31\n"
        "ds_read_b32 v32, v32\n"
        "ds_read_b32 v33, v33\n"
        "ds_read_b32 v34, v34\n"
        "ds_read_b32 v35, v35\n"
        "buffer_load_dwordx4 v129, s[20:23], s56 offen offset:1024 lds\n"
        "v_lshl_add_u32 v36, v36, 2, s67\n"
        "v_lshl_add_u32 v37, v37, 2, s67\n"
        "v_lshl_add_u32 v38, v38, 2, s67\n"
        "v_lshl_add_u32 v39, v39, 2, s67\n"
        "v_lshl_add_u32 v40, v40, 2, s67\n"
        "v_lshl_add_u32 v41, v41, 2, s67\n"
        "v_lshl_add_u32 v42, v42, 2, s67\n"
        "v_lshl_add_u32 v43, v43, 2, s67\n"
        "ds_read_b32 v36, v36\n"
        "ds_read_b32 v37, v37\n"
        "ds_read_b32 v38, v38\n"
        "ds_read_b32 v39, v39\n"
        "ds_read_b32 v40, v40\n"
        "ds_read_b32 v41, v41\n"
        "ds_read_b32 v42, v42\n"
        "ds_read_b32 v43, v43\n"
        "buffer_load_dwordx4 v130, s[20:23], s56 offen offset:2048 lds\n"
        "s_waitcnt vmcnt(9)\n"
        "s_barrier\n"
        "s_waitcnt vmcnt(6)\n"
        "ds_read_b128 a[212:215], v137 offset:0\n"
        "ds_read_b128 a[208:211], v137 offset:768\n"
        "ds_read_b128 a[204:207], v137 offset:32\n"
        "ds_read_b128 a[200:203], v137 offset:800\n"
        "ds_read_b128 a[196:199], v137 offset:64\n"
        "ds_read_b128 a[192:195], v137 offset:832\n"
        "s_add_u32 m0, s65, 8832\n"
        "s_nop 0\n"
        "buffer_load_dwordx4 v128, s[20:23], s56 offen offset:384 lds\n"
        "s_waitcnt lgkmcnt(5)\n"
        "v_mfma_f32_16x16x32_bf16 v[112:115], a[212:215], v[148:151], 0\n"
        "s_waitcnt lgkmcnt(4)\n"
        "ds_read_b128 a[216:219], v240 offset:0\n"
        "ds_read_b128 a[220:223], v240 offset:1536\n"
        "ds_read_b128 a[224:227], v240 offset:12288\n"
        "ds_read_b128 a[228:231], v240 offset:13824\n"
        "v_mfma_f32_16x16x32_bf16 v[116:119], a[208:211], v[148:151], 0\n"
        "s_waitcnt lgkmcnt(7)\n"
        "v_mfma_f32_16x16x32_bf16 v[120:123], a[204:207], v[148:151], 0\n"
        "s_waitcnt lgkmcnt(6)\n"
        "v_mfma_f32_16x16x32_bf16 v[124:127], a[200:203], v[148:151], 0\n"
        "s_waitcnt lgkmcnt(5)\n"
        "buffer_load_dwordx4 v129, s[20:23], s56 offen offset:1408 lds\n"
        "v_mfma_f32_16x16x32_bf16 v[96:99], a[196:199], v[148:151], 0\n"
        "s_waitcnt lgkmcnt(4)\n"
        "ds_read_b128 v[44:47], v143 offset:8192\n"
        "v_cvt_pk_bf16_f32 v112, v112, v113\n"
        "v_mfma_f32_16x16x32_bf16 v[100:103], a[192:195], v[148:151], 0\n"
        "s_waitcnt lgkmcnt(4)\n"
        "s_waitcnt vmcnt(5)\n"
        "ds_read_b64_tr_b16 a[72:73], v255 offset:3072\n"
        "ds_read_b64_tr_b16 a[74:75], v255 offset:3840\n"
        "v_accvgpr_write_b32 a0, v112\n"
        "v_cvt_pk_bf16_f32 v114, v114, v115\n"
        "v_accvgpr_write_b32 a1, v114\n"
        "v_cvt_pk_bf16_f32 v116, v116, v117\n"
        "v_accvgpr_write_b32 a2, v116\n"
        "v_cvt_pk_bf16_f32 v118, v118, v119\n"
        "v_accvgpr_write_b32 a3, v118\n"
        "s_nop 1\n"
        "v_mfma_f32_16x16x32_bf16 v[156:159], a[0:3], a[216:219], 0\n"
        "s_waitcnt lgkmcnt(5)\n"
        "ds_read_b64_tr_b16 a[96:97], v255 offset:3104\n"
        "ds_read_b64_tr_b16 a[98:99], v255 offset:3872\n"
        "v_mfma_f32_16x16x32_bf16 v[176:179], a[0:3], a[220:223], 0\n"
        "s_waitcnt lgkmcnt(5)\n"
        "buffer_load_dwordx4 v130, s[20:23], s56 offen offset:2432 lds\n"
        "ds_read_b64_tr_b16 a[120:121], v255 offset:3136\n"
        "ds_read_b64_tr_b16 a[122:123], v255 offset:3904\n"
        "v_mfma_f32_16x16x32_bf16 v[196:199], a[0:3], a[224:227], 0\n"
        "s_add_u32 m0, s65, 12288\n"
        "v_cvt_pk_bf16_f32 v120, v120, v121\n"
        "v_accvgpr_write_b32 a24, v120\n"
        "v_mfma_f32_16x16x32_bf16 v[216:219], a[0:3], a[228:231], 0\n"
        "s_waitcnt lgkmcnt(4)\n"
        "v_cvt_pk_bf16_f32 v122, v122, v123\n"
        "v_accvgpr_write_b32 a25, v122\n"
        "v_mfma_f32_16x16x32_bf16 v[152:155], a[72:75], a[216:219], 0\n"
        "buffer_load_dwordx4 v128, s[20:23], s57 offen lds\n"
        "s_waitcnt vmcnt(4)\n"
        "ds_read_b128 a[188:191], v137 offset:6144\n"
        "v_mfma_f32_16x16x32_bf16 v[172:175], a[72:75], a[220:223], 0\n"
        "ds_read_b128 a[184:187], v137 offset:6912\n"
        "ds_read_b128 a[180:183], v137 offset:6176\n"
        "v_cvt_pk_bf16_f32 v124, v124, v125\n"
        "v_accvgpr_write_b32 a26, v124\n"
        "v_cvt_pk_bf16_f32 v126, v126, v127\n"
        "v_accvgpr_write_b32 a27, v126\n"
        "s_nop 1\n"
        "v_mfma_f32_16x16x32_bf16 v[224:227], a[24:27], a[228:231], 0\n"
        "buffer_load_dwordx4 v129, s[20:23], s57 offen offset:1024 lds\n"
        "ds_read_b128 a[176:179], v137 offset:6944\n"
        "ds_read_b128 a[172:175], v137 offset:6208\n"
        "v_mfma_f32_16x16x32_bf16 v[204:207], a[24:27], a[224:227], 0\n"
        "ds_read_b128 a[168:171], v137 offset:6976\n"
        "ds_read_b128 a[232:235], v241 offset:0\n"
        "v_mfma_f32_16x16x32_bf16 v[184:187], a[24:27], a[220:223], 0\n"
        "ds_read_b128 a[236:239], v241 offset:1536\n"
        "ds_read_b128 a[240:243], v241 offset:12288\n"
        "v_mfma_f32_16x16x32_bf16 v[164:167], a[24:27], a[216:219], 0\n"
        "buffer_load_dwordx4 v130, s[20:23], s57 offen offset:2048 lds\n"
        "v_mfma_f32_16x16x32_bf16 v[192:195], a[72:75], a[224:227], 0\n"
        "s_add_u32 m0, s65, 14976\n"
        "ds_read_b128 a[244:247], v241 offset:13824\n"
        "v_cvt_pk_bf16_f32 v96, v96, v97\n"
        "v_mfma_f32_16x16x32_bf16 v[212:215], a[72:75], a[228:231], 0\n"
        "v_accvgpr_write_b32 a48, v96\n"
        "v_cvt_pk_bf16_f32 v98, v98, v99\n"
        "v_accvgpr_write_b32 a49, v98\n"
        "v_cvt_pk_bf16_f32 v100, v100, v101\n"
        "v_accvgpr_write_b32 a50, v100\n"
        "v_cvt_pk_bf16_f32 v102, v102, v103\n"
        "v_accvgpr_write_b32 a51, v102\n"
        "s_nop 1\n"
        "v_mfma_f32_16x16x32_bf16 v[160:163], a[48:51], a[216:219], 0\n"
        "v_lshl_add_u32 v44, v44, 2, s67\n"
        "v_lshl_add_u32 v45, v45, 2, s67\n"
        "v_mfma_f32_16x16x32_bf16 v[180:183], a[48:51], a[220:223], 0\n"
        "s_waitcnt lgkmcnt(5)\n"
        "ds_read_b32 v44, v44\n"
        "ds_read_b32 v45, v45\n"
        "v_mfma_f32_16x16x32_bf16 v[104:107], a[188:191], v[148:151], 0\n"
        "buffer_load_dwordx4 v128, s[20:23], s57 offen offset:384 lds\n"
        "v_mfma_f32_16x16x32_bf16 v[200:203], a[48:51], a[224:227], 0\n"
        "v_lshl_add_u32 v46, v46, 2, s67\n"
        "v_lshl_add_u32 v47, v47, 2, s67\n"
        "v_mfma_f32_16x16x32_bf16 v[108:111], a[184:187], v[148:151], 0\n"
        "ds_read_b32 v46, v46\n"
        "ds_read_b32 v47, v47\n"
        "v_mfma_f32_16x16x32_bf16 v[220:223], a[48:51], a[228:231], 0\n"
        "buffer_load_dwordx4 v129, s[20:23], s57 offen offset:1408 lds\n"
        "v_mfma_f32_16x16x32_bf16 v[88:91], a[96:99], a[228:231], 0\n"
        "ds_read_b128 v[48:51], v140 offset:9216\n"
        "v_cvt_pk_bf16_f32 v104, v104, v105\n"
        "v_mfma_f32_16x16x32_bf16 v[80:83], a[96:99], a[224:227], 0\n"
        "s_waitcnt vmcnt(5)\n"
        "ds_read_b64_tr_b16 a[76:77], v255 offset:9216\n"
        "ds_read_b64_tr_b16 a[78:79], v255 offset:9984\n"
        "v_mfma_f32_16x16x32_bf16 v[68:71], a[96:99], a[220:223], 0\n"
        "ds_read_b64_tr_b16 a[100:101], v255 offset:9248\n"
        "ds_read_b64_tr_b16 a[102:103], v255 offset:10016\n"
        "v_mfma_f32_16x16x32_bf16 v[64:67], a[96:99], a[216:219], 0\n"
        "buffer_load_dwordx4 v130, s[20:23], s57 offen offset:2432 lds\n"
        "ds_read_b64_tr_b16 a[124:125], v255 offset:9280\n"
        "ds_read_b64_tr_b16 a[126:127], v255 offset:10048\n"
        "v_mfma_f32_16x16x32_bf16 v[112:115], a[180:183], v[148:151], 0\n"
        "s_add_u32 m0, s65, 0\n"
        "v_accvgpr_write_b32 a4, v104\n"
        "v_cvt_pk_bf16_f32 v106, v106, v107\n"
        "v_mfma_f32_16x16x32_bf16 v[116:119], a[176:179], v[148:151], 0\n"
        "v_accvgpr_write_b32 a5, v106\n"
        "v_cvt_pk_bf16_f32 v108, v108, v109\n"
        "v_mfma_f32_16x16x32_bf16 v[72:75], a[120:123], a[216:219], 0\n"
        "buffer_load_dwordx4 v128, s[20:23], s58 offen lds\n"
        "v_mfma_f32_16x16x32_bf16 v[76:79], a[120:123], a[220:223], 0\n"
        "v_accvgpr_write_b32 a6, v108\n"
        "v_cvt_pk_bf16_f32 v110, v110, v111\n"
        "v_mfma_f32_16x16x32_bf16 v[84:87], a[120:123], a[224:227], 0\n"
        "v_accvgpr_write_b32 a7, v110\n"
        "v_cvt_pk_bf16_f32 v112, v112, v113\n"
        "v_mfma_f32_16x16x32_bf16 v[92:95], a[120:123], a[228:231], 0\n"
        "s_waitcnt lgkmcnt(2)\n"
        "v_accvgpr_write_b32 a28, v112\n"
        "v_cvt_pk_bf16_f32 v114, v114, v115\n"
        "v_mfma_f32_16x16x32_bf16 v[156:159], a[4:7], a[232:235], v[156:159]\n"
        "buffer_load_dwordx4 v129, s[20:23], s58 offen offset:1024 lds\n"
        "v_mfma_f32_16x16x32_bf16 v[176:179], a[4:7], a[236:239], v[176:179]\n"
        "v_lshl_add_u32 v48, v48, 2, s67\n"
        "v_lshl_add_u32 v49, v49, 2, s67\n"
        "v_mfma_f32_16x16x32_bf16 v[196:199], a[4:7], a[240:243], v[196:199]\n"
        "ds_read_b32 v48, v48\n"
        "ds_read_b32 v49, v49\n"
        "v_mfma_f32_16x16x32_bf16 v[216:219], a[4:7], a[244:247], v[216:219]\n"
        ".long 0xbec200ff\n"
        "41:\n"
        "s_branch 60b\n"
        "v_lshl_add_u32 v50, v50, 2, s67\n"
        "v_lshl_add_u32 v51, v51, 2, s67\n"
        "v_mfma_f32_16x16x32_bf16 v[120:123], a[172:175], v[148:151], 0\n"
        "buffer_load_dwordx4 v130, s[20:23], s58 offen offset:2048 lds\n"
        "v_mfma_f32_16x16x32_bf16 v[124:127], a[168:171], v[148:151], 0\n"
        "s_add_u32 m0, s65, 2688\n"
        "ds_read_b32 v50, v50\n"
        "ds_read_b32 v51, v51\n"
        "v_mfma_f32_16x16x32_bf16 v[152:155], a[76:79], a[232:235], v[152:155]\n"
        "ds_read_b128 a[248:251], v240 offset:128\n"
        "v_accvgpr_write_b32 a29, v114\n"
        "v_mfma_f32_16x16x32_bf16 v[172:175], a[76:79], a[236:239], v[172:175]\n"
        "s_waitcnt vmcnt(6)\n"
        "ds_read_b128 a[212:215], v137 offset:12288\n"
        "ds_read_b128 a[208:211], v137 offset:13056\n"
        "v_mfma_f32_16x16x32_bf16 v[192:195], a[76:79], a[240:243], v[192:195]\n"
        "buffer_load_dwordx4 v128, s[20:23], s58 offen offset:384 lds\n"
        "ds_read_b128 a[204:207], v137 offset:12320\n"
        "v_cvt_pk_bf16_f32 v116, v116, v117\n"
        "v_mfma_f32_16x16x32_bf16 v[212:215], a[76:79], a[244:247], v[212:215]\n"
        "ds_read_b128 a[200:203], v137 offset:13088\n"
        "ds_read_b128 a[196:199], v137 offset:12352\n"
        "v_accvgpr_write_b32 a30, v116\n"
        "v_cvt_pk_bf16_f32 v118, v118, v119\n"
        "v_accvgpr_write_b32 a31, v118\n"
        "s_nop 1\n"
        "v_mfma_f32_16x16x32_bf16 v[224:227], a[28:31], a[244:247], v[224:227]\n"
        "ds_read_b128 a[192:195], v137 offset:13120\n"
        "ds_read_b128 a[216:219], v240 offset:1664\n"
        "v_mfma_f32_16x16x32_bf16 v[204:207], a[28:31], a[240:243], v[204:207]\n"
        "buffer_load_dwordx4 v129, s[20:23], s58 offen offset:1408 lds\n"
        "v_mfma_f32_16x16x32_bf16 v[184:187], a[28:31], a[236:239], v[184:187]\n"
        "ds_read_b128 a[220:223], v240 offset:12416\n"
        "ds_read_b128 a[224:227], v240 offset:13952\n"
        "v_mfma_f32_16x16x32_bf16 v[164:167], a[28:31], a[232:235], v[164:167]\n"
        "s_waitcnt lgkmcnt(8)\n"
        "v_cvt_pk_bf16_f32 v120, v120, v121\n"
        "v_accvgpr_write_b32 a52, v120\n"
        "v_mfma_f32_16x16x32_bf16 v[96:99], a[212:215], v[148:151], 0\n"
        "s_waitcnt lgkmcnt(7)\n"
        "v_cvt_pk_bf16_f32 v122, v122, v123\n"
        "v_accvgpr_write_b32 a53, v122\n"
        "v_mfma_f32_16x16x32_bf16 v[100:103], a[208:211], v[148:151], 0\n"
        "buffer_load_dwordx4 v130, s[20:23], s58 offen offset:2432 lds\n"
        "v_mfma_f32_16x16x32_bf16 v[88:91], a[100:103], a[244:247], v[88:91]\n"
        "s_add_u32 m0, s65, 6144\n"
        "v_cvt_pk_bf16_f32 v124, v124, v125\n"
        "v_accvgpr_write_b32 a54, v124\n"
        "v_mfma_f32_16x16x32_bf16 v[80:83], a[100:103], a[240:243], v[80:83]\n"
        "v_cvt_pk_bf16_f32 v126, v126, v127\n"
        "v_accvgpr_write_b32 a55, v126\n"
        "v_mfma_f32_16x16x32_bf16 v[68:71], a[100:103], a[236:239], v[68:71]\n"
        "v_cvt_pk_bf16_f32 v96, v96, v97\n"
        "v_accvgpr_write_b32 a8, v96\n"
        "v_mfma_f32_16x16x32_bf16 v[64:67], a[100:103], a[232:235], v[64:67]\n"
        "buffer_load_dwordx4 v128, s[20:23], s59 offen lds\n"
        "v_mfma_f32_16x16x32_bf16 v[160:163], a[52:55], a[232:235], v[160:163]\n"
        "s_waitcnt vmcnt(7)\n"
        "ds_read_b64_tr_b16 a[80:81], v255 offset:15360\n"
        "ds_read_b64_tr_b16 a[82:83], v255 offset:16128\n"
        "v_mfma_f32_16x16x32_bf16 v[180:183], a[52:55], a[236:239], v[180:183]\n"
        "ds_read_b64_tr_b16 a[104:105], v255 offset:15392\n"
        "ds_read_b64_tr_b16 a[106:107], v255 offset:16160\n"
        "v_mfma_f32_16x16x32_bf16 v[200:203], a[52:55], a[240:243], v[200:203]\n"
        "ds_read_b64_tr_b16 a[128:129], v255 offset:15424\n"
        "ds_read_b64_tr_b16 a[130:131], v255 offset:16192\n"
        "v_mfma_f32_16x16x32_bf16 v[220:223], a[52:55], a[244:247], v[220:223]\n"
        "s_waitcnt lgkmcnt(6)\n"
        "v_cvt_pk_bf16_f32 v98, v98, v99\n"
        "v_accvgpr_write_b32 a9, v98\n"
        "v_mfma_f32_16x16x32_bf16 v[104:107], a[204:207], v[148:151], 0\n"
        "buffer_load_dwordx4 v129, s[20:23], s59 offen offset:1024 lds\n"
        "v_mfma_f32_16x16x32_bf16 v[108:111], a[200:203], v[148:151], 0\n"
        "v_cvt_pk_bf16_f32 v100, v100, v101\n"
        "v_accvgpr_write_b32 a10, v100\n"
        "v_mfma_f32_16x16x32_bf16 v[72:75], a[124:127], a[232:235], v[72:75]\n"
        "v_cvt_pk_bf16_f32 v102, v102, v103\n"
        "v_accvgpr_write_b32 a11, v102\n"
        "v_mfma_f32_16x16x32_bf16 v[76:79], a[124:127], a[236:239], v[76:79]\n"
        "buffer_load_dwordx4 v130, s[20:23], s59 offen offset:2048 lds\n"
        "v_mfma_f32_16x16x32_bf16 v[84:87], a[124:127], a[240:243], v[84:87]\n"
        "s_add_u32 m0, s65, 8832\n"
        "v_cvt_pk_bf16_f32 v104, v104, v105\n"
        "v_accvgpr_write_b32 a32, v104\n"
        "v_mfma_f32_16x16x32_bf16 v[92:95], a[124:127], a[244:247], v[92:95]\n"
        "s_waitcnt vmcnt(6)\n"
        "ds_read_b128 a[188:191], v137 offset:0\n"
        "ds_read_b128 a[184:187], v137 offset:768\n"
        "v_mfma_f32_16x16x32_bf16 v[156:159], a[8:11], a[248:251], v[156:159]\n"
        "buffer_load_dwordx4 v128, s[20:23], s59 offen offset:384 lds\n"
        "ds_read_b128 a[180:183], v137 offset:32\n"
        "v_mfma_f32_16x16x32_bf16 v[176:179], a[8:11], a[216:219], v[176:179]\n"
        "ds_read_b128 a[176:179], v137 offset:800\n"
        "ds_read_b128 a[172:175], v137 offset:64\n"
        "v_mfma_f32_16x16x32_bf16 v[196:199], a[8:11], a[220:223], v[196:199]\n"
        "ds_read_b128 a[168:171], v137 offset:832\n"
        "v_cvt_pk_bf16_f32 v106, v106, v107\n"
        "v_mfma_f32_16x16x32_bf16 v[216:219], a[8:11], a[224:227], v[216:219]\n"
        "v_accvgpr_write_b32 a33, v106\n"
        "v_cvt_pk_bf16_f32 v108, v108, v109\n"
        "v_mfma_f32_16x16x32_bf16 v[112:115], a[196:199], v[148:151], 0\n"
        "buffer_load_dwordx4 v129, s[20:23], s59 offen offset:1408 lds\n"
        "v_mfma_f32_16x16x32_bf16 v[116:119], a[192:195], v[148:151], 0\n"
        "s_waitcnt lgkmcnt(6)\n"
        "v_accvgpr_write_b32 a34, v108\n"
        "v_cvt_pk_bf16_f32 v110, v110, v111\n"
        "v_mfma_f32_16x16x32_bf16 v[152:155], a[80:83], a[248:251], v[152:155]\n"
        "ds_read_b128 a[228:231], v241 offset:128\n"
        "v_accvgpr_write_b32 a35, v110\n"
        "v_mfma_f32_16x16x32_bf16 v[172:175], a[80:83], a[216:219], v[172:175]\n"
        "ds_read_b128 a[232:235], v241 offset:1664\n"
        "v_cvt_pk_bf16_f32 v112, v112, v113\n"
        "v_mfma_f32_16x16x32_bf16 v[192:195], a[80:83], a[220:223], v[192:195]\n"
        "buffer_load_dwordx4 v130, s[20:23], s59 offen offset:2432 lds\n"
        "v_mfma_f32_16x16x32_bf16 v[212:215], a[80:83], a[224:227], v[212:215]\n"
        "s_add_u32 m0, s65, 12288\n"
        "ds_read_b128 a[236:239], v241 offset:12416\n"
        "ds_read_b128 a[240:243], v241 offset:13952\n"
        "v_mfma_f32_16x16x32_bf16 v[224:227], a[32:35], a[224:227], v[224:227]\n"
        "ds_read_b128 v[52:55], v141 offset:9216\n"
        "v_accvgpr_write_b32 a56, v112\n"
        "v_mfma_f32_16x16x32_bf16 v[204:207], a[32:35], a[220:223], v[204:207]\n"
        "v_cvt_pk_bf16_f32 v114, v114, v115\n"
        "v_accvgpr_write_b32 a57, v114\n"
        "v_mfma_f32_16x16x32_bf16 v[184:187], a[32:35], a[216:219], v[184:187]\n"
        "buffer_load_dwordx4 v128, s[20:23], s60 offen lds\n"
        "v_mfma_f32_16x16x32_bf16 v[164:167], a[32:35], a[248:251], v[164:167]\n"
        "s_waitcnt lgkmcnt(4)\n"
        "v_cvt_pk_bf16_f32 v116, v116, v117\n"
        "v_accvgpr_write_b32 a58, v116\n"
        "v_mfma_f32_16x16x32_bf16 v[120:123], a[188:191], v[148:151], 0\n"
        "s_waitcnt vmcnt(7)\n"
        "ds_read_b64_tr_b16 a[84:85], v255 offset:3072\n"
        "ds_read_b64_tr_b16 a[86:87], v255 offset:3840\n"
        "v_mfma_f32_16x16x32_bf16 v[124:127], a[184:187], v[148:151], 0\n"
        "ds_read_b64_tr_b16 a[108:109], v255 offset:3104\n"
        "ds_read_b64_tr_b16 a[110:111], v255 offset:3872\n"
        "v_mfma_f32_16x16x32_bf16 v[88:91], a[104:107], a[224:227], v[88:91]\n"
        "buffer_load_dwordx4 v129, s[20:23], s60 offen offset:1024 lds\n"
        "ds_read_b64_tr_b16 a[132:133], v255 offset:3136\n"
        "ds_read_b64_tr_b16 a[134:135], v255 offset:3904\n"
        "v_mfma_f32_16x16x32_bf16 v[80:83], a[104:107], a[220:223], v[80:83]\n"
        "v_cvt_pk_bf16_f32 v118, v118, v119\n"
        "v_accvgpr_write_b32 a59, v118\n"
        "v_mfma_f32_16x16x32_bf16 v[68:71], a[104:107], a[216:219], v[68:71]\n"
        "v_cvt_pk_bf16_f32 v120, v120, v121\n"
        "v_accvgpr_write_b32 a12, v120\n"
        "v_mfma_f32_16x16x32_bf16 v[64:67], a[104:107], a[248:251], v[64:67]\n"
        "buffer_load_dwordx4 v130, s[20:23], s60 offen offset:2048 lds\n"
        "v_mfma_f32_16x16x32_bf16 v[160:163], a[56:59], a[248:251], v[160:163]\n"
        "s_add_u32 m0, s65, 14976\n"
        "v_cvt_pk_bf16_f32 v122, v122, v123\n"
        "v_accvgpr_write_b32 a13, v122\n"
        "v_mfma_f32_16x16x32_bf16 v[180:183], a[56:59], a[216:219], v[180:183]\n"
        "s_waitcnt lgkmcnt(4)\n"
        "v_lshl_add_u32 v52, v52, 2, s67\n"
        "v_lshl_add_u32 v53, v53, 2, s67\n"
        "v_mfma_f32_16x16x32_bf16 v[200:203], a[56:59], a[220:223], v[200:203]\n"
        "ds_read_b32 v52, v52\n"
        "ds_read_b32 v53, v53\n"
        "v_mfma_f32_16x16x32_bf16 v[220:223], a[56:59], a[224:227], v[220:223]\n"
        "buffer_load_dwordx4 v128, s[20:23], s60 offen offset:384 lds\n"
        "v_mfma_f32_16x16x32_bf16 v[96:99], a[180:183], v[148:151], 0\n"
        "v_lshl_add_u32 v54, v54, 2, s67\n"
        "v_lshl_add_u32 v55, v55, 2, s67\n"
        "v_mfma_f32_16x16x32_bf16 v[100:103], a[176:179], v[148:151], 0\n"
        "ds_read_b32 v54, v54\n"
        "ds_read_b32 v55, v55\n"
        "v_mfma_f32_16x16x32_bf16 v[72:75], a[128:131], a[248:251], v[72:75]\n"
        "v_cvt_pk_bf16_f32 v124, v124, v125\n"
        "v_accvgpr_write_b32 a14, v124\n"
        "v_mfma_f32_16x16x32_bf16 v[76:79], a[128:131], a[216:219], v[76:79]\n"
        "buffer_load_dwordx4 v129, s[20:23], s60 offen offset:1408 lds\n"
        "v_mfma_f32_16x16x32_bf16 v[84:87], a[128:131], a[220:223], v[84:87]\n"
        "v_cvt_pk_bf16_f32 v126, v126, v127\n"
        "v_accvgpr_write_b32 a15, v126\n"
        "v_mfma_f32_16x16x32_bf16 v[92:95], a[128:131], a[224:227], v[92:95]\n"
        "v_cvt_pk_bf16_f32 v96, v96, v97\n"
        "v_accvgpr_write_b32 a36, v96\n"
        "v_mfma_f32_16x16x32_bf16 v[156:159], a[12:15], a[228:231], v[156:159]\n"
        "s_waitcnt vmcnt(8)\n"
        "ds_read_b128 a[212:215], v137 offset:6144\n"
        "ds_read_b128 a[208:211], v137 offset:6912\n"
        "v_mfma_f32_16x16x32_bf16 v[176:179], a[12:15], a[232:235], v[176:179]\n"
        "buffer_load_dwordx4 v130, s[20:23], s60 offen offset:2432 lds\n"
        "ds_read_b128 a[204:207], v137 offset:6176\n"
        "v_mfma_f32_16x16x32_bf16 v[196:199], a[12:15], a[236:239], v[196:199]\n"
        "ds_read_b128 a[200:203], v137 offset:6944\n"
        "ds_read_b128 a[196:199], v137 offset:6208\n"
        "v_mfma_f32_16x16x32_bf16 v[216:219], a[12:15], a[240:243], v[216:219]\n"
        "ds_read_b128 a[192:195], v137 offset:6976\n"
        "v_cvt_pk_bf16_f32 v98, v98, v99\n"
        "v_mfma_f32_16x16x32_bf16 v[104:107], a[172:175], v[148:151], 0\n"
        "v_accvgpr_write_b32 a37, v98\n"
        "v_cvt_pk_bf16_f32 v100, v100, v101\n"
        "v_mfma_f32_16x16x32_bf16 v[108:111], a[168:171], v[148:151], 0\n"
        "v_accvgpr_write_b32 a38, v100\n"
        "v_cvt_pk_bf16_f32 v102, v102, v103\n"
        "v_mfma_f32_16x16x32_bf16 v[152:155], a[84:87], a[228:231], v[152:155]\n"
        "ds_read_b128 a[244:247], v240 offset:256\n"
        "v_accvgpr_write_b32 a39, v102\n"
        "v_mfma_f32_16x16x32_bf16 v[172:175], a[84:87], a[232:235], v[172:175]\n"
        "s_mov_b32 m0, s48\n"
        "ds_read_b128 a[248:251], v240 offset:1792\n"
        "v_cvt_pk_bf16_f32 v104, v104, v105\n"
        "v_mfma_f32_16x16x32_bf16 v[192:195], a[84:87], a[236:239], v[192:195]\n"
        "s_sub_u32 s64, s36, 4194304\n"
        "ds_read_b128 a[216:219], v240 offset:12544\n"
        "v_accvgpr_write_b32 a60, v104\n"
        "v_mfma_f32_16x16x32_bf16 v[212:215], a[84:87], a[240:243], v[212:215]\n"
        "buffer_load_dwordx4 v249, s[12:15], s64 offen lds\n"
        "v_mfma_f32_16x16x32_bf16 v[224:227], a[36:39], a[240:243], v[224:227]\n"
        "ds_read_b128 a[220:223], v240 offset:14080\n"
        "v_cvt_pk_bf16_f32 v106, v106, v107\n"
        "v_mfma_f32_16x16x32_bf16 v[204:207], a[36:39], a[236:239], v[204:207]\n"
        "ds_read_b128 v[56:59], v142 offset:9216\n"
        "v_accvgpr_write_b32 a61, v106\n"
        "v_mfma_f32_16x16x32_bf16 v[184:187], a[36:39], a[232:235], v[184:187]\n"
        "s_waitcnt vmcnt(7)\n"
        "ds_read_b64_tr_b16 a[88:89], v255 offset:9216\n"
        "ds_read_b64_tr_b16 a[90:91], v255 offset:9984\n"
        "v_mfma_f32_16x16x32_bf16 v[164:167], a[36:39], a[228:231], v[164:167]\n"
        "s_waitcnt lgkmcnt(6)\n"
        "ds_read_b64_tr_b16 a[112:113], v255 offset:9248\n"
        "ds_read_b64_tr_b16 a[114:115], v255 offset:10016\n"
        "v_mfma_f32_16x16x32_bf16 v[112:115], a[212:215], v[148:151], 0\n"
        "buffer_load_dwordx4 v253, s[12:15], s64 offen offset:1024 lds\n"
        "ds_read_b64_tr_b16 a[136:137], v255 offset:9280\n"
        "v_mfma_f32_16x16x32_bf16 v[116:119], a[208:211], v[148:151], 0\n"
        "ds_read_b64_tr_b16 a[138:139], v255 offset:10048\n"
        "s_add_u32 m0, s65, 0\n"
        "v_cvt_pk_bf16_f32 v108, v108, v109\n"
        "v_mfma_f32_16x16x32_bf16 v[88:91], a[108:111], a[240:243], v[88:91]\n"
        "s_mov_b32 s64, 768\n"
        "v_accvgpr_write_b32 a62, v108\n"
        "v_cvt_pk_bf16_f32 v110, v110, v111\n"
        "v_mfma_f32_16x16x32_bf16 v[80:83], a[108:111], a[236:239], v[80:83]\n"
        "buffer_load_dwordx4 v128, s[20:23], s64 offen lds\n"
        "v_mfma_f32_16x16x32_bf16 v[68:71], a[108:111], a[232:235], v[68:71]\n"
        "v_accvgpr_write_b32 a63, v110\n"
        "v_cvt_pk_bf16_f32 v112, v112, v113\n"
        "v_mfma_f32_16x16x32_bf16 v[64:67], a[108:111], a[228:231], v[64:67]\n"
        "v_accvgpr_write_b32 a16, v112\n"
        "v_cvt_pk_bf16_f32 v114, v114, v115\n"
        "v_mfma_f32_16x16x32_bf16 v[160:163], a[60:63], a[228:231], v[160:163]\n"
        "v_accvgpr_write_b32 a17, v114\n"
        "v_cvt_pk_bf16_f32 v116, v116, v117\n"
        "v_mfma_f32_16x16x32_bf16 v[180:183], a[60:63], a[232:235], v[180:183]\n"
        "buffer_load_dwordx4 v129, s[20:23], s64 offen offset:1024 lds\n"
        "v_mfma_f32_16x16x32_bf16 v[200:203], a[60:63], a[236:239], v[200:203]\n"
        "ds_read_b128 v[60:63], v143 offset:9216\n"
        "v_accvgpr_write_b32 a18, v116\n"
        "v_mfma_f32_16x16x32_bf16 v[220:223], a[60:63], a[240:243], v[220:223]\n"
        "v_cvt_pk_bf16_f32 v118, v118, v119\n"
        "v_accvgpr_write_b32 a19, v118\n"
        "v_mfma_f32_16x16x32_bf16 v[120:123], a[204:207], v[148:151], 0\n"
        "s_waitcnt lgkmcnt(1)\n"
        "v_lshl_add_u32 v56, v56, 2, s67\n"
        "v_lshl_add_u32 v57, v57, 2, s67\n"
        "v_mfma_f32_16x16x32_bf16 v[124:127], a[200:203], v[148:151], 0\n"
        "buffer_load_dwordx4 v130, s[20:23], s64 offen offset:2048 lds\n"
        "v_mfma_f32_16x16x32_bf16 v[72:75], a[132:135], a[228:231], v[72:75]\n"
        "s_add_u32 m0, s65, 2304\n"
        "ds_read_b32 v56, v56\n"
        "ds_read_b32 v57, v57\n"
        "v_mfma_f32_16x16x32_bf16 v[76:79], a[132:135], a[232:235], v[76:79]\n"
        "v_lshl_add_u32 v58, v58, 2, s67\n"
        "v_lshl_add_u32 v59, v59, 2, s67\n"
        "v_mfma_f32_16x16x32_bf16 v[84:87], a[132:135], a[236:239], v[84:87]\n"
        "ds_read_b32 v58, v58\n"
        "ds_read_b32 v59, v59\n"
        "v_mfma_f32_16x16x32_bf16 v[92:95], a[132:135], a[240:243], v[92:95]\n"
        "buffer_load_dwordx4 v128, s[20:23], s56 offen offset:768 lds\n"
        "v_mfma_f32_16x16x32_bf16 v[156:159], a[16:19], a[244:247], v[156:159]\n"
        "v_cvt_pk_bf16_f32 v120, v120, v121\n"
        "v_accvgpr_write_b32 a40, v120\n"
        "v_mfma_f32_16x16x32_bf16 v[176:179], a[16:19], a[248:251], v[176:179]\n"
        "v_cvt_pk_bf16_f32 v122, v122, v123\n"
        "v_accvgpr_write_b32 a41, v122\n"
        "v_mfma_f32_16x16x32_bf16 v[196:199], a[16:19], a[216:219], v[196:199]\n"
        "v_cvt_pk_bf16_f32 v124, v124, v125\n"
        "v_accvgpr_write_b32 a42, v124\n"
        "v_mfma_f32_16x16x32_bf16 v[216:219], a[16:19], a[220:223], v[216:219]\n"
        "buffer_load_dwordx4 v129, s[20:23], s56 offen offset:1792 lds\n"
        "v_mfma_f32_16x16x32_bf16 v[96:99], a[196:199], v[148:151], 0\n"
        "ds_read_b128 a[224:227], v241 offset:256\n"
        "v_cvt_pk_bf16_f32 v126, v126, v127\n"
        "v_mfma_f32_16x16x32_bf16 v[100:103], a[192:195], v[148:151], 0\n"
        "s_waitcnt vmcnt(10)\n"
        "ds_read_b128 a[188:191], v137 offset:12288\n"
        "ds_read_b128 a[184:187], v137 offset:13056\n"
        "v_mfma_f32_16x16x32_bf16 v[152:155], a[88:91], a[244:247], v[152:155]\n"
        "ds_read_b128 a[180:183], v137 offset:12320\n"
        "ds_read_b128 a[176:179], v137 offset:13088\n"
        "v_mfma_f32_16x16x32_bf16 v[172:175], a[88:91], a[248:251], v[172:175]\n"
        "buffer_load_dwordx4 v130, s[20:23], s56 offen offset:2816 lds\n"
        "ds_read_b128 a[172:175], v137 offset:12352\n"
        "v_mfma_f32_16x16x32_bf16 v[192:195], a[88:91], a[216:219], v[192:195]\n"
        "ds_read_b128 a[168:171], v137 offset:13120\n"
        "s_add_u32 m0, s65, 5376\n"
        "ds_read_b128 a[228:231], v241 offset:1792\n"
        "v_accvgpr_write_b32 a43, v126\n"
        "v_mfma_f32_16x16x32_bf16 v[212:215], a[88:91], a[220:223], v[212:215]\n"
        "ds_read_b128 a[232:235], v241 offset:12544\n"
        "ds_read_b128 a[236:239], v241 offset:14080\n"
        "v_mfma_f32_16x16x32_bf16 v[224:227], a[40:43], a[220:223], v[224:227]\n"
        "s_waitcnt lgkmcnt(10)\n"
        "v_lshl_add_u32 v60, v60, 2, s67\n"
        "v_lshl_add_u32 v61, v61, 2, s67\n"
        "v_lshl_add_u32 v62, v62, 2, s67\n"
        "v_mfma_f32_16x16x32_bf16 v[204:207], a[40:43], a[216:219], v[204:207]\n"
        "buffer_load_dwordx4 v128, s[20:23], s57 offen offset:768 lds\n"
        "v_lshl_add_u32 v63, v63, 2, s67\n"
        "v_mfma_f32_16x16x32_bf16 v[184:187], a[40:43], a[248:251], v[184:187]\n"
        "ds_read_b32 v60, v60\n"
        "ds_read_b32 v61, v61\n"
        "v_mfma_f32_16x16x32_bf16 v[164:167], a[40:43], a[244:247], v[164:167]\n"
        "s_waitcnt lgkmcnt(7)\n"
        "ds_read_b32 v62, v62\n"
        "ds_read_b32 v63, v63\n"
        "v_mfma_f32_16x16x32_bf16 v[104:107], a[188:191], v[148:151], 0\n"
        "v_cvt_pk_bf16_f32 v96, v96, v97\n"
        "v_accvgpr_write_b32 a64, v96\n"
        "v_mfma_f32_16x16x32_bf16 v[108:111], a[184:187], v[148:151], 0\n"
        "buffer_load_dwordx4 v129, s[20:23], s57 offen offset:1792 lds\n"
        "v_mfma_f32_16x16x32_bf16 v[88:91], a[112:115], a[220:223], v[88:91]\n"
        "v_cvt_pk_bf16_f32 v98, v98, v99\n"
        "v_accvgpr_write_b32 a65, v98\n"
        "v_mfma_f32_16x16x32_bf16 v[80:83], a[112:115], a[216:219], v[80:83]\n"
        "v_cvt_pk_bf16_f32 v100, v100, v101\n"
        "v_accvgpr_write_b32 a66, v100\n"
        "v_mfma_f32_16x16x32_bf16 v[68:71], a[112:115], a[248:251], v[68:71]\n"
        "v_cvt_pk_bf16_f32 v102, v102, v103\n"
        "v_accvgpr_write_b32 a67, v102\n"
        "v_mfma_f32_16x16x32_bf16 v[64:67], a[112:115], a[244:247], v[64:67]\n"
        "s_waitcnt vmcnt(10)\n"
        "ds_read_b64_tr_b16 a[92:93], v255 offset:15360\n"
        "ds_read_b64_tr_b16 a[94:95], v255 offset:16128\n"
        "v_mfma_f32_16x16x32_bf16 v[160:163], a[64:67], a[244:247], v[160:163]\n"
        "ds_read_b64_tr_b16 a[116:117], v255 offset:15392\n"
        "ds_read_b64_tr_b16 a[118:119], v255 offset:16160\n"
        "v_mfma_f32_16x16x32_bf16 v[180:183], a[64:67], a[248:251], v[180:183]\n"
        "ds_read_b64_tr_b16 a[140:141], v255 offset:15424\n"
        "ds_read_b64_tr_b16 a[142:143], v255 offset:16192\n"
        "v_mfma_f32_16x16x32_bf16 v[200:203], a[64:67], a[216:219], v[200:203]\n"
        "v_cvt_pk_bf16_f32 v104, v104, v105\n"
        "v_accvgpr_write_b32 a20, v104\n"
        "v_mfma_f32_16x16x32_bf16 v[220:223], a[64:67], a[220:223], v[220:223]\n"
        "v_cvt_pk_bf16_f32 v106, v106, v107\n"
        "v_accvgpr_write_b32 a21, v106\n"
        "v_mfma_f32_16x16x32_bf16 v[112:115], a[180:183], v[148:151], 0\n"
        "v_cvt_pk_bf16_f32 v108, v108, v109\n"
        "v_accvgpr_write_b32 a22, v108\n"
        "v_mfma_f32_16x16x32_bf16 v[116:119], a[176:179], v[148:151], 0\n"
        "v_cvt_pk_bf16_f32 v110, v110, v111\n"
        "v_accvgpr_write_b32 a23, v110\n"
        "v_mfma_f32_16x16x32_bf16 v[72:75], a[136:139], a[244:247], v[72:75]\n"
        "v_mfma_f32_16x16x32_bf16 v[76:79], a[136:139], a[248:251], v[76:79]\n"
        "v_mfma_f32_16x16x32_bf16 v[156:159], a[20:23], a[224:227], v[156:159]\n"
        "s_waitcnt lgkmcnt(0)\n"
        "v_cvt_pk_bf16_f32 v112, v112, v113\n"
        "v_accvgpr_write_b32 a44, v112\n"
        "v_mfma_f32_16x16x32_bf16 v[176:179], a[20:23], a[228:231], v[176:179]\n"
        "v_cvt_pk_bf16_f32 v114, v114, v115\n"
        "v_accvgpr_write_b32 a45, v114\n"
        "v_mfma_f32_16x16x32_bf16 v[196:199], a[20:23], a[232:235], v[196:199]\n"
        "v_cvt_pk_bf16_f32 v116, v116, v117\n"
        "v_accvgpr_write_b32 a46, v116\n"
        "v_cvt_pk_bf16_f32 v156, v156, v157\n"
        "v_mfma_f32_16x16x32_bf16 v[216:219], a[20:23], a[236:239], v[216:219]\n"
        "v_cvt_pk_bf16_f32 v157, v158, v159\n"
        "v_cvt_pk_bf16_f32 v176, v176, v177\n"
        "v_cvt_pk_bf16_f32 v177, v178, v179\n"
        "v_mfma_f32_16x16x32_bf16 v[120:123], a[172:175], v[148:151], 0\n"
        "v_cvt_pk_bf16_f32 v196, v196, v197\n"
        "v_cvt_pk_bf16_f32 v197, v198, v199\n"
        "v_mfma_f32_16x16x32_bf16 v[124:127], a[168:171], v[148:151], 0\n"
        "v_cvt_pk_bf16_f32 v118, v118, v119\n"
        "v_cvt_pk_bf16_f32 v216, v216, v217\n"
        "v_cvt_pk_bf16_f32 v217, v218, v219\n"
        "v_mfma_f32_16x16x32_bf16 v[84:87], a[136:139], a[216:219], v[84:87]\n"
        "v_accvgpr_write_b32 a47, v118\n"
        "v_cvt_pk_bf16_f32 v120, v120, v121\n"
        "v_mfma_f32_16x16x32_bf16 v[92:95], a[136:139], a[220:223], v[92:95]\n"
        "v_accvgpr_write_b32 a68, v120\n"
        "v_cvt_pk_bf16_f32 v122, v122, v123\n"
        "v_mfma_f32_16x16x32_bf16 v[224:227], a[44:47], a[236:239], v[224:227]\n"
        "v_accvgpr_write_b32 a69, v122\n"
        "v_cvt_pk_bf16_f32 v124, v124, v125\n"
        "v_mfma_f32_16x16x32_bf16 v[204:207], a[44:47], a[232:235], v[204:207]\n"
        "v_accvgpr_write_b32 a70, v124\n"
        "v_cvt_pk_bf16_f32 v126, v126, v127\n"
        "v_mfma_f32_16x16x32_bf16 v[184:187], a[44:47], a[228:231], v[184:187]\n"
        "v_accvgpr_write_b32 a71, v126\n"
        "s_nop 0\n"
        "v_cvt_pk_bf16_f32 v218, v224, v225\n"
        "v_mfma_f32_16x16x32_bf16 v[164:167], a[44:47], a[224:227], v[164:167]\n"
        "v_cvt_pk_bf16_f32 v219, v226, v227\n"
        "v_cvt_pk_bf16_f32 v198, v204, v205\n"
        "v_cvt_pk_bf16_f32 v199, v206, v207\n"
        "v_mfma_f32_16x16x32_bf16 v[152:155], a[92:95], a[224:227], v[152:155]\n"
        "v_cvt_pk_bf16_f32 v178, v184, v185\n"
        "v_cvt_pk_bf16_f32 v179, v186, v187\n"
        "v_mfma_f32_16x16x32_bf16 v[64:67], a[116:119], a[224:227], v[64:67]\n"
        "buffer_load_dwordx4 v130, s[20:23], s57 offen offset:2816 lds\n"
        "v_cvt_pk_bf16_f32 v158, v164, v165\n"
        "v_mfma_f32_16x16x32_bf16 v[72:75], a[140:143], a[224:227], v[72:75]\n"
        "s_add_u32 m0, s65, 8448\n"
        "v_cvt_pk_bf16_f32 v159, v166, v167\n"
        "v_cvt_pk_bf16_f32 v152, v152, v153\n"
        "v_mfma_f32_16x16x32_bf16 v[172:175], a[92:95], a[228:231], v[172:175]\n"
        "v_cvt_pk_bf16_f32 v153, v154, v155\n"
        "v_cvt_pk_bf16_f32 v154, v64, v65\n"
        "v_mfma_f32_16x16x32_bf16 v[160:163], a[68:71], a[224:227], v[160:163]\n"
        "v_cvt_pk_bf16_f32 v155, v66, v67\n"
        "v_cvt_pk_bf16_f32 v168, v72, v73\n"
        "v_mfma_f32_16x16x32_bf16 v[180:183], a[68:71], a[228:231], v[180:183]\n"
        "buffer_load_dwordx4 v128, s[20:23], s58 offen offset:768 lds\n"
        "v_mfma_f32_16x16x32_bf16 v[200:203], a[68:71], a[232:235], v[200:203]\n"
        "v_cvt_pk_bf16_f32 v169, v74, v75\n"
        "v_cvt_pk_bf16_f32 v172, v172, v173\n"
        "v_mfma_f32_16x16x32_bf16 v[220:223], a[68:71], a[236:239], v[220:223]\n"
        "v_cvt_pk_bf16_f32 v166, v160, v161\n"
        "v_cvt_pk_bf16_f32 v160, v160, v161\n"
        "v_cvt_pk_bf16_f32 v161, v162, v163\n"
        "v_cvt_pk_bf16_f32 v167, v162, v163\n"
        "v_cvt_pk_bf16_f32 v186, v180, v181\n"
        "v_cvt_pk_bf16_f32 v180, v180, v181\n"
        "v_mfma_f32_16x16x32_bf16 v[68:71], a[116:119], a[228:231], v[68:71]\n"
        "buffer_load_dwordx4 v129, s[20:23], s58 offen offset:1792 lds\n"
        "v_cvt_pk_bf16_f32 v181, v182, v183\n"
        "v_cvt_pk_bf16_f32 v187, v182, v183\n"
        "v_cvt_pk_bf16_f32 v206, v200, v201\n"
        "v_cvt_pk_bf16_f32 v200, v200, v201\n"
        "v_mfma_f32_16x16x32_bf16 v[76:79], a[140:143], a[228:231], v[76:79]\n"
        "s_waitcnt vmcnt(11)\n"
        "s_barrier\n"
        "ds_read_b128 v[162:165], v251 offset:0\n"
        "ds_read_b128 v[182:185], v251 offset:512\n"
        "v_cvt_pk_bf16_f32 v201, v202, v203\n"
        "v_cvt_pk_bf16_f32 v207, v202, v203\n"
        "v_cvt_pk_bf16_f32 v226, v220, v221\n"
        "v_cvt_pk_bf16_f32 v220, v220, v221\n"
        "v_mfma_f32_16x16x32_bf16 v[192:195], a[92:95], a[232:235], v[192:195]\n"
        "ds_read_b128 v[202:205], v251 offset:4096\n"
        "ds_read_b128 a[240:243], v240 offset:0\n"
        "v_cvt_pk_bf16_f32 v221, v222, v223\n"
        "v_cvt_pk_bf16_f32 v227, v222, v223\n"
        "v_cvt_pk_bf16_f32 v173, v174, v175\n"
        "v_mfma_f32_16x16x32_bf16 v[80:83], a[116:119], a[232:235], v[80:83]\n"
        "buffer_load_dwordx4 v130, s[20:23], s58 offen offset:2816 lds\n"
        "v_mfma_f32_16x16x32_bf16 v[84:87], a[140:143], a[232:235], v[84:87]\n"
        "s_add_u32 m0, s65, 11520\n"
        "ds_read_b128 a[244:247], v241 offset:0\n"
        "ds_read_b128 a[248:251], v240 offset:128\n"
        "v_mfma_f32_16x16x32_bf16 v[212:215], a[92:95], a[236:239], v[212:215]\n"
        "v_cvt_pk_bf16_f32 v174, v68, v69\n"
        "v_cvt_pk_bf16_f32 v175, v70, v71\n"
        "v_mfma_f32_16x16x32_bf16 v[88:91], a[116:119], a[236:239], v[88:91]\n"
        "buffer_load_dwordx4 v128, s[20:23], s59 offen offset:768 lds\n"
        "v_mfma_f32_16x16x32_bf16 v[92:95], a[140:143], a[236:239], v[92:95]\n"
        "s_waitcnt vmcnt(10)\n"
        "ds_read_b64_tr_b16 a[144:145], v255 offset:0\n"
        "ds_read_b64_tr_b16 a[146:147], v255 offset:768\n"
        "v_mfma_f32_16x16x32_bf16 v[64:67], v[152:155], v[156:159], v[0:3]\n"
        "s_waitcnt lgkmcnt(6)\n"
        "ds_read_b64_tr_b16 a[168:169], v255 offset:32\n"
        "ds_read_b64_tr_b16 a[170:171], v255 offset:800\n"
        "v_cvt_pk_bf16_f32 v190, v76, v77\n"
        "v_cvt_pk_bf16_f32 v191, v78, v79\n"
        "v_mfma_f32_16x16x32_bf16 v[64:67], v[168:171], v[160:163], v[64:67]\n"
        "ds_read_b64_tr_b16 a[192:193], v255 offset:64\n"
        "ds_read_b64_tr_b16 a[194:195], v255 offset:832\n"
        "v_cvt_pk_bf16_f32 v192, v192, v193\n"
        "v_cvt_pk_bf16_f32 v193, v194, v195\n"
        "v_mfma_f32_16x16x32_bf16 v[96:99], v[172:175], v[156:159], v[4:7]\n"
        "buffer_load_dwordx4 v129, s[20:23], s59 offen offset:1792 lds\n"
        "v_cvt_pk_bf16_f32 v194, v80, v81\n"
        "v_cvt_pk_bf16_f32 v195, v82, v83\n"
        "v_exp_f32 v64, v64\n"
        "v_mfma_f32_16x16x32_bf16 v[96:99], v[188:191], v[164:167], v[96:99]\n"
        "ds_read_b128 v[162:165], v252 offset:0\n"
        "v_exp_f32 v65, v65\n"
        "v_exp_f32 v66, v66\n"
        "v_cvt_pk_bf16_f32 v64, v64, v65\n"
        "v_mfma_f32_16x16x32_bf16 v[72:75], v[152:155], v[176:179], v[16:19]\n"
        "buffer_load_dwordx4 v130, s[20:23], s59 offen offset:2816 lds\n"
        "v_cvt_pk_bf16_f32 v208, v84, v85\n"
        "v_cvt_pk_bf16_f32 v209, v86, v87\n"
        "v_exp_f32 v96, v96\n"
        "v_mfma_f32_16x16x32_bf16 v[72:75], v[168:171], v[180:183], v[72:75]\n"
        "s_add_u32 m0, s65, 14592\n"
        "s_waitcnt vmcnt(9)\n"
        "ds_read_b64_tr_b16 a[148:149], v255 offset:3072\n"
        "ds_read_b64_tr_b16 a[150:151], v255 offset:3840\n"
        "v_exp_f32 v67, v67\n"
        "v_exp_f32 v97, v97\n"
        "v_mfma_f32_16x16x32_bf16 v[100:103], v[172:175], v[176:179], v[20:23]\n"
        "buffer_load_dwordx4 v128, s[20:23], s60 offen offset:768 lds\n"
        "ds_read_b64_tr_b16 a[172:173], v255 offset:3104\n"
        "ds_read_b64_tr_b16 a[174:175], v255 offset:3872\n"
        "v_cvt_pk_bf16_f32 v212, v212, v213\n"
        "v_cvt_pk_bf16_f32 v213, v214, v215\n"
        "v_mfma_f32_16x16x32_bf16 v[100:103], v[188:191], v[184:187], v[100:103]\n"
        "ds_read_b128 v[182:185], v252 offset:512\n"
        "ds_read_b64_tr_b16 a[196:197], v255 offset:3136\n"
        "ds_read_b64_tr_b16 a[198:199], v255 offset:3904\n"
        "v_cvt_pk_bf16_f32 v65, v66, v67\n"
        "v_exp_f32 v98, v98\n"
        "v_mfma_f32_16x16x32_bf16 v[68:71], v[192:195], v[156:159], v[8:11]\n"
        "s_waitcnt lgkmcnt(7)\n"
        "ds_read_b128 a[216:219], v240 offset:1536\n"
        "ds_read_b128 a[220:223], v241 offset:1536\n"
        "v_cvt_pk_bf16_f32 v66, v96, v97\n"
        "v_exp_f32 v72, v72\n"
        "v_cvt_pk_bf16_f32 v214, v88, v89\n"
        "v_mfma_f32_16x16x32_bf16 v[68:71], v[208:211], v[160:163], v[68:71]\n"
        "buffer_load_dwordx4 v129, s[20:23], s60 offen offset:1792 lds\n"
        "s_waitcnt vmcnt(8)\n"
        "ds_read_b64_tr_b16 a[152:153], v255 offset:6144\n"
        "ds_read_b64_tr_b16 a[154:155], v255 offset:6912\n"
        "v_cvt_pk_bf16_f32 v215, v90, v91\n"
        "v_cvt_pk_bf16_f32 v230, v92, v93\n"
        "v_mfma_f32_16x16x32_bf16 v[76:79], v[192:195], v[176:179], v[24:27]\n"
        "s_waitcnt lgkmcnt(6)\n"
        "ds_read_b64_tr_b16 a[176:177], v255 offset:6176\n"
        "ds_read_b64_tr_b16 a[178:179], v255 offset:6944\n"
        "v_exp_f32 v99, v99\n"
        "v_exp_f32 v73, v73\n"
        "v_mfma_f32_16x16x32_bf16 v[76:79], v[208:211], v[180:183], v[76:79]\n"
        "buffer_load_dwordx4 v130, s[20:23], s60 offen offset:2816 lds\n"
        "ds_read_b64_tr_b16 a[200:201], v255 offset:6208\n"
        "ds_read_b64_tr_b16 a[202:203], v255 offset:6976\n"
        "v_cvt_pk_bf16_f32 v231, v94, v95\n"
        "v_cvt_pk_bf16_f32 v67, v98, v99\n"
        "v_mfma_f32_16x16x32_bf16 v[104:107], v[212:215], v[156:159], v[12:15]\n"
        ".long 0xbec200ff\n"
        "42:\n"
        "s_branch 60b\n"
        "s_add_u32 m0, s40, 98304\n"
        "ds_read_b128 a[224:227], v240 offset:1664\n"
        "v_exp_f32 v74, v74\n"
        "v_mfma_f32_16x16x32_bf16 v[104:107], v[228:231], v[164:167], v[104:107]\n"
        "s_sub_u32 s64, s47, 6291456\n"
        "v_cvt_pk_bf16_f32 v72, v72, v73\n"
        "v_exp_f32 v100, v100\n"
        "v_mfma_f32_16x16x32_bf16 v[108:111], v[212:215], v[176:179], v[28:31]\n"
        "buffer_load_dwordx4 v242, s[8:11], s64 offen lds\n"
        "v_mfma_f32_16x16x32_bf16 v[108:111], v[228:231], v[184:187], v[108:111]\n"
        "s_add_u32 m0, s40, 99328\n"
        "v_exp_f32 v75, v75\n"
        "v_mfma_f32_16x16x32_bf16 v[80:83], v[152:155], v[196:199], v[32:35]\n"
        "ds_read_b128 a[228:231], v240 offset:12288\n"
        "v_exp_f32 v101, v101\n"
        "v_mfma_f32_16x16x32_bf16 v[80:83], v[168:171], v[200:203], v[80:83]\n"
        "buffer_load_dwordx4 v243, s[8:11], s64 offen lds\n"
        "v_cvt_pk_bf16_f32 v73, v74, v75\n"
        "v_mfma_f32_16x16x32_bf16 v[112:115], a[240:243], a[144:147], 0\n"
        "s_add_u32 m0, s40, 100352\n"
        "ds_read_b128 a[232:235], v241 offset:12288\n"
        "ds_read_b128 a[236:239], v240 offset:12416\n"
        "v_cvt_pk_bf16_f32 v74, v100, v101\n"
        "v_mfma_f32_16x16x32_bf16 v[112:115], a[244:247], a[148:151], v[112:115]\n"
        "s_waitcnt lgkmcnt(3)\n"
        "v_exp_f32 v102, v102\n"
        "v_mfma_f32_16x16x32_bf16 v[112:115], a[248:251], a[152:155], v[112:115]\n"
        "buffer_load_dwordx4 v244, s[8:11], s64 offen lds\n"
        "v_mfma_f32_16x16x32_bf16 v[116:119], a[248:251], a[176:179], 0\n"
        "s_add_u32 m0, s40, 110592\n"
        "v_exp_f32 v68, v68\n"
        "v_mfma_f32_16x16x32_bf16 v[116:119], a[244:247], a[172:175], v[116:119]\n"
        "s_sub_u32 s64, s47, 5505024\n"
        "v_exp_f32 v103, v103\n"
        "v_mfma_f32_16x16x32_bf16 v[116:119], a[240:243], a[168:171], v[116:119]\n"
        "buffer_load_dwordx4 v242, s[8:11], s64 offen lds\n"
        "v_cvt_pk_bf16_f32 v75, v102, v103\n"
        "v_mfma_f32_16x16x32_bf16 v[96:99], v[172:175], v[196:199], v[36:39]\n"
        "s_add_u32 m0, s40, 111616\n"
        "s_waitcnt vmcnt(10)\n"
        "ds_read_b64_tr_b16 a[156:157], v255 offset:9216\n"
        "ds_read_b64_tr_b16 a[158:159], v255 offset:9984\n"
        "v_mfma_f32_16x16x32_bf16 v[96:99], v[188:191], v[204:207], v[96:99]\n"
        "ds_read_b64_tr_b16 a[180:181], v255 offset:9248\n"
        "ds_read_b64_tr_b16 a[182:183], v255 offset:10016\n"
        "ds_read_b128 v[202:205], v252 offset:4096\n"
        "v_mfma_f32_16x16x32_bf16 v[120:123], a[240:243], a[192:195], 0\n"
        "buffer_load_dwordx4 v243, s[8:11], s64 offen lds\n"
        "ds_read_b64_tr_b16 a[204:205], v255 offset:9280\n"
        "v_mfma_f32_16x16x32_bf16 v[120:123], a[244:247], a[196:199], v[120:123]\n"
        "ds_read_b64_tr_b16 a[206:207], v255 offset:10048\n"
        "s_add_u32 m0, s40, 112640\n"
        "ds_read_b128 a[240:243], v240 offset:13824\n"
        "v_mfma_f32_16x16x32_bf16 v[120:123], a[248:251], a[200:203], v[120:123]\n"
        "s_waitcnt vmcnt(8)\n"
        "ds_read_b64_tr_b16 a[160:161], v255 offset:12288\n"
        "ds_read_b64_tr_b16 a[162:163], v255 offset:13056\n"
        "v_mfma_f32_16x16x32_bf16 v[124:127], a[216:219], a[144:147], 0\n"
        "buffer_load_dwordx4 v244, s[8:11], s64 offen lds\n"
        "ds_read_b64_tr_b16 a[184:185], v255 offset:12320\n"
        "v_mfma_f32_16x16x32_bf16 v[124:127], a[220:223], a[148:151], v[124:127]\n"
        "ds_read_b64_tr_b16 a[186:187], v255 offset:13088\n"
        "s_xor_b32 m0, s48, 8192\n"
        "ds_read_b64_tr_b16 a[208:209], v255 offset:12352\n"
        "ds_read_b64_tr_b16 a[210:211], v255 offset:13120\n"
        "v_mfma_f32_16x16x32_bf16 v[124:127], a[224:227], a[152:155], v[124:127]\n"
        "s_sub_u32 s64, s36, 2097152\n"
        "ds_read_b128 a[244:247], v241 offset:13824\n"
        "ds_read_b128 a[248:251], v240 offset:13952\n"
        "v_mfma_f32_16x16x32_bf16 v[156:159], a[224:227], a[176:179], 0\n"
        "buffer_load_dwordx4 v249, s[12:15], s64 offen lds\n"
        "v_mfma_f32_16x16x32_bf16 v[156:159], a[220:223], a[172:175], v[156:159]\n"
        "ds_read_b128 v[222:225], v251 offset:4608\n"
        "v_exp_f32 v69, v69\n"
        "v_mfma_f32_16x16x32_bf16 v[156:159], a[216:219], a[168:171], v[156:159]\n"
        "v_exp_f32 v70, v70\n"
        "v_cvt_pk_bf16_f32 v68, v68, v69\n"
        "v_mfma_f32_16x16x32_bf16 v[84:87], v[192:195], v[196:199], v[40:43]\n"
        "s_waitcnt lgkmcnt(7)\n"
        "v_exp_f32 v76, v76\n"
        "v_mfma_f32_16x16x32_bf16 v[84:87], v[208:211], v[200:203], v[84:87]\n"
        "buffer_load_dwordx4 v253, s[12:15], s64 offen offset:1024 lds\n"
        "v_mfma_f32_16x16x32_bf16 v[160:163], a[216:219], a[192:195], 0\n"
        "s_add_u32 m0, s65, 12288\n"
        "v_exp_f32 v71, v71\n"
        "v_mfma_f32_16x16x32_bf16 v[160:163], a[220:223], a[196:199], v[160:163]\n"
        "ds_read_b128 a[216:219], v241 offset:128\n"
        "v_exp_f32 v77, v77\n"
        "v_mfma_f32_16x16x32_bf16 v[160:163], a[224:227], a[200:203], v[160:163]\n"
        "ds_read_b128 a[220:223], v240 offset:256\n"
        "v_cvt_pk_bf16_f32 v69, v70, v71\n"
        "v_cvt_pk_bf16_f32 v76, v76, v77\n"
        "v_mfma_f32_16x16x32_bf16 v[164:167], a[228:231], a[144:147], 0\n"
        "s_waitcnt lgkmcnt(2)\n"
        "buffer_load_dword v136, s[52:55], 0 offen lds\n"
        "v_mfma_f32_16x16x32_bf16 v[164:167], a[232:235], a[148:151], v[164:167]\n"
        "ds_read_b128 a[224:227], v241 offset:256\n"
        "v_exp_f32 v78, v78\n"
        "v_mfma_f32_16x16x32_bf16 v[164:167], a[236:239], a[152:155], v[164:167]\n"
        "v_exp_f32 v104, v104\n"
        "v_mfma_f32_16x16x32_bf16 v[100:103], v[212:215], v[196:199], v[44:47]\n"
        "v_exp_f32 v79, v79\n"
        "v_mfma_f32_16x16x32_bf16 v[100:103], v[228:231], v[204:207], v[100:103]\n"
        "v_exp_f32 v105, v105\n"
        "v_cvt_pk_bf16_f32 v77, v78, v79\n"
        "v_mfma_f32_16x16x32_bf16 v[176:179], a[236:239], a[176:179], 0\n"
        "v_exp_f32 v106, v106\n"
        "v_cvt_pk_bf16_f32 v70, v104, v105\n"
        "v_mfma_f32_16x16x32_bf16 v[176:179], a[232:235], a[172:175], v[176:179]\n"
        "v_exp_f32 v108, v108\n"
        "v_mfma_f32_16x16x32_bf16 v[176:179], a[228:231], a[168:171], v[176:179]\n"
        "s_waitcnt vmcnt(9)\n"
        "ds_read_b64_tr_b16 a[164:165], v255 offset:15360\n"
        "ds_read_b64_tr_b16 a[166:167], v255 offset:16128\n"
        "v_mfma_f32_16x16x32_bf16 v[180:183], a[228:231], a[192:195], 0\n"
        "ds_read_b64_tr_b16 a[188:189], v255 offset:15392\n"
        "ds_read_b64_tr_b16 a[190:191], v255 offset:16160\n"
        "v_mfma_f32_16x16x32_bf16 v[180:183], a[232:235], a[196:199], v[180:183]\n"
        "ds_read_b64_tr_b16 a[212:213], v255 offset:15424\n"
        "ds_read_b64_tr_b16 a[214:215], v255 offset:16192\n"
        "v_mfma_f32_16x16x32_bf16 v[180:183], a[236:239], a[200:203], v[180:183]\n"
        "ds_read_b128 a[228:231], v241 offset:1664\n"
        "ds_read_b128 a[232:235], v240 offset:1792\n"
        "v_mfma_f32_16x16x32_bf16 v[184:187], a[240:243], a[144:147], 0\n"
        "ds_read_b128 a[236:239], v241 offset:1792\n"
        "v_exp_f32 v107, v107\n"
        "v_mfma_f32_16x16x32_bf16 v[184:187], a[244:247], a[148:151], v[184:187]\n"
        "v_exp_f32 v109, v109\n"
        "v_cvt_pk_bf16_f32 v71, v106, v107\n"
        "v_mfma_f32_16x16x32_bf16 v[184:187], a[248:251], a[152:155], v[184:187]\n"
        "v_exp_f32 v110, v110\n"
        "v_cvt_pk_bf16_f32 v78, v108, v109\n"
        "v_mfma_f32_16x16x32_bf16 v[196:199], a[248:251], a[176:179], 0\n"
        "v_exp_f32 v111, v111\n"
        "v_mfma_f32_16x16x32_bf16 v[196:199], a[244:247], a[172:175], v[196:199]\n"
        "v_cvt_pk_bf16_f32 v79, v110, v111\n"
        "v_exp_f32 v80, v80\n"
        "v_mfma_f32_16x16x32_bf16 v[196:199], a[240:243], a[168:171], v[196:199]\n"
        "v_exp_f32 v81, v81\n"
        "v_mfma_f32_16x16x32_bf16 v[200:203], a[240:243], a[192:195], 0\n"
        "v_exp_f32 v82, v82\n"
        "v_cvt_pk_bf16_f32 v80, v80, v81\n"
        "v_mfma_f32_16x16x32_bf16 v[200:203], a[244:247], a[196:199], v[200:203]\n"
        "ds_read_b128 a[240:243], v241 offset:12416\n"
        "v_exp_f32 v83, v83\n"
        "v_mfma_f32_16x16x32_bf16 v[200:203], a[248:251], a[200:203], v[200:203]\n"
        "ds_read_b128 a[244:247], v240 offset:12544\n"
        "v_cvt_pk_bf16_f32 v81, v82, v83\n"
        "v_mfma_f32_16x16x32_bf16 v[88:91], v[152:155], v[216:219], v[48:51]\n"
        "ds_read_b128 a[248:251], v241 offset:12544\n"
        "v_exp_f32 v96, v96\n"
        "v_mfma_f32_16x16x32_bf16 v[88:91], v[168:171], v[220:223], v[88:91]\n"
        "s_waitcnt lgkmcnt(3)\n"
        "v_exp_f32 v97, v97\n"
        "v_mfma_f32_16x16x32_bf16 v[112:115], a[216:219], a[156:159], v[112:115]\n"
        "v_exp_f32 v98, v98\n"
        "v_cvt_pk_bf16_f32 v82, v96, v97\n"
        "v_mfma_f32_16x16x32_bf16 v[112:115], a[220:223], a[160:163], v[112:115]\n"
        "v_exp_f32 v99, v99\n"
        "v_mfma_f32_16x16x32_bf16 v[112:115], a[224:227], a[164:167], v[112:115]\n"
        "v_cvt_pk_bf16_f32 v83, v98, v99\n"
        "v_exp_f32 v84, v84\n"
        "v_mfma_f32_16x16x32_bf16 v[116:119], a[224:227], a[188:191], v[116:119]\n"
        "v_exp_f32 v85, v85\n"
        "v_mfma_f32_16x16x32_bf16 v[116:119], a[220:223], a[184:187], v[116:119]\n"
        "v_exp_f32 v86, v86\n"
        "v_cvt_pk_bf16_f32 v84, v84, v85\n"
        "v_mfma_f32_16x16x32_bf16 v[116:119], a[216:219], a[180:183], v[116:119]\n"
        "v_exp_f32 v87, v87\n"
        "v_mfma_f32_16x16x32_bf16 v[120:123], a[216:219], a[204:207], v[120:123]\n"
        "v_cvt_pk_bf16_f32 v85, v86, v87\n"
        "v_exp_f32 v100, v100\n"
        "v_mfma_f32_16x16x32_bf16 v[120:123], a[220:223], a[208:211], v[120:123]\n"
        "ds_read_b128 a[216:219], v241 offset:13952\n"
        "v_exp_f32 v101, v101\n"
        "v_mfma_f32_16x16x32_bf16 v[120:123], a[224:227], a[212:215], v[120:123]\n"
        "ds_read_b128 a[220:223], v240 offset:14080\n"
        "v_exp_f32 v102, v102\n"
        "v_mfma_f32_16x16x32_bf16 v[124:127], a[228:231], a[156:159], v[124:127]\n"
        "ds_read_b128 a[224:227], v241 offset:14080\n"
        "v_cvt_pk_bf16_f32 v86, v100, v101\n"
        "v_cvt_pk_bf16_f32 v128, v112, v113\n"
        "v_mfma_f32_16x16x32_bf16 v[124:127], a[232:235], a[160:163], v[124:127]\n"
        "v_exp_f32 v103, v103\n"
        "v_cvt_pk_bf16_f32 v129, v114, v115\n"
        "v_mfma_f32_16x16x32_bf16 v[124:127], a[236:239], a[164:167], v[124:127]\n"
        "v_cvt_pk_bf16_f32 v87, v102, v103\n"
        "v_cvt_pk_bf16_f32 v136, v116, v117\n"
        "v_mfma_f32_16x16x32_bf16 v[104:107], v[172:175], v[216:219], v[52:55]\n"
        "v_cvt_pk_bf16_f32 v137, v118, v119\n"
        "v_cvt_pk_bf16_f32 v144, v120, v121\n"
        "v_mfma_f32_16x16x32_bf16 v[104:107], v[188:191], v[224:227], v[104:107]\n"
        "ds_read_b128 v[222:225], v252 offset:4608\n"
        "v_cvt_pk_bf16_f32 v145, v122, v123\n"
        "v_mfma_f32_16x16x32_bf16 v[156:159], a[236:239], a[188:191], v[156:159]\n"
        "v_exp_f32 v88, v88\n"
        "v_mfma_f32_16x16x32_bf16 v[156:159], a[232:235], a[184:187], v[156:159]\n"
        "v_cvt_pk_bf16_f32 v130, v124, v125\n"
        "v_cvt_pk_bf16_f32 v131, v126, v127\n"
        "v_mfma_f32_16x16x32_bf16 v[156:159], a[228:231], a[180:183], v[156:159]\n"
        "v_exp_f32 v89, v89\n"
        "v_mfma_f32_16x16x32_bf16 v[160:163], a[228:231], a[204:207], v[160:163]\n"
        "v_exp_f32 v90, v90\n"
        "v_mfma_f32_16x16x32_bf16 v[160:163], a[232:235], a[208:211], v[160:163]\n"
        "v_exp_f32 v91, v91\n"
        "v_mfma_f32_16x16x32_bf16 v[160:163], a[236:239], a[212:215], v[160:163]\n"
        "s_waitcnt lgkmcnt(1)\n"
        "v_exp_f32 v104, v104\n"
        "v_mfma_f32_16x16x32_bf16 v[164:167], a[240:243], a[156:159], v[164:167]\n"
        "v_cvt_pk_bf16_f32 v138, v156, v157\n"
        "v_cvt_pk_bf16_f32 v139, v158, v159\n"
        "v_mfma_f32_16x16x32_bf16 v[164:167], a[244:247], a[160:163], v[164:167]\n"
        "v_cvt_pk_bf16_f32 v88, v88, v89\n"
        "v_cvt_pk_bf16_f32 v89, v90, v91\n"
        "v_mfma_f32_16x16x32_bf16 v[164:167], a[248:251], a[164:167], v[164:167]\n"
        "v_exp_f32 v105, v105\n"
        "v_mfma_f32_16x16x32_bf16 v[92:95], v[192:195], v[216:219], v[56:59]\n"
        "s_waitcnt lgkmcnt(0)\n"
        "v_cvt_pk_bf16_f32 v146, v160, v161\n"
        "v_cvt_pk_bf16_f32 v147, v162, v163\n"
        "v_mfma_f32_16x16x32_bf16 v[92:95], v[208:211], v[220:223], v[92:95]\n"
        "v_exp_f32 v106, v106\n"
        "v_mfma_f32_16x16x32_bf16 v[176:179], a[248:251], a[188:191], v[176:179]\n"
        "v_exp_f32 v107, v107\n"
        "v_mfma_f32_16x16x32_bf16 v[176:179], a[244:247], a[184:187], v[176:179]\n"
        "v_cvt_pk_bf16_f32 v132, v164, v165\n"
        "v_cvt_pk_bf16_f32 v133, v166, v167\n"
        "v_mfma_f32_16x16x32_bf16 v[176:179], a[240:243], a[180:183], v[176:179]\n"
        "v_cvt_pk_bf16_f32 v90, v104, v105\n"
        "v_cvt_pk_bf16_f32 v91, v106, v107\n"
        "v_mfma_f32_16x16x32_bf16 v[180:183], a[240:243], a[204:207], v[180:183]\n"
        "v_exp_f32 v92, v92\n"
        "v_mfma_f32_16x16x32_bf16 v[180:183], a[244:247], a[208:211], v[180:183]\n"
        "v_exp_f32 v93, v93\n"
        "v_mfma_f32_16x16x32_bf16 v[180:183], a[248:251], a[212:215], v[180:183]\n"
        "v_mov_b64 v[96:97], 0\n"
        "v_cvt_pk_bf16_f32 v140, v176, v177\n"
        "v_mfma_f32_16x16x32_bf16 v[184:187], a[216:219], a[156:159], v[184:187]\n"
        "v_cvt_pk_bf16_f32 v141, v178, v179\n"
        "v_cvt_pk_bf16_f32 v92, v92, v93\n"
        "v_mfma_f32_16x16x32_bf16 v[184:187], a[220:223], a[160:163], v[184:187]\n"
        "v_exp_f32 v94, v94\n"
        "v_mfma_f32_16x16x32_bf16 v[184:187], a[224:227], a[164:167], v[184:187]\n"
        "v_exp_f32 v95, v95\n"
        "v_mfma_f32_16x16x32_bf16 v[108:111], v[212:215], v[216:219], v[60:63]\n"
        "v_cvt_pk_bf16_f32 v148, v180, v181\n"
        "v_cvt_pk_bf16_f32 v149, v182, v183\n"
        "v_mfma_f32_16x16x32_bf16 v[108:111], v[228:231], v[224:227], v[108:111]\n"
        "v_add_u32 v240, 24576, v240\n"
        "v_add_u32 v241, 24576, v241\n"
        "v_mfma_f32_16x16x32_bf16 v[196:199], a[224:227], a[188:191], v[196:199]\n"
        "v_cvt_pk_bf16_f32 v134, v184, v185\n"
        "v_cvt_pk_bf16_f32 v135, v186, v187\n"
        "v_mfma_f32_16x16x32_bf16 v[196:199], a[220:223], a[184:187], v[196:199]\n"
        "v_mov_b64 v[172:173], 0\n"
        "v_cvt_pk_bf16_f32 v93, v94, v95\n"
        "v_mfma_f32_16x16x32_bf16 v[196:199], a[216:219], a[180:183], v[196:199]\n"
        "v_exp_f32 v108, v108\n"
        "v_mfma_f32_16x16x32_bf16 v[200:203], a[216:219], a[204:207], v[200:203]\n"
        "v_exp_f32 v109, v109\n"
        "v_mfma_f32_16x16x32_bf16 v[200:203], a[220:223], a[208:211], v[200:203]\n"
        "v_exp_f32 v110, v110\n"
        "v_mfma_f32_16x16x32_bf16 v[200:203], a[224:227], a[212:215], v[200:203]\n"
        "v_exp_f32 v111, v111\n"
        "v_smfmac_f32_16x16x64_bf16 v[96:99], a[252:255], v[64:71], v237\n"
        "v_cvt_pk_bf16_f32 v142, v196, v197\n"
        "v_mfma_f32_16x16x32_bf16 v[100:103], v[132:135], v[68:71], 0\n"
        "v_cvt_pk_bf16_f32 v143, v198, v199\n"
        "v_cvt_pk_bf16_f32 v94, v108, v109\n"
        "v_mfma_f32_16x16x32_bf16 v[100:103], v[128:131], v[64:67], v[100:103]\n"
        "v_mov_b64 v[192:193], 0\n"
        "v_cvt_pk_bf16_f32 v150, v200, v201\n"
        "v_mfma_f32_16x16x32_bf16 v[104:107], v[136:139], v[64:67], 0\n"
        "v_cvt_pk_bf16_f32 v151, v202, v203\n"
        "v_add_f32 v97, v96, v97\n"
        "v_mfma_f32_16x16x32_bf16 v[104:107], v[140:143], v[68:71], v[104:107]\n"
        "v_rcp_f32 v96, v97\n"
        "v_cvt_pk_bf16_f32 v95, v110, v111\n"
        "v_mfma_f32_16x16x32_bf16 v[108:111], v[148:151], v[68:71], 0\n"
        "v_mul_f32 v100, v96, v100\n"
        "v_mul_f32 v101, v96, v101\n"
        "v_mfma_f32_16x16x32_bf16 v[108:111], v[144:147], v[64:67], v[108:111]\n"
        "v_mul_f32 v102, v96, v102\n"
        "v_mul_f32 v103, v96, v103\n"
        "v_smfmac_f32_16x16x64_bf16 v[172:175], a[252:255], v[72:79], v237\n"
        "s_waitcnt vmcnt(6)\n"
        "s_barrier\n"
        "ds_read_b128 a[236:239], v240 offset:0\n"
        "v_mfma_f32_16x16x32_bf16 v[176:179], v[132:135], v[76:79], 0\n"
        "ds_read_b128 a[240:243], v241 offset:0\n"
        "ds_read_b128 a[244:247], v240 offset:128\n"
        "v_mfma_f32_16x16x32_bf16 v[176:179], v[128:131], v[72:75], v[176:179]\n"
        "ds_read_b128 a[248:251], v241 offset:128\n"
        "ds_read_b128 a[216:219], v240 offset:256\n"
        "v_mfma_f32_16x16x32_bf16 v[180:183], v[136:139], v[72:75], 0\n"
        "ds_read_b128 a[220:223], v241 offset:256\n"
        "v_cvt_pk_bf16_f32 v100, v100, v101\n"
        "v_mfma_f32_16x16x32_bf16 v[180:183], v[140:143], v[76:79], v[180:183]\n"
        "v_cvt_pk_bf16_f32 v101, v102, v103\n"
        "v_mul_f32 v104, v96, v104\n"
        "v_mfma_f32_16x16x32_bf16 v[184:187], v[148:151], v[76:79], 0\n"
        "v_mul_f32 v105, v96, v105\n"
        "v_mul_f32 v106, v96, v106\n"
        "v_mfma_f32_16x16x32_bf16 v[184:187], v[144:147], v[72:75], v[184:187]\n"
        "v_mul_f32 v107, v96, v107\n"
        "v_cvt_pk_bf16_f32 v102, v104, v105\n"
        "v_smfmac_f32_16x16x64_bf16 v[192:195], a[252:255], v[80:87], v237\n"
        "v_cvt_pk_bf16_f32 v103, v106, v107\n"
        "v_mfma_f32_16x16x32_bf16 v[196:199], v[132:135], v[84:87], 0\n"
        "ds_write_b128 v245, v[100:103] offset:0\n"
        "v_mfma_f32_16x16x32_bf16 v[196:199], v[128:131], v[80:83], v[196:199]\n"
        "buffer_load_dwordx4 v238, s[52:55], s63 offen lds\n"
        "v_mfma_f32_16x16x32_bf16 v[200:203], v[136:139], v[80:83], 0\n"
        "v_mov_b64 v[124:125], 0\n"
        "v_mul_f32 v108, v96, v108\n"
        "v_mfma_f32_16x16x32_bf16 v[200:203], v[140:143], v[84:87], v[200:203]\n"
        "v_mul_f32 v109, v96, v109\n"
        "v_mul_f32 v110, v96, v110\n"
        "v_mul_f32 v111, v96, v111\n"
        "v_mfma_f32_16x16x32_bf16 v[204:207], v[148:151], v[84:87], 0\n"
        "v_cvt_pk_bf16_f32 v108, v108, v109\n"
        "v_cvt_pk_bf16_f32 v109, v110, v111\n"
        "v_mfma_f32_16x16x32_bf16 v[204:207], v[144:147], v[80:83], v[204:207]\n"
        "s_waitcnt lgkmcnt(1)\n"
        "ds_write_b64 v246, v[108:109] offset:0\n"
        "v_mfma_f32_16x16x32_bf16 v[112:115], a[72:75], a[236:239], 0\n"
        "v_max_u32 v236, v96, v97\n"
        "v_add_f32 v173, v172, v173\n"
        "v_mfma_f32_16x16x32_bf16 v[112:115], a[76:79], a[240:243], v[112:115]\n"
        "v_rcp_f32 v172, v173\n"
        "v_mfma_f32_16x16x32_bf16 v[112:115], a[80:83], a[244:247], v[112:115]\n"
        "v_mul_f32 v176, v172, v176\n"
        "v_mul_f32 v177, v172, v177\n"
        "v_mfma_f32_16x16x32_bf16 v[112:115], a[84:87], a[248:251], v[112:115]\n"
        "buffer_load_dwordx4 v239, s[52:55], s63 offen offset:1024 lds\n"
        "v_mfma_f32_16x16x32_bf16 v[112:115], a[88:91], a[216:219], v[112:115]\n"
        "v_mul_f32 v178, v172, v178\n"
        "v_mul_f32 v179, v172, v179\n"
        "v_mfma_f32_16x16x32_bf16 v[112:115], a[92:95], a[220:223], v[112:115]\n"
        "v_cvt_pk_bf16_f32 v176, v176, v177\n"
        "v_cvt_pk_bf16_f32 v177, v178, v179\n"
        "v_smfmac_f32_16x16x64_bf16 v[124:127], a[252:255], v[88:95], v237\n"
        "v_mul_f32 v180, v172, v180\n"
        "v_mfma_f32_16x16x32_bf16 v[80:83], v[132:135], v[92:95], 0\n"
        "v_mul_f32 v181, v172, v181\n"
        "v_mul_f32 v182, v172, v182\n"
        "v_mfma_f32_16x16x32_bf16 v[80:83], v[128:131], v[88:91], v[80:83]\n"
        "v_cvt_pk_bf16_f32 v152, v112, v113\n"
        "v_cvt_pk_bf16_f32 v153, v114, v115\n"
        "v_mfma_f32_16x16x32_bf16 v[84:87], v[136:139], v[88:91], 0\n"
        "v_mul_f32 v183, v172, v183\n"
        "v_cvt_pk_bf16_f32 v178, v180, v181\n"
        "v_mfma_f32_16x16x32_bf16 v[84:87], v[140:143], v[92:95], v[84:87]\n"
        "v_cvt_pk_bf16_f32 v179, v182, v183\n"
        "v_mul_f32 v184, v172, v184\n"
        "v_mfma_f32_16x16x32_bf16 v[96:99], v[148:151], v[92:95], 0\n"
        "buffer_load_dwordx4 v247, s[52:55], s63 offen offset:2048 lds\n"
        "v_mfma_f32_16x16x32_bf16 v[96:99], v[144:147], v[88:91], v[96:99]\n"
        "ds_write_b128 v245, v[176:179] offset:256\n"
        "v_mfma_f32_16x16x32_bf16 v[116:119], a[116:119], a[220:223], 0\n"
        "v_mul_f32 v185, v172, v185\n"
        "v_mul_f32 v186, v172, v186\n"
        "v_mfma_f32_16x16x32_bf16 v[116:119], a[112:115], a[216:219], v[116:119]\n"
        "v_mul_f32 v187, v172, v187\n"
        "v_add_f32 v125, v124, v125\n"
        "v_mfma_f32_16x16x32_bf16 v[116:119], a[108:111], a[248:251], v[116:119]\n"
        "v_rcp_f32 v124, v125\n"
        "v_mfma_f32_16x16x32_bf16 v[116:119], a[104:107], a[244:247], v[116:119]\n"
        "s_add_u32 m0, s65, 15360\n"
        "v_cvt_pk_bf16_f32 v184, v184, v185\n"
        "v_cvt_pk_bf16_f32 v185, v186, v187\n"
        "v_mfma_f32_16x16x32_bf16 v[116:119], a[100:103], a[240:243], v[116:119]\n"
        "ds_write_b64 v246, v[184:185] offset:256\n"
        "v_mfma_f32_16x16x32_bf16 v[116:119], a[96:99], a[236:239], v[116:119]\n"
        "v_mul_f32 v80, v124, v80\n"
        "v_mul_f32 v81, v124, v81\n"
        "v_mfma_f32_16x16x32_bf16 v[120:123], a[120:123], a[236:239], 0\n"
        "buffer_load_dwordx4 v238, s[52:55], s34 offen lds\n"
        "v_mfma_f32_16x16x32_bf16 v[120:123], a[124:127], a[240:243], v[120:123]\n"
        "v_mul_f32 v82, v124, v82\n"
        "v_mul_f32 v83, v124, v83\n"
        "v_mfma_f32_16x16x32_bf16 v[120:123], a[128:131], a[244:247], v[120:123]\n"
        "v_max3_u32 v236, v236, v172, v173\n"
        "v_cvt_pk_bf16_f32 v80, v80, v81\n"
        "v_mfma_f32_16x16x32_bf16 v[120:123], a[132:135], a[248:251], v[120:123]\n"
        "v_cvt_pk_bf16_f32 v81, v82, v83\n"
        "v_mul_f32 v84, v124, v84\n"
        "v_mfma_f32_16x16x32_bf16 v[120:123], a[136:139], a[216:219], v[120:123]\n"
        "v_mul_f32 v85, v124, v85\n"
        "v_mul_f32 v86, v124, v86\n"
        "v_mfma_f32_16x16x32_bf16 v[120:123], a[140:143], a[220:223], v[120:123]\n"
        "v_mul_f32 v87, v124, v87\n"
        "v_cvt_pk_bf16_f32 v82, v84, v85\n"
        "v_mfma_f32_16x16x32_bf16 v[112:115], a[20:23], a[220:223], 0\n"
        "v_cvt_pk_bf16_f32 v83, v86, v87\n"
        "v_mul_f32 v96, v124, v96\n"
        "v_mfma_f32_16x16x32_bf16 v[112:115], a[16:19], a[216:219], v[112:115]\n"
        "ds_write_b128 v245, v[80:83] offset:768\n"
        "v_mfma_f32_16x16x32_bf16 v[112:115], a[12:15], a[248:251], v[112:115]\n"
        "buffer_load_dwordx4 v239, s[52:55], s34 offen offset:1024 lds\n"
        "v_mfma_f32_16x16x32_bf16 v[112:115], a[8:11], a[244:247], v[112:115]\n"
        "v_mul_f32 v97, v124, v97\n"
        "v_mul_f32 v98, v124, v98\n"
        "v_mfma_f32_16x16x32_bf16 v[112:115], a[4:7], a[240:243], v[112:115]\n"
        "v_mul_f32 v99, v124, v99\n"
        "v_cvt_pk_bf16_f32 v154, v116, v117\n"
        "v_mfma_f32_16x16x32_bf16 v[112:115], a[0:3], a[236:239], v[112:115]\n"
        "v_cvt_pk_bf16_f32 v155, v118, v119\n"
        "v_cvt_pk_bf16_f32 v96, v96, v97\n"
        "v_mfma_f32_16x16x32_bf16 v[116:119], a[24:27], a[236:239], 0\n"
        "v_cvt_pk_bf16_f32 v97, v98, v99\n"
        "v_cvt_pk_bf16_f32 v168, v120, v121\n"
        "v_mfma_f32_16x16x32_bf16 v[116:119], a[28:31], a[240:243], v[116:119]\n"
        "ds_write_b64 v246, v[96:97] offset:768\n"
        "v_mfma_f32_16x16x32_bf16 v[116:119], a[32:35], a[244:247], v[116:119]\n"
        "v_cvt_pk_bf16_f32 v169, v122, v123\n"
        "v_add_f32 v193, v192, v193\n"
        "v_mfma_f32_16x16x32_bf16 v[116:119], a[36:39], a[248:251], v[116:119]\n"
        "v_rcp_f32 v192, v193\n"
        "v_mfma_f32_16x16x32_bf16 v[116:119], a[40:43], a[216:219], v[116:119]\n"
        "buffer_load_dwordx4 v247, s[52:55], s34 offen offset:2048 lds\n"
        "v_mfma_f32_16x16x32_bf16 v[116:119], a[44:47], a[220:223], v[116:119]\n"
        "v_mul_f32 v196, v192, v196\n"
        "v_mul_f32 v197, v192, v197\n"
        "v_mfma_f32_16x16x32_bf16 v[120:123], a[68:71], a[220:223], 0\n"
        "v_mul_f32 v198, v192, v198\n"
        "v_mul_f32 v199, v192, v199\n"
        "v_mfma_f32_16x16x32_bf16 v[120:123], a[64:67], a[216:219], v[120:123]\n"
        "v_cvt_pk_bf16_f32 v156, v112, v113\n"
        "v_cvt_pk_bf16_f32 v157, v114, v115\n"
        "v_mfma_f32_16x16x32_bf16 v[120:123], a[60:63], a[248:251], v[120:123]\n"
        "ds_read_b128 a[224:227], v240 offset:1536\n"
        "v_cvt_pk_bf16_f32 v196, v196, v197\n"
        "v_mfma_f32_16x16x32_bf16 v[120:123], a[56:59], a[244:247], v[120:123]\n"
        "s_add_u32 m0, s65, 0\n"
        "ds_read_b128 a[228:231], v241 offset:1536\n"
        "v_cvt_pk_bf16_f32 v197, v198, v199\n"
        "v_mfma_f32_16x16x32_bf16 v[120:123], a[52:55], a[240:243], v[120:123]\n"
        "ds_read_b128 a[232:235], v240 offset:1664\n"
        "v_mul_f32 v200, v192, v200\n"
        "v_mfma_f32_16x16x32_bf16 v[120:123], a[48:51], a[236:239], v[120:123]\n"
        "v_mul_f32 v201, v192, v201\n"
        "v_mul_f32 v202, v192, v202\n"
        "v_mfma_f32_16x16x32_bf16 v[112:115], a[236:239], a[144:147], 0\n"
        "buffer_load_dwordx4 v232, s[52:55], 0 offen lds\n"
        "v_mfma_f32_16x16x32_bf16 v[112:115], a[240:243], a[148:151], v[112:115]\n"
        "v_mul_f32 v203, v192, v203\n"
        "v_cvt_pk_bf16_f32 v198, v200, v201\n"
        "v_mfma_f32_16x16x32_bf16 v[112:115], a[244:247], a[152:155], v[112:115]\n"
        "v_cvt_pk_bf16_f32 v199, v202, v203\n"
        "v_mul_f32 v204, v192, v204\n"
        "v_mfma_f32_16x16x32_bf16 v[112:115], a[248:251], a[156:159], v[112:115]\n"
        "ds_write_b128 v245, v[196:199] offset:512\n"
        "v_mfma_f32_16x16x32_bf16 v[112:115], a[216:219], a[160:163], v[112:115]\n"
        "v_mul_f32 v205, v192, v205\n"
        "v_mul_f32 v206, v192, v206\n"
        "v_mfma_f32_16x16x32_bf16 v[112:115], a[220:223], a[164:167], v[112:115]\n"
        "v_mul_f32 v207, v192, v207\n"
        "v_cvt_pk_bf16_f32 v158, v116, v117\n"
        "v_cvt_pk_bf16_f32 v159, v118, v119\n"
        "v_mfma_f32_16x16x32_bf16 v[116:119], a[220:223], a[188:191], 0\n"
        "v_cvt_pk_bf16_f32 v204, v204, v205\n"
        "v_cvt_pk_bf16_f32 v205, v206, v207\n"
        "v_mfma_f32_16x16x32_bf16 v[116:119], a[216:219], a[184:187], v[116:119]\n"
        "ds_write_b64 v246, v[204:205] offset:512\n"
        "v_mfma_f32_16x16x32_bf16 v[116:119], a[248:251], a[180:183], v[116:119]\n"
        "buffer_load_dwordx4 v233, s[52:55], 0 offen offset:1024 lds\n"
        "v_mfma_f32_16x16x32_bf16 v[116:119], a[244:247], a[176:179], v[116:119]\n"
        "s_waitcnt vmcnt(5)\n"
        "ds_read_b64_tr_b16 v[64:65], v255 offset:12288\n"
        "ds_read_b64_tr_b16 v[66:67], v255 offset:13056\n"
        "v_mfma_f32_16x16x32_bf16 v[116:119], a[240:243], a[172:175], v[116:119]\n"
        "v_max3_u32 v236, v236, v192, v193\n"
        "v_max3_u32 v236, v236, v124, v125\n"
        "v_mfma_f32_16x16x32_bf16 v[116:119], a[236:239], a[168:171], v[116:119]\n"
        "ds_read_b64_tr_b16 v[68:69], v255 offset:12320\n"
        "ds_read_b64_tr_b16 v[70:71], v255 offset:13088\n"
        "v_cvt_pk_bf16_f32 v160, v120, v121\n"
        "v_cvt_pk_bf16_f32 v161, v122, v123\n"
        "v_cvt_pk_bf16_f32 v166, v120, v121\n"
        "v_cvt_pk_bf16_f32 v167, v122, v123\n"
        "v_mfma_f32_16x16x32_bf16 v[120:123], a[236:239], a[192:195], 0\n"
        "ds_read_b128 a[236:239], v241 offset:1664\n"
        "v_cmp_lt_u32 vcc, s24, v236\n"
        "v_mfma_f32_16x16x32_bf16 v[120:123], a[240:243], a[196:199], v[120:123]\n"
        "ds_read_b128 a[240:243], v240 offset:1792\n"
        "s_cmp_lg_u64 vcc, 0\n"
        "s_addc_u32 s25, s25, s25\n"
        "v_mfma_f32_16x16x32_bf16 v[120:123], a[244:247], a[200:203], v[120:123]\n"
        "ds_read_b64_tr_b16 v[72:73], v255 offset:12352\n"
        "ds_read_b64_tr_b16 v[74:75], v255 offset:13120\n"
        "v_mfma_f32_16x16x32_bf16 v[120:123], a[248:251], a[204:207], v[120:123]\n"
        "ds_read_b128 a[244:247], v241 offset:1792\n"
        "v_cvt_pk_bf16_f32 v128, v112, v113\n"
        "v_mfma_f32_16x16x32_bf16 v[120:123], a[216:219], a[208:211], v[120:123]\n"
        "s_waitcnt lgkmcnt(7)\n"
        "ds_write_b128 v235, v[64:67] offset:12288\n"
        "v_mfma_f32_16x16x32_bf16 v[120:123], a[220:223], a[212:215], v[120:123]\n"
        "v_cvt_pk_bf16_f32 v129, v114, v115\n"
        "v_and_b32 v239, 32, v245\n"
        "v_mfma_f32_16x16x32_bf16 v[112:115], a[72:75], a[224:227], 0\n"
        "buffer_load_dwordx4 v234, s[52:55], 0 offen offset:2048 lds\n"
        "v_mfma_f32_16x16x32_bf16 v[112:115], a[76:79], a[228:231], v[112:115]\n"
        "s_waitcnt vmcnt(3)\n"
        "ds_read_b64_tr_b16 v[76:77], v255 offset:15360\n"
        "ds_read_b64_tr_b16 v[78:79], v255 offset:16128\n"
        "v_mfma_f32_16x16x32_bf16 v[112:115], a[80:83], a[232:235], v[112:115]\n"
        "s_waitcnt lgkmcnt(6)\n"
        "v_cvt_pk_bf16_f32 v136, v116, v117\n"
        "v_cvt_pk_bf16_f32 v137, v118, v119\n"
        "v_mfma_f32_16x16x32_bf16 v[112:115], a[84:87], a[236:239], v[112:115]\n"
        "s_add_u32 m0, s65, 3072\n"
        "v_xor_b32 v251, 0x2000, v251\n"
        "v_xor_b32 v252, 0x2000, v252\n"
        "v_mfma_f32_16x16x32_bf16 v[112:115], a[88:91], a[240:243], v[112:115]\n"
        "s_waitcnt lgkmcnt(3)\n"
        "v_mad_i32_i24 v239, v239, -12, v235\n"
        "v_cvt_pk_bf16_f32 v144, v120, v121\n"
        "v_mfma_f32_16x16x32_bf16 v[112:115], a[92:95], a[244:247], v[112:115]\n"
        "ds_write_b128 v239, v[68:71] offset:12480\n"
        "s_barrier\n"
        "v_mfma_f32_16x16x32_bf16 v[116:119], a[116:119], a[244:247], 0\n"
        "buffer_load_dwordx4 v232, s[52:55], s61 offen lds\n"
        "v_mfma_f32_16x16x32_bf16 v[116:119], a[112:115], a[240:243], v[116:119]\n"
        "ds_read_b64_tr_b16 v[88:89], v255 offset:15392\n"
        "ds_read_b64_tr_b16 v[90:91], v255 offset:16160\n"
        "v_mfma_f32_16x16x32_bf16 v[116:119], a[108:111], a[236:239], v[116:119]\n"
        "v_cvt_pk_bf16_f32 v145, v122, v123\n"
        "v_cvt_pk_bf16_f32 v172, v112, v113\n"
        "v_mfma_f32_16x16x32_bf16 v[116:119], a[104:107], a[232:235], v[116:119]\n"
        "v_cvt_pk_bf16_f32 v173, v114, v115\n"
        "v_add_u32 v247, s45, v245\n"
        "v_mfma_f32_16x16x32_bf16 v[116:119], a[100:103], a[228:231], v[116:119]\n"
        "v_mfma_f32_16x16x32_bf16 v[116:119], a[96:99], a[224:227], v[116:119]\n"
        "ds_write_b128 v254, v[72:75] offset:12288\n"
        "v_mfma_f32_16x16x32_bf16 v[120:123], a[120:123], a[224:227], 0\n"
        "s_nop 2\n"
        "v_mfma_f32_16x16x32_bf16 v[120:123], a[124:127], a[228:231], v[120:123]\n"
        "ds_read_b128 v[162:165], v251 offset:0\n"
        "s_nop 0\n"
        "v_mfma_f32_16x16x32_bf16 v[120:123], a[128:131], a[232:235], v[120:123]\n"
        "buffer_load_dwordx4 v233, s[52:55], s61 offen offset:1024 lds\n"
        "v_mfma_f32_16x16x32_bf16 v[120:123], a[132:135], a[236:239], v[120:123]\n"
        "ds_read_b64_tr_b16 v[92:93], v255 offset:15424\n"
        "ds_read_b64_tr_b16 v[94:95], v255 offset:16192\n"
        "v_mfma_f32_16x16x32_bf16 v[120:123], a[136:139], a[240:243], v[120:123]\n"
        "v_cvt_pk_bf16_f32 v174, v116, v117\n"
        "v_cvt_pk_bf16_f32 v175, v118, v119\n"
        "v_mfma_f32_16x16x32_bf16 v[120:123], a[140:143], a[244:247], v[120:123]\n"
        "s_nop 2\n"
        "v_mfma_f32_16x16x32_bf16 v[112:115], a[20:23], a[244:247], 0\n"
        "s_nop 2\n"
        "v_mfma_f32_16x16x32_bf16 v[112:115], a[16:19], a[240:243], v[112:115]\n"
        "v_cvt_pk_bf16_f32 v190, v120, v121\n"
        "v_cvt_pk_bf16_f32 v191, v122, v123\n"
        "v_mfma_f32_16x16x32_bf16 v[112:115], a[12:15], a[236:239], v[112:115]\n"
        "v_mfma_f32_16x16x32_bf16 v[112:115], a[8:11], a[232:235], v[112:115]\n"
        "s_waitcnt vmcnt(2)\n"
        "ds_read_b64_tr_b16 v[212:213], v255 offset:0\n"
        "ds_read_b64_tr_b16 v[214:215], v255 offset:768\n"
        "v_mfma_f32_16x16x32_bf16 v[112:115], a[4:7], a[228:231], v[112:115]\n"
        "buffer_load_dwordx4 v234, s[52:55], s61 offen offset:2048 lds\n"
        "v_mfma_f32_16x16x32_bf16 v[112:115], a[0:3], a[224:227], v[112:115]\n"
        "ds_read_b128 v[182:185], v251 offset:512\n"
        "s_nop 0\n"
        "v_mfma_f32_16x16x32_bf16 v[116:119], a[24:27], a[224:227], 0\n"
        "s_nop 2\n"
        "v_mfma_f32_16x16x32_bf16 v[116:119], a[28:31], a[228:231], v[116:119]\n"
        "s_waitcnt lgkmcnt(3)\n"
        "ds_write_b128 v235, v[76:79] offset:15360\n"
        "v_mfma_f32_16x16x32_bf16 v[116:119], a[32:35], a[232:235], v[116:119]\n"
        "v_cvt_pk_bf16_f32 v176, v112, v113\n"
        "v_cvt_pk_bf16_f32 v177, v114, v115\n"
        "v_mfma_f32_16x16x32_bf16 v[116:119], a[36:39], a[236:239], v[116:119]\n"
        "s_add_u32 m0, s65, 6144\n"
        "v_mfma_f32_16x16x32_bf16 v[116:119], a[40:43], a[240:243], v[116:119]\n"
        "ds_read_b64_tr_b16 v[216:217], v255 offset:32\n"
        "ds_read_b64_tr_b16 v[218:219], v255 offset:800\n"
        "v_mfma_f32_16x16x32_bf16 v[116:119], a[44:47], a[244:247], v[116:119]\n"
        "s_nop 2\n"
        "v_mfma_f32_16x16x32_bf16 v[120:123], a[68:71], a[244:247], 0\n"
        ".long 0xbec200ff\n"
        "43:\n"
        "s_branch 60b\n"
        "buffer_load_dwordx4 v232, s[52:55], s56 offen lds\n"
        "v_mfma_f32_16x16x32_bf16 v[120:123], a[64:67], a[240:243], v[120:123]\n"
        "s_nop 1\n"
        "v_cvt_pk_bf16_f32 v178, v116, v117\n"
        "v_mfma_f32_16x16x32_bf16 v[120:123], a[60:63], a[236:239], v[120:123]\n"
        "ds_write_b128 v239, v[88:91] offset:15552\n"
        "v_mfma_f32_16x16x32_bf16 v[120:123], a[56:59], a[232:235], v[120:123]\n"
        "v_cvt_pk_bf16_f32 v179, v118, v119\n"
        "v_mfma_f32_16x16x32_bf16 v[120:123], a[52:55], a[228:231], v[120:123]\n"
        "v_mfma_f32_16x16x32_bf16 v[120:123], a[48:51], a[224:227], v[120:123]\n"
        "ds_read_b64_tr_b16 v[220:221], v255 offset:64\n"
        "ds_read_b64_tr_b16 v[222:223], v255 offset:832\n"
        "v_mfma_f32_16x16x32_bf16 v[112:115], a[224:227], a[144:147], 0\n"
        "s_nop 2\n"
        "v_mfma_f32_16x16x32_bf16 v[112:115], a[228:231], a[148:151], v[112:115]\n"
        "s_nop 0\n"
        "v_cvt_pk_bf16_f32 v180, v120, v121\n"
        "v_cvt_pk_bf16_f32 v181, v122, v123\n"
        "v_mfma_f32_16x16x32_bf16 v[112:115], a[232:235], a[152:155], v[112:115]\n"
        "buffer_load_dwordx4 v233, s[52:55], s56 offen offset:1024 lds\n"
        "v_mfma_f32_16x16x32_bf16 v[112:115], a[236:239], a[156:159], v[112:115]\n"
        "ds_write_b128 v254, v[92:95] offset:15360\n"
        "v_mfma_f32_16x16x32_bf16 v[112:115], a[240:243], a[160:163], v[112:115]\n"
        "v_cvt_pk_bf16_f32 v186, v120, v121\n"
        "v_cvt_pk_bf16_f32 v187, v122, v123\n"
        "v_mfma_f32_16x16x32_bf16 v[112:115], a[244:247], a[164:167], v[112:115]\n"
        "ds_read_b128 a[248:251], v240 offset:12288\n"
        "s_nop 0\n"
        "v_mfma_f32_16x16x32_bf16 v[96:99], v[152:155], v[156:159], v[0:3]\n"
        "s_waitcnt vmcnt(2)\n"
        "ds_read_b64_tr_b16 v[224:225], v255 offset:3072\n"
        "ds_read_b64_tr_b16 v[226:227], v255 offset:3840\n"
        "v_mfma_f32_16x16x32_bf16 v[96:99], v[168:171], v[160:163], v[96:99]\n"
        "ds_read_b128 a[216:219], v241 offset:12288\n"
        "s_sub_u32 s31, s30, s28\n"
        "v_cvt_pk_bf16_f32 v130, v112, v113\n"
        "v_mfma_f32_16x16x32_bf16 v[100:103], v[172:175], v[156:159], v[4:7]\n"
        "ds_read_b128 a[220:223], v240 offset:12416\n"
        "s_sub_u32 s45, s28, s29\n"
        "v_cvt_pk_bf16_f32 v131, v114, v115\n"
        "v_mfma_f32_16x16x32_bf16 v[100:103], v[188:191], v[164:167], v[100:103]\n"
        "s_add_u32 s28, s28, s31\n"
        "ds_read_b128 v[162:165], v252 offset:0\n"
        "v_mfma_f32_16x16x32_bf16 v[116:119], a[244:247], a[188:191], 0\n"
        "buffer_load_dwordx4 v234, s[52:55], s56 offen offset:2048 lds\n"
        "v_mfma_f32_16x16x32_bf16 v[116:119], a[240:243], a[184:187], v[116:119]\n"
        "s_waitcnt lgkmcnt(7)\n"
        "ds_write_b128 v235, v[212:215] offset:0\n"
        "v_mfma_f32_16x16x32_bf16 v[116:119], a[236:239], a[180:183], v[116:119]\n"
        "v_exp_f32 v96, v96\n"
        "v_mfma_f32_16x16x32_bf16 v[116:119], a[232:235], a[176:179], v[116:119]\n"
        "ds_read_b64_tr_b16 v[88:89], v255 offset:3104\n"
        "ds_read_b64_tr_b16 v[90:91], v255 offset:3872\n"
        "v_mfma_f32_16x16x32_bf16 v[116:119], a[228:231], a[172:175], v[116:119]\n"
        "v_exp_f32 v97, v97\n"
        "v_mfma_f32_16x16x32_bf16 v[116:119], a[224:227], a[168:171], v[116:119]\n"
        "s_add_u32 m0, s65, 9216\n"
        "v_exp_f32 v98, v98\n"
        "v_mfma_f32_16x16x32_bf16 v[104:107], v[152:155], v[176:179], v[16:19]\n"
        "v_exp_f32 v100, v100\n"
        "v_mfma_f32_16x16x32_bf16 v[104:107], v[168:171], v[180:183], v[104:107]\n"
        "ds_write_b128 v239, v[216:219] offset:192\n"
        "v_mfma_f32_16x16x32_bf16 v[108:111], v[172:175], v[176:179], v[20:23]\n"
        "buffer_load_dwordx4 v232, s[52:55], s62 offen lds\n"
        "v_mfma_f32_16x16x32_bf16 v[108:111], v[188:191], v[184:187], v[108:111]\n"
        "ds_read_b128 v[182:185], v252 offset:512\n"
        "v_cvt_pk_bf16_f32 v64, v96, v97\n"
        "v_mfma_f32_16x16x32_bf16 v[120:123], a[224:227], a[192:195], 0\n"
        "ds_read_b128 a[224:227], v241 offset:12416\n"
        "ds_read_b64_tr_b16 v[92:93], v255 offset:3136\n"
        "v_mfma_f32_16x16x32_bf16 v[120:123], a[228:231], a[196:199], v[120:123]\n"
        "ds_read_b64_tr_b16 v[94:95], v255 offset:3904\n"
        "ds_read_b128 a[228:231], v240 offset:12544\n"
        "v_mfma_f32_16x16x32_bf16 v[120:123], a[232:235], a[200:203], v[120:123]\n"
        "ds_read_b128 a[232:235], v241 offset:12544\n"
        "v_cvt_pk_bf16_f32 v138, v116, v117\n"
        "v_mfma_f32_16x16x32_bf16 v[120:123], a[236:239], a[204:207], v[120:123]\n"
        "ds_read_b128 v[202:205], v251 offset:4096\n"
        "v_cvt_pk_bf16_f32 v139, v118, v119\n"
        "v_mfma_f32_16x16x32_bf16 v[120:123], a[240:243], a[208:211], v[120:123]\n"
        "ds_write_b128 v254, v[220:223] offset:0\n"
        "v_mfma_f32_16x16x32_bf16 v[120:123], a[244:247], a[212:215], v[120:123]\n"
        "s_waitcnt lgkmcnt(9)\n"
        "v_exp_f32 v99, v99\n"
        "v_mfma_f32_16x16x32_bf16 v[112:115], a[0:3], a[248:251], 0\n"
        "buffer_load_dwordx4 v233, s[52:55], s62 offen offset:1024 lds\n"
        "v_mfma_f32_16x16x32_bf16 v[112:115], a[4:7], a[216:219], v[112:115]\n"
        "s_waitcnt vmcnt(2)\n"
        "ds_read_b64_tr_b16 v[212:213], v255 offset:6144\n"
        "ds_read_b64_tr_b16 v[214:215], v255 offset:6912\n"
        "v_mfma_f32_16x16x32_bf16 v[112:115], a[8:11], a[220:223], v[112:115]\n"
        "s_waitcnt lgkmcnt(5)\n"
        "v_exp_f32 v101, v101\n"
        "v_mfma_f32_16x16x32_bf16 v[112:115], a[12:15], a[224:227], v[112:115]\n"
        "v_cvt_pk_bf16_f32 v65, v98, v99\n"
        "v_cvt_pk_bf16_f32 v66, v100, v101\n"
        "v_mfma_f32_16x16x32_bf16 v[112:115], a[16:19], a[228:231], v[112:115]\n"
        "s_waitcnt lgkmcnt(3)\n"
        "v_exp_f32 v102, v102\n"
        "v_mfma_f32_16x16x32_bf16 v[112:115], a[20:23], a[232:235], v[112:115]\n"
        "ds_write_b128 v235, v[224:227] offset:3072\n"
        "v_mfma_f32_16x16x32_bf16 v[116:119], a[44:47], a[232:235], 0\n"
        "v_exp_f32 v103, v103\n"
        "v_mfma_f32_16x16x32_bf16 v[116:119], a[40:43], a[228:231], v[116:119]\n"
        "v_cvt_pk_bf16_f32 v67, v102, v103\n"
        "v_cvt_pk_bf16_f32 v146, v120, v121\n"
        "v_mfma_f32_16x16x32_bf16 v[116:119], a[36:39], a[224:227], v[116:119]\n"
        "buffer_load_dwordx4 v234, s[52:55], s62 offen offset:2048 lds\n"
        "s_add_u32 s41, s30, s40\n"
        "s_add_u32 s35, s47, 786432\n"
        "s_sub_u32 s46, s35, 100663296\n"
        "s_cmp_lt_u32 s46, 786432\n"
        "s_mov_b32 s30, s29\n"
        "v_mfma_f32_16x16x32_bf16 v[116:119], a[32:35], a[220:223], v[116:119]\n"
        "s_cselect_b32 s35, s46, s35\n"
        "s_add_u32 s29, s29, s45\n"
        "ds_read_b64_tr_b16 v[216:217], v255 offset:6176\n"
        "v_mfma_f32_16x16x32_bf16 v[116:119], a[28:31], a[216:219], v[116:119]\n"
        "ds_read_b64_tr_b16 v[218:219], v255 offset:6944\n"
        "v_cvt_pk_bf16_f32 v147, v122, v123\n"
        "v_mfma_f32_16x16x32_bf16 v[116:119], a[24:27], a[248:251], v[116:119]\n"
        "v_exp_f32 v104, v104\n"
        "v_mfma_f32_16x16x32_bf16 v[120:123], a[48:51], a[248:251], 0\n"
        "ds_write_b128 v239, v[88:91] offset:3264\n"
        "v_mfma_f32_16x16x32_bf16 v[120:123], a[52:55], a[216:219], v[120:123]\n"
        "v_exp_f32 v105, v105\n"
        "v_mfma_f32_16x16x32_bf16 v[120:123], a[56:59], a[220:223], v[120:123]\n"
        "s_mov_b32 m0, s48\n"
        "v_exp_f32 v106, v106\n"
        "v_mfma_f32_16x16x32_bf16 v[120:123], a[60:63], a[224:227], v[120:123]\n"
        "ds_read_b64_tr_b16 v[220:221], v255 offset:6208\n"
        "ds_read_b64_tr_b16 v[222:223], v255 offset:6976\n"
        "v_mfma_f32_16x16x32_bf16 v[120:123], a[64:67], a[228:231], v[120:123]\n"
        "buffer_load_dwordx4 v249, s[12:15], s36 offen offset:0 lds\n"
        "v_mfma_f32_16x16x32_bf16 v[120:123], a[68:71], a[232:235], v[120:123]\n"
        "v_cvt_pk_bf16_f32 v72, v104, v105\n"
        "v_cvt_pk_bf16_f32 v196, v112, v113\n"
        "v_cvt_pk_bf16_f32 v197, v114, v115\n"
        "v_mfma_f32_16x16x32_bf16 v[112:115], a[92:95], a[232:235], 0\n"
        "v_exp_f32 v108, v108\n"
        "v_mfma_f32_16x16x32_bf16 v[112:115], a[88:91], a[228:231], v[112:115]\n"
        "ds_write_b128 v254, v[92:95] offset:3072\n"
        "v_mfma_f32_16x16x32_bf16 v[112:115], a[84:87], a[224:227], v[112:115]\n"
        "v_exp_f32 v107, v107\n"
        "v_mfma_f32_16x16x32_bf16 v[112:115], a[80:83], a[220:223], v[112:115]\n"
        "v_exp_f32 v109, v109\n"
        "v_mfma_f32_16x16x32_bf16 v[112:115], a[76:79], a[216:219], v[112:115]\n"
        "s_waitcnt vmcnt(1)\n"
        "ds_read_b64_tr_b16 v[224:225], v255 offset:9216\n"
        "ds_read_b64_tr_b16 v[226:227], v255 offset:9984\n"
        "v_mfma_f32_16x16x32_bf16 v[112:115], a[72:75], a[248:251], v[112:115]\n"
        "buffer_load_dwordx4 v253, s[12:15], s36 offen offset:1024 lds\n"
        "v_cvt_pk_bf16_f32 v198, v116, v117\n"
        "v_cvt_pk_bf16_f32 v199, v118, v119\n"
        "s_nop 1\n"
        "v_mfma_f32_16x16x32_bf16 v[96:99], v[152:155], v[196:199], v[32:35]\n"
        "v_cvt_pk_bf16_f32 v73, v106, v107\n"
        "v_cvt_pk_bf16_f32 v74, v108, v109\n"
        "v_cvt_pk_bf16_f32 v200, v120, v121\n"
        "v_cvt_pk_bf16_f32 v201, v122, v123\n"
        "s_nop 1\n"
        "v_mfma_f32_16x16x32_bf16 v[96:99], v[168:171], v[200:203], v[96:99]\n"
        "v_exp_f32 v110, v110\n"
        "v_mfma_f32_16x16x32_bf16 v[100:103], v[172:175], v[196:199], v[36:39]\n"
        "s_waitcnt lgkmcnt(3)\n"
        "ds_write_b128 v235, v[212:215] offset:6144\n"
        "v_cvt_pk_bf16_f32 v206, v120, v121\n"
        "v_cvt_pk_bf16_f32 v207, v122, v123\n"
        "s_nop 1\n"
        "v_mfma_f32_16x16x32_bf16 v[100:103], v[188:191], v[204:207], v[100:103]\n"
        "s_cmp_eq_u32 s37, 100663296\n"
        "v_exp_f32 v111, v111\n"
        "v_mfma_f32_16x16x32_bf16 v[116:119], a[96:99], a[248:251], 0\n"
        "s_cselect_b32 s46, 0, s37\n"
        "ds_read_b128 v[202:205], v252 offset:4096\n"
        "v_cvt_pk_bf16_f32 v75, v110, v111\n"
        "v_mfma_f32_16x16x32_bf16 v[116:119], a[100:103], a[216:219], v[116:119]\n"
        "ds_read_b64_tr_b16 v[88:89], v255 offset:9248\n"
        "ds_read_b64_tr_b16 v[90:91], v255 offset:10016\n"
        "s_add_u32 m0, s41, 0\n"
        "v_mfma_f32_16x16x32_bf16 v[116:119], a[104:107], a[220:223], v[116:119]\n"
        "buffer_load_dwordx4 v242, s[8:11], s47 offen lds\n"
        "v_mfma_f32_16x16x32_bf16 v[116:119], a[108:111], a[224:227], v[116:119]\n"
        "s_add_u32 s38, s46, s43\n"
        "v_cvt_pk_bf16_f32 v192, v112, v113\n"
        "v_cvt_pk_bf16_f32 v193, v114, v115\n"
        "v_mfma_f32_16x16x32_bf16 v[116:119], a[112:115], a[228:231], v[116:119]\n"
        "s_add_u32 s39, s46, s44\n"
        "v_exp_f32 v96, v96\n"
        "v_mfma_f32_16x16x32_bf16 v[116:119], a[116:119], a[232:235], v[116:119]\n"
        "ds_write_b128 v239, v[216:219] offset:6336\n"
        "s_add_u32 s37, s37, 6291456\n"
        "v_mfma_f32_16x16x32_bf16 v[120:123], a[140:143], a[232:235], 0\n"
        "v_exp_f32 v97, v97\n"
        "v_mfma_f32_16x16x32_bf16 v[120:123], a[136:139], a[228:231], v[120:123]\n"
        "s_add_u32 m0, s41, 1024\n"
        "v_exp_f32 v98, v98\n"
        "v_mfma_f32_16x16x32_bf16 v[120:123], a[132:135], a[224:227], v[120:123]\n"
        "ds_read_b64_tr_b16 v[92:93], v255 offset:9280\n"
        "ds_read_b64_tr_b16 v[94:95], v255 offset:10048\n"
        "v_mfma_f32_16x16x32_bf16 v[120:123], a[128:131], a[220:223], v[120:123]\n"
        "buffer_load_dwordx4 v243, s[8:11], s47 offen lds\n"
        "v_mfma_f32_16x16x32_bf16 v[120:123], a[124:127], a[216:219], v[120:123]\n"
        "v_cvt_pk_bf16_f32 v80, v96, v97\n"
        "v_cvt_pk_bf16_f32 v194, v116, v117\n"
        "v_mfma_f32_16x16x32_bf16 v[120:123], a[120:123], a[248:251], v[120:123]\n"
        "v_exp_f32 v100, v100\n"
        "v_mfma_f32_16x16x32_bf16 v[112:115], a[248:251], a[144:147], 0\n"
        "ds_write_b128 v254, v[220:223] offset:6144\n"
        "v_mfma_f32_16x16x32_bf16 v[112:115], a[216:219], a[148:151], v[112:115]\n"
        "v_cvt_pk_bf16_f32 v195, v118, v119\n"
        "v_add_u32 v245, s45, v245\n"
        "v_mfma_f32_16x16x32_bf16 v[112:115], a[220:223], a[152:155], v[112:115]\n"
        "s_add_u32 m0, s41, 2048\n"
        "ds_read_b128 a[236:239], v240 offset:13824\n"
        "v_cvt_pk_bf16_f32 v208, v120, v121\n"
        "v_mfma_f32_16x16x32_bf16 v[112:115], a[224:227], a[156:159], v[112:115]\n"
        "ds_read_b128 a[240:243], v241 offset:13824\n"
        "v_cvt_pk_bf16_f32 v209, v122, v123\n"
        "v_mfma_f32_16x16x32_bf16 v[112:115], a[228:231], a[160:163], v[112:115]\n"
        "buffer_load_dwordx4 v244, s[8:11], s47 offen lds\n"
        "v_mfma_f32_16x16x32_bf16 v[112:115], a[232:235], a[164:167], v[112:115]\n"
        "ds_read_b128 a[244:247], v240 offset:13952\n"
        "v_add_u32 v246, s45, v246\n"
        "v_mfma_f32_16x16x32_bf16 v[104:107], v[192:195], v[156:159], v[8:11]\n"
        "v_exp_f32 v99, v99\n"
        "v_mfma_f32_16x16x32_bf16 v[104:107], v[208:211], v[160:163], v[104:107]\n"
        "s_waitcnt lgkmcnt(4)\n"
        "ds_write_b128 v235, v[224:227] offset:9216\n"
        "v_mfma_f32_16x16x32_bf16 v[116:119], a[232:235], a[188:191], 0\n"
        "v_exp_f32 v101, v101\n"
        "v_mfma_f32_16x16x32_bf16 v[116:119], a[228:231], a[184:187], v[116:119]\n"
        "s_add_u32 m0, s41, 12288\n"
        "v_cvt_pk_bf16_f32 v81, v98, v99\n"
        "v_cvt_pk_bf16_f32 v82, v100, v101\n"
        "v_mfma_f32_16x16x32_bf16 v[116:119], a[224:227], a[180:183], v[116:119]\n"
        "v_exp_f32 v102, v102\n"
        "v_mfma_f32_16x16x32_bf16 v[116:119], a[220:223], a[176:179], v[116:119]\n"
        "buffer_load_dwordx4 v242, s[8:11], s35 offen lds\n"
        "v_mfma_f32_16x16x32_bf16 v[116:119], a[216:219], a[172:175], v[116:119]\n"
        "v_exp_f32 v103, v103\n"
        "v_mfma_f32_16x16x32_bf16 v[116:119], a[248:251], a[168:171], v[116:119]\n"
        "v_cvt_pk_bf16_f32 v83, v102, v103\n"
        "v_cvt_pk_bf16_f32 v132, v112, v113\n"
        "v_mfma_f32_16x16x32_bf16 v[108:111], v[192:195], v[176:179], v[24:27]\n"
        "ds_write_b128 v239, v[88:91] offset:9408\n"
        "v_mfma_f32_16x16x32_bf16 v[108:111], v[208:211], v[180:183], v[108:111]\n"
        "v_cvt_pk_bf16_f32 v133, v114, v115\n"
        "s_nop 0\n"
        "v_mfma_f32_16x16x32_bf16 v[120:123], a[248:251], a[192:195], 0\n"
        "ds_read_b128 a[248:251], v241 offset:13952\n"
        "s_add_u32 m0, s41, 13312\n"
        "v_cvt_pk_bf16_f32 v140, v116, v117\n"
        "v_mfma_f32_16x16x32_bf16 v[120:123], a[216:219], a[196:199], v[120:123]\n"
        "ds_read_b128 a[216:219], v240 offset:14080\n"
        "v_cvt_pk_bf16_f32 v141, v118, v119\n"
        "v_mfma_f32_16x16x32_bf16 v[120:123], a[220:223], a[200:203], v[120:123]\n"
        "buffer_load_dwordx4 v243, s[8:11], s35 offen lds\n"
        "v_mfma_f32_16x16x32_bf16 v[120:123], a[224:227], a[204:207], v[120:123]\n"
        "ds_read_b128 a[220:223], v241 offset:14080\n"
        "v_mfma_f32_16x16x32_bf16 v[120:123], a[228:231], a[208:211], v[120:123]\n"
        "v_exp_f32 v104, v104\n"
        "v_mfma_f32_16x16x32_bf16 v[120:123], a[232:235], a[212:215], v[120:123]\n"
        "ds_write_b128 v254, v[92:95] offset:9216\n"
        "v_mfma_f32_16x16x32_bf16 v[96:99], v[192:195], v[196:199], v[40:43]\n"
        "v_exp_f32 v105, v105\n"
        "v_mfma_f32_16x16x32_bf16 v[96:99], v[208:211], v[200:203], v[96:99]\n"
        "s_waitcnt lgkmcnt(5)\n"
        "s_add_u32 m0, s41, 14336\n"
        "v_exp_f32 v106, v106\n"
        "v_mfma_f32_16x16x32_bf16 v[112:115], a[72:75], a[236:239], 0\n"
        "v_cvt_pk_bf16_f32 v68, v104, v105\n"
        "v_cvt_pk_bf16_f32 v148, v120, v121\n"
        "v_mfma_f32_16x16x32_bf16 v[112:115], a[76:79], a[240:243], v[112:115]\n"
        "buffer_load_dwordx4 v244, s[8:11], s35 offen lds\n"
        "v_mfma_f32_16x16x32_bf16 v[112:115], a[80:83], a[244:247], v[112:115]\n"
        "s_waitcnt lgkmcnt(1)\n"
        "v_exp_f32 v107, v107\n"
        "v_mfma_f32_16x16x32_bf16 v[112:115], a[84:87], a[248:251], v[112:115]\n"
        "v_cvt_pk_bf16_f32 v69, v106, v107\n"
        "v_cvt_pk_bf16_f32 v149, v122, v123\n"
        "v_mfma_f32_16x16x32_bf16 v[112:115], a[88:91], a[216:219], v[112:115]\n"
        "v_exp_f32 v108, v108\n"
        "v_mfma_f32_16x16x32_bf16 v[112:115], a[92:95], a[220:223], v[112:115]\n"
        "v_exp_f32 v109, v109\n"
        "v_mfma_f32_16x16x32_bf16 v[116:119], a[116:119], a[220:223], 0\n"
        "v_exp_f32 v110, v110\n"
        "v_mfma_f32_16x16x32_bf16 v[116:119], a[112:115], a[216:219], v[116:119]\n"
        "v_cvt_pk_bf16_f32 v76, v108, v109\n"
        "v_add_u32 v240, s31, v240\n"
        "v_mfma_f32_16x16x32_bf16 v[116:119], a[108:111], a[248:251], v[116:119]\n"
        "v_exp_f32 v111, v111\n"
        "v_mfma_f32_16x16x32_bf16 v[116:119], a[104:107], a[244:247], v[116:119]\n"
        "v_cvt_pk_bf16_f32 v212, v112, v113\n"
        "v_cvt_pk_bf16_f32 v213, v114, v115\n"
        "v_mfma_f32_16x16x32_bf16 v[116:119], a[100:103], a[240:243], v[116:119]\n"
        "v_cvt_pk_bf16_f32 v77, v110, v111\n"
        "v_add_u32 v241, s31, v241\n"
        "v_mfma_f32_16x16x32_bf16 v[116:119], a[96:99], a[236:239], v[116:119]\n"
        "v_exp_f32 v96, v96\n"
        "v_mfma_f32_16x16x32_bf16 v[120:123], a[120:123], a[236:239], 0\n"
        "v_exp_f32 v97, v97\n"
        "v_mfma_f32_16x16x32_bf16 v[120:123], a[124:127], a[240:243], v[120:123]\n"
        "s_add_u32 s47, s47, 6291456\n"
        "v_exp_f32 v98, v98\n"
        "v_mfma_f32_16x16x32_bf16 v[120:123], a[128:131], a[244:247], v[120:123]\n"
        "s_add_u32 s36, s36, 2097152\n"
        "v_cvt_pk_bf16_f32 v214, v116, v117\n"
        "v_cvt_pk_bf16_f32 v215, v118, v119\n"
        "v_mfma_f32_16x16x32_bf16 v[120:123], a[132:135], a[248:251], v[120:123]\n"
        "s_xor_b32 s48, s48, 8192\n"
        "v_cvt_pk_bf16_f32 v84, v96, v97\n"
        "v_and_b32 v254, 32, v250\n"
        "v_mfma_f32_16x16x32_bf16 v[120:123], a[136:139], a[216:219], v[120:123]\n"
        "v_exp_f32 v99, v99\n"
        "v_mfma_f32_16x16x32_bf16 v[120:123], a[140:143], a[220:223], v[120:123]\n"
        "v_cvt_pk_bf16_f32 v85, v98, v99\n"
        "v_mad_i32_i24 v254, v254, -12, v248\n"
        "v_mfma_f32_16x16x32_bf16 v[112:115], a[220:223], a[164:167], 0\n"
        "v_add_u32 v254, 1728, v254\n"
        "s_nop 0\n"
        "v_mfma_f32_16x16x32_bf16 v[112:115], a[216:219], a[160:163], v[112:115]\n"
        "s_nop 1\n"
        "v_cvt_pk_bf16_f32 v230, v120, v121\n"
        "v_mfma_f32_16x16x32_bf16 v[112:115], a[248:251], a[156:159], v[112:115]\n"
        "s_waitcnt lgkmcnt(0)\n"
        "s_barrier\n"
        "v_cvt_pk_bf16_f32 v231, v122, v123\n"
        "v_mfma_f32_16x16x32_bf16 v[112:115], a[244:247], a[152:155], v[112:115]\n"
        "v_mfma_f32_16x16x32_bf16 v[112:115], a[240:243], a[148:151], v[112:115]\n"
        "v_mfma_f32_16x16x32_bf16 v[112:115], a[236:239], a[144:147], v[112:115]\n"
        "s_nop 2\n"
        "v_mfma_f32_16x16x32_bf16 v[100:103], v[212:215], v[156:159], v[12:15]\n"
        "s_nop 2\n"
        "v_mfma_f32_16x16x32_bf16 v[100:103], v[228:231], v[164:167], v[100:103]\n"
        "v_cvt_pk_bf16_f32 v134, v112, v113\n"
        "v_cvt_pk_bf16_f32 v135, v114, v115\n"
        "v_mfma_f32_16x16x32_bf16 v[116:119], a[236:239], a[168:171], 0\n"
        "v_mfma_f32_16x16x32_bf16 v[116:119], a[240:243], a[172:175], v[116:119]\n"
        "v_mfma_f32_16x16x32_bf16 v[116:119], a[244:247], a[176:179], v[116:119]\n"
        "v_mfma_f32_16x16x32_bf16 v[116:119], a[248:251], a[180:183], v[116:119]\n"
        "v_mfma_f32_16x16x32_bf16 v[116:119], a[216:219], a[184:187], v[116:119]\n"
        "s_nop 0\n"
        "v_exp_f32 v100, v100\n"
        "v_mfma_f32_16x16x32_bf16 v[116:119], a[220:223], a[188:191], v[116:119]\n"
        "v_exp_f32 v101, v101\n"
        "v_mfma_f32_16x16x32_bf16 v[104:107], v[212:215], v[176:179], v[28:31]\n"
        "v_exp_f32 v102, v102\n"
        "v_mfma_f32_16x16x32_bf16 v[104:107], v[228:231], v[184:187], v[104:107]\n"
        "v_exp_f32 v103, v103\n"
        "v_mfma_f32_16x16x32_bf16 v[120:123], a[220:223], a[212:215], 0\n"
        "v_cvt_pk_bf16_f32 v70, v100, v101\n"
        "v_cvt_pk_bf16_f32 v71, v102, v103\n"
        "v_mfma_f32_16x16x32_bf16 v[120:123], a[216:219], a[208:211], v[120:123]\n"
        "v_cvt_pk_bf16_f32 v142, v116, v117\n"
        "v_cvt_pk_bf16_f32 v143, v118, v119\n"
        "v_mfma_f32_16x16x32_bf16 v[120:123], a[248:251], a[204:207], v[120:123]\n"
        "v_mov_b64 v[96:97], 0\n"
        "v_mfma_f32_16x16x32_bf16 v[120:123], a[244:247], a[200:203], v[120:123]\n"
        "v_exp_f32 v104, v104\n"
        "v_mfma_f32_16x16x32_bf16 v[120:123], a[240:243], a[196:199], v[120:123]\n"
        "v_exp_f32 v105, v105\n"
        "v_mfma_f32_16x16x32_bf16 v[120:123], a[236:239], a[192:195], v[120:123]\n"
        "v_exp_f32 v106, v106\n"
        "v_mfma_f32_16x16x32_bf16 v[108:111], v[212:215], v[196:199], v[44:47]\n"
        "v_exp_f32 v107, v107\n"
        "v_mfma_f32_16x16x32_bf16 v[108:111], v[228:231], v[204:207], v[108:111]\n"
        "v_cvt_pk_bf16_f32 v78, v104, v105\n"
        "v_cvt_pk_bf16_f32 v79, v106, v107\n"
        "v_mfma_f32_16x16x32_bf16 v[112:115], a[0:3], a[236:239], 0\n"
        "s_nop 0\n"
        "v_cvt_pk_bf16_f32 v150, v120, v121\n"
        "v_cvt_pk_bf16_f32 v151, v122, v123\n"
        "v_mfma_f32_16x16x32_bf16 v[112:115], a[4:7], a[240:243], v[112:115]\n"
        "v_mfma_f32_16x16x32_bf16 v[112:115], a[8:11], a[244:247], v[112:115]\n"
        "v_exp_f32 v108, v108\n"
        "v_mfma_f32_16x16x32_bf16 v[112:115], a[12:15], a[248:251], v[112:115]\n"
        "v_exp_f32 v109, v109\n"
        "v_mfma_f32_16x16x32_bf16 v[112:115], a[16:19], a[216:219], v[112:115]\n"
        "v_exp_f32 v110, v110\n"
        "v_mfma_f32_16x16x32_bf16 v[112:115], a[20:23], a[220:223], v[112:115]\n"
        "v_exp_f32 v111, v111\n"
        "v_smfmac_f32_16x16x64_bf16 v[96:99], a[252:255], v[64:71], v237\n"
        "v_cvt_pk_bf16_f32 v86, v108, v109\n"
        "v_mfma_f32_16x16x32_bf16 v[100:103], v[132:135], v[68:71], 0\n"
        "v_mov_b64 v[124:125], 0\n"
        "v_cvt_pk_bf16_f32 v87, v110, v111\n"
        "v_mfma_f32_16x16x32_bf16 v[100:103], v[128:131], v[64:67], v[100:103]\n"
        "s_nop 0\n"
        "v_cvt_pk_bf16_f32 v216, v112, v113\n"
        "v_cvt_pk_bf16_f32 v217, v114, v115\n"
        "v_mfma_f32_16x16x32_bf16 v[104:107], v[136:139], v[64:67], 0\n"
        "ds_read_b128 v[222:225], v251 offset:4608\n"
        "v_add_f32 v97, v96, v97\n"
        "v_mfma_f32_16x16x32_bf16 v[104:107], v[140:143], v[68:71], v[104:107]\n"
        "v_rcp_f32 v96, v97\n"
        "v_mfma_f32_16x16x32_bf16 v[108:111], v[148:151], v[68:71], 0\n"
        "v_mul_f32 v100, v96, v100\n"
        "v_mul_f32 v101, v96, v101\n"
        "v_mfma_f32_16x16x32_bf16 v[108:111], v[144:147], v[64:67], v[108:111]\n"
        "v_mul_f32 v102, v96, v102\n"
        "v_mul_f32 v103, v96, v103\n"
        "v_mfma_f32_16x16x32_bf16 v[116:119], a[44:47], a[220:223], 0\n"
        "v_cvt_pk_bf16_f32 v100, v100, v101\n"
        "v_cvt_pk_bf16_f32 v101, v102, v103\n"
        "v_mfma_f32_16x16x32_bf16 v[116:119], a[40:43], a[216:219], v[116:119]\n"
        "v_mul_f32 v104, v96, v104\n"
        "v_mul_f32 v105, v96, v105\n"
        "v_mfma_f32_16x16x32_bf16 v[116:119], a[36:39], a[248:251], v[116:119]\n"
        "v_mul_f32 v106, v96, v106\n"
        "v_mul_f32 v107, v96, v107\n"
        "v_mfma_f32_16x16x32_bf16 v[116:119], a[32:35], a[244:247], v[116:119]\n"
        "v_cvt_pk_bf16_f32 v102, v104, v105\n"
        "v_cvt_pk_bf16_f32 v103, v106, v107\n"
        "v_mfma_f32_16x16x32_bf16 v[116:119], a[28:31], a[240:243], v[116:119]\n"
        "ds_write_b128 v245, v[100:103] offset:0\n"
        "v_mfma_f32_16x16x32_bf16 v[116:119], a[24:27], a[236:239], v[116:119]\n"
        "v_mul_f32 v108, v96, v108\n"
        "v_mul_f32 v109, v96, v109\n"
        "v_smfmac_f32_16x16x64_bf16 v[124:127], a[252:255], v[72:79], v237\n"
        "v_mul_f32 v110, v96, v110\n"
        "v_mul_f32 v111, v96, v111\n"
        "v_mfma_f32_16x16x32_bf16 v[64:67], v[132:135], v[76:79], 0\n"
        "v_cvt_pk_bf16_f32 v108, v108, v109\n"
        "v_cvt_pk_bf16_f32 v109, v110, v111\n"
        "v_mfma_f32_16x16x32_bf16 v[64:67], v[128:131], v[72:75], v[64:67]\n"
        "ds_read_b128 v[156:159], v247 offset:0\n"
        "v_cvt_pk_bf16_f32 v218, v116, v117\n"
        "v_mfma_f32_16x16x32_bf16 v[68:71], v[136:139], v[72:75], 0\n"
        "ds_write_b64 v246, v[108:109] offset:0\n"
        "v_mfma_f32_16x16x32_bf16 v[68:71], v[140:143], v[76:79], v[68:71]\n"
        "ds_read_b128 v[160:163], v247 offset:4096\n"
        "v_cvt_pk_bf16_f32 v219, v118, v119\n"
        "v_mfma_f32_16x16x32_bf16 v[120:123], v[148:151], v[76:79], 0\n"
        "ds_read_b128 v[164:167], v247 offset:4352\n"
        "v_max_u32 v236, v96, v97\n"
        "v_mfma_f32_16x16x32_bf16 v[120:123], v[144:147], v[72:75], v[120:123]\n"
        "v_mov_b64 v[116:117], 0\n"
        "v_add_f32 v125, v124, v125\n"
        "v_mfma_f32_16x16x32_bf16 v[112:115], a[48:51], a[236:239], 0\n"
        "v_rcp_f32 v124, v125\n"
        "v_mfma_f32_16x16x32_bf16 v[112:115], a[52:55], a[240:243], v[112:115]\n"
        "v_mul_f32 v64, v124, v64\n"
        "v_mul_f32 v65, v124, v65\n"
        "v_mfma_f32_16x16x32_bf16 v[112:115], a[56:59], a[244:247], v[112:115]\n"
        "v_mul_f32 v66, v124, v66\n"
        "v_mul_f32 v67, v124, v67\n"
        "v_mfma_f32_16x16x32_bf16 v[112:115], a[60:63], a[248:251], v[112:115]\n"
        "v_cvt_pk_bf16_f32 v64, v64, v65\n"
        "v_cvt_pk_bf16_f32 v65, v66, v67\n"
        "v_mfma_f32_16x16x32_bf16 v[112:115], a[64:67], a[216:219], v[112:115]\n"
        "v_mul_f32 v68, v124, v68\n"
        "v_mul_f32 v69, v124, v69\n"
        "v_mfma_f32_16x16x32_bf16 v[112:115], a[68:71], a[220:223], v[112:115]\n"
        "ds_read_b128 a[216:219], v248 offset:0\n"
        "v_mul_f32 v70, v124, v70\n"
        "v_smfmac_f32_16x16x64_bf16 v[116:119], a[252:255], v[80:87], v237\n"
        "ds_read_b128 a[220:223], v248 offset:3072\n"
        "v_mul_f32 v71, v124, v71\n"
        "v_mfma_f32_16x16x32_bf16 v[72:75], v[132:135], v[84:87], 0\n"
        "v_cvt_pk_bf16_f32 v66, v68, v69\n"
        "v_cvt_pk_bf16_f32 v67, v70, v71\n"
        "v_mfma_f32_16x16x32_bf16 v[72:75], v[128:131], v[80:83], v[72:75]\n"
        "ds_write_b128 v245, v[64:67] offset:256\n"
        "v_mfma_f32_16x16x32_bf16 v[76:79], v[136:139], v[80:83], 0\n"
        "v_cvt_pk_bf16_f32 v220, v112, v113\n"
        "v_cvt_pk_bf16_f32 v221, v114, v115\n"
        "v_mfma_f32_16x16x32_bf16 v[76:79], v[140:143], v[84:87], v[76:79]\n"
        "ds_read_b128 a[224:227], v248 offset:32\n"
        "v_cvt_pk_bf16_f32 v226, v112, v113\n"
        "v_mfma_f32_16x16x32_bf16 v[96:99], v[148:151], v[84:87], 0\n"
        "ds_read_b128 a[228:231], v248 offset:3104\n"
        "v_cvt_pk_bf16_f32 v227, v114, v115\n"
        "v_mfma_f32_16x16x32_bf16 v[96:99], v[144:147], v[80:83], v[96:99]\n"
        "v_mul_f32 v120, v124, v120\n"
        "v_mul_f32 v121, v124, v121\n"
        "v_mfma_f32_16x16x32_bf16 v[100:103], v[152:155], v[216:219], v[48:51]\n"
        "s_waitcnt lgkmcnt(3)\n"
        "ds_read_b128 v[152:155], v247 offset:256\n"
        "v_mul_f32 v122, v124, v122\n"
        "v_mfma_f32_16x16x32_bf16 v[100:103], v[168:171], v[220:223], v[100:103]\n"
        "v_mul_f32 v123, v124, v123\n"
        "v_cvt_pk_bf16_f32 v120, v120, v121\n"
        "v_mfma_f32_16x16x32_bf16 v[104:107], v[172:175], v[216:219], v[52:55]\n"
        "v_cvt_pk_bf16_f32 v121, v122, v123\n"
        "v_max3_u32 v236, v236, v124, v125\n"
        "v_mfma_f32_16x16x32_bf16 v[104:107], v[188:191], v[224:227], v[104:107]\n"
        "ds_write_b64 v246, v[120:121] offset:256\n"
        "v_mfma_f32_16x16x32_bf16 v[176:179], v[156:159], a[216:219], 0\n"
        "ds_read_b128 v[222:225], v252 offset:4608\n"
        "v_add_f32 v117, v116, v117\n"
        "v_mfma_f32_16x16x32_bf16 v[176:179], v[160:163], a[220:223], v[176:179]\n"
        "ds_read_b128 a[232:235], v254 offset:0\n"
        "v_mfma_f32_16x16x32_bf16 v[196:199], v[164:167], a[220:223], 0\n"
        "s_waitcnt lgkmcnt(3)\n"
        "ds_read_b128 a[220:223], v254 offset:3072\n"
        "v_mfma_f32_16x16x32_bf16 v[196:199], v[152:155], a[216:219], v[196:199]\n"
        "v_rcp_f32 v116, v117\n"
        "v_mfma_f32_16x16x32_bf16 v[200:203], v[152:155], a[224:227], 0\n"
        "ds_read_b128 a[216:219], v254 offset:32\n"
        "v_mul_f32 v72, v116, v72\n"
        "v_mfma_f32_16x16x32_bf16 v[200:203], v[164:167], a[228:231], v[200:203]\n"
        "v_exp_f32 v100, v100\n"
        "v_mfma_f32_16x16x32_bf16 v[180:183], v[160:163], a[228:231], 0\n"
        "ds_read_b128 a[228:231], v254 offset:3104\n"
        "v_mul_f32 v73, v116, v73\n"
        "v_mfma_f32_16x16x32_bf16 v[180:183], v[156:159], a[224:227], v[180:183]\n"
        "ds_read_b128 a[236:239], v247 offset:8192\n"
        "v_mul_f32 v74, v116, v74\n"
        "v_mfma_f32_16x16x32_bf16 v[108:111], v[192:195], v[216:219], v[56:59]\n"
        "s_waitcnt lgkmcnt(4)\n"
        "ds_read_b128 a[224:227], v248 offset:16\n"
        "v_mul_f32 v75, v116, v75\n"
        "v_mfma_f32_16x16x32_bf16 v[108:111], v[208:211], v[220:223], v[108:111]\n"
        "ds_read_b128 a[240:243], v247 offset:12288\n"
        "v_cvt_pk_bf16_f32 v72, v72, v73\n"
        "v_mfma_f32_16x16x32_bf16 v[112:115], v[212:215], v[216:219], v[60:63]\n"
        "ds_read_b128 a[244:247], v247 offset:12544\n"
        "v_cvt_pk_bf16_f32 v73, v74, v75\n"
        "v_mfma_f32_16x16x32_bf16 v[112:115], v[228:231], v[224:227], v[112:115]\n"
        "ds_read_b128 a[248:251], v247 offset:8448\n"
        "v_mul_f32 v76, v116, v76\n"
        "v_mfma_f32_16x16x32_bf16 v[184:187], v[156:159], a[232:235], 0\n"
        "s_waitcnt lgkmcnt(6)\n"
        "v_exp_f32 v101, v101\n"
        "v_mfma_f32_16x16x32_bf16 v[184:187], v[160:163], a[220:223], v[184:187]\n"
        "v_mul_f32 v77, v116, v77\n"
        "v_mul_f32 v78, v116, v78\n"
        "v_mfma_f32_16x16x32_bf16 v[204:207], v[164:167], a[220:223], 0\n"
        "ds_read_b128 a[220:223], v248 offset:3088\n"
        "v_mul_f32 v79, v116, v79\n"
        "v_mfma_f32_16x16x32_bf16 v[204:207], v[152:155], a[232:235], v[204:207]\n"
        "ds_read_b128 a[232:235], v254 offset:16\n"
        "v_cvt_pk_bf16_f32 v88, v100, v101\n"
        "v_mfma_f32_16x16x32_bf16 v[192:195], v[152:155], a[216:219], 0\n"
        "s_waitcnt lgkmcnt(4)\n"
        "v_exp_f32 v102, v102\n"
        "v_mfma_f32_16x16x32_bf16 v[192:195], v[164:167], a[228:231], v[192:195]\n"
        "v_exp_f32 v104, v104\n"
        "v_mfma_f32_16x16x32_bf16 v[172:175], v[160:163], a[228:231], 0\n"
        "ds_read_b128 a[228:231], v254 offset:3088\n"
        "v_cvt_pk_bf16_f32 v74, v76, v77\n"
        "v_mfma_f32_16x16x32_bf16 v[172:175], v[156:159], a[216:219], v[172:175]\n"
        "ds_read_b128 a[216:219], v248 offset:6144\n"
        "v_cvt_pk_bf16_f32 v75, v78, v79\n"
        "v_mfma_f32_16x16x32_bf16 v[212:215], v[156:159], a[224:227], 0\n"
        "s_waitcnt lgkmcnt(3)\n"
        "ds_write_b128 v245, v[72:75] offset:512\n"
        "v_mfma_f32_16x16x32_bf16 v[212:215], v[160:163], a[220:223], v[212:215]\n"
        "v_exp_f32 v103, v103\n"
        "v_mfma_f32_16x16x32_bf16 v[216:219], v[164:167], a[220:223], 0\n"
        "ds_read_b128 a[220:223], v248 offset:9216\n"
        "v_cvt_pk_bf16_f32 v89, v102, v103\n"
        "v_mfma_f32_16x16x32_bf16 v[216:219], v[152:155], a[224:227], v[216:219]\n"
        "s_waitcnt lgkmcnt(4)\n"
        "ds_read_b128 a[224:227], v248 offset:6176\n"
        "v_mul_f32 v96, v116, v96\n"
        "v_mfma_f32_16x16x32_bf16 v[224:227], v[152:155], a[232:235], 0\n"
        "s_waitcnt lgkmcnt(4)\n"
        "v_exp_f32 v105, v105\n"
        "v_mfma_f32_16x16x32_bf16 v[224:227], v[164:167], a[228:231], v[224:227]\n"
        "v_exp_f32 v106, v106\n"
        "v_mfma_f32_16x16x32_bf16 v[220:223], v[160:163], a[228:231], 0\n"
        "ds_read_b128 a[228:231], v248 offset:9248\n"
        "v_cvt_pk_bf16_f32 v90, v104, v105\n"
        "v_mfma_f32_16x16x32_bf16 v[220:223], v[156:159], a[232:235], v[220:223]\n"
        "s_waitcnt lgkmcnt(4)\n"
        "ds_read_b128 a[232:235], v254 offset:6144\n"
        "v_mul_f32 v97, v116, v97\n"
        "v_mfma_f32_16x16x32_bf16 v[176:179], a[236:239], a[216:219], v[176:179]\n"
        "s_waitcnt lgkmcnt(3)\n"
        "v_mul_f32 v98, v116, v98\n"
        "v_mul_f32 v99, v116, v99\n"
        "v_mfma_f32_16x16x32_bf16 v[176:179], a[240:243], a[220:223], v[176:179]\n"
        "v_exp_f32 v107, v107\n"
        "v_mfma_f32_16x16x32_bf16 v[196:199], a[244:247], a[220:223], v[196:199]\n"
        "ds_read_b128 a[220:223], v254 offset:9216\n"
        "v_cvt_pk_bf16_f32 v91, v106, v107\n"
        "v_mfma_f32_16x16x32_bf16 v[196:199], a[248:251], a[216:219], v[196:199]\n"
        "s_waitcnt lgkmcnt(3)\n"
        "ds_read_b128 a[216:219], v254 offset:6176\n"
        "v_cvt_pk_bf16_f32 v96, v96, v97\n"
        "v_mfma_f32_16x16x32_bf16 v[200:203], a[248:251], a[224:227], v[200:203]\n"
        "s_waitcnt lgkmcnt(3)\n"
        "ds_read_b128 v[156:159], v247 offset:16384\n"
        "v_cvt_pk_bf16_f32 v97, v98, v99\n"
        "v_mfma_f32_16x16x32_bf16 v[200:203], a[244:247], a[228:231], v[200:203]\n"
        "ds_write_b64 v246, v[96:97] offset:512\n"
        "v_mfma_f32_16x16x32_bf16 v[180:183], a[240:243], a[228:231], v[180:183]\n"
        "ds_read_b128 a[228:231], v254 offset:9248\n"
        "v_max3_u32 v236, v236, v116, v117\n"
        "v_mfma_f32_16x16x32_bf16 v[180:183], a[236:239], a[224:227], v[180:183]\n"
        "s_waitcnt lgkmcnt(5)\n"
        "ds_read_b128 a[224:227], v248 offset:6160\n"
        "v_bfe_u32 v238, v250, 3, 4\n"
        "v_mfma_f32_16x16x32_bf16 v[184:187], a[236:239], a[232:235], v[184:187]\n"
        ".long 0xbec200ff\n"
        "44:\n"
        "s_branch 60b\n"
        "s_waitcnt lgkmcnt(5)\n"
        "ds_read_b128 v[160:163], v247 offset:20480\n"
        "v_mad_i32_i24 v238, v238, -4, v250\n"
        "v_mfma_f32_16x16x32_bf16 v[184:187], a[240:243], a[220:223], v[184:187]\n"
        "ds_read_b128 v[164:167], v247 offset:20736\n"
        "v_add_u32 v238, s49, v238\n"
        "v_mfma_f32_16x16x32_bf16 v[204:207], a[244:247], a[220:223], v[204:207]\n"
        "ds_read_b128 a[220:223], v248 offset:9232\n"
        "v_mfma_f32_16x16x32_bf16 v[204:207], a[248:251], a[232:235], v[204:207]\n"
        "s_waitcnt lgkmcnt(6)\n"
        "ds_read_b128 a[232:235], v254 offset:6160\n"
        "v_mfma_f32_16x16x32_bf16 v[192:195], a[248:251], a[216:219], v[192:195]\n"
        "s_waitcnt lgkmcnt(5)\n"
        "ds_read_b128 v[152:155], v247 offset:16640\n"
        "v_mfma_f32_16x16x32_bf16 v[192:195], a[244:247], a[228:231], v[192:195]\n"
        "v_exp_f32 v108, v108\n"
        "v_mfma_f32_16x16x32_bf16 v[172:175], a[240:243], a[228:231], v[172:175]\n"
        "ds_read_b128 a[228:231], v254 offset:9232\n"
        "v_mfma_f32_16x16x32_bf16 v[172:175], a[236:239], a[216:219], v[172:175]\n"
        "s_waitcnt lgkmcnt(5)\n"
        "ds_read_b128 a[216:219], v248 offset:12288\n"
        "v_mfma_f32_16x16x32_bf16 v[212:215], a[236:239], a[224:227], v[212:215]\n"
        "s_waitcnt lgkmcnt(4)\n"
        "v_exp_f32 v109, v109\n"
        "v_mfma_f32_16x16x32_bf16 v[212:215], a[240:243], a[220:223], v[212:215]\n"
        "v_exp_f32 v110, v110\n"
        "v_mfma_f32_16x16x32_bf16 v[216:219], a[244:247], a[220:223], v[216:219]\n"
        "ds_read_b128 a[220:223], v248 offset:15360\n"
        "v_cvt_pk_bf16_f32 v92, v108, v109\n"
        "v_mfma_f32_16x16x32_bf16 v[216:219], a[248:251], a[224:227], v[216:219]\n"
        "s_waitcnt lgkmcnt(3)\n"
        "ds_read_b128 a[224:227], v248 offset:12320\n"
        "v_mfma_f32_16x16x32_bf16 v[224:227], a[248:251], a[232:235], v[224:227]\n"
        "s_waitcnt lgkmcnt(3)\n"
        "v_exp_f32 v112, v112\n"
        "v_mfma_f32_16x16x32_bf16 v[224:227], a[244:247], a[228:231], v[224:227]\n"
        "v_exp_f32 v111, v111\n"
        "v_mfma_f32_16x16x32_bf16 v[220:223], a[240:243], a[228:231], v[220:223]\n"
        "ds_read_b128 a[228:231], v248 offset:15392\n"
        "v_cvt_pk_bf16_f32 v93, v110, v111\n"
        "v_mfma_f32_16x16x32_bf16 v[220:223], a[236:239], a[232:235], v[220:223]\n"
        "s_waitcnt lgkmcnt(3)\n"
        "ds_read_b128 a[232:235], v254 offset:12288\n"
        "v_mfma_f32_16x16x32_bf16 v[176:179], v[156:159], a[216:219], v[176:179]\n"
        "s_waitcnt lgkmcnt(3)\n"
        "v_exp_f32 v113, v113\n"
        "v_mfma_f32_16x16x32_bf16 v[176:179], v[160:163], a[220:223], v[176:179]\n"
        "v_exp_f32 v114, v114\n"
        "v_mfma_f32_16x16x32_bf16 v[196:199], v[164:167], a[220:223], v[196:199]\n"
        "ds_read_b128 a[220:223], v254 offset:15360\n"
        "v_cvt_pk_bf16_f32 v94, v112, v113\n"
        "v_mfma_f32_16x16x32_bf16 v[196:199], v[152:155], a[216:219], v[196:199]\n"
        "s_waitcnt lgkmcnt(3)\n"
        "ds_read_b128 a[216:219], v254 offset:12320\n"
        "v_mfma_f32_16x16x32_bf16 v[200:203], v[152:155], a[224:227], v[200:203]\n"
        "s_waitcnt lgkmcnt(3)\n"
        "v_exp_f32 v115, v115\n"
        "v_mfma_f32_16x16x32_bf16 v[200:203], v[164:167], a[228:231], v[200:203]\n"
        "v_cvt_pk_bf16_f32 v95, v114, v115\n"
        "v_mfma_f32_16x16x32_bf16 v[180:183], v[160:163], a[228:231], v[180:183]\n"
        "s_waitcnt vmcnt(0)\n"
        "s_barrier\n"
        "ds_read_b128 a[228:231], v254 offset:15392\n"
        "v_mfma_f32_16x16x32_bf16 v[180:183], v[156:159], a[224:227], v[180:183]\n"
        "s_waitcnt lgkmcnt(3)\n"
        "ds_read_b128 a[224:227], v248 offset:12304\n"
        "v_mfma_f32_16x16x32_bf16 v[184:187], v[156:159], a[232:235], v[184:187]\n"
        "s_waitcnt lgkmcnt(3)\n"
        "v_mfma_f32_16x16x32_bf16 v[184:187], v[160:163], a[220:223], v[184:187]\n"
        "v_mfma_f32_16x16x32_bf16 v[204:207], v[164:167], a[220:223], v[204:207]\n"
        "ds_read_b128 a[220:223], v248 offset:15376\n"
        "v_mfma_f32_16x16x32_bf16 v[204:207], v[152:155], a[232:235], v[204:207]\n"
        "s_waitcnt lgkmcnt(3)\n"
        "ds_read_b128 a[232:235], v254 offset:12304\n"
        "v_mfma_f32_16x16x32_bf16 v[192:195], v[152:155], a[216:219], v[192:195]\n"
        "s_waitcnt lgkmcnt(3)\n"
        "ds_read_b128 a[236:239], v240 offset:0\n"
        "v_mfma_f32_16x16x32_bf16 v[192:195], v[164:167], a[228:231], v[192:195]\n"
        "ds_read_b128 a[240:243], v241 offset:0\n"
        "v_mfma_f32_16x16x32_bf16 v[172:175], v[160:163], a[228:231], v[172:175]\n"
        "ds_read_b128 a[228:231], v254 offset:15376\n"
        "v_mfma_f32_16x16x32_bf16 v[172:175], v[156:159], a[216:219], v[172:175]\n"
        "s_waitcnt lgkmcnt(5)\n"
        "ds_read_b128 a[244:247], v240 offset:128\n"
        "v_mfma_f32_16x16x32_bf16 v[212:215], v[156:159], a[224:227], v[212:215]\n"
        "s_waitcnt lgkmcnt(5)\n"
        "ds_read_b128 a[248:251], v241 offset:128\n"
        "v_mfma_f32_16x16x32_bf16 v[212:215], v[160:163], a[220:223], v[212:215]\n"
        "ds_read_b128 a[216:219], v240 offset:256\n"
        "v_mfma_f32_16x16x32_bf16 v[216:219], v[164:167], a[220:223], v[216:219]\n"
        "ds_read_b128 a[220:223], v241 offset:256\n"
        "v_mfma_f32_16x16x32_bf16 v[216:219], v[152:155], a[224:227], v[216:219]\n"
        "s_waitcnt lgkmcnt(6)\n"
        "v_mfma_f32_16x16x32_bf16 v[224:227], v[152:155], a[232:235], v[224:227]\n"
        "s_waitcnt lgkmcnt(4)\n"
        "v_mfma_f32_16x16x32_bf16 v[224:227], v[164:167], a[228:231], v[224:227]\n"
        "v_mfma_f32_16x16x32_bf16 v[220:223], v[160:163], a[228:231], v[220:223]\n"
        "v_mfma_f32_16x16x32_bf16 v[220:223], v[156:159], a[232:235], v[220:223]\n"
        "s_waitcnt lgkmcnt(0)\n"
        "s_cmp_eq_u32 s42, 2\n"
        "s_cbranch_scc1 12f\n"
        "s_cmp_eq_u32 s42, 3\n"
        "s_cbranch_scc1 12f\n"
        ".p2align 6\n"
        "s_nop 0\n"
        "s_nop 0\n"
        "s_nop 0\n"
        "s_nop 0\n"
        "11:\n"
        "10:\n"
        "v_mfma_f32_16x16x32_bf16 v[112:115], a[72:75], a[236:239], 0\n"
        "v_mov_b64 v[124:125], 0\n"
        "v_cvt_pk_bf16_f32 v232, v176, v180\n"
        "v_mfma_f32_16x16x32_bf16 v[112:115], a[76:79], a[240:243], v[112:115]\n"
        "s_waitcnt lgkmcnt(3)\n"
        "s_add_u32 s41, s30, s40\n"
        "v_cvt_pk_bf16_f32 v233, v184, v172\n"
        "v_cvt_pk_bf16_f32 v234, v177, v181\n"
        "v_mfma_f32_16x16x32_bf16 v[112:115], a[80:83], a[244:247], v[112:115]\n"
        "s_waitcnt lgkmcnt(2)\n"
        "buffer_store_dwordx2 v[232:233], v250, s[16:19], s38 offen offset:0 sc1\n"
        "v_cvt_pk_bf16_f32 v235, v185, v173\n"
        "v_mfma_f32_16x16x32_bf16 v[112:115], a[84:87], a[248:251], v[112:115]\n"
        "s_waitcnt lgkmcnt(1)\n"
        "v_cvt_pk_bf16_f32 v232, v178, v182\n"
        "v_cvt_pk_bf16_f32 v233, v186, v174\n"
        "v_mfma_f32_16x16x32_bf16 v[112:115], a[88:91], a[216:219], v[112:115]\n"
        "s_waitcnt lgkmcnt(0)\n"
        "v_mfma_f32_16x16x32_bf16 v[112:115], a[92:95], a[220:223], v[112:115]\n"
        "s_nop 2\n"
        "v_smfmac_f32_16x16x64_bf16 v[124:127], a[252:255], v[88:95], v237\n"
        "s_nop 1\n"
        "v_mfma_f32_16x16x32_bf16 v[80:83], v[132:135], v[92:95], 0\n"
        "buffer_store_dwordx2 v[234:235], v250, s[16:19], s38 offen offset:384 sc1\n"
        "v_cvt_pk_bf16_f32 v152, v112, v113\n"
        "v_mfma_f32_16x16x32_bf16 v[80:83], v[128:131], v[88:91], v[80:83]\n"
        "v_cvt_pk_bf16_f32 v153, v114, v115\n"
        "v_cvt_pk_bf16_f32 v234, v179, v183\n"
        "v_mfma_f32_16x16x32_bf16 v[84:87], v[136:139], v[88:91], 0\n"
        "v_cvt_pk_bf16_f32 v235, v187, v175\n"
        "v_add_f32 v125, v124, v125\n"
        "v_mfma_f32_16x16x32_bf16 v[84:87], v[140:143], v[92:95], v[84:87]\n"
        "v_rcp_f32 v124, v125\n"
        "v_mfma_f32_16x16x32_bf16 v[96:99], v[148:151], v[92:95], 0\n"
        "buffer_store_dwordx2 v[232:233], v250, s[16:19], s38 offen offset:768 sc1\n"
        "v_mul_f32 v80, v124, v80\n"
        "v_mfma_f32_16x16x32_bf16 v[96:99], v[144:147], v[88:91], v[96:99]\n"
        "s_add_u32 s35, s47, 786432\n"
        "v_mul_f32 v81, v124, v81\n"
        "v_mul_f32 v82, v124, v82\n"
        "v_mfma_f32_16x16x32_bf16 v[116:119], a[116:119], a[220:223], 0\n"
        "s_sub_u32 s46, s35, 100663296\n"
        "v_mul_f32 v83, v124, v83\n"
        "v_cvt_pk_bf16_f32 v80, v80, v81\n"
        "v_mfma_f32_16x16x32_bf16 v[116:119], a[112:115], a[216:219], v[116:119]\n"
        "s_cmp_lt_u32 s46, 786432\n"
        "v_cvt_pk_bf16_f32 v81, v82, v83\n"
        "v_cvt_pk_bf16_f32 v232, v196, v200\n"
        "v_mfma_f32_16x16x32_bf16 v[116:119], a[108:111], a[248:251], v[116:119]\n"
        "buffer_store_dwordx2 v[234:235], v250, s[16:19], s38 offen offset:1152 sc1\n"
        "v_cvt_pk_bf16_f32 v233, v204, v192\n"
        "v_mfma_f32_16x16x32_bf16 v[116:119], a[104:107], a[244:247], v[116:119]\n"
        "s_cselect_b32 s35, s46, s35\n"
        "v_mul_f32 v84, v124, v84\n"
        "v_mul_f32 v85, v124, v85\n"
        "v_mfma_f32_16x16x32_bf16 v[116:119], a[100:103], a[240:243], v[116:119]\n"
        "v_mul_f32 v86, v124, v86\n"
        "v_mul_f32 v87, v124, v87\n"
        "v_mfma_f32_16x16x32_bf16 v[116:119], a[96:99], a[236:239], v[116:119]\n"
        "v_cvt_pk_bf16_f32 v82, v84, v85\n"
        "v_cvt_pk_bf16_f32 v83, v86, v87\n"
        "v_mfma_f32_16x16x32_bf16 v[120:123], a[120:123], a[236:239], 0\n"
        "ds_write_b128 v245, v[80:83] offset:768\n"
        "v_mfma_f32_16x16x32_bf16 v[120:123], a[124:127], a[240:243], v[120:123]\n"
        "buffer_store_dwordx2 v[232:233], v250, s[16:19], s39 offen offset:0 sc1\n"
        "v_mul_f32 v96, v124, v96\n"
        "v_mfma_f32_16x16x32_bf16 v[120:123], a[128:131], a[244:247], v[120:123]\n"
        "v_mul_f32 v97, v124, v97\n"
        "v_mul_f32 v98, v124, v98\n"
        "v_mfma_f32_16x16x32_bf16 v[120:123], a[132:135], a[248:251], v[120:123]\n"
        "v_mul_f32 v99, v124, v99\n"
        "v_cvt_pk_bf16_f32 v234, v197, v201\n"
        "v_mfma_f32_16x16x32_bf16 v[120:123], a[136:139], a[216:219], v[120:123]\n"
        "v_cvt_pk_bf16_f32 v235, v205, v193\n"
        "v_cvt_pk_bf16_f32 v154, v116, v117\n"
        "v_mfma_f32_16x16x32_bf16 v[120:123], a[140:143], a[220:223], v[120:123]\n"
        "buffer_store_dwordx2 v[234:235], v250, s[16:19], s39 offen offset:384 sc1\n"
        "v_cvt_pk_bf16_f32 v155, v118, v119\n"
        "v_mfma_f32_16x16x32_bf16 v[112:115], a[20:23], a[220:223], 0\n"
        "v_cvt_pk_bf16_f32 v96, v96, v97\n"
        "v_cvt_pk_bf16_f32 v97, v98, v99\n"
        "v_mfma_f32_16x16x32_bf16 v[112:115], a[16:19], a[216:219], v[112:115]\n"
        "ds_write_b64 v246, v[96:97] offset:768\n"
        "v_mfma_f32_16x16x32_bf16 v[112:115], a[12:15], a[248:251], v[112:115]\n"
        "v_max3_u32 v236, v236, v124, v125\n"
        "v_cvt_pk_bf16_f32 v232, v198, v202\n"
        "v_mfma_f32_16x16x32_bf16 v[112:115], a[8:11], a[244:247], v[112:115]\n"
        "v_cvt_pk_bf16_f32 v233, v206, v194\n"
        "v_cmp_lt_u32 vcc, s24, v236\n"
        "v_mfma_f32_16x16x32_bf16 v[112:115], a[4:7], a[240:243], v[112:115]\n"
        "buffer_store_dwordx2 v[232:233], v250, s[16:19], s39 offen offset:768 sc1\n"
        "v_cvt_pk_bf16_f32 v168, v120, v121\n"
        "v_mfma_f32_16x16x32_bf16 v[112:115], a[0:3], a[236:239], v[112:115]\n"
        "s_cmp_lg_u64 vcc, 0\n"
        "s_addc_u32 s25, s25, s25\n"
        "v_cvt_pk_bf16_f32 v169, v122, v123\n"
        "v_mfma_f32_16x16x32_bf16 v[116:119], a[24:27], a[236:239], 0\n"
        "v_cvt_pk_bf16_f32 v234, v199, v203\n"
        "v_cvt_pk_bf16_f32 v235, v207, v195\n"
        "v_mfma_f32_16x16x32_bf16 v[116:119], a[28:31], a[240:243], v[116:119]\n"
        "v_cvt_pk_bf16_f32 v232, v212, v220\n"
        "v_cvt_pk_bf16_f32 v156, v112, v113\n"
        "v_mfma_f32_16x16x32_bf16 v[116:119], a[32:35], a[244:247], v[116:119]\n"
        "buffer_store_dwordx2 v[234:235], v250, s[16:19], s39 offen offset:1152 sc1\n"
        "v_cvt_pk_bf16_f32 v157, v114, v115\n"
        "v_mfma_f32_16x16x32_bf16 v[116:119], a[36:39], a[248:251], v[116:119]\n"
        "v_cvt_pk_bf16_f32 v234, v213, v221\n"
        "v_xor_b32 v251, 0x2000, v251\n"
        "v_mfma_f32_16x16x32_bf16 v[116:119], a[40:43], a[216:219], v[116:119]\n"
        "v_xor_b32 v252, 0x2000, v252\n"
        "v_mfma_f32_16x16x32_bf16 v[116:119], a[44:47], a[220:223], v[116:119]\n"
        "s_nop 2\n"
        "v_mfma_f32_16x16x32_bf16 v[120:123], a[68:71], a[220:223], 0\n"
        "s_nop 2\n"
        "v_mfma_f32_16x16x32_bf16 v[120:123], a[64:67], a[216:219], v[120:123]\n"
        "buffer_store_dword v232, v238, s[16:19], s38 offen offset:0 sc1\n"
        "v_mfma_f32_16x16x32_bf16 v[120:123], a[60:63], a[248:251], v[120:123]\n"
        "ds_read_b128 a[224:227], v240 offset:1536\n"
        "v_cvt_pk_bf16_f32 v158, v116, v117\n"
        "v_mfma_f32_16x16x32_bf16 v[120:123], a[56:59], a[244:247], v[120:123]\n"
        "ds_read_b128 a[228:231], v241 offset:1536\n"
        "v_cvt_pk_bf16_f32 v159, v118, v119\n"
        "v_mfma_f32_16x16x32_bf16 v[120:123], a[52:55], a[240:243], v[120:123]\n"
        "ds_read_b128 a[232:235], v240 offset:1664\n"
        "v_cvt_pk_bf16_f32 v232, v214, v222\n"
        "v_mfma_f32_16x16x32_bf16 v[120:123], a[48:51], a[236:239], v[120:123]\n"
        "buffer_store_dword v234, v238, s[16:19], s38 offen offset:384 sc1\n"
        "v_mfma_f32_16x16x32_bf16 v[112:115], a[236:239], a[144:147], 0\n"
        "v_add_u32 v247, s45, v247\n"
        "v_cvt_pk_bf16_f32 v234, v215, v223\n"
        "v_mfma_f32_16x16x32_bf16 v[112:115], a[240:243], a[148:151], v[112:115]\n"
        "s_nop 2\n"
        "v_mfma_f32_16x16x32_bf16 v[112:115], a[244:247], a[152:155], v[112:115]\n"
        "v_cvt_pk_bf16_f32 v160, v120, v121\n"
        "v_cvt_pk_bf16_f32 v161, v122, v123\n"
        "v_mfma_f32_16x16x32_bf16 v[112:115], a[248:251], a[156:159], v[112:115]\n"
        "v_cvt_pk_bf16_f32 v166, v120, v121\n"
        "v_cvt_pk_bf16_f32 v167, v122, v123\n"
        "v_mfma_f32_16x16x32_bf16 v[112:115], a[216:219], a[160:163], v[112:115]\n"
        "buffer_store_dword v232, v238, s[16:19], s38 offen offset:768 sc1\n"
        "v_mfma_f32_16x16x32_bf16 v[112:115], a[220:223], a[164:167], v[112:115]\n"
        "s_nop 2\n"
        "v_mfma_f32_16x16x32_bf16 v[116:119], a[220:223], a[188:191], 0\n"
        "v_cvt_pk_bf16_f32 v232, v216, v224\n"
        "s_nop 0\n"
        "v_mfma_f32_16x16x32_bf16 v[116:119], a[216:219], a[184:187], v[116:119]\n"
        "s_nop 0\n"
        "v_cvt_pk_bf16_f32 v128, v112, v113\n"
        "v_cvt_pk_bf16_f32 v129, v114, v115\n"
        "v_mfma_f32_16x16x32_bf16 v[116:119], a[248:251], a[180:183], v[116:119]\n"
        "buffer_store_dword v234, v238, s[16:19], s38 offen offset:1152 sc1\n"
        "v_mfma_f32_16x16x32_bf16 v[116:119], a[244:247], a[176:179], v[116:119]\n"
        "s_nop 0\n"
        "v_cvt_pk_bf16_f32 v234, v217, v225\n"
        "v_mfma_f32_16x16x32_bf16 v[116:119], a[240:243], a[172:175], v[116:119]\n"
        "v_mfma_f32_16x16x32_bf16 v[116:119], a[236:239], a[168:171], v[116:119]\n"
        "s_nop 2\n"
        "v_mfma_f32_16x16x32_bf16 v[120:123], a[236:239], a[192:195], 0\n"
        "ds_read_b128 a[236:239], v241 offset:1664\n"
        "s_nop 0\n"
        "v_mfma_f32_16x16x32_bf16 v[120:123], a[240:243], a[196:199], v[120:123]\n"
        "buffer_store_dword v232, v238, s[16:19], s39 offen offset:0 sc1\n"
        "v_mfma_f32_16x16x32_bf16 v[120:123], a[244:247], a[200:203], v[120:123]\n"
        "ds_read_b128 a[240:243], v240 offset:1792\n"
        "ds_read_b128 a[244:247], v241 offset:1792\n"
        "v_mfma_f32_16x16x32_bf16 v[120:123], a[248:251], a[204:207], v[120:123]\n"
        "v_cvt_pk_bf16_f32 v136, v116, v117\n"
        "v_cvt_pk_bf16_f32 v137, v118, v119\n"
        "v_mfma_f32_16x16x32_bf16 v[120:123], a[216:219], a[208:211], v[120:123]\n"
        "v_cvt_pk_bf16_f32 v232, v218, v226\n"
        "v_mfma_f32_16x16x32_bf16 v[120:123], a[220:223], a[212:215], v[120:123]\n"
        "s_waitcnt lgkmcnt(5)\n"
        "buffer_store_dword v234, v238, s[16:19], s39 offen offset:384 sc1\n"
        "v_mfma_f32_16x16x32_bf16 v[112:115], a[72:75], a[224:227], 0\n"
        "s_waitcnt lgkmcnt(4)\n"
        "s_nop 1\n"
        "v_mfma_f32_16x16x32_bf16 v[112:115], a[76:79], a[228:231], v[112:115]\n"
        "s_waitcnt lgkmcnt(3)\n"
        "v_cvt_pk_bf16_f32 v234, v219, v227\n"
        "s_nop 0\n"
        "v_mfma_f32_16x16x32_bf16 v[112:115], a[80:83], a[232:235], v[112:115]\n"
        "s_waitcnt lgkmcnt(2)\n"
        "v_cvt_pk_bf16_f32 v144, v120, v121\n"
        "v_cvt_pk_bf16_f32 v145, v122, v123\n"
        "v_mfma_f32_16x16x32_bf16 v[112:115], a[84:87], a[236:239], v[112:115]\n"
        "s_waitcnt lgkmcnt(1)\n"
        "v_mfma_f32_16x16x32_bf16 v[112:115], a[88:91], a[240:243], v[112:115]\n"
        "s_waitcnt lgkmcnt(0)\n"
        "buffer_store_dword v232, v238, s[16:19], s39 offen offset:768 sc1\n"
        "v_mfma_f32_16x16x32_bf16 v[112:115], a[92:95], a[244:247], v[112:115]\n"
        "s_nop 2\n"
        "v_mfma_f32_16x16x32_bf16 v[116:119], a[116:119], a[244:247], 0\n"
        "s_nop 2\n"
        "v_mfma_f32_16x16x32_bf16 v[116:119], a[112:115], a[240:243], v[116:119]\n"
        "v_cvt_pk_bf16_f32 v172, v112, v113\n"
        "v_cvt_pk_bf16_f32 v173, v114, v115\n"
        "v_mfma_f32_16x16x32_bf16 v[116:119], a[108:111], a[236:239], v[116:119]\n"
        "buffer_store_dword v234, v238, s[16:19], s39 offen offset:1152 sc1\n"
        "v_mfma_f32_16x16x32_bf16 v[116:119], a[104:107], a[232:235], v[116:119]\n"
        "v_mfma_f32_16x16x32_bf16 v[116:119], a[100:103], a[228:231], v[116:119]\n"
        "v_mfma_f32_16x16x32_bf16 v[116:119], a[96:99], a[224:227], v[116:119]\n"
        "s_mov_b32 m0, s48\n"
        "s_nop 1\n"
        "v_mfma_f32_16x16x32_bf16 v[120:123], a[120:123], a[224:227], 0\n"
        "ds_read_b128 v[162:165], v251 offset:0\n"
        "s_nop 0\n"
        "v_mfma_f32_16x16x32_bf16 v[120:123], a[124:127], a[228:231], v[120:123]\n"
        "buffer_load_dwordx4 v249, s[12:15], s36 offen offset:0 lds\n"
        "v_mfma_f32_16x16x32_bf16 v[120:123], a[128:131], a[232:235], v[120:123]\n"
        "v_cvt_pk_bf16_f32 v174, v116, v117\n"
        "v_cvt_pk_bf16_f32 v175, v118, v119\n"
        "v_mfma_f32_16x16x32_bf16 v[120:123], a[132:135], a[236:239], v[120:123]\n"
        "v_mfma_f32_16x16x32_bf16 v[120:123], a[136:139], a[240:243], v[120:123]\n"
        "v_mfma_f32_16x16x32_bf16 v[120:123], a[140:143], a[244:247], v[120:123]\n"
        "s_nop 2\n"
        "v_mfma_f32_16x16x32_bf16 v[112:115], a[20:23], a[244:247], 0\n"
        "s_nop 2\n"
        "v_mfma_f32_16x16x32_bf16 v[112:115], a[16:19], a[240:243], v[112:115]\n"
        "v_cvt_pk_bf16_f32 v190, v120, v121\n"
        "v_cvt_pk_bf16_f32 v191, v122, v123\n"
        "v_mfma_f32_16x16x32_bf16 v[112:115], a[12:15], a[236:239], v[112:115]\n"
        "v_mfma_f32_16x16x32_bf16 v[112:115], a[8:11], a[232:235], v[112:115]\n"
        "v_mfma_f32_16x16x32_bf16 v[112:115], a[4:7], a[228:231], v[112:115]\n"
        "ds_read_b128 v[182:185], v251 offset:512\n"
        "v_mfma_f32_16x16x32_bf16 v[112:115], a[0:3], a[224:227], v[112:115]\n"
        "s_nop 2\n"
        "v_mfma_f32_16x16x32_bf16 v[116:119], a[24:27], a[224:227], 0\n"
        "s_nop 2\n"
        "v_mfma_f32_16x16x32_bf16 v[116:119], a[28:31], a[228:231], v[116:119]\n"
        "v_cvt_pk_bf16_f32 v176, v112, v113\n"
        "v_cvt_pk_bf16_f32 v177, v114, v115\n"
        "v_mfma_f32_16x16x32_bf16 v[116:119], a[32:35], a[232:235], v[116:119]\n"
        "v_mfma_f32_16x16x32_bf16 v[116:119], a[36:39], a[236:239], v[116:119]\n"
        "buffer_load_dwordx4 v253, s[12:15], s36 offen offset:1024 lds\n"
        "v_mfma_f32_16x16x32_bf16 v[116:119], a[40:43], a[240:243], v[116:119]\n"
        "v_mfma_f32_16x16x32_bf16 v[116:119], a[44:47], a[244:247], v[116:119]\n"
        "s_nop 2\n"
        "v_mfma_f32_16x16x32_bf16 v[120:123], a[68:71], a[244:247], 0\n"
        "s_nop 2\n"
        "v_mfma_f32_16x16x32_bf16 v[120:123], a[64:67], a[240:243], v[120:123]\n"
        "v_cvt_pk_bf16_f32 v178, v116, v117\n"
        "v_cvt_pk_bf16_f32 v179, v118, v119\n"
        "v_mfma_f32_16x16x32_bf16 v[120:123], a[60:63], a[236:239], v[120:123]\n"
        "v_mfma_f32_16x16x32_bf16 v[120:123], a[56:59], a[232:235], v[120:123]\n"
        "v_mfma_f32_16x16x32_bf16 v[120:123], a[52:55], a[228:231], v[120:123]\n"
        "v_mfma_f32_16x16x32_bf16 v[120:123], a[48:51], a[224:227], v[120:123]\n"
        "s_nop 2\n"
        "v_mfma_f32_16x16x32_bf16 v[112:115], a[224:227], a[144:147], 0\n"
        "s_nop 2\n"
        "v_mfma_f32_16x16x32_bf16 v[112:115], a[228:231], a[148:151], v[112:115]\n"
        "v_cvt_pk_bf16_f32 v180, v120, v121\n"
        "v_cvt_pk_bf16_f32 v181, v122, v123\n"
        "v_mfma_f32_16x16x32_bf16 v[112:115], a[232:235], a[152:155], v[112:115]\n"
        "v_cvt_pk_bf16_f32 v186, v120, v121\n"
        "v_cvt_pk_bf16_f32 v187, v122, v123\n"
        "v_mfma_f32_16x16x32_bf16 v[112:115], a[236:239], a[156:159], v[112:115]\n"
        "s_add_u32 m0, s41, 0\n"
        "v_mfma_f32_16x16x32_bf16 v[112:115], a[240:243], a[160:163], v[112:115]\n"
        "ds_read_b128 a[248:251], v240 offset:12288\n"
        "v_mfma_f32_16x16x32_bf16 v[112:115], a[244:247], a[164:167], v[112:115]\n"
        "buffer_load_dwordx4 v242, s[8:11], s47 offen lds\n"
        "v_mfma_f32_16x16x32_bf16 v[96:99], v[152:155], v[156:159], v[0:3]\n"
        "s_waitcnt lgkmcnt(2)\n"
        "ds_read_b128 a[216:219], v241 offset:12288\n"
        "s_nop 0\n"
        "v_mfma_f32_16x16x32_bf16 v[96:99], v[168:171], v[160:163], v[96:99]\n"
        "ds_read_b128 a[220:223], v240 offset:12416\n"
        "s_sub_u32 s31, s30, s28\n"
        "s_nop 0\n"
        "v_mfma_f32_16x16x32_bf16 v[100:103], v[172:175], v[156:159], v[4:7]\n"
        "s_sub_u32 s45, s28, s29\n"
        "v_cvt_pk_bf16_f32 v130, v112, v113\n"
        "v_cvt_pk_bf16_f32 v131, v114, v115\n"
        "v_mfma_f32_16x16x32_bf16 v[100:103], v[188:191], v[164:167], v[100:103]\n"
        "s_add_u32 s28, s28, s31\n"
        "ds_read_b128 v[162:165], v252 offset:0\n"
        "v_mfma_f32_16x16x32_bf16 v[116:119], a[244:247], a[188:191], 0\n"
        "s_mov_b32 s30, s29\n"
        "v_exp_f32 v96, v96\n"
        "v_mfma_f32_16x16x32_bf16 v[116:119], a[240:243], a[184:187], v[116:119]\n"
        "s_add_u32 s29, s29, s45\n"
        "v_exp_f32 v97, v97\n"
        "v_mfma_f32_16x16x32_bf16 v[116:119], a[236:239], a[180:183], v[116:119]\n"
        "v_exp_f32 v98, v98\n"
        "v_mfma_f32_16x16x32_bf16 v[116:119], a[232:235], a[176:179], v[116:119]\n"
        "v_exp_f32 v100, v100\n"
        "v_mfma_f32_16x16x32_bf16 v[116:119], a[228:231], a[172:175], v[116:119]\n"
        "v_exp_f32 v99, v99\n"
        "v_mfma_f32_16x16x32_bf16 v[116:119], a[224:227], a[168:171], v[116:119]\n"
        "v_exp_f32 v101, v101\n"
        "v_mfma_f32_16x16x32_bf16 v[104:107], v[152:155], v[176:179], v[16:19]\n"
        "s_waitcnt lgkmcnt(4)\n"
        "v_exp_f32 v102, v102\n"
        "v_mfma_f32_16x16x32_bf16 v[104:107], v[168:171], v[180:183], v[104:107]\n"
        "s_add_u32 m0, s41, 1024\n"
        "v_cvt_pk_bf16_f32 v64, v96, v97\n"
        "v_cvt_pk_bf16_f32 v65, v98, v99\n"
        "v_mfma_f32_16x16x32_bf16 v[108:111], v[172:175], v[176:179], v[20:23]\n"
        "v_cvt_pk_bf16_f32 v138, v116, v117\n"
        "v_cvt_pk_bf16_f32 v139, v118, v119\n"
        "v_mfma_f32_16x16x32_bf16 v[108:111], v[188:191], v[184:187], v[108:111]\n"
        "buffer_load_dwordx4 v243, s[8:11], s47 offen lds\n"
        "v_mfma_f32_16x16x32_bf16 v[120:123], a[224:227], a[192:195], 0\n"
        "ds_read_b128 a[224:227], v241 offset:12416\n"
        "v_cvt_pk_bf16_f32 v66, v100, v101\n"
        "v_mfma_f32_16x16x32_bf16 v[120:123], a[228:231], a[196:199], v[120:123]\n"
        "ds_read_b128 a[228:231], v240 offset:12544\n"
        "v_mfma_f32_16x16x32_bf16 v[120:123], a[232:235], a[200:203], v[120:123]\n"
        "ds_read_b128 a[232:235], v241 offset:12544\n"
        "v_mfma_f32_16x16x32_bf16 v[120:123], a[236:239], a[204:207], v[120:123]\n"
        "ds_read_b128 v[202:205], v251 offset:4096\n"
        "v_mfma_f32_16x16x32_bf16 v[120:123], a[240:243], a[208:211], v[120:123]\n"
        "ds_read_b128 v[182:185], v252 offset:512\n"
        "v_mfma_f32_16x16x32_bf16 v[120:123], a[244:247], a[212:215], v[120:123]\n"
        "s_waitcnt lgkmcnt(8)\n"
        "v_exp_f32 v103, v103\n"
        "v_mfma_f32_16x16x32_bf16 v[112:115], a[0:3], a[248:251], 0\n"
        "s_waitcnt lgkmcnt(7)\n"
        "v_cvt_pk_bf16_f32 v67, v102, v103\n"
        "s_nop 0\n"
        "v_mfma_f32_16x16x32_bf16 v[112:115], a[4:7], a[216:219], v[112:115]\n"
        "s_waitcnt lgkmcnt(6)\n"
        "v_exp_f32 v104, v104\n"
        "v_mfma_f32_16x16x32_bf16 v[112:115], a[8:11], a[220:223], v[112:115]\n"
        "s_waitcnt lgkmcnt(4)\n"
        "v_exp_f32 v105, v105\n"
        "v_mfma_f32_16x16x32_bf16 v[112:115], a[12:15], a[224:227], v[112:115]\n"
        "s_waitcnt lgkmcnt(3)\n"
        "v_exp_f32 v106, v106\n"
        "v_mfma_f32_16x16x32_bf16 v[112:115], a[16:19], a[228:231], v[112:115]\n"
        "s_waitcnt lgkmcnt(2)\n"
        "v_cvt_pk_bf16_f32 v72, v104, v105\n"
        "v_cvt_pk_bf16_f32 v146, v120, v121\n"
        "v_mfma_f32_16x16x32_bf16 v[112:115], a[20:23], a[232:235], v[112:115]\n"
        "s_add_u32 m0, s41, 2048\n"
        "v_exp_f32 v108, v108\n"
        "v_mfma_f32_16x16x32_bf16 v[116:119], a[44:47], a[232:235], 0\n"
        "v_cvt_pk_bf16_f32 v147, v122, v123\n"
        "v_add_u32 v245, s45, v245\n"
        "v_mfma_f32_16x16x32_bf16 v[116:119], a[40:43], a[228:231], v[116:119]\n"
        "buffer_load_dwordx4 v244, s[8:11], s47 offen lds\n"
        "v_mfma_f32_16x16x32_bf16 v[116:119], a[36:39], a[224:227], v[116:119]\n"
        "v_exp_f32 v107, v107\n"
        "v_mfma_f32_16x16x32_bf16 v[116:119], a[32:35], a[220:223], v[116:119]\n"
        "v_exp_f32 v109, v109\n"
        "v_mfma_f32_16x16x32_bf16 v[116:119], a[28:31], a[216:219], v[116:119]\n"
        "v_cvt_pk_bf16_f32 v73, v106, v107\n"
        "v_cvt_pk_bf16_f32 v74, v108, v109\n"
        "v_mfma_f32_16x16x32_bf16 v[116:119], a[24:27], a[248:251], v[116:119]\n"
        "v_exp_f32 v110, v110\n"
        "v_mfma_f32_16x16x32_bf16 v[120:123], a[48:51], a[248:251], 0\n"
        "v_exp_f32 v111, v111\n"
        "v_mfma_f32_16x16x32_bf16 v[120:123], a[52:55], a[216:219], v[120:123]\n"
        "v_cvt_pk_bf16_f32 v75, v110, v111\n"
        "v_cvt_pk_bf16_f32 v196, v112, v113\n"
        "v_mfma_f32_16x16x32_bf16 v[120:123], a[56:59], a[220:223], v[120:123]\n"
        "v_cvt_pk_bf16_f32 v197, v114, v115\n"
        "v_cvt_pk_bf16_f32 v198, v116, v117\n"
        "v_mfma_f32_16x16x32_bf16 v[120:123], a[60:63], a[224:227], v[120:123]\n"
        "v_cvt_pk_bf16_f32 v199, v118, v119\n"
        "v_add_u32 v246, s45, v246\n"
        "v_mfma_f32_16x16x32_bf16 v[120:123], a[64:67], a[228:231], v[120:123]\n"
        "v_mfma_f32_16x16x32_bf16 v[120:123], a[68:71], a[232:235], v[120:123]\n"
        "s_nop 2\n"
        "v_mfma_f32_16x16x32_bf16 v[112:115], a[92:95], a[232:235], 0\n"
        "s_nop 2\n"
        "v_mfma_f32_16x16x32_bf16 v[112:115], a[88:91], a[228:231], v[112:115]\n"
        "s_add_u32 m0, s41, 12288\n"
        "v_cvt_pk_bf16_f32 v200, v120, v121\n"
        "v_cvt_pk_bf16_f32 v201, v122, v123\n"
        "v_mfma_f32_16x16x32_bf16 v[112:115], a[84:87], a[224:227], v[112:115]\n"
        "v_cvt_pk_bf16_f32 v206, v120, v121\n"
        "v_cvt_pk_bf16_f32 v207, v122, v123\n"
        "v_mfma_f32_16x16x32_bf16 v[112:115], a[80:83], a[220:223], v[112:115]\n"
        "buffer_load_dwordx4 v242, s[8:11], s35 offen lds\n"
        "v_mfma_f32_16x16x32_bf16 v[112:115], a[76:79], a[216:219], v[112:115]\n"
        "v_mfma_f32_16x16x32_bf16 v[112:115], a[72:75], a[248:251], v[112:115]\n"
        "s_nop 2\n"
        "v_mfma_f32_16x16x32_bf16 v[96:99], v[152:155], v[196:199], v[32:35]\n"
        "s_waitcnt lgkmcnt(1)\n"
        "s_nop 1\n"
        "v_mfma_f32_16x16x32_bf16 v[96:99], v[168:171], v[200:203], v[96:99]\n"
        "s_nop 0\n"
        "v_cvt_pk_bf16_f32 v192, v112, v113\n"
        "v_cvt_pk_bf16_f32 v193, v114, v115\n"
        "v_mfma_f32_16x16x32_bf16 v[100:103], v[172:175], v[196:199], v[36:39]\n"
        "v_mfma_f32_16x16x32_bf16 v[100:103], v[188:191], v[204:207], v[100:103]\n"
        "ds_read_b128 v[202:205], v252 offset:4096\n"
        "s_cmp_eq_u32 s37, 100663296\n"
        "v_mfma_f32_16x16x32_bf16 v[116:119], a[96:99], a[248:251], 0\n"
        "s_cselect_b32 s46, 0, s37\n"
        "v_exp_f32 v96, v96\n"
        "v_mfma_f32_16x16x32_bf16 v[116:119], a[100:103], a[216:219], v[116:119]\n"
        "s_add_u32 s38, s46, s43\n"
        "v_exp_f32 v97, v97\n"
        "v_mfma_f32_16x16x32_bf16 v[116:119], a[104:107], a[220:223], v[116:119]\n"
        "s_add_u32 s39, s46, s44\n"
        "v_exp_f32 v98, v98\n"
        "v_mfma_f32_16x16x32_bf16 v[116:119], a[108:111], a[224:227], v[116:119]\n"
        "s_add_u32 s37, s37, 6291456\n"
        "v_exp_f32 v100, v100\n"
        "v_mfma_f32_16x16x32_bf16 v[116:119], a[112:115], a[228:231], v[116:119]\n"
        "v_exp_f32 v99, v99\n"
        "v_mfma_f32_16x16x32_bf16 v[116:119], a[116:119], a[232:235], v[116:119]\n"
        "s_add_u32 m0, s41, 13312\n"
        "v_exp_f32 v101, v101\n"
        "v_mfma_f32_16x16x32_bf16 v[120:123], a[140:143], a[232:235], 0\n"
        "v_exp_f32 v102, v102\n"
        "v_mfma_f32_16x16x32_bf16 v[120:123], a[136:139], a[228:231], v[120:123]\n"
        "buffer_load_dwordx4 v243, s[8:11], s35 offen lds\n"
        "v_mfma_f32_16x16x32_bf16 v[120:123], a[132:135], a[224:227], v[120:123]\n"
        "v_cvt_pk_bf16_f32 v80, v96, v97\n"
        "v_cvt_pk_bf16_f32 v194, v116, v117\n"
        "v_mfma_f32_16x16x32_bf16 v[120:123], a[128:131], a[220:223], v[120:123]\n"
        "v_cvt_pk_bf16_f32 v195, v118, v119\n"
        "v_cvt_pk_bf16_f32 v81, v98, v99\n"
        "v_mfma_f32_16x16x32_bf16 v[120:123], a[124:127], a[216:219], v[120:123]\n"
        "v_cvt_pk_bf16_f32 v82, v100, v101\n"
        "v_mfma_f32_16x16x32_bf16 v[120:123], a[120:123], a[248:251], v[120:123]\n"
        "v_exp_f32 v103, v103\n"
        "v_mfma_f32_16x16x32_bf16 v[112:115], a[248:251], a[144:147], 0\n"
        "v_cvt_pk_bf16_f32 v83, v102, v103\n"
        "s_nop 0\n"
        "v_mfma_f32_16x16x32_bf16 v[112:115], a[216:219], a[148:151], v[112:115]\n"
        "s_nop 2\n"
        "v_mfma_f32_16x16x32_bf16 v[112:115], a[220:223], a[152:155], v[112:115]\n"
        "ds_read_b128 a[236:239], v240 offset:13824\n"
        "v_cvt_pk_bf16_f32 v208, v120, v121\n"
        "v_mfma_f32_16x16x32_bf16 v[112:115], a[224:227], a[156:159], v[112:115]\n"
        "ds_read_b128 a[240:243], v241 offset:13824\n"
        "v_cvt_pk_bf16_f32 v209, v122, v123\n"
        "v_mfma_f32_16x16x32_bf16 v[112:115], a[228:231], a[160:163], v[112:115]\n"
        "ds_read_b128 a[244:247], v240 offset:13952\n"
        "v_mfma_f32_16x16x32_bf16 v[112:115], a[232:235], a[164:167], v[112:115]\n"
        "s_nop 2\n"
        "v_mfma_f32_16x16x32_bf16 v[104:107], v[192:195], v[156:159], v[8:11]\n"
        "s_nop 1\n"
        "v_mfma_f32_16x16x32_bf16 v[104:107], v[208:211], v[160:163], v[104:107]\n"
        "s_add_u32 m0, s41, 14336\n"
        "v_cvt_pk_bf16_f32 v132, v112, v113\n"
        "v_cvt_pk_bf16_f32 v133, v114, v115\n"
        "v_mfma_f32_16x16x32_bf16 v[116:119], a[232:235], a[188:191], 0\n"
        "v_mfma_f32_16x16x32_bf16 v[116:119], a[228:231], a[184:187], v[116:119]\n"
        "buffer_load_dwordx4 v244, s[8:11], s35 offen lds\n"
        "v_mfma_f32_16x16x32_bf16 v[116:119], a[224:227], a[180:183], v[116:119]\n"
        "v_mfma_f32_16x16x32_bf16 v[116:119], a[220:223], a[176:179], v[116:119]\n"
        "v_exp_f32 v104, v104\n"
        "v_mfma_f32_16x16x32_bf16 v[116:119], a[216:219], a[172:175], v[116:119]\n"
        "v_exp_f32 v105, v105\n"
        "v_mfma_f32_16x16x32_bf16 v[116:119], a[248:251], a[168:171], v[116:119]\n"
        "v_exp_f32 v106, v106\n"
        "v_mfma_f32_16x16x32_bf16 v[108:111], v[192:195], v[176:179], v[24:27]\n"
        "s_waitcnt lgkmcnt(4)\n"
        "v_exp_f32 v107, v107\n"
        "v_mfma_f32_16x16x32_bf16 v[108:111], v[208:211], v[180:183], v[108:111]\n"
        "v_cvt_pk_bf16_f32 v68, v104, v105\n"
        "v_cvt_pk_bf16_f32 v69, v106, v107\n"
        "v_mfma_f32_16x16x32_bf16 v[120:123], a[248:251], a[192:195], 0\n"
        "ds_read_b128 a[248:251], v241 offset:13952\n"
        "v_cvt_pk_bf16_f32 v140, v116, v117\n"
        "v_mfma_f32_16x16x32_bf16 v[120:123], a[216:219], a[196:199], v[120:123]\n"
        "ds_read_b128 a[216:219], v240 offset:14080\n"
        "v_cvt_pk_bf16_f32 v141, v118, v119\n"
        "v_mfma_f32_16x16x32_bf16 v[120:123], a[220:223], a[200:203], v[120:123]\n"
        "ds_read_b128 a[220:223], v241 offset:14080\n"
        "v_mfma_f32_16x16x32_bf16 v[120:123], a[224:227], a[204:207], v[120:123]\n"
        "v_exp_f32 v108, v108\n"
        "v_mfma_f32_16x16x32_bf16 v[120:123], a[228:231], a[208:211], v[120:123]\n"
        "v_exp_f32 v109, v109\n"
        "v_mfma_f32_16x16x32_bf16 v[120:123], a[232:235], a[212:215], v[120:123]\n"
        "v_exp_f32 v110, v110\n"
        "v_mfma_f32_16x16x32_bf16 v[96:99], v[192:195], v[196:199], v[40:43]\n"
        "s_waitcnt lgkmcnt(6)\n"
        "v_exp_f32 v111, v111\n"
        "v_mfma_f32_16x16x32_bf16 v[96:99], v[208:211], v[200:203], v[96:99]\n"
        "s_waitcnt lgkmcnt(5)\n"
        "v_cvt_pk_bf16_f32 v76, v108, v109\n"
        "v_cvt_pk_bf16_f32 v77, v110, v111\n"
        "v_mfma_f32_16x16x32_bf16 v[112:115], a[72:75], a[236:239], 0\n"
        "s_waitcnt lgkmcnt(4)\n"
        "s_nop 0\n"
        "v_cvt_pk_bf16_f32 v148, v120, v121\n"
        "v_mfma_f32_16x16x32_bf16 v[112:115], a[76:79], a[240:243], v[112:115]\n"
        ".long 0xbec200ff\n"
        "45:\n"
        "s_branch 61b\n"
        "s_waitcnt lgkmcnt(3)\n"
        "v_cvt_pk_bf16_f32 v149, v122, v123\n"
        "v_mfma_f32_16x16x32_bf16 v[112:115], a[80:83], a[244:247], v[112:115]\n"
        "s_waitcnt lgkmcnt(2)\n"
        "v_exp_f32 v96, v96\n"
        "v_mfma_f32_16x16x32_bf16 v[112:115], a[84:87], a[248:251], v[112:115]\n"
        "s_waitcnt lgkmcnt(1)\n"
        "v_exp_f32 v97, v97\n"
        "v_mfma_f32_16x16x32_bf16 v[112:115], a[88:91], a[216:219], v[112:115]\n"
        "s_waitcnt lgkmcnt(0)\n"
        "v_exp_f32 v98, v98\n"
        "v_mfma_f32_16x16x32_bf16 v[112:115], a[92:95], a[220:223], v[112:115]\n"
        "v_exp_f32 v99, v99\n"
        "v_mfma_f32_16x16x32_bf16 v[116:119], a[116:119], a[220:223], 0\n"
        "v_cvt_pk_bf16_f32 v84, v96, v97\n"
        "v_cvt_pk_bf16_f32 v85, v98, v99\n"
        "v_mfma_f32_16x16x32_bf16 v[116:119], a[112:115], a[216:219], v[116:119]\n"
        "v_add_u32 v240, s31, v240\n"
        "v_add_u32 v241, s31, v241\n"
        "v_mfma_f32_16x16x32_bf16 v[116:119], a[108:111], a[248:251], v[116:119]\n"
        "v_cvt_pk_bf16_f32 v212, v112, v113\n"
        "v_cvt_pk_bf16_f32 v213, v114, v115\n"
        "v_mfma_f32_16x16x32_bf16 v[116:119], a[104:107], a[244:247], v[116:119]\n"
        "v_mfma_f32_16x16x32_bf16 v[116:119], a[100:103], a[240:243], v[116:119]\n"
        "v_mfma_f32_16x16x32_bf16 v[116:119], a[96:99], a[236:239], v[116:119]\n"
        "s_nop 2\n"
        "v_mfma_f32_16x16x32_bf16 v[120:123], a[120:123], a[236:239], 0\n"
        "s_nop 2\n"
        "v_mfma_f32_16x16x32_bf16 v[120:123], a[124:127], a[240:243], v[120:123]\n"
        "s_add_u32 s47, s47, 6291456\n"
        "v_cvt_pk_bf16_f32 v214, v116, v117\n"
        "v_cvt_pk_bf16_f32 v215, v118, v119\n"
        "v_mfma_f32_16x16x32_bf16 v[120:123], a[128:131], a[244:247], v[120:123]\n"
        "s_add_u32 s36, s36, 2097152\n"
        "v_mfma_f32_16x16x32_bf16 v[120:123], a[132:135], a[248:251], v[120:123]\n"
        "s_xor_b32 s48, s48, 8192\n"
        "v_mfma_f32_16x16x32_bf16 v[120:123], a[136:139], a[216:219], v[120:123]\n"
        "v_mfma_f32_16x16x32_bf16 v[120:123], a[140:143], a[220:223], v[120:123]\n"
        "s_nop 2\n"
        "v_mfma_f32_16x16x32_bf16 v[112:115], a[220:223], a[164:167], 0\n"
        "s_nop 2\n"
        "v_mfma_f32_16x16x32_bf16 v[112:115], a[216:219], a[160:163], v[112:115]\n"
        "v_cvt_pk_bf16_f32 v230, v120, v121\n"
        "v_cvt_pk_bf16_f32 v231, v122, v123\n"
        "v_mfma_f32_16x16x32_bf16 v[112:115], a[248:251], a[156:159], v[112:115]\n"
        "s_barrier\n"
        "v_mfma_f32_16x16x32_bf16 v[112:115], a[244:247], a[152:155], v[112:115]\n"
        "v_mfma_f32_16x16x32_bf16 v[112:115], a[240:243], a[148:151], v[112:115]\n"
        "v_mfma_f32_16x16x32_bf16 v[112:115], a[236:239], a[144:147], v[112:115]\n"
        "s_nop 2\n"
        "v_mfma_f32_16x16x32_bf16 v[100:103], v[212:215], v[156:159], v[12:15]\n"
        "s_nop 2\n"
        "v_mfma_f32_16x16x32_bf16 v[100:103], v[228:231], v[164:167], v[100:103]\n"
        "v_cvt_pk_bf16_f32 v134, v112, v113\n"
        "v_cvt_pk_bf16_f32 v135, v114, v115\n"
        "v_mfma_f32_16x16x32_bf16 v[116:119], a[236:239], a[168:171], 0\n"
        "v_mfma_f32_16x16x32_bf16 v[116:119], a[240:243], a[172:175], v[116:119]\n"
        "v_mfma_f32_16x16x32_bf16 v[116:119], a[244:247], a[176:179], v[116:119]\n"
        "v_mfma_f32_16x16x32_bf16 v[116:119], a[248:251], a[180:183], v[116:119]\n"
        "v_mfma_f32_16x16x32_bf16 v[116:119], a[216:219], a[184:187], v[116:119]\n"
        "s_nop 0\n"
        "v_exp_f32 v100, v100\n"
        "v_mfma_f32_16x16x32_bf16 v[116:119], a[220:223], a[188:191], v[116:119]\n"
        "v_exp_f32 v101, v101\n"
        "v_mfma_f32_16x16x32_bf16 v[104:107], v[212:215], v[176:179], v[28:31]\n"
        "v_exp_f32 v102, v102\n"
        "v_mfma_f32_16x16x32_bf16 v[104:107], v[228:231], v[184:187], v[104:107]\n"
        "v_exp_f32 v103, v103\n"
        "v_mfma_f32_16x16x32_bf16 v[120:123], a[220:223], a[212:215], 0\n"
        "v_cvt_pk_bf16_f32 v70, v100, v101\n"
        "v_cvt_pk_bf16_f32 v71, v102, v103\n"
        "v_mfma_f32_16x16x32_bf16 v[120:123], a[216:219], a[208:211], v[120:123]\n"
        "v_cvt_pk_bf16_f32 v142, v116, v117\n"
        "v_cvt_pk_bf16_f32 v143, v118, v119\n"
        "v_mfma_f32_16x16x32_bf16 v[120:123], a[248:251], a[204:207], v[120:123]\n"
        "v_mov_b64 v[96:97], 0\n"
        "v_mfma_f32_16x16x32_bf16 v[120:123], a[244:247], a[200:203], v[120:123]\n"
        "v_exp_f32 v104, v104\n"
        "v_mfma_f32_16x16x32_bf16 v[120:123], a[240:243], a[196:199], v[120:123]\n"
        "v_exp_f32 v105, v105\n"
        "v_mfma_f32_16x16x32_bf16 v[120:123], a[236:239], a[192:195], v[120:123]\n"
        "v_exp_f32 v106, v106\n"
        "v_mfma_f32_16x16x32_bf16 v[108:111], v[212:215], v[196:199], v[44:47]\n"
        "v_exp_f32 v107, v107\n"
        "v_mfma_f32_16x16x32_bf16 v[108:111], v[228:231], v[204:207], v[108:111]\n"
        "v_cvt_pk_bf16_f32 v78, v104, v105\n"
        "v_cvt_pk_bf16_f32 v79, v106, v107\n"
        "v_mfma_f32_16x16x32_bf16 v[112:115], a[0:3], a[236:239], 0\n"
        "s_nop 0\n"
        "v_cvt_pk_bf16_f32 v150, v120, v121\n"
        "v_cvt_pk_bf16_f32 v151, v122, v123\n"
        "v_mfma_f32_16x16x32_bf16 v[112:115], a[4:7], a[240:243], v[112:115]\n"
        "v_mfma_f32_16x16x32_bf16 v[112:115], a[8:11], a[244:247], v[112:115]\n"
        "v_exp_f32 v108, v108\n"
        "v_mfma_f32_16x16x32_bf16 v[112:115], a[12:15], a[248:251], v[112:115]\n"
        "v_exp_f32 v109, v109\n"
        "v_mfma_f32_16x16x32_bf16 v[112:115], a[16:19], a[216:219], v[112:115]\n"
        "v_exp_f32 v110, v110\n"
        "v_mfma_f32_16x16x32_bf16 v[112:115], a[20:23], a[220:223], v[112:115]\n"
        "v_exp_f32 v111, v111\n"
        "v_smfmac_f32_16x16x64_bf16 v[96:99], a[252:255], v[64:71], v237\n"
        "v_cvt_pk_bf16_f32 v86, v108, v109\n"
        "v_mfma_f32_16x16x32_bf16 v[100:103], v[132:135], v[68:71], 0\n"
        "v_mov_b64 v[124:125], 0\n"
        "v_cvt_pk_bf16_f32 v87, v110, v111\n"
        "v_mfma_f32_16x16x32_bf16 v[100:103], v[128:131], v[64:67], v[100:103]\n"
        "s_nop 0\n"
        "v_cvt_pk_bf16_f32 v216, v112, v113\n"
        "v_cvt_pk_bf16_f32 v217, v114, v115\n"
        "v_mfma_f32_16x16x32_bf16 v[104:107], v[136:139], v[64:67], 0\n"
        "ds_read_b128 v[222:225], v251 offset:4608\n"
        "v_add_f32 v97, v96, v97\n"
        "v_mfma_f32_16x16x32_bf16 v[104:107], v[140:143], v[68:71], v[104:107]\n"
        "v_rcp_f32 v96, v97\n"
        "v_mfma_f32_16x16x32_bf16 v[108:111], v[148:151], v[68:71], 0\n"
        "v_mul_f32 v100, v96, v100\n"
        "v_mul_f32 v101, v96, v101\n"
        "v_mfma_f32_16x16x32_bf16 v[108:111], v[144:147], v[64:67], v[108:111]\n"
        "v_mul_f32 v102, v96, v102\n"
        "v_mul_f32 v103, v96, v103\n"
        "v_mfma_f32_16x16x32_bf16 v[116:119], a[44:47], a[220:223], 0\n"
        "v_cvt_pk_bf16_f32 v100, v100, v101\n"
        "v_cvt_pk_bf16_f32 v101, v102, v103\n"
        "v_mfma_f32_16x16x32_bf16 v[116:119], a[40:43], a[216:219], v[116:119]\n"
        "v_mul_f32 v104, v96, v104\n"
        "v_mul_f32 v105, v96, v105\n"
        "v_mfma_f32_16x16x32_bf16 v[116:119], a[36:39], a[248:251], v[116:119]\n"
        "v_mul_f32 v106, v96, v106\n"
        "v_mul_f32 v107, v96, v107\n"
        "v_mfma_f32_16x16x32_bf16 v[116:119], a[32:35], a[244:247], v[116:119]\n"
        "v_cvt_pk_bf16_f32 v102, v104, v105\n"
        "v_cvt_pk_bf16_f32 v103, v106, v107\n"
        "v_mfma_f32_16x16x32_bf16 v[116:119], a[28:31], a[240:243], v[116:119]\n"
        "ds_write_b128 v245, v[100:103] offset:0\n"
        "v_mfma_f32_16x16x32_bf16 v[116:119], a[24:27], a[236:239], v[116:119]\n"
        "v_mul_f32 v108, v96, v108\n"
        "v_mul_f32 v109, v96, v109\n"
        "v_smfmac_f32_16x16x64_bf16 v[124:127], a[252:255], v[72:79], v237\n"
        "v_mul_f32 v110, v96, v110\n"
        "v_mfma_f32_16x16x32_bf16 v[64:67], v[132:135], v[76:79], 0\n"
        "v_mul_f32 v111, v96, v111\n"
        "v_cvt_pk_bf16_f32 v108, v108, v109\n"
        "v_mfma_f32_16x16x32_bf16 v[64:67], v[128:131], v[72:75], v[64:67]\n"
        "ds_read_b128 v[156:159], v247 offset:0\n"
        "v_cvt_pk_bf16_f32 v218, v116, v117\n"
        "v_mfma_f32_16x16x32_bf16 v[68:71], v[136:139], v[72:75], 0\n"
        "ds_read_b128 v[160:163], v247 offset:4096\n"
        "v_cvt_pk_bf16_f32 v219, v118, v119\n"
        "v_mfma_f32_16x16x32_bf16 v[68:71], v[140:143], v[76:79], v[68:71]\n"
        "ds_read_b128 v[164:167], v247 offset:4352\n"
        "v_cvt_pk_bf16_f32 v109, v110, v111\n"
        "v_mfma_f32_16x16x32_bf16 v[120:123], v[148:151], v[76:79], 0\n"
        "ds_write_b64 v246, v[108:109] offset:0\n"
        "v_mfma_f32_16x16x32_bf16 v[120:123], v[144:147], v[72:75], v[120:123]\n"
        "v_max_u32 v236, v96, v97\n"
        "v_mov_b64 v[116:117], 0\n"
        "v_mfma_f32_16x16x32_bf16 v[112:115], a[48:51], a[236:239], 0\n"
        "v_add_f32 v125, v124, v125\n"
        "v_mfma_f32_16x16x32_bf16 v[112:115], a[52:55], a[240:243], v[112:115]\n"
        "v_rcp_f32 v124, v125\n"
        "v_mfma_f32_16x16x32_bf16 v[112:115], a[56:59], a[244:247], v[112:115]\n"
        "v_mul_f32 v64, v124, v64\n"
        "v_mul_f32 v65, v124, v65\n"
        "v_mfma_f32_16x16x32_bf16 v[112:115], a[60:63], a[248:251], v[112:115]\n"
        "v_mul_f32 v66, v124, v66\n"
        "v_mul_f32 v67, v124, v67\n"
        "v_mfma_f32_16x16x32_bf16 v[112:115], a[64:67], a[216:219], v[112:115]\n"
        "v_cvt_pk_bf16_f32 v64, v64, v65\n"
        "v_cvt_pk_bf16_f32 v65, v66, v67\n"
        "v_mfma_f32_16x16x32_bf16 v[112:115], a[68:71], a[220:223], v[112:115]\n"
        "ds_read_b128 a[216:219], v248 offset:0\n"
        "v_mul_f32 v68, v124, v68\n"
        "v_smfmac_f32_16x16x64_bf16 v[116:119], a[252:255], v[80:87], v237\n"
        "ds_read_b128 a[220:223], v248 offset:3072\n"
        "v_mfma_f32_16x16x32_bf16 v[72:75], v[132:135], v[84:87], 0\n"
        "v_mul_f32 v69, v124, v69\n"
        "v_mul_f32 v70, v124, v70\n"
        "v_mfma_f32_16x16x32_bf16 v[72:75], v[128:131], v[80:83], v[72:75]\n"
        "v_cvt_pk_bf16_f32 v220, v112, v113\n"
        "v_cvt_pk_bf16_f32 v221, v114, v115\n"
        "v_mfma_f32_16x16x32_bf16 v[76:79], v[136:139], v[80:83], 0\n"
        "ds_read_b128 a[224:227], v248 offset:32\n"
        "v_cvt_pk_bf16_f32 v226, v112, v113\n"
        "v_mfma_f32_16x16x32_bf16 v[76:79], v[140:143], v[84:87], v[76:79]\n"
        "ds_read_b128 a[228:231], v248 offset:3104\n"
        "v_cvt_pk_bf16_f32 v227, v114, v115\n"
        "v_mfma_f32_16x16x32_bf16 v[96:99], v[148:151], v[84:87], 0\n"
        "v_mul_f32 v71, v124, v71\n"
        "v_cvt_pk_bf16_f32 v66, v68, v69\n"
        "v_mfma_f32_16x16x32_bf16 v[96:99], v[144:147], v[80:83], v[96:99]\n"
        "v_cvt_pk_bf16_f32 v67, v70, v71\n"
        "v_mul_f32 v120, v124, v120\n"
        "v_mfma_f32_16x16x32_bf16 v[100:103], v[152:155], v[216:219], v[48:51]\n"
        "s_waitcnt lgkmcnt(9)\n"
        "ds_read_b128 v[152:155], v247 offset:256\n"
        "v_mul_f32 v121, v124, v121\n"
        "v_mfma_f32_16x16x32_bf16 v[100:103], v[168:171], v[220:223], v[100:103]\n"
        "ds_write_b128 v245, v[64:67] offset:256\n"
        "v_mfma_f32_16x16x32_bf16 v[104:107], v[172:175], v[216:219], v[52:55]\n"
        "v_mul_f32 v122, v124, v122\n"
        "v_mul_f32 v123, v124, v123\n"
        "v_mfma_f32_16x16x32_bf16 v[104:107], v[188:191], v[224:227], v[104:107]\n"
        "s_waitcnt lgkmcnt(5)\n"
        "v_cvt_pk_bf16_f32 v120, v120, v121\n"
        "v_cvt_pk_bf16_f32 v121, v122, v123\n"
        "v_mfma_f32_16x16x32_bf16 v[176:179], v[156:159], a[216:219], 0\n"
        "s_waitcnt lgkmcnt(4)\n"
        "ds_write_b64 v246, v[120:121] offset:256\n"
        "v_mfma_f32_16x16x32_bf16 v[176:179], v[160:163], a[220:223], v[176:179]\n"
        "ds_read_b128 v[222:225], v252 offset:4608\n"
        "v_max3_u32 v236, v236, v124, v125\n"
        "v_mfma_f32_16x16x32_bf16 v[196:199], v[164:167], a[220:223], 0\n"
        "s_waitcnt lgkmcnt(3)\n"
        "ds_read_b128 a[232:235], v254 offset:0\n"
        "v_add_f32 v117, v116, v117\n"
        "v_mfma_f32_16x16x32_bf16 v[196:199], v[152:155], a[216:219], v[196:199]\n"
        "ds_read_b128 a[220:223], v254 offset:3072\n"
        "v_mfma_f32_16x16x32_bf16 v[200:203], v[152:155], a[224:227], 0\n"
        "ds_read_b128 a[216:219], v254 offset:32\n"
        "v_mfma_f32_16x16x32_bf16 v[200:203], v[164:167], a[228:231], v[200:203]\n"
        "v_rcp_f32 v116, v117\n"
        "v_mfma_f32_16x16x32_bf16 v[180:183], v[160:163], a[228:231], 0\n"
        "ds_read_b128 a[228:231], v254 offset:3104\n"
        "v_mul_f32 v72, v116, v72\n"
        "v_mfma_f32_16x16x32_bf16 v[180:183], v[156:159], a[224:227], v[180:183]\n"
        "ds_read_b128 a[236:239], v247 offset:8192\n"
        "v_mul_f32 v73, v116, v73\n"
        "v_mfma_f32_16x16x32_bf16 v[108:111], v[192:195], v[216:219], v[56:59]\n"
        "s_waitcnt lgkmcnt(5)\n"
        "ds_read_b128 a[224:227], v248 offset:16\n"
        "v_mul_f32 v74, v116, v74\n"
        "v_mfma_f32_16x16x32_bf16 v[108:111], v[208:211], v[220:223], v[108:111]\n"
        "ds_read_b128 a[240:243], v247 offset:12288\n"
        "v_mul_f32 v75, v116, v75\n"
        "v_mfma_f32_16x16x32_bf16 v[112:115], v[212:215], v[216:219], v[60:63]\n"
        "ds_read_b128 a[244:247], v247 offset:12544\n"
        "v_cvt_pk_bf16_f32 v72, v72, v73\n"
        "v_mfma_f32_16x16x32_bf16 v[112:115], v[228:231], v[224:227], v[112:115]\n"
        "s_waitcnt lgkmcnt(7)\n"
        "ds_read_b128 a[248:251], v247 offset:8448\n"
        "v_cvt_pk_bf16_f32 v73, v74, v75\n"
        "v_mfma_f32_16x16x32_bf16 v[184:187], v[156:159], a[232:235], 0\n"
        "s_waitcnt lgkmcnt(7)\n"
        "v_exp_f32 v100, v100\n"
        "v_mfma_f32_16x16x32_bf16 v[184:187], v[160:163], a[220:223], v[184:187]\n"
        "v_exp_f32 v101, v101\n"
        "v_mfma_f32_16x16x32_bf16 v[204:207], v[164:167], a[220:223], 0\n"
        "ds_read_b128 a[220:223], v248 offset:3088\n"
        "v_mul_f32 v76, v116, v76\n"
        "v_mfma_f32_16x16x32_bf16 v[204:207], v[152:155], a[232:235], v[204:207]\n"
        "s_waitcnt lgkmcnt(7)\n"
        "ds_read_b128 a[232:235], v254 offset:16\n"
        "v_mul_f32 v77, v116, v77\n"
        "v_mfma_f32_16x16x32_bf16 v[192:195], v[152:155], a[216:219], 0\n"
        "s_waitcnt lgkmcnt(7)\n"
        "v_mul_f32 v78, v116, v78\n"
        "v_mul_f32 v79, v116, v79\n"
        "v_mfma_f32_16x16x32_bf16 v[192:195], v[164:167], a[228:231], v[192:195]\n"
        "v_exp_f32 v102, v102\n"
        "v_mfma_f32_16x16x32_bf16 v[172:175], v[160:163], a[228:231], 0\n"
        "ds_read_b128 a[228:231], v254 offset:3088\n"
        "v_cvt_pk_bf16_f32 v88, v100, v101\n"
        "v_mfma_f32_16x16x32_bf16 v[172:175], v[156:159], a[216:219], v[172:175]\n"
        "s_waitcnt lgkmcnt(6)\n"
        "ds_read_b128 a[216:219], v248 offset:6144\n"
        "v_cvt_pk_bf16_f32 v74, v76, v77\n"
        "v_mfma_f32_16x16x32_bf16 v[212:215], v[156:159], a[224:227], 0\n"
        "s_waitcnt lgkmcnt(3)\n"
        "v_exp_f32 v104, v104\n"
        "v_mfma_f32_16x16x32_bf16 v[212:215], v[160:163], a[220:223], v[212:215]\n"
        "v_exp_f32 v103, v103\n"
        "v_mfma_f32_16x16x32_bf16 v[216:219], v[164:167], a[220:223], 0\n"
        "ds_read_b128 a[220:223], v248 offset:9216\n"
        "v_cvt_pk_bf16_f32 v75, v78, v79\n"
        "v_mfma_f32_16x16x32_bf16 v[216:219], v[152:155], a[224:227], v[216:219]\n"
        "s_waitcnt lgkmcnt(3)\n"
        "ds_write_b128 v245, v[72:75] offset:512\n"
        "v_mfma_f32_16x16x32_bf16 v[224:227], v[152:155], a[232:235], 0\n"
        "s_waitcnt lgkmcnt(3)\n"
        "v_exp_f32 v105, v105\n"
        "v_mfma_f32_16x16x32_bf16 v[224:227], v[164:167], a[228:231], v[224:227]\n"
        "v_cvt_pk_bf16_f32 v89, v102, v103\n"
        "v_cvt_pk_bf16_f32 v90, v104, v105\n"
        "v_mfma_f32_16x16x32_bf16 v[220:223], v[160:163], a[228:231], 0\n"
        "ds_read_b128 a[224:227], v248 offset:6176\n"
        "ds_read_b128 a[228:231], v248 offset:9248\n"
        "v_mfma_f32_16x16x32_bf16 v[220:223], v[156:159], a[232:235], v[220:223]\n"
        "s_waitcnt lgkmcnt(4)\n"
        "ds_read_b128 a[232:235], v254 offset:6144\n"
        "v_mul_f32 v96, v116, v96\n"
        "v_mfma_f32_16x16x32_bf16 v[176:179], a[236:239], a[216:219], v[176:179]\n"
        "s_waitcnt lgkmcnt(4)\n"
        "v_exp_f32 v106, v106\n"
        "v_mfma_f32_16x16x32_bf16 v[176:179], a[240:243], a[220:223], v[176:179]\n"
        "v_mul_f32 v97, v116, v97\n"
        "v_mul_f32 v98, v116, v98\n"
        "v_mfma_f32_16x16x32_bf16 v[196:199], a[244:247], a[220:223], v[196:199]\n"
        "ds_read_b128 a[220:223], v254 offset:9216\n"
        "v_mul_f32 v99, v116, v99\n"
        "v_mfma_f32_16x16x32_bf16 v[196:199], a[248:251], a[216:219], v[196:199]\n"
        "s_waitcnt lgkmcnt(3)\n"
        "ds_read_b128 a[216:219], v254 offset:6176\n"
        "v_cvt_pk_bf16_f32 v96, v96, v97\n"
        "v_mfma_f32_16x16x32_bf16 v[200:203], a[248:251], a[224:227], v[200:203]\n"
        "s_waitcnt lgkmcnt(3)\n"
        "ds_read_b128 v[156:159], v247 offset:16384\n"
        "v_cvt_pk_bf16_f32 v97, v98, v99\n"
        "v_mfma_f32_16x16x32_bf16 v[200:203], a[244:247], a[228:231], v[200:203]\n"
        "ds_write_b64 v246, v[96:97] offset:512\n"
        "v_mfma_f32_16x16x32_bf16 v[180:183], a[240:243], a[228:231], v[180:183]\n"
        "ds_read_b128 a[228:231], v254 offset:9248\n"
        "v_max3_u32 v236, v236, v116, v117\n"
        "v_mfma_f32_16x16x32_bf16 v[180:183], a[236:239], a[224:227], v[180:183]\n"
        "s_waitcnt lgkmcnt(5)\n"
        "ds_read_b128 a[224:227], v248 offset:6160\n"
        "v_mfma_f32_16x16x32_bf16 v[184:187], a[236:239], a[232:235], v[184:187]\n"
        "s_waitcnt lgkmcnt(5)\n"
        "ds_read_b128 v[160:163], v247 offset:20480\n"
        "v_mfma_f32_16x16x32_bf16 v[184:187], a[240:243], a[220:223], v[184:187]\n"
        "ds_read_b128 v[164:167], v247 offset:20736\n"
        "v_mfma_f32_16x16x32_bf16 v[204:207], a[244:247], a[220:223], v[204:207]\n"
        "ds_read_b128 a[220:223], v248 offset:9232\n"
        "v_mfma_f32_16x16x32_bf16 v[204:207], a[248:251], a[232:235], v[204:207]\n"
        "s_waitcnt lgkmcnt(7)\n"
        "ds_read_b128 a[232:235], v254 offset:6160\n"
        "v_mfma_f32_16x16x32_bf16 v[192:195], a[248:251], a[216:219], v[192:195]\n"
        "s_waitcnt lgkmcnt(5)\n"
        "ds_read_b128 v[152:155], v247 offset:16640\n"
        "v_mfma_f32_16x16x32_bf16 v[192:195], a[244:247], a[228:231], v[192:195]\n"
        "v_exp_f32 v107, v107\n"
        "v_mfma_f32_16x16x32_bf16 v[172:175], a[240:243], a[228:231], v[172:175]\n"
        "ds_read_b128 a[228:231], v254 offset:9232\n"
        "v_cvt_pk_bf16_f32 v91, v106, v107\n"
        "v_mfma_f32_16x16x32_bf16 v[172:175], a[236:239], a[216:219], v[172:175]\n"
        "s_waitcnt lgkmcnt(6)\n"
        "ds_read_b128 a[216:219], v248 offset:12288\n"
        "v_mfma_f32_16x16x32_bf16 v[212:215], a[236:239], a[224:227], v[212:215]\n"
        "s_waitcnt lgkmcnt(4)\n"
        "v_exp_f32 v108, v108\n"
        "v_mfma_f32_16x16x32_bf16 v[212:215], a[240:243], a[220:223], v[212:215]\n"
        "v_exp_f32 v109, v109\n"
        "v_mfma_f32_16x16x32_bf16 v[216:219], a[244:247], a[220:223], v[216:219]\n"
        "ds_read_b128 a[220:223], v248 offset:15360\n"
        "v_cvt_pk_bf16_f32 v92, v108, v109\n"
        "v_mfma_f32_16x16x32_bf16 v[216:219], a[248:251], a[224:227], v[216:219]\n"
        "s_waitcnt lgkmcnt(4)\n"
        "ds_read_b128 a[224:227], v248 offset:12320\n"
        "v_mfma_f32_16x16x32_bf16 v[224:227], a[248:251], a[232:235], v[224:227]\n"
        "s_waitcnt lgkmcnt(3)\n"
        "v_exp_f32 v110, v110\n"
        "v_mfma_f32_16x16x32_bf16 v[224:227], a[244:247], a[228:231], v[224:227]\n"
        "v_exp_f32 v112, v112\n"
        "v_mfma_f32_16x16x32_bf16 v[220:223], a[240:243], a[228:231], v[220:223]\n"
        "ds_read_b128 a[228:231], v248 offset:15392\n"
        "v_mfma_f32_16x16x32_bf16 v[220:223], a[236:239], a[232:235], v[220:223]\n"
        "s_waitcnt lgkmcnt(3)\n"
        "ds_read_b128 a[232:235], v254 offset:12288\n"
        "v_mfma_f32_16x16x32_bf16 v[176:179], v[156:159], a[216:219], v[176:179]\n"
        "s_waitcnt lgkmcnt(3)\n"
        "v_exp_f32 v111, v111\n"
        "v_mfma_f32_16x16x32_bf16 v[176:179], v[160:163], a[220:223], v[176:179]\n"
        "v_exp_f32 v113, v113\n"
        "v_mfma_f32_16x16x32_bf16 v[196:199], v[164:167], a[220:223], v[196:199]\n"
        "ds_read_b128 a[220:223], v254 offset:15360\n"
        "v_cvt_pk_bf16_f32 v93, v110, v111\n"
        "v_mfma_f32_16x16x32_bf16 v[196:199], v[152:155], a[216:219], v[196:199]\n"
        "s_waitcnt lgkmcnt(3)\n"
        "ds_read_b128 a[216:219], v254 offset:12320\n"
        "v_cvt_pk_bf16_f32 v94, v112, v113\n"
        "v_mfma_f32_16x16x32_bf16 v[200:203], v[152:155], a[224:227], v[200:203]\n"
        "s_waitcnt lgkmcnt(3)\n"
        "v_exp_f32 v114, v114\n"
        "v_mfma_f32_16x16x32_bf16 v[200:203], v[164:167], a[228:231], v[200:203]\n"
        "v_exp_f32 v115, v115\n"
        "v_mfma_f32_16x16x32_bf16 v[180:183], v[160:163], a[228:231], v[180:183]\n"
        "s_waitcnt vmcnt(0)\n"
        "s_barrier\n"
        "ds_read_b128 a[228:231], v254 offset:15392\n"
        "v_cvt_pk_bf16_f32 v95, v114, v115\n"
        "v_mfma_f32_16x16x32_bf16 v[180:183], v[156:159], a[224:227], v[180:183]\n"
        "s_waitcnt lgkmcnt(3)\n"
        "ds_read_b128 a[224:227], v248 offset:12304\n"
        "v_mfma_f32_16x16x32_bf16 v[184:187], v[156:159], a[232:235], v[184:187]\n"
        "s_waitcnt lgkmcnt(3)\n"
        "v_mfma_f32_16x16x32_bf16 v[184:187], v[160:163], a[220:223], v[184:187]\n"
        "v_mfma_f32_16x16x32_bf16 v[204:207], v[164:167], a[220:223], v[204:207]\n"
        "ds_read_b128 a[220:223], v248 offset:15376\n"
        "v_mfma_f32_16x16x32_bf16 v[204:207], v[152:155], a[232:235], v[204:207]\n"
        "s_waitcnt lgkmcnt(3)\n"
        "ds_read_b128 a[232:235], v254 offset:12304\n"
        "v_mfma_f32_16x16x32_bf16 v[192:195], v[152:155], a[216:219], v[192:195]\n"
        "s_waitcnt lgkmcnt(3)\n"
        "ds_read_b128 a[236:239], v240 offset:0\n"
        "v_mfma_f32_16x16x32_bf16 v[192:195], v[164:167], a[228:231], v[192:195]\n"
        "ds_read_b128 a[240:243], v241 offset:0\n"
        "v_mfma_f32_16x16x32_bf16 v[172:175], v[160:163], a[228:231], v[172:175]\n"
        "ds_read_b128 a[228:231], v254 offset:15376\n"
        "v_mfma_f32_16x16x32_bf16 v[172:175], v[156:159], a[216:219], v[172:175]\n"
        "s_waitcnt lgkmcnt(5)\n"
        "ds_read_b128 a[244:247], v240 offset:128\n"
        "v_mfma_f32_16x16x32_bf16 v[212:215], v[156:159], a[224:227], v[212:215]\n"
        "s_waitcnt lgkmcnt(5)\n"
        "ds_read_b128 a[248:251], v241 offset:128\n"
        "v_mfma_f32_16x16x32_bf16 v[212:215], v[160:163], a[220:223], v[212:215]\n"
        "ds_read_b128 a[216:219], v240 offset:256\n"
        "v_mfma_f32_16x16x32_bf16 v[216:219], v[164:167], a[220:223], v[216:219]\n"
        "ds_read_b128 a[220:223], v241 offset:256\n"
        "v_mfma_f32_16x16x32_bf16 v[216:219], v[152:155], a[224:227], v[216:219]\n"
        "s_waitcnt lgkmcnt(7)\n"
        "v_mfma_f32_16x16x32_bf16 v[224:227], v[152:155], a[232:235], v[224:227]\n"
        "s_waitcnt lgkmcnt(4)\n"
        "v_mfma_f32_16x16x32_bf16 v[224:227], v[164:167], a[228:231], v[224:227]\n"
        "s_sub_u32 s27, s27, 1\n"
        "v_mfma_f32_16x16x32_bf16 v[220:223], v[160:163], a[228:231], v[220:223]\n"
        "s_cmp_lg_u32 s27, 0\n"
        "v_mfma_f32_16x16x32_bf16 v[220:223], v[156:159], a[232:235], v[220:223]\n"
        "s_cbranch_scc1 10b\n"
        "s_branch 19f\n"
        ".p2align 12\n"
        "51:\n"
        "s_branch 61b\n"
        "s_nop 0\n"
        "s_nop 0\n"
        "s_nop 0\n"
        "12:\n"
        "10:\n"
        "v_mfma_f32_16x16x32_bf16 v[112:115], a[72:75], a[236:239], 0\n"
        "v_mov_b64 v[124:125], 0\n"
        "v_cvt_pk_bf16_f32 v232, v176, v180\n"
        "v_mfma_f32_16x16x32_bf16 v[112:115], a[76:79], a[240:243], v[112:115]\n"
        "s_waitcnt lgkmcnt(3)\n"
        "s_add_u32 s41, s30, s40\n"
        "v_cvt_pk_bf16_f32 v233, v184, v172\n"
        "v_cvt_pk_bf16_f32 v234, v177, v181\n"
        "v_mfma_f32_16x16x32_bf16 v[112:115], a[80:83], a[244:247], v[112:115]\n"
        "s_waitcnt lgkmcnt(2)\n"
        "buffer_store_dwordx2 v[232:233], v250, s[16:19], s38 offen offset:0 sc1\n"
        "v_cvt_pk_bf16_f32 v235, v185, v173\n"
        "v_mfma_f32_16x16x32_bf16 v[112:115], a[84:87], a[248:251], v[112:115]\n"
        "s_waitcnt lgkmcnt(1)\n"
        "v_cvt_pk_bf16_f32 v232, v178, v182\n"
        "v_cvt_pk_bf16_f32 v233, v186, v174\n"
        "v_mfma_f32_16x16x32_bf16 v[112:115], a[88:91], a[216:219], v[112:115]\n"
        "s_waitcnt lgkmcnt(0)\n"
        "v_mfma_f32_16x16x32_bf16 v[112:115], a[92:95], a[220:223], v[112:115]\n"
        "s_nop 2\n"
        "v_smfmac_f32_16x16x64_bf16 v[124:127], a[252:255], v[88:95], v237\n"
        "s_nop 1\n"
        "v_mfma_f32_16x16x32_bf16 v[80:83], v[132:135], v[92:95], 0\n"
        "buffer_store_dwordx2 v[234:235], v250, s[16:19], s38 offen offset:384 sc1\n"
        "v_cvt_pk_bf16_f32 v152, v112, v113\n"
        "v_mfma_f32_16x16x32_bf16 v[80:83], v[128:131], v[88:91], v[80:83]\n"
        "v_cvt_pk_bf16_f32 v153, v114, v115\n"
        "v_cvt_pk_bf16_f32 v234, v179, v183\n"
        "v_mfma_f32_16x16x32_bf16 v[84:87], v[136:139], v[88:91], 0\n"
        "v_cvt_pk_bf16_f32 v235, v187, v175\n"
        "v_add_f32 v125, v124, v125\n"
        "v_mfma_f32_16x16x32_bf16 v[84:87], v[140:143], v[92:95], v[84:87]\n"
        "v_rcp_f32 v124, v125\n"
        "v_mfma_f32_16x16x32_bf16 v[96:99], v[148:151], v[92:95], 0\n"
        "buffer_store_dwordx2 v[232:233], v250, s[16:19], s38 offen offset:768 sc1\n"
        "v_mul_f32 v80, v124, v80\n"
        "v_mfma_f32_16x16x32_bf16 v[96:99], v[144:147], v[88:91], v[96:99]\n"
        "s_add_u32 s35, s47, 786432\n"
        "v_mul_f32 v81, v124, v81\n"
        "v_mul_f32 v82, v124, v82\n"
        "v_mfma_f32_16x16x32_bf16 v[116:119], a[116:119], a[220:223], 0\n"
        "s_sub_u32 s46, s35, 100663296\n"
        "v_mul_f32 v83, v124, v83\n"
        "v_cvt_pk_bf16_f32 v80, v80, v81\n"
        "v_mfma_f32_16x16x32_bf16 v[116:119], a[112:115], a[216:219], v[116:119]\n"
        "s_cmp_lt_u32 s46, 786432\n"
        "v_cvt_pk_bf16_f32 v81, v82, v83\n"
        "v_cvt_pk_bf16_f32 v232, v196, v200\n"
        "v_mfma_f32_16x16x32_bf16 v[116:119], a[108:111], a[248:251], v[116:119]\n"
        "buffer_store_dwordx2 v[234:235], v250, s[16:19], s38 offen offset:1152 sc1\n"
        "v_cvt_pk_bf16_f32 v233, v204, v192\n"
        "v_mfma_f32_16x16x32_bf16 v[116:119], a[104:107], a[244:247], v[116:119]\n"
        "s_cselect_b32 s35, s46, s35\n"
        "v_mul_f32 v84, v124, v84\n"
        "v_mul_f32 v85, v124, v85\n"
        "v_mfma_f32_16x16x32_bf16 v[116:119], a[100:103], a[240:243], v[116:119]\n"
        "v_mul_f32 v86, v124, v86\n"
        "v_mul_f32 v87, v124, v87\n"
        "v_mfma_f32_16x16x32_bf16 v[116:119], a[96:99], a[236:239], v[116:119]\n"
        "v_cvt_pk_bf16_f32 v82, v84, v85\n"
        "v_cvt_pk_bf16_f32 v83, v86, v87\n"
        "v_mfma_f32_16x16x32_bf16 v[120:123], a[120:123], a[236:239], 0\n"
        "ds_write_b128 v245, v[80:83] offset:768\n"
        "v_mfma_f32_16x16x32_bf16 v[120:123], a[124:127], a[240:243], v[120:123]\n"
        "buffer_store_dwordx2 v[232:233], v250, s[16:19], s39 offen offset:0 sc1\n"
        "v_mul_f32 v96, v124, v96\n"
        "v_mfma_f32_16x16x32_bf16 v[120:123], a[128:131], a[244:247], v[120:123]\n"
        "v_mul_f32 v97, v124, v97\n"
        "v_mul_f32 v98, v124, v98\n"
        "v_mfma_f32_16x16x32_bf16 v[120:123], a[132:135], a[248:251], v[120:123]\n"
        "v_mul_f32 v99, v124, v99\n"
        "v_cvt_pk_bf16_f32 v234, v197, v201\n"
        "v_mfma_f32_16x16x32_bf16 v[120:123], a[136:139], a[216:219], v[120:123]\n"
        "v_cvt_pk_bf16_f32 v235, v205, v193\n"
        "v_cvt_pk_bf16_f32 v154, v116, v117\n"
        "v_mfma_f32_16x16x32_bf16 v[120:123], a[140:143], a[220:223], v[120:123]\n"
        "buffer_store_dwordx2 v[234:235], v250, s[16:19], s39 offen offset:384 sc1\n"
        "v_cvt_pk_bf16_f32 v155, v118, v119\n"
        "v_mfma_f32_16x16x32_bf16 v[112:115], a[20:23], a[220:223], 0\n"
        "v_cvt_pk_bf16_f32 v96, v96, v97\n"
        "v_cvt_pk_bf16_f32 v97, v98, v99\n"
        "v_mfma_f32_16x16x32_bf16 v[112:115], a[16:19], a[216:219], v[112:115]\n"
        "ds_write_b64 v246, v[96:97] offset:768\n"
        "v_mfma_f32_16x16x32_bf16 v[112:115], a[12:15], a[248:251], v[112:115]\n"
        "v_max3_u32 v236, v236, v124, v125\n"
        "v_cvt_pk_bf16_f32 v232, v198, v202\n"
        "v_mfma_f32_16x16x32_bf16 v[112:115], a[8:11], a[244:247], v[112:115]\n"
        "v_cvt_pk_bf16_f32 v233, v206, v194\n"
        "v_cmp_lt_u32 vcc, s24, v236\n"
        "v_mfma_f32_16x16x32_bf16 v[112:115], a[4:7], a[240:243], v[112:115]\n"
        "buffer_store_dwordx2 v[232:233], v250, s[16:19], s39 offen offset:768 sc1\n"
        "v_cvt_pk_bf16_f32 v168, v120, v121\n"
        "v_mfma_f32_16x16x32_bf16 v[112:115], a[0:3], a[236:239], v[112:115]\n"
        "s_cmp_lg_u64 vcc, 0\n"
        "s_addc_u32 s25, s25, s25\n"
        "v_cvt_pk_bf16_f32 v169, v122, v123\n"
        "v_mfma_f32_16x16x32_bf16 v[116:119], a[24:27], a[236:239], 0\n"
        "v_cvt_pk_bf16_f32 v234, v199, v203\n"
        "v_cvt_pk_bf16_f32 v235, v207, v195\n"
        "v_mfma_f32_16x16x32_bf16 v[116:119], a[28:31], a[240:243], v[116:119]\n"
        "v_cvt_pk_bf16_f32 v232, v212, v220\n"
        "v_cvt_pk_bf16_f32 v156, v112, v113\n"
        "v_mfma_f32_16x16x32_bf16 v[116:119], a[32:35], a[244:247], v[116:119]\n"
        "buffer_store_dwordx2 v[234:235], v250, s[16:19], s39 offen offset:1152 sc1\n"
        "v_cvt_pk_bf16_f32 v157, v114, v115\n"
        "v_mfma_f32_16x16x32_bf16 v[116:119], a[36:39], a[248:251], v[116:119]\n"
        "v_cvt_pk_bf16_f32 v234, v213, v221\n"
        "v_xor_b32 v251, 0x2000, v251\n"
        "v_mfma_f32_16x16x32_bf16 v[116:119], a[40:43], a[216:219], v[116:119]\n"
        "v_xor_b32 v252, 0x2000, v252\n"
        "v_mfma_f32_16x16x32_bf16 v[116:119], a[44:47], a[220:223], v[116:119]\n"
        "s_nop 2\n"
        "v_mfma_f32_16x16x32_bf16 v[120:123], a[68:71], a[220:223], 0\n"
        "s_nop 2\n"
        "v_mfma_f32_16x16x32_bf16 v[120:123], a[64:67], a[216:219], v[120:123]\n"
        "buffer_store_dword v232, v238, s[16:19], s38 offen offset:0 sc1\n"
        "v_mfma_f32_16x16x32_bf16 v[120:123], a[60:63], a[248:251], v[120:123]\n"
        "ds_read_b128 a[224:227], v240 offset:1536\n"
        "v_cvt_pk_bf16_f32 v158, v116, v117\n"
        "v_mfma_f32_16x16x32_bf16 v[120:123], a[56:59], a[244:247], v[120:123]\n"
        "ds_read_b128 a[228:231], v241 offset:1536\n"
        "v_cvt_pk_bf16_f32 v159, v118, v119\n"
        "v_mfma_f32_16x16x32_bf16 v[120:123], a[52:55], a[240:243], v[120:123]\n"
        "ds_read_b128 a[232:235], v240 offset:1664\n"
        "v_cvt_pk_bf16_f32 v232, v214, v222\n"
        "v_mfma_f32_16x16x32_bf16 v[120:123], a[48:51], a[236:239], v[120:123]\n"
        "buffer_store_dword v234, v238, s[16:19], s38 offen offset:384 sc1\n"
        "v_mfma_f32_16x16x32_bf16 v[112:115], a[236:239], a[144:147], 0\n"
        "v_add_u32 v247, s45, v247\n"
        "v_cvt_pk_bf16_f32 v234, v215, v223\n"
        "v_mfma_f32_16x16x32_bf16 v[112:115], a[240:243], a[148:151], v[112:115]\n"
        "s_nop 2\n"
        "v_mfma_f32_16x16x32_bf16 v[112:115], a[244:247], a[152:155], v[112:115]\n"
        "v_cvt_pk_bf16_f32 v160, v120, v121\n"
        "v_cvt_pk_bf16_f32 v161, v122, v123\n"
        "v_mfma_f32_16x16x32_bf16 v[112:115], a[248:251], a[156:159], v[112:115]\n"
        "v_cvt_pk_bf16_f32 v166, v120, v121\n"
        "v_cvt_pk_bf16_f32 v167, v122, v123\n"
        "v_mfma_f32_16x16x32_bf16 v[112:115], a[216:219], a[160:163], v[112:115]\n"
        "buffer_store_dword v232, v238, s[16:19], s38 offen offset:768 sc1\n"
        "v_mfma_f32_16x16x32_bf16 v[112:115], a[220:223], a[164:167], v[112:115]\n"
        "s_nop 2\n"
        "v_mfma_f32_16x16x32_bf16 v[116:119], a[220:223], a[188:191], 0\n"
        "v_cvt_pk_bf16_f32 v232, v216, v224\n"
        "s_nop 0\n"
        "v_mfma_f32_16x16x32_bf16 v[116:119], a[216:219], a[184:187], v[116:119]\n"
        "s_nop 0\n"
        "v_cvt_pk_bf16_f32 v128, v112, v113\n"
        "v_cvt_pk_bf16_f32 v129, v114, v115\n"
        "v_mfma_f32_16x16x32_bf16 v[116:119], a[248:251], a[180:183], v[116:119]\n"
        "buffer_store_dword v234, v238, s[16:19], s38 offen offset:1152 sc1\n"
        "v_mfma_f32_16x16x32_bf16 v[116:119], a[244:247], a[176:179], v[116:119]\n"
        "s_nop 0\n"
        "v_cvt_pk_bf16_f32 v234, v217, v225\n"
        "v_mfma_f32_16x16x32_bf16 v[116:119], a[240:243], a[172:175], v[116:119]\n"
        "v_mfma_f32_16x16x32_bf16 v[116:119], a[236:239], a[168:171], v[116:119]\n"
        "s_nop 2\n"
        "v_mfma_f32_16x16x32_bf16 v[120:123], a[236:239], a[192:195], 0\n"
        "ds_read_b128 a[236:239], v241 offset:1664\n"
        "s_nop 0\n"
        "v_mfma_f32_16x16x32_bf16 v[120:123], a[240:243], a[196:199], v[120:123]\n"
        "buffer_store_dword v232, v238, s[16:19], s39 offen offset:0 sc1\n"
        "v_mfma_f32_16x16x32_bf16 v[120:123], a[244:247], a[200:203], v[120:123]\n"
        "ds_read_b128 a[240:243], v240 offset:1792\n"
        "ds_read_b128 a[244:247], v241 offset:1792\n"
        "v_mfma_f32_16x16x32_bf16 v[120:123], a[248:251], a[204:207], v[120:123]\n"
        "v_cvt_pk_bf16_f32 v136, v116, v117\n"
        "v_cvt_pk_bf16_f32 v137, v118, v119\n"
        "v_mfma_f32_16x16x32_bf16 v[120:123], a[216:219], a[208:211], v[120:123]\n"
        "v_cvt_pk_bf16_f32 v232, v218, v226\n"
        "v_mfma_f32_16x16x32_bf16 v[120:123], a[220:223], a[212:215], v[120:123]\n"
        "s_waitcnt lgkmcnt(5)\n"
        "buffer_store_dword v234, v238, s[16:19], s39 offen offset:384 sc1\n"
        "v_mfma_f32_16x16x32_bf16 v[112:115], a[72:75], a[224:227], 0\n"
        "s_waitcnt lgkmcnt(4)\n"
        "s_nop 1\n"
        "v_mfma_f32_16x16x32_bf16 v[112:115], a[76:79], a[228:231], v[112:115]\n"
        "s_waitcnt lgkmcnt(3)\n"
        "v_cvt_pk_bf16_f32 v234, v219, v227\n"
        "s_nop 0\n"
        "v_mfma_f32_16x16x32_bf16 v[112:115], a[80:83], a[232:235], v[112:115]\n"
        "s_waitcnt lgkmcnt(2)\n"
        "v_cvt_pk_bf16_f32 v144, v120, v121\n"
        "v_cvt_pk_bf16_f32 v145, v122, v123\n"
        "v_mfma_f32_16x16x32_bf16 v[112:115], a[84:87], a[236:239], v[112:115]\n"
        "s_waitcnt lgkmcnt(1)\n"
        "v_mfma_f32_16x16x32_bf16 v[112:115], a[88:91], a[240:243], v[112:115]\n"
        "s_waitcnt lgkmcnt(0)\n"
        "buffer_store_dword v232, v238, s[16:19], s39 offen offset:768 sc1\n"
        "v_mfma_f32_16x16x32_bf16 v[112:115], a[92:95], a[244:247], v[112:115]\n"
        "s_nop 2\n"
        "v_mfma_f32_16x16x32_bf16 v[116:119], a[116:119], a[244:247], 0\n"
        "s_nop 2\n"
        "v_mfma_f32_16x16x32_bf16 v[116:119], a[112:115], a[240:243], v[116:119]\n"
        "v_cvt_pk_bf16_f32 v172, v112, v113\n"
        "v_cvt_pk_bf16_f32 v173, v114, v115\n"
        "v_mfma_f32_16x16x32_bf16 v[116:119], a[108:111], a[236:239], v[116:119]\n"
        "buffer_store_dword v234, v238, s[16:19], s39 offen offset:1152 sc1\n"
        "v_mfma_f32_16x16x32_bf16 v[116:119], a[104:107], a[232:235], v[116:119]\n"
        "v_mfma_f32_16x16x32_bf16 v[116:119], a[100:103], a[228:231], v[116:119]\n"
        "v_mfma_f32_16x16x32_bf16 v[116:119], a[96:99], a[224:227], v[116:119]\n"
        "s_mov_b32 m0, s48\n"
        "s_nop 1\n"
        "v_mfma_f32_16x16x32_bf16 v[120:123], a[120:123], a[224:227], 0\n"
        "ds_read_b128 v[162:165], v251 offset:0\n"
        "s_nop 0\n"
        "v_mfma_f32_16x16x32_bf16 v[120:123], a[124:127], a[228:231], v[120:123]\n"
        "buffer_load_dwordx4 v249, s[12:15], s36 offen offset:0 lds\n"
        "v_mfma_f32_16x16x32_bf16 v[120:123], a[128:131], a[232:235], v[120:123]\n"
        "v_cvt_pk_bf16_f32 v174, v116, v117\n"
        "v_cvt_pk_bf16_f32 v175, v118, v119\n"
        "v_mfma_f32_16x16x32_bf16 v[120:123], a[132:135], a[236:239], v[120:123]\n"
        "v_mfma_f32_16x16x32_bf16 v[120:123], a[136:139], a[240:243], v[120:123]\n"
        "v_mfma_f32_16x16x32_bf16 v[120:123], a[140:143], a[244:247], v[120:123]\n"
        "s_nop 2\n"
        "v_mfma_f32_16x16x32_bf16 v[112:115], a[20:23], a[244:247], 0\n"
        "s_nop 2\n"
        "v_mfma_f32_16x16x32_bf16 v[112:115], a[16:19], a[240:243], v[112:115]\n"
        "v_cvt_pk_bf16_f32 v190, v120, v121\n"
        "v_cvt_pk_bf16_f32 v191, v122, v123\n"
        "v_mfma_f32_16x16x32_bf16 v[112:115], a[12:15], a[236:239], v[112:115]\n"
        "v_mfma_f32_16x16x32_bf16 v[112:115], a[8:11], a[232:235], v[112:115]\n"
        "v_mfma_f32_16x16x32_bf16 v[112:115], a[4:7], a[228:231], v[112:115]\n"
        "ds_read_b128 v[182:185], v251 offset:512\n"
        "v_mfma_f32_16x16x32_bf16 v[112:115], a[0:3], a[224:227], v[112:115]\n"
        "s_nop 2\n"
        "v_mfma_f32_16x16x32_bf16 v[116:119], a[24:27], a[224:227], 0\n"
        "s_nop 2\n"
        "v_mfma_f32_16x16x32_bf16 v[116:119], a[28:31], a[228:231], v[116:119]\n"
        "v_cvt_pk_bf16_f32 v176, v112, v113\n"
        "v_cvt_pk_bf16_f32 v177, v114, v115\n"
        "v_mfma_f32_16x16x32_bf16 v[116:119], a[32:35], a[232:235], v[116:119]\n"
        "v_mfma_f32_16x16x32_bf16 v[116:119], a[36:39], a[236:239], v[116:119]\n"
        "buffer_load_dwordx4 v253, s[12:15], s36 offen offset:1024 lds\n"
        "v_mfma_f32_16x16x32_bf16 v[116:119], a[40:43], a[240:243], v[116:119]\n"
        "v_mfma_f32_16x16x32_bf16 v[116:119], a[44:47], a[244:247], v[116:119]\n"
        "s_nop 2\n"
        "v_mfma_f32_16x16x32_bf16 v[120:123], a[68:71], a[244:247], 0\n"
        "s_nop 2\n"
        "v_mfma_f32_16x16x32_bf16 v[120:123], a[64:67], a[240:243], v[120:123]\n"
        "v_cvt_pk_bf16_f32 v178, v116, v117\n"
        "v_cvt_pk_bf16_f32 v179, v118, v119\n"
        "v_mfma_f32_16x16x32_bf16 v[120:123], a[60:63], a[236:239], v[120:123]\n"
        "v_mfma_f32_16x16x32_bf16 v[120:123], a[56:59], a[232:235], v[120:123]\n"
        "v_mfma_f32_16x16x32_bf16 v[120:123], a[52:55], a[228:231], v[120:123]\n"
        "v_mfma_f32_16x16x32_bf16 v[120:123], a[48:51], a[224:227], v[120:123]\n"
        "s_nop 2\n"
        "v_mfma_f32_16x16x32_bf16 v[112:115], a[224:227], a[144:147], 0\n"
        "s_nop 2\n"
        "v_mfma_f32_16x16x32_bf16 v[112:115], a[228:231], a[148:151], v[112:115]\n"
        "v_cvt_pk_bf16_f32 v180, v120, v121\n"
        "v_cvt_pk_bf16_f32 v181, v122, v123\n"
        "v_mfma_f32_16x16x32_bf16 v[112:115], a[232:235], a[152:155], v[112:115]\n"
        "v_cvt_pk_bf16_f32 v186, v120, v121\n"
        "v_cvt_pk_bf16_f32 v187, v122, v123\n"
        "v_mfma_f32_16x16x32_bf16 v[112:115], a[236:239], a[156:159], v[112:115]\n"
        "s_add_u32 m0, s41, 0\n"
        "v_mfma_f32_16x16x32_bf16 v[112:115], a[240:243], a[160:163], v[112:115]\n"
        "ds_read_b128 a[248:251], v240 offset:12288\n"
        "v_mfma_f32_16x16x32_bf16 v[112:115], a[244:247], a[164:167], v[112:115]\n"
        "buffer_load_dwordx4 v242, s[8:11], s47 offen lds\n"
        "v_mfma_f32_16x16x32_bf16 v[96:99], v[152:155], v[156:159], v[0:3]\n"
        "s_waitcnt lgkmcnt(2)\n"
        "ds_read_b128 a[216:219], v241 offset:12288\n"
        "s_nop 0\n"
        "v_mfma_f32_16x16x32_bf16 v[96:99], v[168:171], v[160:163], v[96:99]\n"
        "ds_read_b128 a[220:223], v240 offset:12416\n"
        "s_sub_u32 s31, s30, s28\n"
        "s_nop 0\n"
        "v_mfma_f32_16x16x32_bf16 v[100:103], v[172:175], v[156:159], v[4:7]\n"
        "s_sub_u32 s45, s28, s29\n"
        "v_cvt_pk_bf16_f32 v130, v112, v113\n"
        "v_cvt_pk_bf16_f32 v131, v114, v115\n"
        "v_mfma_f32_16x16x32_bf16 v[100:103], v[188:191], v[164:167], v[100:103]\n"
        "s_add_u32 s28, s28, s31\n"
        "ds_read_b128 v[162:165], v252 offset:0\n"
        "v_mfma_f32_16x16x32_bf16 v[116:119], a[244:247], a[188:191], 0\n"
        "s_mov_b32 s30, s29\n"
        "v_exp_f32 v96, v96\n"
        "v_mfma_f32_16x16x32_bf16 v[116:119], a[240:243], a[184:187], v[116:119]\n"
        "s_add_u32 s29, s29, s45\n"
        "v_exp_f32 v97, v97\n"
        "v_mfma_f32_16x16x32_bf16 v[116:119], a[236:239], a[180:183], v[116:119]\n"
        "v_exp_f32 v98, v98\n"
        "v_mfma_f32_16x16x32_bf16 v[116:119], a[232:235], a[176:179], v[116:119]\n"
        "v_exp_f32 v100, v100\n"
        "v_mfma_f32_16x16x32_bf16 v[116:119], a[228:231], a[172:175], v[116:119]\n"
        "v_exp_f32 v99, v99\n"
        "v_mfma_f32_16x16x32_bf16 v[116:119], a[224:227], a[168:171], v[116:119]\n"
        "v_exp_f32 v101, v101\n"
        "v_mfma_f32_16x16x32_bf16 v[104:107], v[152:155], v[176:179], v[16:19]\n"
        "s_waitcnt lgkmcnt(4)\n"
        "v_exp_f32 v102, v102\n"
        "v_mfma_f32_16x16x32_bf16 v[104:107], v[168:171], v[180:183], v[104:107]\n"
        "s_add_u32 m0, s41, 1024\n"
        "v_cvt_pk_bf16_f32 v64, v96, v97\n"
        "v_cvt_pk_bf16_f32 v65, v98, v99\n"
        "v_mfma_f32_16x16x32_bf16 v[108:111], v[172:175], v[176:179], v[20:23]\n"
        "v_cvt_pk_bf16_f32 v138, v116, v117\n"
        "v_cvt_pk_bf16_f32 v139, v118, v119\n"
        "v_mfma_f32_16x16x32_bf16 v[108:111], v[188:191], v[184:187], v[108:111]\n"
        "buffer_load_dwordx4 v243, s[8:11], s47 offen lds\n"
        "v_mfma_f32_16x16x32_bf16 v[120:123], a[224:227], a[192:195], 0\n"
        "ds_read_b128 a[224:227], v241 offset:12416\n"
        "v_cvt_pk_bf16_f32 v66, v100, v101\n"
        "v_mfma_f32_16x16x32_bf16 v[120:123], a[228:231], a[196:199], v[120:123]\n"
        "ds_read_b128 a[228:231], v240 offset:12544\n"
        "v_mfma_f32_16x16x32_bf16 v[120:123], a[232:235], a[200:203], v[120:123]\n"
        "ds_read_b128 a[232:235], v241 offset:12544\n"
        "v_mfma_f32_16x16x32_bf16 v[120:123], a[236:239], a[204:207], v[120:123]\n"
        "ds_read_b128 v[202:205], v251 offset:4096\n"
        "v_mfma_f32_16x16x32_bf16 v[120:123], a[240:243], a[208:211], v[120:123]\n"
        "ds_read_b128 v[182:185], v252 offset:512\n"
        "v_mfma_f32_16x16x32_bf16 v[120:123], a[244:247], a[212:215], v[120:123]\n"
        "s_waitcnt lgkmcnt(8)\n"
        "v_exp_f32 v103, v103\n"
        "v_mfma_f32_16x16x32_bf16 v[112:115], a[0:3], a[248:251], 0\n"
        "s_waitcnt lgkmcnt(7)\n"
        "v_cvt_pk_bf16_f32 v67, v102, v103\n"
        "s_nop 0\n"
        "v_mfma_f32_16x16x32_bf16 v[112:115], a[4:7], a[216:219], v[112:115]\n"
        "s_waitcnt lgkmcnt(6)\n"
        "v_exp_f32 v104, v104\n"
        "v_mfma_f32_16x16x32_bf16 v[112:115], a[8:11], a[220:223], v[112:115]\n"
        "s_waitcnt lgkmcnt(4)\n"
        "v_exp_f32 v105, v105\n"
        "v_mfma_f32_16x16x32_bf16 v[112:115], a[12:15], a[224:227], v[112:115]\n"
        "s_waitcnt lgkmcnt(3)\n"
        "v_exp_f32 v106, v106\n"
        "v_mfma_f32_16x16x32_bf16 v[112:115], a[16:19], a[228:231], v[112:115]\n"
        "s_waitcnt lgkmcnt(2)\n"
        "v_cvt_pk_bf16_f32 v72, v104, v105\n"
        "v_cvt_pk_bf16_f32 v146, v120, v121\n"
        "v_mfma_f32_16x16x32_bf16 v[112:115], a[20:23], a[232:235], v[112:115]\n"
        "s_add_u32 m0, s41, 2048\n"
        "v_exp_f32 v108, v108\n"
        "v_mfma_f32_16x16x32_bf16 v[116:119], a[44:47], a[232:235], 0\n"
        "v_cvt_pk_bf16_f32 v147, v122, v123\n"
        "v_add_u32 v245, s45, v245\n"
        "v_mfma_f32_16x16x32_bf16 v[116:119], a[40:43], a[228:231], v[116:119]\n"
        "buffer_load_dwordx4 v244, s[8:11], s47 offen lds\n"
        "v_mfma_f32_16x16x32_bf16 v[116:119], a[36:39], a[224:227], v[116:119]\n"
        "v_exp_f32 v107, v107\n"
        "v_mfma_f32_16x16x32_bf16 v[116:119], a[32:35], a[220:223], v[116:119]\n"
        "v_exp_f32 v109, v109\n"
        "v_mfma_f32_16x16x32_bf16 v[116:119], a[28:31], a[216:219], v[116:119]\n"
        "v_cvt_pk_bf16_f32 v73, v106, v107\n"
        "v_cvt_pk_bf16_f32 v74, v108, v109\n"
        "v_mfma_f32_16x16x32_bf16 v[116:119], a[24:27], a[248:251], v[116:119]\n"
        "v_exp_f32 v110, v110\n"
        "v_mfma_f32_16x16x32_bf16 v[120:123], a[48:51], a[248:251], 0\n"
        "v_exp_f32 v111, v111\n"
        "v_mfma_f32_16x16x32_bf16 v[120:123], a[52:55], a[216:219], v[120:123]\n"
        "v_cvt_pk_bf16_f32 v75, v110, v111\n"
        "v_cvt_pk_bf16_f32 v196, v112, v113\n"
        "v_mfma_f32_16x16x32_bf16 v[120:123], a[56:59], a[220:223], v[120:123]\n"
        "v_cvt_pk_bf16_f32 v197, v114, v115\n"
        "v_cvt_pk_bf16_f32 v198, v116, v117\n"
        "v_mfma_f32_16x16x32_bf16 v[120:123], a[60:63], a[224:227], v[120:123]\n"
        "v_cvt_pk_bf16_f32 v199, v118, v119\n"
        "v_add_u32 v246, s45, v246\n"
        "v_mfma_f32_16x16x32_bf16 v[120:123], a[64:67], a[228:231], v[120:123]\n"
        "v_mfma_f32_16x16x32_bf16 v[120:123], a[68:71], a[232:235], v[120:123]\n"
        "s_nop 2\n"
        "v_mfma_f32_16x16x32_bf16 v[112:115], a[92:95], a[232:235], 0\n"
        "s_nop 2\n"
        "v_mfma_f32_16x16x32_bf16 v[112:115], a[88:91], a[228:231], v[112:115]\n"
        "s_add_u32 m0, s41, 12288\n"
        "v_cvt_pk_bf16_f32 v200, v120, v121\n"
        "v_cvt_pk_bf16_f32 v201, v122, v123\n"
        "v_mfma_f32_16x16x32_bf16 v[112:115], a[84:87], a[224:227], v[112:115]\n"
        "v_cvt_pk_bf16_f32 v206, v120, v121\n"
        "v_cvt_pk_bf16_f32 v207, v122, v123\n"
        "v_mfma_f32_16x16x32_bf16 v[112:115], a[80:83], a[220:223], v[112:115]\n"
        "buffer_load_dwordx4 v242, s[8:11], s35 offen lds\n"
        "v_mfma_f32_16x16x32_bf16 v[112:115], a[76:79], a[216:219], v[112:115]\n"
        "v_mfma_f32_16x16x32_bf16 v[112:115], a[72:75], a[248:251], v[112:115]\n"
        "s_nop 2\n"
        "v_mfma_f32_16x16x32_bf16 v[96:99], v[152:155], v[196:199], v[32:35]\n"
        "s_waitcnt lgkmcnt(1)\n"
        "s_nop 1\n"
        "v_mfma_f32_16x16x32_bf16 v[96:99], v[168:171], v[200:203], v[96:99]\n"
        "s_nop 0\n"
        "v_cvt_pk_bf16_f32 v192, v112, v113\n"
        "v_cvt_pk_bf16_f32 v193, v114, v115\n"
        "v_mfma_f32_16x16x32_bf16 v[100:103], v[172:175], v[196:199], v[36:39]\n"
        "v_mfma_f32_16x16x32_bf16 v[100:103], v[188:191], v[204:207], v[100:103]\n"
        "ds_read_b128 v[202:205], v252 offset:4096\n"
        "s_cmp_eq_u32 s37, 100663296\n"
        "v_mfma_f32_16x16x32_bf16 v[116:119], a[96:99], a[248:251], 0\n"
        "s_cselect_b32 s46, 0, s37\n"
        "v_exp_f32 v96, v96\n"
        "v_mfma_f32_16x16x32_bf16 v[116:119], a[100:103], a[216:219], v[116:119]\n"
        "s_add_u32 s38, s46, s43\n"
        "v_exp_f32 v97, v97\n"
        "v_mfma_f32_16x16x32_bf16 v[116:119], a[104:107], a[220:223], v[116:119]\n"
        "s_add_u32 s39, s46, s44\n"
        "v_exp_f32 v98, v98\n"
        "v_mfma_f32_16x16x32_bf16 v[116:119], a[108:111], a[224:227], v[116:119]\n"
        "s_add_u32 s37, s37, 6291456\n"
        "v_exp_f32 v100, v100\n"
        "v_mfma_f32_16x16x32_bf16 v[116:119], a[112:115], a[228:231], v[116:119]\n"
        "v_exp_f32 v99, v99\n"
        "v_mfma_f32_16x16x32_bf16 v[116:119], a[116:119], a[232:235], v[116:119]\n"
        "s_add_u32 m0, s41, 13312\n"
        "v_exp_f32 v101, v101\n"
        "v_mfma_f32_16x16x32_bf16 v[120:123], a[140:143], a[232:235], 0\n"
        "v_exp_f32 v102, v102\n"
        "v_mfma_f32_16x16x32_bf16 v[120:123], a[136:139], a[228:231], v[120:123]\n"
        "buffer_load_dwordx4 v243, s[8:11], s35 offen lds\n"
        "v_mfma_f32_16x16x32_bf16 v[120:123], a[132:135], a[224:227], v[120:123]\n"
        "v_cvt_pk_bf16_f32 v80, v96, v97\n"
        "v_cvt_pk_bf16_f32 v194, v116, v117\n"
        "v_mfma_f32_16x16x32_bf16 v[120:123], a[128:131], a[220:223], v[120:123]\n"
        "v_cvt_pk_bf16_f32 v195, v118, v119\n"
        "v_cvt_pk_bf16_f32 v81, v98, v99\n"
        "v_mfma_f32_16x16x32_bf16 v[120:123], a[124:127], a[216:219], v[120:123]\n"
        "v_cvt_pk_bf16_f32 v82, v100, v101\n"
        "v_mfma_f32_16x16x32_bf16 v[120:123], a[120:123], a[248:251], v[120:123]\n"
        "v_exp_f32 v103, v103\n"
        "v_mfma_f32_16x16x32_bf16 v[112:115], a[248:251], a[144:147], 0\n"
        "v_cvt_pk_bf16_f32 v83, v102, v103\n"
        "s_nop 0\n"
        "v_mfma_f32_16x16x32_bf16 v[112:115], a[216:219], a[148:151], v[112:115]\n"
        "s_nop 2\n"
        "v_mfma_f32_16x16x32_bf16 v[112:115], a[220:223], a[152:155], v[112:115]\n"
        "ds_read_b128 a[236:239], v240 offset:13824\n"
        "v_cvt_pk_bf16_f32 v208, v120, v121\n"
        "v_mfma_f32_16x16x32_bf16 v[112:115], a[224:227], a[156:159], v[112:115]\n"
        "ds_read_b128 a[240:243], v241 offset:13824\n"
        "v_cvt_pk_bf16_f32 v209, v122, v123\n"
        "v_mfma_f32_16x16x32_bf16 v[112:115], a[228:231], a[160:163], v[112:115]\n"
        "ds_read_b128 a[244:247], v240 offset:13952\n"
        "v_mfma_f32_16x16x32_bf16 v[112:115], a[232:235], a[164:167], v[112:115]\n"
        "s_nop 2\n"
        "v_mfma_f32_16x16x32_bf16 v[104:107], v[192:195], v[156:159], v[8:11]\n"
        "s_nop 1\n"
        "v_mfma_f32_16x16x32_bf16 v[104:107], v[208:211], v[160:163], v[104:107]\n"
        "s_add_u32 m0, s41, 14336\n"
        "v_cvt_pk_bf16_f32 v132, v112, v113\n"
        "v_cvt_pk_bf16_f32 v133, v114, v115\n"
        "v_mfma_f32_16x16x32_bf16 v[116:119], a[232:235], a[188:191], 0\n"
        "v_mfma_f32_16x16x32_bf16 v[116:119], a[228:231], a[184:187], v[116:119]\n"
        "buffer_load_dwordx4 v244, s[8:11], s35 offen lds\n"
        "v_mfma_f32_16x16x32_bf16 v[116:119], a[224:227], a[180:183], v[116:119]\n"
        "v_mfma_f32_16x16x32_bf16 v[116:119], a[220:223], a[176:179], v[116:119]\n"
        "v_exp_f32 v104, v104\n"
        "v_mfma_f32_16x16x32_bf16 v[116:119], a[216:219], a[172:175], v[116:119]\n"
        "v_exp_f32 v105, v105\n"
        "v_mfma_f32_16x16x32_bf16 v[116:119], a[248:251], a[168:171], v[116:119]\n"
        "v_exp_f32 v106, v106\n"
        "v_mfma_f32_16x16x32_bf16 v[108:111], v[192:195], v[176:179], v[24:27]\n"
        "s_waitcnt lgkmcnt(4)\n"
        "v_exp_f32 v107, v107\n"
        "v_mfma_f32_16x16x32_bf16 v[108:111], v[208:211], v[180:183], v[108:111]\n"
        "v_cvt_pk_bf16_f32 v68, v104, v105\n"
        "v_cvt_pk_bf16_f32 v69, v106, v107\n"
        "v_mfma_f32_16x16x32_bf16 v[120:123], a[248:251], a[192:195], 0\n"
        "ds_read_b128 a[248:251], v241 offset:13952\n"
        "v_cvt_pk_bf16_f32 v140, v116, v117\n"
        "v_mfma_f32_16x16x32_bf16 v[120:123], a[216:219], a[196:199], v[120:123]\n"
        "ds_read_b128 a[216:219], v240 offset:14080\n"
        "v_cvt_pk_bf16_f32 v141, v118, v119\n"
        "v_mfma_f32_16x16x32_bf16 v[120:123], a[220:223], a[200:203], v[120:123]\n"
        "ds_read_b128 a[220:223], v241 offset:14080\n"
        "v_mfma_f32_16x16x32_bf16 v[120:123], a[224:227], a[204:207], v[120:123]\n"
        "v_exp_f32 v108, v108\n"
        "v_mfma_f32_16x16x32_bf16 v[120:123], a[228:231], a[208:211], v[120:123]\n"
        "v_exp_f32 v109, v109\n"
        "v_mfma_f32_16x16x32_bf16 v[120:123], a[232:235], a[212:215], v[120:123]\n"
        "v_exp_f32 v110, v110\n"
        "v_mfma_f32_16x16x32_bf16 v[96:99], v[192:195], v[196:199], v[40:43]\n"
        "s_waitcnt lgkmcnt(6)\n"
        "v_exp_f32 v111, v111\n"
        "v_mfma_f32_16x16x32_bf16 v[96:99], v[208:211], v[200:203], v[96:99]\n"
        "s_waitcnt lgkmcnt(5)\n"
        "v_cvt_pk_bf16_f32 v76, v108, v109\n"
        "v_cvt_pk_bf16_f32 v77, v110, v111\n"
        "v_mfma_f32_16x16x32_bf16 v[112:115], a[72:75], a[236:239], 0\n"
        "s_waitcnt lgkmcnt(4)\n"
        "s_nop 0\n"
        "v_cvt_pk_bf16_f32 v148, v120, v121\n"
        "v_mfma_f32_16x16x32_bf16 v[112:115], a[76:79], a[240:243], v[112:115]\n"
        "s_waitcnt lgkmcnt(3)\n"
        "v_cvt_pk_bf16_f32 v149, v122, v123\n"
        "v_mfma_f32_16x16x32_bf16 v[112:115], a[80:83], a[244:247], v[112:115]\n"
        "s_waitcnt lgkmcnt(2)\n"
        "v_exp_f32 v96, v96\n"
        "v_mfma_f32_16x16x32_bf16 v[112:115], a[84:87], a[248:251], v[112:115]\n"
        "s_waitcnt lgkmcnt(1)\n"
        "v_exp_f32 v97, v97\n"
        "v_mfma_f32_16x16x32_bf16 v[112:115], a[88:91], a[216:219], v[112:115]\n"
        "s_waitcnt lgkmcnt(0)\n"
        "v_exp_f32 v98, v98\n"
        "v_mfma_f32_16x16x32_bf16 v[112:115], a[92:95], a[220:223], v[112:115]\n"
        "v_exp_f32 v99, v99\n"
        "v_mfma_f32_16x16x32_bf16 v[116:119], a[116:119], a[220:223], 0\n"
        "v_cvt_pk_bf16_f32 v84, v96, v97\n"
        "v_cvt_pk_bf16_f32 v85, v98, v99\n"
        "v_mfma_f32_16x16x32_bf16 v[116:119], a[112:115], a[216:219], v[116:119]\n"
        "v_add_u32 v240, s31, v240\n"
        "v_add_u32 v241, s31, v241\n"
        "v_mfma_f32_16x16x32_bf16 v[116:119], a[108:111], a[248:251], v[116:119]\n"
        "v_cvt_pk_bf16_f32 v212, v112, v113\n"
        "v_cvt_pk_bf16_f32 v213, v114, v115\n"
        "v_mfma_f32_16x16x32_bf16 v[116:119], a[104:107], a[244:247], v[116:119]\n"
        "v_mfma_f32_16x16x32_bf16 v[116:119], a[100:103], a[240:243], v[116:119]\n"
        "v_mfma_f32_16x16x32_bf16 v[116:119], a[96:99], a[236:239], v[116:119]\n"
        "s_nop 2\n"
        "v_mfma_f32_16x16x32_bf16 v[120:123], a[120:123], a[236:239], 0\n"
        "s_nop 2\n"
        "v_mfma_f32_16x16x32_bf16 v[120:123], a[124:127], a[240:243], v[120:123]\n"
        "s_add_u32 s47, s47, 6291456\n"
        "v_cvt_pk_bf16_f32 v214, v116, v117\n"
        "v_cvt_pk_bf16_f32 v215, v118, v119\n"
        "v_mfma_f32_16x16x32_bf16 v[120:123], a[128:131], a[244:247], v[120:123]\n"
        "s_add_u32 s36, s36, 2097152\n"
        "v_mfma_f32_16x16x32_bf16 v[120:123], a[132:135], a[248:251], v[120:123]\n"
        "s_xor_b32 s48, s48, 8192\n"
        "v_mfma_f32_16x16x32_bf16 v[120:123], a[136:139], a[216:219], v[120:123]\n"
        "v_mfma_f32_16x16x32_bf16 v[120:123], a[140:143], a[220:223], v[120:123]\n"
        "s_nop 2\n"
        "v_mfma_f32_16x16x32_bf16 v[112:115], a[220:223], a[164:167], 0\n"
        "s_nop 2\n"
        "v_mfma_f32_16x16x32_bf16 v[112:115], a[216:219], a[160:163], v[112:115]\n"
        "v_cvt_pk_bf16_f32 v230, v120, v121\n"
        "v_cvt_pk_bf16_f32 v231, v122, v123\n"
        "v_mfma_f32_16x16x32_bf16 v[112:115], a[248:251], a[156:159], v[112:115]\n"
        "v_mfma_f32_16x16x32_bf16 v[112:115], a[244:247], a[152:155], v[112:115]\n"
        "v_mfma_f32_16x16x32_bf16 v[112:115], a[240:243], a[148:151], v[112:115]\n"
        "v_mfma_f32_16x16x32_bf16 v[112:115], a[236:239], a[144:147], v[112:115]\n"
        "s_barrier\n"
        "s_nop 2\n"
        "v_mfma_f32_16x16x32_bf16 v[100:103], v[212:215], v[156:159], v[12:15]\n"
        "s_nop 2\n"
        "v_mfma_f32_16x16x32_bf16 v[100:103], v[228:231], v[164:167], v[100:103]\n"
        "v_cvt_pk_bf16_f32 v134, v112, v113\n"
        "v_cvt_pk_bf16_f32 v135, v114, v115\n"
        "v_mfma_f32_16x16x32_bf16 v[116:119], a[236:239], a[168:171], 0\n"
        "v_mfma_f32_16x16x32_bf16 v[116:119], a[240:243], a[172:175], v[116:119]\n"
        "v_mfma_f32_16x16x32_bf16 v[116:119], a[244:247], a[176:179], v[116:119]\n"
        "v_mfma_f32_16x16x32_bf16 v[116:119], a[248:251], a[180:183], v[116:119]\n"
        "v_mfma_f32_16x16x32_bf16 v[116:119], a[216:219], a[184:187], v[116:119]\n"
        "s_nop 0\n"
        "v_exp_f32 v100, v100\n"
        "v_mfma_f32_16x16x32_bf16 v[116:119], a[220:223], a[188:191], v[116:119]\n"
        "v_exp_f32 v101, v101\n"
        "v_mfma_f32_16x16x32_bf16 v[104:107], v[212:215], v[176:179], v[28:31]\n"
        "v_exp_f32 v102, v102\n"
        "v_mfma_f32_16x16x32_bf16 v[104:107], v[228:231], v[184:187], v[104:107]\n"
        "v_exp_f32 v103, v103\n"
        "v_mfma_f32_16x16x32_bf16 v[120:123], a[220:223], a[212:215], 0\n"
        "v_cvt_pk_bf16_f32 v70, v100, v101\n"
        "v_cvt_pk_bf16_f32 v71, v102, v103\n"
        "v_mfma_f32_16x16x32_bf16 v[120:123], a[216:219], a[208:211], v[120:123]\n"
        "v_cvt_pk_bf16_f32 v142, v116, v117\n"
        "v_cvt_pk_bf16_f32 v143, v118, v119\n"
        "v_mfma_f32_16x16x32_bf16 v[120:123], a[248:251], a[204:207], v[120:123]\n"
        "v_mov_b64 v[96:97], 0\n"
        "v_mfma_f32_16x16x32_bf16 v[120:123], a[244:247], a[200:203], v[120:123]\n"
        "v_exp_f32 v104, v104\n"
        "v_mfma_f32_16x16x32_bf16 v[120:123], a[240:243], a[196:199], v[120:123]\n"
        "v_exp_f32 v105, v105\n"
        "v_mfma_f32_16x16x32_bf16 v[120:123], a[236:239], a[192:195], v[120:123]\n"
        "v_exp_f32 v106, v106\n"
        "v_mfma_f32_16x16x32_bf16 v[108:111], v[212:215], v[196:199], v[44:47]\n"
        "v_exp_f32 v107, v107\n"
        "v_mfma_f32_16x16x32_bf16 v[108:111], v[228:231], v[204:207], v[108:111]\n"
        "v_cvt_pk_bf16_f32 v78, v104, v105\n"
        "v_cvt_pk_bf16_f32 v79, v106, v107\n"
        "v_mfma_f32_16x16x32_bf16 v[112:115], a[0:3], a[236:239], 0\n"
        "s_nop 0\n"
        "v_cvt_pk_bf16_f32 v150, v120, v121\n"
        "v_cvt_pk_bf16_f32 v151, v122, v123\n"
        "v_mfma_f32_16x16x32_bf16 v[112:115], a[4:7], a[240:243], v[112:115]\n"
        "v_mfma_f32_16x16x32_bf16 v[112:115], a[8:11], a[244:247], v[112:115]\n"
        "v_exp_f32 v108, v108\n"
        "v_mfma_f32_16x16x32_bf16 v[112:115], a[12:15], a[248:251], v[112:115]\n"
        "v_exp_f32 v109, v109\n"
        "v_mfma_f32_16x16x32_bf16 v[112:115], a[16:19], a[216:219], v[112:115]\n"
        "v_exp_f32 v110, v110\n"
        "v_mfma_f32_16x16x32_bf16 v[112:115], a[20:23], a[220:223], v[112:115]\n"
        "v_exp_f32 v111, v111\n"
        "v_smfmac_f32_16x16x64_bf16 v[96:99], a[252:255], v[64:71], v237\n"
        "v_cvt_pk_bf16_f32 v86, v108, v109\n"
        "v_mfma_f32_16x16x32_bf16 v[100:103], v[132:135], v[68:71], 0\n"
        "v_mov_b64 v[124:125], 0\n"
        "v_cvt_pk_bf16_f32 v87, v110, v111\n"
        "v_mfma_f32_16x16x32_bf16 v[100:103], v[128:131], v[64:67], v[100:103]\n"
        "s_nop 0\n"
        "v_cvt_pk_bf16_f32 v216, v112, v113\n"
        "v_cvt_pk_bf16_f32 v217, v114, v115\n"
        "v_mfma_f32_16x16x32_bf16 v[104:107], v[136:139], v[64:67], 0\n"
        ".long 0xbec200ff\n"
        "47:\n"
        "s_branch 61b\n"
        "ds_read_b128 v[222:225], v251 offset:4608\n"
        "v_add_f32 v97, v96, v97\n"
        "v_mfma_f32_16x16x32_bf16 v[104:107], v[140:143], v[68:71], v[104:107]\n"
        "v_rcp_f32 v96, v97\n"
        "v_mfma_f32_16x16x32_bf16 v[108:111], v[148:151], v[68:71], 0\n"
        "v_mul_f32 v100, v96, v100\n"
        "v_mul_f32 v101, v96, v101\n"
        "v_mfma_f32_16x16x32_bf16 v[108:111], v[144:147], v[64:67], v[108:111]\n"
        "v_mul_f32 v102, v96, v102\n"
        "v_mul_f32 v103, v96, v103\n"
        "v_mfma_f32_16x16x32_bf16 v[116:119], a[44:47], a[220:223], 0\n"
        "v_cvt_pk_bf16_f32 v100, v100, v101\n"
        "v_cvt_pk_bf16_f32 v101, v102, v103\n"
        "v_mfma_f32_16x16x32_bf16 v[116:119], a[40:43], a[216:219], v[116:119]\n"
        "v_mul_f32 v104, v96, v104\n"
        "v_mul_f32 v105, v96, v105\n"
        "v_mfma_f32_16x16x32_bf16 v[116:119], a[36:39], a[248:251], v[116:119]\n"
        "v_mul_f32 v106, v96, v106\n"
        "v_mul_f32 v107, v96, v107\n"
        "v_mfma_f32_16x16x32_bf16 v[116:119], a[32:35], a[244:247], v[116:119]\n"
        "v_cvt_pk_bf16_f32 v102, v104, v105\n"
        "v_cvt_pk_bf16_f32 v103, v106, v107\n"
        "v_mfma_f32_16x16x32_bf16 v[116:119], a[28:31], a[240:243], v[116:119]\n"
        "ds_write_b128 v245, v[100:103] offset:0\n"
        "v_mfma_f32_16x16x32_bf16 v[116:119], a[24:27], a[236:239], v[116:119]\n"
        "v_mul_f32 v108, v96, v108\n"
        "v_mul_f32 v109, v96, v109\n"
        "v_smfmac_f32_16x16x64_bf16 v[124:127], a[252:255], v[72:79], v237\n"
        "v_mul_f32 v110, v96, v110\n"
        "v_mfma_f32_16x16x32_bf16 v[64:67], v[132:135], v[76:79], 0\n"
        "v_mul_f32 v111, v96, v111\n"
        "v_cvt_pk_bf16_f32 v108, v108, v109\n"
        "v_mfma_f32_16x16x32_bf16 v[64:67], v[128:131], v[72:75], v[64:67]\n"
        "ds_read_b128 v[156:159], v247 offset:0\n"
        "v_cvt_pk_bf16_f32 v218, v116, v117\n"
        "v_mfma_f32_16x16x32_bf16 v[68:71], v[136:139], v[72:75], 0\n"
        "ds_read_b128 v[160:163], v247 offset:4096\n"
        "v_cvt_pk_bf16_f32 v219, v118, v119\n"
        "v_mfma_f32_16x16x32_bf16 v[68:71], v[140:143], v[76:79], v[68:71]\n"
        "ds_read_b128 v[164:167], v247 offset:4352\n"
        "v_cvt_pk_bf16_f32 v109, v110, v111\n"
        "v_mfma_f32_16x16x32_bf16 v[120:123], v[148:151], v[76:79], 0\n"
        "ds_write_b64 v246, v[108:109] offset:0\n"
        "v_mfma_f32_16x16x32_bf16 v[120:123], v[144:147], v[72:75], v[120:123]\n"
        "v_max_u32 v236, v96, v97\n"
        "v_mov_b64 v[116:117], 0\n"
        "v_mfma_f32_16x16x32_bf16 v[112:115], a[48:51], a[236:239], 0\n"
        "v_add_f32 v125, v124, v125\n"
        "v_mfma_f32_16x16x32_bf16 v[112:115], a[52:55], a[240:243], v[112:115]\n"
        "v_rcp_f32 v124, v125\n"
        "v_mfma_f32_16x16x32_bf16 v[112:115], a[56:59], a[244:247], v[112:115]\n"
        "v_mul_f32 v64, v124, v64\n"
        "v_mul_f32 v65, v124, v65\n"
        "v_mfma_f32_16x16x32_bf16 v[112:115], a[60:63], a[248:251], v[112:115]\n"
        "v_mul_f32 v66, v124, v66\n"
        "v_mul_f32 v67, v124, v67\n"
        "v_mfma_f32_16x16x32_bf16 v[112:115], a[64:67], a[216:219], v[112:115]\n"
        "v_cvt_pk_bf16_f32 v64, v64, v65\n"
        "v_cvt_pk_bf16_f32 v65, v66, v67\n"
        "v_mfma_f32_16x16x32_bf16 v[112:115], a[68:71], a[220:223], v[112:115]\n"
        "ds_read_b128 a[216:219], v248 offset:0\n"
        "v_mul_f32 v68, v124, v68\n"
        "v_smfmac_f32_16x16x64_bf16 v[116:119], a[252:255], v[80:87], v237\n"
        "ds_read_b128 a[220:223], v248 offset:3072\n"
        "v_mfma_f32_16x16x32_bf16 v[72:75], v[132:135], v[84:87], 0\n"
        "v_mul_f32 v69, v124, v69\n"
        "v_mul_f32 v70, v124, v70\n"
        "v_mfma_f32_16x16x32_bf16 v[72:75], v[128:131], v[80:83], v[72:75]\n"
        "v_cvt_pk_bf16_f32 v220, v112, v113\n"
        "v_cvt_pk_bf16_f32 v221, v114, v115\n"
        "v_mfma_f32_16x16x32_bf16 v[76:79], v[136:139], v[80:83], 0\n"
        "ds_read_b128 a[224:227], v248 offset:32\n"
        "v_cvt_pk_bf16_f32 v226, v112, v113\n"
        "v_mfma_f32_16x16x32_bf16 v[76:79], v[140:143], v[84:87], v[76:79]\n"
        "ds_read_b128 a[228:231], v248 offset:3104\n"
        "v_cvt_pk_bf16_f32 v227, v114, v115\n"
        "v_mfma_f32_16x16x32_bf16 v[96:99], v[148:151], v[84:87], 0\n"
        "v_mul_f32 v71, v124, v71\n"
        "v_cvt_pk_bf16_f32 v66, v68, v69\n"
        "v_mfma_f32_16x16x32_bf16 v[96:99], v[144:147], v[80:83], v[96:99]\n"
        "v_cvt_pk_bf16_f32 v67, v70, v71\n"
        "v_mul_f32 v120, v124, v120\n"
        "v_mfma_f32_16x16x32_bf16 v[100:103], v[152:155], v[216:219], v[48:51]\n"
        "s_waitcnt lgkmcnt(9)\n"
        "ds_read_b128 v[152:155], v247 offset:256\n"
        "v_mul_f32 v121, v124, v121\n"
        "v_mfma_f32_16x16x32_bf16 v[100:103], v[168:171], v[220:223], v[100:103]\n"
        "ds_write_b128 v245, v[64:67] offset:256\n"
        "v_mfma_f32_16x16x32_bf16 v[104:107], v[172:175], v[216:219], v[52:55]\n"
        "v_mul_f32 v122, v124, v122\n"
        "v_mul_f32 v123, v124, v123\n"
        "v_mfma_f32_16x16x32_bf16 v[104:107], v[188:191], v[224:227], v[104:107]\n"
        "s_waitcnt lgkmcnt(5)\n"
        "v_cvt_pk_bf16_f32 v120, v120, v121\n"
        "v_cvt_pk_bf16_f32 v121, v122, v123\n"
        "v_mfma_f32_16x16x32_bf16 v[176:179], v[156:159], a[216:219], 0\n"
        "s_waitcnt lgkmcnt(4)\n"
        "ds_write_b64 v246, v[120:121] offset:256\n"
        "v_mfma_f32_16x16x32_bf16 v[176:179], v[160:163], a[220:223], v[176:179]\n"
        "ds_read_b128 v[222:225], v252 offset:4608\n"
        "v_max3_u32 v236, v236, v124, v125\n"
        "v_mfma_f32_16x16x32_bf16 v[196:199], v[164:167], a[220:223], 0\n"
        "s_waitcnt lgkmcnt(3)\n"
        "ds_read_b128 a[232:235], v254 offset:0\n"
        "v_add_f32 v117, v116, v117\n"
        "v_mfma_f32_16x16x32_bf16 v[196:199], v[152:155], a[216:219], v[196:199]\n"
        "ds_read_b128 a[220:223], v254 offset:3072\n"
        "v_mfma_f32_16x16x32_bf16 v[200:203], v[152:155], a[224:227], 0\n"
        "ds_read_b128 a[216:219], v254 offset:32\n"
        "v_mfma_f32_16x16x32_bf16 v[200:203], v[164:167], a[228:231], v[200:203]\n"
        "v_rcp_f32 v116, v117\n"
        "v_mfma_f32_16x16x32_bf16 v[180:183], v[160:163], a[228:231], 0\n"
        "ds_read_b128 a[228:231], v254 offset:3104\n"
        "v_mul_f32 v72, v116, v72\n"
        "v_mfma_f32_16x16x32_bf16 v[180:183], v[156:159], a[224:227], v[180:183]\n"
        "ds_read_b128 a[236:239], v247 offset:8192\n"
        "v_mul_f32 v73, v116, v73\n"
        "v_mfma_f32_16x16x32_bf16 v[108:111], v[192:195], v[216:219], v[56:59]\n"
        "s_waitcnt lgkmcnt(5)\n"
        "ds_read_b128 a[224:227], v248 offset:16\n"
        "v_mul_f32 v74, v116, v74\n"
        "v_mfma_f32_16x16x32_bf16 v[108:111], v[208:211], v[220:223], v[108:111]\n"
        "ds_read_b128 a[240:243], v247 offset:12288\n"
        "v_mul_f32 v75, v116, v75\n"
        "v_mfma_f32_16x16x32_bf16 v[112:115], v[212:215], v[216:219], v[60:63]\n"
        "ds_read_b128 a[244:247], v247 offset:12544\n"
        "v_cvt_pk_bf16_f32 v72, v72, v73\n"
        "v_mfma_f32_16x16x32_bf16 v[112:115], v[228:231], v[224:227], v[112:115]\n"
        "s_waitcnt lgkmcnt(7)\n"
        "ds_read_b128 a[248:251], v247 offset:8448\n"
        "v_cvt_pk_bf16_f32 v73, v74, v75\n"
        "v_mfma_f32_16x16x32_bf16 v[184:187], v[156:159], a[232:235], 0\n"
        "s_waitcnt lgkmcnt(7)\n"
        "v_exp_f32 v100, v100\n"
        "v_mfma_f32_16x16x32_bf16 v[184:187], v[160:163], a[220:223], v[184:187]\n"
        "v_exp_f32 v101, v101\n"
        "v_mfma_f32_16x16x32_bf16 v[204:207], v[164:167], a[220:223], 0\n"
        "ds_read_b128 a[220:223], v248 offset:3088\n"
        "v_mul_f32 v76, v116, v76\n"
        "v_mfma_f32_16x16x32_bf16 v[204:207], v[152:155], a[232:235], v[204:207]\n"
        "s_waitcnt lgkmcnt(7)\n"
        "ds_read_b128 a[232:235], v254 offset:16\n"
        "v_mul_f32 v77, v116, v77\n"
        "v_mfma_f32_16x16x32_bf16 v[192:195], v[152:155], a[216:219], 0\n"
        "s_waitcnt lgkmcnt(7)\n"
        "v_mul_f32 v78, v116, v78\n"
        "v_mul_f32 v79, v116, v79\n"
        "v_mfma_f32_16x16x32_bf16 v[192:195], v[164:167], a[228:231], v[192:195]\n"
        "v_exp_f32 v102, v102\n"
        "v_mfma_f32_16x16x32_bf16 v[172:175], v[160:163], a[228:231], 0\n"
        "ds_read_b128 a[228:231], v254 offset:3088\n"
        "v_cvt_pk_bf16_f32 v88, v100, v101\n"
        "v_mfma_f32_16x16x32_bf16 v[172:175], v[156:159], a[216:219], v[172:175]\n"
        "s_waitcnt lgkmcnt(6)\n"
        "ds_read_b128 a[216:219], v248 offset:6144\n"
        "v_cvt_pk_bf16_f32 v74, v76, v77\n"
        "v_mfma_f32_16x16x32_bf16 v[212:215], v[156:159], a[224:227], 0\n"
        "s_waitcnt lgkmcnt(3)\n"
        "v_exp_f32 v104, v104\n"
        "v_mfma_f32_16x16x32_bf16 v[212:215], v[160:163], a[220:223], v[212:215]\n"
        "v_exp_f32 v103, v103\n"
        "v_mfma_f32_16x16x32_bf16 v[216:219], v[164:167], a[220:223], 0\n"
        "ds_read_b128 a[220:223], v248 offset:9216\n"
        "v_cvt_pk_bf16_f32 v75, v78, v79\n"
        "v_mfma_f32_16x16x32_bf16 v[216:219], v[152:155], a[224:227], v[216:219]\n"
        "s_waitcnt lgkmcnt(3)\n"
        "ds_write_b128 v245, v[72:75] offset:512\n"
        "v_mfma_f32_16x16x32_bf16 v[224:227], v[152:155], a[232:235], 0\n"
        "s_waitcnt lgkmcnt(3)\n"
        "v_exp_f32 v105, v105\n"
        "v_mfma_f32_16x16x32_bf16 v[224:227], v[164:167], a[228:231], v[224:227]\n"
        "v_cvt_pk_bf16_f32 v89, v102, v103\n"
        "v_cvt_pk_bf16_f32 v90, v104, v105\n"
        "v_mfma_f32_16x16x32_bf16 v[220:223], v[160:163], a[228:231], 0\n"
        "ds_read_b128 a[224:227], v248 offset:6176\n"
        "ds_read_b128 a[228:231], v248 offset:9248\n"
        "v_mfma_f32_16x16x32_bf16 v[220:223], v[156:159], a[232:235], v[220:223]\n"
        "s_waitcnt lgkmcnt(4)\n"
        "ds_read_b128 a[232:235], v254 offset:6144\n"
        "v_mul_f32 v96, v116, v96\n"
        "v_mfma_f32_16x16x32_bf16 v[176:179], a[236:239], a[216:219], v[176:179]\n"
        "s_waitcnt lgkmcnt(4)\n"
        "v_exp_f32 v106, v106\n"
        "v_mfma_f32_16x16x32_bf16 v[176:179], a[240:243], a[220:223], v[176:179]\n"
        "v_mul_f32 v97, v116, v97\n"
        "v_mul_f32 v98, v116, v98\n"
        "v_mfma_f32_16x16x32_bf16 v[196:199], a[244:247], a[220:223], v[196:199]\n"
        "ds_read_b128 a[220:223], v254 offset:9216\n"
        "v_mul_f32 v99, v116, v99\n"
        "v_mfma_f32_16x16x32_bf16 v[196:199], a[248:251], a[216:219], v[196:199]\n"
        "s_waitcnt lgkmcnt(3)\n"
        "ds_read_b128 a[216:219], v254 offset:6176\n"
        "v_cvt_pk_bf16_f32 v96, v96, v97\n"
        "v_mfma_f32_16x16x32_bf16 v[200:203], a[248:251], a[224:227], v[200:203]\n"
        "s_waitcnt lgkmcnt(3)\n"
        "ds_read_b128 v[156:159], v247 offset:16384\n"
        "v_cvt_pk_bf16_f32 v97, v98, v99\n"
        "v_mfma_f32_16x16x32_bf16 v[200:203], a[244:247], a[228:231], v[200:203]\n"
        "ds_write_b64 v246, v[96:97] offset:512\n"
        "v_mfma_f32_16x16x32_bf16 v[180:183], a[240:243], a[228:231], v[180:183]\n"
        "ds_read_b128 a[228:231], v254 offset:9248\n"
        "v_max3_u32 v236, v236, v116, v117\n"
        "v_mfma_f32_16x16x32_bf16 v[180:183], a[236:239], a[224:227], v[180:183]\n"
        "s_waitcnt lgkmcnt(5)\n"
        "ds_read_b128 a[224:227], v248 offset:6160\n"
        "v_mfma_f32_16x16x32_bf16 v[184:187], a[236:239], a[232:235], v[184:187]\n"
        "s_waitcnt lgkmcnt(5)\n"
        "ds_read_b128 v[160:163], v247 offset:20480\n"
        "v_mfma_f32_16x16x32_bf16 v[184:187], a[240:243], a[220:223], v[184:187]\n"
        "ds_read_b128 v[164:167], v247 offset:20736\n"
        "v_mfma_f32_16x16x32_bf16 v[204:207], a[244:247], a[220:223], v[204:207]\n"
        "ds_read_b128 a[220:223], v248 offset:9232\n"
        "v_mfma_f32_16x16x32_bf16 v[204:207], a[248:251], a[232:235], v[204:207]\n"
        "s_waitcnt lgkmcnt(7)\n"
        "ds_read_b128 a[232:235], v254 offset:6160\n"
        "v_mfma_f32_16x16x32_bf16 v[192:195], a[248:251], a[216:219], v[192:195]\n"
        "s_waitcnt lgkmcnt(5)\n"
        "ds_read_b128 v[152:155], v247 offset:16640\n"
        "v_mfma_f32_16x16x32_bf16 v[192:195], a[244:247], a[228:231], v[192:195]\n"
        "v_exp_f32 v107, v107\n"
        "v_mfma_f32_16x16x32_bf16 v[172:175], a[240:243], a[228:231], v[172:175]\n"
        "ds_read_b128 a[228:231], v254 offset:9232\n"
        "v_cvt_pk_bf16_f32 v91, v106, v107\n"
        "v_mfma_f32_16x16x32_bf16 v[172:175], a[236:239], a[216:219], v[172:175]\n"
        "s_waitcnt lgkmcnt(6)\n"
        "ds_read_b128 a[216:219], v248 offset:12288\n"
        "v_mfma_f32_16x16x32_bf16 v[212:215], a[236:239], a[224:227], v[212:215]\n"
        "s_waitcnt lgkmcnt(4)\n"
        "v_exp_f32 v108, v108\n"
        "v_mfma_f32_16x16x32_bf16 v[212:215], a[240:243], a[220:223], v[212:215]\n"
        "v_exp_f32 v109, v109\n"
        "v_mfma_f32_16x16x32_bf16 v[216:219], a[244:247], a[220:223], v[216:219]\n"
        "ds_read_b128 a[220:223], v248 offset:15360\n"
        "v_cvt_pk_bf16_f32 v92, v108, v109\n"
        "v_mfma_f32_16x16x32_bf16 v[216:219], a[248:251], a[224:227], v[216:219]\n"
        "s_waitcnt lgkmcnt(4)\n"
        "ds_read_b128 a[224:227], v248 offset:12320\n"
        "v_mfma_f32_16x16x32_bf16 v[224:227], a[248:251], a[232:235], v[224:227]\n"
        "s_waitcnt lgkmcnt(3)\n"
        "v_exp_f32 v110, v110\n"
        "v_mfma_f32_16x16x32_bf16 v[224:227], a[244:247], a[228:231], v[224:227]\n"
        "v_exp_f32 v112, v112\n"
        "v_mfma_f32_16x16x32_bf16 v[220:223], a[240:243], a[228:231], v[220:223]\n"
        "ds_read_b128 a[228:231], v248 offset:15392\n"
        "v_mfma_f32_16x16x32_bf16 v[220:223], a[236:239], a[232:235], v[220:223]\n"
        "s_waitcnt lgkmcnt(3)\n"
        "ds_read_b128 a[232:235], v254 offset:12288\n"
        "v_mfma_f32_16x16x32_bf16 v[176:179], v[156:159], a[216:219], v[176:179]\n"
        "s_waitcnt lgkmcnt(3)\n"
        "v_exp_f32 v111, v111\n"
        "v_mfma_f32_16x16x32_bf16 v[176:179], v[160:163], a[220:223], v[176:179]\n"
        "v_exp_f32 v113, v113\n"
        "v_mfma_f32_16x16x32_bf16 v[196:199], v[164:167], a[220:223], v[196:199]\n"
        "ds_read_b128 a[220:223], v254 offset:15360\n"
        "v_cvt_pk_bf16_f32 v93, v110, v111\n"
        "v_mfma_f32_16x16x32_bf16 v[196:199], v[152:155], a[216:219], v[196:199]\n"
        "s_waitcnt lgkmcnt(3)\n"
        "ds_read_b128 a[216:219], v254 offset:12320\n"
        "v_cvt_pk_bf16_f32 v94, v112, v113\n"
        "v_mfma_f32_16x16x32_bf16 v[200:203], v[152:155], a[224:227], v[200:203]\n"
        "s_waitcnt lgkmcnt(3)\n"
        "v_exp_f32 v114, v114\n"
        "v_mfma_f32_16x16x32_bf16 v[200:203], v[164:167], a[228:231], v[200:203]\n"
        "v_exp_f32 v115, v115\n"
        "v_mfma_f32_16x16x32_bf16 v[180:183], v[160:163], a[228:231], v[180:183]\n"
        "s_waitcnt vmcnt(0)\n"
        "ds_read_b128 a[228:231], v254 offset:15392\n"
        "v_cvt_pk_bf16_f32 v95, v114, v115\n"
        "v_mfma_f32_16x16x32_bf16 v[180:183], v[156:159], a[224:227], v[180:183]\n"
        "s_waitcnt lgkmcnt(3)\n"
        "ds_read_b128 a[224:227], v248 offset:12304\n"
        "v_mfma_f32_16x16x32_bf16 v[184:187], v[156:159], a[232:235], v[184:187]\n"
        "s_waitcnt lgkmcnt(3)\n"
        "v_mfma_f32_16x16x32_bf16 v[184:187], v[160:163], a[220:223], v[184:187]\n"
        "s_barrier\n"
        "v_mfma_f32_16x16x32_bf16 v[204:207], v[164:167], a[220:223], v[204:207]\n"
        "ds_read_b128 a[220:223], v248 offset:15376\n"
        "v_mfma_f32_16x16x32_bf16 v[204:207], v[152:155], a[232:235], v[204:207]\n"
        "s_waitcnt lgkmcnt(3)\n"
        "ds_read_b128 a[232:235], v254 offset:12304\n"
        "v_mfma_f32_16x16x32_bf16 v[192:195], v[152:155], a[216:219], v[192:195]\n"
        "s_waitcnt lgkmcnt(3)\n"
        "ds_read_b128 a[236:239], v240 offset:0\n"
        "v_mfma_f32_16x16x32_bf16 v[192:195], v[164:167], a[228:231], v[192:195]\n"
        "ds_read_b128 a[240:243], v241 offset:0\n"
        "v_mfma_f32_16x16x32_bf16 v[172:175], v[160:163], a[228:231], v[172:175]\n"
        "ds_read_b128 a[228:231], v254 offset:15376\n"
        "v_mfma_f32_16x16x32_bf16 v[172:175], v[156:159], a[216:219], v[172:175]\n"
        "s_waitcnt lgkmcnt(5)\n"
        "ds_read_b128 a[244:247], v240 offset:128\n"
        "v_mfma_f32_16x16x32_bf16 v[212:215], v[156:159], a[224:227], v[212:215]\n"
        "s_waitcnt lgkmcnt(5)\n"
        "ds_read_b128 a[248:251], v241 offset:128\n"
        "v_mfma_f32_16x16x32_bf16 v[212:215], v[160:163], a[220:223], v[212:215]\n"
        "ds_read_b128 a[216:219], v240 offset:256\n"
        "v_mfma_f32_16x16x32_bf16 v[216:219], v[164:167], a[220:223], v[216:219]\n"
        "ds_read_b128 a[220:223], v241 offset:256\n"
        "v_mfma_f32_16x16x32_bf16 v[216:219], v[152:155], a[224:227], v[216:219]\n"
        "s_waitcnt lgkmcnt(7)\n"
        "v_mfma_f32_16x16x32_bf16 v[224:227], v[152:155], a[232:235], v[224:227]\n"
        "s_waitcnt lgkmcnt(4)\n"
        "v_mfma_f32_16x16x32_bf16 v[224:227], v[164:167], a[228:231], v[224:227]\n"
        "s_sub_u32 s27, s27, 1\n"
        "v_mfma_f32_16x16x32_bf16 v[220:223], v[160:163], a[228:231], v[220:223]\n"
        "s_cmp_lg_u32 s27, 0\n"
        "v_mfma_f32_16x16x32_bf16 v[220:223], v[156:159], a[232:235], v[220:223]\n"
        "s_cbranch_scc1 10b\n"
        "19:\n"
        ".long 0xbec200ff\n"
        "54:\n"
        "s_branch 61b\n"
        "s_waitcnt lgkmcnt(0)\n"
        "s_and_b32 s46, s42, 1\n"
        "s_lshl_b32 s31, s46, 8\n"
        "s_sub_u32 s31, s45, s31\n"
        "v_add_u32 v247, s31, v247\n"
        "ds_read_b128 a[144:147], v247 offset:0\n"
        "ds_read_b128 a[0:3], v248 offset:0\n"
        "ds_read_b128 a[148:151], v247 offset:4096\n"
        "ds_read_b128 a[4:7], v248 offset:3072\n"
        "v_mfma_f32_16x16x32_bf16 v[80:83], v[132:135], v[92:95], 0\n"
        "ds_read_b128 a[152:155], v247 offset:8192\n"
        "ds_read_b128 a[8:11], v248 offset:6144\n"
        "s_mul_i32 s45, s46, 786432\n"
        "v_mov_b64 v[124:125], 0\n"
        "v_cvt_pk_bf16_f32 v232, v176, v180\n"
        "v_cvt_pk_bf16_f32 v233, v184, v172\n"
        "v_cvt_pk_bf16_f32 v234, v212, v220\n"
        "v_mfma_f32_16x16x32_bf16 v[80:83], v[128:131], v[88:91], v[80:83]\n"
        "buffer_store_dwordx2 v[232:233], v250, s[16:19], s38 offen offset:0 sc1\n"
        "ds_read_b128 a[156:159], v247 offset:12288\n"
        "ds_read_b128 a[12:15], v248 offset:9216\n"
        "v_mfma_f32_16x16x32_bf16 v[84:87], v[136:139], v[88:91], 0\n"
        "buffer_store_dword v234, v238, s[16:19], s38 offen offset:0 sc1\n"
        "ds_read_b128 a[160:163], v247 offset:16384\n"
        "ds_read_b128 a[16:19], v248 offset:12288\n"
        "v_cvt_pk_bf16_f32 v232, v177, v181\n"
        "v_cvt_pk_bf16_f32 v233, v185, v173\n"
        "v_mfma_f32_16x16x32_bf16 v[84:87], v[140:143], v[92:95], v[84:87]\n"
        "s_sub_u32 s37, s37, s45\n"
        "ds_read_b128 a[164:167], v247 offset:20480\n"
        "ds_read_b128 a[20:23], v248 offset:15360\n"
        "v_cvt_pk_bf16_f32 v234, v213, v221\n"
        "v_mfma_f32_16x16x32_bf16 v[96:99], v[148:151], v[92:95], 0\n"
        "buffer_store_dwordx2 v[232:233], v250, s[16:19], s38 offen offset:384 sc1\n"
        "s_cmp_eq_u32 s46, 0\n"
        "s_cselect_b32 s43, s43, s44\n"
        "ds_read_b128 a[44:47], v248 offset:15392\n"
        "v_mfma_f32_16x16x32_bf16 v[96:99], v[144:147], v[88:91], v[96:99]\n"
        "buffer_store_dword v234, v238, s[16:19], s38 offen offset:384 sc1\n"
        "s_add_u32 s28, s37, 786432\n"
        "ds_read_b128 a[40:43], v248 offset:12320\n"
        "v_cvt_pk_bf16_f32 v232, v178, v182\n"
        "v_cvt_pk_bf16_f32 v233, v186, v174\n"
        "v_smfmac_f32_16x16x64_bf16 v[124:127], a[252:255], v[88:95], v237\n"
        "s_waitcnt lgkmcnt(12)\n"
        "s_cmp_eq_u32 s28, 100663296\n"
        "s_cselect_b32 s28, 0, s28\n"
        "ds_read_b128 a[36:39], v248 offset:9248\n"
        "v_cvt_pk_bf16_f32 v234, v214, v222\n"
        "v_mfma_f32_16x16x32_bf16 v[0:3], a[144:147], a[0:3], 0\n"
        "s_waitcnt lgkmcnt(11)\n"
        "buffer_store_dwordx2 v[232:233], v250, s[16:19], s38 offen offset:768 sc1\n"
        "s_lshl_b32 s29, s42, 2\n"
        "ds_read_b128 a[32:35], v248 offset:6176\n"
        "v_mfma_f32_16x16x32_bf16 v[0:3], a[148:151], a[4:7], v[0:3]\n"
        "s_waitcnt lgkmcnt(10)\n"
        "buffer_store_dword v234, v238, s[16:19], s38 offen offset:768 sc1\n"
        "s_add_u32 s29, s29, 147456\n"
        "ds_read_b128 a[28:31], v248 offset:3104\n"
        "v_cvt_pk_bf16_f32 v232, v179, v183\n"
        "v_cvt_pk_bf16_f32 v233, v187, v175\n"
        "v_mfma_f32_16x16x32_bf16 v[0:3], a[152:155], a[8:11], v[0:3]\n"
        "s_waitcnt lgkmcnt(9)\n"
        "ds_read_b128 a[24:27], v248 offset:32\n"
        "ds_read_b128 a[48:51], v254 offset:0\n"
        "v_cvt_pk_bf16_f32 v234, v215, v223\n"
        "v_mfma_f32_16x16x32_bf16 v[0:3], a[156:159], a[12:15], v[0:3]\n"
        "s_waitcnt lgkmcnt(9)\n"
        "buffer_store_dwordx2 v[232:233], v250, s[16:19], s38 offen offset:1152 sc1\n"
        "ds_read_b128 a[52:55], v254 offset:3072\n"
        "v_add_f32 v125, v124, v125\n"
        "v_mfma_f32_16x16x32_bf16 v[0:3], a[160:163], a[16:19], v[0:3]\n"
        "s_waitcnt lgkmcnt(8)\n"
        "buffer_store_dword v234, v238, s[16:19], s38 offen offset:1152 sc1\n"
        "v_rcp_f32 v124, v125\n"
        "v_cvt_pk_bf16_f32 v232, v196, v200\n"
        "v_cvt_pk_bf16_f32 v233, v204, v192\n"
        "v_mfma_f32_16x16x32_bf16 v[0:3], a[164:167], a[20:23], v[0:3]\n"
        "s_waitcnt lgkmcnt(7)\n"
        "ds_read_b128 a[56:59], v254 offset:6144\n"
        "ds_read_b128 a[60:63], v254 offset:9216\n"
        "v_mul_f32 v80, v124, v80\n"
        "v_mul_f32 v81, v124, v81\n"
        "v_mul_f32 v82, v124, v82\n"
        "v_mul_f32 v83, v124, v83\n"
        "v_mfma_f32_16x16x32_bf16 v[4:7], a[164:167], a[44:47], 0\n"
        "s_waitcnt lgkmcnt(8)\n"
        "buffer_store_dwordx2 v[232:233], v250, s[16:19], s39 offen offset:0 sc1\n"
        "ds_read_b128 a[64:67], v254 offset:12288\n"
        "v_cvt_pk_bf16_f32 v234, v216, v224\n"
        "v_mfma_f32_16x16x32_bf16 v[4:7], a[160:163], a[40:43], v[4:7]\n"
        "s_waitcnt lgkmcnt(8)\n"
        "buffer_store_dword v234, v238, s[16:19], s39 offen offset:0 sc1\n"
        "v_cvt_pk_bf16_f32 v80, v80, v81\n"
        "v_cvt_pk_bf16_f32 v81, v82, v83\n"
        "v_cvt_pk_bf16_f32 v232, v197, v201\n"
        "v_cvt_pk_bf16_f32 v233, v205, v193\n"
        "v_mfma_f32_16x16x32_bf16 v[4:7], a[156:159], a[36:39], v[4:7]\n"
        "s_waitcnt lgkmcnt(7)\n"
        "ds_read_b128 a[68:71], v254 offset:15360\n"
        "ds_read_b128 a[92:95], v254 offset:15392\n"
        "v_mul_f32 v84, v124, v84\n"
        "v_mul_f32 v85, v124, v85\n"
        "v_mul_f32 v86, v124, v86\n"
        "v_mul_f32 v87, v124, v87\n"
        "v_mfma_f32_16x16x32_bf16 v[4:7], a[152:155], a[32:35], v[4:7]\n"
        "s_waitcnt lgkmcnt(8)\n"
        "buffer_store_dwordx2 v[232:233], v250, s[16:19], s39 offen offset:384 sc1\n"
        "ds_read_b128 a[88:91], v254 offset:12320\n"
        "v_cvt_pk_bf16_f32 v234, v217, v225\n"
        "v_mfma_f32_16x16x32_bf16 v[4:7], a[148:151], a[28:31], v[4:7]\n"
        "s_waitcnt lgkmcnt(8)\n"
        "buffer_store_dword v234, v238, s[16:19], s39 offen offset:384 sc1\n"
        "v_cvt_pk_bf16_f32 v82, v84, v85\n"
        "v_cvt_pk_bf16_f32 v83, v86, v87\n"
        "v_cvt_pk_bf16_f32 v232, v198, v202\n"
        "v_cvt_pk_bf16_f32 v233, v206, v194\n"
        "v_mfma_f32_16x16x32_bf16 v[4:7], a[144:147], a[24:27], v[4:7]\n"
        "s_waitcnt lgkmcnt(7)\n"
        "ds_read_b128 a[84:87], v254 offset:9248\n"
        "ds_read_b128 a[80:83], v254 offset:6176\n"
        "v_mul_f32 v96, v124, v96\n"
        "v_mul_f32 v97, v124, v97\n"
        "v_mul_f32 v98, v124, v98\n"
        "v_mul_f32 v99, v124, v99\n"
        "v_mfma_f32_16x16x32_bf16 v[8:11], a[144:147], a[48:51], 0\n"
        "s_waitcnt lgkmcnt(8)\n"
        "buffer_store_dwordx2 v[232:233], v250, s[16:19], s39 offen offset:768 sc1\n"
        "ds_read_b128 a[76:79], v254 offset:3104\n"
        "v_cvt_pk_bf16_f32 v234, v218, v226\n"
        "v_mfma_f32_16x16x32_bf16 v[8:11], a[148:151], a[52:55], v[8:11]\n"
        "s_waitcnt lgkmcnt(8)\n"
        "buffer_store_dword v234, v238, s[16:19], s39 offen offset:768 sc1\n"
        "v_cvt_pk_bf16_f32 v96, v96, v97\n"
        "v_cvt_pk_bf16_f32 v97, v98, v99\n"
        "v_mov_b32 v100, s29\n"
        "v_cvt_pk_bf16_f32 v232, v199, v203\n"
        "v_mfma_f32_16x16x32_bf16 v[8:11], a[152:155], a[56:59], v[8:11]\n"
        "s_waitcnt lgkmcnt(7)\n"
        "ds_write_b128 v245, v[80:83] offset:768\n"
        "ds_read_b128 a[72:75], v254 offset:32\n"
        "v_mfma_f32_16x16x32_bf16 v[8:11], a[156:159], a[60:63], v[8:11]\n"
        "s_waitcnt lgkmcnt(8)\n"
        "ds_read_b128 a[96:99], v248 offset:16\n"
        "ds_read_b128 a[100:103], v248 offset:3088\n"
        "v_cvt_pk_bf16_f32 v233, v207, v195\n"
        "v_cvt_pk_bf16_f32 v234, v219, v227\n"
        "v_max3_u32 v236, v236, v124, v125\n"
        "v_mfma_f32_16x16x32_bf16 v[8:11], a[160:163], a[64:67], v[8:11]\n"
        "s_waitcnt lgkmcnt(9)\n"
        "buffer_store_dwordx2 v[232:233], v250, s[16:19], s39 offen offset:1152 sc1\n"
        "ds_read_b128 a[104:107], v248 offset:6160\n"
        "v_mfma_f32_16x16x32_bf16 v[8:11], a[164:167], a[68:71], v[8:11]\n"
        "s_waitcnt lgkmcnt(9)\n"
        "buffer_store_dword v234, v238, s[16:19], s39 offen offset:1152 sc1\n"
        "v_cmp_lt_u32 vcc, s24, v236\n"
        "v_mfma_f32_16x16x32_bf16 v[12:15], a[164:167], a[92:95], 0\n"
        "s_waitcnt lgkmcnt(8)\n"
        "ds_write_b64 v246, v[96:97] offset:768\n"
        "ds_read_b128 a[108:111], v248 offset:9232\n"
        "s_cmp_lg_u64 vcc, 0\n"
        "s_addc_u32 s25, s25, s25\n"
        "v_mfma_f32_16x16x32_bf16 v[12:15], a[160:163], a[88:91], v[12:15]\n"
        "s_waitcnt lgkmcnt(9)\n"
        "ds_read_b128 a[112:115], v248 offset:12304\n"
        "ds_read_b128 a[116:119], v248 offset:15376\n"
        "v_mov_b32 v101, s25\n"
        "v_mfma_f32_16x16x32_bf16 v[12:15], a[156:159], a[84:87], v[12:15]\n"
        "s_waitcnt lgkmcnt(10)\n"
        "ds_read_b128 a[140:143], v254 offset:15376\n"
        "ds_read_b128 a[136:139], v254 offset:12304\n"
        "v_mfma_f32_16x16x32_bf16 v[12:15], a[152:155], a[80:83], v[12:15]\n"
        "s_waitcnt lgkmcnt(11)\n"
        "ds_write_b32 v100, v101\n"
        "v_mfma_f32_16x16x32_bf16 v[12:15], a[148:151], a[76:79], v[12:15]\n"
        "s_waitcnt lgkmcnt(10)\n"
        "ds_read_b128 a[132:135], v254 offset:9232\n"
        "ds_read_b128 a[128:131], v254 offset:6160\n"
        "v_mfma_f32_16x16x32_bf16 v[12:15], a[144:147], a[72:75], v[12:15]\n"
        "s_waitcnt lgkmcnt(2)\n"
        "s_barrier\n"
        "ds_read_b128 a[124:127], v254 offset:3088\n"
        "ds_read_b128 a[120:123], v254 offset:16\n"
        "v_mov_b32 v102, 147456\n"
        "v_mfma_f32_16x16x32_bf16 v[16:19], a[144:147], a[96:99], 0\n"
        "ds_read_b128 a[168:171], v247 offset:512\n"
        "ds_read_b128 a[172:175], v247 offset:4608\n"
        "v_mfma_f32_16x16x32_bf16 v[16:19], a[148:151], a[100:103], v[16:19]\n"
        "ds_read_b128 a[176:179], v247 offset:8704\n"
        "ds_read_b128 a[180:183], v247 offset:12800\n"
        "v_mfma_f32_16x16x32_bf16 v[16:19], a[152:155], a[104:107], v[16:19]\n"
        "ds_read_b128 a[184:187], v247 offset:16896\n"
        "ds_read_b128 a[188:191], v247 offset:20992\n"
        "v_mfma_f32_16x16x32_bf16 v[16:19], a[156:159], a[108:111], v[16:19]\n"
        "v_cvt_pk_bf16_f32 v48, v0, v4\n"
        "v_cvt_pk_bf16_f32 v49, v8, v12\n"
        "v_mfma_f32_16x16x32_bf16 v[16:19], a[160:163], a[112:115], v[16:19]\n"
        "v_cvt_pk_bf16_f32 v50, v1, v5\n"
        "v_cvt_pk_bf16_f32 v51, v9, v13\n"
        "v_mfma_f32_16x16x32_bf16 v[16:19], a[164:167], a[116:119], v[16:19]\n"
        "v_cvt_pk_bf16_f32 v52, v2, v6\n"
        "v_cvt_pk_bf16_f32 v53, v10, v14\n"
        "v_mfma_f32_16x16x32_bf16 v[20:23], a[164:167], a[140:143], 0\n"
        "v_cvt_pk_bf16_f32 v54, v3, v7\n"
        "v_cvt_pk_bf16_f32 v55, v11, v15\n"
        "v_mfma_f32_16x16x32_bf16 v[20:23], a[160:163], a[136:139], v[20:23]\n"
        "s_waitcnt lgkmcnt(9)\n"
        "ds_read_b128 v[104:107], v102\n"
        "v_mfma_f32_16x16x32_bf16 v[20:23], a[156:159], a[132:135], v[20:23]\n"
        "s_waitcnt lgkmcnt(9)\n"
        "s_add_u32 s38, s37, s43\n"
        "v_mfma_f32_16x16x32_bf16 v[20:23], a[152:155], a[128:131], v[20:23]\n"
        "s_waitcnt lgkmcnt(8)\n"
        "s_add_u32 s39, s28, s43\n"
        "v_mfma_f32_16x16x32_bf16 v[20:23], a[148:151], a[124:127], v[20:23]\n"
        "s_waitcnt lgkmcnt(7)\n"
        "v_mfma_f32_16x16x32_bf16 v[20:23], a[144:147], a[120:123], v[20:23]\n"
        "s_waitcnt lgkmcnt(6)\n"
        "v_mfma_f32_16x16x32_bf16 v[24:27], a[168:171], a[0:3], 0\n"
        "s_waitcnt lgkmcnt(5)\n"
        "v_mfma_f32_16x16x32_bf16 v[24:27], a[172:175], a[4:7], v[24:27]\n"
        "s_waitcnt lgkmcnt(4)\n"
        "v_mfma_f32_16x16x32_bf16 v[24:27], a[176:179], a[8:11], v[24:27]\n"
        "s_waitcnt lgkmcnt(3)\n"
        "v_mfma_f32_16x16x32_bf16 v[24:27], a[180:183], a[12:15], v[24:27]\n"
        "s_waitcnt lgkmcnt(2)\n"
        "v_mfma_f32_16x16x32_bf16 v[24:27], a[184:187], a[16:19], v[24:27]\n"
        "s_waitcnt lgkmcnt(1)\n"
        "v_mfma_f32_16x16x32_bf16 v[24:27], a[188:191], a[20:23], v[24:27]\n"
        "v_mfma_f32_16x16x32_bf16 v[28:31], a[188:191], a[44:47], 0\n"
        "v_mfma_f32_16x16x32_bf16 v[28:31], a[184:187], a[40:43], v[28:31]\n"
        "v_cvt_pk_bf16_f32 v56, v16, v20\n"
        "v_cvt_pk_bf16_f32 v58, v17, v21\n"
        "v_mfma_f32_16x16x32_bf16 v[28:31], a[180:183], a[36:39], v[28:31]\n"
        "v_cvt_pk_bf16_f32 v60, v18, v22\n"
        "v_cvt_pk_bf16_f32 v62, v19, v23\n"
        "v_mfma_f32_16x16x32_bf16 v[28:31], a[176:179], a[32:35], v[28:31]\n"
        "s_waitcnt lgkmcnt(0)\n"
        "v_or3_b32 v104, v104, v105, v106\n"
        "v_or_b32 v104, v104, v107\n"
        "v_mfma_f32_16x16x32_bf16 v[28:31], a[172:175], a[28:31], v[28:31]\n"
        "s_nop 0\n"
        "v_readfirstlane_b32 s25, v104\n"
        "v_mfma_f32_16x16x32_bf16 v[28:31], a[168:171], a[24:27], v[28:31]\n"
        "v_mfma_f32_16x16x32_bf16 v[32:35], a[168:171], a[48:51], 0\n"
        "v_mfma_f32_16x16x32_bf16 v[32:35], a[172:175], a[52:55], v[32:35]\n"
        "v_mfma_f32_16x16x32_bf16 v[32:35], a[176:179], a[56:59], v[32:35]\n"
        "v_mfma_f32_16x16x32_bf16 v[32:35], a[180:183], a[60:63], v[32:35]\n"
        "v_mfma_f32_16x16x32_bf16 v[32:35], a[184:187], a[64:67], v[32:35]\n"
        "v_mfma_f32_16x16x32_bf16 v[32:35], a[188:191], a[68:71], v[32:35]\n"
        "buffer_store_dwordx2 v[48:49], v250, s[16:19], s38 offen offset:0 sc1\n"
        "v_mfma_f32_16x16x32_bf16 v[36:39], a[188:191], a[92:95], 0\n"
        "v_cvt_pk_bf16_f32 v64, v24, v28\n"
        "v_cvt_pk_bf16_f32 v66, v25, v29\n"
        "v_mfma_f32_16x16x32_bf16 v[36:39], a[184:187], a[88:91], v[36:39]\n"
        "buffer_store_dwordx2 v[50:51], v250, s[16:19], s38 offen offset:384 sc1\n"
        "v_cvt_pk_bf16_f32 v68, v26, v30\n"
        "v_mfma_f32_16x16x32_bf16 v[36:39], a[180:183], a[84:87], v[36:39]\n"
        "v_cvt_pk_bf16_f32 v70, v27, v31\n"
        "v_mfma_f32_16x16x32_bf16 v[36:39], a[176:179], a[80:83], v[36:39]\n"
        "buffer_store_dwordx2 v[52:53], v250, s[16:19], s38 offen offset:768 sc1\n"
        "v_mfma_f32_16x16x32_bf16 v[36:39], a[172:175], a[76:79], v[36:39]\n"
        "v_mfma_f32_16x16x32_bf16 v[36:39], a[168:171], a[72:75], v[36:39]\n"
        "buffer_store_dwordx2 v[54:55], v250, s[16:19], s38 offen offset:1152 sc1\n"
        "s_nop 0\n"
        "v_mfma_f32_16x16x32_bf16 v[40:43], a[168:171], a[96:99], 0\n"
        "s_nop 2\n"
        "v_mfma_f32_16x16x32_bf16 v[40:43], a[172:175], a[100:103], v[40:43]\n"
        "buffer_store_dword v56, v238, s[16:19], s38 offen offset:0 sc1\n"
        "v_mfma_f32_16x16x32_bf16 v[40:43], a[176:179], a[104:107], v[40:43]\n"
        "v_cvt_pk_bf16_f32 v65, v32, v36\n"
        "v_cvt_pk_bf16_f32 v67, v33, v37\n"
        "v_mfma_f32_16x16x32_bf16 v[40:43], a[180:183], a[108:111], v[40:43]\n"
        "buffer_store_dword v58, v238, s[16:19], s38 offen offset:384 sc1\n"
        "v_mfma_f32_16x16x32_bf16 v[40:43], a[184:187], a[112:115], v[40:43]\n"
        "v_cvt_pk_bf16_f32 v69, v34, v38\n"
        "v_cvt_pk_bf16_f32 v71, v35, v39\n"
        "v_mfma_f32_16x16x32_bf16 v[40:43], a[188:191], a[116:119], v[40:43]\n"
        "buffer_store_dword v60, v238, s[16:19], s38 offen offset:768 sc1\n"
        "v_mfma_f32_16x16x32_bf16 v[44:47], a[188:191], a[140:143], 0\n"
        "buffer_store_dword v62, v238, s[16:19], s38 offen offset:1152 sc1\n"
        "v_mfma_f32_16x16x32_bf16 v[44:47], a[184:187], a[136:139], v[44:47]\n"
        "buffer_store_dwordx2 v[64:65], v250, s[16:19], s39 offen offset:0 sc1\n"
        "v_mfma_f32_16x16x32_bf16 v[44:47], a[180:183], a[132:135], v[44:47]\n"
        "buffer_store_dwordx2 v[66:67], v250, s[16:19], s39 offen offset:384 sc1\n"
        "v_mfma_f32_16x16x32_bf16 v[44:47], a[176:179], a[128:131], v[44:47]\n"
        "buffer_store_dwordx2 v[68:69], v250, s[16:19], s39 offen offset:768 sc1\n"
        "v_mfma_f32_16x16x32_bf16 v[44:47], a[172:175], a[124:127], v[44:47]\n"
        "buffer_store_dwordx2 v[70:71], v250, s[16:19], s39 offen offset:1152 sc1\n"
        "v_mfma_f32_16x16x32_bf16 v[44:47], a[168:171], a[120:123], v[44:47]\n"
        "s_nop 2\n"
        "s_nop 4\n"
        "v_cvt_pk_bf16_f32 v72, v40, v44\n"
        "v_cvt_pk_bf16_f32 v74, v41, v45\n"
        "buffer_store_dword v72, v238, s[16:19], s39 offen offset:0 sc1\n"
        "v_cvt_pk_bf16_f32 v76, v42, v46\n"
        "buffer_store_dword v74, v238, s[16:19], s39 offen offset:384 sc1\n"
        "v_cvt_pk_bf16_f32 v78, v43, v47\n"
        "buffer_store_dword v76, v238, s[16:19], s39 offen offset:768 sc1\n"
        "buffer_store_dword v78, v238, s[16:19], s39 offen offset:1152 sc1\n"
        "s_cmp_lg_u32 s25, 0\n"
        "s_cbranch_scc1 30f\n"
        "s_endpgm\n"
        "30:\n"
        : "={s25}"(flg), "={s42}"(w), "+{s[0:1]}"(ka), "+{s2}"(bid), "+{v0}"(tid)
        : "s"(ldsb)
        : "v1", "v2", "v3", "v4", "v5", "v6", "v7", "v8", "v9", "v10", "v11", "v12", "v13", "v14", "v15", "v16", "v17", "v18", "v19", "v20", "v21", "v22", "v23", "v24", "v25", "v26", "v27", "v28", "v29", "v30", "v31", "v32", "v33", "v34", "v35", "v36", "v37", "v38", "v39", "v40", "v41", "v42", "v43", "v44", "v45", "v46", "v47", "v48", "v49", "v50", "v51", "v52", "v53", "v54", "v55", "v56", "v57", "v58", "v59", "v60", "v61", "v62", "v63", "v64", "v65", "v66", "v67", "v68", "v69", "v70", "v71", "v72", "v73", "v74", "v75", "v76", "v77", "v78", "v79", "v80", "v81", "v82", "v83", "v84", "v85", "v86", "v87", "v88", "v89", "v90", "v91", "v92", "v93", "v94", "v95", "v96", "v97", "v98", "v99", "v100", "v101", "v102", "v103", "v104", "v105", "v106", "v107", "v108", "v109", "v110", "v111", "v112", "v113", "v114", "v115", "v116", "v117", "v118", "v119", "v120", "v121", "v122", "v123", "v124", "v125", "v126", "v127", "v128", "v129", "v130", "v131", "v132", "v133", "v134", "v135", "v136", "v137", "v138", "v139", "v140", "v141", "v142", "v143", "v144", "v145", "v146", "v147", "v148", "v149", "v150", "v151", "v152", "v153", "v154", "v155", "v156", "v157", "v158", "v159", "v160", "v161", "v162", "v163", "v164", "v165", "v166", "v167", "v168", "v169", "v170", "v171", "v172", "v173", "v174", "v175", "v176", "v177", "v178", "v179", "v180", "v181", "v182", "v183", "v184", "v185", "v186", "v187", "v188", "v189", "v190", "v191", "v192", "v193", "v194", "v195", "v196", "v197", "v198", "v199", "v200", "v201", "v202", "v203", "v204", "v205", "v206", "v207", "v208", "v209", "v210", "v211", "v212", "v213", "v214", "v215", "v216", "v217", "v218", "v219", "v220", "v221", "v222", "v223", "v224", "v225", "v226", "v227", "v228", "v229", "v230", "v231", "v232", "v233", "v234", "v235", "v236", "v237", "v238", "v239", "v240", "v241", "v242", "v243", "v244", "v245", "v246", "v247", "v248", "v249", "v250", "v251", "v252", "v253", "v254", "v255", "a0", "a1", "a2", "a3", "a4", "a5", "a6", "a7", "a8", "a9", "a10", "a11", "a12", "a13", "a14", "a15", "a16", "a17", "a18", "a19", "a20", "a21", "a22", "a23", "a24", "a25", "a26", "a27", "a28", "a29", "a30", "a31", "a32", "a33", "a34", "a35", "a36", "a37", "a38", "a39", "a40", "a41", "a42", "a43", "a44", "a45", "a46", "a47", "a48", "a49", "a50", "a51", "a52", "a53", "a54", "a55", "a56", "a57", "a58", "a59", "a60", "a61", "a62", "a63", "a64", "a65", "a66", "a67", "a68", "a69", "a70", "a71", "a72", "a73", "a74", "a75", "a76", "a77", "a78", "a79", "a80", "a81", "a82", "a83", "a84", "a85", "a86", "a87", "a88", "a89", "a90", "a91", "a92", "a93", "a94", "a95", "a96", "a97", "a98", "a99", "a100", "a101", "a102", "a103", "a104", "a105", "a106", "a107", "a108", "a109", "a110", "a111", "a112", "a113", "a114", "a115", "a116", "a117", "a118", "a119", "a120", "a121", "a122", "a123", "a124", "a125", "a126", "a127", "a128", "a129", "a130", "a131", "a132", "a133", "a134", "a135", "a136", "a137", "a138", "a139", "a140", "a141", "a142", "a143", "a144", "a145", "a146", "a147", "a148", "a149", "a150", "a151", "a152", "a153", "a154", "a155", "a156", "a157", "a158", "a159", "a160", "a161", "a162", "a163", "a164", "a165", "a166", "a167", "a168", "a169", "a170", "a171", "a172", "a173", "a174", "a175", "a176", "a177", "a178", "a179", "a180", "a181", "a182", "a183", "a184", "a185", "a186", "a187", "a188", "a189", "a190", "a191", "a192", "a193", "a194", "a195", "a196", "a197", "a198", "a199", "a200", "a201", "a202", "a203", "a204", "a205", "a206", "a207", "a208", "a209", "a210", "a211", "a212", "a213", "a214", "a215", "a216", "a217", "a218", "a219", "a220", "a221", "a222", "a223", "a224", "a225", "a226", "a227", "a228", "a229", "a230", "a231", "a232", "a233", "a234", "a235", "a236", "a237", "a238", "a239", "a240", "a241", "a242", "a243", "a244", "a245", "a246", "a247", "a248", "a249", "a250", "a251", "a252", "a253", "a254", "a255", "s8", "s9", "s10", "s11", "s12", "s13", "s14", "s15", "s16", "s17", "s18", "s19", "s20", "s21", "s22", "s23", "s24", "s26", "s27", "s28", "s29", "s30", "s31", "s34", "s35", "s36", "s37", "s38", "s39", "s40", "s41", "s43", "s44", "s45", "s46", "s47", "s48", "s49", "s50", "s51", "s52", "s53", "s54", "s55", "s56", "s57", "s58", "s59", "s60", "s61", "s62", "s63", "s64", "s65", "s66", "s67", "s68", "s69", "s70", "s71", "s72", "s73", "s74", "s75", "s76", "s77", "s78", "s79", "s80", "s81", "s82", "s83", "vcc", "scc", "memory");
    if (flg) {
        unsigned ln;
        asm volatile("s_waitcnt vmcnt(0)\n v_mbcnt_lo_u32_b32 %0, -1, 0\n v_mbcnt_hi_u32_b32 %0, -1, %0" : "=v"(ln) :: "memory");
        const unsigned t = w * 64u + ln, wx = bid & 63u, wy0 = bid >> 6;
        typedef const char* __attribute__((address_space(4))) const* kargp;
        const char *x = ((kargp)ka)[0], *wqkv = ((kargp)ka)[1], *wo = ((kargp)ka)[2], *rpb = ((kargp)ka)[3], *rpi = ((kargp)ka)[4],
                   *mask = ((kargp)ka)[5];
        char* out = (char*)((kargp)ka)[6];
        unsigned short* xs = (unsigned short*)smem;
        unsigned short* qs = xs + 64 * 192;
        const unsigned short* W1 = (const unsigned short*)wqkv;
        const unsigned short* W2 = (const unsigned short*)wo;
        for (unsigned i = 0; i < 16u; ++i) {
            if (!((flg >> (15u - i)) & 1u)) continue;
            const unsigned wy = wy0 + 4u * i;
            __syncthreads();
            for (unsigned e = t; e < 64u * 96u; e += 256u) {
                const unsigned tok = e / 96u, c2 = e % 96u;
                const unsigned pix = ((8u * wy + 4u + (tok >> 3)) & 511u) * 512u + ((8u * wx + 4u + (tok & 7u)) & 511u);
                ((unsigned*)xs)[e] = ((const unsigned*)x)[pix * 96u + c2];
            }
            __syncthreads();
            for (unsigned col = t; col < 576u; col += 256u)
                for (unsigned tb = 0; tb < 64u; tb += 8u) {
                    float a[8] = {0.f, 0.f, 0.f, 0.f, 0.f, 0.f, 0.f, 0.f};
                    for (unsigned c = 0; c < 192u; ++c) {
                        const float wt = bf2f(W1[c * 576u + col]);
#pragma unroll
                        for (int j = 0; j < 8; ++j) a[j] += bf2f(xs[(tb + j) * 192u + c]) * wt;
                    }
#pragma unroll
                    for (int j = 0; j < 8; ++j) qs[(tb + j) * 578u + col] = f2bf(a[j]);
                }
            __syncthreads();
            {
                float qv[48], o[48];
#pragma unroll
                for (int d = 0; d < 48; ++d) { qv[d] = bf2f(qs[ln * 578u + w * 48u + d]); o[d] = 0.f; }
                const unsigned short* mrow = (const unsigned short*)mask + (size_t)(wy * 64u + wx) * 4096u + ln * 64u;
                float mx = -__builtin_inff(), l = 0.f;
                for (int pass = 0; pass < 2; ++pass)
                    for (unsigned k = 0; k < 64u; ++k) {
                        float s = 0.f;
#pragma unroll
                        for (int d = 0; d < 48; ++d) s += qv[d] * bf2f(qs[k * 578u + 192u + w * 48u + d]);
                        s = s * 0.14433756729740643f + bf2f(((const unsigned short*)rpb)[((const unsigned*)rpi)[ln * 64u + k] * 4u + w]) + bf2f(mrow[k]);
                        if (pass == 0) mx = fmaxf(mx, s);
                        else {
                            const float p = __expf(s - mx);
                            l += p;
#pragma unroll
                            for (int d = 0; d < 48; ++d) o[d] += p * bf2f(qs[k * 578u + 384u + w * 48u + d]);
                        }
                    }
#pragma unroll
                for (int d = 0; d < 48; ++d) xs[ln * 192u + w * 48u + d] = f2bf(o[d] / l);
            }
            __syncthreads();
            if (t < 192u)
                for (unsigned tb = 0; tb < 64u; tb += 8u) {
                    float a[8] = {0.f, 0.f, 0.f, 0.f, 0.f, 0.f, 0.f, 0.f};
                    for (unsigned c = 0; c < 192u; ++c) {
                        const float wt = bf2f(W2[c * 192u + t]);
#pragma unroll
                        for (int j = 0; j < 8; ++j) a[j] += bf2f(xs[(tb + j) * 192u + c]) * wt;
                    }
                    const unsigned row = (8u * wy + 4u + (tb >> 3)) & 511u;
#pragma unroll
                    for (int j = 0; j < 8; ++j)
                        ((unsigned short*)out)[(row * 512u + ((8u * wx + 4u + j) & 511u)) * 192u + t] = f2bf(a[j]);
                }
        }
    }
}


extern "C" void kernel_launch(void* const* d_in, const int* in_sizes, int n_in, void* d_out, int out_size, void* d_ws,
                              size_t ws_size, hipStream_t stream) {
    win_attn_kernel<<<256, 256, 0, stream>>>((const char*)d_in[0], (const char*)d_in[1], (const char*)d_in[2],
                                             (const char*)d_in[3], (const char*)d_in[4], (const char*)d_in[5],
                                             (char*)d_out, (char*)d_ws);

}
```

```cpp
#include <hip/hip_runtime.h>
#include <hip/hip_bf16.h>

typedef unsigned int u32x4 __attribute__((ext_vector_type(4)));

static __device__ __forceinline__ float bf2f(unsigned short u) { return __uint_as_float((unsigned)u << 16); }
static __device__ __forceinline__ unsigned short f2bf(float f) { return __builtin_bit_cast(unsigned short, (__bf16)f); }

__global__ __launch_bounds__(256, 1) __attribute__((aligned(4096))) void win_attn_kernel(const char* x_, const char* wqkv_, const char* wo_, const char* rpb_,
                                                          const char* rpi_, const char* mask_, char* out_, char* ws) {
    __shared__ __attribute__((aligned(16))) char smem[163840];
    const void* ka = (const void*)__builtin_amdgcn_kernarg_segment_ptr();
    unsigned bid = blockIdx.x, tid = threadIdx.x, flg, w;
    unsigned ldsb = (unsigned)(size_t)smem;
    asm volatile(
        "s_load_dwordx16 s[68:83], s[0:1], 0\n"
        "v_readfirstlane_b32 s42, v0\n"
        "v_and_b32 v1, 63, v0\n"
        "s_lshr_b32 s31, s2, 1\n"
        "s_lshr_b32 s42, s42, 6\n"
        "s_and_b32 s31, s31, 0x7c\n"
        "v_lshlrev_b32 v2, 4, v1\n"
        "s_add_u32 s31, s31, s42\n"
        "s_and_b32 s35, s31, 31\n"
        "s_min_u32 s35, s35, 31\n"
        "s_getpc_b64 s[66:67]\n"
        "v_lshl_or_b32 v10, s35, 10, v2\n"
        "global_load_dwordx4 v[60:63], v10, s[66:67]\n"
        "s_lshl_b32 s50, s31, 6\n"
        "s_mul_i32 s50, s50, 27\n"
        "v_min_u32 v136, 26, v1\n"
        "v_lshl_add_u32 v136, v136, 6, s50\n"
        "v_mul_u32_u24 v11, 171, v1\n"
        "v_lshrrev_b32 v4, 10, v11\n"
        "v_bfe_u32 v7, v11, 13, 1\n"
        "v_add_u32 v11, 10944, v11\n"
        "v_lshrrev_b32 v5, 10, v11\n"
        "v_bfe_u32 v8, v11, 13, 1\n"
        "v_add_u32 v11, 10944, v11\n"
        "v_lshrrev_b32 v6, 10, v11\n"
        "v_bfe_u32 v9, v11, 13, 1\n"
        "s_mul_i32 s40, s42, 3072\n"
        "s_mov_b32 s10, 100663296\n"
        "s_mov_b32 s11, 0x20000\n"
        "s_mov_b32 s58, 16384\n"
        "s_mov_b32 s59, 0x20000\n"
        "s_mov_b32 s62, 1800\n"
        "s_mov_b32 s63, 0x20000\n"
        "s_mov_b32 s22, 221184\n"
        "s_mov_b32 s23, 0x20000\n"
        "s_and_b32 s51, s42, 1\n"
        "v_lshlrev_b32 v134, 3, v0\n"
        "s_lshl_b32 s35, s51, 7\n"
        "v_and_b32 v10, 48, v1\n"
        "v_xor_b32 v131, s35, v2\n"
        "s_lshl_b32 s46, s42, 12\n"
        "v_xor_b32 v131, v131, v10\n"
        "v_xor_b32 v132, 64, v131\n"
        "s_and_b32 s31, s2, 63\n"
        "s_lshl_b32 s31, s31, 3\n"
        "s_add_u32 s31, s31, 4\n"
        "s_lshl_b32 s35, s42, 5\n"
        "s_movk_i32 s41, 384\n"
        "s_movk_i32 s45, -384\n"
        "v_lshrrev_b32 v10, 2, v4\n"
        "v_mad_i32_i24 v11, v10, s45, v2\n"
        "v_add_u32 v10, s31, v10\n"
        "v_lshl_or_b32 v242, v7, 4, s35\n"
        "v_and_b32 v10, 511, v10\n"
        "v_xor_b32 v11, v11, v242\n"
        "v_mad_u32_u24 v242, v10, s41, v11\n"
        "v_lshrrev_b32 v10, 2, v5\n"
        "v_mad_i32_i24 v11, v10, s45, v2\n"
        "v_add_u32 v10, s31, v10\n"
        "v_add_u32 v11, 1024, v11\n"
        "v_lshl_or_b32 v243, v8, 4, s35\n"
        "v_and_b32 v10, 511, v10\n"
        "v_xor_b32 v11, v11, v243\n"
        "v_mad_u32_u24 v243, v10, s41, v11\n"
        "v_lshrrev_b32 v10, 2, v6\n"
        "v_mad_i32_i24 v11, v10, s45, v2\n"
        "v_add_u32 v10, s31, v10\n"
        "v_add_u32 v11, 2048, v11\n"
        "v_lshl_or_b32 v244, v9, 4, s35\n"
        "v_and_b32 v10, 511, v10\n"
        "v_xor_b32 v11, v11, v244\n"
        "v_mad_u32_u24 v244, v10, s41, v11\n"
        "s_mul_i32 s43, s31, 384\n"
        "s_add_u32 s31, s31, 4\n"
        "s_and_b32 s31, s31, 511\n"
        "s_mul_i32 s44, s31, 384\n"
        "s_lshr_b32 s31, s2, 6\n"
        "s_lshl_b32 s31, s31, 3\n"
        "s_add_u32 s47, s31, s42\n"
        "s_add_u32 s47, s47, 68\n"
        "s_mul_i32 s47, s47, 196608\n"
        "s_lshl2_add_u32 s37, s51, s31\n"
        "s_add_u32 s37, s37, 4\n"
        "s_mul_i32 s37, s37, 196608\n"
        "s_mul_i32 s65, s42, 18432\n"
        "s_mov_b32 s14, 33554432\n"
        "s_mov_b32 s15, 0x20000\n"
        "s_mul_i32 s35, s42, 96\n"
        "s_movk_i32 s31, 1056\n"
        "s_movk_i32 s45, 4608\n"
        "v_add_u32 v3, s35, v2\n"
        "s_waitcnt lgkmcnt(0)\n"
        "s_mov_b64 s[60:61], s[74:75]\n"
        "s_mov_b64 s[20:21], s[70:71]\n"
        "s_mov_b64 s[56:57], s[76:77]\n"
        "s_mov_b64 s[8:9], s[68:69]\n"
        "buffer_load_dwordx2 v[144:145], v134, s[60:63], 0 offen\n"
        "s_add_u32 m0, s46, 122880\n"
        "s_mov_b32 s64, s46\n"
        "buffer_load_dword v136, s[20:23], 0 offen lds\n"
        "s_mov_b64 s[52:53], s[72:73]\n"
        "s_mov_b64 s[12:13], s[78:79]\n"
        "buffer_load_dwordx4 v131, s[56:59], s64 offen lds\n"
        "s_mov_b64 s[16:17], s[80:81]\n"
        "v_bfe_u32 v12, v4, 2, 1\n"
        "v_mad_u32_u24 v128, v4, s31, v3\n"
        "v_sub_u32 v12, v12, v7\n"
        "v_mad_i32_i24 v128, v12, s45, v128\n"
        "v_bfe_u32 v13, v5, 2, 1\n"
        "buffer_load_dwordx4 v131, s[56:59], s64 offen offset:1024 lds\n"
        "v_mad_u32_u24 v129, v5, s31, v3\n"
        "v_sub_u32 v13, v13, v8\n"
        "v_mad_i32_i24 v129, v13, s45, v129\n"
        "v_bfe_u32 v14, v6, 2, 1\n"
        "v_mad_u32_u24 v130, v6, s31, v3\n"
        "v_sub_u32 v14, v14, v9\n"
        "v_mad_i32_i24 v130, v14, s45, v130\n"
        "s_lshl_b32 s48, s42, 11\n"
        "s_lshl_b32 s36, s2, 13\n"
        "v_and_b32 v19, 16, v1\n"
        "buffer_load_dwordx4 v132, s[56:59], s64 offen offset:2048 lds\n"
        "s_add_u32 s36, s36, s48\n"
        "s_add_u32 s36, s36, 4194304\n"
        "s_add_u32 s48, s48, 155648\n"
        "s_lshl_b32 s35, s51, 6\n"
        "v_xor_b32 v249, v2, v19\n"
        "v_xor_b32 v249, s35, v249\n"
        "v_xor_b32 v253, 32, v249\n"
        "v_and_b32 v15, 15, v1\n"
        "v_lshrrev_b32 v16, 4, v1\n"
        "v_and_b32 v17, 12, v1\n"
        "buffer_load_dwordx4 v132, s[56:59], s64 offen offset:3072 lds\n"
        "v_and_b32 v18, 0xf0, v2\n"
        "v_add_u32 v17, v17, v15\n"
        "v_bfe_u32 v10, v1, 2, 3\n"
        "v_lshrrev_b32 v11, 5, v1\n"
        "v_lshl_or_b32 v10, v11, 4, v10\n"
        "v_and_b32 v255, 3, v1\n"
        "v_mul_u32_u24 v10, 96, v10\n"
        "v_lshl_add_u32 v255, v255, 3, s65\n"
        "v_add_u32 v255, v255, v10\n"
        "v_lshrrev_b32 v20, 1, v15\n"
        "s_add_u32 m0, s40, 73728\n"
        "s_sub_u32 s64, s47, 12582912\n"
        "buffer_load_dwordx4 v242, s[8:11], s64 offen lds\n"
        "v_xor_b32 v20, v20, v16\n"
        "v_lshlrev_b32 v20, 4, v20\n"
        "v_mad_u32_u24 v240, v17, s41, v20\n"
        "v_lshl_or_b32 v251, v17, 7, v20\n"
        "v_add_u32 v240, 73728, v240\n"
        "v_add_u32 v251, 155648, v251\n"
        "v_xor_b32 v241, 64, v240\n"
        "v_xor_b32 v252, 64, v251\n"
        "s_lshl_b32 s67, s42, 10\n"
        "s_add_u32 s67, s67, 139264\n"
        "s_add_u32 m0, s40, 74752\n"
        "s_nop 0\n"
        "buffer_load_dwordx4 v243, s[8:11], s64 offen lds\n"
        "v_lshlrev_b32 v135, 2, v0\n"
        "v_add_u32 v135, 139264, v135\n"
        "v_lshlrev_b32 v11, 5, v16\n"
        "v_xor_b32 v11, v11, v18\n"
        "v_lshl_or_b32 v11, v17, 8, v11\n"
        "v_add_u32 v140, 122880, v11\n"
        "v_xor_b32 v141, 16, v140\n"
        "v_xor_b32 v142, 128, v140\n"
        "v_xor_b32 v143, 144, v140\n"
        "v_lshrrev_b32 v10, 2, v15\n"
        "s_add_u32 m0, s40, 75776\n"
        "s_nop 0\n"
        "buffer_load_dwordx4 v244, s[8:11], s64 offen lds\n"
        "v_cmp_eq_u32 vcc, v10, v16\n"
        "v_mov_b32 v11, 0x3fb9\n"
        "v_mov_b32 v189, 0\n"
        "v_and_b32 v10, 48, v2\n"
        "v_cndmask_b32 v188, 0, v11, vcc\n"
        "v_lshlrev_b64 v[188:189], v10, v[188:189]\n"
        "v_cmp_gt_u32 vcc, 32, v1\n"
        "v_mov_b32 v11, 15957\n"
        "v_lshrrev_b32 v149, 2, v15\n"
        "v_lshrrev_b32 v151, 3, v19\n"
        "v_mov_b32 v148, 14702\n"
        "v_cndmask_b32 v11, v148, v11, vcc\n"
        "v_xor_b32 v151, v149, v151\n"
        "v_cmp_eq_u32 vcc, 0, v151\n"
        "v_mov_b32 v149, 0\n"
        "v_cndmask_b32 v148, 0, v11, vcc\n"
        "v_lshlrev_b64 v[148:149], v10, v[148:149]\n"
        "v_cmp_eq_u32 vcc, 1, v151\n"
        "v_mov_b32 v151, 0\n"
        "v_cndmask_b32 v150, 0, v11, vcc\n"
        "v_lshlrev_b64 v[150:151], v10, v[150:151]\n"
        "v_and_b32 v10, 8, v1\n"
        "v_add_u32 v10, v10, v15\n"
        "v_mul_u32_u24 v10, 96, v10\n"
        "v_add3_u32 v137, v10, v19, s65\n"
        "v_and_b32 v237, 1, v1\n"
        "v_mul_u32_u24 v237, 0xaaaa, v237\n"
        "v_mov_b64 v[170:171], v[188:189]\n"
        "v_mov_b64 v[210:211], v[188:189]\n"
        "v_mov_b64 v[228:229], v[188:189]\n"
        "v_add_u32 v237, 0x4444, v237\n"
        "v_mov_b32 v11, 0x3f803f80\n"
        "v_accvgpr_write_b32 a252, v11\n"
        "v_accvgpr_write_b32 a253, v11\n"
        "v_accvgpr_write_b32 a254, v11\n"
        "v_accvgpr_write_b32 a255, v11\n"
        "v_lshl_or_b32 v10, v16, 10, v18\n"
        "s_lshl_b32 s31, s42, 12\n"
        "s_add_u32 s31, s31, 73728\n"
        "s_lshl_b32 s35, s51, 9\n"
        "s_add_u32 s35, s35, 73728\n"
        "v_add_u32 v245, s31, v10\n"
        "s_sub_u32 s50, s35, s31\n"
        "v_lshrrev_b32 v10, 5, v1\n"
        "v_lshrrev_b32 v11, 1, v19\n"
        "s_sub_u32 s31, s48, 65536\n"
        "v_lshl_or_b32 v10, v10, 10, v18\n"
        "v_add3_u32 v246, v10, v11, s31\n"
        "v_mul_u32_u24 v10, 384, v16\n"
        "s_movk_i32 s45, 1728\n"
        "v_lshrrev_b32 v235, 2, v15\n"
        "v_lshrrev_b32 v254, 1, v15\n"
        "v_and_b32 v11, 1, v1\n"
        "v_mad_u32_u24 v235, v235, 48, v10\n"
        "v_and_b32 v248, 1, v254\n"
        "v_mad_u32_u24 v235, v248, s45, v235\n"
        "v_mul_u32_u24 v248, 36, v11\n"
        "v_lshlrev_b32 v11, 5, v11\n"
        "v_xor_b32 v254, v254, v248\n"
        "v_add3_u32 v235, v235, v11, s65\n"
        "v_mad_u32_u24 v254, v254, 48, v10\n"
        "v_add3_u32 v254, v254, 16, s65\n"
        "s_lshr_b32 s31, s42, 1\n"
        "s_mul_i32 s35, s31, 36864\n"
        "s_movk_i32 s45, 18048\n"
        "v_mad_u32_u24 v10, v15, 48, v10\n"
        "v_lshrrev_b32 v248, 3, v15\n"
        "v_mad_u32_u24 v10, v248, s45, v10\n"
        "v_add_u32 v248, s35, v10\n"
        "s_mul_i32 s49, s31, -192\n"
        "s_add_u32 s49, s49, 128\n"
        "s_mul_i32 s31, s31, 256\n"
        "v_mul_u32_u24 v10, 196608, v16\n"
        "v_mul_u32_u24 v11, 8, v15\n"
        "v_add3_u32 v250, v10, v11, s31\n"
        "s_lshl_b32 s35, s51, 6\n"
        "s_add_u32 s66, s31, s35\n"
        "s_lshl_b32 s35, s42, 5\n"
        "s_sub_u32 s35, s35, s66\n"
        "s_add_u32 s27, s35, 64\n"
        "s_movk_i32 s31, 288\n"
        "s_movk_i32 s35, 3072\n"
        "s_movk_i32 s41, 1536\n"
        "s_movk_i32 s45, 12288\n"
        "v_mad_u32_u24 v10, v4, s31, v2\n"
        "v_bfe_u32 v11, v10, 6, 1\n"
        "v_add_u32 v10, s66, v10\n"
        "v_mad_i32_i24 v10, v11, s27, v10\n"
        "v_lshrrev_b32 v11, 4, v4\n"
        "v_mad_i32_i24 v238, v12, s41, v10\n"
        "v_sub_u32 v7, v7, v11\n"
        "v_mad_u32_u24 v238, v11, s45, v238\n"
        "v_mad_i32_i24 v232, v7, s35, v10\n"
        "v_mad_u32_u24 v10, v5, s31, v2\n"
        "v_bfe_u32 v11, v10, 6, 1\n"
        "v_add_u32 v10, s66, v10\n"
        "v_mad_i32_i24 v10, v11, s27, v10\n"
        "v_lshrrev_b32 v11, 4, v5\n"
        "v_mad_i32_i24 v239, v13, s41, v10\n"
        "v_sub_u32 v8, v8, v11\n"
        "v_mad_u32_u24 v239, v11, s45, v239\n"
        "v_mad_i32_i24 v233, v8, s35, v10\n"
        "v_mad_u32_u24 v10, v6, s31, v2\n"
        "v_bfe_u32 v11, v10, 6, 1\n"
        "v_add_u32 v10, s66, v10\n"
        "v_mad_i32_i24 v10, v11, s27, v10\n"
        "v_lshrrev_b32 v11, 4, v6\n"
        "v_mad_i32_i24 v247, v14, s41, v10\n"
        "v_sub_u32 v9, v9, v11\n"
        "v_mad_u32_u24 v247, v11, s45, v247\n"
        "v_mad_i32_i24 v234, v9, s35, v10\n"
        "s_add_u32 s38, s37, s43\n"
        "s_mov_b32 s26, 0x3fb8aa3b\n"
        "s_mov_b32 s24, 0x5d800000\n"
        "s_mov_b32 s27, 14\n"
        "s_mov_b32 s28, 98304\n"
        "s_mov_b32 s29, 73728\n"
        "s_mov_b32 s30, 122880\n"
        "s_mov_b32 s45, s50\n"
        "s_mov_b32 s25, 0\n"
        "s_mov_b32 s54, 73728\n"
        "s_mov_b32 s55, 0x20000\n"
        "s_mov_b32 s18, 100663296\n"
        "s_mov_b32 s19, 0x20000\n"
        "s_mov_b32 s56, 36864\n"
        "s_mov_b32 s57, 73728\n"
        "s_mov_b32 s58, 110592\n"
        "s_mov_b32 s59, 147456\n"
        "s_mov_b32 s60, 184320\n"
        "s_mov_b32 s61, 18432\n"
        "s_mov_b32 s62, 55296\n"
        "s_mov_b32 s63, 12288\n"
        "s_mov_b32 s34, 49152\n"
        "s_add_u32 m0, s40, 86016\n"
        "s_sub_u32 s64, s47, 11796480\n"
        "buffer_load_dwordx4 v242, s[8:11], s64 offen lds\n"
        "s_waitcnt vmcnt(9)\n"
        "v_and_b32 v146, 0xffff0000, v144\n"
        "v_and_b32 v147, 0xffff0000, v145\n"
        "v_lshlrev_b32 v144, 16, v144\n"
        "v_lshlrev_b32 v145, 16, v145\n"
        "s_add_u32 m0, s40, 87040\n"
        "v_mul_f32 v146, s26, v146\n"
        "buffer_load_dwordx4 v243, s[8:11], s64 offen lds\n"
        "v_mul_f32 v147, s26, v147\n"
        "v_mul_f32 v144, s26, v144\n"
        "v_mul_f32 v145, s26, v145\n"
        "ds_write2st64_b32 v135, v144, v146 offset0:0 offset1:4\n"
        "ds_write2st64_b32 v135, v145, v147 offset0:8 offset1:12\n"
        "s_cmp_eq_u32 s42, 0\n"
        "s_cbranch_scc1 41f\n"
        "s_cmp_eq_u32 s42, 1\n"
        "s_cbranch_scc1 42f\n"
        "s_cmp_eq_u32 s42, 2\n"
        "s_cbranch_scc1 43f\n"
        "s_cmp_eq_u32 s42, 3\n"
        "s_cbranch_scc1 44f\n"
        "60:\n"
        "s_add_u32 m0, s40, 88064\n"
        "s_nop 0\n"
        "buffer_load_dwordx4 v244, s[8:11], s64 offen lds\n"
        "s_waitcnt lgkmcnt(0)\n"
        "s_waitcnt vmcnt(6)\n"
        "s_barrier\n"
        "s_add_u32 m0, s65, 0\n"
        "ds_read_b128 v[0:3], v140 offset:0\n"
        "buffer_load_dwordx4 v128, s[20:23], 0 offen lds\n"
        "buffer_load_dwordx4 v129, s[20:23], 0 offen offset:1024 lds\n"
        "ds_read_b128 v[4:7], v141 offset:0\n"
        "ds_read_b128 v[16:19], v140 offset:1024\n"
        "ds_read_b128 v[20:23], v141 offset:1024\n"
        "ds_read_b128 v[8:11], v142 offset:0\n"
        "ds_read_b128 v[24:27], v142 offset:1024\n"
        "ds_read_b128 v[12:15], v143 offset:0\n"
        "ds_read_b128 v[28:31], v143 offset:1024\n"
        "ds_read_b128 v[32:35], v140 offset:8192\n"
        "ds_read_b128 v[36:39], v141 offset:8192\n"
        "ds_read_b128 v[40:43], v142 offset:8192\n"
        "s_cmp_eq_u32 s42, 0\n"
        "s_cbranch_scc1 51f\n"
        "s_cmp_eq_u32 s42, 1\n"
        "s_cbranch_scc1 54f\n"
        "s_cmp_eq_u32 s42, 2\n"
        "s_cbranch_scc1 45f\n"
        "s_cmp_eq_u32 s42, 3\n"
        "s_cbranch_scc1 47f\n"
        "61:\n"
        "buffer_load_dwordx4 v130, s[20:23], 0 offen offset:2048 lds\n"
        "s_add_u32 m0, s65, 2688\n"
        "s_waitcnt lgkmcnt(10)\n"
        "v_lshl_add_u32 v0, v0, 2, s67\n"
        "buffer_load_dwordx4 v128, s[20:23], 0 offen offset:384 lds\n"
        "v_lshl_add_u32 v1, v1, 2, s67\n"
        "v_lshl_add_u32 v2, v2, 2, s67\n"
        "v_lshl_add_u32 v3, v3, 2, s67\n"
        "s_waitcnt lgkmcnt(9)\n"
        "v_lshl_add_u32 v4, v4, 2, s67\n"
        "v_lshl_add_u32 v5, v5, 2, s67\n"
        "v_lshl_add_u32 v6, v6, 2, s67\n"
        "v_lshl_add_u32 v7, v7, 2, s67\n"
        "s_waitcnt lgkmcnt(8)\n"
        "v_lshl_add_u32 v16, v16, 2, s67\n"
        "v_lshl_add_u32 v17, v17, 2, s67\n"
        "v_lshl_add_u32 v18, v18, 2, s67\n"
        "v_lshl_add_u32 v19, v19, 2, s67\n"
        "buffer_load_dwordx4 v129, s[20:23], 0 offen offset:1408 lds\n"
        "ds_read_b32 v0, v0\n"
        "ds_read_b32 v1, v1\n"
        "ds_read_b32 v2, v2\n"
        "ds_read_b32 v3, v3\n"
        "ds_read_b32 v4, v4\n"
        "ds_read_b32 v5, v5\n"
        "ds_read_b32 v6, v6\n"
        "ds_read_b32 v7, v7\n"
        "ds_read_b32 v16, v16\n"
        "ds_read_b32 v17, v17\n"
        "ds_read_b32 v18, v18\n"
        "ds_read_b32 v19, v19\n"
        "buffer_load_dwordx4 v130, s[20:23], 0 offen offset:2432 lds\n"
        "s_waitcnt lgkmcnt(15)\n"
        "v_lshl_add_u32 v20, v20, 2, s67\n"
        "v_lshl_add_u32 v21, v21, 2, s67\n"
        "v_lshl_add_u32 v22, v22, 2, s67\n"
        "v_lshl_add_u32 v23, v23, 2, s67\n"
        "v_lshl_add_u32 v8, v8, 2, s67\n"
        "v_lshl_add_u32 v9, v9, 2, s67\n"
        "v_lshl_add_u32 v10, v10, 2, s67\n"
        "v_lshl_add_u32 v11, v11, 2, s67\n"
        "v_lshl_add_u32 v24, v24, 2, s67\n"
        "v_lshl_add_u32 v25, v25, 2, s67\n"
        "v_lshl_add_u32 v26, v26, 2, s67\n"
        "v_lshl_add_u32 v27, v27, 2, s67\n"
        "ds_read_b32 v20, v20\n"
        "ds_read_b32 v21, v21\n"
        "ds_read_b32 v22, v22\n"
        "ds_read_b32 v23, v23\n"
        "ds_read_b32 v8, v8\n"
        "ds_read_b32 v9, v9\n"
        "ds_read_b32 v10, v10\n"
        "ds_read_b32 v11, v11\n"
        "ds_read_b32 v24, v24\n"
        "ds_read_b32 v25, v25\n"
        "ds_read_b32 v26, v26\n"
        "ds_read_b32 v27, v27\n"
        "s_cmp_eq_u32 s42, 0\n"
        "s_cbranch_scc1 48f\n"
        "62:\n"
        "s_add_u32 m0, s65, 6144\n"
        "v_lshl_add_u32 v12, v12, 2, s67\n"
        "buffer_load_dwordx4 v128, s[20:23], s56 offen lds\n"
        "v_lshl_add_u32 v13, v13, 2, s67\n"
        "v_lshl_add_u32 v14, v14, 2, s67\n"
        "v_lshl_add_u32 v15, v15, 2, s67\n"
        "v_lshl_add_u32 v28, v28, 2, s67\n"
        "v_lshl_add_u32 v29, v29, 2, s67\n"
        "v_lshl_add_u32 v30, v30, 2, s67\n"
        "v_lshl_add_u32 v31, v31, 2, s67\n"
        "s_waitcnt lgkmcnt(15)\n"
        "v_lshl_add_u32 v32, v32, 2, s67\n"
        "v_lshl_add_u32 v33, v33, 2, s67\n"
        "v_lshl_add_u32 v34, v34, 2, s67\n"
        "v_lshl_add_u32 v35, v35, 2, s67\n"
        "ds_read_b32 v12, v12\n"
        "ds_read_b32 v13, v13\n"
        "ds_read_b32 v14, v14\n"
        "ds_read_b32 v15, v15\n"
        "ds_read_b32 v28, v28\n"
        "ds_read_b32 v29, v29\n"
        "ds_read_b32 v30, v30\n"
        "ds_read_b32 v31, v31\n"
        "ds_read_b32 v32, v32\n"
        "ds_read_b32 v33, v33\n"
        "ds_read_b32 v34, v34\n"
        "ds_read_b32 v35, v35\n"
        "buffer_load_dwordx4 v129, s[20:23], s56 offen offset:1024 lds\n"
        "v_lshl_add_u32 v36, v36, 2, s67\n"
        "v_lshl_add_u32 v37, v37, 2, s67\n"
        "v_lshl_add_u32 v38, v38, 2, s67\n"
        "v_lshl_add_u32 v39, v39, 2, s67\n"
        "v_lshl_add_u32 v40, v40, 2, s67\n"
        "v_lshl_add_u32 v41, v41, 2, s67\n"
        "v_lshl_add_u32 v42, v42, 2, s67\n"
        "v_lshl_add_u32 v43, v43, 2, s67\n"
        "ds_read_b32 v36, v36\n"
        "ds_read_b32 v37, v37\n"
        "ds_read_b32 v38, v38\n"
        "ds_read_b32 v39, v39\n"
        "ds_read_b32 v40, v40\n"
        "ds_read_b32 v41, v41\n"
        "ds_read_b32 v42, v42\n"
        "ds_read_b32 v43, v43\n"
        "buffer_load_dwordx4 v130, s[20:23], s56 offen offset:2048 lds\n"
        "s_waitcnt vmcnt(9)\n"
        "s_barrier\n"
        "s_waitcnt vmcnt(6)\n"
        "ds_read_b128 a[212:215], v137 offset:0\n"
        "ds_read_b128 a[208:211], v137 offset:768\n"
        "ds_read_b128 a[204:207], v137 offset:32\n"
        "ds_read_b128 a[200:203], v137 offset:800\n"
        "ds_read_b128 a[196:199], v137 offset:64\n"
        "ds_read_b128 a[192:195], v137 offset:832\n"
        "s_add_u32 m0, s65, 8832\n"
        "s_nop 0\n"
        "buffer_load_dwordx4 v128, s[20:23], s56 offen offset:384 lds\n"
        "s_waitcnt lgkmcnt(5)\n"
        "v_mfma_f32_16x16x32_bf16 v[112:115], a[212:215], v[148:151], 0\n"
        "s_waitcnt lgkmcnt(4)\n"
        "ds_read_b128 a[216:219], v240 offset:0\n"
        "ds_read_b128 a[220:223], v240 offset:1536\n"
        "ds_read_b128 a[224:227], v240 offset:12288\n"
        "ds_read_b128 a[228:231], v240 offset:13824\n"
        "v_mfma_f32_16x16x32_bf16 v[116:119], a[208:211], v[148:151], 0\n"
        "s_waitcnt lgkmcnt(7)\n"
        "v_mfma_f32_16x16x32_bf16 v[120:123], a[204:207], v[148:151], 0\n"
        "s_waitcnt lgkmcnt(6)\n"
        "v_mfma_f32_16x16x32_bf16 v[124:127], a[200:203], v[148:151], 0\n"
        "s_waitcnt lgkmcnt(5)\n"
        "buffer_load_dwordx4 v129, s[20:23], s56 offen offset:1408 lds\n"
        "v_mfma_f32_16x16x32_bf16 v[96:99], a[196:199], v[148:151], 0\n"
        "s_waitcnt lgkmcnt(4)\n"
        "ds_read_b128 v[44:47], v143 offset:8192\n"
        "v_cvt_pk_bf16_f32 v112, v112, v113\n"
        "v_mfma_f32_16x16x32_bf16 v[100:103], a[192:195], v[148:151], 0\n"
        "s_waitcnt lgkmcnt(4)\n"
        "s_waitcnt vmcnt(5)\n"
        "ds_read_b64_tr_b16 a[72:73], v255 offset:3072\n"
        "ds_read_b64_tr_b16 a[74:75], v255 offset:3840\n"
        "v_accvgpr_write_b32 a0, v112\n"
        "v_cvt_pk_bf16_f32 v114, v114, v115\n"
        "v_accvgpr_write_b32 a1, v114\n"
        "v_cvt_pk_bf16_f32 v116, v116, v117\n"
        "v_accvgpr_write_b32 a2, v116\n"
        "v_cvt_pk_bf16_f32 v118, v118, v119\n"
        "v_accvgpr_write_b32 a3, v118\n"
        "s_nop 1\n"
        "v_mfma_f32_16x16x32_bf16 v[156:159], a[0:3], a[216:219], 0\n"
        "s_waitcnt lgkmcnt(5)\n"
        "ds_read_b64_tr_b16 a[96:97], v255 offset:3104\n"
        "ds_read_b64_tr_b16 a[98:99], v255 offset:3872\n"
        "v_mfma_f32_16x16x32_bf16 v[176:179], a[0:3], a[220:223], 0\n"
        "s_waitcnt lgkmcnt(5)\n"
        "buffer_load_dwordx4 v130, s[20:23], s56 offen offset:2432 lds\n"
        "ds_read_b64_tr_b16 a[120:121], v255 offset:3136\n"
        "ds_read_b64_tr_b16 a[122:123], v255 offset:3904\n"
        "v_mfma_f32_16x16x32_bf16 v[196:199], a[0:3], a[224:227], 0\n"
        "s_add_u32 m0, s65, 12288\n"
        "v_cvt_pk_bf16_f32 v120, v120, v121\n"
        "v_accvgpr_write_b32 a24, v120\n"
        "v_mfma_f32_16x16x32_bf16 v[216:219], a[0:3], a[228:231], 0\n"
        "s_waitcnt lgkmcnt(4)\n"
        "v_cvt_pk_bf16_f32 v122, v122, v123\n"
        "v_accvgpr_write_b32 a25, v122\n"
        "v_mfma_f32_16x16x32_bf16 v[152:155], a[72:75], a[216:219], 0\n"
        "buffer_load_dwordx4 v128, s[20:23], s57 offen lds\n"
        "s_waitcnt vmcnt(4)\n"
        "ds_read_b128 a[188:191], v137 offset:6144\n"
        "v_mfma_f32_16x16x32_bf16 v[172:175], a[72:75], a[220:223], 0\n"
        "ds_read_b128 a[184:187], v137 offset:6912\n"
        "ds_read_b128 a[180:183], v137 offset:6176\n"
        "v_cvt_pk_bf16_f32 v124, v124, v125\n"
        "v_accvgpr_write_b32 a26, v124\n"
        "v_cvt_pk_bf16_f32 v126, v126, v127\n"
        "v_accvgpr_write_b32 a27, v126\n"
        "s_nop 1\n"
        "v_mfma_f32_16x16x32_bf16 v[224:227], a[24:27], a[228:231], 0\n"
        "buffer_load_dwordx4 v129, s[20:23], s57 offen offset:1024 lds\n"
        "ds_read_b128 a[176:179], v137 offset:6944\n"
        "ds_read_b128 a[172:175], v137 offset:6208\n"
        "v_mfma_f32_16x16x32_bf16 v[204:207], a[24:27], a[224:227], 0\n"
        "ds_read_b128 a[168:171], v137 offset:6976\n"
        "ds_read_b128 a[232:235], v241 offset:0\n"
        "v_mfma_f32_16x16x32_bf16 v[184:187], a[24:27], a[220:223], 0\n"
        "ds_read_b128 a[236:239], v241 offset:1536\n"
        "ds_read_b128 a[240:243], v241 offset:12288\n"
        "v_mfma_f32_16x16x32_bf16 v[164:167], a[24:27], a[216:219], 0\n"
        "buffer_load_dwordx4 v130, s[20:23], s57 offen offset:2048 lds\n"
        "v_mfma_f32_16x16x32_bf16 v[192:195], a[72:75], a[224:227], 0\n"
        "s_add_u32 m0, s65, 14976\n"
        "ds_read_b128 a[244:247], v241 offset:13824\n"
        "v_cvt_pk_bf16_f32 v96, v96, v97\n"
        "v_mfma_f32_16x16x32_bf16 v[212:215], a[72:75], a[228:231], 0\n"
        "v_accvgpr_write_b32 a48, v96\n"
        "v_cvt_pk_bf16_f32 v98, v98, v99\n"
        "v_accvgpr_write_b32 a49, v98\n"
        "v_cvt_pk_bf16_f32 v100, v100, v101\n"
        "v_accvgpr_write_b32 a50, v100\n"
        "v_cvt_pk_bf16_f32 v102, v102, v103\n"
        "v_accvgpr_write_b32 a51, v102\n"
        "s_nop 1\n"
        "v_mfma_f32_16x16x32_bf16 v[160:163], a[48:51], a[216:219], 0\n"
        "v_lshl_add_u32 v44, v44, 2, s67\n"
        "v_lshl_add_u32 v45, v45, 2, s67\n"
        "v_mfma_f32_16x16x32_bf16 v[180:183], a[48:51], a[220:223], 0\n"
        "s_waitcnt lgkmcnt(5)\n"
        "ds_read_b32 v44, v44\n"
        "ds_read_b32 v45, v45\n"
        "v_mfma_f32_16x16x32_bf16 v[104:107], a[188:191], v[148:151], 0\n"
        "buffer_load_dwordx4 v128, s[20:23], s57 offen offset:384 lds\n"
        "v_mfma_f32_16x16x32_bf16 v[200:203], a[48:51], a[224:227], 0\n"
        "v_lshl_add_u32 v46, v46, 2, s67\n"
        "v_lshl_add_u32 v47, v47, 2, s67\n"
        "v_mfma_f32_16x16x32_bf16 v[108:111], a[184:187], v[148:151], 0\n"
        "ds_read_b32 v46, v46\n"
        "ds_read_b32 v47, v47\n"
        "v_mfma_f32_16x16x32_bf16 v[220:223], a[48:51], a[228:231], 0\n"
        "buffer_load_dwordx4 v129, s[20:23], s57 offen offset:1408 lds\n"
        "v_mfma_f32_16x16x32_bf16 v[88:91], a[96:99], a[228:231], 0\n"
        "ds_read_b128 v[48:51], v140 offset:9216\n"
        "v_cvt_pk_bf16_f32 v104, v104, v105\n"
        "v_mfma_f32_16x16x32_bf16 v[80:83], a[96:99], a[224:227], 0\n"
        "s_waitcnt vmcnt(5)\n"
        "ds_read_b64_tr_b16 a[76:77], v255 offset:9216\n"
        "ds_read_b64_tr_b16 a[78:79], v255 offset:9984\n"
        "v_mfma_f32_16x16x32_bf16 v[68:71], a[96:99], a[220:223], 0\n"
        "ds_read_b64_tr_b16 a[100:101], v255 offset:9248\n"
        "ds_read_b64_tr_b16 a[102:103], v255 offset:10016\n"
        "v_mfma_f32_16x16x32_bf16 v[64:67], a[96:99], a[216:219], 0\n"
        "buffer_load_dwordx4 v130, s[20:23], s57 offen offset:2432 lds\n"
        "ds_read_b64_tr_b16 a[124:125], v255 offset:9280\n"
        "ds_read_b64_tr_b16 a[126:127], v255 offset:10048\n"
        "v_mfma_f32_16x16x32_bf16 v[112:115], a[180:183], v[148:151], 0\n"
        "s_add_u32 m0, s65, 0\n"
        "v_accvgpr_write_b32 a4, v104\n"
        "v_cvt_pk_bf16_f32 v106, v106, v107\n"
        "v_mfma_f32_16x16x32_bf16 v[116:119], a[176:179], v[148:151], 0\n"
        "v_accvgpr_write_b32 a5, v106\n"
        "v_cvt_pk_bf16_f32 v108, v108, v109\n"
        "v_mfma_f32_16x16x32_bf16 v[72:75], a[120:123], a[216:219], 0\n"
        "buffer_load_dwordx4 v128, s[20:23], s58 offen lds\n"
        "v_mfma_f32_16x16x32_bf16 v[76:79], a[120:123], a[220:223], 0\n"
        "v_accvgpr_write_b32 a6, v108\n"
        "v_cvt_pk_bf16_f32 v110, v110, v111\n"
        "v_mfma_f32_16x16x32_bf16 v[84:87], a[120:123], a[224:227], 0\n"
        "v_accvgpr_write_b32 a7, v110\n"
        "v_cvt_pk_bf16_f32 v112, v112, v113\n"
        "v_mfma_f32_16x16x32_bf16 v[92:95], a[120:123], a[228:231], 0\n"
        "s_waitcnt lgkmcnt(2)\n"
        "v_accvgpr_write_b32 a28, v112\n"
        "v_cvt_pk_bf16_f32 v114, v114, v115\n"
        "v_mfma_f32_16x16x32_bf16 v[156:159], a[4:7], a[232:235], v[156:159]\n"
        "buffer_load_dwordx4 v129, s[20:23], s58 offen offset:1024 lds\n"
        "v_mfma_f32_16x16x32_bf16 v[176:179], a[4:7], a[236:239], v[176:179]\n"
        "v_lshl_add_u32 v48, v48, 2, s67\n"
        "v_lshl_add_u32 v49, v49, 2, s67\n"
        "v_mfma_f32_16x16x32_bf16 v[196:199], a[4:7], a[240:243], v[196:199]\n"
        "ds_read_b32 v48, v48\n"
        "ds_read_b32 v49, v49\n"
        "v_mfma_f32_16x16x32_bf16 v[216:219], a[4:7], a[244:247], v[216:219]\n"
        ".long 0xbec200ff\n"
        "41:\n"
        "s_branch 60b\n"
        "v_lshl_add_u32 v50, v50, 2, s67\n"
        "v_lshl_add_u32 v51, v51, 2, s67\n"
        "v_mfma_f32_16x16x32_bf16 v[120:123], a[172:175], v[148:151], 0\n"
        "buffer_load_dwordx4 v130, s[20:23], s58 offen offset:2048 lds\n"
        "v_mfma_f32_16x16x32_bf16 v[124:127], a[168:171], v[148:151], 0\n"
        "s_add_u32 m0, s65, 2688\n"
        "ds_read_b32 v50, v50\n"
        "ds_read_b32 v51, v51\n"
        "v_mfma_f32_16x16x32_bf16 v[152:155], a[76:79], a[232:235], v[152:155]\n"
        "ds_read_b128 a[248:251], v240 offset:128\n"
        "v_accvgpr_write_b32 a29, v114\n"
        "v_mfma_f32_16x16x32_bf16 v[172:175], a[76:79], a[236:239], v[172:175]\n"
        "s_waitcnt vmcnt(6)\n"
        "ds_read_b128 a[212:215], v137 offset:12288\n"
        "ds_read_b128 a[208:211], v137 offset:13056\n"
        "v_mfma_f32_16x16x32_bf16 v[192:195], a[76:79], a[240:243], v[192:195]\n"
        "buffer_load_dwordx4 v128, s[20:23], s58 offen offset:384 lds\n"
        "ds_read_b128 a[204:207], v137 offset:12320\n"
        "v_cvt_pk_bf16_f32 v116, v116, v117\n"
        "v_mfma_f32_16x16x32_bf16 v[212:215], a[76:79], a[244:247], v[212:215]\n"
        "ds_read_b128 a[200:203], v137 offset:13088\n"
        "ds_read_b128 a[196:199], v137 offset:12352\n"
        "v_accvgpr_write_b32 a30, v116\n"
        "v_cvt_pk_bf16_f32 v118, v118, v119\n"
        "v_accvgpr_write_b32 a31, v118\n"
        "s_nop 1\n"
        "v_mfma_f32_16x16x32_bf16 v[224:227], a[28:31], a[244:247], v[224:227]\n"
        "ds_read_b128 a[192:195], v137 offset:13120\n"
        "ds_read_b128 a[216:219], v240 offset:1664\n"
        "v_mfma_f32_16x16x32_bf16 v[204:207], a[28:31], a[240:243], v[204:207]\n"
        "buffer_load_dwordx4 v129, s[20:23], s58 offen offset:1408 lds\n"
        "v_mfma_f32_16x16x32_bf16 v[184:187], a[28:31], a[236:239], v[184:187]\n"
        "ds_read_b128 a[220:223], v240 offset:12416\n"
        "ds_read_b128 a[224:227], v240 offset:13952\n"
        "v_mfma_f32_16x16x32_bf16 v[164:167], a[28:31], a[232:235], v[164:167]\n"
        "s_waitcnt lgkmcnt(8)\n"
        "v_cvt_pk_bf16_f32 v120, v120, v121\n"
        "v_accvgpr_write_b32 a52, v120\n"
        "v_mfma_f32_16x16x32_bf16 v[96:99], a[212:215], v[148:151], 0\n"
        "s_waitcnt lgkmcnt(7)\n"
        "v_cvt_pk_bf16_f32 v122, v122, v123\n"
        "v_accvgpr_write_b32 a53, v122\n"
        "v_mfma_f32_16x16x32_bf16 v[100:103], a[208:211], v[148:151], 0\n"
        "buffer_load_dwordx4 v130, s[20:23], s58 offen offset:2432 lds\n"
        "v_mfma_f32_16x16x32_bf16 v[88:91], a[100:103], a[244:247], v[88:91]\n"
        "s_add_u32 m0, s65, 6144\n"
        "v_cvt_pk_bf16_f32 v124, v124, v125\n"
        "v_accvgpr_write_b32 a54, v124\n"
        "v_mfma_f32_16x16x32_bf16 v[80:83], a[100:103], a[240:243], v[80:83]\n"
        "v_cvt_pk_bf16_f32 v126, v126, v127\n"
        "v_accvgpr_write_b32 a55, v126\n"
        "v_mfma_f32_16x16x32_bf16 v[68:71], a[100:103], a[236:239], v[68:71]\n"
        "v_cvt_pk_bf16_f32 v96, v96, v97\n"
        "v_accvgpr_write_b32 a8, v96\n"
        "v_mfma_f32_16x16x32_bf16 v[64:67], a[100:103], a[232:235], v[64:67]\n"
        "buffer_load_dwordx4 v128, s[20:23], s59 offen lds\n"
        "v_mfma_f32_16x16x32_bf16 v[160:163], a[52:55], a[232:235], v[160:163]\n"
        "s_waitcnt vmcnt(7)\n"
        "ds_read_b64_tr_b16 a[80:81], v255 offset:15360\n"
        "ds_read_b64_tr_b16 a[82:83], v255 offset:16128\n"
        "v_mfma_f32_16x16x32_bf16 v[180:183], a[52:55], a[236:239], v[180:183]\n"
        "ds_read_b64_tr_b16 a[104:105], v255 offset:15392\n"
        "ds_read_b64_tr_b16 a[106:107], v255 offset:16160\n"
        "v_mfma_f32_16x16x32_bf16 v[200:203], a[52:55], a[240:243], v[200:203]\n"
        "ds_read_b64_tr_b16 a[128:129], v255 offset:15424\n"
        "ds_read_b64_tr_b16 a[130:131], v255 offset:16192\n"
        "v_mfma_f32_16x16x32_bf16 v[220:223], a[52:55], a[244:247], v[220:223]\n"
        "s_waitcnt lgkmcnt(6)\n"
        "v_cvt_pk_bf16_f32 v98, v98, v99\n"
        "v_accvgpr_write_b32 a9, v98\n"
        "v_mfma_f32_16x16x32_bf16 v[104:107], a[204:207], v[148:151], 0\n"
        "buffer_load_dwordx4 v129, s[20:23], s59 offen offset:1024 lds\n"
        "v_mfma_f32_16x16x32_bf16 v[108:111], a[200:203], v[148:151], 0\n"
        "v_cvt_pk_bf16_f32 v100, v100, v101\n"
        "v_accvgpr_write_b32 a10, v100\n"
        "v_mfma_f32_16x16x32_bf16 v[72:75], a[124:127], a[232:235], v[72:75]\n"
        "v_cvt_pk_bf16_f32 v102, v102, v103\n"
        "v_accvgpr_write_b32 a11, v102\n"
        "v_mfma_f32_16x16x32_bf16 v[76:79], a[124:127], a[236:239], v[76:79]\n"
        "buffer_load_dwordx4 v130, s[20:23], s59 offen offset:2048 lds\n"
        "v_mfma_f32_16x16x32_bf16 v[84:87], a[124:127], a[240:243], v[84:87]\n"
        "s_add_u32 m0, s65, 8832\n"
        "v_cvt_pk_bf16_f32 v104, v104, v105\n"
        "v_accvgpr_write_b32 a32, v104\n"
        "v_mfma_f32_16x16x32_bf16 v[92:95], a[124:127], a[244:247], v[92:95]\n"
        "s_waitcnt vmcnt(6)\n"
        "ds_read_b128 a[188:191], v137 offset:0\n"
        "ds_read_b128 a[184:187], v137 offset:768\n"
        "v_mfma_f32_16x16x32_bf16 v[156:159], a[8:11], a[248:251], v[156:159]\n"
        "buffer_load_dwordx4 v128, s[20:23], s59 offen offset:384 lds\n"
        "ds_read_b128 a[180:183], v137 offset:32\n"
        "v_mfma_f32_16x16x32_bf16 v[176:179], a[8:11], a[216:219], v[176:179]\n"
        "ds_read_b128 a[176:179], v137 offset:800\n"
        "ds_read_b128 a[172:175], v137 offset:64\n"
        "v_mfma_f32_16x16x32_bf16 v[196:199], a[8:11], a[220:223], v[196:199]\n"
        "ds_read_b128 a[168:171], v137 offset:832\n"
        "v_cvt_pk_bf16_f32 v106, v106, v107\n"
        "v_mfma_f32_16x16x32_bf16 v[216:219], a[8:11], a[224:227], v[216:219]\n"
        "v_accvgpr_write_b32 a33, v106\n"
        "v_cvt_pk_bf16_f32 v108, v108, v109\n"
        "v_mfma_f32_16x16x32_bf16 v[112:115], a[196:199], v[148:151], 0\n"
        "buffer_load_dwordx4 v129, s[20:23], s59 offen offset:1408 lds\n"
        "v_mfma_f32_16x16x32_bf16 v[116:119], a[192:195], v[148:151], 0\n"
        "s_waitcnt lgkmcnt(6)\n"
        "v_accvgpr_write_b32 a34, v108\n"
        "v_cvt_pk_bf16_f32 v110, v110, v111\n"
        "v_mfma_f32_16x16x32_bf16 v[152:155], a[80:83], a[248:251], v[152:155]\n"
        "ds_read_b128 a[228:231], v241 offset:128\n"
        "v_accvgpr_write_b32 a35, v110\n"
        "v_mfma_f32_16x16x32_bf16 v[172:175], a[80:83], a[216:219], v[172:175]\n"
        "ds_read_b128 a[232:235], v241 offset:1664\n"
        "v_cvt_pk_bf16_f32 v112, v112, v113\n"
        "v_mfma_f32_16x16x32_bf16 v[192:195], a[80:83], a[220:223], v[192:195]\n"
        "buffer_load_dwordx4 v130, s[20:23], s59 offen offset:2432 lds\n"
        "v_mfma_f32_16x16x32_bf16 v[212:215], a[80:83], a[224:227], v[212:215]\n"
        "s_add_u32 m0, s65, 12288\n"
        "ds_read_b128 a[236:239], v241 offset:12416\n"
        "ds_read_b128 a[240:243], v241 offset:13952\n"
        "v_mfma_f32_16x16x32_bf16 v[224:227], a[32:35], a[224:227], v[224:227]\n"
        "ds_read_b128 v[52:55], v141 offset:9216\n"
        "v_accvgpr_write_b32 a56, v112\n"
        "v_mfma_f32_16x16x32_bf16 v[204:207], a[32:35], a[220:223], v[204:207]\n"
        "v_cvt_pk_bf16_f32 v114, v114, v115\n"
        "v_accvgpr_write_b32 a57, v114\n"
        "v_mfma_f32_16x16x32_bf16 v[184:187], a[32:35], a[216:219], v[184:187]\n"
        "buffer_load_dwordx4 v128, s[20:23], s60 offen lds\n"
        "v_mfma_f32_16x16x32_bf16 v[164:167], a[32:35], a[248:251], v[164:167]\n"
        "s_waitcnt lgkmcnt(4)\n"
        "v_cvt_pk_bf16_f32 v116, v116, v117\n"
        "v_accvgpr_write_b32 a58, v116\n"
        "v_mfma_f32_16x16x32_bf16 v[120:123], a[188:191], v[148:151], 0\n"
        "s_waitcnt vmcnt(7)\n"
        "ds_read_b64_tr_b16 a[84:85], v255 offset:3072\n"
        "ds_read_b64_tr_b16 a[86:87], v255 offset:3840\n"
        "v_mfma_f32_16x16x32_bf16 v[124:127], a[184:187], v[148:151], 0\n"
        "ds_read_b64_tr_b16 a[108:109], v255 offset:3104\n"
        "ds_read_b64_tr_b16 a[110:111], v255 offset:3872\n"
        "v_mfma_f32_16x16x32_bf16 v[88:91], a[104:107], a[224:227], v[88:91]\n"
        "buffer_load_dwordx4 v129, s[20:23], s60 offen offset:1024 lds\n"
        "ds_read_b64_tr_b16 a[132:133], v255 offset:3136\n"
        "ds_read_b64_tr_b16 a[134:135], v255 offset:3904\n"
        "v_mfma_f32_16x16x32_bf16 v[80:83], a[104:107], a[220:223], v[80:83]\n"
        "v_cvt_pk_bf16_f32 v118, v118, v119\n"
        "v_accvgpr_write_b32 a59, v118\n"
        "v_mfma_f32_16x16x32_bf16 v[68:71], a[104:107], a[216:219], v[68:71]\n"
        "v_cvt_pk_bf16_f32 v120, v120, v121\n"
        "v_accvgpr_write_b32 a12, v120\n"
        "v_mfma_f32_16x16x32_bf16 v[64:67], a[104:107], a[248:251], v[64:67]\n"
        "buffer_load_dwordx4 v130, s[20:23], s60 offen offset:2048 lds\n"
        "v_mfma_f32_16x16x32_bf16 v[160:163], a[56:59], a[248:251], v[160:163]\n"
        "s_add_u32 m0, s65, 14976\n"
        "v_cvt_pk_bf16_f32 v122, v122, v123\n"
        "v_accvgpr_write_b32 a13, v122\n"
        "v_mfma_f32_16x16x32_bf16 v[180:183], a[56:59], a[216:219], v[180:183]\n"
        "s_waitcnt lgkmcnt(4)\n"
        "v_lshl_add_u32 v52, v52, 2, s67\n"
        "v_lshl_add_u32 v53, v53, 2, s67\n"
        "v_mfma_f32_16x16x32_bf16 v[200:203], a[56:59], a[220:223], v[200:203]\n"
        "ds_read_b32 v52, v52\n"
        "ds_read_b32 v53, v53\n"
        "v_mfma_f32_16x16x32_bf16 v[220:223], a[56:59], a[224:227], v[220:223]\n"
        "buffer_load_dwordx4 v128, s[20:23], s60 offen offset:384 lds\n"
        "v_mfma_f32_16x16x32_bf16 v[96:99], a[180:183], v[148:151], 0\n"
        "v_lshl_add_u32 v54, v54, 2, s67\n"
        "v_lshl_add_u32 v55, v55, 2, s67\n"
        "v_mfma_f32_16x16x32_bf16 v[100:103], a[176:179], v[148:151], 0\n"
        "ds_read_b32 v54, v54\n"
        "ds_read_b32 v55, v55\n"
        "v_mfma_f32_16x16x32_bf16 v[72:75], a[128:131], a[248:251], v[72:75]\n"
        "v_cvt_pk_bf16_f32 v124, v124, v125\n"
        "v_accvgpr_write_b32 a14, v124\n"
        "v_mfma_f32_16x16x32_bf16 v[76:79], a[128:131], a[216:219], v[76:79]\n"
        "buffer_load_dwordx4 v129, s[20:23], s60 offen offset:1408 lds\n"
        "v_mfma_f32_16x16x32_bf16 v[84:87], a[128:131], a[220:223], v[84:87]\n"
        "v_cvt_pk_bf16_f32 v126, v126, v127\n"
        "v_accvgpr_write_b32 a15, v126\n"
        "v_mfma_f32_16x16x32_bf16 v[92:95], a[128:131], a[224:227], v[92:95]\n"
        "v_cvt_pk_bf16_f32 v96, v96, v97\n"
        "v_accvgpr_write_b32 a36, v96\n"
        "v_mfma_f32_16x16x32_bf16 v[156:159], a[12:15], a[228:231], v[156:159]\n"
        "s_waitcnt vmcnt(8)\n"
        "ds_read_b128 a[212:215], v137 offset:6144\n"
        "ds_read_b128 a[208:211], v137 offset:6912\n"
        "v_mfma_f32_16x16x32_bf16 v[176:179], a[12:15], a[232:235], v[176:179]\n"
        "buffer_load_dwordx4 v130, s[20:23], s60 offen offset:2432 lds\n"
        "ds_read_b128 a[204:207], v137 offset:6176\n"
        "v_mfma_f32_16x16x32_bf16 v[196:199], a[12:15], a[236:239], v[196:199]\n"
        "ds_read_b128 a[200:203], v137 offset:6944\n"
        "ds_read_b128 a[196:199], v137 offset:6208\n"
        "v_mfma_f32_16x16x32_bf16 v[216:219], a[12:15], a[240:243], v[216:219]\n"
        "ds_read_b128 a[192:195], v137 offset:6976\n"
        "v_cvt_pk_bf16_f32 v98, v98, v99\n"
        "v_mfma_f32_16x16x32_bf16 v[104:107], a[172:175], v[148:151], 0\n"
        "v_accvgpr_write_b32 a37, v98\n"
        "v_cvt_pk_bf16_f32 v100, v100, v101\n"
        "v_mfma_f32_16x16x32_bf16 v[108:111], a[168:171], v[148:151], 0\n"
        "v_accvgpr_write_b32 a38, v100\n"
        "v_cvt_pk_bf16_f32 v102, v102, v103\n"
        "v_mfma_f32_16x16x32_bf16 v[152:155], a[84:87], a[228:231], v[152:155]\n"
        "ds_read_b128 a[244:247], v240 offset:256\n"
        "v_accvgpr_write_b32 a39, v102\n"
        "v_mfma_f32_16x16x32_bf16 v[172:175], a[84:87], a[232:235], v[172:175]\n"
        "s_mov_b32 m0, s48\n"
        "ds_read_b128 a[248:251], v240 offset:1792\n"
        "v_cvt_pk_bf16_f32 v104, v104, v105\n"
        "v_mfma_f32_16x16x32_bf16 v[192:195], a[84:87], a[236:239], v[192:195]\n"
        "s_sub_u32 s64, s36, 4194304\n"
        "ds_read_b128 a[216:219], v240 offset:12544\n"
        "v_accvgpr_write_b32 a60, v104\n"
        "v_mfma_f32_16x16x32_bf16 v[212:215], a[84:87], a[240:243], v[212:215]\n"
        "buffer_load_dwordx4 v249, s[12:15], s64 offen lds\n"
        "v_mfma_f32_16x16x32_bf16 v[224:227], a[36:39], a[240:243], v[224:227]\n"
        "ds_read_b128 a[220:223], v240 offset:14080\n"
        "v_cvt_pk_bf16_f32 v106, v106, v107\n"
        "v_mfma_f32_16x16x32_bf16 v[204:207], a[36:39], a[236:239], v[204:207]\n"
        "ds_read_b128 v[56:59], v142 offset:9216\n"
        "v_accvgpr_write_b32 a61, v106\n"
        "v_mfma_f32_16x16x32_bf16 v[184:187], a[36:39], a[232:235], v[184:187]\n"
        "s_waitcnt vmcnt(7)\n"
        "ds_read_b64_tr_b16 a[88:89], v255 offset:9216\n"
        "ds_read_b64_tr_b16 a[90:91], v255 offset:9984\n"
        "v_mfma_f32_16x16x32_bf16 v[164:167], a[36:39], a[228:231], v[164:167]\n"
        "s_waitcnt lgkmcnt(6)\n"
        "ds_read_b64_tr_b16 a[112:113], v255 offset:9248\n"
        "ds_read_b64_tr_b16 a[114:115], v255 offset:10016\n"
        "v_mfma_f32_16x16x32_bf16 v[112:115], a[212:215], v[148:151], 0\n"
        "buffer_load_dwordx4 v253, s[12:15], s64 offen offset:1024 lds\n"
        "ds_read_b64_tr_b16 a[136:137], v255 offset:9280\n"
        "v_mfma_f32_16x16x32_bf16 v[116:119], a[208:211], v[148:151], 0\n"
        "ds_read_b64_tr_b16 a[138:139], v255 offset:10048\n"
        "s_add_u32 m0, s65, 0\n"
        "v_cvt_pk_bf16_f32 v108, v108, v109\n"
        "v_mfma_f32_16x16x32_bf16 v[88:91], a[108:111], a[240:243], v[88:91]\n"
        "s_mov_b32 s64, 768\n"
        "v_accvgpr_write_b32 a62, v108\n"
        "v_cvt_pk_bf16_f32 v110, v110, v111\n"
        "v_mfma_f32_16x16x32_bf16 v[80:83], a[108:111], a[236:239], v[80:83]\n"
        "buffer_load_dwordx4 v128, s[20:23], s64 offen lds\n"
        "v_mfma_f32_16x16x32_bf16 v[68:71], a[108:111], a[232:235], v[68:71]\n"
        "v_accvgpr_write_b32 a63, v110\n"
        "v_cvt_pk_bf16_f32 v112, v112, v113\n"
        "v_mfma_f32_16x16x32_bf16 v[64:67], a[108:111], a[228:231], v[64:67]\n"
        "v_accvgpr_write_b32 a16, v112\n"
        "v_cvt_pk_bf16_f32 v114, v114, v115\n"
        "v_mfma_f32_16x16x32_bf16 v[160:163], a[60:63], a[228:231], v[160:163]\n"
        "v_accvgpr_write_b32 a17, v114\n"
        "v_cvt_pk_bf16_f32 v116, v116, v117\n"
        "v_mfma_f32_16x16x32_bf16 v[180:183], a[60:63], a[232:235], v[180:183]\n"
        "buffer_load_dwordx4 v129, s[20:23], s64 offen offset:1024 lds\n"
        "v_mfma_f32_16x16x32_bf16 v[200:203], a[60:63], a[236:239], v[200:203]\n"
        "ds_read_b128 v[60:63], v143 offset:9216\n"
        "v_accvgpr_write_b32 a18, v116\n"
        "v_mfma_f32_16x16x32_bf16 v[220:223], a[60:63], a[240:243], v[220:223]\n"
        "v_cvt_pk_bf16_f32 v118, v118, v119\n"
        "v_accvgpr_write_b32 a19, v118\n"
        "v_mfma_f32_16x16x32_bf16 v[120:123], a[204:207], v[148:151], 0\n"
        "s_waitcnt lgkmcnt(1)\n"
        "v_lshl_add_u32 v56, v56, 2, s67\n"
        "v_lshl_add_u32 v57, v57, 2, s67\n"
        "v_mfma_f32_16x16x32_bf16 v[124:127], a[200:203], v[148:151], 0\n"
        "buffer_load_dwordx4 v130, s[20:23], s64 offen offset:2048 lds\n"
        "v_mfma_f32_16x16x32_bf16 v[72:75], a[132:135], a[228:231], v[72:75]\n"
        "s_add_u32 m0, s65, 2304\n"
        "ds_read_b32 v56, v56\n"
        "ds_read_b32 v57, v57\n"
        "v_mfma_f32_16x16x32_bf16 v[76:79], a[132:135], a[232:235], v[76:79]\n"
        "v_lshl_add_u32 v58, v58, 2, s67\n"
        "v_lshl_add_u32 v59, v59, 2, s67\n"
        "v_mfma_f32_16x16x32_bf16 v[84:87], a[132:135], a[236:239], v[84:87]\n"
        "ds_read_b32 v58, v58\n"
        "ds_read_b32 v59, v59\n"
        "v_mfma_f32_16x16x32_bf16 v[92:95], a[132:135], a[240:243], v[92:95]\n"
        "buffer_load_dwordx4 v128, s[20:23], s56 offen offset:768 lds\n"
        "v_mfma_f32_16x16x32_bf16 v[156:159], a[16:19], a[244:247], v[156:159]\n"
        "v_cvt_pk_bf16_f32 v120, v120, v121\n"
        "v_accvgpr_write_b32 a40, v120\n"
        "v_mfma_f32_16x16x32_bf16 v[176:179], a[16:19], a[248:251], v[176:179]\n"
        "v_cvt_pk_bf16_f32 v122, v122, v123\n"
        "v_accvgpr_write_b32 a41, v122\n"
        "v_mfma_f32_16x16x32_bf16 v[196:199], a[16:19], a[216:219], v[196:199]\n"
        "v_cvt_pk_bf16_f32 v124, v124, v125\n"
        "v_accvgpr_write_b32 a42, v124\n"
        "v_mfma_f32_16x16x32_bf16 v[216:219], a[16:19], a[220:223], v[216:219]\n"
        "buffer_load_dwordx4 v129, s[20:23], s56 offen offset:1792 lds\n"
        "v_mfma_f32_16x16x32_bf16 v[96:99], a[196:199], v[148:151], 0\n"
        "ds_read_b128 a[224:227], v241 offset:256\n"
        "v_cvt_pk_bf16_f32 v126, v126, v127\n"
        "v_mfma_f32_16x16x32_bf16 v[100:103], a[192:195], v[148:151], 0\n"
        "s_waitcnt vmcnt(10)\n"
        "ds_read_b128 a[188:191], v137 offset:12288\n"
        "ds_read_b128 a[184:187], v137 offset:13056\n"
        "v_mfma_f32_16x16x32_bf16 v[152:155], a[88:91], a[244:247], v[152:155]\n"
        "ds_read_b128 a[180:183], v137 offset:12320\n"
        "ds_read_b128 a[176:179], v137 offset:13088\n"
        "v_mfma_f32_16x16x32_bf16 v[172:175], a[88:91], a[248:251], v[172:175]\n"
        "buffer_load_dwordx4 v130, s[20:23], s56 offen offset:2816 lds\n"
        "ds_read_b128 a[172:175], v137 offset:12352\n"
        "v_mfma_f32_16x16x32_bf16 v[192:195], a[88:91], a[216:219], v[192:195]\n"
        "ds_read_b128 a[168:171], v137 offset:13120\n"
        "s_add_u32 m0, s65, 5376\n"
        "ds_read_b128 a[228:231], v241 offset:1792\n"
        "v_accvgpr_write_b32 a43, v126\n"
        "v_mfma_f32_16x16x32_bf16 v[212:215], a[88:91], a[220:223], v[212:215]\n"
        "ds_read_b128 a[232:235], v241 offset:12544\n"
        "ds_read_b128 a[236:239], v241 offset:14080\n"
        "v_mfma_f32_16x16x32_bf16 v[224:227], a[40:43], a[220:223], v[224:227]\n"
        "s_waitcnt lgkmcnt(10)\n"
        "v_lshl_add_u32 v60, v60, 2, s67\n"
        "v_lshl_add_u32 v61, v61, 2, s67\n"
        "v_lshl_add_u32 v62, v62, 2, s67\n"
        "v_mfma_f32_16x16x32_bf16 v[204:207], a[40:43], a[216:219], v[204:207]\n"
        "buffer_load_dwordx4 v128, s[20:23], s57 offen offset:768 lds\n"
        "v_lshl_add_u32 v63, v63, 2, s67\n"
        "v_mfma_f32_16x16x32_bf16 v[184:187], a[40:43], a[248:251], v[184:187]\n"
        "ds_read_b32 v60, v60\n"
        "ds_read_b32 v61, v61\n"
        "v_mfma_f32_16x16x32_bf16 v[164:167], a[40:43], a[244:247], v[164:167]\n"
        "s_waitcnt lgkmcnt(7)\n"
        "ds_read_b32 v62, v62\n"
        "ds_read_b32 v63, v63\n"
        "v_mfma_f32_16x16x32_bf16 v[104:107], a[188:191], v[148:151], 0\n"
        "v_cvt_pk_bf16_f32 v96, v96, v97\n"
        "v_accvgpr_write_b32 a64, v96\n"
        "v_mfma_f32_16x16x32_bf16 v[108:111], a[184:187], v[148:151], 0\n"
        "buffer_load_dwordx4 v129, s[20:23], s57 offen offset:1792 lds\n"
        "v_mfma_f32_16x16x32_bf16 v[88:91], a[112:115], a[220:223], v[88:91]\n"
        "v_cvt_pk_bf16_f32 v98, v98, v99\n"
        "v_accvgpr_write_b32 a65, v98\n"
        "v_mfma_f32_16x16x32_bf16 v[80:83], a[112:115], a[216:219], v[80:83]\n"
        "v_cvt_pk_bf16_f32 v100, v100, v101\n"
        "v_accvgpr_write_b32 a66, v100\n"
        "v_mfma_f32_16x16x32_bf16 v[68:71], a[112:115], a[248:251], v[68:71]\n"
        "v_cvt_pk_bf16_f32 v102, v102, v103\n"
        "v_accvgpr_write_b32 a67, v102\n"
        "v_mfma_f32_16x16x32_bf16 v[64:67], a[112:115], a[244:247], v[64:67]\n"
        "s_waitcnt vmcnt(10)\n"
        "ds_read_b64_tr_b16 a[92:93], v255 offset:15360\n"
        "ds_read_b64_tr_b16 a[94:95], v255 offset:16128\n"
        "v_mfma_f32_16x16x32_bf16 v[160:163], a[64:67], a[244:247], v[160:163]\n"
        "ds_read_b64_tr_b16 a[116:117], v255 offset:15392\n"
        "ds_read_b64_tr_b16 a[118:119], v255 offset:16160\n"
        "v_mfma_f32_16x16x32_bf16 v[180:183], a[64:67], a[248:251], v[180:183]\n"
        "ds_read_b64_tr_b16 a[140:141], v255 offset:15424\n"
        "ds_read_b64_tr_b16 a[142:143], v255 offset:16192\n"
        "v_mfma_f32_16x16x32_bf16 v[200:203], a[64:67], a[216:219], v[200:203]\n"
        "v_cvt_pk_bf16_f32 v104, v104, v105\n"
        "v_accvgpr_write_b32 a20, v104\n"
        "v_mfma_f32_16x16x32_bf16 v[220:223], a[64:67], a[220:223], v[220:223]\n"
        "v_cvt_pk_bf16_f32 v106, v106, v107\n"
        "v_accvgpr_write_b32 a21, v106\n"
        "v_mfma_f32_16x16x32_bf16 v[112:115], a[180:183], v[148:151], 0\n"
        "v_cvt_pk_bf16_f32 v108, v108, v109\n"
        "v_accvgpr_write_b32 a22, v108\n"
        "v_mfma_f32_16x16x32_bf16 v[116:119], a[176:179], v[148:151], 0\n"
        "v_cvt_pk_bf16_f32 v110, v110, v111\n"
        "v_accvgpr_write_b32 a23, v110\n"
        "v_mfma_f32_16x16x32_bf16 v[72:75], a[136:139], a[244:247], v[72:75]\n"
        "v_mfma_f32_16x16x32_bf16 v[76:79], a[136:139], a[248:251], v[76:79]\n"
        "v_mfma_f32_16x16x32_bf16 v[156:159], a[20:23], a[224:227], v[156:159]\n"
        "s_waitcnt lgkmcnt(0)\n"
        "v_cvt_pk_bf16_f32 v112, v112, v113\n"
        "v_accvgpr_write_b32 a44, v112\n"
        "v_mfma_f32_16x16x32_bf16 v[176:179], a[20:23], a[228:231], v[176:179]\n"
        "v_cvt_pk_bf16_f32 v114, v114, v115\n"
        "v_accvgpr_write_b32 a45, v114\n"
        "v_mfma_f32_16x16x32_bf16 v[196:199], a[20:23], a[232:235], v[196:199]\n"
        "v_cvt_pk_bf16_f32 v116, v116, v117\n"
        "v_accvgpr_write_b32 a46, v116\n"
        "v_cvt_pk_bf16_f32 v156, v156, v157\n"
        "v_mfma_f32_16x16x32_bf16 v[216:219], a[20:23], a[236:239], v[216:219]\n"
        "v_cvt_pk_bf16_f32 v157, v158, v159\n"
        "v_cvt_pk_bf16_f32 v176, v176, v177\n"
        "v_cvt_pk_bf16_f32 v177, v178, v179\n"
        "v_mfma_f32_16x16x32_bf16 v[120:123], a[172:175], v[148:151], 0\n"
        "v_cvt_pk_bf16_f32 v196, v196, v197\n"
        "v_cvt_pk_bf16_f32 v197, v198, v199\n"
        "v_mfma_f32_16x16x32_bf16 v[124:127], a[168:171], v[148:151], 0\n"
        "v_cvt_pk_bf16_f32 v118, v118, v119\n"
        "v_cvt_pk_bf16_f32 v216, v216, v217\n"
        "v_cvt_pk_bf16_f32 v217, v218, v219\n"
        "v_mfma_f32_16x16x32_bf16 v[84:87], a[136:139], a[216:219], v[84:87]\n"
        "v_accvgpr_write_b32 a47, v118\n"
        "v_cvt_pk_bf16_f32 v120, v120, v121\n"
        "v_mfma_f32_16x16x32_bf16 v[92:95], a[136:139], a[220:223], v[92:95]\n"
        "v_accvgpr_write_b32 a68, v120\n"
        "v_cvt_pk_bf16_f32 v122, v122, v123\n"
        "v_mfma_f32_16x16x32_bf16 v[224:227], a[44:47], a[236:239], v[224:227]\n"
        "v_accvgpr_write_b32 a69, v122\n"
        "v_cvt_pk_bf16_f32 v124, v124, v125\n"
        "v_mfma_f32_16x16x32_bf16 v[204:207], a[44:47], a[232:235], v[204:207]\n"
        "v_accvgpr_write_b32 a70, v124\n"
        "v_cvt_pk_bf16_f32 v126, v126, v127\n"
        "v_mfma_f32_16x16x32_bf16 v[184:187], a[44:47], a[228:231], v[184:187]\n"
        "v_accvgpr_write_b32 a71, v126\n"
        "s_nop 0\n"
        "v_cvt_pk_bf16_f32 v218, v224, v225\n"
        "v_mfma_f32_16x16x32_bf16 v[164:167], a[44:47], a[224:227], v[164:167]\n"
        "v_cvt_pk_bf16_f32 v219, v226, v227\n"
        "v_cvt_pk_bf16_f32 v198, v204, v205\n"
        "v_cvt_pk_bf16_f32 v199, v206, v207\n"
        "v_mfma_f32_16x16x32_bf16 v[152:155], a[92:95], a[224:227], v[152:155]\n"
        "v_cvt_pk_bf16_f32 v178, v184, v185\n"
        "v_cvt_pk_bf16_f32 v179, v186, v187\n"
        "v_mfma_f32_16x16x32_bf16 v[64:67], a[116:119], a[224:227], v[64:67]\n"
        "buffer_load_dwordx4 v130, s[20:23], s57 offen offset:2816 lds\n"
        "v_cvt_pk_bf16_f32 v158, v164, v165\n"
        "v_mfma_f32_16x16x32_bf16 v[72:75], a[140:143], a[224:227], v[72:75]\n"
        "s_add_u32 m0, s65, 8448\n"
        "v_cvt_pk_bf16_f32 v159, v166, v167\n"
        "v_cvt_pk_bf16_f32 v152, v152, v153\n"
        "v_mfma_f32_16x16x32_bf16 v[172:175], a[92:95], a[228:231], v[172:175]\n"
        "v_cvt_pk_bf16_f32 v153, v154, v155\n"
        "v_cvt_pk_bf16_f32 v154, v64, v65\n"
        "v_mfma_f32_16x16x32_bf16 v[160:163], a[68:71], a[224:227], v[160:163]\n"
        "v_cvt_pk_bf16_f32 v155, v66, v67\n"
        "v_cvt_pk_bf16_f32 v168, v72, v73\n"
        "v_mfma_f32_16x16x32_bf16 v[180:183], a[68:71], a[228:231], v[180:183]\n"
        "buffer_load_dwordx4 v128, s[20:23], s58 offen offset:768 lds\n"
        "v_mfma_f32_16x16x32_bf16 v[200:203], a[68:71], a[232:235], v[200:203]\n"
        "v_cvt_pk_bf16_f32 v169, v74, v75\n"
        "v_cvt_pk_bf16_f32 v172, v172, v173\n"
        "v_mfma_f32_16x16x32_bf16 v[220:223], a[68:71], a[236:239], v[220:223]\n"
        "v_cvt_pk_bf16_f32 v166, v160, v161\n"
        "v_cvt_pk_bf16_f32 v160, v160, v161\n"
        "v_cvt_pk_bf16_f32 v161, v162, v163\n"
        "v_cvt_pk_bf16_f32 v167, v162, v163\n"
        "v_cvt_pk_bf16_f32 v186, v180, v181\n"
        "v_cvt_pk_bf16_f32 v180, v180, v181\n"
        "v_mfma_f32_16x16x32_bf16 v[68:71], a[116:119], a[228:231], v[68:71]\n"
        "buffer_load_dwordx4 v129, s[20:23], s58 offen offset:1792 lds\n"
        "v_cvt_pk_bf16_f32 v181, v182, v183\n"
        "v_cvt_pk_bf16_f32 v187, v182, v183\n"
        "v_cvt_pk_bf16_f32 v206, v200, v201\n"
        "v_cvt_pk_bf16_f32 v200, v200, v201\n"
        "v_mfma_f32_16x16x32_bf16 v[76:79], a[140:143], a[228:231], v[76:79]\n"
        "s_waitcnt vmcnt(11)\n"
        "s_barrier\n"
        "ds_read_b128 v[162:165], v251 offset:0\n"
        "ds_read_b128 v[182:185], v251 offset:512\n"
        "v_cvt_pk_bf16_f32 v201, v202, v203\n"
        "v_cvt_pk_bf16_f32 v207, v202, v203\n"
        "v_cvt_pk_bf16_f32 v226, v220, v221\n"
        "v_cvt_pk_bf16_f32 v220, v220, v221\n"
        "v_mfma_f32_16x16x32_bf16 v[192:195], a[92:95], a[232:235], v[192:195]\n"
        "ds_read_b128 v[202:205], v251 offset:4096\n"
        "ds_read_b128 a[240:243], v240 offset:0\n"
        "v_cvt_pk_bf16_f32 v221, v222, v223\n"
        "v_cvt_pk_bf16_f32 v227, v222, v223\n"
        "v_cvt_pk_bf16_f32 v173, v174, v175\n"
        "v_mfma_f32_16x16x32_bf16 v[80:83], a[116:119], a[232:235], v[80:83]\n"
        "buffer_load_dwordx4 v130, s[20:23], s58 offen offset:2816 lds\n"
        "v_mfma_f32_16x16x32_bf16 v[84:87], a[140:143], a[232:235], v[84:87]\n"
        "s_add_u32 m0, s65, 11520\n"
        "ds_read_b128 a[244:247], v241 offset:0\n"
        "ds_read_b128 a[248:251], v240 offset:128\n"
        "v_mfma_f32_16x16x32_bf16 v[212:215], a[92:95], a[236:239], v[212:215]\n"
        "v_cvt_pk_bf16_f32 v174, v68, v69\n"
        "v_cvt_pk_bf16_f32 v175, v70, v71\n"
        "v_mfma_f32_16x16x32_bf16 v[88:91], a[116:119], a[236:239], v[88:91]\n"
        "buffer_load_dwordx4 v128, s[20:23], s59 offen offset:768 lds\n"
        "v_mfma_f32_16x16x32_bf16 v[92:95], a[140:143], a[236:239], v[92:95]\n"
        "s_waitcnt vmcnt(10)\n"
        "ds_read_b64_tr_b16 a[144:145], v255 offset:0\n"
        "ds_read_b64_tr_b16 a[146:147], v255 offset:768\n"
        "v_mfma_f32_16x16x32_bf16 v[64:67], v[152:155], v[156:159], v[0:3]\n"
        "s_waitcnt lgkmcnt(6)\n"
        "ds_read_b64_tr_b16 a[168:169], v255 offset:32\n"
        "ds_read_b64_tr_b16 a[170:171], v255 offset:800\n"
        "v_cvt_pk_bf16_f32 v190, v76, v77\n"
        "v_cvt_pk_bf16_f32 v191, v78, v79\n"
        "v_mfma_f32_16x16x32_bf16 v[64:67], v[168:171], v[160:163], v[64:67]\n"
        "ds_read_b64_tr_b16 a[192:193], v255 offset:64\n"
        "ds_read_b64_tr_b16 a[194:195], v255 offset:832\n"
        "v_cvt_pk_bf16_f32 v192, v192, v193\n"
        "v_cvt_pk_bf16_f32 v193, v194, v195\n"
        "v_mfma_f32_16x16x32_bf16 v[96:99], v[172:175], v[156:159], v[4:7]\n"
        "buffer_load_dwordx4 v129, s[20:23], s59 offen offset:1792 lds\n"
        "v_cvt_pk_bf16_f32 v194, v80, v81\n"
        "v_cvt_pk_bf16_f32 v195, v82, v83\n"
        "v_exp_f32 v64, v64\n"
        "v_mfma_f32_16x16x32_bf16 v[96:99], v[188:191], v[164:167], v[96:99]\n"
        "ds_read_b128 v[162:165], v252 offset:0\n"
        "v_exp_f32 v65, v65\n"
        "v_exp_f32 v66, v66\n"
        "v_cvt_pk_bf16_f32 v64, v64, v65\n"
        "v_mfma_f32_16x16x32_bf16 v[72:75], v[152:155], v[176:179], v[16:19]\n"
        "buffer_load_dwordx4 v130, s[20:23], s59 offen offset:2816 lds\n"
        "v_cvt_pk_bf16_f32 v208, v84, v85\n"
        "v_cvt_pk_bf16_f32 v209, v86, v87\n"
        "v_exp_f32 v96, v96\n"
        "v_mfma_f32_16x16x32_bf16 v[72:75], v[168:171], v[180:183], v[72:75]\n"
        "s_add_u32 m0, s65, 14592\n"
        "s_waitcnt vmcnt(9)\n"
        "ds_read_b64_tr_b16 a[148:149], v255 offset:3072\n"
        "ds_read_b64_tr_b16 a[150:151], v255 offset:3840\n"
        "v_exp_f32 v67, v67\n"
        "v_exp_f32 v97, v97\n"
        "v_mfma_f32_16x16x32_bf16 v[100:103], v[172:175], v[176:179], v[20:23]\n"
        "buffer_load_dwordx4 v128, s[20:23], s60 offen offset:768 lds\n"
        "ds_read_b64_tr_b16 a[172:173], v255 offset:3104\n"
        "ds_read_b64_tr_b16 a[174:175], v255 offset:3872\n"
        "v_cvt_pk_bf16_f32 v212, v212, v213\n"
        "v_cvt_pk_bf16_f32 v213, v214, v215\n"
        "v_mfma_f32_16x16x32_bf16 v[100:103], v[188:191], v[184:187], v[100:103]\n"
        "ds_read_b128 v[182:185], v252 offset:512\n"
        "ds_read_b64_tr_b16 a[196:197], v255 offset:3136\n"
        "ds_read_b64_tr_b16 a[198:199], v255 offset:3904\n"
        "v_cvt_pk_bf16_f32 v65, v66, v67\n"
        "v_exp_f32 v98, v98\n"
        "v_mfma_f32_16x16x32_bf16 v[68:71], v[192:195], v[156:159], v[8:11]\n"
        "s_waitcnt lgkmcnt(7)\n"
        "ds_read_b128 a[216:219], v240 offset:1536\n"
        "ds_read_b128 a[220:223], v241 offset:1536\n"
        "v_cvt_pk_bf16_f32 v66, v96, v97\n"
        "v_exp_f32 v72, v72\n"
        "v_cvt_pk_bf16_f32 v214, v88, v89\n"
        "v_mfma_f32_16x16x32_bf16 v[68:71], v[208:211], v[160:163], v[68:71]\n"
        "buffer_load_dwordx4 v129, s[20:23], s60 offen offset:1792 lds\n"
        "s_waitcnt vmcnt(8)\n"
        "ds_read_b64_tr_b16 a[152:153], v255 offset:6144\n"
        "ds_read_b64_tr_b16 a[154:155], v255 offset:6912\n"
        "v_cvt_pk_bf16_f32 v215, v90, v91\n"
        "v_cvt_pk_bf16_f32 v230, v92, v93\n"
        "v_mfma_f32_16x16x32_bf16 v[76:79], v[192:195], v[176:179], v[24:27]\n"
        "s_waitcnt lgkmcnt(6)\n"
        "ds_read_b64_tr_b16 a[176:177], v255 offset:6176\n"
        "ds_read_b64_tr_b16 a[178:179], v255 offset:6944\n"
        "v_exp_f32 v99, v99\n"
        "v_exp_f32 v73, v73\n"
        "v_mfma_f32_16x16x32_bf16 v[76:79], v[208:211], v[180:183], v[76:79]\n"
        ".long 0xbec200ff\n"
        "42:\n"
        "s_branch 60b\n"
        "buffer_load_dwordx4 v130, s[20:23], s60 offen offset:2816 lds\n"
        "ds_read_b64_tr_b16 a[200:201], v255 offset:6208\n"
        "ds_read_b64_tr_b16 a[202:203], v255 offset:6976\n"
        "v_cvt_pk_bf16_f32 v231, v94, v95\n"
        "v_cvt_pk_bf16_f32 v67, v98, v99\n"
        "v_mfma_f32_16x16x32_bf16 v[104:107], v[212:215], v[156:159], v[12:15]\n"
        "s_add_u32 m0, s40, 98304\n"
        "ds_read_b128 a[224:227], v240 offset:1664\n"
        "v_exp_f32 v74, v74\n"
        "v_mfma_f32_16x16x32_bf16 v[104:107], v[228:231], v[164:167], v[104:107]\n"
        "s_sub_u32 s64, s47, 6291456\n"
        "v_cvt_pk_bf16_f32 v72, v72, v73\n"
        "v_exp_f32 v100, v100\n"
        "v_mfma_f32_16x16x32_bf16 v[108:111], v[212:215], v[176:179], v[28:31]\n"
        "buffer_load_dwordx4 v242, s[8:11], s64 offen lds\n"
        "v_mfma_f32_16x16x32_bf16 v[108:111], v[228:231], v[184:187], v[108:111]\n"
        "s_add_u32 m0, s40, 99328\n"
        "v_exp_f32 v75, v75\n"
        "v_mfma_f32_16x16x32_bf16 v[80:83], v[152:155], v[196:199], v[32:35]\n"
        "ds_read_b128 a[228:231], v240 offset:12288\n"
        "v_exp_f32 v101, v101\n"
        "v_mfma_f32_16x16x32_bf16 v[80:83], v[168:171], v[200:203], v[80:83]\n"
        "buffer_load_dwordx4 v243, s[8:11], s64 offen lds\n"
        "v_cvt_pk_bf16_f32 v73, v74, v75\n"
        "v_mfma_f32_16x16x32_bf16 v[112:115], a[240:243], a[144:147], 0\n"
        "s_add_u32 m0, s40, 100352\n"
        "ds_read_b128 a[232:235], v241 offset:12288\n"
        "ds_read_b128 a[236:239], v240 offset:12416\n"
        "v_cvt_pk_bf16_f32 v74, v100, v101\n"
        "v_mfma_f32_16x16x32_bf16 v[112:115], a[244:247], a[148:151], v[112:115]\n"
        "s_waitcnt lgkmcnt(3)\n"
        "v_exp_f32 v102, v102\n"
        "v_mfma_f32_16x16x32_bf16 v[112:115], a[248:251], a[152:155], v[112:115]\n"
        "buffer_load_dwordx4 v244, s[8:11], s64 offen lds\n"
        "v_mfma_f32_16x16x32_bf16 v[116:119], a[248:251], a[176:179], 0\n"
        "s_add_u32 m0, s40, 110592\n"
        "v_exp_f32 v68, v68\n"
        "v_mfma_f32_16x16x32_bf16 v[116:119], a[244:247], a[172:175], v[116:119]\n"
        "s_sub_u32 s64, s47, 5505024\n"
        "v_exp_f32 v103, v103\n"
        "v_mfma_f32_16x16x32_bf16 v[116:119], a[240:243], a[168:171], v[116:119]\n"
        "buffer_load_dwordx4 v242, s[8:11], s64 offen lds\n"
        "v_cvt_pk_bf16_f32 v75, v102, v103\n"
        "v_mfma_f32_16x16x32_bf16 v[96:99], v[172:175], v[196:199], v[36:39]\n"
        "s_add_u32 m0, s40, 111616\n"
        "s_waitcnt vmcnt(10)\n"
        "ds_read_b64_tr_b16 a[156:157], v255 offset:9216\n"
        "ds_read_b64_tr_b16 a[158:159], v255 offset:9984\n"
        "v_mfma_f32_16x16x32_bf16 v[96:99], v[188:191], v[204:207], v[96:99]\n"
        "ds_read_b64_tr_b16 a[180:181], v255 offset:9248\n"
        "ds_read_b64_tr_b16 a[182:183], v255 offset:10016\n"
        "ds_read_b128 v[202:205], v252 offset:4096\n"
        "v_mfma_f32_16x16x32_bf16 v[120:123], a[240:243], a[192:195], 0\n"
        "buffer_load_dwordx4 v243, s[8:11], s64 offen lds\n"
        "ds_read_b64_tr_b16 a[204:205], v255 offset:9280\n"
        "v_mfma_f32_16x16x32_bf16 v[120:123], a[244:247], a[196:199], v[120:123]\n"
        "ds_read_b64_tr_b16 a[206:207], v255 offset:10048\n"
        "s_add_u32 m0, s40, 112640\n"
        "ds_read_b128 a[240:243], v240 offset:13824\n"
        "v_mfma_f32_16x16x32_bf16 v[120:123], a[248:251], a[200:203], v[120:123]\n"
        "s_waitcnt vmcnt(8)\n"
        "ds_read_b64_tr_b16 a[160:161], v255 offset:12288\n"
        "ds_read_b64_tr_b16 a[162:163], v255 offset:13056\n"
        "v_mfma_f32_16x16x32_bf16 v[124:127], a[216:219], a[144:147], 0\n"
        "buffer_load_dwordx4 v244, s[8:11], s64 offen lds\n"
        "ds_read_b64_tr_b16 a[184:185], v255 offset:12320\n"
        "v_mfma_f32_16x16x32_bf16 v[124:127], a[220:223], a[148:151], v[124:127]\n"
        "ds_read_b64_tr_b16 a[186:187], v255 offset:13088\n"
        "s_xor_b32 m0, s48, 8192\n"
        "ds_read_b64_tr_b16 a[208:209], v255 offset:12352\n"
        "ds_read_b64_tr_b16 a[210:211], v255 offset:13120\n"
        "v_mfma_f32_16x16x32_bf16 v[124:127], a[224:227], a[152:155], v[124:127]\n"
        "s_sub_u32 s64, s36, 2097152\n"
        "ds_read_b128 a[244:247], v241 offset:13824\n"
        "ds_read_b128 a[248:251], v240 offset:13952\n"
        "v_mfma_f32_16x16x32_bf16 v[156:159], a[224:227], a[176:179], 0\n"
        "buffer_load_dwordx4 v249, s[12:15], s64 offen lds\n"
        "v_mfma_f32_16x16x32_bf16 v[156:159], a[220:223], a[172:175], v[156:159]\n"
        "ds_read_b128 v[222:225], v251 offset:4608\n"
        "v_exp_f32 v69, v69\n"
        "v_mfma_f32_16x16x32_bf16 v[156:159], a[216:219], a[168:171], v[156:159]\n"
        "v_exp_f32 v70, v70\n"
        "v_cvt_pk_bf16_f32 v68, v68, v69\n"
        "v_mfma_f32_16x16x32_bf16 v[84:87], v[192:195], v[196:199], v[40:43]\n"
        "s_waitcnt lgkmcnt(7)\n"
        "v_exp_f32 v76, v76\n"
        "v_mfma_f32_16x16x32_bf16 v[84:87], v[208:211], v[200:203], v[84:87]\n"
        "buffer_load_dwordx4 v253, s[12:15], s64 offen offset:1024 lds\n"
        "v_mfma_f32_16x16x32_bf16 v[160:163], a[216:219], a[192:195], 0\n"
        "s_add_u32 m0, s65, 12288\n"
        "v_exp_f32 v71, v71\n"
        "v_mfma_f32_16x16x32_bf16 v[160:163], a[220:223], a[196:199], v[160:163]\n"
        "ds_read_b128 a[216:219], v241 offset:128\n"
        "v_exp_f32 v77, v77\n"
        "v_mfma_f32_16x16x32_bf16 v[160:163], a[224:227], a[200:203], v[160:163]\n"
        "ds_read_b128 a[220:223], v240 offset:256\n"
        "v_cvt_pk_bf16_f32 v69, v70, v71\n"
        "v_cvt_pk_bf16_f32 v76, v76, v77\n"
        "v_mfma_f32_16x16x32_bf16 v[164:167], a[228:231], a[144:147], 0\n"
        "s_waitcnt lgkmcnt(2)\n"
        "buffer_load_dword v136, s[52:55], 0 offen lds\n"
        "v_mfma_f32_16x16x32_bf16 v[164:167], a[232:235], a[148:151], v[164:167]\n"
        "ds_read_b128 a[224:227], v241 offset:256\n"
        "v_exp_f32 v78, v78\n"
        "v_mfma_f32_16x16x32_bf16 v[164:167], a[236:239], a[152:155], v[164:167]\n"
        "v_exp_f32 v104, v104\n"
        "v_mfma_f32_16x16x32_bf16 v[100:103], v[212:215], v[196:199], v[44:47]\n"
        "v_exp_f32 v79, v79\n"
        "v_mfma_f32_16x16x32_bf16 v[100:103], v[228:231], v[204:207], v[100:103]\n"
        "v_exp_f32 v105, v105\n"
        "v_cvt_pk_bf16_f32 v77, v78, v79\n"
        "v_mfma_f32_16x16x32_bf16 v[176:179], a[236:239], a[176:179], 0\n"
        "v_exp_f32 v106, v106\n"
        "v_cvt_pk_bf16_f32 v70, v104, v105\n"
        "v_mfma_f32_16x16x32_bf16 v[176:179], a[232:235], a[172:175], v[176:179]\n"
        "v_exp_f32 v108, v108\n"
        "v_mfma_f32_16x16x32_bf16 v[176:179], a[228:231], a[168:171], v[176:179]\n"
        "s_waitcnt vmcnt(9)\n"
        "ds_read_b64_tr_b16 a[164:165], v255 offset:15360\n"
        "ds_read_b64_tr_b16 a[166:167], v255 offset:16128\n"
        "v_mfma_f32_16x16x32_bf16 v[180:183], a[228:231], a[192:195], 0\n"
        "ds_read_b64_tr_b16 a[188:189], v255 offset:15392\n"
        "ds_read_b64_tr_b16 a[190:191], v255 offset:16160\n"
        "v_mfma_f32_16x16x32_bf16 v[180:183], a[232:235], a[196:199], v[180:183]\n"
        "ds_read_b64_tr_b16 a[212:213], v255 offset:15424\n"
        "ds_read_b64_tr_b16 a[214:215], v255 offset:16192\n"
        "v_mfma_f32_16x16x32_bf16 v[180:183], a[236:239], a[200:203], v[180:183]\n"
        "ds_read_b128 a[228:231], v241 offset:1664\n"
        "ds_read_b128 a[232:235], v240 offset:1792\n"
        "v_mfma_f32_16x16x32_bf16 v[184:187], a[240:243], a[144:147], 0\n"
        "ds_read_b128 a[236:239], v241 offset:1792\n"
        "v_exp_f32 v107, v107\n"
        "v_mfma_f32_16x16x32_bf16 v[184:187], a[244:247], a[148:151], v[184:187]\n"
        "v_exp_f32 v109, v109\n"
        "v_cvt_pk_bf16_f32 v71, v106, v107\n"
        "v_mfma_f32_16x16x32_bf16 v[184:187], a[248:251], a[152:155], v[184:187]\n"
        "v_exp_f32 v110, v110\n"
        "v_cvt_pk_bf16_f32 v78, v108, v109\n"
        "v_mfma_f32_16x16x32_bf16 v[196:199], a[248:251], a[176:179], 0\n"
        "v_exp_f32 v111, v111\n"
        "v_mfma_f32_16x16x32_bf16 v[196:199], a[244:247], a[172:175], v[196:199]\n"
        "v_cvt_pk_bf16_f32 v79, v110, v111\n"
        "v_exp_f32 v80, v80\n"
        "v_mfma_f32_16x16x32_bf16 v[196:199], a[240:243], a[168:171], v[196:199]\n"
        "v_exp_f32 v81, v81\n"
        "v_mfma_f32_16x16x32_bf16 v[200:203], a[240:243], a[192:195], 0\n"
        "v_exp_f32 v82, v82\n"
        "v_cvt_pk_bf16_f32 v80, v80, v81\n"
        "v_mfma_f32_16x16x32_bf16 v[200:203], a[244:247], a[196:199], v[200:203]\n"
        "ds_read_b128 a[240:243], v241 offset:12416\n"
        "v_exp_f32 v83, v83\n"
        "v_mfma_f32_16x16x32_bf16 v[200:203], a[248:251], a[200:203], v[200:203]\n"
        "ds_read_b128 a[244:247], v240 offset:12544\n"
        "v_cvt_pk_bf16_f32 v81, v82, v83\n"
        "v_mfma_f32_16x16x32_bf16 v[88:91], v[152:155], v[216:219], v[48:51]\n"
        "ds_read_b128 a[248:251], v241 offset:12544\n"
        "v_exp_f32 v96, v96\n"
        "v_mfma_f32_16x16x32_bf16 v[88:91], v[168:171], v[220:223], v[88:91]\n"
        "s_waitcnt lgkmcnt(3)\n"
        "v_exp_f32 v97, v97\n"
        "v_mfma_f32_16x16x32_bf16 v[112:115], a[216:219], a[156:159], v[112:115]\n"
        "v_exp_f32 v98, v98\n"
        "v_cvt_pk_bf16_f32 v82, v96, v97\n"
        "v_mfma_f32_16x16x32_bf16 v[112:115], a[220:223], a[160:163], v[112:115]\n"
        "v_exp_f32 v99, v99\n"
        "v_mfma_f32_16x16x32_bf16 v[112:115], a[224:227], a[164:167], v[112:115]\n"
        "v_cvt_pk_bf16_f32 v83, v98, v99\n"
        "v_exp_f32 v84, v84\n"
        "v_mfma_f32_16x16x32_bf16 v[116:119], a[224:227], a[188:191], v[116:119]\n"
        "v_exp_f32 v85, v85\n"
        "v_mfma_f32_16x16x32_bf16 v[116:119], a[220:223], a[184:187], v[116:119]\n"
        "v_exp_f32 v86, v86\n"
        "v_cvt_pk_bf16_f32 v84, v84, v85\n"
        "v_mfma_f32_16x16x32_bf16 v[116:119], a[216:219], a[180:183], v[116:119]\n"
        "v_exp_f32 v87, v87\n"
        "v_mfma_f32_16x16x32_bf16 v[120:123], a[216:219], a[204:207], v[120:123]\n"
        "v_cvt_pk_bf16_f32 v85, v86, v87\n"
        "v_exp_f32 v100, v100\n"
        "v_mfma_f32_16x16x32_bf16 v[120:123], a[220:223], a[208:211], v[120:123]\n"
        "ds_read_b128 a[216:219], v241 offset:13952\n"
        "v_exp_f32 v101, v101\n"
        "v_mfma_f32_16x16x32_bf16 v[120:123], a[224:227], a[212:215], v[120:123]\n"
        "ds_read_b128 a[220:223], v240 offset:14080\n"
        "v_exp_f32 v102, v102\n"
        "v_mfma_f32_16x16x32_bf16 v[124:127], a[228:231], a[156:159], v[124:127]\n"
        "ds_read_b128 a[224:227], v241 offset:14080\n"
        "v_cvt_pk_bf16_f32 v86, v100, v101\n"
        "v_cvt_pk_bf16_f32 v128, v112, v113\n"
        "v_mfma_f32_16x16x32_bf16 v[124:127], a[232:235], a[160:163], v[124:127]\n"
        "v_exp_f32 v103, v103\n"
        "v_cvt_pk_bf16_f32 v129, v114, v115\n"
        "v_mfma_f32_16x16x32_bf16 v[124:127], a[236:239], a[164:167], v[124:127]\n"
        "v_cvt_pk_bf16_f32 v87, v102, v103\n"
        "v_cvt_pk_bf16_f32 v136, v116, v117\n"
        "v_mfma_f32_16x16x32_bf16 v[104:107], v[172:175], v[216:219], v[52:55]\n"
        "v_cvt_pk_bf16_f32 v137, v118, v119\n"
        "v_cvt_pk_bf16_f32 v144, v120, v121\n"
        "v_mfma_f32_16x16x32_bf16 v[104:107], v[188:191], v[224:227], v[104:107]\n"
        "ds_read_b128 v[222:225], v252 offset:4608\n"
        "v_cvt_pk_bf16_f32 v145, v122, v123\n"
        "v_mfma_f32_16x16x32_bf16 v[156:159], a[236:239], a[188:191], v[156:159]\n"
        "v_exp_f32 v88, v88\n"
        "v_mfma_f32_16x16x32_bf16 v[156:159], a[232:235], a[184:187], v[156:159]\n"
        "v_cvt_pk_bf16_f32 v130, v124, v125\n"
        "v_cvt_pk_bf16_f32 v131, v126, v127\n"
        "v_mfma_f32_16x16x32_bf16 v[156:159], a[228:231], a[180:183], v[156:159]\n"
        "v_exp_f32 v89, v89\n"
        "v_mfma_f32_16x16x32_bf16 v[160:163], a[228:231], a[204:207], v[160:163]\n"
        "v_exp_f32 v90, v90\n"
        "v_mfma_f32_16x16x32_bf16 v[160:163], a[232:235], a[208:211], v[160:163]\n"
        "v_exp_f32 v91, v91\n"
        "v_mfma_f32_16x16x32_bf16 v[160:163], a[236:239], a[212:215], v[160:163]\n"
        "s_waitcnt lgkmcnt(1)\n"
        "v_exp_f32 v104, v104\n"
        "v_mfma_f32_16x16x32_bf16 v[164:167], a[240:243], a[156:159], v[164:167]\n"
        "v_cvt_pk_bf16_f32 v138, v156, v157\n"
        "v_cvt_pk_bf16_f32 v139, v158, v159\n"
        "v_mfma_f32_16x16x32_bf16 v[164:167], a[244:247], a[160:163], v[164:167]\n"
        "v_cvt_pk_bf16_f32 v88, v88, v89\n"
        "v_cvt_pk_bf16_f32 v89, v90, v91\n"
        "v_mfma_f32_16x16x32_bf16 v[164:167], a[248:251], a[164:167], v[164:167]\n"
        "v_exp_f32 v105, v105\n"
        "v_mfma_f32_16x16x32_bf16 v[92:95], v[192:195], v[216:219], v[56:59]\n"
        "s_waitcnt lgkmcnt(0)\n"
        "v_cvt_pk_bf16_f32 v146, v160, v161\n"
        "v_cvt_pk_bf16_f32 v147, v162, v163\n"
        "v_mfma_f32_16x16x32_bf16 v[92:95], v[208:211], v[220:223], v[92:95]\n"
        "v_exp_f32 v106, v106\n"
        "v_mfma_f32_16x16x32_bf16 v[176:179], a[248:251], a[188:191], v[176:179]\n"
        "v_exp_f32 v107, v107\n"
        "v_mfma_f32_16x16x32_bf16 v[176:179], a[244:247], a[184:187], v[176:179]\n"
        "v_cvt_pk_bf16_f32 v132, v164, v165\n"
        "v_cvt_pk_bf16_f32 v133, v166, v167\n"
        "v_mfma_f32_16x16x32_bf16 v[176:179], a[240:243], a[180:183], v[176:179]\n"
        "v_cvt_pk_bf16_f32 v90, v104, v105\n"
        "v_cvt_pk_bf16_f32 v91, v106, v107\n"
        "v_mfma_f32_16x16x32_bf16 v[180:183], a[240:243], a[204:207], v[180:183]\n"
        "v_exp_f32 v92, v92\n"
        "v_mfma_f32_16x16x32_bf16 v[180:183], a[244:247], a[208:211], v[180:183]\n"
        "v_exp_f32 v93, v93\n"
        "v_mfma_f32_16x16x32_bf16 v[180:183], a[248:251], a[212:215], v[180:183]\n"
        "v_mov_b64 v[96:97], 0\n"
        "v_cvt_pk_bf16_f32 v140, v176, v177\n"
        "v_mfma_f32_16x16x32_bf16 v[184:187], a[216:219], a[156:159], v[184:187]\n"
        "v_cvt_pk_bf16_f32 v141, v178, v179\n"
        "v_cvt_pk_bf16_f32 v92, v92, v93\n"
        "v_mfma_f32_16x16x32_bf16 v[184:187], a[220:223], a[160:163], v[184:187]\n"
        "v_exp_f32 v94, v94\n"
        "v_mfma_f32_16x16x32_bf16 v[184:187], a[224:227], a[164:167], v[184:187]\n"
        "v_exp_f32 v95, v95\n"
        "v_mfma_f32_16x16x32_bf16 v[108:111], v[212:215], v[216:219], v[60:63]\n"
        "v_cvt_pk_bf16_f32 v148, v180, v181\n"
        "v_cvt_pk_bf16_f32 v149, v182, v183\n"
        "v_mfma_f32_16x16x32_bf16 v[108:111], v[228:231], v[224:227], v[108:111]\n"
        "v_add_u32 v240, 24576, v240\n"
        "v_add_u32 v241, 24576, v241\n"
        "v_mfma_f32_16x16x32_bf16 v[196:199], a[224:227], a[188:191], v[196:199]\n"
        "v_cvt_pk_bf16_f32 v134, v184, v185\n"
        "v_cvt_pk_bf16_f32 v135, v186, v187\n"
        "v_mfma_f32_16x16x32_bf16 v[196:199], a[220:223], a[184:187], v[196:199]\n"
        "v_mov_b64 v[172:173], 0\n"
        "v_cvt_pk_bf16_f32 v93, v94, v95\n"
        "v_mfma_f32_16x16x32_bf16 v[196:199], a[216:219], a[180:183], v[196:199]\n"
        "v_exp_f32 v108, v108\n"
        "v_mfma_f32_16x16x32_bf16 v[200:203], a[216:219], a[204:207], v[200:203]\n"
        "v_exp_f32 v109, v109\n"
        "v_mfma_f32_16x16x32_bf16 v[200:203], a[220:223], a[208:211], v[200:203]\n"
        "v_exp_f32 v110, v110\n"
        "v_mfma_f32_16x16x32_bf16 v[200:203], a[224:227], a[212:215], v[200:203]\n"
        "v_exp_f32 v111, v111\n"
        "v_smfmac_f32_16x16x64_bf16 v[96:99], a[252:255], v[64:71], v237\n"
        "v_cvt_pk_bf16_f32 v142, v196, v197\n"
        "v_mfma_f32_16x16x32_bf16 v[100:103], v[132:135], v[68:71], 0\n"
        "v_cvt_pk_bf16_f32 v143, v198, v199\n"
        "v_cvt_pk_bf16_f32 v94, v108, v109\n"
        "v_mfma_f32_16x16x32_bf16 v[100:103], v[128:131], v[64:67], v[100:103]\n"
        "v_mov_b64 v[192:193], 0\n"
        "v_cvt_pk_bf16_f32 v150, v200, v201\n"
        "v_mfma_f32_16x16x32_bf16 v[104:107], v[136:139], v[64:67], 0\n"
        "v_cvt_pk_bf16_f32 v151, v202, v203\n"
        "v_add_f32 v97, v96, v97\n"
        "v_mfma_f32_16x16x32_bf16 v[104:107], v[140:143], v[68:71], v[104:107]\n"
        "v_rcp_f32 v96, v97\n"
        "v_cvt_pk_bf16_f32 v95, v110, v111\n"
        "v_mfma_f32_16x16x32_bf16 v[108:111], v[148:151], v[68:71], 0\n"
        "v_mul_f32 v100, v96, v100\n"
        "v_mul_f32 v101, v96, v101\n"
        "v_mfma_f32_16x16x32_bf16 v[108:111], v[144:147], v[64:67], v[108:111]\n"
        "v_mul_f32 v102, v96, v102\n"
        "v_mul_f32 v103, v96, v103\n"
        "v_smfmac_f32_16x16x64_bf16 v[172:175], a[252:255], v[72:79], v237\n"
        "s_waitcnt vmcnt(6)\n"
        "s_barrier\n"
        "ds_read_b128 a[236:239], v240 offset:0\n"
        "v_mfma_f32_16x16x32_bf16 v[176:179], v[132:135], v[76:79], 0\n"
        "ds_read_b128 a[240:243], v241 offset:0\n"
        "ds_read_b128 a[244:247], v240 offset:128\n"
        "v_mfma_f32_16x16x32_bf16 v[176:179], v[128:131], v[72:75], v[176:179]\n"
        "ds_read_b128 a[248:251], v241 offset:128\n"
        "ds_read_b128 a[216:219], v240 offset:256\n"
        "v_mfma_f32_16x16x32_bf16 v[180:183], v[136:139], v[72:75], 0\n"
        "ds_read_b128 a[220:223], v241 offset:256\n"
        "v_cvt_pk_bf16_f32 v100, v100, v101\n"
        "v_mfma_f32_16x16x32_bf16 v[180:183], v[140:143], v[76:79], v[180:183]\n"
        "v_cvt_pk_bf16_f32 v101, v102, v103\n"
        "v_mul_f32 v104, v96, v104\n"
        "v_mfma_f32_16x16x32_bf16 v[184:187], v[148:151], v[76:79], 0\n"
        "v_mul_f32 v105, v96, v105\n"
        "v_mul_f32 v106, v96, v106\n"
        "v_mfma_f32_16x16x32_bf16 v[184:187], v[144:147], v[72:75], v[184:187]\n"
        "v_mul_f32 v107, v96, v107\n"
        "v_cvt_pk_bf16_f32 v102, v104, v105\n"
        "v_smfmac_f32_16x16x64_bf16 v[192:195], a[252:255], v[80:87], v237\n"
        "v_cvt_pk_bf16_f32 v103, v106, v107\n"
        "v_mfma_f32_16x16x32_bf16 v[196:199], v[132:135], v[84:87], 0\n"
        "ds_write_b128 v245, v[100:103] offset:0\n"
        "v_mfma_f32_16x16x32_bf16 v[196:199], v[128:131], v[80:83], v[196:199]\n"
        "buffer_load_dwordx4 v238, s[52:55], s63 offen lds\n"
        "v_mfma_f32_16x16x32_bf16 v[200:203], v[136:139], v[80:83], 0\n"
        "v_mov_b64 v[124:125], 0\n"
        "v_mul_f32 v108, v96, v108\n"
        "v_mfma_f32_16x16x32_bf16 v[200:203], v[140:143], v[84:87], v[200:203]\n"
        "v_mul_f32 v109, v96, v109\n"
        "v_mul_f32 v110, v96, v110\n"
        "v_mul_f32 v111, v96, v111\n"
        "v_mfma_f32_16x16x32_bf16 v[204:207], v[148:151], v[84:87], 0\n"
        "v_cvt_pk_bf16_f32 v108, v108, v109\n"
        "v_cvt_pk_bf16_f32 v109, v110, v111\n"
        "v_mfma_f32_16x16x32_bf16 v[204:207], v[144:147], v[80:83], v[204:207]\n"
        "s_waitcnt lgkmcnt(1)\n"
        "ds_write_b64 v246, v[108:109] offset:0\n"
        "v_mfma_f32_16x16x32_bf16 v[112:115], a[72:75], a[236:239], 0\n"
        "v_max_u32 v236, v96, v97\n"
        "v_add_f32 v173, v172, v173\n"
        "v_mfma_f32_16x16x32_bf16 v[112:115], a[76:79], a[240:243], v[112:115]\n"
        "v_rcp_f32 v172, v173\n"
        "v_mfma_f32_16x16x32_bf16 v[112:115], a[80:83], a[244:247], v[112:115]\n"
        "v_mul_f32 v176, v172, v176\n"
        "v_mul_f32 v177, v172, v177\n"
        "v_mfma_f32_16x16x32_bf16 v[112:115], a[84:87], a[248:251], v[112:115]\n"
        "buffer_load_dwordx4 v239, s[52:55], s63 offen offset:1024 lds\n"
        "v_mfma_f32_16x16x32_bf16 v[112:115], a[88:91], a[216:219], v[112:115]\n"
        "v_mul_f32 v178, v172, v178\n"
        "v_mul_f32 v179, v172, v179\n"
        "v_mfma_f32_16x16x32_bf16 v[112:115], a[92:95], a[220:223], v[112:115]\n"
        "v_cvt_pk_bf16_f32 v176, v176, v177\n"
        "v_cvt_pk_bf16_f32 v177, v178, v179\n"
        "v_smfmac_f32_16x16x64_bf16 v[124:127], a[252:255], v[88:95], v237\n"
        "v_mul_f32 v180, v172, v180\n"
        "v_mfma_f32_16x16x32_bf16 v[80:83], v[132:135], v[92:95], 0\n"
        "v_mul_f32 v181, v172, v181\n"
        "v_mul_f32 v182, v172, v182\n"
        "v_mfma_f32_16x16x32_bf16 v[80:83], v[128:131], v[88:91], v[80:83]\n"
        "v_cvt_pk_bf16_f32 v152, v112, v113\n"
        "v_cvt_pk_bf16_f32 v153, v114, v115\n"
        "v_mfma_f32_16x16x32_bf16 v[84:87], v[136:139], v[88:91], 0\n"
        "v_mul_f32 v183, v172, v183\n"
        "v_cvt_pk_bf16_f32 v178, v180, v181\n"
        "v_mfma_f32_16x16x32_bf16 v[84:87], v[140:143], v[92:95], v[84:87]\n"
        "v_cvt_pk_bf16_f32 v179, v182, v183\n"
        "v_mul_f32 v184, v172, v184\n"
        "v_mfma_f32_16x16x32_bf16 v[96:99], v[148:151], v[92:95], 0\n"
        "buffer_load_dwordx4 v247, s[52:55], s63 offen offset:2048 lds\n"
        "v_mfma_f32_16x16x32_bf16 v[96:99], v[144:147], v[88:91], v[96:99]\n"
        "ds_write_b128 v245, v[176:179] offset:256\n"
        "v_mfma_f32_16x16x32_bf16 v[116:119], a[116:119], a[220:223], 0\n"
        "v_mul_f32 v185, v172, v185\n"
        "v_mul_f32 v186, v172, v186\n"
        "v_mfma_f32_16x16x32_bf16 v[116:119], a[112:115], a[216:219], v[116:119]\n"
        "v_mul_f32 v187, v172, v187\n"
        "v_add_f32 v125, v124, v125\n"
        "v_mfma_f32_16x16x32_bf16 v[116:119], a[108:111], a[248:251], v[116:119]\n"
        "v_rcp_f32 v124, v125\n"
        "v_mfma_f32_16x16x32_bf16 v[116:119], a[104:107], a[244:247], v[116:119]\n"
        "s_add_u32 m0, s65, 15360\n"
        "v_cvt_pk_bf16_f32 v184, v184, v185\n"
        "v_cvt_pk_bf16_f32 v185, v186, v187\n"
        "v_mfma_f32_16x16x32_bf16 v[116:119], a[100:103], a[240:243], v[116:119]\n"
        "ds_write_b64 v246, v[184:185] offset:256\n"
        "v_mfma_f32_16x16x32_bf16 v[116:119], a[96:99], a[236:239], v[116:119]\n"
        "v_mul_f32 v80, v124, v80\n"
        "v_mul_f32 v81, v124, v81\n"
        "v_mfma_f32_16x16x32_bf16 v[120:123], a[120:123], a[236:239], 0\n"
        "buffer_load_dwordx4 v238, s[52:55], s34 offen lds\n"
        "v_mfma_f32_16x16x32_bf16 v[120:123], a[124:127], a[240:243], v[120:123]\n"
        "v_mul_f32 v82, v124, v82\n"
        "v_mul_f32 v83, v124, v83\n"
        "v_mfma_f32_16x16x32_bf16 v[120:123], a[128:131], a[244:247], v[120:123]\n"
        "v_max3_u32 v236, v236, v172, v173\n"
        "v_cvt_pk_bf16_f32 v80, v80, v81\n"
        "v_mfma_f32_16x16x32_bf16 v[120:123], a[132:135], a[248:251], v[120:123]\n"
        "v_cvt_pk_bf16_f32 v81, v82, v83\n"
        "v_mul_f32 v84, v124, v84\n"
        "v_mfma_f32_16x16x32_bf16 v[120:123], a[136:139], a[216:219], v[120:123]\n"
        "v_mul_f32 v85, v124, v85\n"
        "v_mul_f32 v86, v124, v86\n"
        "v_mfma_f32_16x16x32_bf16 v[120:123], a[140:143], a[220:223], v[120:123]\n"
        "v_mul_f32 v87, v124, v87\n"
        "v_cvt_pk_bf16_f32 v82, v84, v85\n"
        "v_mfma_f32_16x16x32_bf16 v[112:115], a[20:23], a[220:223], 0\n"
        "v_cvt_pk_bf16_f32 v83, v86, v87\n"
        "v_mul_f32 v96, v124, v96\n"
        "v_mfma_f32_16x16x32_bf16 v[112:115], a[16:19], a[216:219], v[112:115]\n"
        "ds_write_b128 v245, v[80:83] offset:768\n"
        "v_mfma_f32_16x16x32_bf16 v[112:115], a[12:15], a[248:251], v[112:115]\n"
        "buffer_load_dwordx4 v239, s[52:55], s34 offen offset:1024 lds\n"
        "v_mfma_f32_16x16x32_bf16 v[112:115], a[8:11], a[244:247], v[112:115]\n"
        "v_mul_f32 v97, v124, v97\n"
        "v_mul_f32 v98, v124, v98\n"
        "v_mfma_f32_16x16x32_bf16 v[112:115], a[4:7], a[240:243], v[112:115]\n"
        "v_mul_f32 v99, v124, v99\n"
        "v_cvt_pk_bf16_f32 v154, v116, v117\n"
        "v_mfma_f32_16x16x32_bf16 v[112:115], a[0:3], a[236:239], v[112:115]\n"
        "v_cvt_pk_bf16_f32 v155, v118, v119\n"
        "v_cvt_pk_bf16_f32 v96, v96, v97\n"
        "v_mfma_f32_16x16x32_bf16 v[116:119], a[24:27], a[236:239], 0\n"
        "v_cvt_pk_bf16_f32 v97, v98, v99\n"
        "v_cvt_pk_bf16_f32 v168, v120, v121\n"
        "v_mfma_f32_16x16x32_bf16 v[116:119], a[28:31], a[240:243], v[116:119]\n"
        "ds_write_b64 v246, v[96:97] offset:768\n"
        "v_mfma_f32_16x16x32_bf16 v[116:119], a[32:35], a[244:247], v[116:119]\n"
        "v_cvt_pk_bf16_f32 v169, v122, v123\n"
        "v_add_f32 v193, v192, v193\n"
        "v_mfma_f32_16x16x32_bf16 v[116:119], a[36:39], a[248:251], v[116:119]\n"
        "v_rcp_f32 v192, v193\n"
        "v_mfma_f32_16x16x32_bf16 v[116:119], a[40:43], a[216:219], v[116:119]\n"
        "buffer_load_dwordx4 v247, s[52:55], s34 offen offset:2048 lds\n"
        "v_mfma_f32_16x16x32_bf16 v[116:119], a[44:47], a[220:223], v[116:119]\n"
        "v_mul_f32 v196, v192, v196\n"
        "v_mul_f32 v197, v192, v197\n"
        "v_mfma_f32_16x16x32_bf16 v[120:123], a[68:71], a[220:223], 0\n"
        "v_mul_f32 v198, v192, v198\n"
        "v_mul_f32 v199, v192, v199\n"
        "v_mfma_f32_16x16x32_bf16 v[120:123], a[64:67], a[216:219], v[120:123]\n"
        "v_cvt_pk_bf16_f32 v156, v112, v113\n"
        "v_cvt_pk_bf16_f32 v157, v114, v115\n"
        "v_mfma_f32_16x16x32_bf16 v[120:123], a[60:63], a[248:251], v[120:123]\n"
        "ds_read_b128 a[224:227], v240 offset:1536\n"
        "v_cvt_pk_bf16_f32 v196, v196, v197\n"
        "v_mfma_f32_16x16x32_bf16 v[120:123], a[56:59], a[244:247], v[120:123]\n"
        "s_add_u32 m0, s65, 0\n"
        "ds_read_b128 a[228:231], v241 offset:1536\n"
        "v_cvt_pk_bf16_f32 v197, v198, v199\n"
        "v_mfma_f32_16x16x32_bf16 v[120:123], a[52:55], a[240:243], v[120:123]\n"
        "ds_read_b128 a[232:235], v240 offset:1664\n"
        "v_mul_f32 v200, v192, v200\n"
        "v_mfma_f32_16x16x32_bf16 v[120:123], a[48:51], a[236:239], v[120:123]\n"
        "v_mul_f32 v201, v192, v201\n"
        "v_mul_f32 v202, v192, v202\n"
        "v_mfma_f32_16x16x32_bf16 v[112:115], a[236:239], a[144:147], 0\n"
        "buffer_load_dwordx4 v232, s[52:55], 0 offen lds\n"
        "v_mfma_f32_16x16x32_bf16 v[112:115], a[240:243], a[148:151], v[112:115]\n"
        "v_mul_f32 v203, v192, v203\n"
        "v_cvt_pk_bf16_f32 v198, v200, v201\n"
        "v_mfma_f32_16x16x32_bf16 v[112:115], a[244:247], a[152:155], v[112:115]\n"
        "v_cvt_pk_bf16_f32 v199, v202, v203\n"
        "v_mul_f32 v204, v192, v204\n"
        "v_mfma_f32_16x16x32_bf16 v[112:115], a[248:251], a[156:159], v[112:115]\n"
        "ds_write_b128 v245, v[196:199] offset:512\n"
        "v_mfma_f32_16x16x32_bf16 v[112:115], a[216:219], a[160:163], v[112:115]\n"
        "v_mul_f32 v205, v192, v205\n"
        "v_mul_f32 v206, v192, v206\n"
        "v_mfma_f32_16x16x32_bf16 v[112:115], a[220:223], a[164:167], v[112:115]\n"
        "v_mul_f32 v207, v192, v207\n"
        "v_cvt_pk_bf16_f32 v158, v116, v117\n"
        "v_cvt_pk_bf16_f32 v159, v118, v119\n"
        "v_mfma_f32_16x16x32_bf16 v[116:119], a[220:223], a[188:191], 0\n"
        "v_cvt_pk_bf16_f32 v204, v204, v205\n"
        "v_cvt_pk_bf16_f32 v205, v206, v207\n"
        "v_mfma_f32_16x16x32_bf16 v[116:119], a[216:219], a[184:187], v[116:119]\n"
        "ds_write_b64 v246, v[204:205] offset:512\n"
        "v_mfma_f32_16x16x32_bf16 v[116:119], a[248:251], a[180:183], v[116:119]\n"
        "buffer_load_dwordx4 v233, s[52:55], 0 offen offset:1024 lds\n"
        "v_mfma_f32_16x16x32_bf16 v[116:119], a[244:247], a[176:179], v[116:119]\n"
        "s_waitcnt vmcnt(5)\n"
        "ds_read_b64_tr_b16 v[64:65], v255 offset:12288\n"
        "ds_read_b64_tr_b16 v[66:67], v255 offset:13056\n"
        "v_mfma_f32_16x16x32_bf16 v[116:119], a[240:243], a[172:175], v[116:119]\n"
        "v_max3_u32 v236, v236, v192, v193\n"
        "v_max3_u32 v236, v236, v124, v125\n"
        "v_mfma_f32_16x16x32_bf16 v[116:119], a[236:239], a[168:171], v[116:119]\n"
        "ds_read_b64_tr_b16 v[68:69], v255 offset:12320\n"
        "ds_read_b64_tr_b16 v[70:71], v255 offset:13088\n"
        "v_cvt_pk_bf16_f32 v160, v120, v121\n"
        "v_cvt_pk_bf16_f32 v161, v122, v123\n"
        "v_cvt_pk_bf16_f32 v166, v120, v121\n"
        "v_cvt_pk_bf16_f32 v167, v122, v123\n"
        "v_mfma_f32_16x16x32_bf16 v[120:123], a[236:239], a[192:195], 0\n"
        "ds_read_b128 a[236:239], v241 offset:1664\n"
        "v_cmp_lt_u32 vcc, s24, v236\n"
        "v_mfma_f32_16x16x32_bf16 v[120:123], a[240:243], a[196:199], v[120:123]\n"
        "ds_read_b128 a[240:243], v240 offset:1792\n"
        "s_cmp_lg_u64 vcc, 0\n"
        "s_addc_u32 s25, s25, s25\n"
        "v_mfma_f32_16x16x32_bf16 v[120:123], a[244:247], a[200:203], v[120:123]\n"
        "ds_read_b64_tr_b16 v[72:73], v255 offset:12352\n"
        "ds_read_b64_tr_b16 v[74:75], v255 offset:13120\n"
        "v_mfma_f32_16x16x32_bf16 v[120:123], a[248:251], a[204:207], v[120:123]\n"
        "ds_read_b128 a[244:247], v241 offset:1792\n"
        "v_cvt_pk_bf16_f32 v128, v112, v113\n"
        "v_mfma_f32_16x16x32_bf16 v[120:123], a[216:219], a[208:211], v[120:123]\n"
        "s_waitcnt lgkmcnt(7)\n"
        "ds_write_b128 v235, v[64:67] offset:12288\n"
        "v_mfma_f32_16x16x32_bf16 v[120:123], a[220:223], a[212:215], v[120:123]\n"
        "v_cvt_pk_bf16_f32 v129, v114, v115\n"
        "v_and_b32 v239, 32, v245\n"
        "v_mfma_f32_16x16x32_bf16 v[112:115], a[72:75], a[224:227], 0\n"
        "buffer_load_dwordx4 v234, s[52:55], 0 offen offset:2048 lds\n"
        "v_mfma_f32_16x16x32_bf16 v[112:115], a[76:79], a[228:231], v[112:115]\n"
        "s_waitcnt vmcnt(3)\n"
        "ds_read_b64_tr_b16 v[76:77], v255 offset:15360\n"
        "ds_read_b64_tr_b16 v[78:79], v255 offset:16128\n"
        "v_mfma_f32_16x16x32_bf16 v[112:115], a[80:83], a[232:235], v[112:115]\n"
        "s_waitcnt lgkmcnt(6)\n"
        "v_cvt_pk_bf16_f32 v136, v116, v117\n"
        "v_cvt_pk_bf16_f32 v137, v118, v119\n"
        "v_mfma_f32_16x16x32_bf16 v[112:115], a[84:87], a[236:239], v[112:115]\n"
        "s_add_u32 m0, s65, 3072\n"
        "v_xor_b32 v251, 0x2000, v251\n"
        "v_xor_b32 v252, 0x2000, v252\n"
        "v_mfma_f32_16x16x32_bf16 v[112:115], a[88:91], a[240:243], v[112:115]\n"
        "s_waitcnt lgkmcnt(3)\n"
        "v_mad_i32_i24 v239, v239, -12, v235\n"
        "v_cvt_pk_bf16_f32 v144, v120, v121\n"
        "v_mfma_f32_16x16x32_bf16 v[112:115], a[92:95], a[244:247], v[112:115]\n"
        "ds_write_b128 v239, v[68:71] offset:12480\n"
        "s_barrier\n"
        "v_mfma_f32_16x16x32_bf16 v[116:119], a[116:119], a[244:247], 0\n"
        "buffer_load_dwordx4 v232, s[52:55], s61 offen lds\n"
        "v_mfma_f32_16x16x32_bf16 v[116:119], a[112:115], a[240:243], v[116:119]\n"
        "ds_read_b64_tr_b16 v[88:89], v255 offset:15392\n"
        "ds_read_b64_tr_b16 v[90:91], v255 offset:16160\n"
        "v_mfma_f32_16x16x32_bf16 v[116:119], a[108:111], a[236:239], v[116:119]\n"
        "v_cvt_pk_bf16_f32 v145, v122, v123\n"
        "v_cvt_pk_bf16_f32 v172, v112, v113\n"
        "v_mfma_f32_16x16x32_bf16 v[116:119], a[104:107], a[232:235], v[116:119]\n"
        "v_cvt_pk_bf16_f32 v173, v114, v115\n"
        "v_add_u32 v247, s45, v245\n"
        "v_mfma_f32_16x16x32_bf16 v[116:119], a[100:103], a[228:231], v[116:119]\n"
        "v_mfma_f32_16x16x32_bf16 v[116:119], a[96:99], a[224:227], v[116:119]\n"
        "ds_write_b128 v254, v[72:75] offset:12288\n"
        "v_mfma_f32_16x16x32_bf16 v[120:123], a[120:123], a[224:227], 0\n"
        "s_nop 2\n"
        "v_mfma_f32_16x16x32_bf16 v[120:123], a[124:127], a[228:231], v[120:123]\n"
        "ds_read_b128 v[162:165], v251 offset:0\n"
        "s_nop 0\n"
        "v_mfma_f32_16x16x32_bf16 v[120:123], a[128:131], a[232:235], v[120:123]\n"
        "buffer_load_dwordx4 v233, s[52:55], s61 offen offset:1024 lds\n"
        "v_mfma_f32_16x16x32_bf16 v[120:123], a[132:135], a[236:239], v[120:123]\n"
        "ds_read_b64_tr_b16 v[92:93], v255 offset:15424\n"
        "ds_read_b64_tr_b16 v[94:95], v255 offset:16192\n"
        "v_mfma_f32_16x16x32_bf16 v[120:123], a[136:139], a[240:243], v[120:123]\n"
        "v_cvt_pk_bf16_f32 v174, v116, v117\n"
        "v_cvt_pk_bf16_f32 v175, v118, v119\n"
        "v_mfma_f32_16x16x32_bf16 v[120:123], a[140:143], a[244:247], v[120:123]\n"
        "s_nop 2\n"
        "v_mfma_f32_16x16x32_bf16 v[112:115], a[20:23], a[244:247], 0\n"
        "s_nop 2\n"
        "v_mfma_f32_16x16x32_bf16 v[112:115], a[16:19], a[240:243], v[112:115]\n"
        "v_cvt_pk_bf16_f32 v190, v120, v121\n"
        "v_cvt_pk_bf16_f32 v191, v122, v123\n"
        "v_mfma_f32_16x16x32_bf16 v[112:115], a[12:15], a[236:239], v[112:115]\n"
        "v_mfma_f32_16x16x32_bf16 v[112:115], a[8:11], a[232:235], v[112:115]\n"
        "s_waitcnt vmcnt(2)\n"
        "ds_read_b64_tr_b16 v[212:213], v255 offset:0\n"
        "ds_read_b64_tr_b16 v[214:215], v255 offset:768\n"
        "v_mfma_f32_16x16x32_bf16 v[112:115], a[4:7], a[228:231], v[112:115]\n"
        "buffer_load_dwordx4 v234, s[52:55], s61 offen offset:2048 lds\n"
        "v_mfma_f32_16x16x32_bf16 v[112:115], a[0:3], a[224:227], v[112:115]\n"
        "ds_read_b128 v[182:185], v251 offset:512\n"
        "s_nop 0\n"
        "v_mfma_f32_16x16x32_bf16 v[116:119], a[24:27], a[224:227], 0\n"
        "s_nop 2\n"
        "v_mfma_f32_16x16x32_bf16 v[116:119], a[28:31], a[228:231], v[116:119]\n"
        "s_waitcnt lgkmcnt(3)\n"
        "ds_write_b128 v235, v[76:79] offset:15360\n"
        "v_mfma_f32_16x16x32_bf16 v[116:119], a[32:35], a[232:235], v[116:119]\n"
        "v_cvt_pk_bf16_f32 v176, v112, v113\n"
        "v_cvt_pk_bf16_f32 v177, v114, v115\n"
        "v_mfma_f32_16x16x32_bf16 v[116:119], a[36:39], a[236:239], v[116:119]\n"
        "s_add_u32 m0, s65, 6144\n"
        "v_mfma_f32_16x16x32_bf16 v[116:119], a[40:43], a[240:243], v[116:119]\n"
        "ds_read_b64_tr_b16 v[216:217], v255 offset:32\n"
        "ds_read_b64_tr_b16 v[218:219], v255 offset:800\n"
        "v_mfma_f32_16x16x32_bf16 v[116:119], a[44:47], a[244:247], v[116:119]\n"
        "s_nop 2\n"
        "v_mfma_f32_16x16x32_bf16 v[120:123], a[68:71], a[244:247], 0\n"
        ".long 0xbec200ff\n"
        "43:\n"
        "s_branch 60b\n"
        "buffer_load_dwordx4 v232, s[52:55], s56 offen lds\n"
        "v_mfma_f32_16x16x32_bf16 v[120:123], a[64:67], a[240:243], v[120:123]\n"
        "s_nop 1\n"
        "v_cvt_pk_bf16_f32 v178, v116, v117\n"
        "v_mfma_f32_16x16x32_bf16 v[120:123], a[60:63], a[236:239], v[120:123]\n"
        "ds_write_b128 v239, v[88:91] offset:15552\n"
        "v_mfma_f32_16x16x32_bf16 v[120:123], a[56:59], a[232:235], v[120:123]\n"
        "v_cvt_pk_bf16_f32 v179, v118, v119\n"
        "v_mfma_f32_16x16x32_bf16 v[120:123], a[52:55], a[228:231], v[120:123]\n"
        "v_mfma_f32_16x16x32_bf16 v[120:123], a[48:51], a[224:227], v[120:123]\n"
        "ds_read_b64_tr_b16 v[220:221], v255 offset:64\n"
        "ds_read_b64_tr_b16 v[222:223], v255 offset:832\n"
        "v_mfma_f32_16x16x32_bf16 v[112:115], a[224:227], a[144:147], 0\n"
        "s_nop 2\n"
        "v_mfma_f32_16x16x32_bf16 v[112:115], a[228:231], a[148:151], v[112:115]\n"
        "s_nop 0\n"
        "v_cvt_pk_bf16_f32 v180, v120, v121\n"
        "v_cvt_pk_bf16_f32 v181, v122, v123\n"
        "v_mfma_f32_16x16x32_bf16 v[112:115], a[232:235], a[152:155], v[112:115]\n"
        "buffer_load_dwordx4 v233, s[52:55], s56 offen offset:1024 lds\n"
        "v_mfma_f32_16x16x32_bf16 v[112:115], a[236:239], a[156:159], v[112:115]\n"
        "ds_write_b128 v254, v[92:95] offset:15360\n"
        "v_mfma_f32_16x16x32_bf16 v[112:115], a[240:243], a[160:163], v[112:115]\n"
        "v_cvt_pk_bf16_f32 v186, v120, v121\n"
        "v_cvt_pk_bf16_f32 v187, v122, v123\n"
        "v_mfma_f32_16x16x32_bf16 v[112:115], a[244:247], a[164:167], v[112:115]\n"
        "ds_read_b128 a[248:251], v240 offset:12288\n"
        "s_nop 0\n"
        "v_mfma_f32_16x16x32_bf16 v[96:99], v[152:155], v[156:159], v[0:3]\n"
        "s_waitcnt vmcnt(2)\n"
        "ds_read_b64_tr_b16 v[224:225], v255 offset:3072\n"
        "ds_read_b64_tr_b16 v[226:227], v255 offset:3840\n"
        "v_mfma_f32_16x16x32_bf16 v[96:99], v[168:171], v[160:163], v[96:99]\n"
        "ds_read_b128 a[216:219], v241 offset:12288\n"
        "s_sub_u32 s31, s30, s28\n"
        "v_cvt_pk_bf16_f32 v130, v112, v113\n"
        "v_mfma_f32_16x16x32_bf16 v[100:103], v[172:175], v[156:159], v[4:7]\n"
        "ds_read_b128 a[220:223], v240 offset:12416\n"
        "s_sub_u32 s45, s28, s29\n"
        "v_cvt_pk_bf16_f32 v131, v114, v115\n"
        "v_mfma_f32_16x16x32_bf16 v[100:103], v[188:191], v[164:167], v[100:103]\n"
        "s_add_u32 s28, s28, s31\n"
        "ds_read_b128 v[162:165], v252 offset:0\n"
        "v_mfma_f32_16x16x32_bf16 v[116:119], a[244:247], a[188:191], 0\n"
        "buffer_load_dwordx4 v234, s[52:55], s56 offen offset:2048 lds\n"
        "v_mfma_f32_16x16x32_bf16 v[116:119], a[240:243], a[184:187], v[116:119]\n"
        "s_waitcnt lgkmcnt(7)\n"
        "ds_write_b128 v235, v[212:215] offset:0\n"
        "v_mfma_f32_16x16x32_bf16 v[116:119], a[236:239], a[180:183], v[116:119]\n"
        "v_exp_f32 v96, v96\n"
        "v_mfma_f32_16x16x32_bf16 v[116:119], a[232:235], a[176:179], v[116:119]\n"
        "ds_read_b64_tr_b16 v[88:89], v255 offset:3104\n"
        "ds_read_b64_tr_b16 v[90:91], v255 offset:3872\n"
        "v_mfma_f32_16x16x32_bf16 v[116:119], a[228:231], a[172:175], v[116:119]\n"
        "v_exp_f32 v97, v97\n"
        "v_mfma_f32_16x16x32_bf16 v[116:119], a[224:227], a[168:171], v[116:119]\n"
        "s_add_u32 m0, s65, 9216\n"
        "v_exp_f32 v98, v98\n"
        "v_mfma_f32_16x16x32_bf16 v[104:107], v[152:155], v[176:179], v[16:19]\n"
        "v_exp_f32 v100, v100\n"
        "v_mfma_f32_16x16x32_bf16 v[104:107], v[168:171], v[180:183], v[104:107]\n"
        "ds_write_b128 v239, v[216:219] offset:192\n"
        "v_mfma_f32_16x16x32_bf16 v[108:111], v[172:175], v[176:179], v[20:23]\n"
        "buffer_load_dwordx4 v232, s[52:55], s62 offen lds\n"
        "v_mfma_f32_16x16x32_bf16 v[108:111], v[188:191], v[184:187], v[108:111]\n"
        "ds_read_b128 v[182:185], v252 offset:512\n"
        "v_cvt_pk_bf16_f32 v64, v96, v97\n"
        "v_mfma_f32_16x16x32_bf16 v[120:123], a[224:227], a[192:195], 0\n"
        "ds_read_b128 a[224:227], v241 offset:12416\n"
        "ds_read_b64_tr_b16 v[92:93], v255 offset:3136\n"
        "v_mfma_f32_16x16x32_bf16 v[120:123], a[228:231], a[196:199], v[120:123]\n"
        "ds_read_b64_tr_b16 v[94:95], v255 offset:3904\n"
        "ds_read_b128 a[228:231], v240 offset:12544\n"
        "v_mfma_f32_16x16x32_bf16 v[120:123], a[232:235], a[200:203], v[120:123]\n"
        "ds_read_b128 a[232:235], v241 offset:12544\n"
        "v_cvt_pk_bf16_f32 v138, v116, v117\n"
        "v_mfma_f32_16x16x32_bf16 v[120:123], a[236:239], a[204:207], v[120:123]\n"
        "ds_read_b128 v[202:205], v251 offset:4096\n"
        "v_cvt_pk_bf16_f32 v139, v118, v119\n"
        "v_mfma_f32_16x16x32_bf16 v[120:123], a[240:243], a[208:211], v[120:123]\n"
        "ds_write_b128 v254, v[220:223] offset:0\n"
        "v_mfma_f32_16x16x32_bf16 v[120:123], a[244:247], a[212:215], v[120:123]\n"
        "s_waitcnt lgkmcnt(9)\n"
        "v_exp_f32 v99, v99\n"
        "v_mfma_f32_16x16x32_bf16 v[112:115], a[0:3], a[248:251], 0\n"
        "buffer_load_dwordx4 v233, s[52:55], s62 offen offset:1024 lds\n"
        "v_mfma_f32_16x16x32_bf16 v[112:115], a[4:7], a[216:219], v[112:115]\n"
        "s_waitcnt vmcnt(2)\n"
        "ds_read_b64_tr_b16 v[212:213], v255 offset:6144\n"
        "ds_read_b64_tr_b16 v[214:215], v255 offset:6912\n"
        "v_mfma_f32_16x16x32_bf16 v[112:115], a[8:11], a[220:223], v[112:115]\n"
        "s_waitcnt lgkmcnt(5)\n"
        "v_exp_f32 v101, v101\n"
        "v_mfma_f32_16x16x32_bf16 v[112:115], a[12:15], a[224:227], v[112:115]\n"
        "v_cvt_pk_bf16_f32 v65, v98, v99\n"
        "v_cvt_pk_bf16_f32 v66, v100, v101\n"
        "v_mfma_f32_16x16x32_bf16 v[112:115], a[16:19], a[228:231], v[112:115]\n"
        "s_waitcnt lgkmcnt(3)\n"
        "v_exp_f32 v102, v102\n"
        "v_mfma_f32_16x16x32_bf16 v[112:115], a[20:23], a[232:235], v[112:115]\n"
        "ds_write_b128 v235, v[224:227] offset:3072\n"
        "v_mfma_f32_16x16x32_bf16 v[116:119], a[44:47], a[232:235], 0\n"
        "v_exp_f32 v103, v103\n"
        "v_mfma_f32_16x16x32_bf16 v[116:119], a[40:43], a[228:231], v[116:119]\n"
        "v_cvt_pk_bf16_f32 v67, v102, v103\n"
        "v_cvt_pk_bf16_f32 v146, v120, v121\n"
        "v_mfma_f32_16x16x32_bf16 v[116:119], a[36:39], a[224:227], v[116:119]\n"
        "buffer_load_dwordx4 v234, s[52:55], s62 offen offset:2048 lds\n"
        "s_add_u32 s41, s30, s40\n"
        "s_add_u32 s35, s47, 786432\n"
        "s_sub_u32 s46, s35, 100663296\n"
        "s_cmp_lt_u32 s46, 786432\n"
        "s_mov_b32 s30, s29\n"
        "v_mfma_f32_16x16x32_bf16 v[116:119], a[32:35], a[220:223], v[116:119]\n"
        "s_cselect_b32 s35, s46, s35\n"
        "s_add_u32 s29, s29, s45\n"
        "ds_read_b64_tr_b16 v[216:217], v255 offset:6176\n"
        "v_mfma_f32_16x16x32_bf16 v[116:119], a[28:31], a[216:219], v[116:119]\n"
        "ds_read_b64_tr_b16 v[218:219], v255 offset:6944\n"
        "v_cvt_pk_bf16_f32 v147, v122, v123\n"
        "v_mfma_f32_16x16x32_bf16 v[116:119], a[24:27], a[248:251], v[116:119]\n"
        "v_exp_f32 v104, v104\n"
        "v_mfma_f32_16x16x32_bf16 v[120:123], a[48:51], a[248:251], 0\n"
        "ds_write_b128 v239, v[88:91] offset:3264\n"
        "v_mfma_f32_16x16x32_bf16 v[120:123], a[52:55], a[216:219], v[120:123]\n"
        "v_exp_f32 v105, v105\n"
        "v_mfma_f32_16x16x32_bf16 v[120:123], a[56:59], a[220:223], v[120:123]\n"
        "s_mov_b32 m0, s48\n"
        "v_exp_f32 v106, v106\n"
        "v_mfma_f32_16x16x32_bf16 v[120:123], a[60:63], a[224:227], v[120:123]\n"
        "ds_read_b64_tr_b16 v[220:221], v255 offset:6208\n"
        "ds_read_b64_tr_b16 v[222:223], v255 offset:6976\n"
        "v_mfma_f32_16x16x32_bf16 v[120:123], a[64:67], a[228:231], v[120:123]\n"
        "buffer_load_dwordx4 v249, s[12:15], s36 offen offset:0 lds\n"
        "v_mfma_f32_16x16x32_bf16 v[120:123], a[68:71], a[232:235], v[120:123]\n"
        "v_cvt_pk_bf16_f32 v72, v104, v105\n"
        "v_cvt_pk_bf16_f32 v196, v112, v113\n"
        "v_cvt_pk_bf16_f32 v197, v114, v115\n"
        "v_mfma_f32_16x16x32_bf16 v[112:115], a[92:95], a[232:235], 0\n"
        "v_exp_f32 v108, v108\n"
        "v_mfma_f32_16x16x32_bf16 v[112:115], a[88:91], a[228:231], v[112:115]\n"
        "ds_write_b128 v254, v[92:95] offset:3072\n"
        "v_mfma_f32_16x16x32_bf16 v[112:115], a[84:87], a[224:227], v[112:115]\n"
        "v_exp_f32 v107, v107\n"
        "v_mfma_f32_16x16x32_bf16 v[112:115], a[80:83], a[220:223], v[112:115]\n"
        "v_exp_f32 v109, v109\n"
        "v_mfma_f32_16x16x32_bf16 v[112:115], a[76:79], a[216:219], v[112:115]\n"
        "s_waitcnt vmcnt(1)\n"
        "ds_read_b64_tr_b16 v[224:225], v255 offset:9216\n"
        "ds_read_b64_tr_b16 v[226:227], v255 offset:9984\n"
        "v_mfma_f32_16x16x32_bf16 v[112:115], a[72:75], a[248:251], v[112:115]\n"
        "buffer_load_dwordx4 v253, s[12:15], s36 offen offset:1024 lds\n"
        "v_cvt_pk_bf16_f32 v198, v116, v117\n"
        "v_cvt_pk_bf16_f32 v199, v118, v119\n"
        "s_nop 1\n"
        "v_mfma_f32_16x16x32_bf16 v[96:99], v[152:155], v[196:199], v[32:35]\n"
        "v_cvt_pk_bf16_f32 v73, v106, v107\n"
        "v_cvt_pk_bf16_f32 v74, v108, v109\n"
        "v_cvt_pk_bf16_f32 v200, v120, v121\n"
        "v_cvt_pk_bf16_f32 v201, v122, v123\n"
        "s_nop 1\n"
        "v_mfma_f32_16x16x32_bf16 v[96:99], v[168:171], v[200:203], v[96:99]\n"
        "v_exp_f32 v110, v110\n"
        "v_mfma_f32_16x16x32_bf16 v[100:103], v[172:175], v[196:199], v[36:39]\n"
        "s_waitcnt lgkmcnt(3)\n"
        "ds_write_b128 v235, v[212:215] offset:6144\n"
        "v_cvt_pk_bf16_f32 v206, v120, v121\n"
        "v_cvt_pk_bf16_f32 v207, v122, v123\n"
        "s_nop 1\n"
        "v_mfma_f32_16x16x32_bf16 v[100:103], v[188:191], v[204:207], v[100:103]\n"
        "s_cmp_eq_u32 s37, 100663296\n"
        "v_exp_f32 v111, v111\n"
        "v_mfma_f32_16x16x32_bf16 v[116:119], a[96:99], a[248:251], 0\n"
        "s_cselect_b32 s46, 0, s37\n"
        "ds_read_b128 v[202:205], v252 offset:4096\n"
        "v_cvt_pk_bf16_f32 v75, v110, v111\n"
        "v_mfma_f32_16x16x32_bf16 v[116:119], a[100:103], a[216:219], v[116:119]\n"
        "ds_read_b64_tr_b16 v[88:89], v255 offset:9248\n"
        "ds_read_b64_tr_b16 v[90:91], v255 offset:10016\n"
        "s_add_u32 m0, s41, 0\n"
        "v_mfma_f32_16x16x32_bf16 v[116:119], a[104:107], a[220:223], v[116:119]\n"
        "buffer_load_dwordx4 v242, s[8:11], s47 offen lds\n"
        "v_mfma_f32_16x16x32_bf16 v[116:119], a[108:111], a[224:227], v[116:119]\n"
        "s_add_u32 s38, s46, s43\n"
        "v_cvt_pk_bf16_f32 v192, v112, v113\n"
        "v_cvt_pk_bf16_f32 v193, v114, v115\n"
        "v_mfma_f32_16x16x32_bf16 v[116:119], a[112:115], a[228:231], v[116:119]\n"
        "s_add_u32 s39, s46, s44\n"
        "v_exp_f32 v96, v96\n"
        "v_mfma_f32_16x16x32_bf16 v[116:119], a[116:119], a[232:235], v[116:119]\n"
        "ds_write_b128 v239, v[216:219] offset:6336\n"
        "s_add_u32 s37, s37, 6291456\n"
        "v_mfma_f32_16x16x32_bf16 v[120:123], a[140:143], a[232:235], 0\n"
        "v_exp_f32 v97, v97\n"
        "v_mfma_f32_16x16x32_bf16 v[120:123], a[136:139], a[228:231], v[120:123]\n"
        "s_add_u32 m0, s41, 1024\n"
        "v_exp_f32 v98, v98\n"
        "v_mfma_f32_16x16x32_bf16 v[120:123], a[132:135], a[224:227], v[120:123]\n"
        "ds_read_b64_tr_b16 v[92:93], v255 offset:9280\n"
        "ds_read_b64_tr_b16 v[94:95], v255 offset:10048\n"
        "v_mfma_f32_16x16x32_bf16 v[120:123], a[128:131], a[220:223], v[120:123]\n"
        "buffer_load_dwordx4 v243, s[8:11], s47 offen lds\n"
        "v_mfma_f32_16x16x32_bf16 v[120:123], a[124:127], a[216:219], v[120:123]\n"
        "v_cvt_pk_bf16_f32 v80, v96, v97\n"
        "v_cvt_pk_bf16_f32 v194, v116, v117\n"
        "v_mfma_f32_16x16x32_bf16 v[120:123], a[120:123], a[248:251], v[120:123]\n"
        "v_exp_f32 v100, v100\n"
        "v_mfma_f32_16x16x32_bf16 v[112:115], a[248:251], a[144:147], 0\n"
        "ds_write_b128 v254, v[220:223] offset:6144\n"
        "v_mfma_f32_16x16x32_bf16 v[112:115], a[216:219], a[148:151], v[112:115]\n"
        "v_cvt_pk_bf16_f32 v195, v118, v119\n"
        "v_add_u32 v245, s45, v245\n"
        "v_mfma_f32_16x16x32_bf16 v[112:115], a[220:223], a[152:155], v[112:115]\n"
        "s_add_u32 m0, s41, 2048\n"
        "ds_read_b128 a[236:239], v240 offset:13824\n"
        "v_cvt_pk_bf16_f32 v208, v120, v121\n"
        "v_mfma_f32_16x16x32_bf16 v[112:115], a[224:227], a[156:159], v[112:115]\n"
        "ds_read_b128 a[240:243], v241 offset:13824\n"
        "v_cvt_pk_bf16_f32 v209, v122, v123\n"
        "v_mfma_f32_16x16x32_bf16 v[112:115], a[228:231], a[160:163], v[112:115]\n"
        "buffer_load_dwordx4 v244, s[8:11], s47 offen lds\n"
        "v_mfma_f32_16x16x32_bf16 v[112:115], a[232:235], a[164:167], v[112:115]\n"
        "ds_read_b128 a[244:247], v240 offset:13952\n"
        "v_add_u32 v246, s45, v246\n"
        "v_mfma_f32_16x16x32_bf16 v[104:107], v[192:195], v[156:159], v[8:11]\n"
        "v_exp_f32 v99, v99\n"
        "v_mfma_f32_16x16x32_bf16 v[104:107], v[208:211], v[160:163], v[104:107]\n"
        "s_waitcnt lgkmcnt(4)\n"
        "ds_write_b128 v235, v[224:227] offset:9216\n"
        "v_mfma_f32_16x16x32_bf16 v[116:119], a[232:235], a[188:191], 0\n"
        "v_exp_f32 v101, v101\n"
        "v_mfma_f32_16x16x32_bf16 v[116:119], a[228:231], a[184:187], v[116:119]\n"
        "s_add_u32 m0, s41, 12288\n"
        "v_cvt_pk_bf16_f32 v81, v98, v99\n"
        "v_cvt_pk_bf16_f32 v82, v100, v101\n"
        "v_mfma_f32_16x16x32_bf16 v[116:119], a[224:227], a[180:183], v[116:119]\n"
        "v_exp_f32 v102, v102\n"
        "v_mfma_f32_16x16x32_bf16 v[116:119], a[220:223], a[176:179], v[116:119]\n"
        "buffer_load_dwordx4 v242, s[8:11], s35 offen lds\n"
        "v_mfma_f32_16x16x32_bf16 v[116:119], a[216:219], a[172:175], v[116:119]\n"
        "v_exp_f32 v103, v103\n"
        "v_mfma_f32_16x16x32_bf16 v[116:119], a[248:251], a[168:171], v[116:119]\n"
        "v_cvt_pk_bf16_f32 v83, v102, v103\n"
        "v_cvt_pk_bf16_f32 v132, v112, v113\n"
        "v_mfma_f32_16x16x32_bf16 v[108:111], v[192:195], v[176:179], v[24:27]\n"
        "ds_write_b128 v239, v[88:91] offset:9408\n"
        "v_mfma_f32_16x16x32_bf16 v[108:111], v[208:211], v[180:183], v[108:111]\n"
        "v_cvt_pk_bf16_f32 v133, v114, v115\n"
        "s_nop 0\n"
        "v_mfma_f32_16x16x32_bf16 v[120:123], a[248:251], a[192:195], 0\n"
        "ds_read_b128 a[248:251], v241 offset:13952\n"
        "s_add_u32 m0, s41, 13312\n"
        "v_cvt_pk_bf16_f32 v140, v116, v117\n"
        "v_mfma_f32_16x16x32_bf16 v[120:123], a[216:219], a[196:199], v[120:123]\n"
        "ds_read_b128 a[216:219], v240 offset:14080\n"
        "v_cvt_pk_bf16_f32 v141, v118, v119\n"
        "v_mfma_f32_16x16x32_bf16 v[120:123], a[220:223], a[200:203], v[120:123]\n"
        "buffer_load_dwordx4 v243, s[8:11], s35 offen lds\n"
        "v_mfma_f32_16x16x32_bf16 v[120:123], a[224:227], a[204:207], v[120:123]\n"
        "ds_read_b128 a[220:223], v241 offset:14080\n"
        "v_mfma_f32_16x16x32_bf16 v[120:123], a[228:231], a[208:211], v[120:123]\n"
        "v_exp_f32 v104, v104\n"
        "v_mfma_f32_16x16x32_bf16 v[120:123], a[232:235], a[212:215], v[120:123]\n"
        "ds_write_b128 v254, v[92:95] offset:9216\n"
        "v_mfma_f32_16x16x32_bf16 v[96:99], v[192:195], v[196:199], v[40:43]\n"
        "v_exp_f32 v105, v105\n"
        "v_mfma_f32_16x16x32_bf16 v[96:99], v[208:211], v[200:203], v[96:99]\n"
        "s_waitcnt lgkmcnt(5)\n"
        "s_add_u32 m0, s41, 14336\n"
        "v_exp_f32 v106, v106\n"
        "v_mfma_f32_16x16x32_bf16 v[112:115], a[72:75], a[236:239], 0\n"
        "v_cvt_pk_bf16_f32 v68, v104, v105\n"
        "v_cvt_pk_bf16_f32 v148, v120, v121\n"
        "v_mfma_f32_16x16x32_bf16 v[112:115], a[76:79], a[240:243], v[112:115]\n"
        "buffer_load_dwordx4 v244, s[8:11], s35 offen lds\n"
        "v_mfma_f32_16x16x32_bf16 v[112:115], a[80:83], a[244:247], v[112:115]\n"
        "s_waitcnt lgkmcnt(1)\n"
        "v_exp_f32 v107, v107\n"
        "v_mfma_f32_16x16x32_bf16 v[112:115], a[84:87], a[248:251], v[112:115]\n"
        "v_cvt_pk_bf16_f32 v69, v106, v107\n"
        "v_cvt_pk_bf16_f32 v149, v122, v123\n"
        "v_mfma_f32_16x16x32_bf16 v[112:115], a[88:91], a[216:219], v[112:115]\n"
        "v_exp_f32 v108, v108\n"
        "v_mfma_f32_16x16x32_bf16 v[112:115], a[92:95], a[220:223], v[112:115]\n"
        "v_exp_f32 v109, v109\n"
        "v_mfma_f32_16x16x32_bf16 v[116:119], a[116:119], a[220:223], 0\n"
        "v_exp_f32 v110, v110\n"
        "v_mfma_f32_16x16x32_bf16 v[116:119], a[112:115], a[216:219], v[116:119]\n"
        "v_cvt_pk_bf16_f32 v76, v108, v109\n"
        "v_add_u32 v240, s31, v240\n"
        "v_mfma_f32_16x16x32_bf16 v[116:119], a[108:111], a[248:251], v[116:119]\n"
        "v_exp_f32 v111, v111\n"
        "v_mfma_f32_16x16x32_bf16 v[116:119], a[104:107], a[244:247], v[116:119]\n"
        "v_cvt_pk_bf16_f32 v212, v112, v113\n"
        "v_cvt_pk_bf16_f32 v213, v114, v115\n"
        "v_mfma_f32_16x16x32_bf16 v[116:119], a[100:103], a[240:243], v[116:119]\n"
        "v_cvt_pk_bf16_f32 v77, v110, v111\n"
        "v_add_u32 v241, s31, v241\n"
        "v_mfma_f32_16x16x32_bf16 v[116:119], a[96:99], a[236:239], v[116:119]\n"
        "v_exp_f32 v96, v96\n"
        "v_mfma_f32_16x16x32_bf16 v[120:123], a[120:123], a[236:239], 0\n"
        "v_exp_f32 v97, v97\n"
        "v_mfma_f32_16x16x32_bf16 v[120:123], a[124:127], a[240:243], v[120:123]\n"
        "s_add_u32 s47, s47, 6291456\n"
        "v_exp_f32 v98, v98\n"
        "v_mfma_f32_16x16x32_bf16 v[120:123], a[128:131], a[244:247], v[120:123]\n"
        "s_add_u32 s36, s36, 2097152\n"
        "v_cvt_pk_bf16_f32 v214, v116, v117\n"
        "v_cvt_pk_bf16_f32 v215, v118, v119\n"
        "v_mfma_f32_16x16x32_bf16 v[120:123], a[132:135], a[248:251], v[120:123]\n"
        "s_xor_b32 s48, s48, 8192\n"
        "v_cvt_pk_bf16_f32 v84, v96, v97\n"
        "v_and_b32 v254, 32, v250\n"
        "v_mfma_f32_16x16x32_bf16 v[120:123], a[136:139], a[216:219], v[120:123]\n"
        "v_exp_f32 v99, v99\n"
        "v_mfma_f32_16x16x32_bf16 v[120:123], a[140:143], a[220:223], v[120:123]\n"
        "v_cvt_pk_bf16_f32 v85, v98, v99\n"
        "v_mad_i32_i24 v254, v254, -12, v248\n"
        "v_mfma_f32_16x16x32_bf16 v[112:115], a[220:223], a[164:167], 0\n"
        "v_add_u32 v254, 1728, v254\n"
        "s_nop 0\n"
        "v_mfma_f32_16x16x32_bf16 v[112:115], a[216:219], a[160:163], v[112:115]\n"
        "s_nop 1\n"
        "v_cvt_pk_bf16_f32 v230, v120, v121\n"
        "v_mfma_f32_16x16x32_bf16 v[112:115], a[248:251], a[156:159], v[112:115]\n"
        "s_waitcnt lgkmcnt(0)\n"
        "s_barrier\n"
        "v_cvt_pk_bf16_f32 v231, v122, v123\n"
        "v_mfma_f32_16x16x32_bf16 v[112:115], a[244:247], a[152:155], v[112:115]\n"
        "v_mfma_f32_16x16x32_bf16 v[112:115], a[240:243], a[148:151], v[112:115]\n"
        "v_mfma_f32_16x16x32_bf16 v[112:115], a[236:239], a[144:147], v[112:115]\n"
        "s_nop 2\n"
        "v_mfma_f32_16x16x32_bf16 v[100:103], v[212:215], v[156:159], v[12:15]\n"
        "s_nop 2\n"
        "v_mfma_f32_16x16x32_bf16 v[100:103], v[228:231], v[164:167], v[100:103]\n"
        "v_cvt_pk_bf16_f32 v134, v112, v113\n"
        "v_cvt_pk_bf16_f32 v135, v114, v115\n"
        "v_mfma_f32_16x16x32_bf16 v[116:119], a[236:239], a[168:171], 0\n"
        "v_mfma_f32_16x16x32_bf16 v[116:119], a[240:243], a[172:175], v[116:119]\n"
        "v_mfma_f32_16x16x32_bf16 v[116:119], a[244:247], a[176:179], v[116:119]\n"
        "v_mfma_f32_16x16x32_bf16 v[116:119], a[248:251], a[180:183], v[116:119]\n"
        "v_mfma_f32_16x16x32_bf16 v[116:119], a[216:219], a[184:187], v[116:119]\n"
        "s_nop 0\n"
        "v_exp_f32 v100, v100\n"
        "v_mfma_f32_16x16x32_bf16 v[116:119], a[220:223], a[188:191], v[116:119]\n"
        "v_exp_f32 v101, v101\n"
        "v_mfma_f32_16x16x32_bf16 v[104:107], v[212:215], v[176:179], v[28:31]\n"
        "v_exp_f32 v102, v102\n"
        "v_mfma_f32_16x16x32_bf16 v[104:107], v[228:231], v[184:187], v[104:107]\n"
        "v_exp_f32 v103, v103\n"
        "v_mfma_f32_16x16x32_bf16 v[120:123], a[220:223], a[212:215], 0\n"
        "v_cvt_pk_bf16_f32 v70, v100, v101\n"
        "v_cvt_pk_bf16_f32 v71, v102, v103\n"
        "v_mfma_f32_16x16x32_bf16 v[120:123], a[216:219], a[208:211], v[120:123]\n"
        "v_cvt_pk_bf16_f32 v142, v116, v117\n"
        "v_cvt_pk_bf16_f32 v143, v118, v119\n"
        "v_mfma_f32_16x16x32_bf16 v[120:123], a[248:251], a[204:207], v[120:123]\n"
        "v_mov_b64 v[96:97], 0\n"
        "v_mfma_f32_16x16x32_bf16 v[120:123], a[244:247], a[200:203], v[120:123]\n"
        "v_exp_f32 v104, v104\n"
        "v_mfma_f32_16x16x32_bf16 v[120:123], a[240:243], a[196:199], v[120:123]\n"
        "v_exp_f32 v105, v105\n"
        "v_mfma_f32_16x16x32_bf16 v[120:123], a[236:239], a[192:195], v[120:123]\n"
        "v_exp_f32 v106, v106\n"
        "v_mfma_f32_16x16x32_bf16 v[108:111], v[212:215], v[196:199], v[44:47]\n"
        "v_exp_f32 v107, v107\n"
        "v_mfma_f32_16x16x32_bf16 v[108:111], v[228:231], v[204:207], v[108:111]\n"
        "v_cvt_pk_bf16_f32 v78, v104, v105\n"
        "v_cvt_pk_bf16_f32 v79, v106, v107\n"
        "v_mfma_f32_16x16x32_bf16 v[112:115], a[0:3], a[236:239], 0\n"
        "s_nop 0\n"
        "v_cvt_pk_bf16_f32 v150, v120, v121\n"
        "v_cvt_pk_bf16_f32 v151, v122, v123\n"
        "v_mfma_f32_16x16x32_bf16 v[112:115], a[4:7], a[240:243], v[112:115]\n"
        "v_mfma_f32_16x16x32_bf16 v[112:115], a[8:11], a[244:247], v[112:115]\n"
        "v_exp_f32 v108, v108\n"
        "v_mfma_f32_16x16x32_bf16 v[112:115], a[12:15], a[248:251], v[112:115]\n"
        "v_exp_f32 v109, v109\n"
        "v_mfma_f32_16x16x32_bf16 v[112:115], a[16:19], a[216:219], v[112:115]\n"
        "v_exp_f32 v110, v110\n"
        "v_mfma_f32_16x16x32_bf16 v[112:115], a[20:23], a[220:223], v[112:115]\n"
        "v_exp_f32 v111, v111\n"
        "v_smfmac_f32_16x16x64_bf16 v[96:99], a[252:255], v[64:71], v237\n"
        "v_cvt_pk_bf16_f32 v86, v108, v109\n"
        "v_mfma_f32_16x16x32_bf16 v[100:103], v[132:135], v[68:71], 0\n"
        "v_mov_b64 v[124:125], 0\n"
        "v_cvt_pk_bf16_f32 v87, v110, v111\n"
        "v_mfma_f32_16x16x32_bf16 v[100:103], v[128:131], v[64:67], v[100:103]\n"
        "s_nop 0\n"
        "v_cvt_pk_bf16_f32 v216, v112, v113\n"
        "v_cvt_pk_bf16_f32 v217, v114, v115\n"
        "v_mfma_f32_16x16x32_bf16 v[104:107], v[136:139], v[64:67], 0\n"
        "ds_read_b128 v[222:225], v251 offset:4608\n"
        "v_add_f32 v97, v96, v97\n"
        "v_mfma_f32_16x16x32_bf16 v[104:107], v[140:143], v[68:71], v[104:107]\n"
        "v_rcp_f32 v96, v97\n"
        "v_mfma_f32_16x16x32_bf16 v[108:111], v[148:151], v[68:71], 0\n"
        "v_mul_f32 v100, v96, v100\n"
        "v_mul_f32 v101, v96, v101\n"
        "v_mfma_f32_16x16x32_bf16 v[108:111], v[144:147], v[64:67], v[108:111]\n"
        "v_mul_f32 v102, v96, v102\n"
        "v_mul_f32 v103, v96, v103\n"
        "v_mfma_f32_16x16x32_bf16 v[116:119], a[44:47], a[220:223], 0\n"
        "v_cvt_pk_bf16_f32 v100, v100, v101\n"
        "v_cvt_pk_bf16_f32 v101, v102, v103\n"
        "v_mfma_f32_16x16x32_bf16 v[116:119], a[40:43], a[216:219], v[116:119]\n"
        "v_mul_f32 v104, v96, v104\n"
        "v_mul_f32 v105, v96, v105\n"
        "v_mfma_f32_16x16x32_bf16 v[116:119], a[36:39], a[248:251], v[116:119]\n"
        "v_mul_f32 v106, v96, v106\n"
        "v_mul_f32 v107, v96, v107\n"
        "v_mfma_f32_16x16x32_bf16 v[116:119], a[32:35], a[244:247], v[116:119]\n"
        "v_cvt_pk_bf16_f32 v102, v104, v105\n"
        "v_cvt_pk_bf16_f32 v103, v106, v107\n"
        "v_mfma_f32_16x16x32_bf16 v[116:119], a[28:31], a[240:243], v[116:119]\n"
        "ds_write_b128 v245, v[100:103] offset:0\n"
        "v_mfma_f32_16x16x32_bf16 v[116:119], a[24:27], a[236:239], v[116:119]\n"
        "v_mul_f32 v108, v96, v108\n"
        "v_mul_f32 v109, v96, v109\n"
        "v_smfmac_f32_16x16x64_bf16 v[124:127], a[252:255], v[72:79], v237\n"
        "v_mul_f32 v110, v96, v110\n"
        "v_mul_f32 v111, v96, v111\n"
        "v_mfma_f32_16x16x32_bf16 v[64:67], v[132:135], v[76:79], 0\n"
        "v_cvt_pk_bf16_f32 v108, v108, v109\n"
        "v_cvt_pk_bf16_f32 v109, v110, v111\n"
        "v_mfma_f32_16x16x32_bf16 v[64:67], v[128:131], v[72:75], v[64:67]\n"
        "ds_read_b128 v[156:159], v247 offset:0\n"
        "v_cvt_pk_bf16_f32 v218, v116, v117\n"
        "v_mfma_f32_16x16x32_bf16 v[68:71], v[136:139], v[72:75], 0\n"
        "ds_write_b64 v246, v[108:109] offset:0\n"
        "v_mfma_f32_16x16x32_bf16 v[68:71], v[140:143], v[76:79], v[68:71]\n"
        "ds_read_b128 v[160:163], v247 offset:4096\n"
        "v_cvt_pk_bf16_f32 v219, v118, v119\n"
        "v_mfma_f32_16x16x32_bf16 v[120:123], v[148:151], v[76:79], 0\n"
        "ds_read_b128 v[164:167], v247 offset:4352\n"
        "v_max_u32 v236, v96, v97\n"
        "v_mfma_f32_16x16x32_bf16 v[120:123], v[144:147], v[72:75], v[120:123]\n"
        "v_mov_b64 v[116:117], 0\n"
        "v_add_f32 v125, v124, v125\n"
        "v_mfma_f32_16x16x32_bf16 v[112:115], a[48:51], a[236:239], 0\n"
        "v_rcp_f32 v124, v125\n"
        "v_mfma_f32_16x16x32_bf16 v[112:115], a[52:55], a[240:243], v[112:115]\n"
        "v_mul_f32 v64, v124, v64\n"
        "v_mul_f32 v65, v124, v65\n"
        "v_mfma_f32_16x16x32_bf16 v[112:115], a[56:59], a[244:247], v[112:115]\n"
        "v_mul_f32 v66, v124, v66\n"
        "v_mul_f32 v67, v124, v67\n"
        "v_mfma_f32_16x16x32_bf16 v[112:115], a[60:63], a[248:251], v[112:115]\n"
        "v_cvt_pk_bf16_f32 v64, v64, v65\n"
        "v_cvt_pk_bf16_f32 v65, v66, v67\n"
        "v_mfma_f32_16x16x32_bf16 v[112:115], a[64:67], a[216:219], v[112:115]\n"
        "v_mul_f32 v68, v124, v68\n"
        "v_mul_f32 v69, v124, v69\n"
        "v_mfma_f32_16x16x32_bf16 v[112:115], a[68:71], a[220:223], v[112:115]\n"
        "ds_read_b128 a[216:219], v248 offset:0\n"
        "v_mul_f32 v70, v124, v70\n"
        "v_smfmac_f32_16x16x64_bf16 v[116:119], a[252:255], v[80:87], v237\n"
        "ds_read_b128 a[220:223], v248 offset:3072\n"
        "v_mul_f32 v71, v124, v71\n"
        "v_mfma_f32_16x16x32_bf16 v[72:75], v[132:135], v[84:87], 0\n"
        "v_cvt_pk_bf16_f32 v66, v68, v69\n"
        "v_cvt_pk_bf16_f32 v67, v70, v71\n"
        "v_mfma_f32_16x16x32_bf16 v[72:75], v[128:131], v[80:83], v[72:75]\n"
        "ds_write_b128 v245, v[64:67] offset:256\n"
        "v_mfma_f32_16x16x32_bf16 v[76:79], v[136:139], v[80:83], 0\n"
        "v_cvt_pk_bf16_f32 v220, v112, v113\n"
        "v_cvt_pk_bf16_f32 v221, v114, v115\n"
        "v_mfma_f32_16x16x32_bf16 v[76:79], v[140:143], v[84:87], v[76:79]\n"
        "ds_read_b128 a[224:227], v248 offset:32\n"
        "v_cvt_pk_bf16_f32 v226, v112, v113\n"
        "v_mfma_f32_16x16x32_bf16 v[96:99], v[148:151], v[84:87], 0\n"
        "ds_read_b128 a[228:231], v248 offset:3104\n"
        "v_cvt_pk_bf16_f32 v227, v114, v115\n"
        "v_mfma_f32_16x16x32_bf16 v[96:99], v[144:147], v[80:83], v[96:99]\n"
        "v_mul_f32 v120, v124, v120\n"
        "v_mul_f32 v121, v124, v121\n"
        "v_mfma_f32_16x16x32_bf16 v[100:103], v[152:155], v[216:219], v[48:51]\n"
        "s_waitcnt lgkmcnt(3)\n"
        "ds_read_b128 v[152:155], v247 offset:256\n"
        "v_mul_f32 v122, v124, v122\n"
        "v_mfma_f32_16x16x32_bf16 v[100:103], v[168:171], v[220:223], v[100:103]\n"
        "v_mul_f32 v123, v124, v123\n"
        "v_cvt_pk_bf16_f32 v120, v120, v121\n"
        "v_mfma_f32_16x16x32_bf16 v[104:107], v[172:175], v[216:219], v[52:55]\n"
        "v_cvt_pk_bf16_f32 v121, v122, v123\n"
        "v_max3_u32 v236, v236, v124, v125\n"
        "v_mfma_f32_16x16x32_bf16 v[104:107], v[188:191], v[224:227], v[104:107]\n"
        "ds_write_b64 v246, v[120:121] offset:256\n"
        "v_mfma_f32_16x16x32_bf16 v[176:179], v[156:159], a[216:219], 0\n"
        "ds_read_b128 v[222:225], v252 offset:4608\n"
        "v_add_f32 v117, v116, v117\n"
        "v_mfma_f32_16x16x32_bf16 v[176:179], v[160:163], a[220:223], v[176:179]\n"
        "ds_read_b128 a[232:235], v254 offset:0\n"
        "v_mfma_f32_16x16x32_bf16 v[196:199], v[164:167], a[220:223], 0\n"
        "s_waitcnt lgkmcnt(3)\n"
        "ds_read_b128 a[220:223], v254 offset:3072\n"
        "v_mfma_f32_16x16x32_bf16 v[196:199], v[152:155], a[216:219], v[196:199]\n"
        "v_rcp_f32 v116, v117\n"
        "v_mfma_f32_16x16x32_bf16 v[200:203], v[152:155], a[224:227], 0\n"
        "ds_read_b128 a[216:219], v254 offset:32\n"
        "v_mul_f32 v72, v116, v72\n"
        "v_mfma_f32_16x16x32_bf16 v[200:203], v[164:167], a[228:231], v[200:203]\n"
        "v_exp_f32 v100, v100\n"
        "v_mfma_f32_16x16x32_bf16 v[180:183], v[160:163], a[228:231], 0\n"
        "ds_read_b128 a[228:231], v254 offset:3104\n"
        "v_mul_f32 v73, v116, v73\n"
        "v_mfma_f32_16x16x32_bf16 v[180:183], v[156:159], a[224:227], v[180:183]\n"
        "ds_read_b128 a[236:239], v247 offset:8192\n"
        "v_mul_f32 v74, v116, v74\n"
        "v_mfma_f32_16x16x32_bf16 v[108:111], v[192:195], v[216:219], v[56:59]\n"
        "s_waitcnt lgkmcnt(4)\n"
        "ds_read_b128 a[224:227], v248 offset:16\n"
        "v_mul_f32 v75, v116, v75\n"
        "v_mfma_f32_16x16x32_bf16 v[108:111], v[208:211], v[220:223], v[108:111]\n"
        "ds_read_b128 a[240:243], v247 offset:12288\n"
        "v_cvt_pk_bf16_f32 v72, v72, v73\n"
        "v_mfma_f32_16x16x32_bf16 v[112:115], v[212:215], v[216:219], v[60:63]\n"
        "ds_read_b128 a[244:247], v247 offset:12544\n"
        "v_cvt_pk_bf16_f32 v73, v74, v75\n"
        "v_mfma_f32_16x16x32_bf16 v[112:115], v[228:231], v[224:227], v[112:115]\n"
        "ds_read_b128 a[248:251], v247 offset:8448\n"
        "v_mul_f32 v76, v116, v76\n"
        "v_mfma_f32_16x16x32_bf16 v[184:187], v[156:159], a[232:235], 0\n"
        "s_waitcnt lgkmcnt(6)\n"
        "v_exp_f32 v101, v101\n"
        "v_mfma_f32_16x16x32_bf16 v[184:187], v[160:163], a[220:223], v[184:187]\n"
        "v_mul_f32 v77, v116, v77\n"
        "v_mul_f32 v78, v116, v78\n"
        "v_mfma_f32_16x16x32_bf16 v[204:207], v[164:167], a[220:223], 0\n"
        "ds_read_b128 a[220:223], v248 offset:3088\n"
        "v_mul_f32 v79, v116, v79\n"
        "v_mfma_f32_16x16x32_bf16 v[204:207], v[152:155], a[232:235], v[204:207]\n"
        "ds_read_b128 a[232:235], v254 offset:16\n"
        "v_cvt_pk_bf16_f32 v88, v100, v101\n"
        "v_mfma_f32_16x16x32_bf16 v[192:195], v[152:155], a[216:219], 0\n"
        "s_waitcnt lgkmcnt(4)\n"
        "v_exp_f32 v102, v102\n"
        "v_mfma_f32_16x16x32_bf16 v[192:195], v[164:167], a[228:231], v[192:195]\n"
        "v_exp_f32 v104, v104\n"
        "v_mfma_f32_16x16x32_bf16 v[172:175], v[160:163], a[228:231], 0\n"
        "ds_read_b128 a[228:231], v254 offset:3088\n"
        "v_cvt_pk_bf16_f32 v74, v76, v77\n"
        "v_mfma_f32_16x16x32_bf16 v[172:175], v[156:159], a[216:219], v[172:175]\n"
        "ds_read_b128 a[216:219], v248 offset:6144\n"
        "v_cvt_pk_bf16_f32 v75, v78, v79\n"
        "v_mfma_f32_16x16x32_bf16 v[212:215], v[156:159], a[224:227], 0\n"
        "s_waitcnt lgkmcnt(3)\n"
        "ds_write_b128 v245, v[72:75] offset:512\n"
        "v_mfma_f32_16x16x32_bf16 v[212:215], v[160:163], a[220:223], v[212:215]\n"
        "v_exp_f32 v103, v103\n"
        "v_mfma_f32_16x16x32_bf16 v[216:219], v[164:167], a[220:223], 0\n"
        "ds_read_b128 a[220:223], v248 offset:9216\n"
        "v_cvt_pk_bf16_f32 v89, v102, v103\n"
        "v_mfma_f32_16x16x32_bf16 v[216:219], v[152:155], a[224:227], v[216:219]\n"
        "s_waitcnt lgkmcnt(4)\n"
        "ds_read_b128 a[224:227], v248 offset:6176\n"
        "v_mul_f32 v96, v116, v96\n"
        "v_mfma_f32_16x16x32_bf16 v[224:227], v[152:155], a[232:235], 0\n"
        "s_waitcnt lgkmcnt(4)\n"
        "v_exp_f32 v105, v105\n"
        "v_mfma_f32_16x16x32_bf16 v[224:227], v[164:167], a[228:231], v[224:227]\n"
        "v_exp_f32 v106, v106\n"
        "v_mfma_f32_16x16x32_bf16 v[220:223], v[160:163], a[228:231], 0\n"
        "ds_read_b128 a[228:231], v248 offset:9248\n"
        "v_cvt_pk_bf16_f32 v90, v104, v105\n"
        "v_mfma_f32_16x16x32_bf16 v[220:223], v[156:159], a[232:235], v[220:223]\n"
        "s_waitcnt lgkmcnt(4)\n"
        "ds_read_b128 a[232:235], v254 offset:6144\n"
        "v_mul_f32 v97, v116, v97\n"
        "v_mfma_f32_16x16x32_bf16 v[176:179], a[236:239], a[216:219], v[176:179]\n"
        "s_waitcnt lgkmcnt(3)\n"
        "v_mul_f32 v98, v116, v98\n"
        "v_mul_f32 v99, v116, v99\n"
        "v_mfma_f32_16x16x32_bf16 v[176:179], a[240:243], a[220:223], v[176:179]\n"
        "v_exp_f32 v107, v107\n"
        "v_mfma_f32_16x16x32_bf16 v[196:199], a[244:247], a[220:223], v[196:199]\n"
        "ds_read_b128 a[220:223], v254 offset:9216\n"
        "v_cvt_pk_bf16_f32 v91, v106, v107\n"
        "v_mfma_f32_16x16x32_bf16 v[196:199], a[248:251], a[216:219], v[196:199]\n"
        "s_waitcnt lgkmcnt(3)\n"
        "ds_read_b128 a[216:219], v254 offset:6176\n"
        "v_cvt_pk_bf16_f32 v96, v96, v97\n"
        "v_mfma_f32_16x16x32_bf16 v[200:203], a[248:251], a[224:227], v[200:203]\n"
        "s_waitcnt lgkmcnt(3)\n"
        "ds_read_b128 v[156:159], v247 offset:16384\n"
        "v_cvt_pk_bf16_f32 v97, v98, v99\n"
        "v_mfma_f32_16x16x32_bf16 v[200:203], a[244:247], a[228:231], v[200:203]\n"
        "ds_write_b64 v246, v[96:97] offset:512\n"
        "v_mfma_f32_16x16x32_bf16 v[180:183], a[240:243], a[228:231], v[180:183]\n"
        ".long 0xbec200ff\n"
        "44:\n"
        "s_branch 60b\n"
        "ds_read_b128 a[228:231], v254 offset:9248\n"
        "v_max3_u32 v236, v236, v116, v117\n"
        "v_mfma_f32_16x16x32_bf16 v[180:183], a[236:239], a[224:227], v[180:183]\n"
        "s_waitcnt lgkmcnt(5)\n"
        "ds_read_b128 a[224:227], v248 offset:6160\n"
        "v_bfe_u32 v238, v250, 3, 4\n"
        "v_mfma_f32_16x16x32_bf16 v[184:187], a[236:239], a[232:235], v[184:187]\n"
        "s_waitcnt lgkmcnt(5)\n"
        "ds_read_b128 v[160:163], v247 offset:20480\n"
        "v_mad_i32_i24 v238, v238, -4, v250\n"
        "v_mfma_f32_16x16x32_bf16 v[184:187], a[240:243], a[220:223], v[184:187]\n"
        "ds_read_b128 v[164:167], v247 offset:20736\n"
        "v_add_u32 v238, s49, v238\n"
        "v_mfma_f32_16x16x32_bf16 v[204:207], a[244:247], a[220:223], v[204:207]\n"
        "ds_read_b128 a[220:223], v248 offset:9232\n"
        "v_mfma_f32_16x16x32_bf16 v[204:207], a[248:251], a[232:235], v[204:207]\n"
        "s_waitcnt lgkmcnt(6)\n"
        "ds_read_b128 a[232:235], v254 offset:6160\n"
        "v_mfma_f32_16x16x32_bf16 v[192:195], a[248:251], a[216:219], v[192:195]\n"
        "s_waitcnt lgkmcnt(5)\n"
        "ds_read_b128 v[152:155], v247 offset:16640\n"
        "v_mfma_f32_16x16x32_bf16 v[192:195], a[244:247], a[228:231], v[192:195]\n"
        "v_exp_f32 v108, v108\n"
        "v_mfma_f32_16x16x32_bf16 v[172:175], a[240:243], a[228:231], v[172:175]\n"
        "ds_read_b128 a[228:231], v254 offset:9232\n"
        "v_mfma_f32_16x16x32_bf16 v[172:175], a[236:239], a[216:219], v[172:175]\n"
        "s_waitcnt lgkmcnt(5)\n"
        "ds_read_b128 a[216:219], v248 offset:12288\n"
        "v_mfma_f32_16x16x32_bf16 v[212:215], a[236:239], a[224:227], v[212:215]\n"
        "s_waitcnt lgkmcnt(4)\n"
        "v_exp_f32 v109, v109\n"
        "v_mfma_f32_16x16x32_bf16 v[212:215], a[240:243], a[220:223], v[212:215]\n"
        "v_exp_f32 v110, v110\n"
        "v_mfma_f32_16x16x32_bf16 v[216:219], a[244:247], a[220:223], v[216:219]\n"
        "ds_read_b128 a[220:223], v248 offset:15360\n"
        "v_cvt_pk_bf16_f32 v92, v108, v109\n"
        "v_mfma_f32_16x16x32_bf16 v[216:219], a[248:251], a[224:227], v[216:219]\n"
        "s_waitcnt lgkmcnt(3)\n"
        "ds_read_b128 a[224:227], v248 offset:12320\n"
        "v_mfma_f32_16x16x32_bf16 v[224:227], a[248:251], a[232:235], v[224:227]\n"
        "s_waitcnt lgkmcnt(3)\n"
        "v_exp_f32 v112, v112\n"
        "v_mfma_f32_16x16x32_bf16 v[224:227], a[244:247], a[228:231], v[224:227]\n"
        "v_exp_f32 v111, v111\n"
        "v_mfma_f32_16x16x32_bf16 v[220:223], a[240:243], a[228:231], v[220:223]\n"
        "ds_read_b128 a[228:231], v248 offset:15392\n"
        "v_cvt_pk_bf16_f32 v93, v110, v111\n"
        "v_mfma_f32_16x16x32_bf16 v[220:223], a[236:239], a[232:235], v[220:223]\n"
        "s_waitcnt lgkmcnt(3)\n"
        "ds_read_b128 a[232:235], v254 offset:12288\n"
        "v_mfma_f32_16x16x32_bf16 v[176:179], v[156:159], a[216:219], v[176:179]\n"
        "s_waitcnt lgkmcnt(3)\n"
        "v_exp_f32 v113, v113\n"
        "v_mfma_f32_16x16x32_bf16 v[176:179], v[160:163], a[220:223], v[176:179]\n"
        "v_exp_f32 v114, v114\n"
        "v_mfma_f32_16x16x32_bf16 v[196:199], v[164:167], a[220:223], v[196:199]\n"
        "ds_read_b128 a[220:223], v254 offset:15360\n"
        "v_cvt_pk_bf16_f32 v94, v112, v113\n"
        "v_mfma_f32_16x16x32_bf16 v[196:199], v[152:155], a[216:219], v[196:199]\n"
        "s_waitcnt lgkmcnt(3)\n"
        "ds_read_b128 a[216:219], v254 offset:12320\n"
        "v_mfma_f32_16x16x32_bf16 v[200:203], v[152:155], a[224:227], v[200:203]\n"
        "s_waitcnt lgkmcnt(3)\n"
        "v_exp_f32 v115, v115\n"
        "v_mfma_f32_16x16x32_bf16 v[200:203], v[164:167], a[228:231], v[200:203]\n"
        "v_cvt_pk_bf16_f32 v95, v114, v115\n"
        "v_mfma_f32_16x16x32_bf16 v[180:183], v[160:163], a[228:231], v[180:183]\n"
        "s_waitcnt vmcnt(0)\n"
        "s_barrier\n"
        "ds_read_b128 a[228:231], v254 offset:15392\n"
        "v_mfma_f32_16x16x32_bf16 v[180:183], v[156:159], a[224:227], v[180:183]\n"
        "s_waitcnt lgkmcnt(3)\n"
        "ds_read_b128 a[224:227], v248 offset:12304\n"
        "v_mfma_f32_16x16x32_bf16 v[184:187], v[156:159], a[232:235], v[184:187]\n"
        "s_waitcnt lgkmcnt(3)\n"
        "v_mfma_f32_16x16x32_bf16 v[184:187], v[160:163], a[220:223], v[184:187]\n"
        "v_mfma_f32_16x16x32_bf16 v[204:207], v[164:167], a[220:223], v[204:207]\n"
        "ds_read_b128 a[220:223], v248 offset:15376\n"
        "v_mfma_f32_16x16x32_bf16 v[204:207], v[152:155], a[232:235], v[204:207]\n"
        "s_waitcnt lgkmcnt(3)\n"
        "ds_read_b128 a[232:235], v254 offset:12304\n"
        "v_mfma_f32_16x16x32_bf16 v[192:195], v[152:155], a[216:219], v[192:195]\n"
        "s_waitcnt lgkmcnt(3)\n"
        "ds_read_b128 a[236:239], v240 offset:0\n"
        "v_mfma_f32_16x16x32_bf16 v[192:195], v[164:167], a[228:231], v[192:195]\n"
        "ds_read_b128 a[240:243], v241 offset:0\n"
        "v_mfma_f32_16x16x32_bf16 v[172:175], v[160:163], a[228:231], v[172:175]\n"
        "ds_read_b128 a[228:231], v254 offset:15376\n"
        "v_mfma_f32_16x16x32_bf16 v[172:175], v[156:159], a[216:219], v[172:175]\n"
        "s_waitcnt lgkmcnt(5)\n"
        "ds_read_b128 a[244:247], v240 offset:128\n"
        "v_mfma_f32_16x16x32_bf16 v[212:215], v[156:159], a[224:227], v[212:215]\n"
        "s_waitcnt lgkmcnt(5)\n"
        "ds_read_b128 a[248:251], v241 offset:128\n"
        "v_mfma_f32_16x16x32_bf16 v[212:215], v[160:163], a[220:223], v[212:215]\n"
        "ds_read_b128 a[216:219], v240 offset:256\n"
        "v_mfma_f32_16x16x32_bf16 v[216:219], v[164:167], a[220:223], v[216:219]\n"
        "ds_read_b128 a[220:223], v241 offset:256\n"
        "v_mfma_f32_16x16x32_bf16 v[216:219], v[152:155], a[224:227], v[216:219]\n"
        "s_waitcnt lgkmcnt(6)\n"
        "v_mfma_f32_16x16x32_bf16 v[224:227], v[152:155], a[232:235], v[224:227]\n"
        "s_waitcnt lgkmcnt(4)\n"
        "v_mfma_f32_16x16x32_bf16 v[224:227], v[164:167], a[228:231], v[224:227]\n"
        "v_mfma_f32_16x16x32_bf16 v[220:223], v[160:163], a[228:231], v[220:223]\n"
        "v_mfma_f32_16x16x32_bf16 v[220:223], v[156:159], a[232:235], v[220:223]\n"
        "s_waitcnt lgkmcnt(0)\n"
        "s_cmp_eq_u32 s42, 2\n"
        "s_cbranch_scc1 12f\n"
        "s_cmp_eq_u32 s42, 3\n"
        "s_cbranch_scc1 12f\n"
        ".p2align 6\n"
        "s_nop 0\n"
        "s_nop 0\n"
        "s_nop 0\n"
        "s_nop 0\n"
        "11:\n"
        "10:\n"
        "v_mfma_f32_16x16x32_bf16 v[112:115], a[72:75], a[236:239], 0\n"
        "v_mov_b64 v[124:125], 0\n"
        "v_cvt_pk_bf16_f32 v232, v176, v180\n"
        "v_mfma_f32_16x16x32_bf16 v[112:115], a[76:79], a[240:243], v[112:115]\n"
        "s_waitcnt lgkmcnt(3)\n"
        "s_add_u32 s41, s30, s40\n"
        "v_cvt_pk_bf16_f32 v233, v184, v172\n"
        "v_cvt_pk_bf16_f32 v234, v177, v181\n"
        "v_mfma_f32_16x16x32_bf16 v[112:115], a[80:83], a[244:247], v[112:115]\n"
        "s_waitcnt lgkmcnt(2)\n"
        "buffer_store_dwordx2 v[232:233], v250, s[16:19], s38 offen offset:0 sc1\n"
        "v_cvt_pk_bf16_f32 v235, v185, v173\n"
        "v_mfma_f32_16x16x32_bf16 v[112:115], a[84:87], a[248:251], v[112:115]\n"
        "s_waitcnt lgkmcnt(1)\n"
        "v_cvt_pk_bf16_f32 v232, v178, v182\n"
        "v_cvt_pk_bf16_f32 v233, v186, v174\n"
        "v_mfma_f32_16x16x32_bf16 v[112:115], a[88:91], a[216:219], v[112:115]\n"
        "s_waitcnt lgkmcnt(0)\n"
        "v_mfma_f32_16x16x32_bf16 v[112:115], a[92:95], a[220:223], v[112:115]\n"
        "s_nop 2\n"
        "v_smfmac_f32_16x16x64_bf16 v[124:127], a[252:255], v[88:95], v237\n"
        "s_nop 1\n"
        "v_mfma_f32_16x16x32_bf16 v[80:83], v[132:135], v[92:95], 0\n"
        "buffer_store_dwordx2 v[234:235], v250, s[16:19], s38 offen offset:384 sc1\n"
        "v_cvt_pk_bf16_f32 v152, v112, v113\n"
        "v_mfma_f32_16x16x32_bf16 v[80:83], v[128:131], v[88:91], v[80:83]\n"
        "v_cvt_pk_bf16_f32 v153, v114, v115\n"
        "v_cvt_pk_bf16_f32 v234, v179, v183\n"
        "v_mfma_f32_16x16x32_bf16 v[84:87], v[136:139], v[88:91], 0\n"
        "v_cvt_pk_bf16_f32 v235, v187, v175\n"
        "v_add_f32 v125, v124, v125\n"
        "v_mfma_f32_16x16x32_bf16 v[84:87], v[140:143], v[92:95], v[84:87]\n"
        "v_rcp_f32 v124, v125\n"
        "v_mfma_f32_16x16x32_bf16 v[96:99], v[148:151], v[92:95], 0\n"
        "buffer_store_dwordx2 v[232:233], v250, s[16:19], s38 offen offset:768 sc1\n"
        "v_mul_f32 v80, v124, v80\n"
        "v_mfma_f32_16x16x32_bf16 v[96:99], v[144:147], v[88:91], v[96:99]\n"
        "s_add_u32 s35, s47, 786432\n"
        "v_mul_f32 v81, v124, v81\n"
        "v_mul_f32 v82, v124, v82\n"
        "v_mfma_f32_16x16x32_bf16 v[116:119], a[116:119], a[220:223], 0\n"
        "s_sub_u32 s46, s35, 100663296\n"
        "v_mul_f32 v83, v124, v83\n"
        "v_cvt_pk_bf16_f32 v80, v80, v81\n"
        "v_mfma_f32_16x16x32_bf16 v[116:119], a[112:115], a[216:219], v[116:119]\n"
        "s_cmp_lt_u32 s46, 786432\n"
        "v_cvt_pk_bf16_f32 v81, v82, v83\n"
        "v_cvt_pk_bf16_f32 v232, v196, v200\n"
        "v_mfma_f32_16x16x32_bf16 v[116:119], a[108:111], a[248:251], v[116:119]\n"
        "buffer_store_dwordx2 v[234:235], v250, s[16:19], s38 offen offset:1152 sc1\n"
        "v_cvt_pk_bf16_f32 v233, v204, v192\n"
        "v_mfma_f32_16x16x32_bf16 v[116:119], a[104:107], a[244:247], v[116:119]\n"
        "s_cselect_b32 s35, s46, s35\n"
        "v_mul_f32 v84, v124, v84\n"
        "v_mul_f32 v85, v124, v85\n"
        "v_mfma_f32_16x16x32_bf16 v[116:119], a[100:103], a[240:243], v[116:119]\n"
        "v_mul_f32 v86, v124, v86\n"
        "v_mul_f32 v87, v124, v87\n"
        "v_mfma_f32_16x16x32_bf16 v[116:119], a[96:99], a[236:239], v[116:119]\n"
        "v_cvt_pk_bf16_f32 v82, v84, v85\n"
        "v_cvt_pk_bf16_f32 v83, v86, v87\n"
        "v_mfma_f32_16x16x32_bf16 v[120:123], a[120:123], a[236:239], 0\n"
        "ds_write_b128 v245, v[80:83] offset:768\n"
        "v_mfma_f32_16x16x32_bf16 v[120:123], a[124:127], a[240:243], v[120:123]\n"
        "buffer_store_dwordx2 v[232:233], v250, s[16:19], s39 offen offset:0 sc1\n"
        "v_mul_f32 v96, v124, v96\n"
        "v_mfma_f32_16x16x32_bf16 v[120:123], a[128:131], a[244:247], v[120:123]\n"
        "v_mul_f32 v97, v124, v97\n"
        "v_mul_f32 v98, v124, v98\n"
        "v_mfma_f32_16x16x32_bf16 v[120:123], a[132:135], a[248:251], v[120:123]\n"
        "v_mul_f32 v99, v124, v99\n"
        "v_cvt_pk_bf16_f32 v234, v197, v201\n"
        "v_mfma_f32_16x16x32_bf16 v[120:123], a[136:139], a[216:219], v[120:123]\n"
        "v_cvt_pk_bf16_f32 v235, v205, v193\n"
        "v_cvt_pk_bf16_f32 v154, v116, v117\n"
        "v_mfma_f32_16x16x32_bf16 v[120:123], a[140:143], a[220:223], v[120:123]\n"
        "buffer_store_dwordx2 v[234:235], v250, s[16:19], s39 offen offset:384 sc1\n"
        "v_cvt_pk_bf16_f32 v155, v118, v119\n"
        "v_mfma_f32_16x16x32_bf16 v[112:115], a[20:23], a[220:223], 0\n"
        "v_cvt_pk_bf16_f32 v96, v96, v97\n"
        "v_cvt_pk_bf16_f32 v97, v98, v99\n"
        "v_mfma_f32_16x16x32_bf16 v[112:115], a[16:19], a[216:219], v[112:115]\n"
        "ds_write_b64 v246, v[96:97] offset:768\n"
        "v_mfma_f32_16x16x32_bf16 v[112:115], a[12:15], a[248:251], v[112:115]\n"
        "v_max3_u32 v236, v236, v124, v125\n"
        "v_cvt_pk_bf16_f32 v232, v198, v202\n"
        "v_mfma_f32_16x16x32_bf16 v[112:115], a[8:11], a[244:247], v[112:115]\n"
        "v_cvt_pk_bf16_f32 v233, v206, v194\n"
        "v_cmp_lt_u32 vcc, s24, v236\n"
        "v_mfma_f32_16x16x32_bf16 v[112:115], a[4:7], a[240:243], v[112:115]\n"
        "buffer_store_dwordx2 v[232:233], v250, s[16:19], s39 offen offset:768 sc1\n"
        "v_cvt_pk_bf16_f32 v168, v120, v121\n"
        "v_mfma_f32_16x16x32_bf16 v[112:115], a[0:3], a[236:239], v[112:115]\n"
        "s_cmp_lg_u64 vcc, 0\n"
        "s_addc_u32 s25, s25, s25\n"
        "v_cvt_pk_bf16_f32 v169, v122, v123\n"
        "v_mfma_f32_16x16x32_bf16 v[116:119], a[24:27], a[236:239], 0\n"
        "v_cvt_pk_bf16_f32 v234, v199, v203\n"
        "v_cvt_pk_bf16_f32 v235, v207, v195\n"
        "v_mfma_f32_16x16x32_bf16 v[116:119], a[28:31], a[240:243], v[116:119]\n"
        "v_cvt_pk_bf16_f32 v232, v212, v220\n"
        "v_cvt_pk_bf16_f32 v156, v112, v113\n"
        "v_mfma_f32_16x16x32_bf16 v[116:119], a[32:35], a[244:247], v[116:119]\n"
        "buffer_store_dwordx2 v[234:235], v250, s[16:19], s39 offen offset:1152 sc1\n"
        "v_cvt_pk_bf16_f32 v157, v114, v115\n"
        "v_mfma_f32_16x16x32_bf16 v[116:119], a[36:39], a[248:251], v[116:119]\n"
        "v_cvt_pk_bf16_f32 v234, v213, v221\n"
        "v_xor_b32 v251, 0x2000, v251\n"
        "v_mfma_f32_16x16x32_bf16 v[116:119], a[40:43], a[216:219], v[116:119]\n"
        "v_xor_b32 v252, 0x2000, v252\n"
        "v_mfma_f32_16x16x32_bf16 v[116:119], a[44:47], a[220:223], v[116:119]\n"
        "s_nop 2\n"
        "v_mfma_f32_16x16x32_bf16 v[120:123], a[68:71], a[220:223], 0\n"
        "s_nop 2\n"
        "v_mfma_f32_16x16x32_bf16 v[120:123], a[64:67], a[216:219], v[120:123]\n"
        "buffer_store_dword v232, v238, s[16:19], s38 offen offset:0 sc1\n"
        "v_mfma_f32_16x16x32_bf16 v[120:123], a[60:63], a[248:251], v[120:123]\n"
        "ds_read_b128 a[224:227], v240 offset:1536\n"
        "v_cvt_pk_bf16_f32 v158, v116, v117\n"
        "v_mfma_f32_16x16x32_bf16 v[120:123], a[56:59], a[244:247], v[120:123]\n"
        "ds_read_b128 a[228:231], v241 offset:1536\n"
        "v_cvt_pk_bf16_f32 v159, v118, v119\n"
        "v_mfma_f32_16x16x32_bf16 v[120:123], a[52:55], a[240:243], v[120:123]\n"
        "ds_read_b128 a[232:235], v240 offset:1664\n"
        "v_cvt_pk_bf16_f32 v232, v214, v222\n"
        "v_mfma_f32_16x16x32_bf16 v[120:123], a[48:51], a[236:239], v[120:123]\n"
        "buffer_store_dword v234, v238, s[16:19], s38 offen offset:384 sc1\n"
        "v_mfma_f32_16x16x32_bf16 v[112:115], a[236:239], a[144:147], 0\n"
        "v_add_u32 v247, s45, v247\n"
        "v_cvt_pk_bf16_f32 v234, v215, v223\n"
        "v_mfma_f32_16x16x32_bf16 v[112:115], a[240:243], a[148:151], v[112:115]\n"
        "s_nop 2\n"
        "v_mfma_f32_16x16x32_bf16 v[112:115], a[244:247], a[152:155], v[112:115]\n"
        "v_cvt_pk_bf16_f32 v160, v120, v121\n"
        "v_cvt_pk_bf16_f32 v161, v122, v123\n"
        "v_mfma_f32_16x16x32_bf16 v[112:115], a[248:251], a[156:159], v[112:115]\n"
        "v_cvt_pk_bf16_f32 v166, v120, v121\n"
        "v_cvt_pk_bf16_f32 v167, v122, v123\n"
        "v_mfma_f32_16x16x32_bf16 v[112:115], a[216:219], a[160:163], v[112:115]\n"
        "buffer_store_dword v232, v238, s[16:19], s38 offen offset:768 sc1\n"
        "v_mfma_f32_16x16x32_bf16 v[112:115], a[220:223], a[164:167], v[112:115]\n"
        "s_nop 2\n"
        "v_mfma_f32_16x16x32_bf16 v[116:119], a[220:223], a[188:191], 0\n"
        "v_cvt_pk_bf16_f32 v232, v216, v224\n"
        "s_nop 0\n"
        "v_mfma_f32_16x16x32_bf16 v[116:119], a[216:219], a[184:187], v[116:119]\n"
        "s_nop 0\n"
        "v_cvt_pk_bf16_f32 v128, v112, v113\n"
        "v_cvt_pk_bf16_f32 v129, v114, v115\n"
        "v_mfma_f32_16x16x32_bf16 v[116:119], a[248:251], a[180:183], v[116:119]\n"
        "buffer_store_dword v234, v238, s[16:19], s38 offen offset:1152 sc1\n"
        "v_mfma_f32_16x16x32_bf16 v[116:119], a[244:247], a[176:179], v[116:119]\n"
        "s_nop 0\n"
        "v_cvt_pk_bf16_f32 v234, v217, v225\n"
        "v_mfma_f32_16x16x32_bf16 v[116:119], a[240:243], a[172:175], v[116:119]\n"
        "v_mfma_f32_16x16x32_bf16 v[116:119], a[236:239], a[168:171], v[116:119]\n"
        "s_nop 2\n"
        "v_mfma_f32_16x16x32_bf16 v[120:123], a[236:239], a[192:195], 0\n"
        "ds_read_b128 a[236:239], v241 offset:1664\n"
        "s_nop 0\n"
        "v_mfma_f32_16x16x32_bf16 v[120:123], a[240:243], a[196:199], v[120:123]\n"
        "buffer_store_dword v232, v238, s[16:19], s39 offen offset:0 sc1\n"
        "v_mfma_f32_16x16x32_bf16 v[120:123], a[244:247], a[200:203], v[120:123]\n"
        "ds_read_b128 a[240:243], v240 offset:1792\n"
        "ds_read_b128 a[244:247], v241 offset:1792\n"
        "v_mfma_f32_16x16x32_bf16 v[120:123], a[248:251], a[204:207], v[120:123]\n"
        "v_cvt_pk_bf16_f32 v136, v116, v117\n"
        "v_cvt_pk_bf16_f32 v137, v118, v119\n"
        "v_mfma_f32_16x16x32_bf16 v[120:123], a[216:219], a[208:211], v[120:123]\n"
        "v_cvt_pk_bf16_f32 v232, v218, v226\n"
        "v_mfma_f32_16x16x32_bf16 v[120:123], a[220:223], a[212:215], v[120:123]\n"
        "s_waitcnt lgkmcnt(5)\n"
        "buffer_store_dword v234, v238, s[16:19], s39 offen offset:384 sc1\n"
        "v_mfma_f32_16x16x32_bf16 v[112:115], a[72:75], a[224:227], 0\n"
        "s_waitcnt lgkmcnt(4)\n"
        "s_nop 1\n"
        "v_mfma_f32_16x16x32_bf16 v[112:115], a[76:79], a[228:231], v[112:115]\n"
        "s_waitcnt lgkmcnt(3)\n"
        "v_cvt_pk_bf16_f32 v234, v219, v227\n"
        "s_nop 0\n"
        "v_mfma_f32_16x16x32_bf16 v[112:115], a[80:83], a[232:235], v[112:115]\n"
        "s_waitcnt lgkmcnt(2)\n"
        "v_cvt_pk_bf16_f32 v144, v120, v121\n"
        "v_cvt_pk_bf16_f32 v145, v122, v123\n"
        "v_mfma_f32_16x16x32_bf16 v[112:115], a[84:87], a[236:239], v[112:115]\n"
        "s_waitcnt lgkmcnt(1)\n"
        "v_mfma_f32_16x16x32_bf16 v[112:115], a[88:91], a[240:243], v[112:115]\n"
        "s_waitcnt lgkmcnt(0)\n"
        "buffer_store_dword v232, v238, s[16:19], s39 offen offset:768 sc1\n"
        "v_mfma_f32_16x16x32_bf16 v[112:115], a[92:95], a[244:247], v[112:115]\n"
        "s_nop 2\n"
        "v_mfma_f32_16x16x32_bf16 v[116:119], a[116:119], a[244:247], 0\n"
        "s_nop 2\n"
        "v_mfma_f32_16x16x32_bf16 v[116:119], a[112:115], a[240:243], v[116:119]\n"
        "v_cvt_pk_bf16_f32 v172, v112, v113\n"
        "v_cvt_pk_bf16_f32 v173, v114, v115\n"
        "v_mfma_f32_16x16x32_bf16 v[116:119], a[108:111], a[236:239], v[116:119]\n"
        "buffer_store_dword v234, v238, s[16:19], s39 offen offset:1152 sc1\n"
        "v_mfma_f32_16x16x32_bf16 v[116:119], a[104:107], a[232:235], v[116:119]\n"
        "v_mfma_f32_16x16x32_bf16 v[116:119], a[100:103], a[228:231], v[116:119]\n"
        "v_mfma_f32_16x16x32_bf16 v[116:119], a[96:99], a[224:227], v[116:119]\n"
        "s_mov_b32 m0, s48\n"
        "s_nop 1\n"
        "v_mfma_f32_16x16x32_bf16 v[120:123], a[120:123], a[224:227], 0\n"
        "ds_read_b128 v[162:165], v251 offset:0\n"
        "s_nop 0\n"
        "v_mfma_f32_16x16x32_bf16 v[120:123], a[124:127], a[228:231], v[120:123]\n"
        "buffer_load_dwordx4 v249, s[12:15], s36 offen offset:0 lds\n"
        "v_mfma_f32_16x16x32_bf16 v[120:123], a[128:131], a[232:235], v[120:123]\n"
        "v_cvt_pk_bf16_f32 v174, v116, v117\n"
        "v_cvt_pk_bf16_f32 v175, v118, v119\n"
        "v_mfma_f32_16x16x32_bf16 v[120:123], a[132:135], a[236:239], v[120:123]\n"
        "v_mfma_f32_16x16x32_bf16 v[120:123], a[136:139], a[240:243], v[120:123]\n"
        "v_mfma_f32_16x16x32_bf16 v[120:123], a[140:143], a[244:247], v[120:123]\n"
        "s_nop 2\n"
        "v_mfma_f32_16x16x32_bf16 v[112:115], a[20:23], a[244:247], 0\n"
        "s_nop 2\n"
        "v_mfma_f32_16x16x32_bf16 v[112:115], a[16:19], a[240:243], v[112:115]\n"
        "v_cvt_pk_bf16_f32 v190, v120, v121\n"
        "v_cvt_pk_bf16_f32 v191, v122, v123\n"
        "v_mfma_f32_16x16x32_bf16 v[112:115], a[12:15], a[236:239], v[112:115]\n"
        "v_mfma_f32_16x16x32_bf16 v[112:115], a[8:11], a[232:235], v[112:115]\n"
        "v_mfma_f32_16x16x32_bf16 v[112:115], a[4:7], a[228:231], v[112:115]\n"
        "ds_read_b128 v[182:185], v251 offset:512\n"
        "v_mfma_f32_16x16x32_bf16 v[112:115], a[0:3], a[224:227], v[112:115]\n"
        "s_nop 2\n"
        "v_mfma_f32_16x16x32_bf16 v[116:119], a[24:27], a[224:227], 0\n"
        "s_nop 2\n"
        "v_mfma_f32_16x16x32_bf16 v[116:119], a[28:31], a[228:231], v[116:119]\n"
        "v_cvt_pk_bf16_f32 v176, v112, v113\n"
        "v_cvt_pk_bf16_f32 v177, v114, v115\n"
        "v_mfma_f32_16x16x32_bf16 v[116:119], a[32:35], a[232:235], v[116:119]\n"
        "v_mfma_f32_16x16x32_bf16 v[116:119], a[36:39], a[236:239], v[116:119]\n"
        "buffer_load_dwordx4 v253, s[12:15], s36 offen offset:1024 lds\n"
        "v_mfma_f32_16x16x32_bf16 v[116:119], a[40:43], a[240:243], v[116:119]\n"
        "v_mfma_f32_16x16x32_bf16 v[116:119], a[44:47], a[244:247], v[116:119]\n"
        "s_nop 2\n"
        "v_mfma_f32_16x16x32_bf16 v[120:123], a[68:71], a[244:247], 0\n"
        "s_nop 2\n"
        "v_mfma_f32_16x16x32_bf16 v[120:123], a[64:67], a[240:243], v[120:123]\n"
        "v_cvt_pk_bf16_f32 v178, v116, v117\n"
        "v_cvt_pk_bf16_f32 v179, v118, v119\n"
        "v_mfma_f32_16x16x32_bf16 v[120:123], a[60:63], a[236:239], v[120:123]\n"
        "v_mfma_f32_16x16x32_bf16 v[120:123], a[56:59], a[232:235], v[120:123]\n"
        "v_mfma_f32_16x16x32_bf16 v[120:123], a[52:55], a[228:231], v[120:123]\n"
        "v_mfma_f32_16x16x32_bf16 v[120:123], a[48:51], a[224:227], v[120:123]\n"
        "s_nop 2\n"
        "v_mfma_f32_16x16x32_bf16 v[112:115], a[224:227], a[144:147], 0\n"
        "s_nop 2\n"
        "v_mfma_f32_16x16x32_bf16 v[112:115], a[228:231], a[148:151], v[112:115]\n"
        "v_cvt_pk_bf16_f32 v180, v120, v121\n"
        "v_cvt_pk_bf16_f32 v181, v122, v123\n"
        "v_mfma_f32_16x16x32_bf16 v[112:115], a[232:235], a[152:155], v[112:115]\n"
        "v_cvt_pk_bf16_f32 v186, v120, v121\n"
        "v_cvt_pk_bf16_f32 v187, v122, v123\n"
        "v_mfma_f32_16x16x32_bf16 v[112:115], a[236:239], a[156:159], v[112:115]\n"
        "s_add_u32 m0, s41, 0\n"
        "v_mfma_f32_16x16x32_bf16 v[112:115], a[240:243], a[160:163], v[112:115]\n"
        "ds_read_b128 a[248:251], v240 offset:12288\n"
        "v_mfma_f32_16x16x32_bf16 v[112:115], a[244:247], a[164:167], v[112:115]\n"
        "buffer_load_dwordx4 v242, s[8:11], s47 offen lds\n"
        "v_mfma_f32_16x16x32_bf16 v[96:99], v[152:155], v[156:159], v[0:3]\n"
        "s_waitcnt lgkmcnt(2)\n"
        "ds_read_b128 a[216:219], v241 offset:12288\n"
        "s_nop 0\n"
        "v_mfma_f32_16x16x32_bf16 v[96:99], v[168:171], v[160:163], v[96:99]\n"
        "ds_read_b128 a[220:223], v240 offset:12416\n"
        "s_sub_u32 s31, s30, s28\n"
        "s_nop 0\n"
        "v_mfma_f32_16x16x32_bf16 v[100:103], v[172:175], v[156:159], v[4:7]\n"
        "s_sub_u32 s45, s28, s29\n"
        "v_cvt_pk_bf16_f32 v130, v112, v113\n"
        "v_cvt_pk_bf16_f32 v131, v114, v115\n"
        "v_mfma_f32_16x16x32_bf16 v[100:103], v[188:191], v[164:167], v[100:103]\n"
        "s_add_u32 s28, s28, s31\n"
        "ds_read_b128 v[162:165], v252 offset:0\n"
        "v_mfma_f32_16x16x32_bf16 v[116:119], a[244:247], a[188:191], 0\n"
        "s_mov_b32 s30, s29\n"
        "v_exp_f32 v96, v96\n"
        "v_mfma_f32_16x16x32_bf16 v[116:119], a[240:243], a[184:187], v[116:119]\n"
        "s_add_u32 s29, s29, s45\n"
        "v_exp_f32 v97, v97\n"
        "v_mfma_f32_16x16x32_bf16 v[116:119], a[236:239], a[180:183], v[116:119]\n"
        "v_exp_f32 v98, v98\n"
        "v_mfma_f32_16x16x32_bf16 v[116:119], a[232:235], a[176:179], v[116:119]\n"
        "v_exp_f32 v100, v100\n"
        "v_mfma_f32_16x16x32_bf16 v[116:119], a[228:231], a[172:175], v[116:119]\n"
        "v_exp_f32 v99, v99\n"
        "v_mfma_f32_16x16x32_bf16 v[116:119], a[224:227], a[168:171], v[116:119]\n"
        "v_exp_f32 v101, v101\n"
        "v_mfma_f32_16x16x32_bf16 v[104:107], v[152:155], v[176:179], v[16:19]\n"
        "s_waitcnt lgkmcnt(4)\n"
        "v_exp_f32 v102, v102\n"
        "v_mfma_f32_16x16x32_bf16 v[104:107], v[168:171], v[180:183], v[104:107]\n"
        "s_add_u32 m0, s41, 1024\n"
        "v_cvt_pk_bf16_f32 v64, v96, v97\n"
        "v_cvt_pk_bf16_f32 v65, v98, v99\n"
        "v_mfma_f32_16x16x32_bf16 v[108:111], v[172:175], v[176:179], v[20:23]\n"
        "v_cvt_pk_bf16_f32 v138, v116, v117\n"
        "v_cvt_pk_bf16_f32 v139, v118, v119\n"
        "v_mfma_f32_16x16x32_bf16 v[108:111], v[188:191], v[184:187], v[108:111]\n"
        "buffer_load_dwordx4 v243, s[8:11], s47 offen lds\n"
        "v_mfma_f32_16x16x32_bf16 v[120:123], a[224:227], a[192:195], 0\n"
        "ds_read_b128 a[224:227], v241 offset:12416\n"
        "v_cvt_pk_bf16_f32 v66, v100, v101\n"
        "v_mfma_f32_16x16x32_bf16 v[120:123], a[228:231], a[196:199], v[120:123]\n"
        "ds_read_b128 a[228:231], v240 offset:12544\n"
        "v_mfma_f32_16x16x32_bf16 v[120:123], a[232:235], a[200:203], v[120:123]\n"
        "ds_read_b128 a[232:235], v241 offset:12544\n"
        "v_mfma_f32_16x16x32_bf16 v[120:123], a[236:239], a[204:207], v[120:123]\n"
        "ds_read_b128 v[202:205], v251 offset:4096\n"
        "v_mfma_f32_16x16x32_bf16 v[120:123], a[240:243], a[208:211], v[120:123]\n"
        "ds_read_b128 v[182:185], v252 offset:512\n"
        "v_mfma_f32_16x16x32_bf16 v[120:123], a[244:247], a[212:215], v[120:123]\n"
        "s_waitcnt lgkmcnt(8)\n"
        "v_exp_f32 v103, v103\n"
        "v_mfma_f32_16x16x32_bf16 v[112:115], a[0:3], a[248:251], 0\n"
        "s_waitcnt lgkmcnt(7)\n"
        "v_cvt_pk_bf16_f32 v67, v102, v103\n"
        "s_nop 0\n"
        "v_mfma_f32_16x16x32_bf16 v[112:115], a[4:7], a[216:219], v[112:115]\n"
        "s_waitcnt lgkmcnt(6)\n"
        "v_exp_f32 v104, v104\n"
        "v_mfma_f32_16x16x32_bf16 v[112:115], a[8:11], a[220:223], v[112:115]\n"
        "s_waitcnt lgkmcnt(4)\n"
        "v_exp_f32 v105, v105\n"
        "v_mfma_f32_16x16x32_bf16 v[112:115], a[12:15], a[224:227], v[112:115]\n"
        "s_waitcnt lgkmcnt(3)\n"
        "v_exp_f32 v106, v106\n"
        "v_mfma_f32_16x16x32_bf16 v[112:115], a[16:19], a[228:231], v[112:115]\n"
        "s_waitcnt lgkmcnt(2)\n"
        "v_cvt_pk_bf16_f32 v72, v104, v105\n"
        "v_cvt_pk_bf16_f32 v146, v120, v121\n"
        "v_mfma_f32_16x16x32_bf16 v[112:115], a[20:23], a[232:235], v[112:115]\n"
        "s_add_u32 m0, s41, 2048\n"
        "v_exp_f32 v108, v108\n"
        "v_mfma_f32_16x16x32_bf16 v[116:119], a[44:47], a[232:235], 0\n"
        "v_cvt_pk_bf16_f32 v147, v122, v123\n"
        "v_add_u32 v245, s45, v245\n"
        "v_mfma_f32_16x16x32_bf16 v[116:119], a[40:43], a[228:231], v[116:119]\n"
        "buffer_load_dwordx4 v244, s[8:11], s47 offen lds\n"
        "v_mfma_f32_16x16x32_bf16 v[116:119], a[36:39], a[224:227], v[116:119]\n"
        "v_exp_f32 v107, v107\n"
        "v_mfma_f32_16x16x32_bf16 v[116:119], a[32:35], a[220:223], v[116:119]\n"
        "v_exp_f32 v109, v109\n"
        "v_mfma_f32_16x16x32_bf16 v[116:119], a[28:31], a[216:219], v[116:119]\n"
        "v_cvt_pk_bf16_f32 v73, v106, v107\n"
        "v_cvt_pk_bf16_f32 v74, v108, v109\n"
        "v_mfma_f32_16x16x32_bf16 v[116:119], a[24:27], a[248:251], v[116:119]\n"
        "v_exp_f32 v110, v110\n"
        "v_mfma_f32_16x16x32_bf16 v[120:123], a[48:51], a[248:251], 0\n"
        "v_exp_f32 v111, v111\n"
        "v_mfma_f32_16x16x32_bf16 v[120:123], a[52:55], a[216:219], v[120:123]\n"
        "v_cvt_pk_bf16_f32 v75, v110, v111\n"
        "v_cvt_pk_bf16_f32 v196, v112, v113\n"
        "v_mfma_f32_16x16x32_bf16 v[120:123], a[56:59], a[220:223], v[120:123]\n"
        "v_cvt_pk_bf16_f32 v197, v114, v115\n"
        "v_cvt_pk_bf16_f32 v198, v116, v117\n"
        "v_mfma_f32_16x16x32_bf16 v[120:123], a[60:63], a[224:227], v[120:123]\n"
        "v_cvt_pk_bf16_f32 v199, v118, v119\n"
        "v_add_u32 v246, s45, v246\n"
        "v_mfma_f32_16x16x32_bf16 v[120:123], a[64:67], a[228:231], v[120:123]\n"
        "v_mfma_f32_16x16x32_bf16 v[120:123], a[68:71], a[232:235], v[120:123]\n"
        "s_nop 2\n"
        "v_mfma_f32_16x16x32_bf16 v[112:115], a[92:95], a[232:235], 0\n"
        "s_nop 2\n"
        "v_mfma_f32_16x16x32_bf16 v[112:115], a[88:91], a[228:231], v[112:115]\n"
        "s_add_u32 m0, s41, 12288\n"
        "v_cvt_pk_bf16_f32 v200, v120, v121\n"
        "v_cvt_pk_bf16_f32 v201, v122, v123\n"
        "v_mfma_f32_16x16x32_bf16 v[112:115], a[84:87], a[224:227], v[112:115]\n"
        "v_cvt_pk_bf16_f32 v206, v120, v121\n"
        "v_cvt_pk_bf16_f32 v207, v122, v123\n"
        "v_mfma_f32_16x16x32_bf16 v[112:115], a[80:83], a[220:223], v[112:115]\n"
        "buffer_load_dwordx4 v242, s[8:11], s35 offen lds\n"
        "v_mfma_f32_16x16x32_bf16 v[112:115], a[76:79], a[216:219], v[112:115]\n"
        "v_mfma_f32_16x16x32_bf16 v[112:115], a[72:75], a[248:251], v[112:115]\n"
        "s_nop 2\n"
        "v_mfma_f32_16x16x32_bf16 v[96:99], v[152:155], v[196:199], v[32:35]\n"
        "s_waitcnt lgkmcnt(1)\n"
        "s_nop 1\n"
        "v_mfma_f32_16x16x32_bf16 v[96:99], v[168:171], v[200:203], v[96:99]\n"
        "s_nop 0\n"
        "v_cvt_pk_bf16_f32 v192, v112, v113\n"
        "v_cvt_pk_bf16_f32 v193, v114, v115\n"
        "v_mfma_f32_16x16x32_bf16 v[100:103], v[172:175], v[196:199], v[36:39]\n"
        "v_mfma_f32_16x16x32_bf16 v[100:103], v[188:191], v[204:207], v[100:103]\n"
        "ds_read_b128 v[202:205], v252 offset:4096\n"
        "s_cmp_eq_u32 s37, 100663296\n"
        "v_mfma_f32_16x16x32_bf16 v[116:119], a[96:99], a[248:251], 0\n"
        "s_cselect_b32 s46, 0, s37\n"
        "v_exp_f32 v96, v96\n"
        "v_mfma_f32_16x16x32_bf16 v[116:119], a[100:103], a[216:219], v[116:119]\n"
        "s_add_u32 s38, s46, s43\n"
        "v_exp_f32 v97, v97\n"
        "v_mfma_f32_16x16x32_bf16 v[116:119], a[104:107], a[220:223], v[116:119]\n"
        "s_add_u32 s39, s46, s44\n"
        "v_exp_f32 v98, v98\n"
        "v_mfma_f32_16x16x32_bf16 v[116:119], a[108:111], a[224:227], v[116:119]\n"
        "s_add_u32 s37, s37, 6291456\n"
        "v_exp_f32 v100, v100\n"
        "v_mfma_f32_16x16x32_bf16 v[116:119], a[112:115], a[228:231], v[116:119]\n"
        "v_exp_f32 v99, v99\n"
        "v_mfma_f32_16x16x32_bf16 v[116:119], a[116:119], a[232:235], v[116:119]\n"
        "s_add_u32 m0, s41, 13312\n"
        "v_exp_f32 v101, v101\n"
        "v_mfma_f32_16x16x32_bf16 v[120:123], a[140:143], a[232:235], 0\n"
        "v_exp_f32 v102, v102\n"
        "v_mfma_f32_16x16x32_bf16 v[120:123], a[136:139], a[228:231], v[120:123]\n"
        "buffer_load_dwordx4 v243, s[8:11], s35 offen lds\n"
        "v_mfma_f32_16x16x32_bf16 v[120:123], a[132:135], a[224:227], v[120:123]\n"
        "v_cvt_pk_bf16_f32 v80, v96, v97\n"
        "v_cvt_pk_bf16_f32 v194, v116, v117\n"
        "v_mfma_f32_16x16x32_bf16 v[120:123], a[128:131], a[220:223], v[120:123]\n"
        "v_cvt_pk_bf16_f32 v195, v118, v119\n"
        "v_cvt_pk_bf16_f32 v81, v98, v99\n"
        "v_mfma_f32_16x16x32_bf16 v[120:123], a[124:127], a[216:219], v[120:123]\n"
        "v_cvt_pk_bf16_f32 v82, v100, v101\n"
        "v_mfma_f32_16x16x32_bf16 v[120:123], a[120:123], a[248:251], v[120:123]\n"
        "v_exp_f32 v103, v103\n"
        "v_mfma_f32_16x16x32_bf16 v[112:115], a[248:251], a[144:147], 0\n"
        "v_cvt_pk_bf16_f32 v83, v102, v103\n"
        "s_nop 0\n"
        "v_mfma_f32_16x16x32_bf16 v[112:115], a[216:219], a[148:151], v[112:115]\n"
        "s_nop 2\n"
        "v_mfma_f32_16x16x32_bf16 v[112:115], a[220:223], a[152:155], v[112:115]\n"
        "ds_read_b128 a[236:239], v240 offset:13824\n"
        "v_cvt_pk_bf16_f32 v208, v120, v121\n"
        "v_mfma_f32_16x16x32_bf16 v[112:115], a[224:227], a[156:159], v[112:115]\n"
        "ds_read_b128 a[240:243], v241 offset:13824\n"
        "v_cvt_pk_bf16_f32 v209, v122, v123\n"
        "v_mfma_f32_16x16x32_bf16 v[112:115], a[228:231], a[160:163], v[112:115]\n"
        "ds_read_b128 a[244:247], v240 offset:13952\n"
        "v_mfma_f32_16x16x32_bf16 v[112:115], a[232:235], a[164:167], v[112:115]\n"
        "s_nop 2\n"
        "v_mfma_f32_16x16x32_bf16 v[104:107], v[192:195], v[156:159], v[8:11]\n"
        "s_nop 1\n"
        "v_mfma_f32_16x16x32_bf16 v[104:107], v[208:211], v[160:163], v[104:107]\n"
        "s_add_u32 m0, s41, 14336\n"
        "v_cvt_pk_bf16_f32 v132, v112, v113\n"
        "v_cvt_pk_bf16_f32 v133, v114, v115\n"
        "v_mfma_f32_16x16x32_bf16 v[116:119], a[232:235], a[188:191], 0\n"
        "v_mfma_f32_16x16x32_bf16 v[116:119], a[228:231], a[184:187], v[116:119]\n"
        "buffer_load_dwordx4 v244, s[8:11], s35 offen lds\n"
        "v_mfma_f32_16x16x32_bf16 v[116:119], a[224:227], a[180:183], v[116:119]\n"
        "v_mfma_f32_16x16x32_bf16 v[116:119], a[220:223], a[176:179], v[116:119]\n"
        "v_exp_f32 v104, v104\n"
        "v_mfma_f32_16x16x32_bf16 v[116:119], a[216:219], a[172:175], v[116:119]\n"
        "v_exp_f32 v105, v105\n"
        "v_mfma_f32_16x16x32_bf16 v[116:119], a[248:251], a[168:171], v[116:119]\n"
        "v_exp_f32 v106, v106\n"
        "v_mfma_f32_16x16x32_bf16 v[108:111], v[192:195], v[176:179], v[24:27]\n"
        "s_waitcnt lgkmcnt(4)\n"
        "v_exp_f32 v107, v107\n"
        "v_mfma_f32_16x16x32_bf16 v[108:111], v[208:211], v[180:183], v[108:111]\n"
        "v_cvt_pk_bf16_f32 v68, v104, v105\n"
        "v_cvt_pk_bf16_f32 v69, v106, v107\n"
        "v_mfma_f32_16x16x32_bf16 v[120:123], a[248:251], a[192:195], 0\n"
        "ds_read_b128 a[248:251], v241 offset:13952\n"
        "v_cvt_pk_bf16_f32 v140, v116, v117\n"
        "v_mfma_f32_16x16x32_bf16 v[120:123], a[216:219], a[196:199], v[120:123]\n"
        "ds_read_b128 a[216:219], v240 offset:14080\n"
        "v_cvt_pk_bf16_f32 v141, v118, v119\n"
        "v_mfma_f32_16x16x32_bf16 v[120:123], a[220:223], a[200:203], v[120:123]\n"
        "ds_read_b128 a[220:223], v241 offset:14080\n"
        "v_mfma_f32_16x16x32_bf16 v[120:123], a[224:227], a[204:207], v[120:123]\n"
        "v_exp_f32 v108, v108\n"
        "v_mfma_f32_16x16x32_bf16 v[120:123], a[228:231], a[208:211], v[120:123]\n"
        "v_exp_f32 v109, v109\n"
        "v_mfma_f32_16x16x32_bf16 v[120:123], a[232:235], a[212:215], v[120:123]\n"
        "v_exp_f32 v110, v110\n"
        "v_mfma_f32_16x16x32_bf16 v[96:99], v[192:195], v[196:199], v[40:43]\n"
        "s_waitcnt lgkmcnt(6)\n"
        "v_exp_f32 v111, v111\n"
        "v_mfma_f32_16x16x32_bf16 v[96:99], v[208:211], v[200:203], v[96:99]\n"
        "s_waitcnt lgkmcnt(5)\n"
        "v_cvt_pk_bf16_f32 v76, v108, v109\n"
        "v_cvt_pk_bf16_f32 v77, v110, v111\n"
        "v_mfma_f32_16x16x32_bf16 v[112:115], a[72:75], a[236:239], 0\n"
        "s_waitcnt lgkmcnt(4)\n"
        "s_nop 0\n"
        "v_cvt_pk_bf16_f32 v148, v120, v121\n"
        "v_mfma_f32_16x16x32_bf16 v[112:115], a[76:79], a[240:243], v[112:115]\n"
        ".long 0xbec200ff\n"
        "45:\n"
        "s_branch 61b\n"
        "s_waitcnt lgkmcnt(3)\n"
        "v_cvt_pk_bf16_f32 v149, v122, v123\n"
        "v_mfma_f32_16x16x32_bf16 v[112:115], a[80:83], a[244:247], v[112:115]\n"
        "s_waitcnt lgkmcnt(2)\n"
        "v_exp_f32 v96, v96\n"
        "v_mfma_f32_16x16x32_bf16 v[112:115], a[84:87], a[248:251], v[112:115]\n"
        "s_waitcnt lgkmcnt(1)\n"
        "v_exp_f32 v97, v97\n"
        "v_mfma_f32_16x16x32_bf16 v[112:115], a[88:91], a[216:219], v[112:115]\n"
        "s_waitcnt lgkmcnt(0)\n"
        "v_exp_f32 v98, v98\n"
        "v_mfma_f32_16x16x32_bf16 v[112:115], a[92:95], a[220:223], v[112:115]\n"
        "v_exp_f32 v99, v99\n"
        "v_mfma_f32_16x16x32_bf16 v[116:119], a[116:119], a[220:223], 0\n"
        "v_cvt_pk_bf16_f32 v84, v96, v97\n"
        "v_cvt_pk_bf16_f32 v85, v98, v99\n"
        "v_mfma_f32_16x16x32_bf16 v[116:119], a[112:115], a[216:219], v[116:119]\n"
        "v_add_u32 v240, s31, v240\n"
        "v_add_u32 v241, s31, v241\n"
        "v_mfma_f32_16x16x32_bf16 v[116:119], a[108:111], a[248:251], v[116:119]\n"
        "v_cvt_pk_bf16_f32 v212, v112, v113\n"
        "v_cvt_pk_bf16_f32 v213, v114, v115\n"
        "v_mfma_f32_16x16x32_bf16 v[116:119], a[104:107], a[244:247], v[116:119]\n"
        "v_mfma_f32_16x16x32_bf16 v[116:119], a[100:103], a[240:243], v[116:119]\n"
        "v_mfma_f32_16x16x32_bf16 v[116:119], a[96:99], a[236:239], v[116:119]\n"
        "s_nop 2\n"
        "v_mfma_f32_16x16x32_bf16 v[120:123], a[120:123], a[236:239], 0\n"
        "s_nop 2\n"
        "v_mfma_f32_16x16x32_bf16 v[120:123], a[124:127], a[240:243], v[120:123]\n"
        "s_add_u32 s47, s47, 6291456\n"
        "v_cvt_pk_bf16_f32 v214, v116, v117\n"
        "v_cvt_pk_bf16_f32 v215, v118, v119\n"
        "v_mfma_f32_16x16x32_bf16 v[120:123], a[128:131], a[244:247], v[120:123]\n"
        "s_add_u32 s36, s36, 2097152\n"
        "v_mfma_f32_16x16x32_bf16 v[120:123], a[132:135], a[248:251], v[120:123]\n"
        "s_xor_b32 s48, s48, 8192\n"
        "v_mfma_f32_16x16x32_bf16 v[120:123], a[136:139], a[216:219], v[120:123]\n"
        "v_mfma_f32_16x16x32_bf16 v[120:123], a[140:143], a[220:223], v[120:123]\n"
        "s_nop 2\n"
        "v_mfma_f32_16x16x32_bf16 v[112:115], a[220:223], a[164:167], 0\n"
        "s_nop 2\n"
        "v_mfma_f32_16x16x32_bf16 v[112:115], a[216:219], a[160:163], v[112:115]\n"
        "v_cvt_pk_bf16_f32 v230, v120, v121\n"
        "v_cvt_pk_bf16_f32 v231, v122, v123\n"
        "v_mfma_f32_16x16x32_bf16 v[112:115], a[248:251], a[156:159], v[112:115]\n"
        "s_barrier\n"
        "v_mfma_f32_16x16x32_bf16 v[112:115], a[244:247], a[152:155], v[112:115]\n"
        "v_mfma_f32_16x16x32_bf16 v[112:115], a[240:243], a[148:151], v[112:115]\n"
        "v_mfma_f32_16x16x32_bf16 v[112:115], a[236:239], a[144:147], v[112:115]\n"
        "s_nop 2\n"
        "v_mfma_f32_16x16x32_bf16 v[100:103], v[212:215], v[156:159], v[12:15]\n"
        "s_nop 2\n"
        "v_mfma_f32_16x16x32_bf16 v[100:103], v[228:231], v[164:167], v[100:103]\n"
        "v_cvt_pk_bf16_f32 v134, v112, v113\n"
        "v_cvt_pk_bf16_f32 v135, v114, v115\n"
        "v_mfma_f32_16x16x32_bf16 v[116:119], a[236:239], a[168:171], 0\n"
        "s_sub_u32 s27, s27, 1\n"
        "v_mfma_f32_16x16x32_bf16 v[116:119], a[240:243], a[172:175], v[116:119]\n"
        "s_cmp_lg_u32 s27, 0\n"
        "v_mfma_f32_16x16x32_bf16 v[116:119], a[244:247], a[176:179], v[116:119]\n"
        "s_cbranch_scc0 19f\n"
        "v_mfma_f32_16x16x32_bf16 v[116:119], a[248:251], a[180:183], v[116:119]\n"
        "v_mfma_f32_16x16x32_bf16 v[116:119], a[216:219], a[184:187], v[116:119]\n"
        "s_nop 0\n"
        "v_exp_f32 v100, v100\n"
        "v_mfma_f32_16x16x32_bf16 v[116:119], a[220:223], a[188:191], v[116:119]\n"
        "v_exp_f32 v101, v101\n"
        "v_mfma_f32_16x16x32_bf16 v[104:107], v[212:215], v[176:179], v[28:31]\n"
        "v_exp_f32 v102, v102\n"
        "v_mfma_f32_16x16x32_bf16 v[104:107], v[228:231], v[184:187], v[104:107]\n"
        "v_exp_f32 v103, v103\n"
        "v_mfma_f32_16x16x32_bf16 v[120:123], a[220:223], a[212:215], 0\n"
        "v_cvt_pk_bf16_f32 v70, v100, v101\n"
        "v_cvt_pk_bf16_f32 v71, v102, v103\n"
        "v_mfma_f32_16x16x32_bf16 v[120:123], a[216:219], a[208:211], v[120:123]\n"
        "v_cvt_pk_bf16_f32 v142, v116, v117\n"
        "v_cvt_pk_bf16_f32 v143, v118, v119\n"
        "v_mfma_f32_16x16x32_bf16 v[120:123], a[248:251], a[204:207], v[120:123]\n"
        "v_mov_b64 v[96:97], 0\n"
        "v_mfma_f32_16x16x32_bf16 v[120:123], a[244:247], a[200:203], v[120:123]\n"
        "v_exp_f32 v104, v104\n"
        "v_mfma_f32_16x16x32_bf16 v[120:123], a[240:243], a[196:199], v[120:123]\n"
        "v_exp_f32 v105, v105\n"
        "v_mfma_f32_16x16x32_bf16 v[120:123], a[236:239], a[192:195], v[120:123]\n"
        "v_exp_f32 v106, v106\n"
        "v_mfma_f32_16x16x32_bf16 v[108:111], v[212:215], v[196:199], v[44:47]\n"
        "v_exp_f32 v107, v107\n"
        "v_mfma_f32_16x16x32_bf16 v[108:111], v[228:231], v[204:207], v[108:111]\n"
        "v_cvt_pk_bf16_f32 v78, v104, v105\n"
        "v_cvt_pk_bf16_f32 v79, v106, v107\n"
        "v_mfma_f32_16x16x32_bf16 v[112:115], a[0:3], a[236:239], 0\n"
        "s_nop 0\n"
        "v_cvt_pk_bf16_f32 v150, v120, v121\n"
        "v_cvt_pk_bf16_f32 v151, v122, v123\n"
        "v_mfma_f32_16x16x32_bf16 v[112:115], a[4:7], a[240:243], v[112:115]\n"
        "v_mfma_f32_16x16x32_bf16 v[112:115], a[8:11], a[244:247], v[112:115]\n"
        "v_exp_f32 v108, v108\n"
        "v_mfma_f32_16x16x32_bf16 v[112:115], a[12:15], a[248:251], v[112:115]\n"
        "v_exp_f32 v109, v109\n"
        "v_mfma_f32_16x16x32_bf16 v[112:115], a[16:19], a[216:219], v[112:115]\n"
        "v_exp_f32 v110, v110\n"
        "v_mfma_f32_16x16x32_bf16 v[112:115], a[20:23], a[220:223], v[112:115]\n"
        "v_exp_f32 v111, v111\n"
        "v_smfmac_f32_16x16x64_bf16 v[96:99], a[252:255], v[64:71], v237\n"
        "v_cvt_pk_bf16_f32 v86, v108, v109\n"
        "v_mfma_f32_16x16x32_bf16 v[100:103], v[132:135], v[68:71], 0\n"
        "v_mov_b64 v[124:125], 0\n"
        "v_cvt_pk_bf16_f32 v87, v110, v111\n"
        "v_mfma_f32_16x16x32_bf16 v[100:103], v[128:131], v[64:67], v[100:103]\n"
        "s_nop 0\n"
        "v_cvt_pk_bf16_f32 v216, v112, v113\n"
        "v_cvt_pk_bf16_f32 v217, v114, v115\n"
        "v_mfma_f32_16x16x32_bf16 v[104:107], v[136:139], v[64:67], 0\n"
        "ds_read_b128 v[222:225], v251 offset:4608\n"
        "v_add_f32 v97, v96, v97\n"
        "v_mfma_f32_16x16x32_bf16 v[104:107], v[140:143], v[68:71], v[104:107]\n"
        "v_rcp_f32 v96, v97\n"
        "v_mfma_f32_16x16x32_bf16 v[108:111], v[148:151], v[68:71], 0\n"
        "v_mul_f32 v100, v96, v100\n"
        "v_mul_f32 v101, v96, v101\n"
        "v_mfma_f32_16x16x32_bf16 v[108:111], v[144:147], v[64:67], v[108:111]\n"
        "v_mul_f32 v102, v96, v102\n"
        "v_mul_f32 v103, v96, v103\n"
        "v_mfma_f32_16x16x32_bf16 v[116:119], a[44:47], a[220:223], 0\n"
        "v_cvt_pk_bf16_f32 v100, v100, v101\n"
        "v_cvt_pk_bf16_f32 v101, v102, v103\n"
        "v_mfma_f32_16x16x32_bf16 v[116:119], a[40:43], a[216:219], v[116:119]\n"
        "v_mul_f32 v104, v96, v104\n"
        "v_mul_f32 v105, v96, v105\n"
        "v_mfma_f32_16x16x32_bf16 v[116:119], a[36:39], a[248:251], v[116:119]\n"
        "v_mul_f32 v106, v96, v106\n"
        "v_mul_f32 v107, v96, v107\n"
        "v_mfma_f32_16x16x32_bf16 v[116:119], a[32:35], a[244:247], v[116:119]\n"
        "v_cvt_pk_bf16_f32 v102, v104, v105\n"
        "v_cvt_pk_bf16_f32 v103, v106, v107\n"
        "v_mfma_f32_16x16x32_bf16 v[116:119], a[28:31], a[240:243], v[116:119]\n"
        "ds_write_b128 v245, v[100:103] offset:0\n"
        "v_mfma_f32_16x16x32_bf16 v[116:119], a[24:27], a[236:239], v[116:119]\n"
        "v_mul_f32 v108, v96, v108\n"
        "v_mul_f32 v109, v96, v109\n"
        "v_smfmac_f32_16x16x64_bf16 v[124:127], a[252:255], v[72:79], v237\n"
        "v_mul_f32 v110, v96, v110\n"
        "v_mfma_f32_16x16x32_bf16 v[64:67], v[132:135], v[76:79], 0\n"
        "v_mul_f32 v111, v96, v111\n"
        "v_cvt_pk_bf16_f32 v108, v108, v109\n"
        "v_mfma_f32_16x16x32_bf16 v[64:67], v[128:131], v[72:75], v[64:67]\n"
        "ds_read_b128 v[156:159], v247 offset:0\n"
        "v_cvt_pk_bf16_f32 v218, v116, v117\n"
        "v_mfma_f32_16x16x32_bf16 v[68:71], v[136:139], v[72:75], 0\n"
        "ds_read_b128 v[160:163], v247 offset:4096\n"
        "v_cvt_pk_bf16_f32 v219, v118, v119\n"
        "v_mfma_f32_16x16x32_bf16 v[68:71], v[140:143], v[76:79], v[68:71]\n"
        "ds_read_b128 v[164:167], v247 offset:4352\n"
        "v_cvt_pk_bf16_f32 v109, v110, v111\n"
        "v_mfma_f32_16x16x32_bf16 v[120:123], v[148:151], v[76:79], 0\n"
        "ds_write_b64 v246, v[108:109] offset:0\n"
        "v_mfma_f32_16x16x32_bf16 v[120:123], v[144:147], v[72:75], v[120:123]\n"
        "v_max_u32 v236, v96, v97\n"
        "v_mov_b64 v[116:117], 0\n"
        "v_mfma_f32_16x16x32_bf16 v[112:115], a[48:51], a[236:239], 0\n"
        "v_add_f32 v125, v124, v125\n"
        "v_mfma_f32_16x16x32_bf16 v[112:115], a[52:55], a[240:243], v[112:115]\n"
        "v_rcp_f32 v124, v125\n"
        "v_mfma_f32_16x16x32_bf16 v[112:115], a[56:59], a[244:247], v[112:115]\n"
        "v_mul_f32 v64, v124, v64\n"
        "v_mul_f32 v65, v124, v65\n"
        "v_mfma_f32_16x16x32_bf16 v[112:115], a[60:63], a[248:251], v[112:115]\n"
        "v_mul_f32 v66, v124, v66\n"
        "v_mul_f32 v67, v124, v67\n"
        "v_mfma_f32_16x16x32_bf16 v[112:115], a[64:67], a[216:219], v[112:115]\n"
        "v_cvt_pk_bf16_f32 v64, v64, v65\n"
        "v_cvt_pk_bf16_f32 v65, v66, v67\n"
        "v_mfma_f32_16x16x32_bf16 v[112:115], a[68:71], a[220:223], v[112:115]\n"
        "ds_read_b128 a[216:219], v248 offset:0\n"
        "v_mul_f32 v68, v124, v68\n"
        "v_smfmac_f32_16x16x64_bf16 v[116:119], a[252:255], v[80:87], v237\n"
        "ds_read_b128 a[220:223], v248 offset:3072\n"
        "v_mfma_f32_16x16x32_bf16 v[72:75], v[132:135], v[84:87], 0\n"
        "v_mul_f32 v69, v124, v69\n"
        "v_mul_f32 v70, v124, v70\n"
        "v_mfma_f32_16x16x32_bf16 v[72:75], v[128:131], v[80:83], v[72:75]\n"
        "v_cvt_pk_bf16_f32 v220, v112, v113\n"
        "v_cvt_pk_bf16_f32 v221, v114, v115\n"
        "v_mfma_f32_16x16x32_bf16 v[76:79], v[136:139], v[80:83], 0\n"
        "ds_read_b128 a[224:227], v248 offset:32\n"
        "v_cvt_pk_bf16_f32 v226, v112, v113\n"
        "v_mfma_f32_16x16x32_bf16 v[76:79], v[140:143], v[84:87], v[76:79]\n"
        "ds_read_b128 a[228:231], v248 offset:3104\n"
        "v_cvt_pk_bf16_f32 v227, v114, v115\n"
        "v_mfma_f32_16x16x32_bf16 v[96:99], v[148:151], v[84:87], 0\n"
        "v_mul_f32 v71, v124, v71\n"
        "v_cvt_pk_bf16_f32 v66, v68, v69\n"
        "v_mfma_f32_16x16x32_bf16 v[96:99], v[144:147], v[80:83], v[96:99]\n"
        "v_cvt_pk_bf16_f32 v67, v70, v71\n"
        "v_mul_f32 v120, v124, v120\n"
        "v_mfma_f32_16x16x32_bf16 v[100:103], v[152:155], v[216:219], v[48:51]\n"
        "s_waitcnt lgkmcnt(9)\n"
        "ds_read_b128 v[152:155], v247 offset:256\n"
        "v_mul_f32 v121, v124, v121\n"
        "v_mfma_f32_16x16x32_bf16 v[100:103], v[168:171], v[220:223], v[100:103]\n"
        "ds_write_b128 v245, v[64:67] offset:256\n"
        "v_mfma_f32_16x16x32_bf16 v[104:107], v[172:175], v[216:219], v[52:55]\n"
        "v_mul_f32 v122, v124, v122\n"
        "v_mul_f32 v123, v124, v123\n"
        "v_mfma_f32_16x16x32_bf16 v[104:107], v[188:191], v[224:227], v[104:107]\n"
        "s_waitcnt lgkmcnt(5)\n"
        "v_cvt_pk_bf16_f32 v120, v120, v121\n"
        "v_cvt_pk_bf16_f32 v121, v122, v123\n"
        "v_mfma_f32_16x16x32_bf16 v[176:179], v[156:159], a[216:219], 0\n"
        "s_waitcnt lgkmcnt(4)\n"
        "ds_write_b64 v246, v[120:121] offset:256\n"
        "v_mfma_f32_16x16x32_bf16 v[176:179], v[160:163], a[220:223], v[176:179]\n"
        "ds_read_b128 v[222:225], v252 offset:4608\n"
        "v_max3_u32 v236, v236, v124, v125\n"
        "v_mfma_f32_16x16x32_bf16 v[196:199], v[164:167], a[220:223], 0\n"
        "s_waitcnt lgkmcnt(3)\n"
        "ds_read_b128 a[232:235], v254 offset:0\n"
        "v_add_f32 v117, v116, v117\n"
        "v_mfma_f32_16x16x32_bf16 v[196:199], v[152:155], a[216:219], v[196:199]\n"
        "ds_read_b128 a[220:223], v254 offset:3072\n"
        "v_mfma_f32_16x16x32_bf16 v[200:203], v[152:155], a[224:227], 0\n"
        "ds_read_b128 a[216:219], v254 offset:32\n"
        "v_mfma_f32_16x16x32_bf16 v[200:203], v[164:167], a[228:231], v[200:203]\n"
        "v_rcp_f32 v116, v117\n"
        "v_mfma_f32_16x16x32_bf16 v[180:183], v[160:163], a[228:231], 0\n"
        "ds_read_b128 a[228:231], v254 offset:3104\n"
        "v_mul_f32 v72, v116, v72\n"
        "v_mfma_f32_16x16x32_bf16 v[180:183], v[156:159], a[224:227], v[180:183]\n"
        "ds_read_b128 a[236:239], v247 offset:8192\n"
        "v_mul_f32 v73, v116, v73\n"
        "v_mfma_f32_16x16x32_bf16 v[108:111], v[192:195], v[216:219], v[56:59]\n"
        "s_waitcnt lgkmcnt(5)\n"
        "ds_read_b128 a[224:227], v248 offset:16\n"
        "v_mul_f32 v74, v116, v74\n"
        "v_mfma_f32_16x16x32_bf16 v[108:111], v[208:211], v[220:223], v[108:111]\n"
        "ds_read_b128 a[240:243], v247 offset:12288\n"
        "v_mul_f32 v75, v116, v75\n"
        "v_mfma_f32_16x16x32_bf16 v[112:115], v[212:215], v[216:219], v[60:63]\n"
        "ds_read_b128 a[244:247], v247 offset:12544\n"
        "v_cvt_pk_bf16_f32 v72, v72, v73\n"
        "v_mfma_f32_16x16x32_bf16 v[112:115], v[228:231], v[224:227], v[112:115]\n"
        "s_waitcnt lgkmcnt(7)\n"
        "ds_read_b128 a[248:251], v247 offset:8448\n"
        "v_cvt_pk_bf16_f32 v73, v74, v75\n"
        "v_mfma_f32_16x16x32_bf16 v[184:187], v[156:159], a[232:235], 0\n"
        "s_waitcnt lgkmcnt(7)\n"
        "v_exp_f32 v100, v100\n"
        "v_mfma_f32_16x16x32_bf16 v[184:187], v[160:163], a[220:223], v[184:187]\n"
        "v_exp_f32 v101, v101\n"
        "v_mfma_f32_16x16x32_bf16 v[204:207], v[164:167], a[220:223], 0\n"
        "ds_read_b128 a[220:223], v248 offset:3088\n"
        "v_mul_f32 v76, v116, v76\n"
        "v_mfma_f32_16x16x32_bf16 v[204:207], v[152:155], a[232:235], v[204:207]\n"
        "s_waitcnt lgkmcnt(7)\n"
        "ds_read_b128 a[232:235], v254 offset:16\n"
        "v_mul_f32 v77, v116, v77\n"
        "v_mfma_f32_16x16x32_bf16 v[192:195], v[152:155], a[216:219], 0\n"
        "s_waitcnt lgkmcnt(7)\n"
        "v_mul_f32 v78, v116, v78\n"
        "v_mul_f32 v79, v116, v79\n"
        "v_mfma_f32_16x16x32_bf16 v[192:195], v[164:167], a[228:231], v[192:195]\n"
        "v_exp_f32 v102, v102\n"
        "v_mfma_f32_16x16x32_bf16 v[172:175], v[160:163], a[228:231], 0\n"
        "ds_read_b128 a[228:231], v254 offset:3088\n"
        "v_cvt_pk_bf16_f32 v88, v100, v101\n"
        "v_mfma_f32_16x16x32_bf16 v[172:175], v[156:159], a[216:219], v[172:175]\n"
        "s_waitcnt lgkmcnt(6)\n"
        "ds_read_b128 a[216:219], v248 offset:6144\n"
        "v_cvt_pk_bf16_f32 v74, v76, v77\n"
        "v_mfma_f32_16x16x32_bf16 v[212:215], v[156:159], a[224:227], 0\n"
        "s_waitcnt lgkmcnt(3)\n"
        "v_exp_f32 v104, v104\n"
        "v_mfma_f32_16x16x32_bf16 v[212:215], v[160:163], a[220:223], v[212:215]\n"
        "v_exp_f32 v103, v103\n"
        "v_mfma_f32_16x16x32_bf16 v[216:219], v[164:167], a[220:223], 0\n"
        "ds_read_b128 a[220:223], v248 offset:9216\n"
        "v_cvt_pk_bf16_f32 v75, v78, v79\n"
        "v_mfma_f32_16x16x32_bf16 v[216:219], v[152:155], a[224:227], v[216:219]\n"
        "s_waitcnt lgkmcnt(3)\n"
        "ds_write_b128 v245, v[72:75] offset:512\n"
        "v_mfma_f32_16x16x32_bf16 v[224:227], v[152:155], a[232:235], 0\n"
        "s_waitcnt lgkmcnt(3)\n"
        "v_exp_f32 v105, v105\n"
        "v_mfma_f32_16x16x32_bf16 v[224:227], v[164:167], a[228:231], v[224:227]\n"
        "v_cvt_pk_bf16_f32 v89, v102, v103\n"
        "v_cvt_pk_bf16_f32 v90, v104, v105\n"
        "v_mfma_f32_16x16x32_bf16 v[220:223], v[160:163], a[228:231], 0\n"
        "ds_read_b128 a[224:227], v248 offset:6176\n"
        "ds_read_b128 a[228:231], v248 offset:9248\n"
        "v_mfma_f32_16x16x32_bf16 v[220:223], v[156:159], a[232:235], v[220:223]\n"
        "s_waitcnt lgkmcnt(4)\n"
        "ds_read_b128 a[232:235], v254 offset:6144\n"
        "v_mul_f32 v96, v116, v96\n"
        "v_mfma_f32_16x16x32_bf16 v[176:179], a[236:239], a[216:219], v[176:179]\n"
        "s_waitcnt lgkmcnt(4)\n"
        "v_exp_f32 v106, v106\n"
        "v_mfma_f32_16x16x32_bf16 v[176:179], a[240:243], a[220:223], v[176:179]\n"
        "v_mul_f32 v97, v116, v97\n"
        "v_mul_f32 v98, v116, v98\n"
        "v_mfma_f32_16x16x32_bf16 v[196:199], a[244:247], a[220:223], v[196:199]\n"
        "ds_read_b128 a[220:223], v254 offset:9216\n"
        "v_mul_f32 v99, v116, v99\n"
        "v_mfma_f32_16x16x32_bf16 v[196:199], a[248:251], a[216:219], v[196:199]\n"
        "s_waitcnt lgkmcnt(3)\n"
        "ds_read_b128 a[216:219], v254 offset:6176\n"
        "v_cvt_pk_bf16_f32 v96, v96, v97\n"
        "v_mfma_f32_16x16x32_bf16 v[200:203], a[248:251], a[224:227], v[200:203]\n"
        "s_waitcnt lgkmcnt(3)\n"
        "ds_read_b128 v[156:159], v247 offset:16384\n"
        "v_cvt_pk_bf16_f32 v97, v98, v99\n"
        "v_mfma_f32_16x16x32_bf16 v[200:203], a[244:247], a[228:231], v[200:203]\n"
        "ds_write_b64 v246, v[96:97] offset:512\n"
        "v_mfma_f32_16x16x32_bf16 v[180:183], a[240:243], a[228:231], v[180:183]\n"
        "ds_read_b128 a[228:231], v254 offset:9248\n"
        "v_max3_u32 v236, v236, v116, v117\n"
        "v_mfma_f32_16x16x32_bf16 v[180:183], a[236:239], a[224:227], v[180:183]\n"
        "s_waitcnt lgkmcnt(5)\n"
        "ds_read_b128 a[224:227], v248 offset:6160\n"
        "v_mfma_f32_16x16x32_bf16 v[184:187], a[236:239], a[232:235], v[184:187]\n"
        "s_waitcnt lgkmcnt(5)\n"
        "ds_read_b128 v[160:163], v247 offset:20480\n"
        "v_mfma_f32_16x16x32_bf16 v[184:187], a[240:243], a[220:223], v[184:187]\n"
        "ds_read_b128 v[164:167], v247 offset:20736\n"
        "v_mfma_f32_16x16x32_bf16 v[204:207], a[244:247], a[220:223], v[204:207]\n"
        "ds_read_b128 a[220:223], v248 offset:9232\n"
        "v_mfma_f32_16x16x32_bf16 v[204:207], a[248:251], a[232:235], v[204:207]\n"
        "s_waitcnt lgkmcnt(7)\n"
        "ds_read_b128 a[232:235], v254 offset:6160\n"
        "v_mfma_f32_16x16x32_bf16 v[192:195], a[248:251], a[216:219], v[192:195]\n"
        "s_waitcnt lgkmcnt(5)\n"
        "ds_read_b128 v[152:155], v247 offset:16640\n"
        "v_mfma_f32_16x16x32_bf16 v[192:195], a[244:247], a[228:231], v[192:195]\n"
        "v_exp_f32 v107, v107\n"
        "v_mfma_f32_16x16x32_bf16 v[172:175], a[240:243], a[228:231], v[172:175]\n"
        "ds_read_b128 a[228:231], v254 offset:9232\n"
        "v_cvt_pk_bf16_f32 v91, v106, v107\n"
        "v_mfma_f32_16x16x32_bf16 v[172:175], a[236:239], a[216:219], v[172:175]\n"
        "s_waitcnt lgkmcnt(6)\n"
        "ds_read_b128 a[216:219], v248 offset:12288\n"
        "v_mfma_f32_16x16x32_bf16 v[212:215], a[236:239], a[224:227], v[212:215]\n"
        "s_waitcnt lgkmcnt(4)\n"
        "v_exp_f32 v108, v108\n"
        "v_mfma_f32_16x16x32_bf16 v[212:215], a[240:243], a[220:223], v[212:215]\n"
        "v_exp_f32 v109, v109\n"
        "v_mfma_f32_16x16x32_bf16 v[216:219], a[244:247], a[220:223], v[216:219]\n"
        "ds_read_b128 a[220:223], v248 offset:15360\n"
        "v_cvt_pk_bf16_f32 v92, v108, v109\n"
        "v_mfma_f32_16x16x32_bf16 v[216:219], a[248:251], a[224:227], v[216:219]\n"
        "s_waitcnt lgkmcnt(4)\n"
        "ds_read_b128 a[224:227], v248 offset:12320\n"
        "v_mfma_f32_16x16x32_bf16 v[224:227], a[248:251], a[232:235], v[224:227]\n"
        "s_waitcnt lgkmcnt(3)\n"
        "v_exp_f32 v110, v110\n"
        "v_mfma_f32_16x16x32_bf16 v[224:227], a[244:247], a[228:231], v[224:227]\n"
        "v_exp_f32 v112, v112\n"
        "v_mfma_f32_16x16x32_bf16 v[220:223], a[240:243], a[228:231], v[220:223]\n"
        "ds_read_b128 a[228:231], v248 offset:15392\n"
        "v_mfma_f32_16x16x32_bf16 v[220:223], a[236:239], a[232:235], v[220:223]\n"
        "s_waitcnt lgkmcnt(3)\n"
        "ds_read_b128 a[232:235], v254 offset:12288\n"
        "v_mfma_f32_16x16x32_bf16 v[176:179], v[156:159], a[216:219], v[176:179]\n"
        "s_waitcnt lgkmcnt(3)\n"
        "v_exp_f32 v111, v111\n"
        "v_mfma_f32_16x16x32_bf16 v[176:179], v[160:163], a[220:223], v[176:179]\n"
        "v_exp_f32 v113, v113\n"
        "v_mfma_f32_16x16x32_bf16 v[196:199], v[164:167], a[220:223], v[196:199]\n"
        "ds_read_b128 a[220:223], v254 offset:15360\n"
        "v_cvt_pk_bf16_f32 v93, v110, v111\n"
        "v_mfma_f32_16x16x32_bf16 v[196:199], v[152:155], a[216:219], v[196:199]\n"
        "s_waitcnt lgkmcnt(3)\n"
        "ds_read_b128 a[216:219], v254 offset:12320\n"
        "v_cvt_pk_bf16_f32 v94, v112, v113\n"
        "v_mfma_f32_16x16x32_bf16 v[200:203], v[152:155], a[224:227], v[200:203]\n"
        "s_waitcnt lgkmcnt(3)\n"
        "v_exp_f32 v114, v114\n"
        "v_mfma_f32_16x16x32_bf16 v[200:203], v[164:167], a[228:231], v[200:203]\n"
        "v_exp_f32 v115, v115\n"
        "v_mfma_f32_16x16x32_bf16 v[180:183], v[160:163], a[228:231], v[180:183]\n"
        "s_waitcnt vmcnt(0)\n"
        "s_barrier\n"
        "ds_read_b128 a[228:231], v254 offset:15392\n"
        "v_cvt_pk_bf16_f32 v95, v114, v115\n"
        "v_mfma_f32_16x16x32_bf16 v[180:183], v[156:159], a[224:227], v[180:183]\n"
        "s_waitcnt lgkmcnt(3)\n"
        "ds_read_b128 a[224:227], v248 offset:12304\n"
        "v_mfma_f32_16x16x32_bf16 v[184:187], v[156:159], a[232:235], v[184:187]\n"
        "s_waitcnt lgkmcnt(3)\n"
        "v_mfma_f32_16x16x32_bf16 v[184:187], v[160:163], a[220:223], v[184:187]\n"
        "v_mfma_f32_16x16x32_bf16 v[204:207], v[164:167], a[220:223], v[204:207]\n"
        "ds_read_b128 a[220:223], v248 offset:15376\n"
        "v_mfma_f32_16x16x32_bf16 v[204:207], v[152:155], a[232:235], v[204:207]\n"
        "s_waitcnt lgkmcnt(3)\n"
        "ds_read_b128 a[232:235], v254 offset:12304\n"
        "v_mfma_f32_16x16x32_bf16 v[192:195], v[152:155], a[216:219], v[192:195]\n"
        "s_waitcnt lgkmcnt(3)\n"
        "ds_read_b128 a[236:239], v240 offset:0\n"
        "v_mfma_f32_16x16x32_bf16 v[192:195], v[164:167], a[228:231], v[192:195]\n"
        "ds_read_b128 a[240:243], v241 offset:0\n"
        "v_mfma_f32_16x16x32_bf16 v[172:175], v[160:163], a[228:231], v[172:175]\n"
        "ds_read_b128 a[228:231], v254 offset:15376\n"
        "v_mfma_f32_16x16x32_bf16 v[172:175], v[156:159], a[216:219], v[172:175]\n"
        "s_waitcnt lgkmcnt(5)\n"
        "ds_read_b128 a[244:247], v240 offset:128\n"
        "v_mfma_f32_16x16x32_bf16 v[212:215], v[156:159], a[224:227], v[212:215]\n"
        "s_waitcnt lgkmcnt(5)\n"
        "ds_read_b128 a[248:251], v241 offset:128\n"
        "v_mfma_f32_16x16x32_bf16 v[212:215], v[160:163], a[220:223], v[212:215]\n"
        "ds_read_b128 a[216:219], v240 offset:256\n"
        "v_mfma_f32_16x16x32_bf16 v[216:219], v[164:167], a[220:223], v[216:219]\n"
        "ds_read_b128 a[220:223], v241 offset:256\n"
        "v_mfma_f32_16x16x32_bf16 v[216:219], v[152:155], a[224:227], v[216:219]\n"
        "s_waitcnt lgkmcnt(7)\n"
        "v_mfma_f32_16x16x32_bf16 v[224:227], v[152:155], a[232:235], v[224:227]\n"
        "s_waitcnt lgkmcnt(4)\n"
        "v_mfma_f32_16x16x32_bf16 v[224:227], v[164:167], a[228:231], v[224:227]\n"
        "v_mfma_f32_16x16x32_bf16 v[220:223], v[160:163], a[228:231], v[220:223]\n"
        "v_mfma_f32_16x16x32_bf16 v[220:223], v[156:159], a[232:235], v[220:223]\n"
        "s_branch 10b\n"
        ".p2align 12\n"
        "51:\n"
        "s_branch 61b\n"
        "s_nop 0\n"
        "s_nop 0\n"
        "s_nop 0\n"
        "12:\n"
        "10:\n"
        "v_mfma_f32_16x16x32_bf16 v[112:115], a[72:75], a[236:239], 0\n"
        "v_mov_b64 v[124:125], 0\n"
        "v_cvt_pk_bf16_f32 v232, v176, v180\n"
        "v_mfma_f32_16x16x32_bf16 v[112:115], a[76:79], a[240:243], v[112:115]\n"
        "s_waitcnt lgkmcnt(3)\n"
        "s_add_u32 s41, s30, s40\n"
        "v_cvt_pk_bf16_f32 v233, v184, v172\n"
        "v_cvt_pk_bf16_f32 v234, v177, v181\n"
        "v_mfma_f32_16x16x32_bf16 v[112:115], a[80:83], a[244:247], v[112:115]\n"
        "s_waitcnt lgkmcnt(2)\n"
        "buffer_store_dwordx2 v[232:233], v250, s[16:19], s38 offen offset:0 sc1\n"
        "v_cvt_pk_bf16_f32 v235, v185, v173\n"
        "v_mfma_f32_16x16x32_bf16 v[112:115], a[84:87], a[248:251], v[112:115]\n"
        "s_waitcnt lgkmcnt(1)\n"
        "v_cvt_pk_bf16_f32 v232, v178, v182\n"
        "v_cvt_pk_bf16_f32 v233, v186, v174\n"
        "v_mfma_f32_16x16x32_bf16 v[112:115], a[88:91], a[216:219], v[112:115]\n"
        "s_waitcnt lgkmcnt(0)\n"
        "v_mfma_f32_16x16x32_bf16 v[112:115], a[92:95], a[220:223], v[112:115]\n"
        "s_nop 2\n"
        "v_smfmac_f32_16x16x64_bf16 v[124:127], a[252:255], v[88:95], v237\n"
        "s_nop 1\n"
        "v_mfma_f32_16x16x32_bf16 v[80:83], v[132:135], v[92:95], 0\n"
        "buffer_store_dwordx2 v[234:235], v250, s[16:19], s38 offen offset:384 sc1\n"
        "v_cvt_pk_bf16_f32 v152, v112, v113\n"
        "v_mfma_f32_16x16x32_bf16 v[80:83], v[128:131], v[88:91], v[80:83]\n"
        "v_cvt_pk_bf16_f32 v153, v114, v115\n"
        "v_cvt_pk_bf16_f32 v234, v179, v183\n"
        "v_mfma_f32_16x16x32_bf16 v[84:87], v[136:139], v[88:91], 0\n"
        "v_cvt_pk_bf16_f32 v235, v187, v175\n"
        "v_add_f32 v125, v124, v125\n"
        "v_mfma_f32_16x16x32_bf16 v[84:87], v[140:143], v[92:95], v[84:87]\n"
        "v_rcp_f32 v124, v125\n"
        "v_mfma_f32_16x16x32_bf16 v[96:99], v[148:151], v[92:95], 0\n"
        "buffer_store_dwordx2 v[232:233], v250, s[16:19], s38 offen offset:768 sc1\n"
        "v_mul_f32 v80, v124, v80\n"
        "v_mfma_f32_16x16x32_bf16 v[96:99], v[144:147], v[88:91], v[96:99]\n"
        "s_add_u32 s35, s47, 786432\n"
        "v_mul_f32 v81, v124, v81\n"
        "v_mul_f32 v82, v124, v82\n"
        "v_mfma_f32_16x16x32_bf16 v[116:119], a[116:119], a[220:223], 0\n"
        "s_sub_u32 s46, s35, 100663296\n"
        "v_mul_f32 v83, v124, v83\n"
        "v_cvt_pk_bf16_f32 v80, v80, v81\n"
        "v_mfma_f32_16x16x32_bf16 v[116:119], a[112:115], a[216:219], v[116:119]\n"
        "s_cmp_lt_u32 s46, 786432\n"
        "v_cvt_pk_bf16_f32 v81, v82, v83\n"
        "v_cvt_pk_bf16_f32 v232, v196, v200\n"
        "v_mfma_f32_16x16x32_bf16 v[116:119], a[108:111], a[248:251], v[116:119]\n"
        "buffer_store_dwordx2 v[234:235], v250, s[16:19], s38 offen offset:1152 sc1\n"
        "v_cvt_pk_bf16_f32 v233, v204, v192\n"
        "v_mfma_f32_16x16x32_bf16 v[116:119], a[104:107], a[244:247], v[116:119]\n"
        "s_cselect_b32 s35, s46, s35\n"
        "v_mul_f32 v84, v124, v84\n"
        "v_mul_f32 v85, v124, v85\n"
        "v_mfma_f32_16x16x32_bf16 v[116:119], a[100:103], a[240:243], v[116:119]\n"
        "v_mul_f32 v86, v124, v86\n"
        "v_mul_f32 v87, v124, v87\n"
        "v_mfma_f32_16x16x32_bf16 v[116:119], a[96:99], a[236:239], v[116:119]\n"
        "v_cvt_pk_bf16_f32 v82, v84, v85\n"
        "v_cvt_pk_bf16_f32 v83, v86, v87\n"
        "v_mfma_f32_16x16x32_bf16 v[120:123], a[120:123], a[236:239], 0\n"
        "ds_write_b128 v245, v[80:83] offset:768\n"
        "v_mfma_f32_16x16x32_bf16 v[120:123], a[124:127], a[240:243], v[120:123]\n"
        "buffer_store_dwordx2 v[232:233], v250, s[16:19], s39 offen offset:0 sc1\n"
        "v_mul_f32 v96, v124, v96\n"
        "v_mfma_f32_16x16x32_bf16 v[120:123], a[128:131], a[244:247], v[120:123]\n"
        "v_mul_f32 v97, v124, v97\n"
        "v_mul_f32 v98, v124, v98\n"
        "v_mfma_f32_16x16x32_bf16 v[120:123], a[132:135], a[248:251], v[120:123]\n"
        "v_mul_f32 v99, v124, v99\n"
        "v_cvt_pk_bf16_f32 v234, v197, v201\n"
        "v_mfma_f32_16x16x32_bf16 v[120:123], a[136:139], a[216:219], v[120:123]\n"
        "v_cvt_pk_bf16_f32 v235, v205, v193\n"
        "v_cvt_pk_bf16_f32 v154, v116, v117\n"
        "v_mfma_f32_16x16x32_bf16 v[120:123], a[140:143], a[220:223], v[120:123]\n"
        "buffer_store_dwordx2 v[234:235], v250, s[16:19], s39 offen offset:384 sc1\n"
        "v_cvt_pk_bf16_f32 v155, v118, v119\n"
        "v_mfma_f32_16x16x32_bf16 v[112:115], a[20:23], a[220:223], 0\n"
        "v_cvt_pk_bf16_f32 v96, v96, v97\n"
        "v_cvt_pk_bf16_f32 v97, v98, v99\n"
        "v_mfma_f32_16x16x32_bf16 v[112:115], a[16:19], a[216:219], v[112:115]\n"
        "ds_write_b64 v246, v[96:97] offset:768\n"
        "v_mfma_f32_16x16x32_bf16 v[112:115], a[12:15], a[248:251], v[112:115]\n"
        "v_max3_u32 v236, v236, v124, v125\n"
        "v_cvt_pk_bf16_f32 v232, v198, v202\n"
        "v_mfma_f32_16x16x32_bf16 v[112:115], a[8:11], a[244:247], v[112:115]\n"
        "v_cvt_pk_bf16_f32 v233, v206, v194\n"
        "v_cmp_lt_u32 vcc, s24, v236\n"
        "v_mfma_f32_16x16x32_bf16 v[112:115], a[4:7], a[240:243], v[112:115]\n"
        "buffer_store_dwordx2 v[232:233], v250, s[16:19], s39 offen offset:768 sc1\n"
        "v_cvt_pk_bf16_f32 v168, v120, v121\n"
        "v_mfma_f32_16x16x32_bf16 v[112:115], a[0:3], a[236:239], v[112:115]\n"
        "s_cmp_lg_u64 vcc, 0\n"
        "s_addc_u32 s25, s25, s25\n"
        "v_cvt_pk_bf16_f32 v169, v122, v123\n"
        "v_mfma_f32_16x16x32_bf16 v[116:119], a[24:27], a[236:239], 0\n"
        "v_cvt_pk_bf16_f32 v234, v199, v203\n"
        "v_cvt_pk_bf16_f32 v235, v207, v195\n"
        "v_mfma_f32_16x16x32_bf16 v[116:119], a[28:31], a[240:243], v[116:119]\n"
        "v_cvt_pk_bf16_f32 v232, v212, v220\n"
        "v_cvt_pk_bf16_f32 v156, v112, v113\n"
        "v_mfma_f32_16x16x32_bf16 v[116:119], a[32:35], a[244:247], v[116:119]\n"
        "buffer_store_dwordx2 v[234:235], v250, s[16:19], s39 offen offset:1152 sc1\n"
        "v_cvt_pk_bf16_f32 v157, v114, v115\n"
        "v_mfma_f32_16x16x32_bf16 v[116:119], a[36:39], a[248:251], v[116:119]\n"
        "v_cvt_pk_bf16_f32 v234, v213, v221\n"
        "v_xor_b32 v251, 0x2000, v251\n"
        "v_mfma_f32_16x16x32_bf16 v[116:119], a[40:43], a[216:219], v[116:119]\n"
        "v_xor_b32 v252, 0x2000, v252\n"
        "v_mfma_f32_16x16x32_bf16 v[116:119], a[44:47], a[220:223], v[116:119]\n"
        "s_nop 2\n"
        "v_mfma_f32_16x16x32_bf16 v[120:123], a[68:71], a[220:223], 0\n"
        "s_nop 2\n"
        "v_mfma_f32_16x16x32_bf16 v[120:123], a[64:67], a[216:219], v[120:123]\n"
        "buffer_store_dword v232, v238, s[16:19], s38 offen offset:0 sc1\n"
        "v_mfma_f32_16x16x32_bf16 v[120:123], a[60:63], a[248:251], v[120:123]\n"
        "ds_read_b128 a[224:227], v240 offset:1536\n"
        "v_cvt_pk_bf16_f32 v158, v116, v117\n"
        "v_mfma_f32_16x16x32_bf16 v[120:123], a[56:59], a[244:247], v[120:123]\n"
        "ds_read_b128 a[228:231], v241 offset:1536\n"
        "v_cvt_pk_bf16_f32 v159, v118, v119\n"
        "v_mfma_f32_16x16x32_bf16 v[120:123], a[52:55], a[240:243], v[120:123]\n"
        "ds_read_b128 a[232:235], v240 offset:1664\n"
        "v_cvt_pk_bf16_f32 v232, v214, v222\n"
        "v_mfma_f32_16x16x32_bf16 v[120:123], a[48:51], a[236:239], v[120:123]\n"
        "buffer_store_dword v234, v238, s[16:19], s38 offen offset:384 sc1\n"
        "v_mfma_f32_16x16x32_bf16 v[112:115], a[236:239], a[144:147], 0\n"
        "v_add_u32 v247, s45, v247\n"
        "v_cvt_pk_bf16_f32 v234, v215, v223\n"
        "v_mfma_f32_16x16x32_bf16 v[112:115], a[240:243], a[148:151], v[112:115]\n"
        "s_nop 2\n"
        "v_mfma_f32_16x16x32_bf16 v[112:115], a[244:247], a[152:155], v[112:115]\n"
        "v_cvt_pk_bf16_f32 v160, v120, v121\n"
        "v_cvt_pk_bf16_f32 v161, v122, v123\n"
        "v_mfma_f32_16x16x32_bf16 v[112:115], a[248:251], a[156:159], v[112:115]\n"
        "v_cvt_pk_bf16_f32 v166, v120, v121\n"
        "v_cvt_pk_bf16_f32 v167, v122, v123\n"
        "v_mfma_f32_16x16x32_bf16 v[112:115], a[216:219], a[160:163], v[112:115]\n"
        "buffer_store_dword v232, v238, s[16:19], s38 offen offset:768 sc1\n"
        "v_mfma_f32_16x16x32_bf16 v[112:115], a[220:223], a[164:167], v[112:115]\n"
        "s_nop 2\n"
        "v_mfma_f32_16x16x32_bf16 v[116:119], a[220:223], a[188:191], 0\n"
        "v_cvt_pk_bf16_f32 v232, v216, v224\n"
        "s_nop 0\n"
        "v_mfma_f32_16x16x32_bf16 v[116:119], a[216:219], a[184:187], v[116:119]\n"
        "s_nop 0\n"
        "v_cvt_pk_bf16_f32 v128, v112, v113\n"
        "v_cvt_pk_bf16_f32 v129, v114, v115\n"
        "v_mfma_f32_16x16x32_bf16 v[116:119], a[248:251], a[180:183], v[116:119]\n"
        "buffer_store_dword v234, v238, s[16:19], s38 offen offset:1152 sc1\n"
        "v_mfma_f32_16x16x32_bf16 v[116:119], a[244:247], a[176:179], v[116:119]\n"
        "s_nop 0\n"
        "v_cvt_pk_bf16_f32 v234, v217, v225\n"
        "v_mfma_f32_16x16x32_bf16 v[116:119], a[240:243], a[172:175], v[116:119]\n"
        "v_mfma_f32_16x16x32_bf16 v[116:119], a[236:239], a[168:171], v[116:119]\n"
        "s_nop 2\n"
        "v_mfma_f32_16x16x32_bf16 v[120:123], a[236:239], a[192:195], 0\n"
        "ds_read_b128 a[236:239], v241 offset:1664\n"
        "s_nop 0\n"
        "v_mfma_f32_16x16x32_bf16 v[120:123], a[240:243], a[196:199], v[120:123]\n"
        "buffer_store_dword v232, v238, s[16:19], s39 offen offset:0 sc1\n"
        "v_mfma_f32_16x16x32_bf16 v[120:123], a[244:247], a[200:203], v[120:123]\n"
        "ds_read_b128 a[240:243], v240 offset:1792\n"
        "ds_read_b128 a[244:247], v241 offset:1792\n"
        "v_mfma_f32_16x16x32_bf16 v[120:123], a[248:251], a[204:207], v[120:123]\n"
        "v_cvt_pk_bf16_f32 v136, v116, v117\n"
        "v_cvt_pk_bf16_f32 v137, v118, v119\n"
        "v_mfma_f32_16x16x32_bf16 v[120:123], a[216:219], a[208:211], v[120:123]\n"
        "v_cvt_pk_bf16_f32 v232, v218, v226\n"
        "v_mfma_f32_16x16x32_bf16 v[120:123], a[220:223], a[212:215], v[120:123]\n"
        "s_waitcnt lgkmcnt(5)\n"
        "buffer_store_dword v234, v238, s[16:19], s39 offen offset:384 sc1\n"
        "v_mfma_f32_16x16x32_bf16 v[112:115], a[72:75], a[224:227], 0\n"
        "s_waitcnt lgkmcnt(4)\n"
        "s_nop 1\n"
        "v_mfma_f32_16x16x32_bf16 v[112:115], a[76:79], a[228:231], v[112:115]\n"
        "s_waitcnt lgkmcnt(3)\n"
        "v_cvt_pk_bf16_f32 v234, v219, v227\n"
        "s_nop 0\n"
        "v_mfma_f32_16x16x32_bf16 v[112:115], a[80:83], a[232:235], v[112:115]\n"
        "s_waitcnt lgkmcnt(2)\n"
        "v_cvt_pk_bf16_f32 v144, v120, v121\n"
        "v_cvt_pk_bf16_f32 v145, v122, v123\n"
        "v_mfma_f32_16x16x32_bf16 v[112:115], a[84:87], a[236:239], v[112:115]\n"
        "s_waitcnt lgkmcnt(1)\n"
        "v_mfma_f32_16x16x32_bf16 v[112:115], a[88:91], a[240:243], v[112:115]\n"
        "s_waitcnt lgkmcnt(0)\n"
        "buffer_store_dword v232, v238, s[16:19], s39 offen offset:768 sc1\n"
        "v_mfma_f32_16x16x32_bf16 v[112:115], a[92:95], a[244:247], v[112:115]\n"
        "s_nop 2\n"
        "v_mfma_f32_16x16x32_bf16 v[116:119], a[116:119], a[244:247], 0\n"
        "s_nop 2\n"
        "v_mfma_f32_16x16x32_bf16 v[116:119], a[112:115], a[240:243], v[116:119]\n"
        "v_cvt_pk_bf16_f32 v172, v112, v113\n"
        "v_cvt_pk_bf16_f32 v173, v114, v115\n"
        "v_mfma_f32_16x16x32_bf16 v[116:119], a[108:111], a[236:239], v[116:119]\n"
        "buffer_store_dword v234, v238, s[16:19], s39 offen offset:1152 sc1\n"
        "v_mfma_f32_16x16x32_bf16 v[116:119], a[104:107], a[232:235], v[116:119]\n"
        "v_mfma_f32_16x16x32_bf16 v[116:119], a[100:103], a[228:231], v[116:119]\n"
        "v_mfma_f32_16x16x32_bf16 v[116:119], a[96:99], a[224:227], v[116:119]\n"
        "s_mov_b32 m0, s48\n"
        "s_nop 1\n"
        "v_mfma_f32_16x16x32_bf16 v[120:123], a[120:123], a[224:227], 0\n"
        "ds_read_b128 v[162:165], v251 offset:0\n"
        "s_nop 0\n"
        "v_mfma_f32_16x16x32_bf16 v[120:123], a[124:127], a[228:231], v[120:123]\n"
        "buffer_load_dwordx4 v249, s[12:15], s36 offen offset:0 lds\n"
        "v_mfma_f32_16x16x32_bf16 v[120:123], a[128:131], a[232:235], v[120:123]\n"
        "v_cvt_pk_bf16_f32 v174, v116, v117\n"
        "v_cvt_pk_bf16_f32 v175, v118, v119\n"
        "v_mfma_f32_16x16x32_bf16 v[120:123], a[132:135], a[236:239], v[120:123]\n"
        "v_mfma_f32_16x16x32_bf16 v[120:123], a[136:139], a[240:243], v[120:123]\n"
        "v_mfma_f32_16x16x32_bf16 v[120:123], a[140:143], a[244:247], v[120:123]\n"
        "s_nop 2\n"
        "v_mfma_f32_16x16x32_bf16 v[112:115], a[20:23], a[244:247], 0\n"
        "s_nop 2\n"
        "v_mfma_f32_16x16x32_bf16 v[112:115], a[16:19], a[240:243], v[112:115]\n"
        "v_cvt_pk_bf16_f32 v190, v120, v121\n"
        "v_cvt_pk_bf16_f32 v191, v122, v123\n"
        "v_mfma_f32_16x16x32_bf16 v[112:115], a[12:15], a[236:239], v[112:115]\n"
        "v_mfma_f32_16x16x32_bf16 v[112:115], a[8:11], a[232:235], v[112:115]\n"
        "v_mfma_f32_16x16x32_bf16 v[112:115], a[4:7], a[228:231], v[112:115]\n"
        "ds_read_b128 v[182:185], v251 offset:512\n"
        "v_mfma_f32_16x16x32_bf16 v[112:115], a[0:3], a[224:227], v[112:115]\n"
        "s_nop 2\n"
        "v_mfma_f32_16x16x32_bf16 v[116:119], a[24:27], a[224:227], 0\n"
        "s_nop 2\n"
        "v_mfma_f32_16x16x32_bf16 v[116:119], a[28:31], a[228:231], v[116:119]\n"
        "v_cvt_pk_bf16_f32 v176, v112, v113\n"
        "v_cvt_pk_bf16_f32 v177, v114, v115\n"
        "v_mfma_f32_16x16x32_bf16 v[116:119], a[32:35], a[232:235], v[116:119]\n"
        "v_mfma_f32_16x16x32_bf16 v[116:119], a[36:39], a[236:239], v[116:119]\n"
        "buffer_load_dwordx4 v253, s[12:15], s36 offen offset:1024 lds\n"
        "v_mfma_f32_16x16x32_bf16 v[116:119], a[40:43], a[240:243], v[116:119]\n"
        "v_mfma_f32_16x16x32_bf16 v[116:119], a[44:47], a[244:247], v[116:119]\n"
        "s_nop 2\n"
        "v_mfma_f32_16x16x32_bf16 v[120:123], a[68:71], a[244:247], 0\n"
        "s_nop 2\n"
        "v_mfma_f32_16x16x32_bf16 v[120:123], a[64:67], a[240:243], v[120:123]\n"
        "v_cvt_pk_bf16_f32 v178, v116, v117\n"
        "v_cvt_pk_bf16_f32 v179, v118, v119\n"
        "v_mfma_f32_16x16x32_bf16 v[120:123], a[60:63], a[236:239], v[120:123]\n"
        "v_mfma_f32_16x16x32_bf16 v[120:123], a[56:59], a[232:235], v[120:123]\n"
        "v_mfma_f32_16x16x32_bf16 v[120:123], a[52:55], a[228:231], v[120:123]\n"
        "v_mfma_f32_16x16x32_bf16 v[120:123], a[48:51], a[224:227], v[120:123]\n"
        "s_nop 2\n"
        "v_mfma_f32_16x16x32_bf16 v[112:115], a[224:227], a[144:147], 0\n"
        "s_nop 2\n"
        "v_mfma_f32_16x16x32_bf16 v[112:115], a[228:231], a[148:151], v[112:115]\n"
        "v_cvt_pk_bf16_f32 v180, v120, v121\n"
        "v_cvt_pk_bf16_f32 v181, v122, v123\n"
        "v_mfma_f32_16x16x32_bf16 v[112:115], a[232:235], a[152:155], v[112:115]\n"
        "v_cvt_pk_bf16_f32 v186, v120, v121\n"
        "v_cvt_pk_bf16_f32 v187, v122, v123\n"
        "v_mfma_f32_16x16x32_bf16 v[112:115], a[236:239], a[156:159], v[112:115]\n"
        "s_add_u32 m0, s41, 0\n"
        "v_mfma_f32_16x16x32_bf16 v[112:115], a[240:243], a[160:163], v[112:115]\n"
        "ds_read_b128 a[248:251], v240 offset:12288\n"
        "v_mfma_f32_16x16x32_bf16 v[112:115], a[244:247], a[164:167], v[112:115]\n"
        "buffer_load_dwordx4 v242, s[8:11], s47 offen lds\n"
        "v_mfma_f32_16x16x32_bf16 v[96:99], v[152:155], v[156:159], v[0:3]\n"
        "s_waitcnt lgkmcnt(2)\n"
        "ds_read_b128 a[216:219], v241 offset:12288\n"
        "s_nop 0\n"
        "v_mfma_f32_16x16x32_bf16 v[96:99], v[168:171], v[160:163], v[96:99]\n"
        "ds_read_b128 a[220:223], v240 offset:12416\n"
        "s_sub_u32 s31, s30, s28\n"
        "s_nop 0\n"
        "v_mfma_f32_16x16x32_bf16 v[100:103], v[172:175], v[156:159], v[4:7]\n"
        "s_sub_u32 s45, s28, s29\n"
        "v_cvt_pk_bf16_f32 v130, v112, v113\n"
        "v_cvt_pk_bf16_f32 v131, v114, v115\n"
        "v_mfma_f32_16x16x32_bf16 v[100:103], v[188:191], v[164:167], v[100:103]\n"
        "s_add_u32 s28, s28, s31\n"
        "ds_read_b128 v[162:165], v252 offset:0\n"
        "v_mfma_f32_16x16x32_bf16 v[116:119], a[244:247], a[188:191], 0\n"
        "s_mov_b32 s30, s29\n"
        "v_exp_f32 v96, v96\n"
        "v_mfma_f32_16x16x32_bf16 v[116:119], a[240:243], a[184:187], v[116:119]\n"
        "s_add_u32 s29, s29, s45\n"
        "v_exp_f32 v97, v97\n"
        "v_mfma_f32_16x16x32_bf16 v[116:119], a[236:239], a[180:183], v[116:119]\n"
        "v_exp_f32 v98, v98\n"
        "v_mfma_f32_16x16x32_bf16 v[116:119], a[232:235], a[176:179], v[116:119]\n"
        "v_exp_f32 v100, v100\n"
        "v_mfma_f32_16x16x32_bf16 v[116:119], a[228:231], a[172:175], v[116:119]\n"
        "v_exp_f32 v99, v99\n"
        "v_mfma_f32_16x16x32_bf16 v[116:119], a[224:227], a[168:171], v[116:119]\n"
        "v_exp_f32 v101, v101\n"
        "v_mfma_f32_16x16x32_bf16 v[104:107], v[152:155], v[176:179], v[16:19]\n"
        "s_waitcnt lgkmcnt(4)\n"
        "v_exp_f32 v102, v102\n"
        "v_mfma_f32_16x16x32_bf16 v[104:107], v[168:171], v[180:183], v[104:107]\n"
        "s_add_u32 m0, s41, 1024\n"
        "v_cvt_pk_bf16_f32 v64, v96, v97\n"
        "v_cvt_pk_bf16_f32 v65, v98, v99\n"
        "v_mfma_f32_16x16x32_bf16 v[108:111], v[172:175], v[176:179], v[20:23]\n"
        "v_cvt_pk_bf16_f32 v138, v116, v117\n"
        "v_cvt_pk_bf16_f32 v139, v118, v119\n"
        "v_mfma_f32_16x16x32_bf16 v[108:111], v[188:191], v[184:187], v[108:111]\n"
        "buffer_load_dwordx4 v243, s[8:11], s47 offen lds\n"
        "v_mfma_f32_16x16x32_bf16 v[120:123], a[224:227], a[192:195], 0\n"
        "ds_read_b128 a[224:227], v241 offset:12416\n"
        "v_cvt_pk_bf16_f32 v66, v100, v101\n"
        "v_mfma_f32_16x16x32_bf16 v[120:123], a[228:231], a[196:199], v[120:123]\n"
        "ds_read_b128 a[228:231], v240 offset:12544\n"
        "v_mfma_f32_16x16x32_bf16 v[120:123], a[232:235], a[200:203], v[120:123]\n"
        "ds_read_b128 a[232:235], v241 offset:12544\n"
        "v_mfma_f32_16x16x32_bf16 v[120:123], a[236:239], a[204:207], v[120:123]\n"
        "ds_read_b128 v[202:205], v251 offset:4096\n"
        "v_mfma_f32_16x16x32_bf16 v[120:123], a[240:243], a[208:211], v[120:123]\n"
        "ds_read_b128 v[182:185], v252 offset:512\n"
        "v_mfma_f32_16x16x32_bf16 v[120:123], a[244:247], a[212:215], v[120:123]\n"
        "s_waitcnt lgkmcnt(8)\n"
        "v_exp_f32 v103, v103\n"
        "v_mfma_f32_16x16x32_bf16 v[112:115], a[0:3], a[248:251], 0\n"
        "s_waitcnt lgkmcnt(7)\n"
        "v_cvt_pk_bf16_f32 v67, v102, v103\n"
        "s_nop 0\n"
        "v_mfma_f32_16x16x32_bf16 v[112:115], a[4:7], a[216:219], v[112:115]\n"
        "s_waitcnt lgkmcnt(6)\n"
        "v_exp_f32 v104, v104\n"
        "v_mfma_f32_16x16x32_bf16 v[112:115], a[8:11], a[220:223], v[112:115]\n"
        "s_waitcnt lgkmcnt(4)\n"
        "v_exp_f32 v105, v105\n"
        "v_mfma_f32_16x16x32_bf16 v[112:115], a[12:15], a[224:227], v[112:115]\n"
        "s_waitcnt lgkmcnt(3)\n"
        "v_exp_f32 v106, v106\n"
        "v_mfma_f32_16x16x32_bf16 v[112:115], a[16:19], a[228:231], v[112:115]\n"
        "s_waitcnt lgkmcnt(2)\n"
        "v_cvt_pk_bf16_f32 v72, v104, v105\n"
        "v_cvt_pk_bf16_f32 v146, v120, v121\n"
        "v_mfma_f32_16x16x32_bf16 v[112:115], a[20:23], a[232:235], v[112:115]\n"
        "s_add_u32 m0, s41, 2048\n"
        "v_exp_f32 v108, v108\n"
        "v_mfma_f32_16x16x32_bf16 v[116:119], a[44:47], a[232:235], 0\n"
        "v_cvt_pk_bf16_f32 v147, v122, v123\n"
        "v_add_u32 v245, s45, v245\n"
        "v_mfma_f32_16x16x32_bf16 v[116:119], a[40:43], a[228:231], v[116:119]\n"
        "buffer_load_dwordx4 v244, s[8:11], s47 offen lds\n"
        "v_mfma_f32_16x16x32_bf16 v[116:119], a[36:39], a[224:227], v[116:119]\n"
        "v_exp_f32 v107, v107\n"
        "v_mfma_f32_16x16x32_bf16 v[116:119], a[32:35], a[220:223], v[116:119]\n"
        "v_exp_f32 v109, v109\n"
        "v_mfma_f32_16x16x32_bf16 v[116:119], a[28:31], a[216:219], v[116:119]\n"
        "v_cvt_pk_bf16_f32 v73, v106, v107\n"
        "v_cvt_pk_bf16_f32 v74, v108, v109\n"
        "v_mfma_f32_16x16x32_bf16 v[116:119], a[24:27], a[248:251], v[116:119]\n"
        "v_exp_f32 v110, v110\n"
        "v_mfma_f32_16x16x32_bf16 v[120:123], a[48:51], a[248:251], 0\n"
        "v_exp_f32 v111, v111\n"
        "v_mfma_f32_16x16x32_bf16 v[120:123], a[52:55], a[216:219], v[120:123]\n"
        "v_cvt_pk_bf16_f32 v75, v110, v111\n"
        "v_cvt_pk_bf16_f32 v196, v112, v113\n"
        "v_mfma_f32_16x16x32_bf16 v[120:123], a[56:59], a[220:223], v[120:123]\n"
        "v_cvt_pk_bf16_f32 v197, v114, v115\n"
        "v_cvt_pk_bf16_f32 v198, v116, v117\n"
        "v_mfma_f32_16x16x32_bf16 v[120:123], a[60:63], a[224:227], v[120:123]\n"
        "v_cvt_pk_bf16_f32 v199, v118, v119\n"
        "v_add_u32 v246, s45, v246\n"
        "v_mfma_f32_16x16x32_bf16 v[120:123], a[64:67], a[228:231], v[120:123]\n"
        "v_mfma_f32_16x16x32_bf16 v[120:123], a[68:71], a[232:235], v[120:123]\n"
        "s_nop 2\n"
        "v_mfma_f32_16x16x32_bf16 v[112:115], a[92:95], a[232:235], 0\n"
        "s_nop 2\n"
        "v_mfma_f32_16x16x32_bf16 v[112:115], a[88:91], a[228:231], v[112:115]\n"
        "s_add_u32 m0, s41, 12288\n"
        "v_cvt_pk_bf16_f32 v200, v120, v121\n"
        "v_cvt_pk_bf16_f32 v201, v122, v123\n"
        "v_mfma_f32_16x16x32_bf16 v[112:115], a[84:87], a[224:227], v[112:115]\n"
        "v_cvt_pk_bf16_f32 v206, v120, v121\n"
        "v_cvt_pk_bf16_f32 v207, v122, v123\n"
        "v_mfma_f32_16x16x32_bf16 v[112:115], a[80:83], a[220:223], v[112:115]\n"
        "buffer_load_dwordx4 v242, s[8:11], s35 offen lds\n"
        "v_mfma_f32_16x16x32_bf16 v[112:115], a[76:79], a[216:219], v[112:115]\n"
        "v_mfma_f32_16x16x32_bf16 v[112:115], a[72:75], a[248:251], v[112:115]\n"
        "s_nop 2\n"
        "v_mfma_f32_16x16x32_bf16 v[96:99], v[152:155], v[196:199], v[32:35]\n"
        "s_waitcnt lgkmcnt(1)\n"
        "s_nop 1\n"
        "v_mfma_f32_16x16x32_bf16 v[96:99], v[168:171], v[200:203], v[96:99]\n"
        "s_nop 0\n"
        "v_cvt_pk_bf16_f32 v192, v112, v113\n"
        "v_cvt_pk_bf16_f32 v193, v114, v115\n"
        "v_mfma_f32_16x16x32_bf16 v[100:103], v[172:175], v[196:199], v[36:39]\n"
        "v_mfma_f32_16x16x32_bf16 v[100:103], v[188:191], v[204:207], v[100:103]\n"
        "ds_read_b128 v[202:205], v252 offset:4096\n"
        "s_cmp_eq_u32 s37, 100663296\n"
        "v_mfma_f32_16x16x32_bf16 v[116:119], a[96:99], a[248:251], 0\n"
        "s_cselect_b32 s46, 0, s37\n"
        "v_exp_f32 v96, v96\n"
        "v_mfma_f32_16x16x32_bf16 v[116:119], a[100:103], a[216:219], v[116:119]\n"
        "s_add_u32 s38, s46, s43\n"
        "v_exp_f32 v97, v97\n"
        "v_mfma_f32_16x16x32_bf16 v[116:119], a[104:107], a[220:223], v[116:119]\n"
        "s_add_u32 s39, s46, s44\n"
        "v_exp_f32 v98, v98\n"
        "v_mfma_f32_16x16x32_bf16 v[116:119], a[108:111], a[224:227], v[116:119]\n"
        "s_add_u32 s37, s37, 6291456\n"
        "v_exp_f32 v100, v100\n"
        "v_mfma_f32_16x16x32_bf16 v[116:119], a[112:115], a[228:231], v[116:119]\n"
        "v_exp_f32 v99, v99\n"
        "v_mfma_f32_16x16x32_bf16 v[116:119], a[116:119], a[232:235], v[116:119]\n"
        "s_add_u32 m0, s41, 13312\n"
        "v_exp_f32 v101, v101\n"
        "v_mfma_f32_16x16x32_bf16 v[120:123], a[140:143], a[232:235], 0\n"
        "v_exp_f32 v102, v102\n"
        "v_mfma_f32_16x16x32_bf16 v[120:123], a[136:139], a[228:231], v[120:123]\n"
        "buffer_load_dwordx4 v243, s[8:11], s35 offen lds\n"
        "v_mfma_f32_16x16x32_bf16 v[120:123], a[132:135], a[224:227], v[120:123]\n"
        "v_cvt_pk_bf16_f32 v80, v96, v97\n"
        "v_cvt_pk_bf16_f32 v194, v116, v117\n"
        "v_mfma_f32_16x16x32_bf16 v[120:123], a[128:131], a[220:223], v[120:123]\n"
        "v_cvt_pk_bf16_f32 v195, v118, v119\n"
        "v_cvt_pk_bf16_f32 v81, v98, v99\n"
        "v_mfma_f32_16x16x32_bf16 v[120:123], a[124:127], a[216:219], v[120:123]\n"
        "v_cvt_pk_bf16_f32 v82, v100, v101\n"
        "v_mfma_f32_16x16x32_bf16 v[120:123], a[120:123], a[248:251], v[120:123]\n"
        "v_exp_f32 v103, v103\n"
        "v_mfma_f32_16x16x32_bf16 v[112:115], a[248:251], a[144:147], 0\n"
        "v_cvt_pk_bf16_f32 v83, v102, v103\n"
        "s_nop 0\n"
        "v_mfma_f32_16x16x32_bf16 v[112:115], a[216:219], a[148:151], v[112:115]\n"
        "s_nop 2\n"
        "v_mfma_f32_16x16x32_bf16 v[112:115], a[220:223], a[152:155], v[112:115]\n"
        "ds_read_b128 a[236:239], v240 offset:13824\n"
        "v_cvt_pk_bf16_f32 v208, v120, v121\n"
        "v_mfma_f32_16x16x32_bf16 v[112:115], a[224:227], a[156:159], v[112:115]\n"
        "ds_read_b128 a[240:243], v241 offset:13824\n"
        "v_cvt_pk_bf16_f32 v209, v122, v123\n"
        "v_mfma_f32_16x16x32_bf16 v[112:115], a[228:231], a[160:163], v[112:115]\n"
        "ds_read_b128 a[244:247], v240 offset:13952\n"
        "v_mfma_f32_16x16x32_bf16 v[112:115], a[232:235], a[164:167], v[112:115]\n"
        "s_nop 2\n"
        "v_mfma_f32_16x16x32_bf16 v[104:107], v[192:195], v[156:159], v[8:11]\n"
        "s_nop 1\n"
        "v_mfma_f32_16x16x32_bf16 v[104:107], v[208:211], v[160:163], v[104:107]\n"
        "s_add_u32 m0, s41, 14336\n"
        "v_cvt_pk_bf16_f32 v132, v112, v113\n"
        "v_cvt_pk_bf16_f32 v133, v114, v115\n"
        "v_mfma_f32_16x16x32_bf16 v[116:119], a[232:235], a[188:191], 0\n"
        "v_mfma_f32_16x16x32_bf16 v[116:119], a[228:231], a[184:187], v[116:119]\n"
        "buffer_load_dwordx4 v244, s[8:11], s35 offen lds\n"
        "v_mfma_f32_16x16x32_bf16 v[116:119], a[224:227], a[180:183], v[116:119]\n"
        "v_mfma_f32_16x16x32_bf16 v[116:119], a[220:223], a[176:179], v[116:119]\n"
        "v_exp_f32 v104, v104\n"
        "v_mfma_f32_16x16x32_bf16 v[116:119], a[216:219], a[172:175], v[116:119]\n"
        "v_exp_f32 v105, v105\n"
        "v_mfma_f32_16x16x32_bf16 v[116:119], a[248:251], a[168:171], v[116:119]\n"
        "v_exp_f32 v106, v106\n"
        "v_mfma_f32_16x16x32_bf16 v[108:111], v[192:195], v[176:179], v[24:27]\n"
        "s_waitcnt lgkmcnt(4)\n"
        "v_exp_f32 v107, v107\n"
        "v_mfma_f32_16x16x32_bf16 v[108:111], v[208:211], v[180:183], v[108:111]\n"
        "v_cvt_pk_bf16_f32 v68, v104, v105\n"
        "v_cvt_pk_bf16_f32 v69, v106, v107\n"
        "v_mfma_f32_16x16x32_bf16 v[120:123], a[248:251], a[192:195], 0\n"
        "ds_read_b128 a[248:251], v241 offset:13952\n"
        "v_cvt_pk_bf16_f32 v140, v116, v117\n"
        "v_mfma_f32_16x16x32_bf16 v[120:123], a[216:219], a[196:199], v[120:123]\n"
        "ds_read_b128 a[216:219], v240 offset:14080\n"
        "v_cvt_pk_bf16_f32 v141, v118, v119\n"
        "v_mfma_f32_16x16x32_bf16 v[120:123], a[220:223], a[200:203], v[120:123]\n"
        "ds_read_b128 a[220:223], v241 offset:14080\n"
        "v_mfma_f32_16x16x32_bf16 v[120:123], a[224:227], a[204:207], v[120:123]\n"
        "v_exp_f32 v108, v108\n"
        "v_mfma_f32_16x16x32_bf16 v[120:123], a[228:231], a[208:211], v[120:123]\n"
        "v_exp_f32 v109, v109\n"
        "v_mfma_f32_16x16x32_bf16 v[120:123], a[232:235], a[212:215], v[120:123]\n"
        "v_exp_f32 v110, v110\n"
        "v_mfma_f32_16x16x32_bf16 v[96:99], v[192:195], v[196:199], v[40:43]\n"
        "s_waitcnt lgkmcnt(6)\n"
        "v_exp_f32 v111, v111\n"
        "v_mfma_f32_16x16x32_bf16 v[96:99], v[208:211], v[200:203], v[96:99]\n"
        "s_waitcnt lgkmcnt(5)\n"
        "v_cvt_pk_bf16_f32 v76, v108, v109\n"
        "v_cvt_pk_bf16_f32 v77, v110, v111\n"
        "v_mfma_f32_16x16x32_bf16 v[112:115], a[72:75], a[236:239], 0\n"
        "s_waitcnt lgkmcnt(4)\n"
        "s_nop 0\n"
        "v_cvt_pk_bf16_f32 v148, v120, v121\n"
        "v_mfma_f32_16x16x32_bf16 v[112:115], a[76:79], a[240:243], v[112:115]\n"
        "s_waitcnt lgkmcnt(3)\n"
        "v_cvt_pk_bf16_f32 v149, v122, v123\n"
        "v_mfma_f32_16x16x32_bf16 v[112:115], a[80:83], a[244:247], v[112:115]\n"
        "s_waitcnt lgkmcnt(2)\n"
        "v_exp_f32 v96, v96\n"
        "v_mfma_f32_16x16x32_bf16 v[112:115], a[84:87], a[248:251], v[112:115]\n"
        "s_waitcnt lgkmcnt(1)\n"
        "v_exp_f32 v97, v97\n"
        "v_mfma_f32_16x16x32_bf16 v[112:115], a[88:91], a[216:219], v[112:115]\n"
        "s_waitcnt lgkmcnt(0)\n"
        "v_exp_f32 v98, v98\n"
        "v_mfma_f32_16x16x32_bf16 v[112:115], a[92:95], a[220:223], v[112:115]\n"
        "v_exp_f32 v99, v99\n"
        "v_mfma_f32_16x16x32_bf16 v[116:119], a[116:119], a[220:223], 0\n"
        "v_cvt_pk_bf16_f32 v84, v96, v97\n"
        "v_cvt_pk_bf16_f32 v85, v98, v99\n"
        "v_mfma_f32_16x16x32_bf16 v[116:119], a[112:115], a[216:219], v[116:119]\n"
        "v_add_u32 v240, s31, v240\n"
        "v_add_u32 v241, s31, v241\n"
        "v_mfma_f32_16x16x32_bf16 v[116:119], a[108:111], a[248:251], v[116:119]\n"
        "v_cvt_pk_bf16_f32 v212, v112, v113\n"
        "v_cvt_pk_bf16_f32 v213, v114, v115\n"
        "v_mfma_f32_16x16x32_bf16 v[116:119], a[104:107], a[244:247], v[116:119]\n"
        "v_mfma_f32_16x16x32_bf16 v[116:119], a[100:103], a[240:243], v[116:119]\n"
        "v_mfma_f32_16x16x32_bf16 v[116:119], a[96:99], a[236:239], v[116:119]\n"
        "s_nop 2\n"
        "v_mfma_f32_16x16x32_bf16 v[120:123], a[120:123], a[236:239], 0\n"
        "s_nop 2\n"
        "v_mfma_f32_16x16x32_bf16 v[120:123], a[124:127], a[240:243], v[120:123]\n"
        "s_add_u32 s47, s47, 6291456\n"
        "v_cvt_pk_bf16_f32 v214, v116, v117\n"
        "v_cvt_pk_bf16_f32 v215, v118, v119\n"
        "v_mfma_f32_16x16x32_bf16 v[120:123], a[128:131], a[244:247], v[120:123]\n"
        "s_add_u32 s36, s36, 2097152\n"
        "v_mfma_f32_16x16x32_bf16 v[120:123], a[132:135], a[248:251], v[120:123]\n"
        "s_xor_b32 s48, s48, 8192\n"
        "v_mfma_f32_16x16x32_bf16 v[120:123], a[136:139], a[216:219], v[120:123]\n"
        "v_mfma_f32_16x16x32_bf16 v[120:123], a[140:143], a[220:223], v[120:123]\n"
        "s_nop 2\n"
        "v_mfma_f32_16x16x32_bf16 v[112:115], a[220:223], a[164:167], 0\n"
        "s_nop 2\n"
        "v_mfma_f32_16x16x32_bf16 v[112:115], a[216:219], a[160:163], v[112:115]\n"
        "v_cvt_pk_bf16_f32 v230, v120, v121\n"
        "v_cvt_pk_bf16_f32 v231, v122, v123\n"
        "v_mfma_f32_16x16x32_bf16 v[112:115], a[248:251], a[156:159], v[112:115]\n"
        "v_mfma_f32_16x16x32_bf16 v[112:115], a[244:247], a[152:155], v[112:115]\n"
        "v_mfma_f32_16x16x32_bf16 v[112:115], a[240:243], a[148:151], v[112:115]\n"
        "v_mfma_f32_16x16x32_bf16 v[112:115], a[236:239], a[144:147], v[112:115]\n"
        "s_barrier\n"
        "s_nop 2\n"
        "v_mfma_f32_16x16x32_bf16 v[100:103], v[212:215], v[156:159], v[12:15]\n"
        "s_nop 2\n"
        "v_mfma_f32_16x16x32_bf16 v[100:103], v[228:231], v[164:167], v[100:103]\n"
        "v_cvt_pk_bf16_f32 v134, v112, v113\n"
        "v_cvt_pk_bf16_f32 v135, v114, v115\n"
        "v_mfma_f32_16x16x32_bf16 v[116:119], a[236:239], a[168:171], 0\n"
        "s_sub_u32 s27, s27, 1\n"
        "v_mfma_f32_16x16x32_bf16 v[116:119], a[240:243], a[172:175], v[116:119]\n"
        "s_cmp_lg_u32 s27, 0\n"
        "v_mfma_f32_16x16x32_bf16 v[116:119], a[244:247], a[176:179], v[116:119]\n"
        "s_cbranch_scc0 19f\n"
        "v_mfma_f32_16x16x32_bf16 v[116:119], a[248:251], a[180:183], v[116:119]\n"
        "v_mfma_f32_16x16x32_bf16 v[116:119], a[216:219], a[184:187], v[116:119]\n"
        "s_nop 0\n"
        "v_exp_f32 v100, v100\n"
        "v_mfma_f32_16x16x32_bf16 v[116:119], a[220:223], a[188:191], v[116:119]\n"
        "v_exp_f32 v101, v101\n"
        "v_mfma_f32_16x16x32_bf16 v[104:107], v[212:215], v[176:179], v[28:31]\n"
        "v_exp_f32 v102, v102\n"
        "v_mfma_f32_16x16x32_bf16 v[104:107], v[228:231], v[184:187], v[104:107]\n"
        "v_exp_f32 v103, v103\n"
        "v_mfma_f32_16x16x32_bf16 v[120:123], a[220:223], a[212:215], 0\n"
        "v_cvt_pk_bf16_f32 v70, v100, v101\n"
        "v_cvt_pk_bf16_f32 v71, v102, v103\n"
        "v_mfma_f32_16x16x32_bf16 v[120:123], a[216:219], a[208:211], v[120:123]\n"
        "v_cvt_pk_bf16_f32 v142, v116, v117\n"
        "v_cvt_pk_bf16_f32 v143, v118, v119\n"
        "v_mfma_f32_16x16x32_bf16 v[120:123], a[248:251], a[204:207], v[120:123]\n"
        "v_mov_b64 v[96:97], 0\n"
        "v_mfma_f32_16x16x32_bf16 v[120:123], a[244:247], a[200:203], v[120:123]\n"
        "v_exp_f32 v104, v104\n"
        "v_mfma_f32_16x16x32_bf16 v[120:123], a[240:243], a[196:199], v[120:123]\n"
        "v_exp_f32 v105, v105\n"
        "v_mfma_f32_16x16x32_bf16 v[120:123], a[236:239], a[192:195], v[120:123]\n"
        "v_exp_f32 v106, v106\n"
        "v_mfma_f32_16x16x32_bf16 v[108:111], v[212:215], v[196:199], v[44:47]\n"
        "v_exp_f32 v107, v107\n"
        "v_mfma_f32_16x16x32_bf16 v[108:111], v[228:231], v[204:207], v[108:111]\n"
        "v_cvt_pk_bf16_f32 v78, v104, v105\n"
        "v_cvt_pk_bf16_f32 v79, v106, v107\n"
        "v_mfma_f32_16x16x32_bf16 v[112:115], a[0:3], a[236:239], 0\n"
        "s_nop 0\n"
        "v_cvt_pk_bf16_f32 v150, v120, v121\n"
        "v_cvt_pk_bf16_f32 v151, v122, v123\n"
        "v_mfma_f32_16x16x32_bf16 v[112:115], a[4:7], a[240:243], v[112:115]\n"
        "v_mfma_f32_16x16x32_bf16 v[112:115], a[8:11], a[244:247], v[112:115]\n"
        "v_exp_f32 v108, v108\n"
        "v_mfma_f32_16x16x32_bf16 v[112:115], a[12:15], a[248:251], v[112:115]\n"
        "v_exp_f32 v109, v109\n"
        "v_mfma_f32_16x16x32_bf16 v[112:115], a[16:19], a[216:219], v[112:115]\n"
        "v_exp_f32 v110, v110\n"
        "v_mfma_f32_16x16x32_bf16 v[112:115], a[20:23], a[220:223], v[112:115]\n"
        "v_exp_f32 v111, v111\n"
        "v_smfmac_f32_16x16x64_bf16 v[96:99], a[252:255], v[64:71], v237\n"
        "v_cvt_pk_bf16_f32 v86, v108, v109\n"
        "v_mfma_f32_16x16x32_bf16 v[100:103], v[132:135], v[68:71], 0\n"
        "v_mov_b64 v[124:125], 0\n"
        "v_cvt_pk_bf16_f32 v87, v110, v111\n"
        "v_mfma_f32_16x16x32_bf16 v[100:103], v[128:131], v[64:67], v[100:103]\n"
        "s_nop 0\n"
        "v_cvt_pk_bf16_f32 v216, v112, v113\n"
        "v_cvt_pk_bf16_f32 v217, v114, v115\n"
        "v_mfma_f32_16x16x32_bf16 v[104:107], v[136:139], v[64:67], 0\n"
        ".long 0xbec200ff\n"
        "47:\n"
        "s_branch 61b\n"
        "ds_read_b128 v[222:225], v251 offset:4608\n"
        "v_add_f32 v97, v96, v97\n"
        "v_mfma_f32_16x16x32_bf16 v[104:107], v[140:143], v[68:71], v[104:107]\n"
        "v_rcp_f32 v96, v97\n"
        "v_mfma_f32_16x16x32_bf16 v[108:111], v[148:151], v[68:71], 0\n"
        "v_mul_f32 v100, v96, v100\n"
        "v_mul_f32 v101, v96, v101\n"
        "v_mfma_f32_16x16x32_bf16 v[108:111], v[144:147], v[64:67], v[108:111]\n"
        "v_mul_f32 v102, v96, v102\n"
        "v_mul_f32 v103, v96, v103\n"
        "v_mfma_f32_16x16x32_bf16 v[116:119], a[44:47], a[220:223], 0\n"
        "v_cvt_pk_bf16_f32 v100, v100, v101\n"
        "v_cvt_pk_bf16_f32 v101, v102, v103\n"
        "v_mfma_f32_16x16x32_bf16 v[116:119], a[40:43], a[216:219], v[116:119]\n"
        "v_mul_f32 v104, v96, v104\n"
        "v_mul_f32 v105, v96, v105\n"
        "v_mfma_f32_16x16x32_bf16 v[116:119], a[36:39], a[248:251], v[116:119]\n"
        "v_mul_f32 v106, v96, v106\n"
        "v_mul_f32 v107, v96, v107\n"
        "v_mfma_f32_16x16x32_bf16 v[116:119], a[32:35], a[244:247], v[116:119]\n"
        "v_cvt_pk_bf16_f32 v102, v104, v105\n"
        "v_cvt_pk_bf16_f32 v103, v106, v107\n"
        "v_mfma_f32_16x16x32_bf16 v[116:119], a[28:31], a[240:243], v[116:119]\n"
        "ds_write_b128 v245, v[100:103] offset:0\n"
        "v_mfma_f32_16x16x32_bf16 v[116:119], a[24:27], a[236:239], v[116:119]\n"
        "v_mul_f32 v108, v96, v108\n"
        "v_mul_f32 v109, v96, v109\n"
        "v_smfmac_f32_16x16x64_bf16 v[124:127], a[252:255], v[72:79], v237\n"
        "v_mul_f32 v110, v96, v110\n"
        "v_mfma_f32_16x16x32_bf16 v[64:67], v[132:135], v[76:79], 0\n"
        "v_mul_f32 v111, v96, v111\n"
        "v_cvt_pk_bf16_f32 v108, v108, v109\n"
        "v_mfma_f32_16x16x32_bf16 v[64:67], v[128:131], v[72:75], v[64:67]\n"
        "ds_read_b128 v[156:159], v247 offset:0\n"
        "v_cvt_pk_bf16_f32 v218, v116, v117\n"
        "v_mfma_f32_16x16x32_bf16 v[68:71], v[136:139], v[72:75], 0\n"
        "ds_read_b128 v[160:163], v247 offset:4096\n"
        "v_cvt_pk_bf16_f32 v219, v118, v119\n"
        "v_mfma_f32_16x16x32_bf16 v[68:71], v[140:143], v[76:79], v[68:71]\n"
        "ds_read_b128 v[164:167], v247 offset:4352\n"
        "v_cvt_pk_bf16_f32 v109, v110, v111\n"
        "v_mfma_f32_16x16x32_bf16 v[120:123], v[148:151], v[76:79], 0\n"
        "ds_write_b64 v246, v[108:109] offset:0\n"
        "v_mfma_f32_16x16x32_bf16 v[120:123], v[144:147], v[72:75], v[120:123]\n"
        "v_max_u32 v236, v96, v97\n"
        "v_mov_b64 v[116:117], 0\n"
        "v_mfma_f32_16x16x32_bf16 v[112:115], a[48:51], a[236:239], 0\n"
        "v_add_f32 v125, v124, v125\n"
        "v_mfma_f32_16x16x32_bf16 v[112:115], a[52:55], a[240:243], v[112:115]\n"
        "v_rcp_f32 v124, v125\n"
        "v_mfma_f32_16x16x32_bf16 v[112:115], a[56:59], a[244:247], v[112:115]\n"
        "v_mul_f32 v64, v124, v64\n"
        "v_mul_f32 v65, v124, v65\n"
        "v_mfma_f32_16x16x32_bf16 v[112:115], a[60:63], a[248:251], v[112:115]\n"
        "v_mul_f32 v66, v124, v66\n"
        "v_mul_f32 v67, v124, v67\n"
        "v_mfma_f32_16x16x32_bf16 v[112:115], a[64:67], a[216:219], v[112:115]\n"
        "v_cvt_pk_bf16_f32 v64, v64, v65\n"
        "v_cvt_pk_bf16_f32 v65, v66, v67\n"
        "v_mfma_f32_16x16x32_bf16 v[112:115], a[68:71], a[220:223], v[112:115]\n"
        "ds_read_b128 a[216:219], v248 offset:0\n"
        "v_mul_f32 v68, v124, v68\n"
        "v_smfmac_f32_16x16x64_bf16 v[116:119], a[252:255], v[80:87], v237\n"
        "ds_read_b128 a[220:223], v248 offset:3072\n"
        "v_mfma_f32_16x16x32_bf16 v[72:75], v[132:135], v[84:87], 0\n"
        "v_mul_f32 v69, v124, v69\n"
        "v_mul_f32 v70, v124, v70\n"
        "v_mfma_f32_16x16x32_bf16 v[72:75], v[128:131], v[80:83], v[72:75]\n"
        "v_cvt_pk_bf16_f32 v220, v112, v113\n"
        "v_cvt_pk_bf16_f32 v221, v114, v115\n"
        "v_mfma_f32_16x16x32_bf16 v[76:79], v[136:139], v[80:83], 0\n"
        "ds_read_b128 a[224:227], v248 offset:32\n"
        "v_cvt_pk_bf16_f32 v226, v112, v113\n"
        "v_mfma_f32_16x16x32_bf16 v[76:79], v[140:143], v[84:87], v[76:79]\n"
        "ds_read_b128 a[228:231], v248 offset:3104\n"
        "v_cvt_pk_bf16_f32 v227, v114, v115\n"
        "v_mfma_f32_16x16x32_bf16 v[96:99], v[148:151], v[84:87], 0\n"
        "v_mul_f32 v71, v124, v71\n"
        "v_cvt_pk_bf16_f32 v66, v68, v69\n"
        "v_mfma_f32_16x16x32_bf16 v[96:99], v[144:147], v[80:83], v[96:99]\n"
        "v_cvt_pk_bf16_f32 v67, v70, v71\n"
        "v_mul_f32 v120, v124, v120\n"
        "v_mfma_f32_16x16x32_bf16 v[100:103], v[152:155], v[216:219], v[48:51]\n"
        "s_waitcnt lgkmcnt(9)\n"
        "ds_read_b128 v[152:155], v247 offset:256\n"
        "v_mul_f32 v121, v124, v121\n"
        "v_mfma_f32_16x16x32_bf16 v[100:103], v[168:171], v[220:223], v[100:103]\n"
        "ds_write_b128 v245, v[64:67] offset:256\n"
        "v_mfma_f32_16x16x32_bf16 v[104:107], v[172:175], v[216:219], v[52:55]\n"
        "v_mul_f32 v122, v124, v122\n"
        "v_mul_f32 v123, v124, v123\n"
        "v_mfma_f32_16x16x32_bf16 v[104:107], v[188:191], v[224:227], v[104:107]\n"
        "s_waitcnt lgkmcnt(5)\n"
        "v_cvt_pk_bf16_f32 v120, v120, v121\n"
        "v_cvt_pk_bf16_f32 v121, v122, v123\n"
        "v_mfma_f32_16x16x32_bf16 v[176:179], v[156:159], a[216:219], 0\n"
        "s_waitcnt lgkmcnt(4)\n"
        "ds_write_b64 v246, v[120:121] offset:256\n"
        "v_mfma_f32_16x16x32_bf16 v[176:179], v[160:163], a[220:223], v[176:179]\n"
        "ds_read_b128 v[222:225], v252 offset:4608\n"
        "v_max3_u32 v236, v236, v124, v125\n"
        "v_mfma_f32_16x16x32_bf16 v[196:199], v[164:167], a[220:223], 0\n"
        "s_waitcnt lgkmcnt(3)\n"
        "ds_read_b128 a[232:235], v254 offset:0\n"
        "v_add_f32 v117, v116, v117\n"
        "v_mfma_f32_16x16x32_bf16 v[196:199], v[152:155], a[216:219], v[196:199]\n"
        "ds_read_b128 a[220:223], v254 offset:3072\n"
        "v_mfma_f32_16x16x32_bf16 v[200:203], v[152:155], a[224:227], 0\n"
        "ds_read_b128 a[216:219], v254 offset:32\n"
        "v_mfma_f32_16x16x32_bf16 v[200:203], v[164:167], a[228:231], v[200:203]\n"
        "v_rcp_f32 v116, v117\n"
        "v_mfma_f32_16x16x32_bf16 v[180:183], v[160:163], a[228:231], 0\n"
        "ds_read_b128 a[228:231], v254 offset:3104\n"
        "v_mul_f32 v72, v116, v72\n"
        "v_mfma_f32_16x16x32_bf16 v[180:183], v[156:159], a[224:227], v[180:183]\n"
        "ds_read_b128 a[236:239], v247 offset:8192\n"
        "v_mul_f32 v73, v116, v73\n"
        "v_mfma_f32_16x16x32_bf16 v[108:111], v[192:195], v[216:219], v[56:59]\n"
        "s_waitcnt lgkmcnt(5)\n"
        "ds_read_b128 a[224:227], v248 offset:16\n"
        "v_mul_f32 v74, v116, v74\n"
        "v_mfma_f32_16x16x32_bf16 v[108:111], v[208:211], v[220:223], v[108:111]\n"
        "ds_read_b128 a[240:243], v247 offset:12288\n"
        "v_mul_f32 v75, v116, v75\n"
        "v_mfma_f32_16x16x32_bf16 v[112:115], v[212:215], v[216:219], v[60:63]\n"
        "ds_read_b128 a[244:247], v247 offset:12544\n"
        "v_cvt_pk_bf16_f32 v72, v72, v73\n"
        "v_mfma_f32_16x16x32_bf16 v[112:115], v[228:231], v[224:227], v[112:115]\n"
        "s_waitcnt lgkmcnt(7)\n"
        "ds_read_b128 a[248:251], v247 offset:8448\n"
        "v_cvt_pk_bf16_f32 v73, v74, v75\n"
        "v_mfma_f32_16x16x32_bf16 v[184:187], v[156:159], a[232:235], 0\n"
        "s_waitcnt lgkmcnt(7)\n"
        "v_exp_f32 v100, v100\n"
        "v_mfma_f32_16x16x32_bf16 v[184:187], v[160:163], a[220:223], v[184:187]\n"
        "v_exp_f32 v101, v101\n"
        "v_mfma_f32_16x16x32_bf16 v[204:207], v[164:167], a[220:223], 0\n"
        "ds_read_b128 a[220:223], v248 offset:3088\n"
        "v_mul_f32 v76, v116, v76\n"
        "v_mfma_f32_16x16x32_bf16 v[204:207], v[152:155], a[232:235], v[204:207]\n"
        "s_waitcnt lgkmcnt(7)\n"
        "ds_read_b128 a[232:235], v254 offset:16\n"
        "v_mul_f32 v77, v116, v77\n"
        "v_mfma_f32_16x16x32_bf16 v[192:195], v[152:155], a[216:219], 0\n"
        "s_waitcnt lgkmcnt(7)\n"
        "v_mul_f32 v78, v116, v78\n"
        "v_mul_f32 v79, v116, v79\n"
        "v_mfma_f32_16x16x32_bf16 v[192:195], v[164:167], a[228:231], v[192:195]\n"
        "v_exp_f32 v102, v102\n"
        "v_mfma_f32_16x16x32_bf16 v[172:175], v[160:163], a[228:231], 0\n"
        "ds_read_b128 a[228:231], v254 offset:3088\n"
        "v_cvt_pk_bf16_f32 v88, v100, v101\n"
        "v_mfma_f32_16x16x32_bf16 v[172:175], v[156:159], a[216:219], v[172:175]\n"
        "s_waitcnt lgkmcnt(6)\n"
        "ds_read_b128 a[216:219], v248 offset:6144\n"
        "v_cvt_pk_bf16_f32 v74, v76, v77\n"
        "v_mfma_f32_16x16x32_bf16 v[212:215], v[156:159], a[224:227], 0\n"
        "s_waitcnt lgkmcnt(3)\n"
        "v_exp_f32 v104, v104\n"
        "v_mfma_f32_16x16x32_bf16 v[212:215], v[160:163], a[220:223], v[212:215]\n"
        "v_exp_f32 v103, v103\n"
        "v_mfma_f32_16x16x32_bf16 v[216:219], v[164:167], a[220:223], 0\n"
        "ds_read_b128 a[220:223], v248 offset:9216\n"
        "v_cvt_pk_bf16_f32 v75, v78, v79\n"
        "v_mfma_f32_16x16x32_bf16 v[216:219], v[152:155], a[224:227], v[216:219]\n"
        "s_waitcnt lgkmcnt(3)\n"
        "ds_write_b128 v245, v[72:75] offset:512\n"
        "v_mfma_f32_16x16x32_bf16 v[224:227], v[152:155], a[232:235], 0\n"
        "s_waitcnt lgkmcnt(3)\n"
        "v_exp_f32 v105, v105\n"
        "v_mfma_f32_16x16x32_bf16 v[224:227], v[164:167], a[228:231], v[224:227]\n"
        "v_cvt_pk_bf16_f32 v89, v102, v103\n"
        "v_cvt_pk_bf16_f32 v90, v104, v105\n"
        "v_mfma_f32_16x16x32_bf16 v[220:223], v[160:163], a[228:231], 0\n"
        "ds_read_b128 a[224:227], v248 offset:6176\n"
        "ds_read_b128 a[228:231], v248 offset:9248\n"
        "v_mfma_f32_16x16x32_bf16 v[220:223], v[156:159], a[232:235], v[220:223]\n"
        "s_waitcnt lgkmcnt(4)\n"
        "ds_read_b128 a[232:235], v254 offset:6144\n"
        "v_mul_f32 v96, v116, v96\n"
        "v_mfma_f32_16x16x32_bf16 v[176:179], a[236:239], a[216:219], v[176:179]\n"
        "s_waitcnt lgkmcnt(4)\n"
        "v_exp_f32 v106, v106\n"
        "v_mfma_f32_16x16x32_bf16 v[176:179], a[240:243], a[220:223], v[176:179]\n"
        "v_mul_f32 v97, v116, v97\n"
        "v_mul_f32 v98, v116, v98\n"
        "v_mfma_f32_16x16x32_bf16 v[196:199], a[244:247], a[220:223], v[196:199]\n"
        "ds_read_b128 a[220:223], v254 offset:9216\n"
        "v_mul_f32 v99, v116, v99\n"
        "v_mfma_f32_16x16x32_bf16 v[196:199], a[248:251], a[216:219], v[196:199]\n"
        "s_waitcnt lgkmcnt(3)\n"
        "ds_read_b128 a[216:219], v254 offset:6176\n"
        "v_cvt_pk_bf16_f32 v96, v96, v97\n"
        "v_mfma_f32_16x16x32_bf16 v[200:203], a[248:251], a[224:227], v[200:203]\n"
        "s_waitcnt lgkmcnt(3)\n"
        "ds_read_b128 v[156:159], v247 offset:16384\n"
        "v_cvt_pk_bf16_f32 v97, v98, v99\n"
        "v_mfma_f32_16x16x32_bf16 v[200:203], a[244:247], a[228:231], v[200:203]\n"
        "ds_write_b64 v246, v[96:97] offset:512\n"
        "v_mfma_f32_16x16x32_bf16 v[180:183], a[240:243], a[228:231], v[180:183]\n"
        "ds_read_b128 a[228:231], v254 offset:9248\n"
        "v_max3_u32 v236, v236, v116, v117\n"
        "v_mfma_f32_16x16x32_bf16 v[180:183], a[236:239], a[224:227], v[180:183]\n"
        "s_waitcnt lgkmcnt(5)\n"
        "ds_read_b128 a[224:227], v248 offset:6160\n"
        "v_mfma_f32_16x16x32_bf16 v[184:187], a[236:239], a[232:235], v[184:187]\n"
        "s_waitcnt lgkmcnt(5)\n"
        "ds_read_b128 v[160:163], v247 offset:20480\n"
        "v_mfma_f32_16x16x32_bf16 v[184:187], a[240:243], a[220:223], v[184:187]\n"
        "ds_read_b128 v[164:167], v247 offset:20736\n"
        "v_mfma_f32_16x16x32_bf16 v[204:207], a[244:247], a[220:223], v[204:207]\n"
        "ds_read_b128 a[220:223], v248 offset:9232\n"
        "v_mfma_f32_16x16x32_bf16 v[204:207], a[248:251], a[232:235], v[204:207]\n"
        "s_waitcnt lgkmcnt(7)\n"
        "ds_read_b128 a[232:235], v254 offset:6160\n"
        "v_mfma_f32_16x16x32_bf16 v[192:195], a[248:251], a[216:219], v[192:195]\n"
        "s_waitcnt lgkmcnt(5)\n"
        "ds_read_b128 v[152:155], v247 offset:16640\n"
        "v_mfma_f32_16x16x32_bf16 v[192:195], a[244:247], a[228:231], v[192:195]\n"
        "v_exp_f32 v107, v107\n"
        "v_mfma_f32_16x16x32_bf16 v[172:175], a[240:243], a[228:231], v[172:175]\n"
        "ds_read_b128 a[228:231], v254 offset:9232\n"
        "v_cvt_pk_bf16_f32 v91, v106, v107\n"
        "v_mfma_f32_16x16x32_bf16 v[172:175], a[236:239], a[216:219], v[172:175]\n"
        "s_waitcnt lgkmcnt(6)\n"
        "ds_read_b128 a[216:219], v248 offset:12288\n"
        "v_mfma_f32_16x16x32_bf16 v[212:215], a[236:239], a[224:227], v[212:215]\n"
        "s_waitcnt lgkmcnt(4)\n"
        "v_exp_f32 v108, v108\n"
        "v_mfma_f32_16x16x32_bf16 v[212:215], a[240:243], a[220:223], v[212:215]\n"
        "v_exp_f32 v109, v109\n"
        "v_mfma_f32_16x16x32_bf16 v[216:219], a[244:247], a[220:223], v[216:219]\n"
        "ds_read_b128 a[220:223], v248 offset:15360\n"
        "v_cvt_pk_bf16_f32 v92, v108, v109\n"
        "v_mfma_f32_16x16x32_bf16 v[216:219], a[248:251], a[224:227], v[216:219]\n"
        "s_waitcnt lgkmcnt(4)\n"
        "ds_read_b128 a[224:227], v248 offset:12320\n"
        "v_mfma_f32_16x16x32_bf16 v[224:227], a[248:251], a[232:235], v[224:227]\n"
        "s_waitcnt lgkmcnt(3)\n"
        "v_exp_f32 v110, v110\n"
        "v_mfma_f32_16x16x32_bf16 v[224:227], a[244:247], a[228:231], v[224:227]\n"
        "v_exp_f32 v112, v112\n"
        "v_mfma_f32_16x16x32_bf16 v[220:223], a[240:243], a[228:231], v[220:223]\n"
        "ds_read_b128 a[228:231], v248 offset:15392\n"
        "v_mfma_f32_16x16x32_bf16 v[220:223], a[236:239], a[232:235], v[220:223]\n"
        "s_waitcnt lgkmcnt(3)\n"
        "ds_read_b128 a[232:235], v254 offset:12288\n"
        "v_mfma_f32_16x16x32_bf16 v[176:179], v[156:159], a[216:219], v[176:179]\n"
        "s_waitcnt lgkmcnt(3)\n"
        "v_exp_f32 v111, v111\n"
        "v_mfma_f32_16x16x32_bf16 v[176:179], v[160:163], a[220:223], v[176:179]\n"
        "v_exp_f32 v113, v113\n"
        "v_mfma_f32_16x16x32_bf16 v[196:199], v[164:167], a[220:223], v[196:199]\n"
        "ds_read_b128 a[220:223], v254 offset:15360\n"
        "v_cvt_pk_bf16_f32 v93, v110, v111\n"
        "v_mfma_f32_16x16x32_bf16 v[196:199], v[152:155], a[216:219], v[196:199]\n"
        "s_waitcnt lgkmcnt(3)\n"
        "ds_read_b128 a[216:219], v254 offset:12320\n"
        "v_cvt_pk_bf16_f32 v94, v112, v113\n"
        "v_mfma_f32_16x16x32_bf16 v[200:203], v[152:155], a[224:227], v[200:203]\n"
        "s_waitcnt lgkmcnt(3)\n"
        "v_exp_f32 v114, v114\n"
        "v_mfma_f32_16x16x32_bf16 v[200:203], v[164:167], a[228:231], v[200:203]\n"
        "v_exp_f32 v115, v115\n"
        "v_mfma_f32_16x16x32_bf16 v[180:183], v[160:163], a[228:231], v[180:183]\n"
        "s_waitcnt vmcnt(0)\n"
        "ds_read_b128 a[228:231], v254 offset:15392\n"
        "v_cvt_pk_bf16_f32 v95, v114, v115\n"
        "v_mfma_f32_16x16x32_bf16 v[180:183], v[156:159], a[224:227], v[180:183]\n"
        "s_waitcnt lgkmcnt(3)\n"
        "ds_read_b128 a[224:227], v248 offset:12304\n"
        "v_mfma_f32_16x16x32_bf16 v[184:187], v[156:159], a[232:235], v[184:187]\n"
        "s_waitcnt lgkmcnt(3)\n"
        "v_mfma_f32_16x16x32_bf16 v[184:187], v[160:163], a[220:223], v[184:187]\n"
        "s_barrier\n"
        "v_mfma_f32_16x16x32_bf16 v[204:207], v[164:167], a[220:223], v[204:207]\n"
        "ds_read_b128 a[220:223], v248 offset:15376\n"
        "v_mfma_f32_16x16x32_bf16 v[204:207], v[152:155], a[232:235], v[204:207]\n"
        "s_waitcnt lgkmcnt(3)\n"
        "ds_read_b128 a[232:235], v254 offset:12304\n"
        "v_mfma_f32_16x16x32_bf16 v[192:195], v[152:155], a[216:219], v[192:195]\n"
        "s_waitcnt lgkmcnt(3)\n"
        "ds_read_b128 a[236:239], v240 offset:0\n"
        "v_mfma_f32_16x16x32_bf16 v[192:195], v[164:167], a[228:231], v[192:195]\n"
        "ds_read_b128 a[240:243], v241 offset:0\n"
        "v_mfma_f32_16x16x32_bf16 v[172:175], v[160:163], a[228:231], v[172:175]\n"
        "ds_read_b128 a[228:231], v254 offset:15376\n"
        "v_mfma_f32_16x16x32_bf16 v[172:175], v[156:159], a[216:219], v[172:175]\n"
        "s_waitcnt lgkmcnt(5)\n"
        "ds_read_b128 a[244:247], v240 offset:128\n"
        "v_mfma_f32_16x16x32_bf16 v[212:215], v[156:159], a[224:227], v[212:215]\n"
        "s_waitcnt lgkmcnt(5)\n"
        "ds_read_b128 a[248:251], v241 offset:128\n"
        "v_mfma_f32_16x16x32_bf16 v[212:215], v[160:163], a[220:223], v[212:215]\n"
        "ds_read_b128 a[216:219], v240 offset:256\n"
        "v_mfma_f32_16x16x32_bf16 v[216:219], v[164:167], a[220:223], v[216:219]\n"
        "ds_read_b128 a[220:223], v241 offset:256\n"
        "v_mfma_f32_16x16x32_bf16 v[216:219], v[152:155], a[224:227], v[216:219]\n"
        "s_waitcnt lgkmcnt(7)\n"
        "v_mfma_f32_16x16x32_bf16 v[224:227], v[152:155], a[232:235], v[224:227]\n"
        "s_waitcnt lgkmcnt(4)\n"
        "v_mfma_f32_16x16x32_bf16 v[224:227], v[164:167], a[228:231], v[224:227]\n"
        "v_mfma_f32_16x16x32_bf16 v[220:223], v[160:163], a[228:231], v[220:223]\n"
        "v_mfma_f32_16x16x32_bf16 v[220:223], v[156:159], a[232:235], v[220:223]\n"
        "s_branch 10b\n"
        ".p2align 6\n"
        "54:\n"
        "s_branch 61b\n"
        "19:\n"
        "v_mfma_f32_16x16x32_bf16 v[116:119], a[248:251], a[180:183], v[116:119]\n"
        "ds_read_b128 a[224:227], v247 offset:0\n"
        "ds_read_b128 a[120:123], v254 offset:0\n"
        "s_and_b32 s46, s42, 1\n"
        "v_mfma_f32_16x16x32_bf16 v[116:119], a[216:219], a[184:187], v[116:119]\n"
        "ds_read_b128 a[228:231], v247 offset:4096\n"
        "ds_read_b128 a[124:127], v254 offset:3072\n"
        "v_mfma_f32_16x16x32_bf16 v[116:119], a[220:223], a[188:191], v[116:119]\n"
        "ds_read_b128 a[232:235], v247 offset:8192\n"
        "s_lshl_b32 s31, s46, 8\n"
        "v_mov_b32 v241, 0\n"
        "v_mfma_f32_16x16x32_bf16 v[104:107], v[212:215], v[176:179], v[28:31]\n"
        "ds_read_b128 a[128:131], v254 offset:6144\n"
        "s_nop 0\n"
        "v_mfma_f32_16x16x32_bf16 v[104:107], v[228:231], v[184:187], v[104:107]\n"
        "ds_read_b128 v[156:159], v247 offset:12288\n"
        "s_sub_u32 s31, s45, s31\n"
        "v_cvt_pk_bf16_f32 v142, v116, v117\n"
        "v_mfma_f32_16x16x32_bf16 v[120:123], a[220:223], a[212:215], 0\n"
        "ds_read_b128 a[132:135], v254 offset:9216\n"
        "v_cvt_pk_bf16_f32 v143, v118, v119\n"
        "v_mfma_f32_16x16x32_bf16 v[120:123], a[216:219], a[208:211], v[120:123]\n"
        "ds_read_b128 v[160:163], v247 offset:16384\n"
        "ds_read_b128 a[136:139], v254 offset:12288\n"
        "s_mul_i32 s45, s46, 786432\n"
        "v_mfma_f32_16x16x32_bf16 v[120:123], a[248:251], a[204:207], v[120:123]\n"
        "ds_read_b128 v[164:167], v247 offset:20480\n"
        "ds_read_b128 a[140:143], v254 offset:15360\n"
        "v_mfma_f32_16x16x32_bf16 v[120:123], a[244:247], a[200:203], v[120:123]\n"
        "ds_read_b128 a[92:95], v248 offset:15360\n"
        "s_sub_u32 s37, s37, s45\n"
        "v_mfma_f32_16x16x32_bf16 v[120:123], a[240:243], a[196:199], v[120:123]\n"
        "ds_read_b128 a[88:91], v248 offset:12288\n"
        "v_mfma_f32_16x16x32_bf16 v[120:123], a[236:239], a[192:195], v[120:123]\n"
        "ds_read_b128 a[84:87], v248 offset:9216\n"
        "s_cmp_eq_u32 s46, 0\n"
        "s_cselect_b32 s43, s43, s44\n"
        "v_mfma_f32_16x16x32_bf16 v[108:111], v[212:215], v[196:199], v[44:47]\n"
        "ds_read_b128 a[80:83], v248 offset:6144\n"
        "s_nop 0\n"
        "v_mfma_f32_16x16x32_bf16 v[108:111], v[228:231], v[204:207], v[108:111]\n"
        "s_waitcnt lgkmcnt(14)\n"
        "ds_read_b128 a[76:79], v248 offset:3072\n"
        "v_cvt_pk_bf16_f32 v150, v120, v121\n"
        "v_mfma_f32_16x16x32_bf16 v[8:11], a[224:227], a[120:123], 0\n"
        "s_waitcnt lgkmcnt(13)\n"
        "ds_read_b128 a[72:75], v248 offset:0\n"
        "v_cvt_pk_bf16_f32 v151, v122, v123\n"
        "v_mfma_f32_16x16x32_bf16 v[8:11], a[228:231], a[124:127], v[8:11]\n"
        "s_waitcnt lgkmcnt(12)\n"
        "v_exp_f32 v100, v100\n"
        "v_mfma_f32_16x16x32_bf16 v[8:11], a[232:235], a[128:131], v[8:11]\n"
        "s_waitcnt lgkmcnt(10)\n"
        "v_exp_f32 v101, v101\n"
        "v_mfma_f32_16x16x32_bf16 v[8:11], v[156:159], a[132:135], v[8:11]\n"
        "s_waitcnt lgkmcnt(8)\n"
        "ds_read_b128 a[96:99], v248 offset:32\n"
        "v_cvt_pk_bf16_f32 v70, v100, v101\n"
        "v_mfma_f32_16x16x32_bf16 v[8:11], v[160:163], a[136:139], v[8:11]\n"
        "s_waitcnt lgkmcnt(7)\n"
        "ds_read_b128 a[100:103], v248 offset:3104\n"
        "v_mfma_f32_16x16x32_bf16 v[8:11], v[164:167], a[140:143], v[8:11]\n"
        "s_waitcnt lgkmcnt(7)\n"
        "ds_read_b128 a[104:107], v248 offset:6176\n"
        "v_mfma_f32_16x16x32_bf16 v[0:3], v[164:167], a[92:95], 0\n"
        "s_waitcnt lgkmcnt(7)\n"
        "ds_read_b128 a[108:111], v248 offset:9248\n"
        "v_mfma_f32_16x16x32_bf16 v[0:3], v[160:163], a[88:91], v[0:3]\n"
        "s_waitcnt lgkmcnt(7)\n"
        "ds_read_b128 a[112:115], v248 offset:12320\n"
        "v_mfma_f32_16x16x32_bf16 v[0:3], v[156:159], a[84:87], v[0:3]\n"
        "s_waitcnt lgkmcnt(7)\n"
        "ds_read_b128 a[116:119], v248 offset:15392\n"
        "v_mfma_f32_16x16x32_bf16 v[0:3], a[232:235], a[80:83], v[0:3]\n"
        "s_waitcnt lgkmcnt(7)\n"
        "v_exp_f32 v102, v102\n"
        "v_mfma_f32_16x16x32_bf16 v[0:3], a[228:231], a[76:79], v[0:3]\n"
        "s_waitcnt lgkmcnt(6)\n"
        "v_exp_f32 v103, v103\n"
        "v_mfma_f32_16x16x32_bf16 v[0:3], a[224:227], a[72:75], v[0:3]\n"
        "s_add_u32 s28, s37, 786432\n"
        "v_exp_f32 v104, v104\n"
        "v_mfma_f32_16x16x32_bf16 v[116:119], a[0:3], a[236:239], 0\n"
        "s_cmp_eq_u32 s28, 100663296\n"
        "s_cselect_b32 s28, 0, s28\n"
        "v_mov_b64 v[96:97], 0\n"
        "v_mfma_f32_16x16x32_bf16 v[116:119], a[4:7], a[240:243], v[116:119]\n"
        "s_lshl_b32 s29, s42, 2\n"
        "v_exp_f32 v105, v105\n"
        "v_mfma_f32_16x16x32_bf16 v[116:119], a[8:11], a[244:247], v[116:119]\n"
        "s_add_u32 s47, s37, s43\n"
        "v_exp_f32 v106, v106\n"
        "v_mfma_f32_16x16x32_bf16 v[116:119], a[12:15], a[248:251], v[116:119]\n"
        "s_add_u32 s35, s28, s43\n"
        "v_exp_f32 v107, v107\n"
        "v_mfma_f32_16x16x32_bf16 v[116:119], a[16:19], a[216:219], v[116:119]\n"
        "ds_read_b128 a[164:167], v254 offset:15392\n"
        "v_cvt_pk_bf16_f32 v71, v102, v103\n"
        "v_mfma_f32_16x16x32_bf16 v[116:119], a[20:23], a[220:223], v[116:119]\n"
        "s_waitcnt lgkmcnt(6)\n"
        "ds_read_b128 a[160:163], v254 offset:12320\n"
        "v_cvt_pk_bf16_f32 v78, v104, v105\n"
        "v_mfma_f32_16x16x32_bf16 v[4:7], a[224:227], a[96:99], 0\n"
        "s_waitcnt lgkmcnt(6)\n"
        "ds_read_b128 a[156:159], v254 offset:9248\n"
        "v_cvt_pk_bf16_f32 v79, v106, v107\n"
        "v_mfma_f32_16x16x32_bf16 v[4:7], a[228:231], a[100:103], v[4:7]\n"
        "s_waitcnt lgkmcnt(6)\n"
        "ds_read_b128 a[152:155], v254 offset:6176\n"
        "v_add_u32 v240, s31, v247\n"
        "v_mfma_f32_16x16x32_bf16 v[4:7], a[232:235], a[104:107], v[4:7]\n"
        "s_waitcnt lgkmcnt(6)\n"
        "ds_read_b128 a[148:151], v254 offset:3104\n"
        "v_cvt_pk_bf16_f32 v216, v116, v117\n"
        "v_mfma_f32_16x16x32_bf16 v[4:7], v[156:159], a[108:111], v[4:7]\n"
        "s_waitcnt lgkmcnt(6)\n"
        "ds_read_b128 a[144:147], v254 offset:32\n"
        "v_cvt_pk_bf16_f32 v217, v118, v119\n"
        "v_mfma_f32_16x16x32_bf16 v[4:7], v[160:163], a[112:115], v[4:7]\n"
        "s_waitcnt lgkmcnt(6)\n"
        "v_exp_f32 v108, v108\n"
        "v_mfma_f32_16x16x32_bf16 v[4:7], v[164:167], a[116:119], v[4:7]\n"
        "v_exp_f32 v109, v109\n"
        "v_smfmac_f32_16x16x64_bf16 v[96:99], a[252:255], v[64:71], v237\n"
        "v_cvt_pk_bf16_f32 v86, v108, v109\n"
        "v_mfma_f32_16x16x32_bf16 v[100:103], v[132:135], v[68:71], 0\n"
        "v_mov_b64 v[124:125], 0\n"
        "v_mov_b32 v249, s29\n"
        "v_mfma_f32_16x16x32_bf16 v[100:103], v[128:131], v[64:67], v[100:103]\n"
        "v_exp_f32 v110, v110\n"
        "v_mfma_f32_16x16x32_bf16 v[104:107], v[136:139], v[64:67], 0\n"
        "v_exp_f32 v111, v111\n"
        "v_mfma_f32_16x16x32_bf16 v[104:107], v[140:143], v[68:71], v[104:107]\n"
        "v_cvt_pk_bf16_f32 v87, v110, v111\n"
        "v_add_f32 v97, v96, v97\n"
        "v_mfma_f32_16x16x32_bf16 v[108:111], v[148:151], v[68:71], 0\n"
        "v_rcp_f32 v96, v97\n"
        "v_mfma_f32_16x16x32_bf16 v[108:111], v[144:147], v[64:67], v[108:111]\n"
        "s_waitcnt lgkmcnt(5)\n"
        "v_cvt_pk_bf16_f32 v232, v0, v4\n"
        "v_mul_f32 v100, v96, v100\n"
        "v_mfma_f32_16x16x32_bf16 v[12:15], v[164:167], a[164:167], 0\n"
        "s_waitcnt lgkmcnt(4)\n"
        "v_mul_f32 v101, v96, v101\n"
        "v_mul_f32 v102, v96, v102\n"
        "v_mfma_f32_16x16x32_bf16 v[12:15], v[160:163], a[160:163], v[12:15]\n"
        "s_waitcnt lgkmcnt(3)\n"
        "ds_read_b128 v[176:179], v247 offset:256\n"
        "v_cvt_pk_bf16_f32 v234, v1, v5\n"
        "v_mfma_f32_16x16x32_bf16 v[12:15], v[156:159], a[156:159], v[12:15]\n"
        "s_waitcnt lgkmcnt(3)\n"
        "ds_read_b128 v[180:183], v247 offset:4352\n"
        "v_mul_f32 v103, v96, v103\n"
        "v_mfma_f32_16x16x32_bf16 v[12:15], a[232:235], a[152:155], v[12:15]\n"
        "s_waitcnt lgkmcnt(3)\n"
        "ds_read_b128 v[184:187], v247 offset:8448\n"
        "v_cvt_pk_bf16_f32 v100, v100, v101\n"
        "v_mfma_f32_16x16x32_bf16 v[12:15], a[228:231], a[148:151], v[12:15]\n"
        "s_waitcnt lgkmcnt(3)\n"
        "ds_read_b128 a[168:171], v248 offset:16\n"
        "v_cvt_pk_bf16_f32 v242, v2, v6\n"
        "v_mfma_f32_16x16x32_bf16 v[12:15], a[224:227], a[144:147], v[12:15]\n"
        "ds_read_b128 a[172:175], v248 offset:3088\n"
        "v_cvt_pk_bf16_f32 v101, v102, v103\n"
        "v_smfmac_f32_16x16x64_bf16 v[124:127], a[252:255], v[72:79], v237\n"
        "ds_read_b128 a[176:179], v248 offset:6160\n"
        "v_mfma_f32_16x16x32_bf16 v[64:67], v[132:135], v[76:79], 0\n"
        "ds_read_b128 a[180:183], v248 offset:9232\n"
        "v_mul_f32 v104, v96, v104\n"
        "v_mfma_f32_16x16x32_bf16 v[64:67], v[128:131], v[72:75], v[64:67]\n"
        "ds_read_b128 a[184:187], v248 offset:12304\n"
        "v_cvt_pk_bf16_f32 v233, v8, v12\n"
        "v_mfma_f32_16x16x32_bf16 v[68:71], v[136:139], v[72:75], 0\n"
        "buffer_store_dwordx2 v[232:233], v250, s[16:19], s38 offen offset:0 sc1\n"
        "ds_read_b128 a[188:191], v248 offset:15376\n"
        "v_mfma_f32_16x16x32_bf16 v[68:71], v[140:143], v[76:79], v[68:71]\n"
        "ds_read_b128 v[196:199], v247 offset:12544\n"
        "v_cvt_pk_bf16_f32 v235, v9, v13\n"
        "v_mfma_f32_16x16x32_bf16 v[112:115], v[148:151], v[76:79], 0\n"
        "ds_read_b128 v[200:203], v247 offset:16640\n"
        "v_add_f32 v125, v124, v125\n"
        "v_mfma_f32_16x16x32_bf16 v[112:115], v[144:147], v[72:75], v[112:115]\n"
        "buffer_store_dwordx2 v[234:235], v250, s[16:19], s38 offen offset:384 sc1\n"
        "ds_read_b128 v[204:207], v247 offset:20736\n"
        "v_mfma_f32_16x16x32_bf16 v[120:123], a[44:47], a[220:223], 0\n"
        "v_rcp_f32 v124, v125\n"
        "v_mfma_f32_16x16x32_bf16 v[120:123], a[40:43], a[216:219], v[120:123]\n"
        "v_cvt_pk_bf16_f32 v243, v10, v14\n"
        "v_mul_f32 v64, v124, v64\n"
        "v_mfma_f32_16x16x32_bf16 v[120:123], a[36:39], a[248:251], v[120:123]\n"
        "buffer_store_dwordx2 v[242:243], v250, s[16:19], s38 offen offset:768 sc1\n"
        "v_mul_f32 v65, v124, v65\n"
        "v_mfma_f32_16x16x32_bf16 v[120:123], a[32:35], a[244:247], v[120:123]\n"
        "ds_read_b128 a[192:195], v254 offset:16\n"
        "v_mul_f32 v66, v124, v66\n"
        "v_mfma_f32_16x16x32_bf16 v[120:123], a[28:31], a[240:243], v[120:123]\n"
        "ds_read_b128 a[196:199], v254 offset:3088\n"
        "v_mul_f32 v67, v124, v67\n"
        "v_mfma_f32_16x16x32_bf16 v[120:123], a[24:27], a[236:239], v[120:123]\n"
        "s_waitcnt lgkmcnt(10)\n"
        "ds_read_b128 a[200:203], v254 offset:6160\n"
        "v_cvt_pk_bf16_f32 v64, v64, v65\n"
        "v_mfma_f32_16x16x32_bf16 v[16:19], a[224:227], a[168:171], 0\n"
        "s_waitcnt lgkmcnt(10)\n"
        "ds_read_b128 a[204:207], v254 offset:9232\n"
        "ds_read_b128 v[222:225], v251 offset:4608\n"
        "v_mfma_f32_16x16x32_bf16 v[16:19], a[228:231], a[172:175], v[16:19]\n"
        "s_waitcnt lgkmcnt(11)\n"
        "ds_read_b128 a[208:211], v254 offset:12304\n"
        "v_cvt_pk_bf16_f32 v65, v66, v67\n"
        "v_mfma_f32_16x16x32_bf16 v[16:19], a[232:235], a[176:179], v[16:19]\n"
        "s_waitcnt lgkmcnt(11)\n"
        "ds_read_b128 a[212:215], v254 offset:15376\n"
        "v_cvt_pk_bf16_f32 v232, v3, v7\n"
        "v_mfma_f32_16x16x32_bf16 v[16:19], v[156:159], a[180:183], v[16:19]\n"
        "s_waitcnt lgkmcnt(11)\n"
        "v_cvt_pk_bf16_f32 v233, v11, v15\n"
        "v_mul_f32 v68, v124, v68\n"
        "v_mfma_f32_16x16x32_bf16 v[16:19], v[160:163], a[184:187], v[16:19]\n"
        "s_waitcnt lgkmcnt(10)\n"
        "buffer_store_dwordx2 v[232:233], v250, s[16:19], s38 offen offset:1152 sc1\n"
        "v_mul_f32 v69, v124, v69\n"
        "v_mfma_f32_16x16x32_bf16 v[16:19], v[164:167], a[188:191], v[16:19]\n"
        "s_waitcnt lgkmcnt(7)\n"
        "v_mul_f32 v70, v124, v70\n"
        "v_mul_f32 v71, v124, v71\n"
        "v_mfma_f32_16x16x32_bf16 v[24:27], v[204:207], a[92:95], 0\n"
        "v_mul_f32 v105, v96, v105\n"
        "v_mul_f32 v106, v96, v106\n"
        "v_mfma_f32_16x16x32_bf16 v[24:27], v[200:203], a[88:91], v[24:27]\n"
        "v_mul_f32 v107, v96, v107\n"
        "v_cvt_pk_bf16_f32 v66, v68, v69\n"
        "v_mfma_f32_16x16x32_bf16 v[24:27], v[196:199], a[84:87], v[24:27]\n"
        "v_cvt_pk_bf16_f32 v67, v70, v71\n"
        "v_mul_f32 v112, v124, v112\n"
        "v_mfma_f32_16x16x32_bf16 v[24:27], v[184:187], a[80:83], v[24:27]\n"
        "ds_write_b128 v245, v[64:67] offset:256\n"
        "v_mfma_f32_16x16x32_bf16 v[24:27], v[180:183], a[76:79], v[24:27]\n"
        "v_mul_f32 v113, v124, v113\n"
        "v_mul_f32 v114, v124, v114\n"
        "v_mfma_f32_16x16x32_bf16 v[24:27], v[176:179], a[72:75], v[24:27]\n"
        "s_waitcnt lgkmcnt(7)\n"
        "v_mul_f32 v115, v124, v115\n"
        "v_cvt_pk_bf16_f32 v102, v104, v105\n"
        "v_mfma_f32_16x16x32_bf16 v[20:23], a[224:227], a[192:195], 0\n"
        "s_waitcnt lgkmcnt(6)\n"
        "v_cvt_pk_bf16_f32 v103, v106, v107\n"
        "v_cvt_pk_bf16_f32 v218, v120, v121\n"
        "v_mfma_f32_16x16x32_bf16 v[20:23], a[228:231], a[196:199], v[20:23]\n"
        "s_waitcnt lgkmcnt(5)\n"
        "ds_write_b128 v245, v[100:103] offset:0\n"
        "v_mfma_f32_16x16x32_bf16 v[20:23], a[232:235], a[200:203], v[20:23]\n"
        "s_waitcnt lgkmcnt(5)\n"
        "v_cvt_pk_bf16_f32 v219, v122, v123\n"
        "v_mul_f32 v108, v96, v108\n"
        "v_mfma_f32_16x16x32_bf16 v[20:23], v[156:159], a[204:207], v[20:23]\n"
        "s_waitcnt lgkmcnt(3)\n"
        "v_mul_f32 v109, v96, v109\n"
        "v_mul_f32 v110, v96, v110\n"
        "v_mfma_f32_16x16x32_bf16 v[20:23], v[160:163], a[208:211], v[20:23]\n"
        "s_waitcnt lgkmcnt(2)\n"
        "v_mul_f32 v111, v96, v111\n"
        "v_cvt_pk_bf16_f32 v112, v112, v113\n"
        "v_mfma_f32_16x16x32_bf16 v[20:23], v[164:167], a[212:215], v[20:23]\n"
        "v_cvt_pk_bf16_f32 v113, v114, v115\n"
        "v_cvt_pk_bf16_f32 v108, v108, v109\n"
        "v_mfma_f32_16x16x32_bf16 v[116:119], a[48:51], a[236:239], 0\n"
        "ds_write_b64 v246, v[112:113] offset:256\n"
        "v_mfma_f32_16x16x32_bf16 v[116:119], a[52:55], a[240:243], v[116:119]\n"
        "v_cvt_pk_bf16_f32 v109, v110, v111\n"
        "v_max_u32 v236, v96, v97\n"
        "v_mfma_f32_16x16x32_bf16 v[116:119], a[56:59], a[244:247], v[116:119]\n"
        "ds_write_b64 v246, v[108:109] offset:0\n"
        "v_mfma_f32_16x16x32_bf16 v[116:119], a[60:63], a[248:251], v[116:119]\n"
        "v_max3_u32 v236, v236, v124, v125\n"
        "v_cvt_pk_bf16_f32 v244, v16, v20\n"
        "v_mfma_f32_16x16x32_bf16 v[116:119], a[64:67], a[216:219], v[116:119]\n"
        "buffer_store_dword v244, v238, s[16:19], s38 offen offset:0 sc1\n"
        "v_mfma_f32_16x16x32_bf16 v[116:119], a[68:71], a[220:223], v[116:119]\n"
        "v_cvt_pk_bf16_f32 v239, v17, v21\n"
        "v_cvt_pk_bf16_f32 v255, v18, v22\n"
        "v_mfma_f32_16x16x32_bf16 v[32:35], v[204:207], a[140:143], 0\n"
        "v_cvt_pk_bf16_f32 v244, v19, v23\n"
        "s_nop 0\n"
        "v_mfma_f32_16x16x32_bf16 v[32:35], v[200:203], a[136:139], v[32:35]\n"
        "buffer_store_dword v239, v238, s[16:19], s38 offen offset:384 sc1\n"
        "v_mfma_f32_16x16x32_bf16 v[32:35], v[196:199], a[132:135], v[32:35]\n"
        "v_cvt_pk_bf16_f32 v220, v116, v117\n"
        "v_cvt_pk_bf16_f32 v221, v118, v119\n"
        "v_mfma_f32_16x16x32_bf16 v[32:35], v[184:187], a[128:131], v[32:35]\n"
        "v_cvt_pk_bf16_f32 v226, v116, v117\n"
        "v_cvt_pk_bf16_f32 v227, v118, v119\n"
        "v_mfma_f32_16x16x32_bf16 v[32:35], v[180:183], a[124:127], v[32:35]\n"
        "v_mfma_f32_16x16x32_bf16 v[32:35], v[176:179], a[120:123], v[32:35]\n"
        "buffer_store_dword v255, v238, s[16:19], s38 offen offset:768 sc1\n"
        "v_mfma_f32_16x16x32_bf16 v[0:3], v[152:155], v[216:219], v[48:51]\n"
        ".long 0xbec200ff\n"
        "48:\n"
        "s_branch 62b\n"
        "v_mfma_f32_16x16x32_bf16 v[0:3], v[168:171], v[220:223], v[0:3]\n"
        "s_nop 2\n"
        "v_mfma_f32_16x16x32_bf16 v[4:7], v[172:175], v[216:219], v[52:55]\n"
        "s_nop 2\n"
        "v_mfma_f32_16x16x32_bf16 v[4:7], v[188:191], v[224:227], v[4:7]\n"
        "buffer_store_dword v244, v238, s[16:19], s38 offen offset:1152 sc1\n"
        "v_mfma_f32_16x16x32_bf16 v[28:31], v[176:179], a[96:99], 0\n"
        "ds_read_b128 v[222:225], v252 offset:4608\n"
        "s_nop 0\n"
        "v_mfma_f32_16x16x32_bf16 v[28:31], v[180:183], a[100:103], v[28:31]\n"
        "v_exp_f32 v0, v0\n"
        "v_mfma_f32_16x16x32_bf16 v[28:31], v[184:187], a[104:107], v[28:31]\n"
        "v_exp_f32 v1, v1\n"
        "v_mfma_f32_16x16x32_bf16 v[28:31], v[196:199], a[108:111], v[28:31]\n"
        "v_exp_f32 v2, v2\n"
        "v_mfma_f32_16x16x32_bf16 v[28:31], v[200:203], a[112:115], v[28:31]\n"
        "v_cvt_pk_bf16_f32 v88, v0, v1\n"
        "v_mfma_f32_16x16x32_bf16 v[28:31], v[204:207], a[116:119], v[28:31]\n"
        "v_exp_f32 v4, v4\n"
        "v_mfma_f32_16x16x32_bf16 v[36:39], v[204:207], a[164:167], 0\n"
        "v_exp_f32 v3, v3\n"
        "v_mfma_f32_16x16x32_bf16 v[36:39], v[200:203], a[160:163], v[36:39]\n"
        "v_exp_f32 v5, v5\n"
        "v_mfma_f32_16x16x32_bf16 v[36:39], v[196:199], a[156:159], v[36:39]\n"
        "v_cvt_pk_bf16_f32 v89, v2, v3\n"
        "v_cvt_pk_bf16_f32 v90, v4, v5\n"
        "v_mfma_f32_16x16x32_bf16 v[36:39], v[184:187], a[152:155], v[36:39]\n"
        "v_exp_f32 v6, v6\n"
        "v_mfma_f32_16x16x32_bf16 v[36:39], v[180:183], a[148:151], v[36:39]\n"
        "v_exp_f32 v7, v7\n"
        "v_mfma_f32_16x16x32_bf16 v[36:39], v[176:179], a[144:147], v[36:39]\n"
        "v_cvt_pk_bf16_f32 v91, v6, v7\n"
        "v_mov_b64 v[96:97], 0\n"
        "v_mfma_f32_16x16x32_bf16 v[8:11], v[192:195], v[216:219], v[56:59]\n"
        "s_waitcnt lgkmcnt(0)\n"
        "v_cvt_pk_bf16_f32 v234, v24, v28\n"
        "v_cvt_pk_bf16_f32 v242, v25, v29\n"
        "v_mfma_f32_16x16x32_bf16 v[8:11], v[208:211], v[220:223], v[8:11]\n"
        "v_cvt_pk_bf16_f32 v232, v26, v30\n"
        "s_nop 0\n"
        "v_mfma_f32_16x16x32_bf16 v[44:47], v[176:179], a[192:195], 0\n"
        "v_cvt_pk_bf16_f32 v235, v32, v36\n"
        "v_cvt_pk_bf16_f32 v243, v33, v37\n"
        "v_mfma_f32_16x16x32_bf16 v[44:47], v[180:183], a[196:199], v[44:47]\n"
        "buffer_store_dwordx2 v[234:235], v250, s[16:19], s39 offen offset:0 sc1\n"
        "v_cvt_pk_bf16_f32 v233, v34, v38\n"
        "v_mfma_f32_16x16x32_bf16 v[44:47], v[184:187], a[200:203], v[44:47]\n"
        "v_exp_f32 v8, v8\n"
        "v_mfma_f32_16x16x32_bf16 v[44:47], v[196:199], a[204:207], v[44:47]\n"
        "v_exp_f32 v9, v9\n"
        "v_mfma_f32_16x16x32_bf16 v[44:47], v[200:203], a[208:211], v[44:47]\n"
        "buffer_store_dwordx2 v[242:243], v250, s[16:19], s39 offen offset:384 sc1\n"
        "v_cvt_pk_bf16_f32 v92, v8, v9\n"
        "v_mfma_f32_16x16x32_bf16 v[44:47], v[204:207], a[212:215], v[44:47]\n"
        "v_exp_f32 v10, v10\n"
        "v_mfma_f32_16x16x32_bf16 v[12:15], v[212:215], v[216:219], v[60:63]\n"
        "v_exp_f32 v11, v11\n"
        "v_mfma_f32_16x16x32_bf16 v[12:15], v[228:231], v[224:227], v[12:15]\n"
        "buffer_store_dwordx2 v[232:233], v250, s[16:19], s39 offen offset:768 sc1\n"
        "v_cvt_pk_bf16_f32 v234, v27, v31\n"
        "v_smfmac_f32_16x16x64_bf16 v[96:99], a[252:255], v[80:87], v237\n"
        "v_cvt_pk_bf16_f32 v235, v35, v39\n"
        "v_mfma_f32_16x16x32_bf16 v[72:75], v[132:135], v[84:87], 0\n"
        "s_barrier\n"
        "ds_read_b128 a[0:3], v240 offset:0\n"
        "ds_read_b128 a[4:7], v240 offset:4096\n"
        "v_mfma_f32_16x16x32_bf16 v[72:75], v[128:131], v[80:83], v[72:75]\n"
        "buffer_store_dwordx2 v[234:235], v250, s[16:19], s39 offen offset:1152 sc1\n"
        "ds_read_b128 a[8:11], v240 offset:8192\n"
        "v_mfma_f32_16x16x32_bf16 v[76:79], v[136:139], v[80:83], 0\n"
        "ds_read_b128 a[12:15], v240 offset:12288\n"
        "ds_read_b128 a[16:19], v240 offset:16384\n"
        "v_mfma_f32_16x16x32_bf16 v[76:79], v[140:143], v[84:87], v[76:79]\n"
        "ds_read_b128 a[20:23], v240 offset:20480\n"
        "v_cvt_pk_bf16_f32 v93, v10, v11\n"
        "v_mfma_f32_16x16x32_bf16 v[100:103], v[148:151], v[84:87], 0\n"
        "v_exp_f32 v12, v12\n"
        "v_mfma_f32_16x16x32_bf16 v[100:103], v[144:147], v[80:83], v[100:103]\n"
        "v_exp_f32 v13, v13\n"
        "v_mfma_f32_16x16x32_bf16 v[40:43], v[204:207], a[188:191], 0\n"
        "v_exp_f32 v14, v14\n"
        "v_mfma_f32_16x16x32_bf16 v[40:43], v[200:203], a[184:187], v[40:43]\n"
        "v_cvt_pk_bf16_f32 v94, v12, v13\n"
        "v_mov_b64 v[124:125], 0\n"
        "v_mfma_f32_16x16x32_bf16 v[40:43], v[196:199], a[180:183], v[40:43]\n"
        "v_exp_f32 v15, v15\n"
        "v_mfma_f32_16x16x32_bf16 v[40:43], v[184:187], a[176:179], v[40:43]\n"
        "v_cvt_pk_bf16_f32 v95, v14, v15\n"
        "v_add_f32 v97, v96, v97\n"
        "v_mfma_f32_16x16x32_bf16 v[40:43], v[180:183], a[172:175], v[40:43]\n"
        "v_rcp_f32 v96, v97\n"
        "v_mfma_f32_16x16x32_bf16 v[40:43], v[176:179], a[168:171], v[40:43]\n"
        "s_waitcnt lgkmcnt(5)\n"
        "v_mul_f32 v72, v96, v72\n"
        "v_mul_f32 v73, v96, v73\n"
        "v_mfma_f32_16x16x32_bf16 v[156:159], a[0:3], a[72:75], 0\n"
        "s_waitcnt lgkmcnt(4)\n"
        "v_mul_f32 v74, v96, v74\n"
        "v_mul_f32 v75, v96, v75\n"
        "v_mfma_f32_16x16x32_bf16 v[156:159], a[4:7], a[76:79], v[156:159]\n"
        "s_waitcnt lgkmcnt(3)\n"
        "v_cvt_pk_bf16_f32 v72, v72, v73\n"
        "v_cvt_pk_bf16_f32 v73, v74, v75\n"
        "v_mfma_f32_16x16x32_bf16 v[156:159], a[8:11], a[80:83], v[156:159]\n"
        "s_waitcnt lgkmcnt(2)\n"
        "v_cvt_pk_bf16_f32 v239, v40, v44\n"
        "v_cvt_pk_bf16_f32 v255, v41, v45\n"
        "v_mfma_f32_16x16x32_bf16 v[156:159], a[12:15], a[84:87], v[156:159]\n"
        "s_waitcnt lgkmcnt(1)\n"
        "buffer_store_dword v239, v238, s[16:19], s39 offen offset:0 sc1\n"
        "v_mfma_f32_16x16x32_bf16 v[156:159], a[16:19], a[88:91], v[156:159]\n"
        "s_waitcnt lgkmcnt(0)\n"
        "v_cvt_pk_bf16_f32 v244, v42, v46\n"
        "v_cvt_pk_bf16_f32 v239, v43, v47\n"
        "v_mfma_f32_16x16x32_bf16 v[156:159], a[20:23], a[92:95], v[156:159]\n"
        "buffer_store_dword v255, v238, s[16:19], s39 offen offset:384 sc1\n"
        "v_smfmac_f32_16x16x64_bf16 v[124:127], a[252:255], v[88:95], v237\n"
        "v_mul_f32 v76, v96, v76\n"
        "v_mfma_f32_16x16x32_bf16 v[104:107], v[132:135], v[92:95], 0\n"
        "buffer_store_dword v244, v238, s[16:19], s39 offen offset:768 sc1\n"
        "v_mfma_f32_16x16x32_bf16 v[104:107], v[128:131], v[88:91], v[104:107]\n"
        "v_mul_f32 v77, v96, v77\n"
        "v_mul_f32 v78, v96, v78\n"
        "v_mfma_f32_16x16x32_bf16 v[108:111], v[136:139], v[88:91], 0\n"
        "buffer_store_dword v239, v238, s[16:19], s39 offen offset:1152 sc1\n"
        "v_mfma_f32_16x16x32_bf16 v[108:111], v[140:143], v[92:95], v[108:111]\n"
        "v_mul_f32 v79, v96, v79\n"
        "v_add_f32 v125, v124, v125\n"
        "v_mfma_f32_16x16x32_bf16 v[112:115], v[148:151], v[92:95], 0\n"
        "v_cvt_pk_bf16_f32 v74, v76, v77\n"
        "v_cvt_pk_bf16_f32 v75, v78, v79\n"
        "v_mfma_f32_16x16x32_bf16 v[112:115], v[144:147], v[88:91], v[112:115]\n"
        "ds_write_b128 v245, v[72:75] offset:512\n"
        "v_mfma_f32_16x16x32_bf16 v[176:179], a[20:23], a[164:167], 0\n"
        "v_rcp_f32 v124, v125\n"
        "v_mfma_f32_16x16x32_bf16 v[176:179], a[16:19], a[160:163], v[176:179]\n"
        "v_mul_f32 v100, v96, v100\n"
        "v_mul_f32 v101, v96, v101\n"
        "v_mfma_f32_16x16x32_bf16 v[176:179], a[12:15], a[156:159], v[176:179]\n"
        "v_mul_f32 v102, v96, v102\n"
        "v_mul_f32 v103, v96, v103\n"
        "v_mfma_f32_16x16x32_bf16 v[176:179], a[8:11], a[152:155], v[176:179]\n"
        "v_mul_f32 v104, v124, v104\n"
        "v_mul_f32 v105, v124, v105\n"
        "v_mfma_f32_16x16x32_bf16 v[176:179], a[4:7], a[148:151], v[176:179]\n"
        "v_mul_f32 v106, v124, v106\n"
        "v_mul_f32 v107, v124, v107\n"
        "v_mfma_f32_16x16x32_bf16 v[176:179], a[0:3], a[144:147], v[176:179]\n"
        "v_cvt_pk_bf16_f32 v100, v100, v101\n"
        "v_cvt_pk_bf16_f32 v101, v102, v103\n"
        "v_mfma_f32_16x16x32_bf16 v[160:163], a[0:3], a[96:99], 0\n"
        "ds_write_b64 v246, v[100:101] offset:512\n"
        "v_mfma_f32_16x16x32_bf16 v[160:163], a[4:7], a[100:103], v[160:163]\n"
        "v_cvt_pk_bf16_f32 v104, v104, v105\n"
        "v_cvt_pk_bf16_f32 v105, v106, v107\n"
        "v_mfma_f32_16x16x32_bf16 v[160:163], a[8:11], a[104:107], v[160:163]\n"
        "v_mul_f32 v108, v124, v108\n"
        "v_mul_f32 v109, v124, v109\n"
        "v_mfma_f32_16x16x32_bf16 v[160:163], a[12:15], a[108:111], v[160:163]\n"
        "v_mul_f32 v110, v124, v110\n"
        "v_mul_f32 v111, v124, v111\n"
        "v_mfma_f32_16x16x32_bf16 v[160:163], a[16:19], a[112:115], v[160:163]\n"
        "v_max3_u32 v236, v236, v96, v97\n"
        "v_cvt_pk_bf16_f32 v106, v108, v109\n"
        "v_mfma_f32_16x16x32_bf16 v[160:163], a[20:23], a[116:119], v[160:163]\n"
        "v_cvt_pk_bf16_f32 v107, v110, v111\n"
        "v_mul_f32 v112, v124, v112\n"
        "v_mfma_f32_16x16x32_bf16 v[164:167], a[20:23], a[140:143], 0\n"
        "ds_write_b128 v245, v[104:107] offset:768\n"
        "v_mfma_f32_16x16x32_bf16 v[164:167], a[16:19], a[136:139], v[164:167]\n"
        "v_mul_f32 v113, v124, v113\n"
        "v_mul_f32 v114, v124, v114\n"
        "v_mfma_f32_16x16x32_bf16 v[164:167], a[12:15], a[132:135], v[164:167]\n"
        "v_mul_f32 v115, v124, v115\n"
        "v_cvt_pk_bf16_f32 v112, v112, v113\n"
        "v_mfma_f32_16x16x32_bf16 v[164:167], a[8:11], a[128:131], v[164:167]\n"
        "v_cvt_pk_bf16_f32 v113, v114, v115\n"
        "v_max3_u32 v236, v236, v124, v125\n"
        "v_mfma_f32_16x16x32_bf16 v[164:167], a[4:7], a[124:127], v[164:167]\n"
        "ds_write_b64 v246, v[112:113] offset:768\n"
        "v_mfma_f32_16x16x32_bf16 v[164:167], a[0:3], a[120:123], v[164:167]\n"
        "v_cmp_lt_u32 vcc, s24, v236\n"
        "v_cvt_pk_bf16_f32 v242, v156, v160\n"
        "v_mfma_f32_16x16x32_bf16 v[180:183], a[0:3], a[168:171], 0\n"
        "s_cmp_lg_u64 vcc, 0\n"
        "s_addc_u32 s25, s25, s25\n"
        "v_mov_b32 v253, s25\n"
        "v_mfma_f32_16x16x32_bf16 v[180:183], a[4:7], a[172:175], v[180:183]\n"
        "ds_write_b32 v249, v253\n"
        "v_mfma_f32_16x16x32_bf16 v[180:183], a[8:11], a[176:179], v[180:183]\n"
        "v_cvt_pk_bf16_f32 v243, v164, v176\n"
        "v_cvt_pk_bf16_f32 v232, v157, v161\n"
        "v_mfma_f32_16x16x32_bf16 v[180:183], a[12:15], a[180:183], v[180:183]\n"
        "buffer_store_dwordx2 v[242:243], v250, s[16:19], s47 offen offset:0 sc1\n"
        "v_cvt_pk_bf16_f32 v233, v165, v177\n"
        "v_mfma_f32_16x16x32_bf16 v[180:183], a[16:19], a[184:187], v[180:183]\n"
        "v_cvt_pk_bf16_f32 v234, v158, v162\n"
        "v_cvt_pk_bf16_f32 v235, v166, v178\n"
        "v_mfma_f32_16x16x32_bf16 v[180:183], a[20:23], a[188:191], v[180:183]\n"
        "s_waitcnt lgkmcnt(0)\n"
        "s_barrier\n"
        "ds_read_b128 a[24:27], v240 offset:512\n"
        "ds_read_b128 a[28:31], v240 offset:4608\n"
        "v_mfma_f32_16x16x32_bf16 v[184:187], a[20:23], a[212:215], 0\n"
        "buffer_store_dwordx2 v[232:233], v250, s[16:19], s47 offen offset:384 sc1\n"
        "ds_read_b128 a[32:35], v240 offset:8704\n"
        "v_mfma_f32_16x16x32_bf16 v[184:187], a[16:19], a[208:211], v[184:187]\n"
        "ds_read_b128 a[36:39], v240 offset:12800\n"
        "ds_read_b128 a[40:43], v240 offset:16896\n"
        "v_mfma_f32_16x16x32_bf16 v[184:187], a[12:15], a[204:207], v[184:187]\n"
        "ds_read_b128 a[44:47], v240 offset:20992\n"
        "v_cvt_pk_bf16_f32 v242, v159, v163\n"
        "v_mfma_f32_16x16x32_bf16 v[184:187], a[8:11], a[200:203], v[184:187]\n"
        "buffer_store_dwordx2 v[234:235], v250, s[16:19], s47 offen offset:768 sc1\n"
        "v_cvt_pk_bf16_f32 v243, v167, v179\n"
        "v_mfma_f32_16x16x32_bf16 v[184:187], a[4:7], a[196:199], v[184:187]\n"
        "v_mfma_f32_16x16x32_bf16 v[184:187], a[0:3], a[192:195], v[184:187]\n"
        "s_waitcnt lgkmcnt(5)\n"
        "s_nop 1\n"
        "v_mfma_f32_16x16x32_bf16 v[52:55], a[24:27], a[168:171], 0\n"
        "s_waitcnt lgkmcnt(4)\n"
        "buffer_store_dwordx2 v[242:243], v250, s[16:19], s47 offen offset:1152 sc1\n"
        "s_nop 0\n"
        "v_mfma_f32_16x16x32_bf16 v[52:55], a[28:31], a[172:175], v[52:55]\n"
        "s_waitcnt lgkmcnt(3)\n"
        "ds_read_b128 v[60:63], v241\n"
        "s_nop 0\n"
        "v_mfma_f32_16x16x32_bf16 v[52:55], a[32:35], a[176:179], v[52:55]\n"
        "s_waitcnt lgkmcnt(3)\n"
        "v_cvt_pk_bf16_f32 v255, v180, v184\n"
        "v_cvt_pk_bf16_f32 v244, v181, v185\n"
        "v_mfma_f32_16x16x32_bf16 v[52:55], a[36:39], a[180:183], v[52:55]\n"
        "s_waitcnt lgkmcnt(2)\n"
        "buffer_store_dword v255, v238, s[16:19], s47 offen offset:0 sc1\n"
        "v_mfma_f32_16x16x32_bf16 v[52:55], a[40:43], a[184:187], v[52:55]\n"
        "s_waitcnt lgkmcnt(1)\n"
        "v_cvt_pk_bf16_f32 v239, v182, v186\n"
        "v_cvt_pk_bf16_f32 v255, v183, v187\n"
        "v_mfma_f32_16x16x32_bf16 v[52:55], a[44:47], a[188:191], v[52:55]\n"
        "v_mfma_f32_16x16x32_bf16 v[196:199], a[44:47], a[92:95], 0\n"
        "buffer_store_dword v244, v238, s[16:19], s47 offen offset:384 sc1\n"
        "v_mfma_f32_16x16x32_bf16 v[196:199], a[40:43], a[88:91], v[196:199]\n"
        "v_mfma_f32_16x16x32_bf16 v[196:199], a[36:39], a[84:87], v[196:199]\n"
        "v_mfma_f32_16x16x32_bf16 v[196:199], a[32:35], a[80:83], v[196:199]\n"
        "buffer_store_dword v239, v238, s[16:19], s47 offen offset:768 sc1\n"
        "v_mfma_f32_16x16x32_bf16 v[196:199], a[28:31], a[76:79], v[196:199]\n"
        "v_mfma_f32_16x16x32_bf16 v[196:199], a[24:27], a[72:75], v[196:199]\n"
        "v_mfma_f32_16x16x32_bf16 v[56:59], a[24:27], a[192:195], 0\n"
        "buffer_store_dword v255, v238, s[16:19], s47 offen offset:1152 sc1\n"
        "v_mfma_f32_16x16x32_bf16 v[56:59], a[28:31], a[196:199], v[56:59]\n"
        "v_mfma_f32_16x16x32_bf16 v[56:59], a[32:35], a[200:203], v[56:59]\n"
        "v_mfma_f32_16x16x32_bf16 v[56:59], a[36:39], a[204:207], v[56:59]\n"
        "s_waitcnt lgkmcnt(0)\n"
        "v_or3_b32 v60, v60, v61, v62\n"
        "v_or_b32 v60, v60, v63\n"
        "v_mfma_f32_16x16x32_bf16 v[56:59], a[40:43], a[208:211], v[56:59]\n"
        "s_nop 0\n"
        "v_readfirstlane_b32 s25, v60\n"
        "v_mfma_f32_16x16x32_bf16 v[56:59], a[44:47], a[212:215], v[56:59]\n"
        "s_nop 2\n"
        "v_mfma_f32_16x16x32_bf16 v[204:207], a[44:47], a[140:143], 0\n"
        "s_nop 2\n"
        "v_mfma_f32_16x16x32_bf16 v[204:207], a[40:43], a[136:139], v[204:207]\n"
        "v_cvt_pk_bf16_f32 v88, v52, v56\n"
        "v_cvt_pk_bf16_f32 v90, v53, v57\n"
        "v_mfma_f32_16x16x32_bf16 v[204:207], a[36:39], a[132:135], v[204:207]\n"
        "v_cvt_pk_bf16_f32 v92, v54, v58\n"
        "v_cvt_pk_bf16_f32 v94, v55, v59\n"
        "v_mfma_f32_16x16x32_bf16 v[204:207], a[32:35], a[128:131], v[204:207]\n"
        "buffer_store_dword v88, v238, s[16:19], s35 offen offset:0 sc1\n"
        "v_mfma_f32_16x16x32_bf16 v[204:207], a[28:31], a[124:127], v[204:207]\n"
        "v_mfma_f32_16x16x32_bf16 v[204:207], a[24:27], a[120:123], v[204:207]\n"
        "buffer_store_dword v90, v238, s[16:19], s35 offen offset:384 sc1\n"
        "v_mfma_f32_16x16x32_bf16 v[200:203], a[24:27], a[96:99], 0\n"
        "v_mfma_f32_16x16x32_bf16 v[200:203], a[28:31], a[100:103], v[200:203]\n"
        "buffer_store_dword v92, v238, s[16:19], s35 offen offset:768 sc1\n"
        "v_mfma_f32_16x16x32_bf16 v[200:203], a[32:35], a[104:107], v[200:203]\n"
        "v_mfma_f32_16x16x32_bf16 v[200:203], a[36:39], a[108:111], v[200:203]\n"
        "buffer_store_dword v94, v238, s[16:19], s35 offen offset:1152 sc1\n"
        "v_mfma_f32_16x16x32_bf16 v[200:203], a[40:43], a[112:115], v[200:203]\n"
        "v_mfma_f32_16x16x32_bf16 v[200:203], a[44:47], a[116:119], v[200:203]\n"
        "v_mfma_f32_16x16x32_bf16 v[48:51], a[44:47], a[164:167], 0\n"
        "v_mfma_f32_16x16x32_bf16 v[48:51], a[40:43], a[160:163], v[48:51]\n"
        "s_nop 2\n"
        "v_mfma_f32_16x16x32_bf16 v[48:51], a[36:39], a[156:159], v[48:51]\n"
        "s_nop 1\n"
        "v_cvt_pk_bf16_f32 v80, v196, v200\n"
        "v_mfma_f32_16x16x32_bf16 v[48:51], a[32:35], a[152:155], v[48:51]\n"
        "v_cvt_pk_bf16_f32 v82, v197, v201\n"
        "v_mfma_f32_16x16x32_bf16 v[48:51], a[28:31], a[148:151], v[48:51]\n"
        "v_cvt_pk_bf16_f32 v84, v198, v202\n"
        "v_mfma_f32_16x16x32_bf16 v[48:51], a[24:27], a[144:147], v[48:51]\n"
        "s_nop 2\n"
        "v_cvt_pk_bf16_f32 v86, v199, v203\n"
        "s_nop 3\n"
        "v_cvt_pk_bf16_f32 v81, v204, v48\n"
        "buffer_store_dwordx2 v[80:81], v250, s[16:19], s35 offen offset:0 sc1\n"
        "v_cvt_pk_bf16_f32 v83, v205, v49\n"
        "buffer_store_dwordx2 v[82:83], v250, s[16:19], s35 offen offset:384 sc1\n"
        "v_cvt_pk_bf16_f32 v85, v206, v50\n"
        "buffer_store_dwordx2 v[84:85], v250, s[16:19], s35 offen offset:768 sc1\n"
        "v_cvt_pk_bf16_f32 v87, v207, v51\n"
        "buffer_store_dwordx2 v[86:87], v250, s[16:19], s35 offen offset:1152 sc1\n"
        "s_cmp_lg_u32 s25, 0\n"
        "s_cbranch_scc1 30f\n"
        "s_endpgm\n"
        "30:\n"
        : "={s25}"(flg), "={s42}"(w), "+{s[0:1]}"(ka), "+{s2}"(bid), "+{v0}"(tid)
        : "s"(ldsb)
        : "v1", "v2", "v3", "v4", "v5", "v6", "v7", "v8", "v9", "v10", "v11", "v12", "v13", "v14", "v15", "v16", "v17", "v18", "v19", "v20", "v21", "v22", "v23", "v24", "v25", "v26", "v27", "v28", "v29", "v30", "v31", "v32", "v33", "v34", "v35", "v36", "v37", "v38", "v39", "v40", "v41", "v42", "v43", "v44", "v45", "v46", "v47", "v48", "v49", "v50", "v51", "v52", "v53", "v54", "v55", "v56", "v57", "v58", "v59", "v60", "v61", "v62", "v63", "v64", "v65", "v66", "v67", "v68", "v69", "v70", "v71", "v72", "v73", "v74", "v75", "v76", "v77", "v78", "v79", "v80", "v81", "v82", "v83", "v84", "v85", "v86", "v87", "v88", "v89", "v90", "v91", "v92", "v93", "v94", "v95", "v96", "v97", "v98", "v99", "v100", "v101", "v102", "v103", "v104", "v105", "v106", "v107", "v108", "v109", "v110", "v111", "v112", "v113", "v114", "v115", "v116", "v117", "v118", "v119", "v120", "v121", "v122", "v123", "v124", "v125", "v126", "v127", "v128", "v129", "v130", "v131", "v132", "v133", "v134", "v135", "v136", "v137", "v138", "v139", "v140", "v141", "v142", "v143", "v144", "v145", "v146", "v147", "v148", "v149", "v150", "v151", "v152", "v153", "v154", "v155", "v156", "v157", "v158", "v159", "v160", "v161", "v162", "v163", "v164", "v165", "v166", "v167", "v168", "v169", "v170", "v171", "v172", "v173", "v174", "v175", "v176", "v177", "v178", "v179", "v180", "v181", "v182", "v183", "v184", "v185", "v186", "v187", "v188", "v189", "v190", "v191", "v192", "v193", "v194", "v195", "v196", "v197", "v198", "v199", "v200", "v201", "v202", "v203", "v204", "v205", "v206", "v207", "v208", "v209", "v210", "v211", "v212", "v213", "v214", "v215", "v216", "v217", "v218", "v219", "v220", "v221", "v222", "v223", "v224", "v225", "v226", "v227", "v228", "v229", "v230", "v231", "v232", "v233", "v234", "v235", "v236", "v237", "v238", "v239", "v240", "v241", "v242", "v243", "v244", "v245", "v246", "v247", "v248", "v249", "v250", "v251", "v252", "v253", "v254", "v255", "a0", "a1", "a2", "a3", "a4", "a5", "a6", "a7", "a8", "a9", "a10", "a11", "a12", "a13", "a14", "a15", "a16", "a17", "a18", "a19", "a20", "a21", "a22", "a23", "a24", "a25", "a26", "a27", "a28", "a29", "a30", "a31", "a32", "a33", "a34", "a35", "a36", "a37", "a38", "a39", "a40", "a41", "a42", "a43", "a44", "a45", "a46", "a47", "a48", "a49", "a50", "a51", "a52", "a53", "a54", "a55", "a56", "a57", "a58", "a59", "a60", "a61", "a62", "a63", "a64", "a65", "a66", "a67", "a68", "a69", "a70", "a71", "a72", "a73", "a74", "a75", "a76", "a77", "a78", "a79", "a80", "a81", "a82", "a83", "a84", "a85", "a86", "a87", "a88", "a89", "a90", "a91", "a92", "a93", "a94", "a95", "a96", "a97", "a98", "a99", "a100", "a101", "a102", "a103", "a104", "a105", "a106", "a107", "a108", "a109", "a110", "a111", "a112", "a113", "a114", "a115", "a116", "a117", "a118", "a119", "a120", "a121", "a122", "a123", "a124", "a125", "a126", "a127", "a128", "a129", "a130", "a131", "a132", "a133", "a134", "a135", "a136", "a137", "a138", "a139", "a140", "a141", "a142", "a143", "a144", "a145", "a146", "a147", "a148", "a149", "a150", "a151", "a152", "a153", "a154", "a155", "a156", "a157", "a158", "a159", "a160", "a161", "a162", "a163", "a164", "a165", "a166", "a167", "a168", "a169", "a170", "a171", "a172", "a173", "a174", "a175", "a176", "a177", "a178", "a179", "a180", "a181", "a182", "a183", "a184", "a185", "a186", "a187", "a188", "a189", "a190", "a191", "a192", "a193", "a194", "a195", "a196", "a197", "a198", "a199", "a200", "a201", "a202", "a203", "a204", "a205", "a206", "a207", "a208", "a209", "a210", "a211", "a212", "a213", "a214", "a215", "a216", "a217", "a218", "a219", "a220", "a221", "a222", "a223", "a224", "a225", "a226", "a227", "a228", "a229", "a230", "a231", "a232", "a233", "a234", "a235", "a236", "a237", "a238", "a239", "a240", "a241", "a242", "a243", "a244", "a245", "a246", "a247", "a248", "a249", "a250", "a251", "a252", "a253", "a254", "a255", "s8", "s9", "s10", "s11", "s12", "s13", "s14", "s15", "s16", "s17", "s18", "s19", "s20", "s21", "s22", "s23", "s24", "s26", "s27", "s28", "s29", "s30", "s31", "s34", "s35", "s36", "s37", "s38", "s39", "s40", "s41", "s43", "s44", "s45", "s46", "s47", "s48", "s49", "s50", "s51", "s52", "s53", "s54", "s55", "s56", "s57", "s58", "s59", "s60", "s61", "s62", "s63", "s64", "s65", "s66", "s67", "s68", "s69", "s70", "s71", "s72", "s73", "s74", "s75", "s76", "s77", "s78", "s79", "s80", "s81", "s82", "s83", "vcc", "scc", "memory");
    if (flg) {
        unsigned ln;
        asm volatile("s_waitcnt vmcnt(0)\n v_mbcnt_lo_u32_b32 %0, -1, 0\n v_mbcnt_hi_u32_b32 %0, -1, %0" : "=v"(ln) :: "memory");
        const unsigned t = w * 64u + ln, wx = bid & 63u, wy0 = bid >> 6;
        typedef const char* __attribute__((address_space(4))) const* kargp;
        const char *x = ((kargp)ka)[0], *wqkv = ((kargp)ka)[1], *wo = ((kargp)ka)[2], *rpb = ((kargp)ka)[3], *rpi = ((kargp)ka)[4],
                   *mask = ((kargp)ka)[5];
        char* out = (char*)((kargp)ka)[6];
        unsigned short* xs = (unsigned short*)smem;
        unsigned short* qs = xs + 64 * 192;
        const unsigned short* W1 = (const unsigned short*)wqkv;
        const unsigned short* W2 = (const unsigned short*)wo;
        for (unsigned i = 0; i < 16u; ++i) {
            if (!((flg >> (15u - i)) & 1u)) continue;
            const unsigned wy = wy0 + 4u * i;
            __syncthreads();
            for (unsigned e = t; e < 64u * 96u; e += 256u) {
                const unsigned tok = e / 96u, c2 = e % 96u;
                const unsigned pix = ((8u * wy + 4u + (tok >> 3)) & 511u) * 512u + ((8u * wx + 4u + (tok & 7u)) & 511u);
                ((unsigned*)xs)[e] = ((const unsigned*)x)[pix * 96u + c2];
            }
            __syncthreads();
            for (unsigned col = t; col < 576u; col += 256u)
                for (unsigned tb = 0; tb < 64u; tb += 8u) {
                    float a[8] = {0.f, 0.f, 0.f, 0.f, 0.f, 0.f, 0.f, 0.f};
                    for (unsigned c = 0; c < 192u; ++c) {
                        const float wt = bf2f(W1[c * 576u + col]);
#pragma unroll
                        for (int j = 0; j < 8; ++j) a[j] += bf2f(xs[(tb + j) * 192u + c]) * wt;
                    }
#pragma unroll
                    for (int j = 0; j < 8; ++j) qs[(tb + j) * 578u + col] = f2bf(a[j]);
                }
            __syncthreads();
            {
                float qv[48], o[48];
#pragma unroll
                for (int d = 0; d < 48; ++d) { qv[d] = bf2f(qs[ln * 578u + w * 48u + d]); o[d] = 0.f; }
                const unsigned short* mrow = (const unsigned short*)mask + (size_t)(wy * 64u + wx) * 4096u + ln * 64u;
                float mx = -__builtin_inff(), l = 0.f;
                for (int pass = 0; pass < 2; ++pass)
                    for (unsigned k = 0; k < 64u; ++k) {
                        float s = 0.f;
#pragma unroll
                        for (int d = 0; d < 48; ++d) s += qv[d] * bf2f(qs[k * 578u + 192u + w * 48u + d]);
                        s = s * 0.14433756729740643f + bf2f(((const unsigned short*)rpb)[((const unsigned*)rpi)[ln * 64u + k] * 4u + w]) + bf2f(mrow[k]);
                        if (pass == 0) mx = fmaxf(mx, s);
                        else {
                            const float p = __expf(s - mx);
                            l += p;
#pragma unroll
                            for (int d = 0; d < 48; ++d) o[d] += p * bf2f(qs[k * 578u + 384u + w * 48u + d]);
                        }
                    }
#pragma unroll
                for (int d = 0; d < 48; ++d) xs[ln * 192u + w * 48u + d] = f2bf(o[d] / l);
            }
            __syncthreads();
            if (t < 192u)
                for (unsigned tb = 0; tb < 64u; tb += 8u) {
                    float a[8] = {0.f, 0.f, 0.f, 0.f, 0.f, 0.f, 0.f, 0.f};
                    for (unsigned c = 0; c < 192u; ++c) {
                        const float wt = bf2f(W2[c * 192u + t]);
#pragma unroll
                        for (int j = 0; j < 8; ++j) a[j] += bf2f(xs[(tb + j) * 192u + c]) * wt;
                    }
                    const unsigned row = (8u * wy + 4u + (tb >> 3)) & 511u;
#pragma unroll
                    for (int j = 0; j < 8; ++j)
                        ((unsigned short*)out)[(row * 512u + ((8u * wx + 4u + j) & 511u)) * 192u + t] = f2bf(a[j]);
                }
        }
    }
}


extern "C" void kernel_launch(void* const* d_in, const int* in_sizes, int n_in, void* d_out, int out_size, void* d_ws,
                              size_t ws_size, hipStream_t stream) {
    win_attn_kernel<<<256, 256, 0, stream>>>((const char*)d_in[0], (const char*)d_in[1], (const char*)d_in[2],
                                             (const char*)d_in[3], (const char*)d_in[4], (const char*)d_in[5],
                                             (char*)d_out, (char*)d_ws);

}
```

```cpp
#include <hip/hip_runtime.h>
#include <hip/hip_bf16.h>

typedef unsigned int u32x4 __attribute__((ext_vector_type(4)));

static __device__ __forceinline__ float bf2f(unsigned short u) { return __uint_as_float((unsigned)u << 16); }
static __device__ __forceinline__ unsigned short f2bf(float f) { return __builtin_bit_cast(unsigned short, (__bf16)f); }

__global__ __launch_bounds__(256, 1) __attribute__((aligned(4096))) void win_attn_kernel(const char* x_, const char* wqkv_, const char* wo_, const char* rpb_,
                                                          const char* rpi_, const char* mask_, char* out_, char* ws) {
    __shared__ __attribute__((aligned(16))) char smem[163840];
    const void* ka = (const void*)__builtin_amdgcn_kernarg_segment_ptr();
    unsigned bid = blockIdx.x, tid = threadIdx.x, flg, w;
    unsigned ldsb = (unsigned)(size_t)smem;
    asm volatile(
        "s_load_dwordx16 s[68:83], s[0:1], 0\n"
        "v_readfirstlane_b32 s42, v0\n"
        "v_and_b32 v1, 63, v0\n"
        "s_lshr_b32 s31, s2, 1\n"
        "s_lshr_b32 s42, s42, 6\n"
        "s_and_b32 s31, s31, 0x7c\n"
        "v_lshlrev_b32 v2, 4, v1\n"
        "s_add_u32 s31, s31, s42\n"
        "s_mul_i32 s35, s31, 320\n"
        "v_min_u32 v10, 4, v1\n"
        "s_getpc_b64 s[66:67]\n"
        "v_lshl_add_u32 v10, v10, 6, s35\n"
        "global_load_dword v60, v10, s[66:67]\n"
        "s_lshl_b32 s50, s31, 6\n"
        "s_mul_i32 s50, s50, 27\n"
        "v_min_u32 v136, 26, v1\n"
        "v_lshl_add_u32 v136, v136, 6, s50\n"
        "v_mul_u32_u24 v11, 171, v1\n"
        "v_lshrrev_b32 v4, 10, v11\n"
        "v_bfe_u32 v7, v11, 13, 1\n"
        "v_add_u32 v11, 10944, v11\n"
        "v_lshrrev_b32 v5, 10, v11\n"
        "v_bfe_u32 v8, v11, 13, 1\n"
        "v_add_u32 v11, 10944, v11\n"
        "v_lshrrev_b32 v6, 10, v11\n"
        "v_bfe_u32 v9, v11, 13, 1\n"
        "s_mul_i32 s40, s42, 3072\n"
        "s_mov_b32 s10, 100663296\n"
        "s_mov_b32 s11, 0x20000\n"
        "s_mov_b32 s58, 16384\n"
        "s_mov_b32 s59, 0x20000\n"
        "s_mov_b32 s62, 1800\n"
        "s_mov_b32 s63, 0x20000\n"
        "s_mov_b32 s22, 221184\n"
        "s_mov_b32 s23, 0x20000\n"
        "s_and_b32 s51, s42, 1\n"
        "v_lshlrev_b32 v134, 3, v0\n"
        "s_lshl_b32 s35, s51, 7\n"
        "v_and_b32 v10, 48, v1\n"
        "v_xor_b32 v131, s35, v2\n"
        "s_lshl_b32 s46, s42, 12\n"
        "v_xor_b32 v131, v131, v10\n"
        "v_xor_b32 v132, 64, v131\n"
        "s_and_b32 s31, s2, 63\n"
        "s_lshl_b32 s31, s31, 3\n"
        "s_add_u32 s31, s31, 4\n"
        "s_lshl_b32 s35, s42, 5\n"
        "s_movk_i32 s41, 384\n"
        "s_movk_i32 s45, -384\n"
        "v_lshrrev_b32 v10, 2, v4\n"
        "v_mad_i32_i24 v11, v10, s45, v2\n"
        "v_add_u32 v10, s31, v10\n"
        "v_lshl_or_b32 v242, v7, 4, s35\n"
        "v_and_b32 v10, 511, v10\n"
        "v_xor_b32 v11, v11, v242\n"
        "v_mad_u32_u24 v242, v10, s41, v11\n"
        "v_lshrrev_b32 v10, 2, v5\n"
        "v_mad_i32_i24 v11, v10, s45, v2\n"
        "v_add_u32 v10, s31, v10\n"
        "v_add_u32 v11, 1024, v11\n"
        "v_lshl_or_b32 v243, v8, 4, s35\n"
        "v_and_b32 v10, 511, v10\n"
        "v_xor_b32 v11, v11, v243\n"
        "v_mad_u32_u24 v243, v10, s41, v11\n"
        "v_lshrrev_b32 v10, 2, v6\n"
        "v_mad_i32_i24 v11, v10, s45, v2\n"
        "v_add_u32 v10, s31, v10\n"
        "v_add_u32 v11, 2048, v11\n"
        "v_lshl_or_b32 v244, v9, 4, s35\n"
        "v_and_b32 v10, 511, v10\n"
        "v_xor_b32 v11, v11, v244\n"
        "v_mad_u32_u24 v244, v10, s41, v11\n"
        "s_mul_i32 s43, s31, 384\n"
        "s_add_u32 s31, s31, 4\n"
        "s_and_b32 s31, s31, 511\n"
        "s_mul_i32 s44, s31, 384\n"
        "s_lshr_b32 s31, s2, 6\n"
        "s_lshl_b32 s31, s31, 3\n"
        "s_add_u32 s47, s31, s42\n"
        "s_add_u32 s47, s47, 68\n"
        "s_mul_i32 s47, s47, 196608\n"
        "s_lshl2_add_u32 s37, s51, s31\n"
        "s_add_u32 s37, s37, 4\n"
        "s_mul_i32 s37, s37, 196608\n"
        "s_mul_i32 s65, s42, 18432\n"
        "s_mov_b32 s14, 33554432\n"
        "s_mov_b32 s15, 0x20000\n"
        "s_mul_i32 s35, s42, 96\n"
        "s_movk_i32 s31, 1056\n"
        "s_movk_i32 s45, 4608\n"
        "v_add_u32 v3, s35, v2\n"
        "s_waitcnt lgkmcnt(0)\n"
        "s_mov_b64 s[60:61], s[74:75]\n"
        "s_mov_b64 s[20:21], s[70:71]\n"
        "s_mov_b64 s[56:57], s[76:77]\n"
        "s_mov_b64 s[8:9], s[68:69]\n"
        "buffer_load_dwordx2 v[144:145], v134, s[60:63], 0 offen\n"
        "s_add_u32 m0, s46, 122880\n"
        "s_mov_b32 s64, s46\n"
        "buffer_load_dword v136, s[20:23], 0 offen lds\n"
        "s_mov_b64 s[52:53], s[72:73]\n"
        "s_mov_b64 s[12:13], s[78:79]\n"
        "buffer_load_dwordx4 v131, s[56:59], s64 offen lds\n"
        "s_mov_b64 s[16:17], s[80:81]\n"
        "v_bfe_u32 v12, v4, 2, 1\n"
        "v_mad_u32_u24 v128, v4, s31, v3\n"
        "v_sub_u32 v12, v12, v7\n"
        "v_mad_i32_i24 v128, v12, s45, v128\n"
        "v_bfe_u32 v13, v5, 2, 1\n"
        "buffer_load_dwordx4 v131, s[56:59], s64 offen offset:1024 lds\n"
        "v_mad_u32_u24 v129, v5, s31, v3\n"
        "v_sub_u32 v13, v13, v8\n"
        "v_mad_i32_i24 v129, v13, s45, v129\n"
        "v_bfe_u32 v14, v6, 2, 1\n"
        "v_mad_u32_u24 v130, v6, s31, v3\n"
        "v_sub_u32 v14, v14, v9\n"
        "v_mad_i32_i24 v130, v14, s45, v130\n"
        "s_lshl_b32 s48, s42, 11\n"
        "s_lshl_b32 s36, s2, 13\n"
        "v_and_b32 v19, 16, v1\n"
        "buffer_load_dwordx4 v132, s[56:59], s64 offen offset:2048 lds\n"
        "s_add_u32 s36, s36, s48\n"
        "s_add_u32 s36, s36, 4194304\n"
        "s_add_u32 s48, s48, 155648\n"
        "s_lshl_b32 s35, s51, 6\n"
        "v_xor_b32 v249, v2, v19\n"
        "v_xor_b32 v249, s35, v249\n"
        "v_xor_b32 v253, 32, v249\n"
        "v_and_b32 v15, 15, v1\n"
        "v_lshrrev_b32 v16, 4, v1\n"
        "v_and_b32 v17, 12, v1\n"
        "buffer_load_dwordx4 v132, s[56:59], s64 offen offset:3072 lds\n"
        "v_and_b32 v18, 0xf0, v2\n"
        "v_add_u32 v17, v17, v15\n"
        "v_bfe_u32 v10, v1, 2, 3\n"
        "v_lshrrev_b32 v11, 5, v1\n"
        "v_lshl_or_b32 v10, v11, 4, v10\n"
        "v_and_b32 v255, 3, v1\n"
        "v_mul_u32_u24 v10, 96, v10\n"
        "v_lshl_add_u32 v255, v255, 3, s65\n"
        "v_add_u32 v255, v255, v10\n"
        "v_lshrrev_b32 v20, 1, v15\n"
        "s_add_u32 m0, s40, 73728\n"
        "s_sub_u32 s64, s47, 12582912\n"
        "buffer_load_dwordx4 v242, s[8:11], s64 offen lds\n"
        "v_xor_b32 v20, v20, v16\n"
        "v_lshlrev_b32 v20, 4, v20\n"
        "v_mad_u32_u24 v240, v17, s41, v20\n"
        "v_lshl_or_b32 v251, v17, 7, v20\n"
        "v_add_u32 v240, 73728, v240\n"
        "v_add_u32 v251, 155648, v251\n"
        "v_xor_b32 v241, 64, v240\n"
        "v_xor_b32 v252, 64, v251\n"
        "s_lshl_b32 s67, s42, 10\n"
        "s_add_u32 s67, s67, 139264\n"
        "s_add_u32 m0, s40, 74752\n"
        "s_nop 0\n"
        "buffer_load_dwordx4 v243, s[8:11], s64 offen lds\n"
        "v_lshlrev_b32 v135, 2, v0\n"
        "v_add_u32 v135, 139264, v135\n"
        "v_lshlrev_b32 v11, 5, v16\n"
        "v_xor_b32 v11, v11, v18\n"
        "v_lshl_or_b32 v11, v17, 8, v11\n"
        "v_add_u32 v140, 122880, v11\n"
        "v_xor_b32 v141, 16, v140\n"
        "v_xor_b32 v142, 128, v140\n"
        "v_xor_b32 v143, 144, v140\n"
        "v_lshrrev_b32 v10, 2, v15\n"
        "s_add_u32 m0, s40, 75776\n"
        "s_nop 0\n"
        "buffer_load_dwordx4 v244, s[8:11], s64 offen lds\n"
        "v_cmp_eq_u32 vcc, v10, v16\n"
        "v_mov_b32 v11, 0x3fb9\n"
        "v_mov_b32 v189, 0\n"
        "v_and_b32 v10, 48, v2\n"
        "v_cndmask_b32 v188, 0, v11, vcc\n"
        "v_lshlrev_b64 v[188:189], v10, v[188:189]\n"
        "v_cmp_gt_u32 vcc, 32, v1\n"
        "v_mov_b32 v11, 15957\n"
        "v_lshrrev_b32 v149, 2, v15\n"
        "v_lshrrev_b32 v151, 3, v19\n"
        "v_mov_b32 v148, 14702\n"
        "v_cndmask_b32 v11, v148, v11, vcc\n"
        "v_xor_b32 v151, v149, v151\n"
        "v_cmp_eq_u32 vcc, 0, v151\n"
        "v_mov_b32 v149, 0\n"
        "v_cndmask_b32 v148, 0, v11, vcc\n"
        "v_lshlrev_b64 v[148:149], v10, v[148:149]\n"
        "v_cmp_eq_u32 vcc, 1, v151\n"
        "v_mov_b32 v151, 0\n"
        "v_cndmask_b32 v150, 0, v11, vcc\n"
        "v_lshlrev_b64 v[150:151], v10, v[150:151]\n"
        "v_and_b32 v10, 8, v1\n"
        "v_add_u32 v10, v10, v15\n"
        "v_mul_u32_u24 v10, 96, v10\n"
        "v_add3_u32 v137, v10, v19, s65\n"
        "v_and_b32 v237, 1, v1\n"
        "v_mul_u32_u24 v237, 0xaaaa, v237\n"
        "v_mov_b64 v[170:171], v[188:189]\n"
        "v_mov_b64 v[210:211], v[188:189]\n"
        "v_mov_b64 v[228:229], v[188:189]\n"
        "v_add_u32 v237, 0x4444, v237\n"
        "v_mov_b32 v11, 0x3f803f80\n"
        "v_accvgpr_write_b32 a252, v11\n"
        "v_accvgpr_write_b32 a253, v11\n"
        "v_accvgpr_write_b32 a254, v11\n"
        "v_accvgpr_write_b32 a255, v11\n"
        "v_lshl_or_b32 v10, v16, 10, v18\n"
        "s_lshl_b32 s31, s42, 12\n"
        "s_add_u32 s31, s31, 73728\n"
        "s_lshl_b32 s35, s51, 9\n"
        "s_add_u32 s35, s35, 73728\n"
        "v_add_u32 v245, s31, v10\n"
        "s_sub_u32 s50, s35, s31\n"
        "v_lshrrev_b32 v10, 5, v1\n"
        "v_lshrrev_b32 v11, 1, v19\n"
        "s_sub_u32 s31, s48, 65536\n"
        "v_lshl_or_b32 v10, v10, 10, v18\n"
        "v_add3_u32 v246, v10, v11, s31\n"
        "v_mul_u32_u24 v10, 384, v16\n"
        "s_movk_i32 s45, 1728\n"
        "v_lshrrev_b32 v235, 2, v15\n"
        "v_lshrrev_b32 v254, 1, v15\n"
        "v_and_b32 v11, 1, v1\n"
        "v_mad_u32_u24 v235, v235, 48, v10\n"
        "v_and_b32 v248, 1, v254\n"
        "v_mad_u32_u24 v235, v248, s45, v235\n"
        "v_mul_u32_u24 v248, 36, v11\n"
        "v_lshlrev_b32 v11, 5, v11\n"
        "v_xor_b32 v254, v254, v248\n"
        "v_add3_u32 v235, v235, v11, s65\n"
        "v_mad_u32_u24 v254, v254, 48, v10\n"
        "v_add3_u32 v254, v254, 16, s65\n"
        "s_lshr_b32 s31, s42, 1\n"
        "s_mul_i32 s35, s31, 36864\n"
        "s_movk_i32 s45, 18048\n"
        "v_mad_u32_u24 v10, v15, 48, v10\n"
        "v_lshrrev_b32 v248, 3, v15\n"
        "v_mad_u32_u24 v10, v248, s45, v10\n"
        "v_add_u32 v248, s35, v10\n"
        "s_mul_i32 s49, s31, -192\n"
        "s_add_u32 s49, s49, 128\n"
        "s_mul_i32 s31, s31, 256\n"
        "v_mul_u32_u24 v10, 196608, v16\n"
        "v_mul_u32_u24 v11, 8, v15\n"
        "v_add3_u32 v250, v10, v11, s31\n"
        "s_lshl_b32 s35, s51, 6\n"
        "s_add_u32 s66, s31, s35\n"
        "s_lshl_b32 s35, s42, 5\n"
        "s_sub_u32 s35, s35, s66\n"
        "s_add_u32 s27, s35, 64\n"
        "s_movk_i32 s31, 288\n"
        "s_movk_i32 s35, 3072\n"
        "s_movk_i32 s41, 1536\n"
        "s_movk_i32 s45, 12288\n"
        "v_mad_u32_u24 v10, v4, s31, v2\n"
        "v_bfe_u32 v11, v10, 6, 1\n"
        "v_add_u32 v10, s66, v10\n"
        "v_mad_i32_i24 v10, v11, s27, v10\n"
        "v_lshrrev_b32 v11, 4, v4\n"
        "v_mad_i32_i24 v238, v12, s41, v10\n"
        "v_sub_u32 v7, v7, v11\n"
        "v_mad_u32_u24 v238, v11, s45, v238\n"
        "v_mad_i32_i24 v232, v7, s35, v10\n"
        "v_mad_u32_u24 v10, v5, s31, v2\n"
        "v_bfe_u32 v11, v10, 6, 1\n"
        "v_add_u32 v10, s66, v10\n"
        "v_mad_i32_i24 v10, v11, s27, v10\n"
        "v_lshrrev_b32 v11, 4, v5\n"
        "v_mad_i32_i24 v239, v13, s41, v10\n"
        "v_sub_u32 v8, v8, v11\n"
        "v_mad_u32_u24 v239, v11, s45, v239\n"
        "v_mad_i32_i24 v233, v8, s35, v10\n"
        "v_mad_u32_u24 v10, v6, s31, v2\n"
        "v_bfe_u32 v11, v10, 6, 1\n"
        "v_add_u32 v10, s66, v10\n"
        "v_mad_i32_i24 v10, v11, s27, v10\n"
        "v_lshrrev_b32 v11, 4, v6\n"
        "v_mad_i32_i24 v247, v14, s41, v10\n"
        "v_sub_u32 v9, v9, v11\n"
        "v_mad_u32_u24 v247, v11, s45, v247\n"
        "v_mad_i32_i24 v234, v9, s35, v10\n"
        "s_add_u32 s38, s37, s43\n"
        "s_mov_b32 s26, 0x3fb8aa3b\n"
        "s_mov_b32 s24, 0x5d800000\n"
        "s_mov_b32 s27, 14\n"
        "s_mov_b32 s28, 98304\n"
        "s_mov_b32 s29, 73728\n"
        "s_mov_b32 s30, 122880\n"
        "s_mov_b32 s45, s50\n"
        "s_mov_b32 s25, 0\n"
        "s_mov_b32 s54, 73728\n"
        "s_mov_b32 s55, 0x20000\n"
        "s_mov_b32 s18, 100663296\n"
        "s_mov_b32 s19, 0x20000\n"
        "s_mov_b32 s56, 36864\n"
        "s_mov_b32 s57, 73728\n"
        "s_mov_b32 s58, 110592\n"
        "s_mov_b32 s59, 147456\n"
        "s_mov_b32 s60, 184320\n"
        "s_mov_b32 s61, 18432\n"
        "s_mov_b32 s62, 55296\n"
        "s_mov_b32 s63, 12288\n"
        "s_mov_b32 s34, 49152\n"
        "s_add_u32 m0, s40, 86016\n"
        "s_sub_u32 s64, s47, 11796480\n"
        "buffer_load_dwordx4 v242, s[8:11], s64 offen lds\n"
        "s_waitcnt vmcnt(9)\n"
        "v_and_b32 v146, 0xffff0000, v144\n"
        "v_and_b32 v147, 0xffff0000, v145\n"
        "v_lshlrev_b32 v144, 16, v144\n"
        "v_lshlrev_b32 v145, 16, v145\n"
        "s_add_u32 m0, s40, 87040\n"
        "v_mul_f32 v146, s26, v146\n"
        "buffer_load_dwordx4 v243, s[8:11], s64 offen lds\n"
        "v_mul_f32 v147, s26, v147\n"
        "v_mul_f32 v144, s26, v144\n"
        "v_mul_f32 v145, s26, v145\n"
        "ds_write2st64_b32 v135, v144, v146 offset0:0 offset1:4\n"
        "ds_write2st64_b32 v135, v145, v147 offset0:8 offset1:12\n"
        "s_cmp_eq_u32 s42, 0\n"
        "s_cbranch_scc1 41f\n"
        "s_cmp_eq_u32 s42, 1\n"
        "s_cbranch_scc1 42f\n"
        "s_cmp_eq_u32 s42, 2\n"
        "s_cbranch_scc1 43f\n"
        "s_cmp_eq_u32 s42, 3\n"
        "s_cbranch_scc1 44f\n"
        "60:\n"
        "s_add_u32 m0, s40, 88064\n"
        "s_nop 0\n"
        "buffer_load_dwordx4 v244, s[8:11], s64 offen lds\n"
        "s_waitcnt lgkmcnt(0)\n"
        "s_waitcnt vmcnt(6)\n"
        "s_barrier\n"
        "s_add_u32 m0, s65, 0\n"
        "ds_read_b128 v[0:3], v140 offset:0\n"
        "buffer_load_dwordx4 v128, s[20:23], 0 offen lds\n"
        "buffer_load_dwordx4 v129, s[20:23], 0 offen offset:1024 lds\n"
        "ds_read_b128 v[4:7], v141 offset:0\n"
        "ds_read_b128 v[16:19], v140 offset:1024\n"
        "ds_read_b128 v[20:23], v141 offset:1024\n"
        "ds_read_b128 v[8:11], v142 offset:0\n"
        "ds_read_b128 v[24:27], v142 offset:1024\n"
        "ds_read_b128 v[12:15], v143 offset:0\n"
        "ds_read_b128 v[28:31], v143 offset:1024\n"
        "ds_read_b128 v[32:35], v140 offset:8192\n"
        "ds_read_b128 v[36:39], v141 offset:8192\n"
        "ds_read_b128 v[40:43], v142 offset:8192\n"
        "s_cmp_eq_u32 s42, 0\n"
        "s_cbranch_scc1 51f\n"
        "s_cmp_eq_u32 s42, 1\n"
        "s_cbranch_scc1 54f\n"
        "s_cmp_eq_u32 s42, 2\n"
        "s_cbranch_scc1 45f\n"
        "s_cmp_eq_u32 s42, 3\n"
        "s_cbranch_scc1 47f\n"
        "61:\n"
        "buffer_load_dwordx4 v130, s[20:23], 0 offen offset:2048 lds\n"
        "s_add_u32 m0, s65, 2688\n"
        "s_waitcnt lgkmcnt(10)\n"
        "v_lshl_add_u32 v0, v0, 2, s67\n"
        "buffer_load_dwordx4 v128, s[20:23], 0 offen offset:384 lds\n"
        "v_lshl_add_u32 v1, v1, 2, s67\n"
        "v_lshl_add_u32 v2, v2, 2, s67\n"
        "v_lshl_add_u32 v3, v3, 2, s67\n"
        "s_waitcnt lgkmcnt(9)\n"
        "v_lshl_add_u32 v4, v4, 2, s67\n"
        "v_lshl_add_u32 v5, v5, 2, s67\n"
        "v_lshl_add_u32 v6, v6, 2, s67\n"
        "v_lshl_add_u32 v7, v7, 2, s67\n"
        "s_waitcnt lgkmcnt(8)\n"
        "v_lshl_add_u32 v16, v16, 2, s67\n"
        "v_lshl_add_u32 v17, v17, 2, s67\n"
        "v_lshl_add_u32 v18, v18, 2, s67\n"
        "v_lshl_add_u32 v19, v19, 2, s67\n"
        "buffer_load_dwordx4 v129, s[20:23], 0 offen offset:1408 lds\n"
        "ds_read_b32 v0, v0\n"
        "ds_read_b32 v1, v1\n"
        "ds_read_b32 v2, v2\n"
        "ds_read_b32 v3, v3\n"
        "ds_read_b32 v4, v4\n"
        "ds_read_b32 v5, v5\n"
        "ds_read_b32 v6, v6\n"
        "ds_read_b32 v7, v7\n"
        "ds_read_b32 v16, v16\n"
        "ds_read_b32 v17, v17\n"
        "ds_read_b32 v18, v18\n"
        "ds_read_b32 v19, v19\n"
        "buffer_load_dwordx4 v130, s[20:23], 0 offen offset:2432 lds\n"
        "s_waitcnt lgkmcnt(15)\n"
        "v_lshl_add_u32 v20, v20, 2, s67\n"
        "v_lshl_add_u32 v21, v21, 2, s67\n"
        "v_lshl_add_u32 v22, v22, 2, s67\n"
        "v_lshl_add_u32 v23, v23, 2, s67\n"
        "v_lshl_add_u32 v8, v8, 2, s67\n"
        "v_lshl_add_u32 v9, v9, 2, s67\n"
        "v_lshl_add_u32 v10, v10, 2, s67\n"
        "v_lshl_add_u32 v11, v11, 2, s67\n"
        "v_lshl_add_u32 v24, v24, 2, s67\n"
        "v_lshl_add_u32 v25, v25, 2, s67\n"
        "v_lshl_add_u32 v26, v26, 2, s67\n"
        "v_lshl_add_u32 v27, v27, 2, s67\n"
        "ds_read_b32 v20, v20\n"
        "ds_read_b32 v21, v21\n"
        "ds_read_b32 v22, v22\n"
        "ds_read_b32 v23, v23\n"
        "ds_read_b32 v8, v8\n"
        "ds_read_b32 v9, v9\n"
        "ds_read_b32 v10, v10\n"
        "ds_read_b32 v11, v11\n"
        "ds_read_b32 v24, v24\n"
        "ds_read_b32 v25, v25\n"
        "ds_read_b32 v26, v26\n"
        "ds_read_b32 v27, v27\n"
        "s_cmp_eq_u32 s42, 0\n"
        "s_cbranch_scc1 48f\n"
        "62:\n"
        "s_add_u32 m0, s65, 6144\n"
        "v_lshl_add_u32 v12, v12, 2, s67\n"
        "buffer_load_dwordx4 v128, s[20:23], s56 offen lds\n"
        "v_lshl_add_u32 v13, v13, 2, s67\n"
        "v_lshl_add_u32 v14, v14, 2, s67\n"
        "v_lshl_add_u32 v15, v15, 2, s67\n"
        "v_lshl_add_u32 v28, v28, 2, s67\n"
        "v_lshl_add_u32 v29, v29, 2, s67\n"
        "v_lshl_add_u32 v30, v30, 2, s67\n"
        "v_lshl_add_u32 v31, v31, 2, s67\n"
        "s_waitcnt lgkmcnt(15)\n"
        "v_lshl_add_u32 v32, v32, 2, s67\n"
        "v_lshl_add_u32 v33, v33, 2, s67\n"
        "v_lshl_add_u32 v34, v34, 2, s67\n"
        "v_lshl_add_u32 v35, v35, 2, s67\n"
        "ds_read_b32 v12, v12\n"
        "ds_read_b32 v13, v13\n"
        "ds_read_b32 v14, v14\n"
        "ds_read_b32 v15, v15\n"
        "ds_read_b32 v28, v28\n"
        "ds_read_b32 v29, v29\n"
        "ds_read_b32 v30, v30\n"
        "ds_read_b32 v31, v31\n"
        "ds_read_b32 v32, v32\n"
        "ds_read_b32 v33, v33\n"
        "ds_read_b32 v34, v34\n"
        "ds_read_b32 v35, v35\n"
        "buffer_load_dwordx4 v129, s[20:23], s56 offen offset:1024 lds\n"
        "v_lshl_add_u32 v36, v36, 2, s67\n"
        "v_lshl_add_u32 v37, v37, 2, s67\n"
        "v_lshl_add_u32 v38, v38, 2, s67\n"
        "v_lshl_add_u32 v39, v39, 2, s67\n"
        "v_lshl_add_u32 v40, v40, 2, s67\n"
        "v_lshl_add_u32 v41, v41, 2, s67\n"
        "v_lshl_add_u32 v42, v42, 2, s67\n"
        "v_lshl_add_u32 v43, v43, 2, s67\n"
        "ds_read_b32 v36, v36\n"
        "ds_read_b32 v37, v37\n"
        "ds_read_b32 v38, v38\n"
        "ds_read_b32 v39, v39\n"
        "ds_read_b32 v40, v40\n"
        "ds_read_b32 v41, v41\n"
        "ds_read_b32 v42, v42\n"
        "ds_read_b32 v43, v43\n"
        "buffer_load_dwordx4 v130, s[20:23], s56 offen offset:2048 lds\n"
        "s_waitcnt vmcnt(9)\n"
        "s_barrier\n"
        "s_waitcnt vmcnt(6)\n"
        "ds_read_b128 a[212:215], v137 offset:0\n"
        "ds_read_b128 a[208:211], v137 offset:768\n"
        "ds_read_b128 a[204:207], v137 offset:32\n"
        "ds_read_b128 a[200:203], v137 offset:800\n"
        "ds_read_b128 a[196:199], v137 offset:64\n"
        "ds_read_b128 a[192:195], v137 offset:832\n"
        "s_add_u32 m0, s65, 8832\n"
        "s_nop 0\n"
        "buffer_load_dwordx4 v128, s[20:23], s56 offen offset:384 lds\n"
        "s_waitcnt lgkmcnt(5)\n"
        "v_mfma_f32_16x16x32_bf16 v[112:115], a[212:215], v[148:151], 0\n"
        "s_waitcnt lgkmcnt(4)\n"
        "ds_read_b128 a[216:219], v240 offset:0\n"
        "ds_read_b128 a[220:223], v240 offset:1536\n"
        "ds_read_b128 a[224:227], v240 offset:12288\n"
        "ds_read_b128 a[228:231], v240 offset:13824\n"
        "v_mfma_f32_16x16x32_bf16 v[116:119], a[208:211], v[148:151], 0\n"
        "s_waitcnt lgkmcnt(7)\n"
        "v_mfma_f32_16x16x32_bf16 v[120:123], a[204:207], v[148:151], 0\n"
        "s_waitcnt lgkmcnt(6)\n"
        "v_mfma_f32_16x16x32_bf16 v[124:127], a[200:203], v[148:151], 0\n"
        "s_waitcnt lgkmcnt(5)\n"
        "buffer_load_dwordx4 v129, s[20:23], s56 offen offset:1408 lds\n"
        "v_mfma_f32_16x16x32_bf16 v[96:99], a[196:199], v[148:151], 0\n"
        "s_waitcnt lgkmcnt(4)\n"
        "ds_read_b128 v[44:47], v143 offset:8192\n"
        "v_cvt_pk_bf16_f32 v112, v112, v113\n"
        "v_mfma_f32_16x16x32_bf16 v[100:103], a[192:195], v[148:151], 0\n"
        "s_waitcnt lgkmcnt(4)\n"
        "s_waitcnt vmcnt(5)\n"
        "ds_read_b64_tr_b16 a[72:73], v255 offset:3072\n"
        "ds_read_b64_tr_b16 a[74:75], v255 offset:3840\n"
        "v_accvgpr_write_b32 a0, v112\n"
        "v_cvt_pk_bf16_f32 v114, v114, v115\n"
        "v_accvgpr_write_b32 a1, v114\n"
        "v_cvt_pk_bf16_f32 v116, v116, v117\n"
        "v_accvgpr_write_b32 a2, v116\n"
        "v_cvt_pk_bf16_f32 v118, v118, v119\n"
        "v_accvgpr_write_b32 a3, v118\n"
        "s_nop 1\n"
        "v_mfma_f32_16x16x32_bf16 v[156:159], a[0:3], a[216:219], 0\n"
        "s_waitcnt lgkmcnt(5)\n"
        "ds_read_b64_tr_b16 a[96:97], v255 offset:3104\n"
        "ds_read_b64_tr_b16 a[98:99], v255 offset:3872\n"
        "v_mfma_f32_16x16x32_bf16 v[176:179], a[0:3], a[220:223], 0\n"
        "s_waitcnt lgkmcnt(5)\n"
        "buffer_load_dwordx4 v130, s[20:23], s56 offen offset:2432 lds\n"
        "ds_read_b64_tr_b16 a[120:121], v255 offset:3136\n"
        "ds_read_b64_tr_b16 a[122:123], v255 offset:3904\n"
        "v_mfma_f32_16x16x32_bf16 v[196:199], a[0:3], a[224:227], 0\n"
        "s_add_u32 m0, s65, 12288\n"
        "v_cvt_pk_bf16_f32 v120, v120, v121\n"
        "v_accvgpr_write_b32 a24, v120\n"
        "v_mfma_f32_16x16x32_bf16 v[216:219], a[0:3], a[228:231], 0\n"
        "s_waitcnt lgkmcnt(4)\n"
        "v_cvt_pk_bf16_f32 v122, v122, v123\n"
        "v_accvgpr_write_b32 a25, v122\n"
        "v_mfma_f32_16x16x32_bf16 v[152:155], a[72:75], a[216:219], 0\n"
        "buffer_load_dwordx4 v128, s[20:23], s57 offen lds\n"
        "s_waitcnt vmcnt(4)\n"
        "ds_read_b128 a[188:191], v137 offset:6144\n"
        "v_mfma_f32_16x16x32_bf16 v[172:175], a[72:75], a[220:223], 0\n"
        "ds_read_b128 a[184:187], v137 offset:6912\n"
        "ds_read_b128 a[180:183], v137 offset:6176\n"
        "v_cvt_pk_bf16_f32 v124, v124, v125\n"
        "v_accvgpr_write_b32 a26, v124\n"
        "v_cvt_pk_bf16_f32 v126, v126, v127\n"
        "v_accvgpr_write_b32 a27, v126\n"
        "s_nop 1\n"
        "v_mfma_f32_16x16x32_bf16 v[224:227], a[24:27], a[228:231], 0\n"
        "buffer_load_dwordx4 v129, s[20:23], s57 offen offset:1024 lds\n"
        "ds_read_b128 a[176:179], v137 offset:6944\n"
        "ds_read_b128 a[172:175], v137 offset:6208\n"
        "v_mfma_f32_16x16x32_bf16 v[204:207], a[24:27], a[224:227], 0\n"
        "ds_read_b128 a[168:171], v137 offset:6976\n"
        "ds_read_b128 a[232:235], v241 offset:0\n"
        "v_mfma_f32_16x16x32_bf16 v[184:187], a[24:27], a[220:223], 0\n"
        "ds_read_b128 a[236:239], v241 offset:1536\n"
        "ds_read_b128 a[240:243], v241 offset:12288\n"
        "v_mfma_f32_16x16x32_bf16 v[164:167], a[24:27], a[216:219], 0\n"
        "buffer_load_dwordx4 v130, s[20:23], s57 offen offset:2048 lds\n"
        "v_mfma_f32_16x16x32_bf16 v[192:195], a[72:75], a[224:227], 0\n"
        "s_add_u32 m0, s65, 14976\n"
        "ds_read_b128 a[244:247], v241 offset:13824\n"
        "v_cvt_pk_bf16_f32 v96, v96, v97\n"
        "v_mfma_f32_16x16x32_bf16 v[212:215], a[72:75], a[228:231], 0\n"
        "v_accvgpr_write_b32 a48, v96\n"
        "v_cvt_pk_bf16_f32 v98, v98, v99\n"
        "v_accvgpr_write_b32 a49, v98\n"
        "v_cvt_pk_bf16_f32 v100, v100, v101\n"
        "v_accvgpr_write_b32 a50, v100\n"
        "v_cvt_pk_bf16_f32 v102, v102, v103\n"
        "v_accvgpr_write_b32 a51, v102\n"
        "s_nop 1\n"
        "v_mfma_f32_16x16x32_bf16 v[160:163], a[48:51], a[216:219], 0\n"
        "v_lshl_add_u32 v44, v44, 2, s67\n"
        "v_lshl_add_u32 v45, v45, 2, s67\n"
        "v_mfma_f32_16x16x32_bf16 v[180:183], a[48:51], a[220:223], 0\n"
        "s_waitcnt lgkmcnt(5)\n"
        "ds_read_b32 v44, v44\n"
        "ds_read_b32 v45, v45\n"
        "v_mfma_f32_16x16x32_bf16 v[104:107], a[188:191], v[148:151], 0\n"
        "buffer_load_dwordx4 v128, s[20:23], s57 offen offset:384 lds\n"
        "v_mfma_f32_16x16x32_bf16 v[200:203], a[48:51], a[224:227], 0\n"
        "v_lshl_add_u32 v46, v46, 2, s67\n"
        "v_lshl_add_u32 v47, v47, 2, s67\n"
        "v_mfma_f32_16x16x32_bf16 v[108:111], a[184:187], v[148:151], 0\n"
        "ds_read_b32 v46, v46\n"
        "ds_read_b32 v47, v47\n"
        "v_mfma_f32_16x16x32_bf16 v[220:223], a[48:51], a[228:231], 0\n"
        "buffer_load_dwordx4 v129, s[20:23], s57 offen offset:1408 lds\n"
        "v_mfma_f32_16x16x32_bf16 v[88:91], a[96:99], a[228:231], 0\n"
        "ds_read_b128 v[48:51], v140 offset:9216\n"
        "v_cvt_pk_bf16_f32 v104, v104, v105\n"
        "v_mfma_f32_16x16x32_bf16 v[80:83], a[96:99], a[224:227], 0\n"
        "s_waitcnt vmcnt(5)\n"
        "ds_read_b64_tr_b16 a[76:77], v255 offset:9216\n"
        "ds_read_b64_tr_b16 a[78:79], v255 offset:9984\n"
        "v_mfma_f32_16x16x32_bf16 v[68:71], a[96:99], a[220:223], 0\n"
        "ds_read_b64_tr_b16 a[100:101], v255 offset:9248\n"
        "ds_read_b64_tr_b16 a[102:103], v255 offset:10016\n"
        "v_mfma_f32_16x16x32_bf16 v[64:67], a[96:99], a[216:219], 0\n"
        "buffer_load_dwordx4 v130, s[20:23], s57 offen offset:2432 lds\n"
        "ds_read_b64_tr_b16 a[124:125], v255 offset:9280\n"
        "ds_read_b64_tr_b16 a[126:127], v255 offset:10048\n"
        "v_mfma_f32_16x16x32_bf16 v[112:115], a[180:183], v[148:151], 0\n"
        "s_add_u32 m0, s65, 0\n"
        "v_accvgpr_write_b32 a4, v104\n"
        "v_cvt_pk_bf16_f32 v106, v106, v107\n"
        "v_mfma_f32_16x16x32_bf16 v[116:119], a[176:179], v[148:151], 0\n"
        "v_accvgpr_write_b32 a5, v106\n"
        "v_cvt_pk_bf16_f32 v108, v108, v109\n"
        "v_mfma_f32_16x16x32_bf16 v[72:75], a[120:123], a[216:219], 0\n"
        "buffer_load_dwordx4 v128, s[20:23], s58 offen lds\n"
        "v_mfma_f32_16x16x32_bf16 v[76:79], a[120:123], a[220:223], 0\n"
        "v_accvgpr_write_b32 a6, v108\n"
        "v_cvt_pk_bf16_f32 v110, v110, v111\n"
        "v_mfma_f32_16x16x32_bf16 v[84:87], a[120:123], a[224:227], 0\n"
        "v_accvgpr_write_b32 a7, v110\n"
        "v_cvt_pk_bf16_f32 v112, v112, v113\n"
        "v_mfma_f32_16x16x32_bf16 v[92:95], a[120:123], a[228:231], 0\n"
        "s_waitcnt lgkmcnt(2)\n"
        "v_accvgpr_write_b32 a28, v112\n"
        "v_cvt_pk_bf16_f32 v114, v114, v115\n"
        "v_mfma_f32_16x16x32_bf16 v[156:159], a[4:7], a[232:235], v[156:159]\n"
        "buffer_load_dwordx4 v129, s[20:23], s58 offen offset:1024 lds\n"
        "v_mfma_f32_16x16x32_bf16 v[176:179], a[4:7], a[236:239], v[176:179]\n"
        "v_lshl_add_u32 v48, v48, 2, s67\n"
        "v_lshl_add_u32 v49, v49, 2, s67\n"
        "v_mfma_f32_16x16x32_bf16 v[196:199], a[4:7], a[240:243], v[196:199]\n"
        "ds_read_b32 v48, v48\n"
        "ds_read_b32 v49, v49\n"
        "v_mfma_f32_16x16x32_bf16 v[216:219], a[4:7], a[244:247], v[216:219]\n"
        ".long 0xbec200ff\n"
        "41:\n"
        "s_branch 60b\n"
        "v_lshl_add_u32 v50, v50, 2, s67\n"
        "v_lshl_add_u32 v51, v51, 2, s67\n"
        "v_mfma_f32_16x16x32_bf16 v[120:123], a[172:175], v[148:151], 0\n"
        "buffer_load_dwordx4 v130, s[20:23], s58 offen offset:2048 lds\n"
        "v_mfma_f32_16x16x32_bf16 v[124:127], a[168:171], v[148:151], 0\n"
        "s_add_u32 m0, s65, 2688\n"
        "ds_read_b32 v50, v50\n"
        "ds_read_b32 v51, v51\n"
        "v_mfma_f32_16x16x32_bf16 v[152:155], a[76:79], a[232:235], v[152:155]\n"
        "ds_read_b128 a[248:251], v240 offset:128\n"
        "v_accvgpr_write_b32 a29, v114\n"
        "v_mfma_f32_16x16x32_bf16 v[172:175], a[76:79], a[236:239], v[172:175]\n"
        "s_waitcnt vmcnt(6)\n"
        "ds_read_b128 a[212:215], v137 offset:12288\n"
        "ds_read_b128 a[208:211], v137 offset:13056\n"
        "v_mfma_f32_16x16x32_bf16 v[192:195], a[76:79], a[240:243], v[192:195]\n"
        "buffer_load_dwordx4 v128, s[20:23], s58 offen offset:384 lds\n"
        "ds_read_b128 a[204:207], v137 offset:12320\n"
        "v_cvt_pk_bf16_f32 v116, v116, v117\n"
        "v_mfma_f32_16x16x32_bf16 v[212:215], a[76:79], a[244:247], v[212:215]\n"
        "ds_read_b128 a[200:203], v137 offset:13088\n"
        "ds_read_b128 a[196:199], v137 offset:12352\n"
        "v_accvgpr_write_b32 a30, v116\n"
        "v_cvt_pk_bf16_f32 v118, v118, v119\n"
        "v_accvgpr_write_b32 a31, v118\n"
        "s_nop 1\n"
        "v_mfma_f32_16x16x32_bf16 v[224:227], a[28:31], a[244:247], v[224:227]\n"
        "ds_read_b128 a[192:195], v137 offset:13120\n"
        "ds_read_b128 a[216:219], v240 offset:1664\n"
        "v_mfma_f32_16x16x32_bf16 v[204:207], a[28:31], a[240:243], v[204:207]\n"
        "buffer_load_dwordx4 v129, s[20:23], s58 offen offset:1408 lds\n"
        "v_mfma_f32_16x16x32_bf16 v[184:187], a[28:31], a[236:239], v[184:187]\n"
        "ds_read_b128 a[220:223], v240 offset:12416\n"
        "ds_read_b128 a[224:227], v240 offset:13952\n"
        "v_mfma_f32_16x16x32_bf16 v[164:167], a[28:31], a[232:235], v[164:167]\n"
        "s_waitcnt lgkmcnt(8)\n"
        "v_cvt_pk_bf16_f32 v120, v120, v121\n"
        "v_accvgpr_write_b32 a52, v120\n"
        "v_mfma_f32_16x16x32_bf16 v[96:99], a[212:215], v[148:151], 0\n"
        "s_waitcnt lgkmcnt(7)\n"
        "v_cvt_pk_bf16_f32 v122, v122, v123\n"
        "v_accvgpr_write_b32 a53, v122\n"
        "v_mfma_f32_16x16x32_bf16 v[100:103], a[208:211], v[148:151], 0\n"
        "buffer_load_dwordx4 v130, s[20:23], s58 offen offset:2432 lds\n"
        "v_mfma_f32_16x16x32_bf16 v[88:91], a[100:103], a[244:247], v[88:91]\n"
        "s_add_u32 m0, s65, 6144\n"
        "v_cvt_pk_bf16_f32 v124, v124, v125\n"
        "v_accvgpr_write_b32 a54, v124\n"
        "v_mfma_f32_16x16x32_bf16 v[80:83], a[100:103], a[240:243], v[80:83]\n"
        "v_cvt_pk_bf16_f32 v126, v126, v127\n"
        "v_accvgpr_write_b32 a55, v126\n"
        "v_mfma_f32_16x16x32_bf16 v[68:71], a[100:103], a[236:239], v[68:71]\n"
        "v_cvt_pk_bf16_f32 v96, v96, v97\n"
        "v_accvgpr_write_b32 a8, v96\n"
        "v_mfma_f32_16x16x32_bf16 v[64:67], a[100:103], a[232:235], v[64:67]\n"
        "buffer_load_dwordx4 v128, s[20:23], s59 offen lds\n"
        "v_mfma_f32_16x16x32_bf16 v[160:163], a[52:55], a[232:235], v[160:163]\n"
        "s_waitcnt vmcnt(7)\n"
        "ds_read_b64_tr_b16 a[80:81], v255 offset:15360\n"
        "ds_read_b64_tr_b16 a[82:83], v255 offset:16128\n"
        "v_mfma_f32_16x16x32_bf16 v[180:183], a[52:55], a[236:239], v[180:183]\n"
        "ds_read_b64_tr_b16 a[104:105], v255 offset:15392\n"
        "ds_read_b64_tr_b16 a[106:107], v255 offset:16160\n"
        "v_mfma_f32_16x16x32_bf16 v[200:203], a[52:55], a[240:243], v[200:203]\n"
        "ds_read_b64_tr_b16 a[128:129], v255 offset:15424\n"
        "ds_read_b64_tr_b16 a[130:131], v255 offset:16192\n"
        "v_mfma_f32_16x16x32_bf16 v[220:223], a[52:55], a[244:247], v[220:223]\n"
        "s_waitcnt lgkmcnt(6)\n"
        "v_cvt_pk_bf16_f32 v98, v98, v99\n"
        "v_accvgpr_write_b32 a9, v98\n"
        "v_mfma_f32_16x16x32_bf16 v[104:107], a[204:207], v[148:151], 0\n"
        "buffer_load_dwordx4 v129, s[20:23], s59 offen offset:1024 lds\n"
        "v_mfma_f32_16x16x32_bf16 v[108:111], a[200:203], v[148:151], 0\n"
        "v_cvt_pk_bf16_f32 v100, v100, v101\n"
        "v_accvgpr_write_b32 a10, v100\n"
        "v_mfma_f32_16x16x32_bf16 v[72:75], a[124:127], a[232:235], v[72:75]\n"
        "v_cvt_pk_bf16_f32 v102, v102, v103\n"
        "v_accvgpr_write_b32 a11, v102\n"
        "v_mfma_f32_16x16x32_bf16 v[76:79], a[124:127], a[236:239], v[76:79]\n"
        "buffer_load_dwordx4 v130, s[20:23], s59 offen offset:2048 lds\n"
        "v_mfma_f32_16x16x32_bf16 v[84:87], a[124:127], a[240:243], v[84:87]\n"
        "s_add_u32 m0, s65, 8832\n"
        "v_cvt_pk_bf16_f32 v104, v104, v105\n"
        "v_accvgpr_write_b32 a32, v104\n"
        "v_mfma_f32_16x16x32_bf16 v[92:95], a[124:127], a[244:247], v[92:95]\n"
        "s_waitcnt vmcnt(6)\n"
        "ds_read_b128 a[188:191], v137 offset:0\n"
        "ds_read_b128 a[184:187], v137 offset:768\n"
        "v_mfma_f32_16x16x32_bf16 v[156:159], a[8:11], a[248:251], v[156:159]\n"
        "buffer_load_dwordx4 v128, s[20:23], s59 offen offset:384 lds\n"
        "ds_read_b128 a[180:183], v137 offset:32\n"
        "v_mfma_f32_16x16x32_bf16 v[176:179], a[8:11], a[216:219], v[176:179]\n"
        "ds_read_b128 a[176:179], v137 offset:800\n"
        "ds_read_b128 a[172:175], v137 offset:64\n"
        "v_mfma_f32_16x16x32_bf16 v[196:199], a[8:11], a[220:223], v[196:199]\n"
        "ds_read_b128 a[168:171], v137 offset:832\n"
        "v_cvt_pk_bf16_f32 v106, v106, v107\n"
        "v_mfma_f32_16x16x32_bf16 v[216:219], a[8:11], a[224:227], v[216:219]\n"
        "v_accvgpr_write_b32 a33, v106\n"
        "v_cvt_pk_bf16_f32 v108, v108, v109\n"
        "v_mfma_f32_16x16x32_bf16 v[112:115], a[196:199], v[148:151], 0\n"
        "buffer_load_dwordx4 v129, s[20:23], s59 offen offset:1408 lds\n"
        "v_mfma_f32_16x16x32_bf16 v[116:119], a[192:195], v[148:151], 0\n"
        "s_waitcnt lgkmcnt(6)\n"
        "v_accvgpr_write_b32 a34, v108\n"
        "v_cvt_pk_bf16_f32 v110, v110, v111\n"
        "v_mfma_f32_16x16x32_bf16 v[152:155], a[80:83], a[248:251], v[152:155]\n"
        "ds_read_b128 a[228:231], v241 offset:128\n"
        "v_accvgpr_write_b32 a35, v110\n"
        "v_mfma_f32_16x16x32_bf16 v[172:175], a[80:83], a[216:219], v[172:175]\n"
        "ds_read_b128 a[232:235], v241 offset:1664\n"
        "v_cvt_pk_bf16_f32 v112, v112, v113\n"
        "v_mfma_f32_16x16x32_bf16 v[192:195], a[80:83], a[220:223], v[192:195]\n"
        "buffer_load_dwordx4 v130, s[20:23], s59 offen offset:2432 lds\n"
        "v_mfma_f32_16x16x32_bf16 v[212:215], a[80:83], a[224:227], v[212:215]\n"
        "s_add_u32 m0, s65, 12288\n"
        "ds_read_b128 a[236:239], v241 offset:12416\n"
        "ds_read_b128 a[240:243], v241 offset:13952\n"
        "v_mfma_f32_16x16x32_bf16 v[224:227], a[32:35], a[224:227], v[224:227]\n"
        "ds_read_b128 v[52:55], v141 offset:9216\n"
        "v_accvgpr_write_b32 a56, v112\n"
        "v_mfma_f32_16x16x32_bf16 v[204:207], a[32:35], a[220:223], v[204:207]\n"
        "v_cvt_pk_bf16_f32 v114, v114, v115\n"
        "v_accvgpr_write_b32 a57, v114\n"
        "v_mfma_f32_16x16x32_bf16 v[184:187], a[32:35], a[216:219], v[184:187]\n"
        "buffer_load_dwordx4 v128, s[20:23], s60 offen lds\n"
        "v_mfma_f32_16x16x32_bf16 v[164:167], a[32:35], a[248:251], v[164:167]\n"
        "s_waitcnt lgkmcnt(4)\n"
        "v_cvt_pk_bf16_f32 v116, v116, v117\n"
        "v_accvgpr_write_b32 a58, v116\n"
        "v_mfma_f32_16x16x32_bf16 v[120:123], a[188:191], v[148:151], 0\n"
        "s_waitcnt vmcnt(7)\n"
        "ds_read_b64_tr_b16 a[84:85], v255 offset:3072\n"
        "ds_read_b64_tr_b16 a[86:87], v255 offset:3840\n"
        "v_mfma_f32_16x16x32_bf16 v[124:127], a[184:187], v[148:151], 0\n"
        "ds_read_b64_tr_b16 a[108:109], v255 offset:3104\n"
        "ds_read_b64_tr_b16 a[110:111], v255 offset:3872\n"
        "v_mfma_f32_16x16x32_bf16 v[88:91], a[104:107], a[224:227], v[88:91]\n"
        "buffer_load_dwordx4 v129, s[20:23], s60 offen offset:1024 lds\n"
        "ds_read_b64_tr_b16 a[132:133], v255 offset:3136\n"
        "ds_read_b64_tr_b16 a[134:135], v255 offset:3904\n"
        "v_mfma_f32_16x16x32_bf16 v[80:83], a[104:107], a[220:223], v[80:83]\n"
        "v_cvt_pk_bf16_f32 v118, v118, v119\n"
        "v_accvgpr_write_b32 a59, v118\n"
        "v_mfma_f32_16x16x32_bf16 v[68:71], a[104:107], a[216:219], v[68:71]\n"
        "v_cvt_pk_bf16_f32 v120, v120, v121\n"
        "v_accvgpr_write_b32 a12, v120\n"
        "v_mfma_f32_16x16x32_bf16 v[64:67], a[104:107], a[248:251], v[64:67]\n"
        "buffer_load_dwordx4 v130, s[20:23], s60 offen offset:2048 lds\n"
        "v_mfma_f32_16x16x32_bf16 v[160:163], a[56:59], a[248:251], v[160:163]\n"
        "s_add_u32 m0, s65, 14976\n"
        "v_cvt_pk_bf16_f32 v122, v122, v123\n"
        "v_accvgpr_write_b32 a13, v122\n"
        "v_mfma_f32_16x16x32_bf16 v[180:183], a[56:59], a[216:219], v[180:183]\n"
        "s_waitcnt lgkmcnt(4)\n"
        "v_lshl_add_u32 v52, v52, 2, s67\n"
        "v_lshl_add_u32 v53, v53, 2, s67\n"
        "v_mfma_f32_16x16x32_bf16 v[200:203], a[56:59], a[220:223], v[200:203]\n"
        "ds_read_b32 v52, v52\n"
        "ds_read_b32 v53, v53\n"
        "v_mfma_f32_16x16x32_bf16 v[220:223], a[56:59], a[224:227], v[220:223]\n"
        "buffer_load_dwordx4 v128, s[20:23], s60 offen offset:384 lds\n"
        "v_mfma_f32_16x16x32_bf16 v[96:99], a[180:183], v[148:151], 0\n"
        "v_lshl_add_u32 v54, v54, 2, s67\n"
        "v_lshl_add_u32 v55, v55, 2, s67\n"
        "v_mfma_f32_16x16x32_bf16 v[100:103], a[176:179], v[148:151], 0\n"
        "ds_read_b32 v54, v54\n"
        "ds_read_b32 v55, v55\n"
        "v_mfma_f32_16x16x32_bf16 v[72:75], a[128:131], a[248:251], v[72:75]\n"
        "v_cvt_pk_bf16_f32 v124, v124, v125\n"
        "v_accvgpr_write_b32 a14, v124\n"
        "v_mfma_f32_16x16x32_bf16 v[76:79], a[128:131], a[216:219], v[76:79]\n"
        "buffer_load_dwordx4 v129, s[20:23], s60 offen offset:1408 lds\n"
        "v_mfma_f32_16x16x32_bf16 v[84:87], a[128:131], a[220:223], v[84:87]\n"
        "v_cvt_pk_bf16_f32 v126, v126, v127\n"
        "v_accvgpr_write_b32 a15, v126\n"
        "v_mfma_f32_16x16x32_bf16 v[92:95], a[128:131], a[224:227], v[92:95]\n"
        "v_cvt_pk_bf16_f32 v96, v96, v97\n"
        "v_accvgpr_write_b32 a36, v96\n"
        "v_mfma_f32_16x16x32_bf16 v[156:159], a[12:15], a[228:231], v[156:159]\n"
        "s_waitcnt vmcnt(8)\n"
        "ds_read_b128 a[212:215], v137 offset:6144\n"
        "ds_read_b128 a[208:211], v137 offset:6912\n"
        "v_mfma_f32_16x16x32_bf16 v[176:179], a[12:15], a[232:235], v[176:179]\n"
        "buffer_load_dwordx4 v130, s[20:23], s60 offen offset:2432 lds\n"
        "ds_read_b128 a[204:207], v137 offset:6176\n"
        "v_mfma_f32_16x16x32_bf16 v[196:199], a[12:15], a[236:239], v[196:199]\n"
        "ds_read_b128 a[200:203], v137 offset:6944\n"
        "ds_read_b128 a[196:199], v137 offset:6208\n"
        "v_mfma_f32_16x16x32_bf16 v[216:219], a[12:15], a[240:243], v[216:219]\n"
        "ds_read_b128 a[192:195], v137 offset:6976\n"
        "v_cvt_pk_bf16_f32 v98, v98, v99\n"
        "v_mfma_f32_16x16x32_bf16 v[104:107], a[172:175], v[148:151], 0\n"
        "v_accvgpr_write_b32 a37, v98\n"
        "v_cvt_pk_bf16_f32 v100, v100, v101\n"
        "v_mfma_f32_16x16x32_bf16 v[108:111], a[168:171], v[148:151], 0\n"
        "v_accvgpr_write_b32 a38, v100\n"
        "v_cvt_pk_bf16_f32 v102, v102, v103\n"
        "v_mfma_f32_16x16x32_bf16 v[152:155], a[84:87], a[228:231], v[152:155]\n"
        "ds_read_b128 a[244:247], v240 offset:256\n"
        "v_accvgpr_write_b32 a39, v102\n"
        "v_mfma_f32_16x16x32_bf16 v[172:175], a[84:87], a[232:235], v[172:175]\n"
        "s_mov_b32 m0, s48\n"
        "ds_read_b128 a[248:251], v240 offset:1792\n"
        "v_cvt_pk_bf16_f32 v104, v104, v105\n"
        "v_mfma_f32_16x16x32_bf16 v[192:195], a[84:87], a[236:239], v[192:195]\n"
        "s_sub_u32 s64, s36, 4194304\n"
        "ds_read_b128 a[216:219], v240 offset:12544\n"
        "v_accvgpr_write_b32 a60, v104\n"
        "v_mfma_f32_16x16x32_bf16 v[212:215], a[84:87], a[240:243], v[212:215]\n"
        "buffer_load_dwordx4 v249, s[12:15], s64 offen lds\n"
        "v_mfma_f32_16x16x32_bf16 v[224:227], a[36:39], a[240:243], v[224:227]\n"
        "ds_read_b128 a[220:223], v240 offset:14080\n"
        "v_cvt_pk_bf16_f32 v106, v106, v107\n"
        "v_mfma_f32_16x16x32_bf16 v[204:207], a[36:39], a[236:239], v[204:207]\n"
        "ds_read_b128 v[56:59], v142 offset:9216\n"
        "v_accvgpr_write_b32 a61, v106\n"
        "v_mfma_f32_16x16x32_bf16 v[184:187], a[36:39], a[232:235], v[184:187]\n"
        "s_waitcnt vmcnt(7)\n"
        "ds_read_b64_tr_b16 a[88:89], v255 offset:9216\n"
        "ds_read_b64_tr_b16 a[90:91], v255 offset:9984\n"
        "v_mfma_f32_16x16x32_bf16 v[164:167], a[36:39], a[228:231], v[164:167]\n"
        "s_waitcnt lgkmcnt(6)\n"
        "ds_read_b64_tr_b16 a[112:113], v255 offset:9248\n"
        "ds_read_b64_tr_b16 a[114:115], v255 offset:10016\n"
        "v_mfma_f32_16x16x32_bf16 v[112:115], a[212:215], v[148:151], 0\n"
        "buffer_load_dwordx4 v253, s[12:15], s64 offen offset:1024 lds\n"
        "ds_read_b64_tr_b16 a[136:137], v255 offset:9280\n"
        "v_mfma_f32_16x16x32_bf16 v[116:119], a[208:211], v[148:151], 0\n"
        "ds_read_b64_tr_b16 a[138:139], v255 offset:10048\n"
        "s_add_u32 m0, s65, 0\n"
        "v_cvt_pk_bf16_f32 v108, v108, v109\n"
        "v_mfma_f32_16x16x32_bf16 v[88:91], a[108:111], a[240:243], v[88:91]\n"
        "s_mov_b32 s64, 768\n"
        "v_accvgpr_write_b32 a62, v108\n"
        "v_cvt_pk_bf16_f32 v110, v110, v111\n"
        "v_mfma_f32_16x16x32_bf16 v[80:83], a[108:111], a[236:239], v[80:83]\n"
        "buffer_load_dwordx4 v128, s[20:23], s64 offen lds\n"
        "v_mfma_f32_16x16x32_bf16 v[68:71], a[108:111], a[232:235], v[68:71]\n"
        "v_accvgpr_write_b32 a63, v110\n"
        "v_cvt_pk_bf16_f32 v112, v112, v113\n"
        "v_mfma_f32_16x16x32_bf16 v[64:67], a[108:111], a[228:231], v[64:67]\n"
        "v_accvgpr_write_b32 a16, v112\n"
        "v_cvt_pk_bf16_f32 v114, v114, v115\n"
        "v_mfma_f32_16x16x32_bf16 v[160:163], a[60:63], a[228:231], v[160:163]\n"
        "v_accvgpr_write_b32 a17, v114\n"
        "v_cvt_pk_bf16_f32 v116, v116, v117\n"
        "v_mfma_f32_16x16x32_bf16 v[180:183], a[60:63], a[232:235], v[180:183]\n"
        "buffer_load_dwordx4 v129, s[20:23], s64 offen offset:1024 lds\n"
        "v_mfma_f32_16x16x32_bf16 v[200:203], a[60:63], a[236:239], v[200:203]\n"
        "ds_read_b128 v[60:63], v143 offset:9216\n"
        "v_accvgpr_write_b32 a18, v116\n"
        "v_mfma_f32_16x16x32_bf16 v[220:223], a[60:63], a[240:243], v[220:223]\n"
        "v_cvt_pk_bf16_f32 v118, v118, v119\n"
        "v_accvgpr_write_b32 a19, v118\n"
        "v_mfma_f32_16x16x32_bf16 v[120:123], a[204:207], v[148:151], 0\n"
        "s_waitcnt lgkmcnt(1)\n"
        "v_lshl_add_u32 v56, v56, 2, s67\n"
        "v_lshl_add_u32 v57, v57, 2, s67\n"
        "v_mfma_f32_16x16x32_bf16 v[124:127], a[200:203], v[148:151], 0\n"
        "buffer_load_dwordx4 v130, s[20:23], s64 offen offset:2048 lds\n"
        "v_mfma_f32_16x16x32_bf16 v[72:75], a[132:135], a[228:231], v[72:75]\n"
        "s_add_u32 m0, s65, 2304\n"
        "ds_read_b32 v56, v56\n"
        "ds_read_b32 v57, v57\n"
        "v_mfma_f32_16x16x32_bf16 v[76:79], a[132:135], a[232:235], v[76:79]\n"
        "v_lshl_add_u32 v58, v58, 2, s67\n"
        "v_lshl_add_u32 v59, v59, 2, s67\n"
        "v_mfma_f32_16x16x32_bf16 v[84:87], a[132:135], a[236:239], v[84:87]\n"
        "ds_read_b32 v58, v58\n"
        "ds_read_b32 v59, v59\n"
        "v_mfma_f32_16x16x32_bf16 v[92:95], a[132:135], a[240:243], v[92:95]\n"
        "buffer_load_dwordx4 v128, s[20:23], s56 offen offset:768 lds\n"
        "v_mfma_f32_16x16x32_bf16 v[156:159], a[16:19], a[244:247], v[156:159]\n"
        "v_cvt_pk_bf16_f32 v120, v120, v121\n"
        "v_accvgpr_write_b32 a40, v120\n"
        "v_mfma_f32_16x16x32_bf16 v[176:179], a[16:19], a[248:251], v[176:179]\n"
        "v_cvt_pk_bf16_f32 v122, v122, v123\n"
        "v_accvgpr_write_b32 a41, v122\n"
        "v_mfma_f32_16x16x32_bf16 v[196:199], a[16:19], a[216:219], v[196:199]\n"
        "v_cvt_pk_bf16_f32 v124, v124, v125\n"
        "v_accvgpr_write_b32 a42, v124\n"
        "v_mfma_f32_16x16x32_bf16 v[216:219], a[16:19], a[220:223], v[216:219]\n"
        "buffer_load_dwordx4 v129, s[20:23], s56 offen offset:1792 lds\n"
        "v_mfma_f32_16x16x32_bf16 v[96:99], a[196:199], v[148:151], 0\n"
        "ds_read_b128 a[224:227], v241 offset:256\n"
        "v_cvt_pk_bf16_f32 v126, v126, v127\n"
        "v_mfma_f32_16x16x32_bf16 v[100:103], a[192:195], v[148:151], 0\n"
        "s_waitcnt vmcnt(10)\n"
        "ds_read_b128 a[188:191], v137 offset:12288\n"
        "ds_read_b128 a[184:187], v137 offset:13056\n"
        "v_mfma_f32_16x16x32_bf16 v[152:155], a[88:91], a[244:247], v[152:155]\n"
        "ds_read_b128 a[180:183], v137 offset:12320\n"
        "ds_read_b128 a[176:179], v137 offset:13088\n"
        "v_mfma_f32_16x16x32_bf16 v[172:175], a[88:91], a[248:251], v[172:175]\n"
        "buffer_load_dwordx4 v130, s[20:23], s56 offen offset:2816 lds\n"
        "ds_read_b128 a[172:175], v137 offset:12352\n"
        "v_mfma_f32_16x16x32_bf16 v[192:195], a[88:91], a[216:219], v[192:195]\n"
        "ds_read_b128 a[168:171], v137 offset:13120\n"
        "s_add_u32 m0, s65, 5376\n"
        "ds_read_b128 a[228:231], v241 offset:1792\n"
        "v_accvgpr_write_b32 a43, v126\n"
        "v_mfma_f32_16x16x32_bf16 v[212:215], a[88:91], a[220:223], v[212:215]\n"
        "ds_read_b128 a[232:235], v241 offset:12544\n"
        "ds_read_b128 a[236:239], v241 offset:14080\n"
        "v_mfma_f32_16x16x32_bf16 v[224:227], a[40:43], a[220:223], v[224:227]\n"
        "s_waitcnt lgkmcnt(10)\n"
        "v_lshl_add_u32 v60, v60, 2, s67\n"
        "v_lshl_add_u32 v61, v61, 2, s67\n"
        "v_lshl_add_u32 v62, v62, 2, s67\n"
        "v_mfma_f32_16x16x32_bf16 v[204:207], a[40:43], a[216:219], v[204:207]\n"
        "buffer_load_dwordx4 v128, s[20:23], s57 offen offset:768 lds\n"
        "v_lshl_add_u32 v63, v63, 2, s67\n"
        "v_mfma_f32_16x16x32_bf16 v[184:187], a[40:43], a[248:251], v[184:187]\n"
        "ds_read_b32 v60, v60\n"
        "ds_read_b32 v61, v61\n"
        "v_mfma_f32_16x16x32_bf16 v[164:167], a[40:43], a[244:247], v[164:167]\n"
        "s_waitcnt lgkmcnt(7)\n"
        "ds_read_b32 v62, v62\n"
        "ds_read_b32 v63, v63\n"
        "v_mfma_f32_16x16x32_bf16 v[104:107], a[188:191], v[148:151], 0\n"
        "v_cvt_pk_bf16_f32 v96, v96, v97\n"
        "v_accvgpr_write_b32 a64, v96\n"
        "v_mfma_f32_16x16x32_bf16 v[108:111], a[184:187], v[148:151], 0\n"
        "buffer_load_dwordx4 v129, s[20:23], s57 offen offset:1792 lds\n"
        "v_mfma_f32_16x16x32_bf16 v[88:91], a[112:115], a[220:223], v[88:91]\n"
        "v_cvt_pk_bf16_f32 v98, v98, v99\n"
        "v_accvgpr_write_b32 a65, v98\n"
        "v_mfma_f32_16x16x32_bf16 v[80:83], a[112:115], a[216:219], v[80:83]\n"
        "v_cvt_pk_bf16_f32 v100, v100, v101\n"
        "v_accvgpr_write_b32 a66, v100\n"
        "v_mfma_f32_16x16x32_bf16 v[68:71], a[112:115], a[248:251], v[68:71]\n"
        "v_cvt_pk_bf16_f32 v102, v102, v103\n"
        "v_accvgpr_write_b32 a67, v102\n"
        "v_mfma_f32_16x16x32_bf16 v[64:67], a[112:115], a[244:247], v[64:67]\n"
        "s_waitcnt vmcnt(10)\n"
        "ds_read_b64_tr_b16 a[92:93], v255 offset:15360\n"
        "ds_read_b64_tr_b16 a[94:95], v255 offset:16128\n"
        "v_mfma_f32_16x16x32_bf16 v[160:163], a[64:67], a[244:247], v[160:163]\n"
        "ds_read_b64_tr_b16 a[116:117], v255 offset:15392\n"
        "ds_read_b64_tr_b16 a[118:119], v255 offset:16160\n"
        "v_mfma_f32_16x16x32_bf16 v[180:183], a[64:67], a[248:251], v[180:183]\n"
        "ds_read_b64_tr_b16 a[140:141], v255 offset:15424\n"
        "ds_read_b64_tr_b16 a[142:143], v255 offset:16192\n"
        "v_mfma_f32_16x16x32_bf16 v[200:203], a[64:67], a[216:219], v[200:203]\n"
        "v_cvt_pk_bf16_f32 v104, v104, v105\n"
        "v_accvgpr_write_b32 a20, v104\n"
        "v_mfma_f32_16x16x32_bf16 v[220:223], a[64:67], a[220:223], v[220:223]\n"
        "v_cvt_pk_bf16_f32 v106, v106, v107\n"
        "v_accvgpr_write_b32 a21, v106\n"
        "v_mfma_f32_16x16x32_bf16 v[112:115], a[180:183], v[148:151], 0\n"
        "v_cvt_pk_bf16_f32 v108, v108, v109\n"
        "v_accvgpr_write_b32 a22, v108\n"
        "v_mfma_f32_16x16x32_bf16 v[116:119], a[176:179], v[148:151], 0\n"
        "v_cvt_pk_bf16_f32 v110, v110, v111\n"
        "v_accvgpr_write_b32 a23, v110\n"
        "v_mfma_f32_16x16x32_bf16 v[72:75], a[136:139], a[244:247], v[72:75]\n"
        "v_mfma_f32_16x16x32_bf16 v[76:79], a[136:139], a[248:251], v[76:79]\n"
        "v_mfma_f32_16x16x32_bf16 v[156:159], a[20:23], a[224:227], v[156:159]\n"
        "s_waitcnt lgkmcnt(0)\n"
        "v_cvt_pk_bf16_f32 v112, v112, v113\n"
        "v_accvgpr_write_b32 a44, v112\n"
        "v_mfma_f32_16x16x32_bf16 v[176:179], a[20:23], a[228:231], v[176:179]\n"
        "v_cvt_pk_bf16_f32 v114, v114, v115\n"
        "v_accvgpr_write_b32 a45, v114\n"
        "v_mfma_f32_16x16x32_bf16 v[196:199], a[20:23], a[232:235], v[196:199]\n"
        "v_cvt_pk_bf16_f32 v116, v116, v117\n"
        "v_accvgpr_write_b32 a46, v116\n"
        "v_cvt_pk_bf16_f32 v156, v156, v157\n"
        "v_mfma_f32_16x16x32_bf16 v[216:219], a[20:23], a[236:239], v[216:219]\n"
        "v_cvt_pk_bf16_f32 v157, v158, v159\n"
        "v_cvt_pk_bf16_f32 v176, v176, v177\n"
        "v_cvt_pk_bf16_f32 v177, v178, v179\n"
        "v_mfma_f32_16x16x32_bf16 v[120:123], a[172:175], v[148:151], 0\n"
        "v_cvt_pk_bf16_f32 v196, v196, v197\n"
        "v_cvt_pk_bf16_f32 v197, v198, v199\n"
        "v_mfma_f32_16x16x32_bf16 v[124:127], a[168:171], v[148:151], 0\n"
        "v_cvt_pk_bf16_f32 v118, v118, v119\n"
        "v_cvt_pk_bf16_f32 v216, v216, v217\n"
        "v_cvt_pk_bf16_f32 v217, v218, v219\n"
        "v_mfma_f32_16x16x32_bf16 v[84:87], a[136:139], a[216:219], v[84:87]\n"
        "v_accvgpr_write_b32 a47, v118\n"
        "v_cvt_pk_bf16_f32 v120, v120, v121\n"
        "v_mfma_f32_16x16x32_bf16 v[92:95], a[136:139], a[220:223], v[92:95]\n"
        "v_accvgpr_write_b32 a68, v120\n"
        "v_cvt_pk_bf16_f32 v122, v122, v123\n"
        "v_mfma_f32_16x16x32_bf16 v[224:227], a[44:47], a[236:239], v[224:227]\n"
        "v_accvgpr_write_b32 a69, v122\n"
        "v_cvt_pk_bf16_f32 v124, v124, v125\n"
        "v_mfma_f32_16x16x32_bf16 v[204:207], a[44:47], a[232:235], v[204:207]\n"
        "v_accvgpr_write_b32 a70, v124\n"
        "v_cvt_pk_bf16_f32 v126, v126, v127\n"
        "v_mfma_f32_16x16x32_bf16 v[184:187], a[44:47], a[228:231], v[184:187]\n"
        "v_accvgpr_write_b32 a71, v126\n"
        "s_nop 0\n"
        "v_cvt_pk_bf16_f32 v218, v224, v225\n"
        "v_mfma_f32_16x16x32_bf16 v[164:167], a[44:47], a[224:227], v[164:167]\n"
        "v_cvt_pk_bf16_f32 v219, v226, v227\n"
        "v_cvt_pk_bf16_f32 v198, v204, v205\n"
        "v_cvt_pk_bf16_f32 v199, v206, v207\n"
        "v_mfma_f32_16x16x32_bf16 v[152:155], a[92:95], a[224:227], v[152:155]\n"
        "v_cvt_pk_bf16_f32 v178, v184, v185\n"
        "v_cvt_pk_bf16_f32 v179, v186, v187\n"
        "v_mfma_f32_16x16x32_bf16 v[64:67], a[116:119], a[224:227], v[64:67]\n"
        "buffer_load_dwordx4 v130, s[20:23], s57 offen offset:2816 lds\n"
        "v_cvt_pk_bf16_f32 v158, v164, v165\n"
        "v_mfma_f32_16x16x32_bf16 v[72:75], a[140:143], a[224:227], v[72:75]\n"
        "s_add_u32 m0, s65, 8448\n"
        "v_cvt_pk_bf16_f32 v159, v166, v167\n"
        "v_cvt_pk_bf16_f32 v152, v152, v153\n"
        "v_mfma_f32_16x16x32_bf16 v[172:175], a[92:95], a[228:231], v[172:175]\n"
        "v_cvt_pk_bf16_f32 v153, v154, v155\n"
        "v_cvt_pk_bf16_f32 v154, v64, v65\n"
        "v_mfma_f32_16x16x32_bf16 v[160:163], a[68:71], a[224:227], v[160:163]\n"
        "v_cvt_pk_bf16_f32 v155, v66, v67\n"
        "v_cvt_pk_bf16_f32 v168, v72, v73\n"
        "v_mfma_f32_16x16x32_bf16 v[180:183], a[68:71], a[228:231], v[180:183]\n"
        "buffer_load_dwordx4 v128, s[20:23], s58 offen offset:768 lds\n"
        "v_mfma_f32_16x16x32_bf16 v[200:203], a[68:71], a[232:235], v[200:203]\n"
        "v_cvt_pk_bf16_f32 v169, v74, v75\n"
        "v_cvt_pk_bf16_f32 v172, v172, v173\n"
        "v_mfma_f32_16x16x32_bf16 v[220:223], a[68:71], a[236:239], v[220:223]\n"
        "v_cvt_pk_bf16_f32 v166, v160, v161\n"
        "v_cvt_pk_bf16_f32 v160, v160, v161\n"
        "v_cvt_pk_bf16_f32 v161, v162, v163\n"
        "v_cvt_pk_bf16_f32 v167, v162, v163\n"
        "v_cvt_pk_bf16_f32 v186, v180, v181\n"
        "v_cvt_pk_bf16_f32 v180, v180, v181\n"
        "v_mfma_f32_16x16x32_bf16 v[68:71], a[116:119], a[228:231], v[68:71]\n"
        "buffer_load_dwordx4 v129, s[20:23], s58 offen offset:1792 lds\n"
        "v_cvt_pk_bf16_f32 v181, v182, v183\n"
        "v_cvt_pk_bf16_f32 v187, v182, v183\n"
        "v_cvt_pk_bf16_f32 v206, v200, v201\n"
        "v_cvt_pk_bf16_f32 v200, v200, v201\n"
        "v_mfma_f32_16x16x32_bf16 v[76:79], a[140:143], a[228:231], v[76:79]\n"
        "s_waitcnt vmcnt(11)\n"
        "s_barrier\n"
        "ds_read_b128 v[162:165], v251 offset:0\n"
        "ds_read_b128 v[182:185], v251 offset:512\n"
        "v_cvt_pk_bf16_f32 v201, v202, v203\n"
        "v_cvt_pk_bf16_f32 v207, v202, v203\n"
        "v_cvt_pk_bf16_f32 v226, v220, v221\n"
        "v_cvt_pk_bf16_f32 v220, v220, v221\n"
        "v_mfma_f32_16x16x32_bf16 v[192:195], a[92:95], a[232:235], v[192:195]\n"
        "ds_read_b128 v[202:205], v251 offset:4096\n"
        "ds_read_b128 a[240:243], v240 offset:0\n"
        "v_cvt_pk_bf16_f32 v221, v222, v223\n"
        "v_cvt_pk_bf16_f32 v227, v222, v223\n"
        "v_cvt_pk_bf16_f32 v173, v174, v175\n"
        "v_mfma_f32_16x16x32_bf16 v[80:83], a[116:119], a[232:235], v[80:83]\n"
        "buffer_load_dwordx4 v130, s[20:23], s58 offen offset:2816 lds\n"
        "v_mfma_f32_16x16x32_bf16 v[84:87], a[140:143], a[232:235], v[84:87]\n"
        "s_add_u32 m0, s65, 11520\n"
        "ds_read_b128 a[244:247], v241 offset:0\n"
        "ds_read_b128 a[248:251], v240 offset:128\n"
        "v_mfma_f32_16x16x32_bf16 v[212:215], a[92:95], a[236:239], v[212:215]\n"
        "v_cvt_pk_bf16_f32 v174, v68, v69\n"
        "v_cvt_pk_bf16_f32 v175, v70, v71\n"
        "v_mfma_f32_16x16x32_bf16 v[88:91], a[116:119], a[236:239], v[88:91]\n"
        "buffer_load_dwordx4 v128, s[20:23], s59 offen offset:768 lds\n"
        "v_mfma_f32_16x16x32_bf16 v[92:95], a[140:143], a[236:239], v[92:95]\n"
        "s_waitcnt vmcnt(10)\n"
        "ds_read_b64_tr_b16 a[144:145], v255 offset:0\n"
        "ds_read_b64_tr_b16 a[146:147], v255 offset:768\n"
        "v_mfma_f32_16x16x32_bf16 v[64:67], v[152:155], v[156:159], v[0:3]\n"
        "s_waitcnt lgkmcnt(6)\n"
        "ds_read_b64_tr_b16 a[168:169], v255 offset:32\n"
        "ds_read_b64_tr_b16 a[170:171], v255 offset:800\n"
        "v_cvt_pk_bf16_f32 v190, v76, v77\n"
        "v_cvt_pk_bf16_f32 v191, v78, v79\n"
        "v_mfma_f32_16x16x32_bf16 v[64:67], v[168:171], v[160:163], v[64:67]\n"
        "ds_read_b64_tr_b16 a[192:193], v255 offset:64\n"
        "ds_read_b64_tr_b16 a[194:195], v255 offset:832\n"
        "v_cvt_pk_bf16_f32 v192, v192, v193\n"
        "v_cvt_pk_bf16_f32 v193, v194, v195\n"
        "v_mfma_f32_16x16x32_bf16 v[96:99], v[172:175], v[156:159], v[4:7]\n"
        "buffer_load_dwordx4 v129, s[20:23], s59 offen offset:1792 lds\n"
        "v_cvt_pk_bf16_f32 v194, v80, v81\n"
        "v_cvt_pk_bf16_f32 v195, v82, v83\n"
        "v_exp_f32 v64, v64\n"
        "v_mfma_f32_16x16x32_bf16 v[96:99], v[188:191], v[164:167], v[96:99]\n"
        "ds_read_b128 v[162:165], v252 offset:0\n"
        "v_exp_f32 v65, v65\n"
        "v_exp_f32 v66, v66\n"
        "v_cvt_pk_bf16_f32 v64, v64, v65\n"
        "v_mfma_f32_16x16x32_bf16 v[72:75], v[152:155], v[176:179], v[16:19]\n"
        "buffer_load_dwordx4 v130, s[20:23], s59 offen offset:2816 lds\n"
        "v_cvt_pk_bf16_f32 v208, v84, v85\n"
        "v_cvt_pk_bf16_f32 v209, v86, v87\n"
        "v_exp_f32 v96, v96\n"
        "v_mfma_f32_16x16x32_bf16 v[72:75], v[168:171], v[180:183], v[72:75]\n"
        "s_add_u32 m0, s65, 14592\n"
        "s_waitcnt vmcnt(9)\n"
        "ds_read_b64_tr_b16 a[148:149], v255 offset:3072\n"
        "ds_read_b64_tr_b16 a[150:151], v255 offset:3840\n"
        "v_exp_f32 v67, v67\n"
        "v_exp_f32 v97, v97\n"
        "v_mfma_f32_16x16x32_bf16 v[100:103], v[172:175], v[176:179], v[20:23]\n"
        "buffer_load_dwordx4 v128, s[20:23], s60 offen offset:768 lds\n"
        "ds_read_b64_tr_b16 a[172:173], v255 offset:3104\n"
        "ds_read_b64_tr_b16 a[174:175], v255 offset:3872\n"
        "v_cvt_pk_bf16_f32 v212, v212, v213\n"
        "v_cvt_pk_bf16_f32 v213, v214, v215\n"
        "v_mfma_f32_16x16x32_bf16 v[100:103], v[188:191], v[184:187], v[100:103]\n"
        "ds_read_b128 v[182:185], v252 offset:512\n"
        "ds_read_b64_tr_b16 a[196:197], v255 offset:3136\n"
        "ds_read_b64_tr_b16 a[198:199], v255 offset:3904\n"
        "v_cvt_pk_bf16_f32 v65, v66, v67\n"
        "v_exp_f32 v98, v98\n"
        "v_mfma_f32_16x16x32_bf16 v[68:71], v[192:195], v[156:159], v[8:11]\n"
        "s_waitcnt lgkmcnt(7)\n"
        "ds_read_b128 a[216:219], v240 offset:1536\n"
        "ds_read_b128 a[220:223], v241 offset:1536\n"
        "v_cvt_pk_bf16_f32 v66, v96, v97\n"
        "v_exp_f32 v72, v72\n"
        "v_cvt_pk_bf16_f32 v214, v88, v89\n"
        "v_mfma_f32_16x16x32_bf16 v[68:71], v[208:211], v[160:163], v[68:71]\n"
        "buffer_load_dwordx4 v129, s[20:23], s60 offen offset:1792 lds\n"
        "s_waitcnt vmcnt(8)\n"
        "ds_read_b64_tr_b16 a[152:153], v255 offset:6144\n"
        "ds_read_b64_tr_b16 a[154:155], v255 offset:6912\n"
        "v_cvt_pk_bf16_f32 v215, v90, v91\n"
        "v_cvt_pk_bf16_f32 v230, v92, v93\n"
        "v_mfma_f32_16x16x32_bf16 v[76:79], v[192:195], v[176:179], v[24:27]\n"
        "s_waitcnt lgkmcnt(6)\n"
        "ds_read_b64_tr_b16 a[176:177], v255 offset:6176\n"
        "ds_read_b64_tr_b16 a[178:179], v255 offset:6944\n"
        "v_exp_f32 v99, v99\n"
        "v_exp_f32 v73, v73\n"
        "v_mfma_f32_16x16x32_bf16 v[76:79], v[208:211], v[180:183], v[76:79]\n"
        ".long 0xbec200ff\n"
        "42:\n"
        "s_branch 60b\n"
        "buffer_load_dwordx4 v130, s[20:23], s60 offen offset:2816 lds\n"
        "ds_read_b64_tr_b16 a[200:201], v255 offset:6208\n"
        "ds_read_b64_tr_b16 a[202:203], v255 offset:6976\n"
        "v_cvt_pk_bf16_f32 v231, v94, v95\n"
        "v_cvt_pk_bf16_f32 v67, v98, v99\n"
        "v_mfma_f32_16x16x32_bf16 v[104:107], v[212:215], v[156:159], v[12:15]\n"
        "s_add_u32 m0, s40, 98304\n"
        "ds_read_b128 a[224:227], v240 offset:1664\n"
        "v_exp_f32 v74, v74\n"
        "v_mfma_f32_16x16x32_bf16 v[104:107], v[228:231], v[164:167], v[104:107]\n"
        "s_sub_u32 s64, s47, 6291456\n"
        "v_cvt_pk_bf16_f32 v72, v72, v73\n"
        "v_exp_f32 v100, v100\n"
        "v_mfma_f32_16x16x32_bf16 v[108:111], v[212:215], v[176:179], v[28:31]\n"
        "buffer_load_dwordx4 v242, s[8:11], s64 offen lds\n"
        "v_mfma_f32_16x16x32_bf16 v[108:111], v[228:231], v[184:187], v[108:111]\n"
        "s_add_u32 m0, s40, 99328\n"
        "v_exp_f32 v75, v75\n"
        "v_mfma_f32_16x16x32_bf16 v[80:83], v[152:155], v[196:199], v[32:35]\n"
        "ds_read_b128 a[228:231], v240 offset:12288\n"
        "v_exp_f32 v101, v101\n"
        "v_mfma_f32_16x16x32_bf16 v[80:83], v[168:171], v[200:203], v[80:83]\n"
        "buffer_load_dwordx4 v243, s[8:11], s64 offen lds\n"
        "v_cvt_pk_bf16_f32 v73, v74, v75\n"
        "v_mfma_f32_16x16x32_bf16 v[112:115], a[240:243], a[144:147], 0\n"
        "s_add_u32 m0, s40, 100352\n"
        "ds_read_b128 a[232:235], v241 offset:12288\n"
        "ds_read_b128 a[236:239], v240 offset:12416\n"
        "v_cvt_pk_bf16_f32 v74, v100, v101\n"
        "v_mfma_f32_16x16x32_bf16 v[112:115], a[244:247], a[148:151], v[112:115]\n"
        "s_waitcnt lgkmcnt(3)\n"
        "v_exp_f32 v102, v102\n"
        "v_mfma_f32_16x16x32_bf16 v[112:115], a[248:251], a[152:155], v[112:115]\n"
        "buffer_load_dwordx4 v244, s[8:11], s64 offen lds\n"
        "v_mfma_f32_16x16x32_bf16 v[116:119], a[248:251], a[176:179], 0\n"
        "s_add_u32 m0, s40, 110592\n"
        "v_exp_f32 v68, v68\n"
        "v_mfma_f32_16x16x32_bf16 v[116:119], a[244:247], a[172:175], v[116:119]\n"
        "s_sub_u32 s64, s47, 5505024\n"
        "v_exp_f32 v103, v103\n"
        "v_mfma_f32_16x16x32_bf16 v[116:119], a[240:243], a[168:171], v[116:119]\n"
        "buffer_load_dwordx4 v242, s[8:11], s64 offen lds\n"
        "v_cvt_pk_bf16_f32 v75, v102, v103\n"
        "v_mfma_f32_16x16x32_bf16 v[96:99], v[172:175], v[196:199], v[36:39]\n"
        "s_add_u32 m0, s40, 111616\n"
        "s_waitcnt vmcnt(10)\n"
        "ds_read_b64_tr_b16 a[156:157], v255 offset:9216\n"
        "ds_read_b64_tr_b16 a[158:159], v255 offset:9984\n"
        "v_mfma_f32_16x16x32_bf16 v[96:99], v[188:191], v[204:207], v[96:99]\n"
        "ds_read_b64_tr_b16 a[180:181], v255 offset:9248\n"
        "ds_read_b64_tr_b16 a[182:183], v255 offset:10016\n"
        "ds_read_b128 v[202:205], v252 offset:4096\n"
        "v_mfma_f32_16x16x32_bf16 v[120:123], a[240:243], a[192:195], 0\n"
        "buffer_load_dwordx4 v243, s[8:11], s64 offen lds\n"
        "ds_read_b64_tr_b16 a[204:205], v255 offset:9280\n"
        "v_mfma_f32_16x16x32_bf16 v[120:123], a[244:247], a[196:199], v[120:123]\n"
        "ds_read_b64_tr_b16 a[206:207], v255 offset:10048\n"
        "s_add_u32 m0, s40, 112640\n"
        "ds_read_b128 a[240:243], v240 offset:13824\n"
        "v_mfma_f32_16x16x32_bf16 v[120:123], a[248:251], a[200:203], v[120:123]\n"
        "s_waitcnt vmcnt(8)\n"
        "ds_read_b64_tr_b16 a[160:161], v255 offset:12288\n"
        "ds_read_b64_tr_b16 a[162:163], v255 offset:13056\n"
        "v_mfma_f32_16x16x32_bf16 v[124:127], a[216:219], a[144:147], 0\n"
        "buffer_load_dwordx4 v244, s[8:11], s64 offen lds\n"
        "ds_read_b64_tr_b16 a[184:185], v255 offset:12320\n"
        "v_mfma_f32_16x16x32_bf16 v[124:127], a[220:223], a[148:151], v[124:127]\n"
        "ds_read_b64_tr_b16 a[186:187], v255 offset:13088\n"
        "s_xor_b32 m0, s48, 8192\n"
        "ds_read_b64_tr_b16 a[208:209], v255 offset:12352\n"
        "ds_read_b64_tr_b16 a[210:211], v255 offset:13120\n"
        "v_mfma_f32_16x16x32_bf16 v[124:127], a[224:227], a[152:155], v[124:127]\n"
        "s_sub_u32 s64, s36, 2097152\n"
        "ds_read_b128 a[244:247], v241 offset:13824\n"
        "ds_read_b128 a[248:251], v240 offset:13952\n"
        "v_mfma_f32_16x16x32_bf16 v[156:159], a[224:227], a[176:179], 0\n"
        "buffer_load_dwordx4 v249, s[12:15], s64 offen lds\n"
        "v_mfma_f32_16x16x32_bf16 v[156:159], a[220:223], a[172:175], v[156:159]\n"
        "ds_read_b128 v[222:225], v251 offset:4608\n"
        "v_exp_f32 v69, v69\n"
        "v_mfma_f32_16x16x32_bf16 v[156:159], a[216:219], a[168:171], v[156:159]\n"
        "v_exp_f32 v70, v70\n"
        "v_cvt_pk_bf16_f32 v68, v68, v69\n"
        "v_mfma_f32_16x16x32_bf16 v[84:87], v[192:195], v[196:199], v[40:43]\n"
        "s_waitcnt lgkmcnt(7)\n"
        "v_exp_f32 v76, v76\n"
        "v_mfma_f32_16x16x32_bf16 v[84:87], v[208:211], v[200:203], v[84:87]\n"
        "buffer_load_dwordx4 v253, s[12:15], s64 offen offset:1024 lds\n"
        "v_mfma_f32_16x16x32_bf16 v[160:163], a[216:219], a[192:195], 0\n"
        "s_add_u32 m0, s65, 12288\n"
        "v_exp_f32 v71, v71\n"
        "v_mfma_f32_16x16x32_bf16 v[160:163], a[220:223], a[196:199], v[160:163]\n"
        "ds_read_b128 a[216:219], v241 offset:128\n"
        "v_exp_f32 v77, v77\n"
        "v_mfma_f32_16x16x32_bf16 v[160:163], a[224:227], a[200:203], v[160:163]\n"
        "ds_read_b128 a[220:223], v240 offset:256\n"
        "v_cvt_pk_bf16_f32 v69, v70, v71\n"
        "v_cvt_pk_bf16_f32 v76, v76, v77\n"
        "v_mfma_f32_16x16x32_bf16 v[164:167], a[228:231], a[144:147], 0\n"
        "s_waitcnt lgkmcnt(2)\n"
        "buffer_load_dword v136, s[52:55], 0 offen lds\n"
        "v_mfma_f32_16x16x32_bf16 v[164:167], a[232:235], a[148:151], v[164:167]\n"
        "ds_read_b128 a[224:227], v241 offset:256\n"
        "v_exp_f32 v78, v78\n"
        "v_mfma_f32_16x16x32_bf16 v[164:167], a[236:239], a[152:155], v[164:167]\n"
        "v_exp_f32 v104, v104\n"
        "v_mfma_f32_16x16x32_bf16 v[100:103], v[212:215], v[196:199], v[44:47]\n"
        "v_exp_f32 v79, v79\n"
        "v_mfma_f32_16x16x32_bf16 v[100:103], v[228:231], v[204:207], v[100:103]\n"
        "v_exp_f32 v105, v105\n"
        "v_cvt_pk_bf16_f32 v77, v78, v79\n"
        "v_mfma_f32_16x16x32_bf16 v[176:179], a[236:239], a[176:179], 0\n"
        "v_exp_f32 v106, v106\n"
        "v_cvt_pk_bf16_f32 v70, v104, v105\n"
        "v_mfma_f32_16x16x32_bf16 v[176:179], a[232:235], a[172:175], v[176:179]\n"
        "v_exp_f32 v108, v108\n"
        "v_mfma_f32_16x16x32_bf16 v[176:179], a[228:231], a[168:171], v[176:179]\n"
        "s_waitcnt vmcnt(9)\n"
        "ds_read_b64_tr_b16 a[164:165], v255 offset:15360\n"
        "ds_read_b64_tr_b16 a[166:167], v255 offset:16128\n"
        "v_mfma_f32_16x16x32_bf16 v[180:183], a[228:231], a[192:195], 0\n"
        "ds_read_b64_tr_b16 a[188:189], v255 offset:15392\n"
        "ds_read_b64_tr_b16 a[190:191], v255 offset:16160\n"
        "v_mfma_f32_16x16x32_bf16 v[180:183], a[232:235], a[196:199], v[180:183]\n"
        "ds_read_b64_tr_b16 a[212:213], v255 offset:15424\n"
        "ds_read_b64_tr_b16 a[214:215], v255 offset:16192\n"
        "v_mfma_f32_16x16x32_bf16 v[180:183], a[236:239], a[200:203], v[180:183]\n"
        "ds_read_b128 a[228:231], v241 offset:1664\n"
        "ds_read_b128 a[232:235], v240 offset:1792\n"
        "v_mfma_f32_16x16x32_bf16 v[184:187], a[240:243], a[144:147], 0\n"
        "ds_read_b128 a[236:239], v241 offset:1792\n"
        "v_exp_f32 v107, v107\n"
        "v_mfma_f32_16x16x32_bf16 v[184:187], a[244:247], a[148:151], v[184:187]\n"
        "v_exp_f32 v109, v109\n"
        "v_cvt_pk_bf16_f32 v71, v106, v107\n"
        "v_mfma_f32_16x16x32_bf16 v[184:187], a[248:251], a[152:155], v[184:187]\n"
        "v_exp_f32 v110, v110\n"
        "v_cvt_pk_bf16_f32 v78, v108, v109\n"
        "v_mfma_f32_16x16x32_bf16 v[196:199], a[248:251], a[176:179], 0\n"
        "v_exp_f32 v111, v111\n"
        "v_mfma_f32_16x16x32_bf16 v[196:199], a[244:247], a[172:175], v[196:199]\n"
        "v_cvt_pk_bf16_f32 v79, v110, v111\n"
        "v_exp_f32 v80, v80\n"
        "v_mfma_f32_16x16x32_bf16 v[196:199], a[240:243], a[168:171], v[196:199]\n"
        "v_exp_f32 v81, v81\n"
        "v_mfma_f32_16x16x32_bf16 v[200:203], a[240:243], a[192:195], 0\n"
        "v_exp_f32 v82, v82\n"
        "v_cvt_pk_bf16_f32 v80, v80, v81\n"
        "v_mfma_f32_16x16x32_bf16 v[200:203], a[244:247], a[196:199], v[200:203]\n"
        "ds_read_b128 a[240:243], v241 offset:12416\n"
        "v_exp_f32 v83, v83\n"
        "v_mfma_f32_16x16x32_bf16 v[200:203], a[248:251], a[200:203], v[200:203]\n"
        "ds_read_b128 a[244:247], v240 offset:12544\n"
        "v_cvt_pk_bf16_f32 v81, v82, v83\n"
        "v_mfma_f32_16x16x32_bf16 v[88:91], v[152:155], v[216:219], v[48:51]\n"
        "ds_read_b128 a[248:251], v241 offset:12544\n"
        "v_exp_f32 v96, v96\n"
        "v_mfma_f32_16x16x32_bf16 v[88:91], v[168:171], v[220:223], v[88:91]\n"
        "s_waitcnt lgkmcnt(3)\n"
        "v_exp_f32 v97, v97\n"
        "v_mfma_f32_16x16x32_bf16 v[112:115], a[216:219], a[156:159], v[112:115]\n"
        "v_exp_f32 v98, v98\n"
        "v_cvt_pk_bf16_f32 v82, v96, v97\n"
        "v_mfma_f32_16x16x32_bf16 v[112:115], a[220:223], a[160:163], v[112:115]\n"
        "v_exp_f32 v99, v99\n"
        "v_mfma_f32_16x16x32_bf16 v[112:115], a[224:227], a[164:167], v[112:115]\n"
        "v_cvt_pk_bf16_f32 v83, v98, v99\n"
        "v_exp_f32 v84, v84\n"
        "v_mfma_f32_16x16x32_bf16 v[116:119], a[224:227], a[188:191], v[116:119]\n"
        "v_exp_f32 v85, v85\n"
        "v_mfma_f32_16x16x32_bf16 v[116:119], a[220:223], a[184:187], v[116:119]\n"
        "v_exp_f32 v86, v86\n"
        "v_cvt_pk_bf16_f32 v84, v84, v85\n"
        "v_mfma_f32_16x16x32_bf16 v[116:119], a[216:219], a[180:183], v[116:119]\n"
        "v_exp_f32 v87, v87\n"
        "v_mfma_f32_16x16x32_bf16 v[120:123], a[216:219], a[204:207], v[120:123]\n"
        "v_cvt_pk_bf16_f32 v85, v86, v87\n"
        "v_exp_f32 v100, v100\n"
        "v_mfma_f32_16x16x32_bf16 v[120:123], a[220:223], a[208:211], v[120:123]\n"
        "ds_read_b128 a[216:219], v241 offset:13952\n"
        "v_exp_f32 v101, v101\n"
        "v_mfma_f32_16x16x32_bf16 v[120:123], a[224:227], a[212:215], v[120:123]\n"
        "ds_read_b128 a[220:223], v240 offset:14080\n"
        "v_exp_f32 v102, v102\n"
        "v_mfma_f32_16x16x32_bf16 v[124:127], a[228:231], a[156:159], v[124:127]\n"
        "ds_read_b128 a[224:227], v241 offset:14080\n"
        "v_cvt_pk_bf16_f32 v86, v100, v101\n"
        "v_cvt_pk_bf16_f32 v128, v112, v113\n"
        "v_mfma_f32_16x16x32_bf16 v[124:127], a[232:235], a[160:163], v[124:127]\n"
        "v_exp_f32 v103, v103\n"
        "v_cvt_pk_bf16_f32 v129, v114, v115\n"
        "v_mfma_f32_16x16x32_bf16 v[124:127], a[236:239], a[164:167], v[124:127]\n"
        "v_cvt_pk_bf16_f32 v87, v102, v103\n"
        "v_cvt_pk_bf16_f32 v136, v116, v117\n"
        "v_mfma_f32_16x16x32_bf16 v[104:107], v[172:175], v[216:219], v[52:55]\n"
        "v_cvt_pk_bf16_f32 v137, v118, v119\n"
        "v_cvt_pk_bf16_f32 v144, v120, v121\n"
        "v_mfma_f32_16x16x32_bf16 v[104:107], v[188:191], v[224:227], v[104:107]\n"
        "ds_read_b128 v[222:225], v252 offset:4608\n"
        "v_cvt_pk_bf16_f32 v145, v122, v123\n"
        "v_mfma_f32_16x16x32_bf16 v[156:159], a[236:239], a[188:191], v[156:159]\n"
        "v_exp_f32 v88, v88\n"
        "v_mfma_f32_16x16x32_bf16 v[156:159], a[232:235], a[184:187], v[156:159]\n"
        "v_cvt_pk_bf16_f32 v130, v124, v125\n"
        "v_cvt_pk_bf16_f32 v131, v126, v127\n"
        "v_mfma_f32_16x16x32_bf16 v[156:159], a[228:231], a[180:183], v[156:159]\n"
        "v_exp_f32 v89, v89\n"
        "v_mfma_f32_16x16x32_bf16 v[160:163], a[228:231], a[204:207], v[160:163]\n"
        "v_exp_f32 v90, v90\n"
        "v_mfma_f32_16x16x32_bf16 v[160:163], a[232:235], a[208:211], v[160:163]\n"
        "v_exp_f32 v91, v91\n"
        "v_mfma_f32_16x16x32_bf16 v[160:163], a[236:239], a[212:215], v[160:163]\n"
        "s_waitcnt lgkmcnt(1)\n"
        "v_exp_f32 v104, v104\n"
        "v_mfma_f32_16x16x32_bf16 v[164:167], a[240:243], a[156:159], v[164:167]\n"
        "v_cvt_pk_bf16_f32 v138, v156, v157\n"
        "v_cvt_pk_bf16_f32 v139, v158, v159\n"
        "v_mfma_f32_16x16x32_bf16 v[164:167], a[244:247], a[160:163], v[164:167]\n"
        "v_cvt_pk_bf16_f32 v88, v88, v89\n"
        "v_cvt_pk_bf16_f32 v89, v90, v91\n"
        "v_mfma_f32_16x16x32_bf16 v[164:167], a[248:251], a[164:167], v[164:167]\n"
        "v_exp_f32 v105, v105\n"
        "v_mfma_f32_16x16x32_bf16 v[92:95], v[192:195], v[216:219], v[56:59]\n"
        "s_waitcnt lgkmcnt(0)\n"
        "v_cvt_pk_bf16_f32 v146, v160, v161\n"
        "v_cvt_pk_bf16_f32 v147, v162, v163\n"
        "v_mfma_f32_16x16x32_bf16 v[92:95], v[208:211], v[220:223], v[92:95]\n"
        "v_exp_f32 v106, v106\n"
        "v_mfma_f32_16x16x32_bf16 v[176:179], a[248:251], a[188:191], v[176:179]\n"
        "v_exp_f32 v107, v107\n"
        "v_mfma_f32_16x16x32_bf16 v[176:179], a[244:247], a[184:187], v[176:179]\n"
        "v_cvt_pk_bf16_f32 v132, v164, v165\n"
        "v_cvt_pk_bf16_f32 v133, v166, v167\n"
        "v_mfma_f32_16x16x32_bf16 v[176:179], a[240:243], a[180:183], v[176:179]\n"
        "v_cvt_pk_bf16_f32 v90, v104, v105\n"
        "v_cvt_pk_bf16_f32 v91, v106, v107\n"
        "v_mfma_f32_16x16x32_bf16 v[180:183], a[240:243], a[204:207], v[180:183]\n"
        "v_exp_f32 v92, v92\n"
        "v_mfma_f32_16x16x32_bf16 v[180:183], a[244:247], a[208:211], v[180:183]\n"
        "v_exp_f32 v93, v93\n"
        "v_mfma_f32_16x16x32_bf16 v[180:183], a[248:251], a[212:215], v[180:183]\n"
        "v_mov_b64 v[96:97], 0\n"
        "v_cvt_pk_bf16_f32 v140, v176, v177\n"
        "v_mfma_f32_16x16x32_bf16 v[184:187], a[216:219], a[156:159], v[184:187]\n"
        "v_cvt_pk_bf16_f32 v141, v178, v179\n"
        "v_cvt_pk_bf16_f32 v92, v92, v93\n"
        "v_mfma_f32_16x16x32_bf16 v[184:187], a[220:223], a[160:163], v[184:187]\n"
        "v_exp_f32 v94, v94\n"
        "v_mfma_f32_16x16x32_bf16 v[184:187], a[224:227], a[164:167], v[184:187]\n"
        "v_exp_f32 v95, v95\n"
        "v_mfma_f32_16x16x32_bf16 v[108:111], v[212:215], v[216:219], v[60:63]\n"
        "v_cvt_pk_bf16_f32 v148, v180, v181\n"
        "v_cvt_pk_bf16_f32 v149, v182, v183\n"
        "v_mfma_f32_16x16x32_bf16 v[108:111], v[228:231], v[224:227], v[108:111]\n"
        "v_add_u32 v240, 24576, v240\n"
        "v_add_u32 v241, 24576, v241\n"
        "v_mfma_f32_16x16x32_bf16 v[196:199], a[224:227], a[188:191], v[196:199]\n"
        "v_cvt_pk_bf16_f32 v134, v184, v185\n"
        "v_cvt_pk_bf16_f32 v135, v186, v187\n"
        "v_mfma_f32_16x16x32_bf16 v[196:199], a[220:223], a[184:187], v[196:199]\n"
        "v_mov_b64 v[172:173], 0\n"
        "v_cvt_pk_bf16_f32 v93, v94, v95\n"
        "v_mfma_f32_16x16x32_bf16 v[196:199], a[216:219], a[180:183], v[196:199]\n"
        "v_exp_f32 v108, v108\n"
        "v_mfma_f32_16x16x32_bf16 v[200:203], a[216:219], a[204:207], v[200:203]\n"
        "v_exp_f32 v109, v109\n"
        "v_mfma_f32_16x16x32_bf16 v[200:203], a[220:223], a[208:211], v[200:203]\n"
        "v_exp_f32 v110, v110\n"
        "v_mfma_f32_16x16x32_bf16 v[200:203], a[224:227], a[212:215], v[200:203]\n"
        "v_exp_f32 v111, v111\n"
        "v_smfmac_f32_16x16x64_bf16 v[96:99], a[252:255], v[64:71], v237\n"
        "v_cvt_pk_bf16_f32 v142, v196, v197\n"
        "v_mfma_f32_16x16x32_bf16 v[100:103], v[132:135], v[68:71], 0\n"
        "v_cvt_pk_bf16_f32 v143, v198, v199\n"
        "v_cvt_pk_bf16_f32 v94, v108, v109\n"
        "v_mfma_f32_16x16x32_bf16 v[100:103], v[128:131], v[64:67], v[100:103]\n"
        "v_mov_b64 v[192:193], 0\n"
        "v_cvt_pk_bf16_f32 v150, v200, v201\n"
        "v_mfma_f32_16x16x32_bf16 v[104:107], v[136:139], v[64:67], 0\n"
        "v_cvt_pk_bf16_f32 v151, v202, v203\n"
        "v_add_f32 v97, v96, v97\n"
        "v_mfma_f32_16x16x32_bf16 v[104:107], v[140:143], v[68:71], v[104:107]\n"
        "v_rcp_f32 v96, v97\n"
        "v_cvt_pk_bf16_f32 v95, v110, v111\n"
        "v_mfma_f32_16x16x32_bf16 v[108:111], v[148:151], v[68:71], 0\n"
        "v_mul_f32 v100, v96, v100\n"
        "v_mul_f32 v101, v96, v101\n"
        "v_mfma_f32_16x16x32_bf16 v[108:111], v[144:147], v[64:67], v[108:111]\n"
        "v_mul_f32 v102, v96, v102\n"
        "v_mul_f32 v103, v96, v103\n"
        "v_smfmac_f32_16x16x64_bf16 v[172:175], a[252:255], v[72:79], v237\n"
        "s_waitcnt vmcnt(6)\n"
        "s_barrier\n"
        "ds_read_b128 a[236:239], v240 offset:0\n"
        "v_mfma_f32_16x16x32_bf16 v[176:179], v[132:135], v[76:79], 0\n"
        "ds_read_b128 a[240:243], v241 offset:0\n"
        "ds_read_b128 a[244:247], v240 offset:128\n"
        "v_mfma_f32_16x16x32_bf16 v[176:179], v[128:131], v[72:75], v[176:179]\n"
        "ds_read_b128 a[248:251], v241 offset:128\n"
        "ds_read_b128 a[216:219], v240 offset:256\n"
        "v_mfma_f32_16x16x32_bf16 v[180:183], v[136:139], v[72:75], 0\n"
        "ds_read_b128 a[220:223], v241 offset:256\n"
        "v_cvt_pk_bf16_f32 v100, v100, v101\n"
        "v_mfma_f32_16x16x32_bf16 v[180:183], v[140:143], v[76:79], v[180:183]\n"
        "v_cvt_pk_bf16_f32 v101, v102, v103\n"
        "v_mul_f32 v104, v96, v104\n"
        "v_mfma_f32_16x16x32_bf16 v[184:187], v[148:151], v[76:79], 0\n"
        "v_mul_f32 v105, v96, v105\n"
        "v_mul_f32 v106, v96, v106\n"
        "v_mfma_f32_16x16x32_bf16 v[184:187], v[144:147], v[72:75], v[184:187]\n"
        "v_mul_f32 v107, v96, v107\n"
        "v_cvt_pk_bf16_f32 v102, v104, v105\n"
        "v_smfmac_f32_16x16x64_bf16 v[192:195], a[252:255], v[80:87], v237\n"
        "v_cvt_pk_bf16_f32 v103, v106, v107\n"
        "v_mfma_f32_16x16x32_bf16 v[196:199], v[132:135], v[84:87], 0\n"
        "ds_write_b128 v245, v[100:103] offset:0\n"
        "v_mfma_f32_16x16x32_bf16 v[196:199], v[128:131], v[80:83], v[196:199]\n"
        "buffer_load_dwordx4 v238, s[52:55], s63 offen lds\n"
        "v_mfma_f32_16x16x32_bf16 v[200:203], v[136:139], v[80:83], 0\n"
        "v_mov_b64 v[124:125], 0\n"
        "v_mul_f32 v108, v96, v108\n"
        "v_mfma_f32_16x16x32_bf16 v[200:203], v[140:143], v[84:87], v[200:203]\n"
        "v_mul_f32 v109, v96, v109\n"
        "v_mul_f32 v110, v96, v110\n"
        "v_mul_f32 v111, v96, v111\n"
        "v_mfma_f32_16x16x32_bf16 v[204:207], v[148:151], v[84:87], 0\n"
        "v_cvt_pk_bf16_f32 v108, v108, v109\n"
        "v_cvt_pk_bf16_f32 v109, v110, v111\n"
        "v_mfma_f32_16x16x32_bf16 v[204:207], v[144:147], v[80:83], v[204:207]\n"
        "s_waitcnt lgkmcnt(1)\n"
        "ds_write_b64 v246, v[108:109] offset:0\n"
        "v_mfma_f32_16x16x32_bf16 v[112:115], a[72:75], a[236:239], 0\n"
        "v_max_u32 v236, v96, v97\n"
        "v_add_f32 v173, v172, v173\n"
        "v_mfma_f32_16x16x32_bf16 v[112:115], a[76:79], a[240:243], v[112:115]\n"
        "v_rcp_f32 v172, v173\n"
        "v_mfma_f32_16x16x32_bf16 v[112:115], a[80:83], a[244:247], v[112:115]\n"
        "v_mul_f32 v176, v172, v176\n"
        "v_mul_f32 v177, v172, v177\n"
        "v_mfma_f32_16x16x32_bf16 v[112:115], a[84:87], a[248:251], v[112:115]\n"
        "buffer_load_dwordx4 v239, s[52:55], s63 offen offset:1024 lds\n"
        "v_mfma_f32_16x16x32_bf16 v[112:115], a[88:91], a[216:219], v[112:115]\n"
        "v_mul_f32 v178, v172, v178\n"
        "v_mul_f32 v179, v172, v179\n"
        "v_mfma_f32_16x16x32_bf16 v[112:115], a[92:95], a[220:223], v[112:115]\n"
        "v_cvt_pk_bf16_f32 v176, v176, v177\n"
        "v_cvt_pk_bf16_f32 v177, v178, v179\n"
        "v_smfmac_f32_16x16x64_bf16 v[124:127], a[252:255], v[88:95], v237\n"
        "v_mul_f32 v180, v172, v180\n"
        "v_mfma_f32_16x16x32_bf16 v[80:83], v[132:135], v[92:95], 0\n"
        "v_mul_f32 v181, v172, v181\n"
        "v_mul_f32 v182, v172, v182\n"
        "v_mfma_f32_16x16x32_bf16 v[80:83], v[128:131], v[88:91], v[80:83]\n"
        "v_cvt_pk_bf16_f32 v152, v112, v113\n"
        "v_cvt_pk_bf16_f32 v153, v114, v115\n"
        "v_mfma_f32_16x16x32_bf16 v[84:87], v[136:139], v[88:91], 0\n"
        "v_mul_f32 v183, v172, v183\n"
        "v_cvt_pk_bf16_f32 v178, v180, v181\n"
        "v_mfma_f32_16x16x32_bf16 v[84:87], v[140:143], v[92:95], v[84:87]\n"
        "v_cvt_pk_bf16_f32 v179, v182, v183\n"
        "v_mul_f32 v184, v172, v184\n"
        "v_mfma_f32_16x16x32_bf16 v[96:99], v[148:151], v[92:95], 0\n"
        "buffer_load_dwordx4 v247, s[52:55], s63 offen offset:2048 lds\n"
        "v_mfma_f32_16x16x32_bf16 v[96:99], v[144:147], v[88:91], v[96:99]\n"
        "ds_write_b128 v245, v[176:179] offset:256\n"
        "v_mfma_f32_16x16x32_bf16 v[116:119], a[116:119], a[220:223], 0\n"
        "v_mul_f32 v185, v172, v185\n"
        "v_mul_f32 v186, v172, v186\n"
        "v_mfma_f32_16x16x32_bf16 v[116:119], a[112:115], a[216:219], v[116:119]\n"
        "v_mul_f32 v187, v172, v187\n"
        "v_add_f32 v125, v124, v125\n"
        "v_mfma_f32_16x16x32_bf16 v[116:119], a[108:111], a[248:251], v[116:119]\n"
        "v_rcp_f32 v124, v125\n"
        "v_mfma_f32_16x16x32_bf16 v[116:119], a[104:107], a[244:247], v[116:119]\n"
        "s_add_u32 m0, s65, 15360\n"
        "v_cvt_pk_bf16_f32 v184, v184, v185\n"
        "v_cvt_pk_bf16_f32 v185, v186, v187\n"
        "v_mfma_f32_16x16x32_bf16 v[116:119], a[100:103], a[240:243], v[116:119]\n"
        "ds_write_b64 v246, v[184:185] offset:256\n"
        "v_mfma_f32_16x16x32_bf16 v[116:119], a[96:99], a[236:239], v[116:119]\n"
        "v_mul_f32 v80, v124, v80\n"
        "v_mul_f32 v81, v124, v81\n"
        "v_mfma_f32_16x16x32_bf16 v[120:123], a[120:123], a[236:239], 0\n"
        "buffer_load_dwordx4 v238, s[52:55], s34 offen lds\n"
        "v_mfma_f32_16x16x32_bf16 v[120:123], a[124:127], a[240:243], v[120:123]\n"
        "v_mul_f32 v82, v124, v82\n"
        "v_mul_f32 v83, v124, v83\n"
        "v_mfma_f32_16x16x32_bf16 v[120:123], a[128:131], a[244:247], v[120:123]\n"
        "v_max3_u32 v236, v236, v172, v173\n"
        "v_cvt_pk_bf16_f32 v80, v80, v81\n"
        "v_mfma_f32_16x16x32_bf16 v[120:123], a[132:135], a[248:251], v[120:123]\n"
        "v_cvt_pk_bf16_f32 v81, v82, v83\n"
        "v_mul_f32 v84, v124, v84\n"
        "v_mfma_f32_16x16x32_bf16 v[120:123], a[136:139], a[216:219], v[120:123]\n"
        "v_mul_f32 v85, v124, v85\n"
        "v_mul_f32 v86, v124, v86\n"
        "v_mfma_f32_16x16x32_bf16 v[120:123], a[140:143], a[220:223], v[120:123]\n"
        "v_mul_f32 v87, v124, v87\n"
        "v_cvt_pk_bf16_f32 v82, v84, v85\n"
        "v_mfma_f32_16x16x32_bf16 v[112:115], a[20:23], a[220:223], 0\n"
        "v_cvt_pk_bf16_f32 v83, v86, v87\n"
        "v_mul_f32 v96, v124, v96\n"
        "v_mfma_f32_16x16x32_bf16 v[112:115], a[16:19], a[216:219], v[112:115]\n"
        "ds_write_b128 v245, v[80:83] offset:768\n"
        "v_mfma_f32_16x16x32_bf16 v[112:115], a[12:15], a[248:251], v[112:115]\n"
        "buffer_load_dwordx4 v239, s[52:55], s34 offen offset:1024 lds\n"
        "v_mfma_f32_16x16x32_bf16 v[112:115], a[8:11], a[244:247], v[112:115]\n"
        "v_mul_f32 v97, v124, v97\n"
        "v_mul_f32 v98, v124, v98\n"
        "v_mfma_f32_16x16x32_bf16 v[112:115], a[4:7], a[240:243], v[112:115]\n"
        "v_mul_f32 v99, v124, v99\n"
        "v_cvt_pk_bf16_f32 v154, v116, v117\n"
        "v_mfma_f32_16x16x32_bf16 v[112:115], a[0:3], a[236:239], v[112:115]\n"
        "v_cvt_pk_bf16_f32 v155, v118, v119\n"
        "v_cvt_pk_bf16_f32 v96, v96, v97\n"
        "v_mfma_f32_16x16x32_bf16 v[116:119], a[24:27], a[236:239], 0\n"
        "v_cvt_pk_bf16_f32 v97, v98, v99\n"
        "v_cvt_pk_bf16_f32 v168, v120, v121\n"
        "v_mfma_f32_16x16x32_bf16 v[116:119], a[28:31], a[240:243], v[116:119]\n"
        "ds_write_b64 v246, v[96:97] offset:768\n"
        "v_mfma_f32_16x16x32_bf16 v[116:119], a[32:35], a[244:247], v[116:119]\n"
        "v_cvt_pk_bf16_f32 v169, v122, v123\n"
        "v_add_f32 v193, v192, v193\n"
        "v_mfma_f32_16x16x32_bf16 v[116:119], a[36:39], a[248:251], v[116:119]\n"
        "v_rcp_f32 v192, v193\n"
        "v_mfma_f32_16x16x32_bf16 v[116:119], a[40:43], a[216:219], v[116:119]\n"
        "buffer_load_dwordx4 v247, s[52:55], s34 offen offset:2048 lds\n"
        "v_mfma_f32_16x16x32_bf16 v[116:119], a[44:47], a[220:223], v[116:119]\n"
        "v_mul_f32 v196, v192, v196\n"
        "v_mul_f32 v197, v192, v197\n"
        "v_mfma_f32_16x16x32_bf16 v[120:123], a[68:71], a[220:223], 0\n"
        "v_mul_f32 v198, v192, v198\n"
        "v_mul_f32 v199, v192, v199\n"
        "v_mfma_f32_16x16x32_bf16 v[120:123], a[64:67], a[216:219], v[120:123]\n"
        "v_cvt_pk_bf16_f32 v156, v112, v113\n"
        "v_cvt_pk_bf16_f32 v157, v114, v115\n"
        "v_mfma_f32_16x16x32_bf16 v[120:123], a[60:63], a[248:251], v[120:123]\n"
        "ds_read_b128 a[224:227], v240 offset:1536\n"
        "v_cvt_pk_bf16_f32 v196, v196, v197\n"
        "v_mfma_f32_16x16x32_bf16 v[120:123], a[56:59], a[244:247], v[120:123]\n"
        "s_add_u32 m0, s65, 0\n"
        "ds_read_b128 a[228:231], v241 offset:1536\n"
        "v_cvt_pk_bf16_f32 v197, v198, v199\n"
        "v_mfma_f32_16x16x32_bf16 v[120:123], a[52:55], a[240:243], v[120:123]\n"
        "ds_read_b128 a[232:235], v240 offset:1664\n"
        "v_mul_f32 v200, v192, v200\n"
        "v_mfma_f32_16x16x32_bf16 v[120:123], a[48:51], a[236:239], v[120:123]\n"
        "v_mul_f32 v201, v192, v201\n"
        "v_mul_f32 v202, v192, v202\n"
        "v_mfma_f32_16x16x32_bf16 v[112:115], a[236:239], a[144:147], 0\n"
        "buffer_load_dwordx4 v232, s[52:55], 0 offen lds\n"
        "v_mfma_f32_16x16x32_bf16 v[112:115], a[240:243], a[148:151], v[112:115]\n"
        "v_mul_f32 v203, v192, v203\n"
        "v_cvt_pk_bf16_f32 v198, v200, v201\n"
        "v_mfma_f32_16x16x32_bf16 v[112:115], a[244:247], a[152:155], v[112:115]\n"
        "v_cvt_pk_bf16_f32 v199, v202, v203\n"
        "v_mul_f32 v204, v192, v204\n"
        "v_mfma_f32_16x16x32_bf16 v[112:115], a[248:251], a[156:159], v[112:115]\n"
        "ds_write_b128 v245, v[196:199] offset:512\n"
        "v_mfma_f32_16x16x32_bf16 v[112:115], a[216:219], a[160:163], v[112:115]\n"
        "v_mul_f32 v205, v192, v205\n"
        "v_mul_f32 v206, v192, v206\n"
        "v_mfma_f32_16x16x32_bf16 v[112:115], a[220:223], a[164:167], v[112:115]\n"
        "v_mul_f32 v207, v192, v207\n"
        "v_cvt_pk_bf16_f32 v158, v116, v117\n"
        "v_cvt_pk_bf16_f32 v159, v118, v119\n"
        "v_mfma_f32_16x16x32_bf16 v[116:119], a[220:223], a[188:191], 0\n"
        "v_cvt_pk_bf16_f32 v204, v204, v205\n"
        "v_cvt_pk_bf16_f32 v205, v206, v207\n"
        "v_mfma_f32_16x16x32_bf16 v[116:119], a[216:219], a[184:187], v[116:119]\n"
        "ds_write_b64 v246, v[204:205] offset:512\n"
        "v_mfma_f32_16x16x32_bf16 v[116:119], a[248:251], a[180:183], v[116:119]\n"
        "buffer_load_dwordx4 v233, s[52:55], 0 offen offset:1024 lds\n"
        "v_mfma_f32_16x16x32_bf16 v[116:119], a[244:247], a[176:179], v[116:119]\n"
        "s_waitcnt vmcnt(5)\n"
        "ds_read_b64_tr_b16 v[64:65], v255 offset:12288\n"
        "ds_read_b64_tr_b16 v[66:67], v255 offset:13056\n"
        "v_mfma_f32_16x16x32_bf16 v[116:119], a[240:243], a[172:175], v[116:119]\n"
        "v_max3_u32 v236, v236, v192, v193\n"
        "v_max3_u32 v236, v236, v124, v125\n"
        "v_mfma_f32_16x16x32_bf16 v[116:119], a[236:239], a[168:171], v[116:119]\n"
        "ds_read_b64_tr_b16 v[68:69], v255 offset:12320\n"
        "ds_read_b64_tr_b16 v[70:71], v255 offset:13088\n"
        "v_cvt_pk_bf16_f32 v160, v120, v121\n"
        "v_cvt_pk_bf16_f32 v161, v122, v123\n"
        "v_cvt_pk_bf16_f32 v166, v120, v121\n"
        "v_cvt_pk_bf16_f32 v167, v122, v123\n"
        "v_mfma_f32_16x16x32_bf16 v[120:123], a[236:239], a[192:195], 0\n"
        "ds_read_b128 a[236:239], v241 offset:1664\n"
        "v_cmp_lt_u32 vcc, s24, v236\n"
        "v_mfma_f32_16x16x32_bf16 v[120:123], a[240:243], a[196:199], v[120:123]\n"
        "ds_read_b128 a[240:243], v240 offset:1792\n"
        "s_cmp_lg_u64 vcc, 0\n"
        "s_addc_u32 s25, s25, s25\n"
        "v_mfma_f32_16x16x32_bf16 v[120:123], a[244:247], a[200:203], v[120:123]\n"
        "ds_read_b64_tr_b16 v[72:73], v255 offset:12352\n"
        "ds_read_b64_tr_b16 v[74:75], v255 offset:13120\n"
        "v_mfma_f32_16x16x32_bf16 v[120:123], a[248:251], a[204:207], v[120:123]\n"
        "ds_read_b128 a[244:247], v241 offset:1792\n"
        "v_cvt_pk_bf16_f32 v128, v112, v113\n"
        "v_mfma_f32_16x16x32_bf16 v[120:123], a[216:219], a[208:211], v[120:123]\n"
        "s_waitcnt lgkmcnt(7)\n"
        "ds_write_b128 v235, v[64:67] offset:12288\n"
        "v_mfma_f32_16x16x32_bf16 v[120:123], a[220:223], a[212:215], v[120:123]\n"
        "v_cvt_pk_bf16_f32 v129, v114, v115\n"
        "v_and_b32 v239, 32, v245\n"
        "v_mfma_f32_16x16x32_bf16 v[112:115], a[72:75], a[224:227], 0\n"
        "buffer_load_dwordx4 v234, s[52:55], 0 offen offset:2048 lds\n"
        "v_mfma_f32_16x16x32_bf16 v[112:115], a[76:79], a[228:231], v[112:115]\n"
        "s_waitcnt vmcnt(3)\n"
        "ds_read_b64_tr_b16 v[76:77], v255 offset:15360\n"
        "ds_read_b64_tr_b16 v[78:79], v255 offset:16128\n"
        "v_mfma_f32_16x16x32_bf16 v[112:115], a[80:83], a[232:235], v[112:115]\n"
        "s_waitcnt lgkmcnt(6)\n"
        "v_cvt_pk_bf16_f32 v136, v116, v117\n"
        "v_cvt_pk_bf16_f32 v137, v118, v119\n"
        "v_mfma_f32_16x16x32_bf16 v[112:115], a[84:87], a[236:239], v[112:115]\n"
        "s_add_u32 m0, s65, 3072\n"
        "v_xor_b32 v251, 0x2000, v251\n"
        "v_xor_b32 v252, 0x2000, v252\n"
        "v_mfma_f32_16x16x32_bf16 v[112:115], a[88:91], a[240:243], v[112:115]\n"
        "s_waitcnt lgkmcnt(3)\n"
        "v_mad_i32_i24 v239, v239, -12, v235\n"
        "v_cvt_pk_bf16_f32 v144, v120, v121\n"
        "v_mfma_f32_16x16x32_bf16 v[112:115], a[92:95], a[244:247], v[112:115]\n"
        "ds_write_b128 v239, v[68:71] offset:12480\n"
        "s_barrier\n"
        "v_mfma_f32_16x16x32_bf16 v[116:119], a[116:119], a[244:247], 0\n"
        "buffer_load_dwordx4 v232, s[52:55], s61 offen lds\n"
        "v_mfma_f32_16x16x32_bf16 v[116:119], a[112:115], a[240:243], v[116:119]\n"
        "ds_read_b64_tr_b16 v[88:89], v255 offset:15392\n"
        "ds_read_b64_tr_b16 v[90:91], v255 offset:16160\n"
        "v_mfma_f32_16x16x32_bf16 v[116:119], a[108:111], a[236:239], v[116:119]\n"
        "v_cvt_pk_bf16_f32 v145, v122, v123\n"
        "v_cvt_pk_bf16_f32 v172, v112, v113\n"
        "v_mfma_f32_16x16x32_bf16 v[116:119], a[104:107], a[232:235], v[116:119]\n"
        "v_cvt_pk_bf16_f32 v173, v114, v115\n"
        "v_add_u32 v247, s45, v245\n"
        "v_mfma_f32_16x16x32_bf16 v[116:119], a[100:103], a[228:231], v[116:119]\n"
        "v_mfma_f32_16x16x32_bf16 v[116:119], a[96:99], a[224:227], v[116:119]\n"
        "ds_write_b128 v254, v[72:75] offset:12288\n"
        "v_mfma_f32_16x16x32_bf16 v[120:123], a[120:123], a[224:227], 0\n"
        "s_nop 2\n"
        "v_mfma_f32_16x16x32_bf16 v[120:123], a[124:127], a[228:231], v[120:123]\n"
        "ds_read_b128 v[162:165], v251 offset:0\n"
        "s_nop 0\n"
        "v_mfma_f32_16x16x32_bf16 v[120:123], a[128:131], a[232:235], v[120:123]\n"
        "buffer_load_dwordx4 v233, s[52:55], s61 offen offset:1024 lds\n"
        "v_mfma_f32_16x16x32_bf16 v[120:123], a[132:135], a[236:239], v[120:123]\n"
        "ds_read_b64_tr_b16 v[92:93], v255 offset:15424\n"
        "ds_read_b64_tr_b16 v[94:95], v255 offset:16192\n"
        "v_mfma_f32_16x16x32_bf16 v[120:123], a[136:139], a[240:243], v[120:123]\n"
        "v_cvt_pk_bf16_f32 v174, v116, v117\n"
        "v_cvt_pk_bf16_f32 v175, v118, v119\n"
        "v_mfma_f32_16x16x32_bf16 v[120:123], a[140:143], a[244:247], v[120:123]\n"
        "s_nop 2\n"
        "v_mfma_f32_16x16x32_bf16 v[112:115], a[20:23], a[244:247], 0\n"
        "s_nop 2\n"
        "v_mfma_f32_16x16x32_bf16 v[112:115], a[16:19], a[240:243], v[112:115]\n"
        "v_cvt_pk_bf16_f32 v190, v120, v121\n"
        "v_cvt_pk_bf16_f32 v191, v122, v123\n"
        "v_mfma_f32_16x16x32_bf16 v[112:115], a[12:15], a[236:239], v[112:115]\n"
        "v_mfma_f32_16x16x32_bf16 v[112:115], a[8:11], a[232:235], v[112:115]\n"
        "s_waitcnt vmcnt(2)\n"
        "ds_read_b64_tr_b16 v[212:213], v255 offset:0\n"
        "ds_read_b64_tr_b16 v[214:215], v255 offset:768\n"
        "v_mfma_f32_16x16x32_bf16 v[112:115], a[4:7], a[228:231], v[112:115]\n"
        "buffer_load_dwordx4 v234, s[52:55], s61 offen offset:2048 lds\n"
        "v_mfma_f32_16x16x32_bf16 v[112:115], a[0:3], a[224:227], v[112:115]\n"
        "ds_read_b128 v[182:185], v251 offset:512\n"
        "s_nop 0\n"
        "v_mfma_f32_16x16x32_bf16 v[116:119], a[24:27], a[224:227], 0\n"
        "s_nop 2\n"
        "v_mfma_f32_16x16x32_bf16 v[116:119], a[28:31], a[228:231], v[116:119]\n"
        "s_waitcnt lgkmcnt(3)\n"
        "ds_write_b128 v235, v[76:79] offset:15360\n"
        "v_mfma_f32_16x16x32_bf16 v[116:119], a[32:35], a[232:235], v[116:119]\n"
        "v_cvt_pk_bf16_f32 v176, v112, v113\n"
        "v_cvt_pk_bf16_f32 v177, v114, v115\n"
        "v_mfma_f32_16x16x32_bf16 v[116:119], a[36:39], a[236:239], v[116:119]\n"
        "s_add_u32 m0, s65, 6144\n"
        "v_mfma_f32_16x16x32_bf16 v[116:119], a[40:43], a[240:243], v[116:119]\n"
        "ds_read_b64_tr_b16 v[216:217], v255 offset:32\n"
        "ds_read_b64_tr_b16 v[218:219], v255 offset:800\n"
        "v_mfma_f32_16x16x32_bf16 v[116:119], a[44:47], a[244:247], v[116:119]\n"
        "s_nop 2\n"
        "v_mfma_f32_16x16x32_bf16 v[120:123], a[68:71], a[244:247], 0\n"
        ".long 0xbec200ff\n"
        "43:\n"
        "s_branch 60b\n"
        "buffer_load_dwordx4 v232, s[52:55], s56 offen lds\n"
        "v_mfma_f32_16x16x32_bf16 v[120:123], a[64:67], a[240:243], v[120:123]\n"
        "s_nop 1\n"
        "v_cvt_pk_bf16_f32 v178, v116, v117\n"
        "v_mfma_f32_16x16x32_bf16 v[120:123], a[60:63], a[236:239], v[120:123]\n"
        "ds_write_b128 v239, v[88:91] offset:15552\n"
        "v_mfma_f32_16x16x32_bf16 v[120:123], a[56:59], a[232:235], v[120:123]\n"
        "v_cvt_pk_bf16_f32 v179, v118, v119\n"
        "v_mfma_f32_16x16x32_bf16 v[120:123], a[52:55], a[228:231], v[120:123]\n"
        "v_mfma_f32_16x16x32_bf16 v[120:123], a[48:51], a[224:227], v[120:123]\n"
        "ds_read_b64_tr_b16 v[220:221], v255 offset:64\n"
        "ds_read_b64_tr_b16 v[222:223], v255 offset:832\n"
        "v_mfma_f32_16x16x32_bf16 v[112:115], a[224:227], a[144:147], 0\n"
        "s_nop 2\n"
        "v_mfma_f32_16x16x32_bf16 v[112:115], a[228:231], a[148:151], v[112:115]\n"
        "s_nop 0\n"
        "v_cvt_pk_bf16_f32 v180, v120, v121\n"
        "v_cvt_pk_bf16_f32 v181, v122, v123\n"
        "v_mfma_f32_16x16x32_bf16 v[112:115], a[232:235], a[152:155], v[112:115]\n"
        "buffer_load_dwordx4 v233, s[52:55], s56 offen offset:1024 lds\n"
        "v_mfma_f32_16x16x32_bf16 v[112:115], a[236:239], a[156:159], v[112:115]\n"
        "ds_write_b128 v254, v[92:95] offset:15360\n"
        "v_mfma_f32_16x16x32_bf16 v[112:115], a[240:243], a[160:163], v[112:115]\n"
        "v_cvt_pk_bf16_f32 v186, v120, v121\n"
        "v_cvt_pk_bf16_f32 v187, v122, v123\n"
        "v_mfma_f32_16x16x32_bf16 v[112:115], a[244:247], a[164:167], v[112:115]\n"
        "ds_read_b128 a[248:251], v240 offset:12288\n"
        "s_nop 0\n"
        "v_mfma_f32_16x16x32_bf16 v[96:99], v[152:155], v[156:159], v[0:3]\n"
        "s_waitcnt vmcnt(2)\n"
        "ds_read_b64_tr_b16 v[224:225], v255 offset:3072\n"
        "ds_read_b64_tr_b16 v[226:227], v255 offset:3840\n"
        "v_mfma_f32_16x16x32_bf16 v[96:99], v[168:171], v[160:163], v[96:99]\n"
        "ds_read_b128 a[216:219], v241 offset:12288\n"
        "s_sub_u32 s31, s30, s28\n"
        "v_cvt_pk_bf16_f32 v130, v112, v113\n"
        "v_mfma_f32_16x16x32_bf16 v[100:103], v[172:175], v[156:159], v[4:7]\n"
        "ds_read_b128 a[220:223], v240 offset:12416\n"
        "s_sub_u32 s45, s28, s29\n"
        "v_cvt_pk_bf16_f32 v131, v114, v115\n"
        "v_mfma_f32_16x16x32_bf16 v[100:103], v[188:191], v[164:167], v[100:103]\n"
        "s_add_u32 s28, s28, s31\n"
        "ds_read_b128 v[162:165], v252 offset:0\n"
        "v_mfma_f32_16x16x32_bf16 v[116:119], a[244:247], a[188:191], 0\n"
        "buffer_load_dwordx4 v234, s[52:55], s56 offen offset:2048 lds\n"
        "v_mfma_f32_16x16x32_bf16 v[116:119], a[240:243], a[184:187], v[116:119]\n"
        "s_waitcnt lgkmcnt(7)\n"
        "ds_write_b128 v235, v[212:215] offset:0\n"
        "v_mfma_f32_16x16x32_bf16 v[116:119], a[236:239], a[180:183], v[116:119]\n"
        "v_exp_f32 v96, v96\n"
        "v_mfma_f32_16x16x32_bf16 v[116:119], a[232:235], a[176:179], v[116:119]\n"
        "ds_read_b64_tr_b16 v[88:89], v255 offset:3104\n"
        "ds_read_b64_tr_b16 v[90:91], v255 offset:3872\n"
        "v_mfma_f32_16x16x32_bf16 v[116:119], a[228:231], a[172:175], v[116:119]\n"
        "v_exp_f32 v97, v97\n"
        "v_mfma_f32_16x16x32_bf16 v[116:119], a[224:227], a[168:171], v[116:119]\n"
        "s_add_u32 m0, s65, 9216\n"
        "v_exp_f32 v98, v98\n"
        "v_mfma_f32_16x16x32_bf16 v[104:107], v[152:155], v[176:179], v[16:19]\n"
        "v_exp_f32 v100, v100\n"
        "v_mfma_f32_16x16x32_bf16 v[104:107], v[168:171], v[180:183], v[104:107]\n"
        "ds_write_b128 v239, v[216:219] offset:192\n"
        "v_mfma_f32_16x16x32_bf16 v[108:111], v[172:175], v[176:179], v[20:23]\n"
        "buffer_load_dwordx4 v232, s[52:55], s62 offen lds\n"
        "v_mfma_f32_16x16x32_bf16 v[108:111], v[188:191], v[184:187], v[108:111]\n"
        "ds_read_b128 v[182:185], v252 offset:512\n"
        "v_cvt_pk_bf16_f32 v64, v96, v97\n"
        "v_mfma_f32_16x16x32_bf16 v[120:123], a[224:227], a[192:195], 0\n"
        "ds_read_b128 a[224:227], v241 offset:12416\n"
        "ds_read_b64_tr_b16 v[92:93], v255 offset:3136\n"
        "v_mfma_f32_16x16x32_bf16 v[120:123], a[228:231], a[196:199], v[120:123]\n"
        "ds_read_b64_tr_b16 v[94:95], v255 offset:3904\n"
        "ds_read_b128 a[228:231], v240 offset:12544\n"
        "v_mfma_f32_16x16x32_bf16 v[120:123], a[232:235], a[200:203], v[120:123]\n"
        "ds_read_b128 a[232:235], v241 offset:12544\n"
        "v_cvt_pk_bf16_f32 v138, v116, v117\n"
        "v_mfma_f32_16x16x32_bf16 v[120:123], a[236:239], a[204:207], v[120:123]\n"
        "ds_read_b128 v[202:205], v251 offset:4096\n"
        "v_cvt_pk_bf16_f32 v139, v118, v119\n"
        "v_mfma_f32_16x16x32_bf16 v[120:123], a[240:243], a[208:211], v[120:123]\n"
        "ds_write_b128 v254, v[220:223] offset:0\n"
        "v_mfma_f32_16x16x32_bf16 v[120:123], a[244:247], a[212:215], v[120:123]\n"
        "s_waitcnt lgkmcnt(9)\n"
        "v_exp_f32 v99, v99\n"
        "v_mfma_f32_16x16x32_bf16 v[112:115], a[0:3], a[248:251], 0\n"
        "buffer_load_dwordx4 v233, s[52:55], s62 offen offset:1024 lds\n"
        "v_mfma_f32_16x16x32_bf16 v[112:115], a[4:7], a[216:219], v[112:115]\n"
        "s_waitcnt vmcnt(2)\n"
        "ds_read_b64_tr_b16 v[212:213], v255 offset:6144\n"
        "ds_read_b64_tr_b16 v[214:215], v255 offset:6912\n"
        "v_mfma_f32_16x16x32_bf16 v[112:115], a[8:11], a[220:223], v[112:115]\n"
        "s_waitcnt lgkmcnt(5)\n"
        "v_exp_f32 v101, v101\n"
        "v_mfma_f32_16x16x32_bf16 v[112:115], a[12:15], a[224:227], v[112:115]\n"
        "v_cvt_pk_bf16_f32 v65, v98, v99\n"
        "v_cvt_pk_bf16_f32 v66, v100, v101\n"
        "v_mfma_f32_16x16x32_bf16 v[112:115], a[16:19], a[228:231], v[112:115]\n"
        "s_waitcnt lgkmcnt(3)\n"
        "v_exp_f32 v102, v102\n"
        "v_mfma_f32_16x16x32_bf16 v[112:115], a[20:23], a[232:235], v[112:115]\n"
        "ds_write_b128 v235, v[224:227] offset:3072\n"
        "v_mfma_f32_16x16x32_bf16 v[116:119], a[44:47], a[232:235], 0\n"
        "v_exp_f32 v103, v103\n"
        "v_mfma_f32_16x16x32_bf16 v[116:119], a[40:43], a[228:231], v[116:119]\n"
        "v_cvt_pk_bf16_f32 v67, v102, v103\n"
        "v_cvt_pk_bf16_f32 v146, v120, v121\n"
        "v_mfma_f32_16x16x32_bf16 v[116:119], a[36:39], a[224:227], v[116:119]\n"
        "buffer_load_dwordx4 v234, s[52:55], s62 offen offset:2048 lds\n"
        "s_add_u32 s41, s30, s40\n"
        "s_add_u32 s35, s47, 786432\n"
        "s_sub_u32 s46, s35, 100663296\n"
        "s_cmp_lt_u32 s46, 786432\n"
        "s_mov_b32 s30, s29\n"
        "v_mfma_f32_16x16x32_bf16 v[116:119], a[32:35], a[220:223], v[116:119]\n"
        "s_cselect_b32 s35, s46, s35\n"
        "s_add_u32 s29, s29, s45\n"
        "ds_read_b64_tr_b16 v[216:217], v255 offset:6176\n"
        "v_mfma_f32_16x16x32_bf16 v[116:119], a[28:31], a[216:219], v[116:119]\n"
        "ds_read_b64_tr_b16 v[218:219], v255 offset:6944\n"
        "v_cvt_pk_bf16_f32 v147, v122, v123\n"
        "v_mfma_f32_16x16x32_bf16 v[116:119], a[24:27], a[248:251], v[116:119]\n"
        "v_exp_f32 v104, v104\n"
        "v_mfma_f32_16x16x32_bf16 v[120:123], a[48:51], a[248:251], 0\n"
        "ds_write_b128 v239, v[88:91] offset:3264\n"
        "v_mfma_f32_16x16x32_bf16 v[120:123], a[52:55], a[216:219], v[120:123]\n"
        "v_exp_f32 v105, v105\n"
        "v_mfma_f32_16x16x32_bf16 v[120:123], a[56:59], a[220:223], v[120:123]\n"
        "s_mov_b32 m0, s48\n"
        "v_exp_f32 v106, v106\n"
        "v_mfma_f32_16x16x32_bf16 v[120:123], a[60:63], a[224:227], v[120:123]\n"
        "ds_read_b64_tr_b16 v[220:221], v255 offset:6208\n"
        "ds_read_b64_tr_b16 v[222:223], v255 offset:6976\n"
        "v_mfma_f32_16x16x32_bf16 v[120:123], a[64:67], a[228:231], v[120:123]\n"
        "buffer_load_dwordx4 v249, s[12:15], s36 offen offset:0 lds\n"
        "v_mfma_f32_16x16x32_bf16 v[120:123], a[68:71], a[232:235], v[120:123]\n"
        "v_cvt_pk_bf16_f32 v72, v104, v105\n"
        "v_cvt_pk_bf16_f32 v196, v112, v113\n"
        "v_cvt_pk_bf16_f32 v197, v114, v115\n"
        "v_mfma_f32_16x16x32_bf16 v[112:115], a[92:95], a[232:235], 0\n"
        "v_exp_f32 v108, v108\n"
        "v_mfma_f32_16x16x32_bf16 v[112:115], a[88:91], a[228:231], v[112:115]\n"
        "ds_write_b128 v254, v[92:95] offset:3072\n"
        "v_mfma_f32_16x16x32_bf16 v[112:115], a[84:87], a[224:227], v[112:115]\n"
        "v_exp_f32 v107, v107\n"
        "v_mfma_f32_16x16x32_bf16 v[112:115], a[80:83], a[220:223], v[112:115]\n"
        "v_exp_f32 v109, v109\n"
        "v_mfma_f32_16x16x32_bf16 v[112:115], a[76:79], a[216:219], v[112:115]\n"
        "s_waitcnt vmcnt(1)\n"
        "ds_read_b64_tr_b16 v[224:225], v255 offset:9216\n"
        "ds_read_b64_tr_b16 v[226:227], v255 offset:9984\n"
        "v_mfma_f32_16x16x32_bf16 v[112:115], a[72:75], a[248:251], v[112:115]\n"
        "buffer_load_dwordx4 v253, s[12:15], s36 offen offset:1024 lds\n"
        "v_cvt_pk_bf16_f32 v198, v116, v117\n"
        "v_cvt_pk_bf16_f32 v199, v118, v119\n"
        "s_nop 1\n"
        "v_mfma_f32_16x16x32_bf16 v[96:99], v[152:155], v[196:199], v[32:35]\n"
        "v_cvt_pk_bf16_f32 v73, v106, v107\n"
        "v_cvt_pk_bf16_f32 v74, v108, v109\n"
        "v_cvt_pk_bf16_f32 v200, v120, v121\n"
        "v_cvt_pk_bf16_f32 v201, v122, v123\n"
        "s_nop 1\n"
        "v_mfma_f32_16x16x32_bf16 v[96:99], v[168:171], v[200:203], v[96:99]\n"
        "v_exp_f32 v110, v110\n"
        "v_mfma_f32_16x16x32_bf16 v[100:103], v[172:175], v[196:199], v[36:39]\n"
        "s_waitcnt lgkmcnt(3)\n"
        "ds_write_b128 v235, v[212:215] offset:6144\n"
        "v_cvt_pk_bf16_f32 v206, v120, v121\n"
        "v_cvt_pk_bf16_f32 v207, v122, v123\n"
        "s_nop 1\n"
        "v_mfma_f32_16x16x32_bf16 v[100:103], v[188:191], v[204:207], v[100:103]\n"
        "s_cmp_eq_u32 s37, 100663296\n"
        "v_exp_f32 v111, v111\n"
        "v_mfma_f32_16x16x32_bf16 v[116:119], a[96:99], a[248:251], 0\n"
        "s_cselect_b32 s46, 0, s37\n"
        "ds_read_b128 v[202:205], v252 offset:4096\n"
        "v_cvt_pk_bf16_f32 v75, v110, v111\n"
        "v_mfma_f32_16x16x32_bf16 v[116:119], a[100:103], a[216:219], v[116:119]\n"
        "ds_read_b64_tr_b16 v[88:89], v255 offset:9248\n"
        "ds_read_b64_tr_b16 v[90:91], v255 offset:10016\n"
        "s_add_u32 m0, s41, 0\n"
        "v_mfma_f32_16x16x32_bf16 v[116:119], a[104:107], a[220:223], v[116:119]\n"
        "buffer_load_dwordx4 v242, s[8:11], s47 offen lds\n"
        "v_mfma_f32_16x16x32_bf16 v[116:119], a[108:111], a[224:227], v[116:119]\n"
        "s_add_u32 s38, s46, s43\n"
        "v_cvt_pk_bf16_f32 v192, v112, v113\n"
        "v_cvt_pk_bf16_f32 v193, v114, v115\n"
        "v_mfma_f32_16x16x32_bf16 v[116:119], a[112:115], a[228:231], v[116:119]\n"
        "s_add_u32 s39, s46, s44\n"
        "v_exp_f32 v96, v96\n"
        "v_mfma_f32_16x16x32_bf16 v[116:119], a[116:119], a[232:235], v[116:119]\n"
        "ds_write_b128 v239, v[216:219] offset:6336\n"
        "s_add_u32 s37, s37, 6291456\n"
        "v_mfma_f32_16x16x32_bf16 v[120:123], a[140:143], a[232:235], 0\n"
        "v_exp_f32 v97, v97\n"
        "v_mfma_f32_16x16x32_bf16 v[120:123], a[136:139], a[228:231], v[120:123]\n"
        "s_add_u32 m0, s41, 1024\n"
        "v_exp_f32 v98, v98\n"
        "v_mfma_f32_16x16x32_bf16 v[120:123], a[132:135], a[224:227], v[120:123]\n"
        "ds_read_b64_tr_b16 v[92:93], v255 offset:9280\n"
        "ds_read_b64_tr_b16 v[94:95], v255 offset:10048\n"
        "v_mfma_f32_16x16x32_bf16 v[120:123], a[128:131], a[220:223], v[120:123]\n"
        "buffer_load_dwordx4 v243, s[8:11], s47 offen lds\n"
        "v_mfma_f32_16x16x32_bf16 v[120:123], a[124:127], a[216:219], v[120:123]\n"
        "v_cvt_pk_bf16_f32 v80, v96, v97\n"
        "v_cvt_pk_bf16_f32 v194, v116, v117\n"
        "v_mfma_f32_16x16x32_bf16 v[120:123], a[120:123], a[248:251], v[120:123]\n"
        "v_exp_f32 v100, v100\n"
        "v_mfma_f32_16x16x32_bf16 v[112:115], a[248:251], a[144:147], 0\n"
        "ds_write_b128 v254, v[220:223] offset:6144\n"
        "v_mfma_f32_16x16x32_bf16 v[112:115], a[216:219], a[148:151], v[112:115]\n"
        "v_cvt_pk_bf16_f32 v195, v118, v119\n"
        "v_add_u32 v245, s45, v245\n"
        "v_mfma_f32_16x16x32_bf16 v[112:115], a[220:223], a[152:155], v[112:115]\n"
        "s_add_u32 m0, s41, 2048\n"
        "ds_read_b128 a[236:239], v240 offset:13824\n"
        "v_cvt_pk_bf16_f32 v208, v120, v121\n"
        "v_mfma_f32_16x16x32_bf16 v[112:115], a[224:227], a[156:159], v[112:115]\n"
        "ds_read_b128 a[240:243], v241 offset:13824\n"
        "v_cvt_pk_bf16_f32 v209, v122, v123\n"
        "v_mfma_f32_16x16x32_bf16 v[112:115], a[228:231], a[160:163], v[112:115]\n"
        "buffer_load_dwordx4 v244, s[8:11], s47 offen lds\n"
        "v_mfma_f32_16x16x32_bf16 v[112:115], a[232:235], a[164:167], v[112:115]\n"
        "ds_read_b128 a[244:247], v240 offset:13952\n"
        "v_add_u32 v246, s45, v246\n"
        "v_mfma_f32_16x16x32_bf16 v[104:107], v[192:195], v[156:159], v[8:11]\n"
        "v_exp_f32 v99, v99\n"
        "v_mfma_f32_16x16x32_bf16 v[104:107], v[208:211], v[160:163], v[104:107]\n"
        "s_waitcnt lgkmcnt(4)\n"
        "ds_write_b128 v235, v[224:227] offset:9216\n"
        "v_mfma_f32_16x16x32_bf16 v[116:119], a[232:235], a[188:191], 0\n"
        "v_exp_f32 v101, v101\n"
        "v_mfma_f32_16x16x32_bf16 v[116:119], a[228:231], a[184:187], v[116:119]\n"
        "s_add_u32 m0, s41, 12288\n"
        "v_cvt_pk_bf16_f32 v81, v98, v99\n"
        "v_cvt_pk_bf16_f32 v82, v100, v101\n"
        "v_mfma_f32_16x16x32_bf16 v[116:119], a[224:227], a[180:183], v[116:119]\n"
        "v_exp_f32 v102, v102\n"
        "v_mfma_f32_16x16x32_bf16 v[116:119], a[220:223], a[176:179], v[116:119]\n"
        "buffer_load_dwordx4 v242, s[8:11], s35 offen lds\n"
        "v_mfma_f32_16x16x32_bf16 v[116:119], a[216:219], a[172:175], v[116:119]\n"
        "v_exp_f32 v103, v103\n"
        "v_mfma_f32_16x16x32_bf16 v[116:119], a[248:251], a[168:171], v[116:119]\n"
        "v_cvt_pk_bf16_f32 v83, v102, v103\n"
        "v_cvt_pk_bf16_f32 v132, v112, v113\n"
        "v_mfma_f32_16x16x32_bf16 v[108:111], v[192:195], v[176:179], v[24:27]\n"
        "ds_write_b128 v239, v[88:91] offset:9408\n"
        "v_mfma_f32_16x16x32_bf16 v[108:111], v[208:211], v[180:183], v[108:111]\n"
        "v_cvt_pk_bf16_f32 v133, v114, v115\n"
        "s_nop 0\n"
        "v_mfma_f32_16x16x32_bf16 v[120:123], a[248:251], a[192:195], 0\n"
        "ds_read_b128 a[248:251], v241 offset:13952\n"
        "s_add_u32 m0, s41, 13312\n"
        "v_cvt_pk_bf16_f32 v140, v116, v117\n"
        "v_mfma_f32_16x16x32_bf16 v[120:123], a[216:219], a[196:199], v[120:123]\n"
        "ds_read_b128 a[216:219], v240 offset:14080\n"
        "v_cvt_pk_bf16_f32 v141, v118, v119\n"
        "v_mfma_f32_16x16x32_bf16 v[120:123], a[220:223], a[200:203], v[120:123]\n"
        "buffer_load_dwordx4 v243, s[8:11], s35 offen lds\n"
        "v_mfma_f32_16x16x32_bf16 v[120:123], a[224:227], a[204:207], v[120:123]\n"
        "ds_read_b128 a[220:223], v241 offset:14080\n"
        "v_mfma_f32_16x16x32_bf16 v[120:123], a[228:231], a[208:211], v[120:123]\n"
        "v_exp_f32 v104, v104\n"
        "v_mfma_f32_16x16x32_bf16 v[120:123], a[232:235], a[212:215], v[120:123]\n"
        "ds_write_b128 v254, v[92:95] offset:9216\n"
        "v_mfma_f32_16x16x32_bf16 v[96:99], v[192:195], v[196:199], v[40:43]\n"
        "v_exp_f32 v105, v105\n"
        "v_mfma_f32_16x16x32_bf16 v[96:99], v[208:211], v[200:203], v[96:99]\n"
        "s_waitcnt lgkmcnt(5)\n"
        "s_add_u32 m0, s41, 14336\n"
        "v_exp_f32 v106, v106\n"
        "v_mfma_f32_16x16x32_bf16 v[112:115], a[72:75], a[236:239], 0\n"
        "v_cvt_pk_bf16_f32 v68, v104, v105\n"
        "v_cvt_pk_bf16_f32 v148, v120, v121\n"
        "v_mfma_f32_16x16x32_bf16 v[112:115], a[76:79], a[240:243], v[112:115]\n"
        "buffer_load_dwordx4 v244, s[8:11], s35 offen lds\n"
        "v_mfma_f32_16x16x32_bf16 v[112:115], a[80:83], a[244:247], v[112:115]\n"
        "s_waitcnt lgkmcnt(1)\n"
        "v_exp_f32 v107, v107\n"
        "v_mfma_f32_16x16x32_bf16 v[112:115], a[84:87], a[248:251], v[112:115]\n"
        "v_cvt_pk_bf16_f32 v69, v106, v107\n"
        "v_cvt_pk_bf16_f32 v149, v122, v123\n"
        "v_mfma_f32_16x16x32_bf16 v[112:115], a[88:91], a[216:219], v[112:115]\n"
        "v_exp_f32 v108, v108\n"
        "v_mfma_f32_16x16x32_bf16 v[112:115], a[92:95], a[220:223], v[112:115]\n"
        "v_exp_f32 v109, v109\n"
        "v_mfma_f32_16x16x32_bf16 v[116:119], a[116:119], a[220:223], 0\n"
        "v_exp_f32 v110, v110\n"
        "v_mfma_f32_16x16x32_bf16 v[116:119], a[112:115], a[216:219], v[116:119]\n"
        "v_cvt_pk_bf16_f32 v76, v108, v109\n"
        "v_add_u32 v240, s31, v240\n"
        "v_mfma_f32_16x16x32_bf16 v[116:119], a[108:111], a[248:251], v[116:119]\n"
        "v_exp_f32 v111, v111\n"
        "v_mfma_f32_16x16x32_bf16 v[116:119], a[104:107], a[244:247], v[116:119]\n"
        "v_cvt_pk_bf16_f32 v212, v112, v113\n"
        "v_cvt_pk_bf16_f32 v213, v114, v115\n"
        "v_mfma_f32_16x16x32_bf16 v[116:119], a[100:103], a[240:243], v[116:119]\n"
        "v_cvt_pk_bf16_f32 v77, v110, v111\n"
        "v_add_u32 v241, s31, v241\n"
        "v_mfma_f32_16x16x32_bf16 v[116:119], a[96:99], a[236:239], v[116:119]\n"
        "v_exp_f32 v96, v96\n"
        "v_mfma_f32_16x16x32_bf16 v[120:123], a[120:123], a[236:239], 0\n"
        "v_exp_f32 v97, v97\n"
        "v_mfma_f32_16x16x32_bf16 v[120:123], a[124:127], a[240:243], v[120:123]\n"
        "s_add_u32 s47, s47, 6291456\n"
        "v_exp_f32 v98, v98\n"
        "v_mfma_f32_16x16x32_bf16 v[120:123], a[128:131], a[244:247], v[120:123]\n"
        "s_add_u32 s36, s36, 2097152\n"
        "v_cvt_pk_bf16_f32 v214, v116, v117\n"
        "v_cvt_pk_bf16_f32 v215, v118, v119\n"
        "v_mfma_f32_16x16x32_bf16 v[120:123], a[132:135], a[248:251], v[120:123]\n"
        "s_xor_b32 s48, s48, 8192\n"
        "v_cvt_pk_bf16_f32 v84, v96, v97\n"
        "v_and_b32 v254, 32, v250\n"
        "v_mfma_f32_16x16x32_bf16 v[120:123], a[136:139], a[216:219], v[120:123]\n"
        "v_exp_f32 v99, v99\n"
        "v_mfma_f32_16x16x32_bf16 v[120:123], a[140:143], a[220:223], v[120:123]\n"
        "v_cvt_pk_bf16_f32 v85, v98, v99\n"
        "v_mad_i32_i24 v254, v254, -12, v248\n"
        "v_mfma_f32_16x16x32_bf16 v[112:115], a[220:223], a[164:167], 0\n"
        "v_add_u32 v254, 1728, v254\n"
        "s_nop 0\n"
        "v_mfma_f32_16x16x32_bf16 v[112:115], a[216:219], a[160:163], v[112:115]\n"
        "s_nop 1\n"
        "v_cvt_pk_bf16_f32 v230, v120, v121\n"
        "v_mfma_f32_16x16x32_bf16 v[112:115], a[248:251], a[156:159], v[112:115]\n"
        "s_waitcnt lgkmcnt(0)\n"
        "s_barrier\n"
        "v_cvt_pk_bf16_f32 v231, v122, v123\n"
        "v_mfma_f32_16x16x32_bf16 v[112:115], a[244:247], a[152:155], v[112:115]\n"
        "v_mfma_f32_16x16x32_bf16 v[112:115], a[240:243], a[148:151], v[112:115]\n"
        "v_mfma_f32_16x16x32_bf16 v[112:115], a[236:239], a[144:147], v[112:115]\n"
        "s_nop 2\n"
        "v_mfma_f32_16x16x32_bf16 v[100:103], v[212:215], v[156:159], v[12:15]\n"
        "s_nop 2\n"
        "v_mfma_f32_16x16x32_bf16 v[100:103], v[228:231], v[164:167], v[100:103]\n"
        "v_cvt_pk_bf16_f32 v134, v112, v113\n"
        "v_cvt_pk_bf16_f32 v135, v114, v115\n"
        "v_mfma_f32_16x16x32_bf16 v[116:119], a[236:239], a[168:171], 0\n"
        "v_mfma_f32_16x16x32_bf16 v[116:119], a[240:243], a[172:175], v[116:119]\n"
        "v_mfma_f32_16x16x32_bf16 v[116:119], a[244:247], a[176:179], v[116:119]\n"
        "v_mfma_f32_16x16x32_bf16 v[116:119], a[248:251], a[180:183], v[116:119]\n"
        "v_mfma_f32_16x16x32_bf16 v[116:119], a[216:219], a[184:187], v[116:119]\n"
        "s_nop 0\n"
        "v_exp_f32 v100, v100\n"
        "v_mfma_f32_16x16x32_bf16 v[116:119], a[220:223], a[188:191], v[116:119]\n"
        "v_exp_f32 v101, v101\n"
        "v_mfma_f32_16x16x32_bf16 v[104:107], v[212:215], v[176:179], v[28:31]\n"
        "v_exp_f32 v102, v102\n"
        "v_mfma_f32_16x16x32_bf16 v[104:107], v[228:231], v[184:187], v[104:107]\n"
        "v_exp_f32 v103, v103\n"
        "v_mfma_f32_16x16x32_bf16 v[120:123], a[220:223], a[212:215], 0\n"
        "v_cvt_pk_bf16_f32 v70, v100, v101\n"
        "v_cvt_pk_bf16_f32 v71, v102, v103\n"
        "v_mfma_f32_16x16x32_bf16 v[120:123], a[216:219], a[208:211], v[120:123]\n"
        "v_cvt_pk_bf16_f32 v142, v116, v117\n"
        "v_cvt_pk_bf16_f32 v143, v118, v119\n"
        "v_mfma_f32_16x16x32_bf16 v[120:123], a[248:251], a[204:207], v[120:123]\n"
        "v_mov_b64 v[96:97], 0\n"
        "v_mfma_f32_16x16x32_bf16 v[120:123], a[244:247], a[200:203], v[120:123]\n"
        "v_exp_f32 v104, v104\n"
        "v_mfma_f32_16x16x32_bf16 v[120:123], a[240:243], a[196:199], v[120:123]\n"
        "v_exp_f32 v105, v105\n"
        "v_mfma_f32_16x16x32_bf16 v[120:123], a[236:239], a[192:195], v[120:123]\n"
        "v_exp_f32 v106, v106\n"
        "v_mfma_f32_16x16x32_bf16 v[108:111], v[212:215], v[196:199], v[44:47]\n"
        "v_exp_f32 v107, v107\n"
        "v_mfma_f32_16x16x32_bf16 v[108:111], v[228:231], v[204:207], v[108:111]\n"
        "v_cvt_pk_bf16_f32 v78, v104, v105\n"
        "v_cvt_pk_bf16_f32 v79, v106, v107\n"
        "v_mfma_f32_16x16x32_bf16 v[112:115], a[0:3], a[236:239], 0\n"
        "s_nop 0\n"
        "v_cvt_pk_bf16_f32 v150, v120, v121\n"
        "v_cvt_pk_bf16_f32 v151, v122, v123\n"
        "v_mfma_f32_16x16x32_bf16 v[112:115], a[4:7], a[240:243], v[112:115]\n"
        "v_mfma_f32_16x16x32_bf16 v[112:115], a[8:11], a[244:247], v[112:115]\n"
        "v_exp_f32 v108, v108\n"
        "v_mfma_f32_16x16x32_bf16 v[112:115], a[12:15], a[248:251], v[112:115]\n"
        "v_exp_f32 v109, v109\n"
        "v_mfma_f32_16x16x32_bf16 v[112:115], a[16:19], a[216:219], v[112:115]\n"
        "v_exp_f32 v110, v110\n"
        "v_mfma_f32_16x16x32_bf16 v[112:115], a[20:23], a[220:223], v[112:115]\n"
        "v_exp_f32 v111, v111\n"
        "v_smfmac_f32_16x16x64_bf16 v[96:99], a[252:255], v[64:71], v237\n"
        "v_cvt_pk_bf16_f32 v86, v108, v109\n"
        "v_mfma_f32_16x16x32_bf16 v[100:103], v[132:135], v[68:71], 0\n"
        "v_mov_b64 v[124:125], 0\n"
        "v_cvt_pk_bf16_f32 v87, v110, v111\n"
        "v_mfma_f32_16x16x32_bf16 v[100:103], v[128:131], v[64:67], v[100:103]\n"
        "s_nop 0\n"
        "v_cvt_pk_bf16_f32 v216, v112, v113\n"
        "v_cvt_pk_bf16_f32 v217, v114, v115\n"
        "v_mfma_f32_16x16x32_bf16 v[104:107], v[136:139], v[64:67], 0\n"
        "ds_read_b128 v[222:225], v251 offset:4608\n"
        "v_add_f32 v97, v96, v97\n"
        "v_mfma_f32_16x16x32_bf16 v[104:107], v[140:143], v[68:71], v[104:107]\n"
        "v_rcp_f32 v96, v97\n"
        "v_mfma_f32_16x16x32_bf16 v[108:111], v[148:151], v[68:71], 0\n"
        "v_mul_f32 v100, v96, v100\n"
        "v_mul_f32 v101, v96, v101\n"
        "v_mfma_f32_16x16x32_bf16 v[108:111], v[144:147], v[64:67], v[108:111]\n"
        "v_mul_f32 v102, v96, v102\n"
        "v_mul_f32 v103, v96, v103\n"
        "v_mfma_f32_16x16x32_bf16 v[116:119], a[44:47], a[220:223], 0\n"
        "v_cvt_pk_bf16_f32 v100, v100, v101\n"
        "v_cvt_pk_bf16_f32 v101, v102, v103\n"
        "v_mfma_f32_16x16x32_bf16 v[116:119], a[40:43], a[216:219], v[116:119]\n"
        "v_mul_f32 v104, v96, v104\n"
        "v_mul_f32 v105, v96, v105\n"
        "v_mfma_f32_16x16x32_bf16 v[116:119], a[36:39], a[248:251], v[116:119]\n"
        "v_mul_f32 v106, v96, v106\n"
        "v_mul_f32 v107, v96, v107\n"
        "v_mfma_f32_16x16x32_bf16 v[116:119], a[32:35], a[244:247], v[116:119]\n"
        "v_cvt_pk_bf16_f32 v102, v104, v105\n"
        "v_cvt_pk_bf16_f32 v103, v106, v107\n"
        "v_mfma_f32_16x16x32_bf16 v[116:119], a[28:31], a[240:243], v[116:119]\n"
        "ds_write_b128 v245, v[100:103] offset:0\n"
        "v_mfma_f32_16x16x32_bf16 v[116:119], a[24:27], a[236:239], v[116:119]\n"
        "v_mul_f32 v108, v96, v108\n"
        "v_mul_f32 v109, v96, v109\n"
        "v_smfmac_f32_16x16x64_bf16 v[124:127], a[252:255], v[72:79], v237\n"
        "v_mul_f32 v110, v96, v110\n"
        "v_mul_f32 v111, v96, v111\n"
        "v_mfma_f32_16x16x32_bf16 v[64:67], v[132:135], v[76:79], 0\n"
        "v_cvt_pk_bf16_f32 v108, v108, v109\n"
        "v_cvt_pk_bf16_f32 v109, v110, v111\n"
        "v_mfma_f32_16x16x32_bf16 v[64:67], v[128:131], v[72:75], v[64:67]\n"
        "ds_read_b128 v[156:159], v247 offset:0\n"
        "v_cvt_pk_bf16_f32 v218, v116, v117\n"
        "v_mfma_f32_16x16x32_bf16 v[68:71], v[136:139], v[72:75], 0\n"
        "ds_write_b64 v246, v[108:109] offset:0\n"
        "v_mfma_f32_16x16x32_bf16 v[68:71], v[140:143], v[76:79], v[68:71]\n"
        "ds_read_b128 v[160:163], v247 offset:4096\n"
        "v_cvt_pk_bf16_f32 v219, v118, v119\n"
        "v_mfma_f32_16x16x32_bf16 v[120:123], v[148:151], v[76:79], 0\n"
        "ds_read_b128 v[164:167], v247 offset:4352\n"
        "v_max_u32 v236, v96, v97\n"
        "v_mfma_f32_16x16x32_bf16 v[120:123], v[144:147], v[72:75], v[120:123]\n"
        "v_mov_b64 v[116:117], 0\n"
        "v_add_f32 v125, v124, v125\n"
        "v_mfma_f32_16x16x32_bf16 v[112:115], a[48:51], a[236:239], 0\n"
        "v_rcp_f32 v124, v125\n"
        "v_mfma_f32_16x16x32_bf16 v[112:115], a[52:55], a[240:243], v[112:115]\n"
        "v_mul_f32 v64, v124, v64\n"
        "v_mul_f32 v65, v124, v65\n"
        "v_mfma_f32_16x16x32_bf16 v[112:115], a[56:59], a[244:247], v[112:115]\n"
        "v_mul_f32 v66, v124, v66\n"
        "v_mul_f32 v67, v124, v67\n"
        "v_mfma_f32_16x16x32_bf16 v[112:115], a[60:63], a[248:251], v[112:115]\n"
        "v_cvt_pk_bf16_f32 v64, v64, v65\n"
        "v_cvt_pk_bf16_f32 v65, v66, v67\n"
        "v_mfma_f32_16x16x32_bf16 v[112:115], a[64:67], a[216:219], v[112:115]\n"
        "v_mul_f32 v68, v124, v68\n"
        "v_mul_f32 v69, v124, v69\n"
        "v_mfma_f32_16x16x32_bf16 v[112:115], a[68:71], a[220:223], v[112:115]\n"
        "ds_read_b128 a[216:219], v248 offset:0\n"
        "v_mul_f32 v70, v124, v70\n"
        "v_smfmac_f32_16x16x64_bf16 v[116:119], a[252:255], v[80:87], v237\n"
        "ds_read_b128 a[220:223], v248 offset:3072\n"
        "v_mul_f32 v71, v124, v71\n"
        "v_mfma_f32_16x16x32_bf16 v[72:75], v[132:135], v[84:87], 0\n"
        "v_cvt_pk_bf16_f32 v66, v68, v69\n"
        "v_cvt_pk_bf16_f32 v67, v70, v71\n"
        "v_mfma_f32_16x16x32_bf16 v[72:75], v[128:131], v[80:83], v[72:75]\n"
        "ds_write_b128 v245, v[64:67] offset:256\n"
        "v_mfma_f32_16x16x32_bf16 v[76:79], v[136:139], v[80:83], 0\n"
        "v_cvt_pk_bf16_f32 v220, v112, v113\n"
        "v_cvt_pk_bf16_f32 v221, v114, v115\n"
        "v_mfma_f32_16x16x32_bf16 v[76:79], v[140:143], v[84:87], v[76:79]\n"
        "ds_read_b128 a[224:227], v248 offset:32\n"
        "v_cvt_pk_bf16_f32 v226, v112, v113\n"
        "v_mfma_f32_16x16x32_bf16 v[96:99], v[148:151], v[84:87], 0\n"
        "ds_read_b128 a[228:231], v248 offset:3104\n"
        "v_cvt_pk_bf16_f32 v227, v114, v115\n"
        "v_mfma_f32_16x16x32_bf16 v[96:99], v[144:147], v[80:83], v[96:99]\n"
        "v_mul_f32 v120, v124, v120\n"
        "v_mul_f32 v121, v124, v121\n"
        "v_mfma_f32_16x16x32_bf16 v[100:103], v[152:155], v[216:219], v[48:51]\n"
        "s_waitcnt lgkmcnt(3)\n"
        "ds_read_b128 v[152:155], v247 offset:256\n"
        "v_mul_f32 v122, v124, v122\n"
        "v_mfma_f32_16x16x32_bf16 v[100:103], v[168:171], v[220:223], v[100:103]\n"
        "v_mul_f32 v123, v124, v123\n"
        "v_cvt_pk_bf16_f32 v120, v120, v121\n"
        "v_mfma_f32_16x16x32_bf16 v[104:107], v[172:175], v[216:219], v[52:55]\n"
        "v_cvt_pk_bf16_f32 v121, v122, v123\n"
        "v_max3_u32 v236, v236, v124, v125\n"
        "v_mfma_f32_16x16x32_bf16 v[104:107], v[188:191], v[224:227], v[104:107]\n"
        "ds_write_b64 v246, v[120:121] offset:256\n"
        "v_mfma_f32_16x16x32_bf16 v[176:179], v[156:159], a[216:219], 0\n"
        "ds_read_b128 v[222:225], v252 offset:4608\n"
        "v_add_f32 v117, v116, v117\n"
        "v_mfma_f32_16x16x32_bf16 v[176:179], v[160:163], a[220:223], v[176:179]\n"
        "ds_read_b128 a[232:235], v254 offset:0\n"
        "v_mfma_f32_16x16x32_bf16 v[196:199], v[164:167], a[220:223], 0\n"
        "s_waitcnt lgkmcnt(3)\n"
        "ds_read_b128 a[220:223], v254 offset:3072\n"
        "v_mfma_f32_16x16x32_bf16 v[196:199], v[152:155], a[216:219], v[196:199]\n"
        "v_rcp_f32 v116, v117\n"
        "v_mfma_f32_16x16x32_bf16 v[200:203], v[152:155], a[224:227], 0\n"
        "ds_read_b128 a[216:219], v254 offset:32\n"
        "v_mul_f32 v72, v116, v72\n"
        "v_mfma_f32_16x16x32_bf16 v[200:203], v[164:167], a[228:231], v[200:203]\n"
        "v_exp_f32 v100, v100\n"
        "v_mfma_f32_16x16x32_bf16 v[180:183], v[160:163], a[228:231], 0\n"
        "ds_read_b128 a[228:231], v254 offset:3104\n"
        "v_mul_f32 v73, v116, v73\n"
        "v_mfma_f32_16x16x32_bf16 v[180:183], v[156:159], a[224:227], v[180:183]\n"
        "ds_read_b128 a[236:239], v247 offset:8192\n"
        "v_mul_f32 v74, v116, v74\n"
        "v_mfma_f32_16x16x32_bf16 v[108:111], v[192:195], v[216:219], v[56:59]\n"
        "s_waitcnt lgkmcnt(4)\n"
        "ds_read_b128 a[224:227], v248 offset:16\n"
        "v_mul_f32 v75, v116, v75\n"
        "v_mfma_f32_16x16x32_bf16 v[108:111], v[208:211], v[220:223], v[108:111]\n"
        "ds_read_b128 a[240:243], v247 offset:12288\n"
        "v_cvt_pk_bf16_f32 v72, v72, v73\n"
        "v_mfma_f32_16x16x32_bf16 v[112:115], v[212:215], v[216:219], v[60:63]\n"
        "ds_read_b128 a[244:247], v247 offset:12544\n"
        "v_cvt_pk_bf16_f32 v73, v74, v75\n"
        "v_mfma_f32_16x16x32_bf16 v[112:115], v[228:231], v[224:227], v[112:115]\n"
        "ds_read_b128 a[248:251], v247 offset:8448\n"
        "v_mul_f32 v76, v116, v76\n"
        "v_mfma_f32_16x16x32_bf16 v[184:187], v[156:159], a[232:235], 0\n"
        "s_waitcnt lgkmcnt(6)\n"
        "v_exp_f32 v101, v101\n"
        "v_mfma_f32_16x16x32_bf16 v[184:187], v[160:163], a[220:223], v[184:187]\n"
        "v_mul_f32 v77, v116, v77\n"
        "v_mul_f32 v78, v116, v78\n"
        "v_mfma_f32_16x16x32_bf16 v[204:207], v[164:167], a[220:223], 0\n"
        "ds_read_b128 a[220:223], v248 offset:3088\n"
        "v_mul_f32 v79, v116, v79\n"
        "v_mfma_f32_16x16x32_bf16 v[204:207], v[152:155], a[232:235], v[204:207]\n"
        "ds_read_b128 a[232:235], v254 offset:16\n"
        "v_cvt_pk_bf16_f32 v88, v100, v101\n"
        "v_mfma_f32_16x16x32_bf16 v[192:195], v[152:155], a[216:219], 0\n"
        "s_waitcnt lgkmcnt(4)\n"
        "v_exp_f32 v102, v102\n"
        "v_mfma_f32_16x16x32_bf16 v[192:195], v[164:167], a[228:231], v[192:195]\n"
        "v_exp_f32 v104, v104\n"
        "v_mfma_f32_16x16x32_bf16 v[172:175], v[160:163], a[228:231], 0\n"
        "ds_read_b128 a[228:231], v254 offset:3088\n"
        "v_cvt_pk_bf16_f32 v74, v76, v77\n"
        "v_mfma_f32_16x16x32_bf16 v[172:175], v[156:159], a[216:219], v[172:175]\n"
        "ds_read_b128 a[216:219], v248 offset:6144\n"
        "v_cvt_pk_bf16_f32 v75, v78, v79\n"
        "v_mfma_f32_16x16x32_bf16 v[212:215], v[156:159], a[224:227], 0\n"
        "s_waitcnt lgkmcnt(3)\n"
        "ds_write_b128 v245, v[72:75] offset:512\n"
        "v_mfma_f32_16x16x32_bf16 v[212:215], v[160:163], a[220:223], v[212:215]\n"
        "v_exp_f32 v103, v103\n"
        "v_mfma_f32_16x16x32_bf16 v[216:219], v[164:167], a[220:223], 0\n"
        "ds_read_b128 a[220:223], v248 offset:9216\n"
        "v_cvt_pk_bf16_f32 v89, v102, v103\n"
        "v_mfma_f32_16x16x32_bf16 v[216:219], v[152:155], a[224:227], v[216:219]\n"
        "s_waitcnt lgkmcnt(4)\n"
        "ds_read_b128 a[224:227], v248 offset:6176\n"
        "v_mul_f32 v96, v116, v96\n"
        "v_mfma_f32_16x16x32_bf16 v[224:227], v[152:155], a[232:235], 0\n"
        "s_waitcnt lgkmcnt(4)\n"
        "v_exp_f32 v105, v105\n"
        "v_mfma_f32_16x16x32_bf16 v[224:227], v[164:167], a[228:231], v[224:227]\n"
        "v_exp_f32 v106, v106\n"
        "v_mfma_f32_16x16x32_bf16 v[220:223], v[160:163], a[228:231], 0\n"
        "ds_read_b128 a[228:231], v248 offset:9248\n"
        "v_cvt_pk_bf16_f32 v90, v104, v105\n"
        "v_mfma_f32_16x16x32_bf16 v[220:223], v[156:159], a[232:235], v[220:223]\n"
        "s_waitcnt lgkmcnt(4)\n"
        "ds_read_b128 a[232:235], v254 offset:6144\n"
        "v_mul_f32 v97, v116, v97\n"
        "v_mfma_f32_16x16x32_bf16 v[176:179], a[236:239], a[216:219], v[176:179]\n"
        "s_waitcnt lgkmcnt(3)\n"
        "v_mul_f32 v98, v116, v98\n"
        "v_mul_f32 v99, v116, v99\n"
        "v_mfma_f32_16x16x32_bf16 v[176:179], a[240:243], a[220:223], v[176:179]\n"
        "v_exp_f32 v107, v107\n"
        "v_mfma_f32_16x16x32_bf16 v[196:199], a[244:247], a[220:223], v[196:199]\n"
        "ds_read_b128 a[220:223], v254 offset:9216\n"
        "v_cvt_pk_bf16_f32 v91, v106, v107\n"
        "v_mfma_f32_16x16x32_bf16 v[196:199], a[248:251], a[216:219], v[196:199]\n"
        "s_waitcnt lgkmcnt(3)\n"
        "ds_read_b128 a[216:219], v254 offset:6176\n"
        "v_cvt_pk_bf16_f32 v96, v96, v97\n"
        "v_mfma_f32_16x16x32_bf16 v[200:203], a[248:251], a[224:227], v[200:203]\n"
        "s_waitcnt lgkmcnt(3)\n"
        "ds_read_b128 v[156:159], v247 offset:16384\n"
        "v_cvt_pk_bf16_f32 v97, v98, v99\n"
        "v_mfma_f32_16x16x32_bf16 v[200:203], a[244:247], a[228:231], v[200:203]\n"
        "ds_write_b64 v246, v[96:97] offset:512\n"
        "v_mfma_f32_16x16x32_bf16 v[180:183], a[240:243], a[228:231], v[180:183]\n"
        ".long 0xbec200ff\n"
        "44:\n"
        "s_branch 60b\n"
        "ds_read_b128 a[228:231], v254 offset:9248\n"
        "v_max3_u32 v236, v236, v116, v117\n"
        "v_mfma_f32_16x16x32_bf16 v[180:183], a[236:239], a[224:227], v[180:183]\n"
        "s_waitcnt lgkmcnt(5)\n"
        "ds_read_b128 a[224:227], v248 offset:6160\n"
        "v_bfe_u32 v238, v250, 3, 4\n"
        "v_mfma_f32_16x16x32_bf16 v[184:187], a[236:239], a[232:235], v[184:187]\n"
        "s_waitcnt lgkmcnt(5)\n"
        "ds_read_b128 v[160:163], v247 offset:20480\n"
        "v_mad_i32_i24 v238, v238, -4, v250\n"
        "v_mfma_f32_16x16x32_bf16 v[184:187], a[240:243], a[220:223], v[184:187]\n"
        "ds_read_b128 v[164:167], v247 offset:20736\n"
        "v_add_u32 v238, s49, v238\n"
        "v_mfma_f32_16x16x32_bf16 v[204:207], a[244:247], a[220:223], v[204:207]\n"
        "ds_read_b128 a[220:223], v248 offset:9232\n"
        "v_mfma_f32_16x16x32_bf16 v[204:207], a[248:251], a[232:235], v[204:207]\n"
        "s_waitcnt lgkmcnt(6)\n"
        "ds_read_b128 a[232:235], v254 offset:6160\n"
        "v_mfma_f32_16x16x32_bf16 v[192:195], a[248:251], a[216:219], v[192:195]\n"
        "s_waitcnt lgkmcnt(5)\n"
        "ds_read_b128 v[152:155], v247 offset:16640\n"
        "v_mfma_f32_16x16x32_bf16 v[192:195], a[244:247], a[228:231], v[192:195]\n"
        "v_exp_f32 v108, v108\n"
        "v_mfma_f32_16x16x32_bf16 v[172:175], a[240:243], a[228:231], v[172:175]\n"
        "ds_read_b128 a[228:231], v254 offset:9232\n"
        "v_mfma_f32_16x16x32_bf16 v[172:175], a[236:239], a[216:219], v[172:175]\n"
        "s_waitcnt lgkmcnt(5)\n"
        "ds_read_b128 a[216:219], v248 offset:12288\n"
        "v_mfma_f32_16x16x32_bf16 v[212:215], a[236:239], a[224:227], v[212:215]\n"
        "s_waitcnt lgkmcnt(4)\n"
        "v_exp_f32 v109, v109\n"
        "v_mfma_f32_16x16x32_bf16 v[212:215], a[240:243], a[220:223], v[212:215]\n"
        "v_exp_f32 v110, v110\n"
        "v_mfma_f32_16x16x32_bf16 v[216:219], a[244:247], a[220:223], v[216:219]\n"
        "ds_read_b128 a[220:223], v248 offset:15360\n"
        "v_cvt_pk_bf16_f32 v92, v108, v109\n"
        "v_mfma_f32_16x16x32_bf16 v[216:219], a[248:251], a[224:227], v[216:219]\n"
        "s_waitcnt lgkmcnt(3)\n"
        "ds_read_b128 a[224:227], v248 offset:12320\n"
        "v_mfma_f32_16x16x32_bf16 v[224:227], a[248:251], a[232:235], v[224:227]\n"
        "s_waitcnt lgkmcnt(3)\n"
        "v_exp_f32 v112, v112\n"
        "v_mfma_f32_16x16x32_bf16 v[224:227], a[244:247], a[228:231], v[224:227]\n"
        "v_exp_f32 v111, v111\n"
        "v_mfma_f32_16x16x32_bf16 v[220:223], a[240:243], a[228:231], v[220:223]\n"
        "ds_read_b128 a[228:231], v248 offset:15392\n"
        "v_cvt_pk_bf16_f32 v93, v110, v111\n"
        "v_mfma_f32_16x16x32_bf16 v[220:223], a[236:239], a[232:235], v[220:223]\n"
        "s_waitcnt lgkmcnt(3)\n"
        "ds_read_b128 a[232:235], v254 offset:12288\n"
        "v_mfma_f32_16x16x32_bf16 v[176:179], v[156:159], a[216:219], v[176:179]\n"
        "s_waitcnt lgkmcnt(3)\n"
        "v_exp_f32 v113, v113\n"
        "v_mfma_f32_16x16x32_bf16 v[176:179], v[160:163], a[220:223], v[176:179]\n"
        "v_exp_f32 v114, v114\n"
        "v_mfma_f32_16x16x32_bf16 v[196:199], v[164:167], a[220:223], v[196:199]\n"
        "ds_read_b128 a[220:223], v254 offset:15360\n"
        "v_cvt_pk_bf16_f32 v94, v112, v113\n"
        "v_mfma_f32_16x16x32_bf16 v[196:199], v[152:155], a[216:219], v[196:199]\n"
        "s_waitcnt lgkmcnt(3)\n"
        "ds_read_b128 a[216:219], v254 offset:12320\n"
        "v_mfma_f32_16x16x32_bf16 v[200:203], v[152:155], a[224:227], v[200:203]\n"
        "s_waitcnt lgkmcnt(3)\n"
        "v_exp_f32 v115, v115\n"
        "v_mfma_f32_16x16x32_bf16 v[200:203], v[164:167], a[228:231], v[200:203]\n"
        "v_cvt_pk_bf16_f32 v95, v114, v115\n"
        "v_mfma_f32_16x16x32_bf16 v[180:183], v[160:163], a[228:231], v[180:183]\n"
        "s_waitcnt vmcnt(0)\n"
        "s_barrier\n"
        "ds_read_b128 a[228:231], v254 offset:15392\n"
        "v_mfma_f32_16x16x32_bf16 v[180:183], v[156:159], a[224:227], v[180:183]\n"
        "s_waitcnt lgkmcnt(3)\n"
        "ds_read_b128 a[224:227], v248 offset:12304\n"
        "v_mfma_f32_16x16x32_bf16 v[184:187], v[156:159], a[232:235], v[184:187]\n"
        "s_waitcnt lgkmcnt(3)\n"
        "v_mfma_f32_16x16x32_bf16 v[184:187], v[160:163], a[220:223], v[184:187]\n"
        "v_mfma_f32_16x16x32_bf16 v[204:207], v[164:167], a[220:223], v[204:207]\n"
        "ds_read_b128 a[220:223], v248 offset:15376\n"
        "v_mfma_f32_16x16x32_bf16 v[204:207], v[152:155], a[232:235], v[204:207]\n"
        "s_waitcnt lgkmcnt(3)\n"
        "ds_read_b128 a[232:235], v254 offset:12304\n"
        "v_mfma_f32_16x16x32_bf16 v[192:195], v[152:155], a[216:219], v[192:195]\n"
        "s_waitcnt lgkmcnt(3)\n"
        "ds_read_b128 a[236:239], v240 offset:0\n"
        "v_mfma_f32_16x16x32_bf16 v[192:195], v[164:167], a[228:231], v[192:195]\n"
        "ds_read_b128 a[240:243], v241 offset:0\n"
        "v_mfma_f32_16x16x32_bf16 v[172:175], v[160:163], a[228:231], v[172:175]\n"
        "ds_read_b128 a[228:231], v254 offset:15376\n"
        "v_mfma_f32_16x16x32_bf16 v[172:175], v[156:159], a[216:219], v[172:175]\n"
        "s_waitcnt lgkmcnt(5)\n"
        "ds_read_b128 a[244:247], v240 offset:128\n"
        "v_mfma_f32_16x16x32_bf16 v[212:215], v[156:159], a[224:227], v[212:215]\n"
        "s_waitcnt lgkmcnt(5)\n"
        "ds_read_b128 a[248:251], v241 offset:128\n"
        "v_mfma_f32_16x16x32_bf16 v[212:215], v[160:163], a[220:223], v[212:215]\n"
        "ds_read_b128 a[216:219], v240 offset:256\n"
        "v_mfma_f32_16x16x32_bf16 v[216:219], v[164:167], a[220:223], v[216:219]\n"
        "ds_read_b128 a[220:223], v241 offset:256\n"
        "v_mfma_f32_16x16x32_bf16 v[216:219], v[152:155], a[224:227], v[216:219]\n"
        "s_waitcnt lgkmcnt(6)\n"
        "v_mfma_f32_16x16x32_bf16 v[224:227], v[152:155], a[232:235], v[224:227]\n"
        "s_waitcnt lgkmcnt(4)\n"
        "v_mfma_f32_16x16x32_bf16 v[224:227], v[164:167], a[228:231], v[224:227]\n"
        "v_mfma_f32_16x16x32_bf16 v[220:223], v[160:163], a[228:231], v[220:223]\n"
        "v_mfma_f32_16x16x32_bf16 v[220:223], v[156:159], a[232:235], v[220:223]\n"
        "s_waitcnt lgkmcnt(0)\n"
        "s_cmp_eq_u32 s42, 2\n"
        "s_cbranch_scc1 12f\n"
        "s_cmp_eq_u32 s42, 3\n"
        "s_cbranch_scc1 12f\n"
        ".p2align 6\n"
        "s_nop 0\n"
        "s_nop 0\n"
        "s_nop 0\n"
        "s_nop 0\n"
        "11:\n"
        "10:\n"
        "v_mfma_f32_16x16x32_bf16 v[112:115], a[72:75], a[236:239], 0\n"
        "v_mov_b64 v[124:125], 0\n"
        "v_cvt_pk_bf16_f32 v232, v176, v180\n"
        "v_mfma_f32_16x16x32_bf16 v[112:115], a[76:79], a[240:243], v[112:115]\n"
        "s_waitcnt lgkmcnt(3)\n"
        "s_add_u32 s41, s30, s40\n"
        "v_cvt_pk_bf16_f32 v233, v184, v172\n"
        "v_cvt_pk_bf16_f32 v234, v177, v181\n"
        "v_mfma_f32_16x16x32_bf16 v[112:115], a[80:83], a[244:247], v[112:115]\n"
        "s_waitcnt lgkmcnt(2)\n"
        "buffer_store_dwordx2 v[232:233], v250, s[16:19], s38 offen offset:0 sc1\n"
        "v_cvt_pk_bf16_f32 v235, v185, v173\n"
        "v_mfma_f32_16x16x32_bf16 v[112:115], a[84:87], a[248:251], v[112:115]\n"
        "s_waitcnt lgkmcnt(1)\n"
        "v_cvt_pk_bf16_f32 v232, v178, v182\n"
        "v_cvt_pk_bf16_f32 v233, v186, v174\n"
        "v_mfma_f32_16x16x32_bf16 v[112:115], a[88:91], a[216:219], v[112:115]\n"
        "s_waitcnt lgkmcnt(0)\n"
        "v_mfma_f32_16x16x32_bf16 v[112:115], a[92:95], a[220:223], v[112:115]\n"
        "s_nop 2\n"
        "v_smfmac_f32_16x16x64_bf16 v[124:127], a[252:255], v[88:95], v237\n"
        "s_nop 1\n"
        "v_mfma_f32_16x16x32_bf16 v[80:83], v[132:135], v[92:95], 0\n"
        "buffer_store_dwordx2 v[234:235], v250, s[16:19], s38 offen offset:384 sc1\n"
        "v_cvt_pk_bf16_f32 v152, v112, v113\n"
        "v_mfma_f32_16x16x32_bf16 v[80:83], v[128:131], v[88:91], v[80:83]\n"
        "v_cvt_pk_bf16_f32 v153, v114, v115\n"
        "v_cvt_pk_bf16_f32 v234, v179, v183\n"
        "v_mfma_f32_16x16x32_bf16 v[84:87], v[136:139], v[88:91], 0\n"
        "v_cvt_pk_bf16_f32 v235, v187, v175\n"
        "v_add_f32 v125, v124, v125\n"
        "v_mfma_f32_16x16x32_bf16 v[84:87], v[140:143], v[92:95], v[84:87]\n"
        "v_rcp_f32 v124, v125\n"
        "v_mfma_f32_16x16x32_bf16 v[96:99], v[148:151], v[92:95], 0\n"
        "buffer_store_dwordx2 v[232:233], v250, s[16:19], s38 offen offset:768 sc1\n"
        "v_mul_f32 v80, v124, v80\n"
        "v_mfma_f32_16x16x32_bf16 v[96:99], v[144:147], v[88:91], v[96:99]\n"
        "s_add_u32 s35, s47, 786432\n"
        "v_mul_f32 v81, v124, v81\n"
        "v_mul_f32 v82, v124, v82\n"
        "v_mfma_f32_16x16x32_bf16 v[116:119], a[116:119], a[220:223], 0\n"
        "s_sub_u32 s46, s35, 100663296\n"
        "v_mul_f32 v83, v124, v83\n"
        "v_cvt_pk_bf16_f32 v80, v80, v81\n"
        "v_mfma_f32_16x16x32_bf16 v[116:119], a[112:115], a[216:219], v[116:119]\n"
        "s_cmp_lt_u32 s46, 786432\n"
        "v_cvt_pk_bf16_f32 v81, v82, v83\n"
        "v_cvt_pk_bf16_f32 v232, v196, v200\n"
        "v_mfma_f32_16x16x32_bf16 v[116:119], a[108:111], a[248:251], v[116:119]\n"
        "buffer_store_dwordx2 v[234:235], v250, s[16:19], s38 offen offset:1152 sc1\n"
        "v_cvt_pk_bf16_f32 v233, v204, v192\n"
        "v_mfma_f32_16x16x32_bf16 v[116:119], a[104:107], a[244:247], v[116:119]\n"
        "s_cselect_b32 s35, s46, s35\n"
        "v_mul_f32 v84, v124, v84\n"
        "v_mul_f32 v85, v124, v85\n"
        "v_mfma_f32_16x16x32_bf16 v[116:119], a[100:103], a[240:243], v[116:119]\n"
        "v_mul_f32 v86, v124, v86\n"
        "v_mul_f32 v87, v124, v87\n"
        "v_mfma_f32_16x16x32_bf16 v[116:119], a[96:99], a[236:239], v[116:119]\n"
        "v_cvt_pk_bf16_f32 v82, v84, v85\n"
        "v_cvt_pk_bf16_f32 v83, v86, v87\n"
        "v_mfma_f32_16x16x32_bf16 v[120:123], a[120:123], a[236:239], 0\n"
        "ds_write_b128 v245, v[80:83] offset:768\n"
        "v_mfma_f32_16x16x32_bf16 v[120:123], a[124:127], a[240:243], v[120:123]\n"
        "buffer_store_dwordx2 v[232:233], v250, s[16:19], s39 offen offset:0 sc1\n"
        "v_mul_f32 v96, v124, v96\n"
        "v_mfma_f32_16x16x32_bf16 v[120:123], a[128:131], a[244:247], v[120:123]\n"
        "v_mul_f32 v97, v124, v97\n"
        "v_mul_f32 v98, v124, v98\n"
        "v_mfma_f32_16x16x32_bf16 v[120:123], a[132:135], a[248:251], v[120:123]\n"
        "v_mul_f32 v99, v124, v99\n"
        "v_cvt_pk_bf16_f32 v234, v197, v201\n"
        "v_mfma_f32_16x16x32_bf16 v[120:123], a[136:139], a[216:219], v[120:123]\n"
        "v_cvt_pk_bf16_f32 v235, v205, v193\n"
        "v_cvt_pk_bf16_f32 v154, v116, v117\n"
        "v_mfma_f32_16x16x32_bf16 v[120:123], a[140:143], a[220:223], v[120:123]\n"
        "buffer_store_dwordx2 v[234:235], v250, s[16:19], s39 offen offset:384 sc1\n"
        "v_cvt_pk_bf16_f32 v155, v118, v119\n"
        "v_mfma_f32_16x16x32_bf16 v[112:115], a[20:23], a[220:223], 0\n"
        "v_cvt_pk_bf16_f32 v96, v96, v97\n"
        "v_cvt_pk_bf16_f32 v97, v98, v99\n"
        "v_mfma_f32_16x16x32_bf16 v[112:115], a[16:19], a[216:219], v[112:115]\n"
        "ds_write_b64 v246, v[96:97] offset:768\n"
        "v_mfma_f32_16x16x32_bf16 v[112:115], a[12:15], a[248:251], v[112:115]\n"
        "v_max3_u32 v236, v236, v124, v125\n"
        "v_cvt_pk_bf16_f32 v232, v198, v202\n"
        "v_mfma_f32_16x16x32_bf16 v[112:115], a[8:11], a[244:247], v[112:115]\n"
        "v_cvt_pk_bf16_f32 v233, v206, v194\n"
        "v_cmp_lt_u32 vcc, s24, v236\n"
        "v_mfma_f32_16x16x32_bf16 v[112:115], a[4:7], a[240:243], v[112:115]\n"
        "buffer_store_dwordx2 v[232:233], v250, s[16:19], s39 offen offset:768 sc1\n"
        "v_cvt_pk_bf16_f32 v168, v120, v121\n"
        "v_mfma_f32_16x16x32_bf16 v[112:115], a[0:3], a[236:239], v[112:115]\n"
        "s_cmp_lg_u64 vcc, 0\n"
        "s_addc_u32 s25, s25, s25\n"
        "v_cvt_pk_bf16_f32 v169, v122, v123\n"
        "v_mfma_f32_16x16x32_bf16 v[116:119], a[24:27], a[236:239], 0\n"
        "v_cvt_pk_bf16_f32 v234, v199, v203\n"
        "v_cvt_pk_bf16_f32 v235, v207, v195\n"
        "v_mfma_f32_16x16x32_bf16 v[116:119], a[28:31], a[240:243], v[116:119]\n"
        "v_cvt_pk_bf16_f32 v232, v212, v220\n"
        "v_cvt_pk_bf16_f32 v156, v112, v113\n"
        "v_mfma_f32_16x16x32_bf16 v[116:119], a[32:35], a[244:247], v[116:119]\n"
        "buffer_store_dwordx2 v[234:235], v250, s[16:19], s39 offen offset:1152 sc1\n"
        "v_cvt_pk_bf16_f32 v157, v114, v115\n"
        "v_mfma_f32_16x16x32_bf16 v[116:119], a[36:39], a[248:251], v[116:119]\n"
        "v_cvt_pk_bf16_f32 v234, v213, v221\n"
        "v_xor_b32 v251, 0x2000, v251\n"
        "v_mfma_f32_16x16x32_bf16 v[116:119], a[40:43], a[216:219], v[116:119]\n"
        "v_xor_b32 v252, 0x2000, v252\n"
        "v_mfma_f32_16x16x32_bf16 v[116:119], a[44:47], a[220:223], v[116:119]\n"
        "s_nop 2\n"
        "v_mfma_f32_16x16x32_bf16 v[120:123], a[68:71], a[220:223], 0\n"
        "s_nop 2\n"
        "v_mfma_f32_16x16x32_bf16 v[120:123], a[64:67], a[216:219], v[120:123]\n"
        "buffer_store_dword v232, v238, s[16:19], s38 offen offset:0 sc1\n"
        "v_mfma_f32_16x16x32_bf16 v[120:123], a[60:63], a[248:251], v[120:123]\n"
        "ds_read_b128 a[224:227], v240 offset:1536\n"
        "v_cvt_pk_bf16_f32 v158, v116, v117\n"
        "v_mfma_f32_16x16x32_bf16 v[120:123], a[56:59], a[244:247], v[120:123]\n"
        "ds_read_b128 a[228:231], v241 offset:1536\n"
        "v_cvt_pk_bf16_f32 v159, v118, v119\n"
        "v_mfma_f32_16x16x32_bf16 v[120:123], a[52:55], a[240:243], v[120:123]\n"
        "ds_read_b128 a[232:235], v240 offset:1664\n"
        "v_cvt_pk_bf16_f32 v232, v214, v222\n"
        "v_mfma_f32_16x16x32_bf16 v[120:123], a[48:51], a[236:239], v[120:123]\n"
        "buffer_store_dword v234, v238, s[16:19], s38 offen offset:384 sc1\n"
        "v_mfma_f32_16x16x32_bf16 v[112:115], a[236:239], a[144:147], 0\n"
        "v_add_u32 v247, s45, v247\n"
        "v_cvt_pk_bf16_f32 v234, v215, v223\n"
        "v_mfma_f32_16x16x32_bf16 v[112:115], a[240:243], a[148:151], v[112:115]\n"
        "s_nop 2\n"
        "v_mfma_f32_16x16x32_bf16 v[112:115], a[244:247], a[152:155], v[112:115]\n"
        "v_cvt_pk_bf16_f32 v160, v120, v121\n"
        "v_cvt_pk_bf16_f32 v161, v122, v123\n"
        "v_mfma_f32_16x16x32_bf16 v[112:115], a[248:251], a[156:159], v[112:115]\n"
        "v_cvt_pk_bf16_f32 v166, v120, v121\n"
        "v_cvt_pk_bf16_f32 v167, v122, v123\n"
        "v_mfma_f32_16x16x32_bf16 v[112:115], a[216:219], a[160:163], v[112:115]\n"
        "buffer_store_dword v232, v238, s[16:19], s38 offen offset:768 sc1\n"
        "v_mfma_f32_16x16x32_bf16 v[112:115], a[220:223], a[164:167], v[112:115]\n"
        "s_nop 2\n"
        "v_mfma_f32_16x16x32_bf16 v[116:119], a[220:223], a[188:191], 0\n"
        "v_cvt_pk_bf16_f32 v232, v216, v224\n"
        "s_nop 0\n"
        "v_mfma_f32_16x16x32_bf16 v[116:119], a[216:219], a[184:187], v[116:119]\n"
        "s_nop 0\n"
        "v_cvt_pk_bf16_f32 v128, v112, v113\n"
        "v_cvt_pk_bf16_f32 v129, v114, v115\n"
        "v_mfma_f32_16x16x32_bf16 v[116:119], a[248:251], a[180:183], v[116:119]\n"
        "buffer_store_dword v234, v238, s[16:19], s38 offen offset:1152 sc1\n"
        "v_mfma_f32_16x16x32_bf16 v[116:119], a[244:247], a[176:179], v[116:119]\n"
        "s_nop 0\n"
        "v_cvt_pk_bf16_f32 v234, v217, v225\n"
        "v_mfma_f32_16x16x32_bf16 v[116:119], a[240:243], a[172:175], v[116:119]\n"
        "v_mfma_f32_16x16x32_bf16 v[116:119], a[236:239], a[168:171], v[116:119]\n"
        "s_nop 2\n"
        "v_mfma_f32_16x16x32_bf16 v[120:123], a[236:239], a[192:195], 0\n"
        "ds_read_b128 a[236:239], v241 offset:1664\n"
        "s_nop 0\n"
        "v_mfma_f32_16x16x32_bf16 v[120:123], a[240:243], a[196:199], v[120:123]\n"
        "buffer_store_dword v232, v238, s[16:19], s39 offen offset:0 sc1\n"
        "v_mfma_f32_16x16x32_bf16 v[120:123], a[244:247], a[200:203], v[120:123]\n"
        "ds_read_b128 a[240:243], v240 offset:1792\n"
        "ds_read_b128 a[244:247], v241 offset:1792\n"
        "v_mfma_f32_16x16x32_bf16 v[120:123], a[248:251], a[204:207], v[120:123]\n"
        "v_cvt_pk_bf16_f32 v136, v116, v117\n"
        "v_cvt_pk_bf16_f32 v137, v118, v119\n"
        "v_mfma_f32_16x16x32_bf16 v[120:123], a[216:219], a[208:211], v[120:123]\n"
        "v_cvt_pk_bf16_f32 v232, v218, v226\n"
        "v_mfma_f32_16x16x32_bf16 v[120:123], a[220:223], a[212:215], v[120:123]\n"
        "s_waitcnt lgkmcnt(5)\n"
        "buffer_store_dword v234, v238, s[16:19], s39 offen offset:384 sc1\n"
        "v_mfma_f32_16x16x32_bf16 v[112:115], a[72:75], a[224:227], 0\n"
        "s_waitcnt lgkmcnt(4)\n"
        "s_nop 1\n"
        "v_mfma_f32_16x16x32_bf16 v[112:115], a[76:79], a[228:231], v[112:115]\n"
        "s_waitcnt lgkmcnt(3)\n"
        "v_cvt_pk_bf16_f32 v234, v219, v227\n"
        "s_nop 0\n"
        "v_mfma_f32_16x16x32_bf16 v[112:115], a[80:83], a[232:235], v[112:115]\n"
        "s_waitcnt lgkmcnt(2)\n"
        "v_cvt_pk_bf16_f32 v144, v120, v121\n"
        "v_cvt_pk_bf16_f32 v145, v122, v123\n"
        "v_mfma_f32_16x16x32_bf16 v[112:115], a[84:87], a[236:239], v[112:115]\n"
        "s_waitcnt lgkmcnt(1)\n"
        "v_mfma_f32_16x16x32_bf16 v[112:115], a[88:91], a[240:243], v[112:115]\n"
        "s_waitcnt lgkmcnt(0)\n"
        "buffer_store_dword v232, v238, s[16:19], s39 offen offset:768 sc1\n"
        "v_mfma_f32_16x16x32_bf16 v[112:115], a[92:95], a[244:247], v[112:115]\n"
        "s_nop 2\n"
        "v_mfma_f32_16x16x32_bf16 v[116:119], a[116:119], a[244:247], 0\n"
        "s_nop 2\n"
        "v_mfma_f32_16x16x32_bf16 v[116:119], a[112:115], a[240:243], v[116:119]\n"
        "v_cvt_pk_bf16_f32 v172, v112, v113\n"
        "v_cvt_pk_bf16_f32 v173, v114, v115\n"
        "v_mfma_f32_16x16x32_bf16 v[116:119], a[108:111], a[236:239], v[116:119]\n"
        "buffer_store_dword v234, v238, s[16:19], s39 offen offset:1152 sc1\n"
        "v_mfma_f32_16x16x32_bf16 v[116:119], a[104:107], a[232:235], v[116:119]\n"
        "v_mfma_f32_16x16x32_bf16 v[116:119], a[100:103], a[228:231], v[116:119]\n"
        "v_mfma_f32_16x16x32_bf16 v[116:119], a[96:99], a[224:227], v[116:119]\n"
        "s_mov_b32 m0, s48\n"
        "s_nop 1\n"
        "v_mfma_f32_16x16x32_bf16 v[120:123], a[120:123], a[224:227], 0\n"
        "ds_read_b128 v[162:165], v251 offset:0\n"
        "s_nop 0\n"
        "v_mfma_f32_16x16x32_bf16 v[120:123], a[124:127], a[228:231], v[120:123]\n"
        "buffer_load_dwordx4 v249, s[12:15], s36 offen offset:0 lds\n"
        "v_mfma_f32_16x16x32_bf16 v[120:123], a[128:131], a[232:235], v[120:123]\n"
        "v_cvt_pk_bf16_f32 v174, v116, v117\n"
        "v_cvt_pk_bf16_f32 v175, v118, v119\n"
        "v_mfma_f32_16x16x32_bf16 v[120:123], a[132:135], a[236:239], v[120:123]\n"
        "v_mfma_f32_16x16x32_bf16 v[120:123], a[136:139], a[240:243], v[120:123]\n"
        "v_mfma_f32_16x16x32_bf16 v[120:123], a[140:143], a[244:247], v[120:123]\n"
        "s_nop 2\n"
        "v_mfma_f32_16x16x32_bf16 v[112:115], a[20:23], a[244:247], 0\n"
        "s_nop 2\n"
        "v_mfma_f32_16x16x32_bf16 v[112:115], a[16:19], a[240:243], v[112:115]\n"
        "v_cvt_pk_bf16_f32 v190, v120, v121\n"
        "v_cvt_pk_bf16_f32 v191, v122, v123\n"
        "v_mfma_f32_16x16x32_bf16 v[112:115], a[12:15], a[236:239], v[112:115]\n"
        "v_mfma_f32_16x16x32_bf16 v[112:115], a[8:11], a[232:235], v[112:115]\n"
        "v_mfma_f32_16x16x32_bf16 v[112:115], a[4:7], a[228:231], v[112:115]\n"
        "ds_read_b128 v[182:185], v251 offset:512\n"
        "v_mfma_f32_16x16x32_bf16 v[112:115], a[0:3], a[224:227], v[112:115]\n"
        "s_nop 2\n"
        "v_mfma_f32_16x16x32_bf16 v[116:119], a[24:27], a[224:227], 0\n"
        "s_nop 2\n"
        "v_mfma_f32_16x16x32_bf16 v[116:119], a[28:31], a[228:231], v[116:119]\n"
        "v_cvt_pk_bf16_f32 v176, v112, v113\n"
        "v_cvt_pk_bf16_f32 v177, v114, v115\n"
        "v_mfma_f32_16x16x32_bf16 v[116:119], a[32:35], a[232:235], v[116:119]\n"
        "v_mfma_f32_16x16x32_bf16 v[116:119], a[36:39], a[236:239], v[116:119]\n"
        "buffer_load_dwordx4 v253, s[12:15], s36 offen offset:1024 lds\n"
        "v_mfma_f32_16x16x32_bf16 v[116:119], a[40:43], a[240:243], v[116:119]\n"
        "v_mfma_f32_16x16x32_bf16 v[116:119], a[44:47], a[244:247], v[116:119]\n"
        "s_nop 2\n"
        "v_mfma_f32_16x16x32_bf16 v[120:123], a[68:71], a[244:247], 0\n"
        "s_nop 2\n"
        "v_mfma_f32_16x16x32_bf16 v[120:123], a[64:67], a[240:243], v[120:123]\n"
        "v_cvt_pk_bf16_f32 v178, v116, v117\n"
        "v_cvt_pk_bf16_f32 v179, v118, v119\n"
        "v_mfma_f32_16x16x32_bf16 v[120:123], a[60:63], a[236:239], v[120:123]\n"
        "v_mfma_f32_16x16x32_bf16 v[120:123], a[56:59], a[232:235], v[120:123]\n"
        "v_mfma_f32_16x16x32_bf16 v[120:123], a[52:55], a[228:231], v[120:123]\n"
        "v_mfma_f32_16x16x32_bf16 v[120:123], a[48:51], a[224:227], v[120:123]\n"
        "s_nop 2\n"
        "v_mfma_f32_16x16x32_bf16 v[112:115], a[224:227], a[144:147], 0\n"
        "s_nop 2\n"
        "v_mfma_f32_16x16x32_bf16 v[112:115], a[228:231], a[148:151], v[112:115]\n"
        "v_cvt_pk_bf16_f32 v180, v120, v121\n"
        "v_cvt_pk_bf16_f32 v181, v122, v123\n"
        "v_mfma_f32_16x16x32_bf16 v[112:115], a[232:235], a[152:155], v[112:115]\n"
        "v_cvt_pk_bf16_f32 v186, v120, v121\n"
        "v_cvt_pk_bf16_f32 v187, v122, v123\n"
        "v_mfma_f32_16x16x32_bf16 v[112:115], a[236:239], a[156:159], v[112:115]\n"
        "s_add_u32 m0, s41, 0\n"
        "v_mfma_f32_16x16x32_bf16 v[112:115], a[240:243], a[160:163], v[112:115]\n"
        "ds_read_b128 a[248:251], v240 offset:12288\n"
        "v_mfma_f32_16x16x32_bf16 v[112:115], a[244:247], a[164:167], v[112:115]\n"
        "buffer_load_dwordx4 v242, s[8:11], s47 offen lds\n"
        "v_mfma_f32_16x16x32_bf16 v[96:99], v[152:155], v[156:159], v[0:3]\n"
        "s_waitcnt lgkmcnt(2)\n"
        "ds_read_b128 a[216:219], v241 offset:12288\n"
        "s_nop 0\n"
        "v_mfma_f32_16x16x32_bf16 v[96:99], v[168:171], v[160:163], v[96:99]\n"
        "ds_read_b128 a[220:223], v240 offset:12416\n"
        "s_sub_u32 s31, s30, s28\n"
        "s_nop 0\n"
        "v_mfma_f32_16x16x32_bf16 v[100:103], v[172:175], v[156:159], v[4:7]\n"
        "s_sub_u32 s45, s28, s29\n"
        "v_cvt_pk_bf16_f32 v130, v112, v113\n"
        "v_cvt_pk_bf16_f32 v131, v114, v115\n"
        "v_mfma_f32_16x16x32_bf16 v[100:103], v[188:191], v[164:167], v[100:103]\n"
        "s_add_u32 s28, s28, s31\n"
        "ds_read_b128 v[162:165], v252 offset:0\n"
        "v_mfma_f32_16x16x32_bf16 v[116:119], a[244:247], a[188:191], 0\n"
        "s_mov_b32 s30, s29\n"
        "v_exp_f32 v96, v96\n"
        "v_mfma_f32_16x16x32_bf16 v[116:119], a[240:243], a[184:187], v[116:119]\n"
        "s_add_u32 s29, s29, s45\n"
        "v_exp_f32 v97, v97\n"
        "v_mfma_f32_16x16x32_bf16 v[116:119], a[236:239], a[180:183], v[116:119]\n"
        "v_exp_f32 v98, v98\n"
        "v_mfma_f32_16x16x32_bf16 v[116:119], a[232:235], a[176:179], v[116:119]\n"
        "v_exp_f32 v100, v100\n"
        "v_mfma_f32_16x16x32_bf16 v[116:119], a[228:231], a[172:175], v[116:119]\n"
        "v_exp_f32 v99, v99\n"
        "v_mfma_f32_16x16x32_bf16 v[116:119], a[224:227], a[168:171], v[116:119]\n"
        "v_exp_f32 v101, v101\n"
        "v_mfma_f32_16x16x32_bf16 v[104:107], v[152:155], v[176:179], v[16:19]\n"
        "s_waitcnt lgkmcnt(4)\n"
        "v_exp_f32 v102, v102\n"
        "v_mfma_f32_16x16x32_bf16 v[104:107], v[168:171], v[180:183], v[104:107]\n"
        "s_add_u32 m0, s41, 1024\n"
        "v_cvt_pk_bf16_f32 v64, v96, v97\n"
        "v_cvt_pk_bf16_f32 v65, v98, v99\n"
        "v_mfma_f32_16x16x32_bf16 v[108:111], v[172:175], v[176:179], v[20:23]\n"
        "v_cvt_pk_bf16_f32 v138, v116, v117\n"
        "v_cvt_pk_bf16_f32 v139, v118, v119\n"
        "v_mfma_f32_16x16x32_bf16 v[108:111], v[188:191], v[184:187], v[108:111]\n"
        "buffer_load_dwordx4 v243, s[8:11], s47 offen lds\n"
        "v_mfma_f32_16x16x32_bf16 v[120:123], a[224:227], a[192:195], 0\n"
        "ds_read_b128 a[224:227], v241 offset:12416\n"
        "v_cvt_pk_bf16_f32 v66, v100, v101\n"
        "v_mfma_f32_16x16x32_bf16 v[120:123], a[228:231], a[196:199], v[120:123]\n"
        "ds_read_b128 a[228:231], v240 offset:12544\n"
        "v_mfma_f32_16x16x32_bf16 v[120:123], a[232:235], a[200:203], v[120:123]\n"
        "ds_read_b128 a[232:235], v241 offset:12544\n"
        "v_mfma_f32_16x16x32_bf16 v[120:123], a[236:239], a[204:207], v[120:123]\n"
        "ds_read_b128 v[202:205], v251 offset:4096\n"
        "v_mfma_f32_16x16x32_bf16 v[120:123], a[240:243], a[208:211], v[120:123]\n"
        "ds_read_b128 v[182:185], v252 offset:512\n"
        "v_mfma_f32_16x16x32_bf16 v[120:123], a[244:247], a[212:215], v[120:123]\n"
        "s_waitcnt lgkmcnt(8)\n"
        "v_exp_f32 v103, v103\n"
        "v_mfma_f32_16x16x32_bf16 v[112:115], a[0:3], a[248:251], 0\n"
        "s_waitcnt lgkmcnt(7)\n"
        "v_cvt_pk_bf16_f32 v67, v102, v103\n"
        "s_nop 0\n"
        "v_mfma_f32_16x16x32_bf16 v[112:115], a[4:7], a[216:219], v[112:115]\n"
        "s_waitcnt lgkmcnt(6)\n"
        "v_exp_f32 v104, v104\n"
        "v_mfma_f32_16x16x32_bf16 v[112:115], a[8:11], a[220:223], v[112:115]\n"
        "s_waitcnt lgkmcnt(4)\n"
        "v_exp_f32 v105, v105\n"
        "v_mfma_f32_16x16x32_bf16 v[112:115], a[12:15], a[224:227], v[112:115]\n"
        "s_waitcnt lgkmcnt(3)\n"
        "v_exp_f32 v106, v106\n"
        "v_mfma_f32_16x16x32_bf16 v[112:115], a[16:19], a[228:231], v[112:115]\n"
        "s_waitcnt lgkmcnt(2)\n"
        "v_cvt_pk_bf16_f32 v72, v104, v105\n"
        "v_cvt_pk_bf16_f32 v146, v120, v121\n"
        "v_mfma_f32_16x16x32_bf16 v[112:115], a[20:23], a[232:235], v[112:115]\n"
        "s_add_u32 m0, s41, 2048\n"
        "v_exp_f32 v108, v108\n"
        "v_mfma_f32_16x16x32_bf16 v[116:119], a[44:47], a[232:235], 0\n"
        "v_cvt_pk_bf16_f32 v147, v122, v123\n"
        "v_add_u32 v245, s45, v245\n"
        "v_mfma_f32_16x16x32_bf16 v[116:119], a[40:43], a[228:231], v[116:119]\n"
        "buffer_load_dwordx4 v244, s[8:11], s47 offen lds\n"
        "v_mfma_f32_16x16x32_bf16 v[116:119], a[36:39], a[224:227], v[116:119]\n"
        "v_exp_f32 v107, v107\n"
        "v_mfma_f32_16x16x32_bf16 v[116:119], a[32:35], a[220:223], v[116:119]\n"
        "v_exp_f32 v109, v109\n"
        "v_mfma_f32_16x16x32_bf16 v[116:119], a[28:31], a[216:219], v[116:119]\n"
        "v_cvt_pk_bf16_f32 v73, v106, v107\n"
        "v_cvt_pk_bf16_f32 v74, v108, v109\n"
        "v_mfma_f32_16x16x32_bf16 v[116:119], a[24:27], a[248:251], v[116:119]\n"
        "v_exp_f32 v110, v110\n"
        "v_mfma_f32_16x16x32_bf16 v[120:123], a[48:51], a[248:251], 0\n"
        "v_exp_f32 v111, v111\n"
        "v_mfma_f32_16x16x32_bf16 v[120:123], a[52:55], a[216:219], v[120:123]\n"
        "v_cvt_pk_bf16_f32 v75, v110, v111\n"
        "v_cvt_pk_bf16_f32 v196, v112, v113\n"
        "v_mfma_f32_16x16x32_bf16 v[120:123], a[56:59], a[220:223], v[120:123]\n"
        "v_cvt_pk_bf16_f32 v197, v114, v115\n"
        "v_cvt_pk_bf16_f32 v198, v116, v117\n"
        "v_mfma_f32_16x16x32_bf16 v[120:123], a[60:63], a[224:227], v[120:123]\n"
        "v_cvt_pk_bf16_f32 v199, v118, v119\n"
        "v_add_u32 v246, s45, v246\n"
        "v_mfma_f32_16x16x32_bf16 v[120:123], a[64:67], a[228:231], v[120:123]\n"
        "v_mfma_f32_16x16x32_bf16 v[120:123], a[68:71], a[232:235], v[120:123]\n"
        "s_nop 2\n"
        "v_mfma_f32_16x16x32_bf16 v[112:115], a[92:95], a[232:235], 0\n"
        "s_nop 2\n"
        "v_mfma_f32_16x16x32_bf16 v[112:115], a[88:91], a[228:231], v[112:115]\n"
        "s_add_u32 m0, s41, 12288\n"
        "v_cvt_pk_bf16_f32 v200, v120, v121\n"
        "v_cvt_pk_bf16_f32 v201, v122, v123\n"
        "v_mfma_f32_16x16x32_bf16 v[112:115], a[84:87], a[224:227], v[112:115]\n"
        "v_cvt_pk_bf16_f32 v206, v120, v121\n"
        "v_cvt_pk_bf16_f32 v207, v122, v123\n"
        "v_mfma_f32_16x16x32_bf16 v[112:115], a[80:83], a[220:223], v[112:115]\n"
        "buffer_load_dwordx4 v242, s[8:11], s35 offen lds\n"
        "v_mfma_f32_16x16x32_bf16 v[112:115], a[76:79], a[216:219], v[112:115]\n"
        "v_mfma_f32_16x16x32_bf16 v[112:115], a[72:75], a[248:251], v[112:115]\n"
        "s_nop 2\n"
        "v_mfma_f32_16x16x32_bf16 v[96:99], v[152:155], v[196:199], v[32:35]\n"
        "s_waitcnt lgkmcnt(1)\n"
        "s_nop 1\n"
        "v_mfma_f32_16x16x32_bf16 v[96:99], v[168:171], v[200:203], v[96:99]\n"
        "s_nop 0\n"
        "v_cvt_pk_bf16_f32 v192, v112, v113\n"
        "v_cvt_pk_bf16_f32 v193, v114, v115\n"
        "v_mfma_f32_16x16x32_bf16 v[100:103], v[172:175], v[196:199], v[36:39]\n"
        "v_mfma_f32_16x16x32_bf16 v[100:103], v[188:191], v[204:207], v[100:103]\n"
        "ds_read_b128 v[202:205], v252 offset:4096\n"
        "s_cmp_eq_u32 s37, 100663296\n"
        "v_mfma_f32_16x16x32_bf16 v[116:119], a[96:99], a[248:251], 0\n"
        "s_cselect_b32 s46, 0, s37\n"
        "v_exp_f32 v96, v96\n"
        "v_mfma_f32_16x16x32_bf16 v[116:119], a[100:103], a[216:219], v[116:119]\n"
        "s_add_u32 s38, s46, s43\n"
        "v_exp_f32 v97, v97\n"
        "v_mfma_f32_16x16x32_bf16 v[116:119], a[104:107], a[220:223], v[116:119]\n"
        "s_add_u32 s39, s46, s44\n"
        "v_exp_f32 v98, v98\n"
        "v_mfma_f32_16x16x32_bf16 v[116:119], a[108:111], a[224:227], v[116:119]\n"
        "s_add_u32 s37, s37, 6291456\n"
        "v_exp_f32 v100, v100\n"
        "v_mfma_f32_16x16x32_bf16 v[116:119], a[112:115], a[228:231], v[116:119]\n"
        "v_exp_f32 v99, v99\n"
        "v_mfma_f32_16x16x32_bf16 v[116:119], a[116:119], a[232:235], v[116:119]\n"
        "s_add_u32 m0, s41, 13312\n"
        "v_exp_f32 v101, v101\n"
        "v_mfma_f32_16x16x32_bf16 v[120:123], a[140:143], a[232:235], 0\n"
        "v_exp_f32 v102, v102\n"
        "v_mfma_f32_16x16x32_bf16 v[120:123], a[136:139], a[228:231], v[120:123]\n"
        "buffer_load_dwordx4 v243, s[8:11], s35 offen lds\n"
        "v_mfma_f32_16x16x32_bf16 v[120:123], a[132:135], a[224:227], v[120:123]\n"
        "v_cvt_pk_bf16_f32 v80, v96, v97\n"
        "v_cvt_pk_bf16_f32 v194, v116, v117\n"
        "v_mfma_f32_16x16x32_bf16 v[120:123], a[128:131], a[220:223], v[120:123]\n"
        "v_cvt_pk_bf16_f32 v195, v118, v119\n"
        "v_cvt_pk_bf16_f32 v81, v98, v99\n"
        "v_mfma_f32_16x16x32_bf16 v[120:123], a[124:127], a[216:219], v[120:123]\n"
        "v_cvt_pk_bf16_f32 v82, v100, v101\n"
        "v_mfma_f32_16x16x32_bf16 v[120:123], a[120:123], a[248:251], v[120:123]\n"
        "v_exp_f32 v103, v103\n"
        "v_mfma_f32_16x16x32_bf16 v[112:115], a[248:251], a[144:147], 0\n"
        "v_cvt_pk_bf16_f32 v83, v102, v103\n"
        "s_nop 0\n"
        "v_mfma_f32_16x16x32_bf16 v[112:115], a[216:219], a[148:151], v[112:115]\n"
        "s_nop 2\n"
        "v_mfma_f32_16x16x32_bf16 v[112:115], a[220:223], a[152:155], v[112:115]\n"
        "ds_read_b128 a[236:239], v240 offset:13824\n"
        "v_cvt_pk_bf16_f32 v208, v120, v121\n"
        "v_mfma_f32_16x16x32_bf16 v[112:115], a[224:227], a[156:159], v[112:115]\n"
        "ds_read_b128 a[240:243], v241 offset:13824\n"
        "v_cvt_pk_bf16_f32 v209, v122, v123\n"
        "v_mfma_f32_16x16x32_bf16 v[112:115], a[228:231], a[160:163], v[112:115]\n"
        "ds_read_b128 a[244:247], v240 offset:13952\n"
        "v_mfma_f32_16x16x32_bf16 v[112:115], a[232:235], a[164:167], v[112:115]\n"
        "s_nop 2\n"
        "v_mfma_f32_16x16x32_bf16 v[104:107], v[192:195], v[156:159], v[8:11]\n"
        "s_nop 1\n"
        "v_mfma_f32_16x16x32_bf16 v[104:107], v[208:211], v[160:163], v[104:107]\n"
        "s_add_u32 m0, s41, 14336\n"
        "v_cvt_pk_bf16_f32 v132, v112, v113\n"
        "v_cvt_pk_bf16_f32 v133, v114, v115\n"
        "v_mfma_f32_16x16x32_bf16 v[116:119], a[232:235], a[188:191], 0\n"
        "v_mfma_f32_16x16x32_bf16 v[116:119], a[228:231], a[184:187], v[116:119]\n"
        "buffer_load_dwordx4 v244, s[8:11], s35 offen lds\n"
        "v_mfma_f32_16x16x32_bf16 v[116:119], a[224:227], a[180:183], v[116:119]\n"
        "v_mfma_f32_16x16x32_bf16 v[116:119], a[220:223], a[176:179], v[116:119]\n"
        "v_exp_f32 v104, v104\n"
        "v_mfma_f32_16x16x32_bf16 v[116:119], a[216:219], a[172:175], v[116:119]\n"
        "v_exp_f32 v105, v105\n"
        "v_mfma_f32_16x16x32_bf16 v[116:119], a[248:251], a[168:171], v[116:119]\n"
        "v_exp_f32 v106, v106\n"
        "v_mfma_f32_16x16x32_bf16 v[108:111], v[192:195], v[176:179], v[24:27]\n"
        "s_waitcnt lgkmcnt(4)\n"
        "v_exp_f32 v107, v107\n"
        "v_mfma_f32_16x16x32_bf16 v[108:111], v[208:211], v[180:183], v[108:111]\n"
        "v_cvt_pk_bf16_f32 v68, v104, v105\n"
        "v_cvt_pk_bf16_f32 v69, v106, v107\n"
        "v_mfma_f32_16x16x32_bf16 v[120:123], a[248:251], a[192:195], 0\n"
        "ds_read_b128 a[248:251], v241 offset:13952\n"
        "v_cvt_pk_bf16_f32 v140, v116, v117\n"
        "v_mfma_f32_16x16x32_bf16 v[120:123], a[216:219], a[196:199], v[120:123]\n"
        "ds_read_b128 a[216:219], v240 offset:14080\n"
        "v_cvt_pk_bf16_f32 v141, v118, v119\n"
        "v_mfma_f32_16x16x32_bf16 v[120:123], a[220:223], a[200:203], v[120:123]\n"
        "ds_read_b128 a[220:223], v241 offset:14080\n"
        "v_mfma_f32_16x16x32_bf16 v[120:123], a[224:227], a[204:207], v[120:123]\n"
        "v_exp_f32 v108, v108\n"
        "v_mfma_f32_16x16x32_bf16 v[120:123], a[228:231], a[208:211], v[120:123]\n"
        "v_exp_f32 v109, v109\n"
        "v_mfma_f32_16x16x32_bf16 v[120:123], a[232:235], a[212:215], v[120:123]\n"
        "v_exp_f32 v110, v110\n"
        "v_mfma_f32_16x16x32_bf16 v[96:99], v[192:195], v[196:199], v[40:43]\n"
        "s_waitcnt lgkmcnt(6)\n"
        "v_exp_f32 v111, v111\n"
        "v_mfma_f32_16x16x32_bf16 v[96:99], v[208:211], v[200:203], v[96:99]\n"
        "s_waitcnt lgkmcnt(5)\n"
        "v_cvt_pk_bf16_f32 v76, v108, v109\n"
        "v_cvt_pk_bf16_f32 v77, v110, v111\n"
        "v_mfma_f32_16x16x32_bf16 v[112:115], a[72:75], a[236:239], 0\n"
        "s_waitcnt lgkmcnt(4)\n"
        "s_nop 0\n"
        "v_cvt_pk_bf16_f32 v148, v120, v121\n"
        "v_mfma_f32_16x16x32_bf16 v[112:115], a[76:79], a[240:243], v[112:115]\n"
        ".long 0xbec200ff\n"
        "45:\n"
        "s_branch 61b\n"
        "s_waitcnt lgkmcnt(3)\n"
        "v_cvt_pk_bf16_f32 v149, v122, v123\n"
        "v_mfma_f32_16x16x32_bf16 v[112:115], a[80:83], a[244:247], v[112:115]\n"
        "s_waitcnt lgkmcnt(2)\n"
        "v_exp_f32 v96, v96\n"
        "v_mfma_f32_16x16x32_bf16 v[112:115], a[84:87], a[248:251], v[112:115]\n"
        "s_waitcnt lgkmcnt(1)\n"
        "v_exp_f32 v97, v97\n"
        "v_mfma_f32_16x16x32_bf16 v[112:115], a[88:91], a[216:219], v[112:115]\n"
        "s_waitcnt lgkmcnt(0)\n"
        "v_exp_f32 v98, v98\n"
        "v_mfma_f32_16x16x32_bf16 v[112:115], a[92:95], a[220:223], v[112:115]\n"
        "v_exp_f32 v99, v99\n"
        "v_mfma_f32_16x16x32_bf16 v[116:119], a[116:119], a[220:223], 0\n"
        "v_cvt_pk_bf16_f32 v84, v96, v97\n"
        "v_cvt_pk_bf16_f32 v85, v98, v99\n"
        "v_mfma_f32_16x16x32_bf16 v[116:119], a[112:115], a[216:219], v[116:119]\n"
        "v_add_u32 v240, s31, v240\n"
        "v_add_u32 v241, s31, v241\n"
        "v_mfma_f32_16x16x32_bf16 v[116:119], a[108:111], a[248:251], v[116:119]\n"
        "v_cvt_pk_bf16_f32 v212, v112, v113\n"
        "v_cvt_pk_bf16_f32 v213, v114, v115\n"
        "v_mfma_f32_16x16x32_bf16 v[116:119], a[104:107], a[244:247], v[116:119]\n"
        "v_mfma_f32_16x16x32_bf16 v[116:119], a[100:103], a[240:243], v[116:119]\n"
        "v_mfma_f32_16x16x32_bf16 v[116:119], a[96:99], a[236:239], v[116:119]\n"
        "s_nop 2\n"
        "v_mfma_f32_16x16x32_bf16 v[120:123], a[120:123], a[236:239], 0\n"
        "s_nop 2\n"
        "v_mfma_f32_16x16x32_bf16 v[120:123], a[124:127], a[240:243], v[120:123]\n"
        "s_add_u32 s47, s47, 6291456\n"
        "v_cvt_pk_bf16_f32 v214, v116, v117\n"
        "v_cvt_pk_bf16_f32 v215, v118, v119\n"
        "v_mfma_f32_16x16x32_bf16 v[120:123], a[128:131], a[244:247], v[120:123]\n"
        "s_add_u32 s36, s36, 2097152\n"
        "v_mfma_f32_16x16x32_bf16 v[120:123], a[132:135], a[248:251], v[120:123]\n"
        "s_xor_b32 s48, s48, 8192\n"
        "v_mfma_f32_16x16x32_bf16 v[120:123], a[136:139], a[216:219], v[120:123]\n"
        "v_mfma_f32_16x16x32_bf16 v[120:123], a[140:143], a[220:223], v[120:123]\n"
        "s_nop 2\n"
        "v_mfma_f32_16x16x32_bf16 v[112:115], a[220:223], a[164:167], 0\n"
        "s_nop 2\n"
        "v_mfma_f32_16x16x32_bf16 v[112:115], a[216:219], a[160:163], v[112:115]\n"
        "v_cvt_pk_bf16_f32 v230, v120, v121\n"
        "v_cvt_pk_bf16_f32 v231, v122, v123\n"
        "v_mfma_f32_16x16x32_bf16 v[112:115], a[248:251], a[156:159], v[112:115]\n"
        "s_barrier\n"
        "v_mfma_f32_16x16x32_bf16 v[112:115], a[244:247], a[152:155], v[112:115]\n"
        "v_mfma_f32_16x16x32_bf16 v[112:115], a[240:243], a[148:151], v[112:115]\n"
        "v_mfma_f32_16x16x32_bf16 v[112:115], a[236:239], a[144:147], v[112:115]\n"
        "s_nop 2\n"
        "v_mfma_f32_16x16x32_bf16 v[100:103], v[212:215], v[156:159], v[12:15]\n"
        "s_nop 2\n"
        "v_mfma_f32_16x16x32_bf16 v[100:103], v[228:231], v[164:167], v[100:103]\n"
        "v_cvt_pk_bf16_f32 v134, v112, v113\n"
        "v_cvt_pk_bf16_f32 v135, v114, v115\n"
        "v_mfma_f32_16x16x32_bf16 v[116:119], a[236:239], a[168:171], 0\n"
        "s_sub_u32 s27, s27, 1\n"
        "v_mfma_f32_16x16x32_bf16 v[116:119], a[240:243], a[172:175], v[116:119]\n"
        "s_cmp_lg_u32 s27, 0\n"
        "v_mfma_f32_16x16x32_bf16 v[116:119], a[244:247], a[176:179], v[116:119]\n"
        "s_cbranch_scc0 19f\n"
        "v_mfma_f32_16x16x32_bf16 v[116:119], a[248:251], a[180:183], v[116:119]\n"
        "v_mfma_f32_16x16x32_bf16 v[116:119], a[216:219], a[184:187], v[116:119]\n"
        "s_nop 0\n"
        "v_exp_f32 v100, v100\n"
        "v_mfma_f32_16x16x32_bf16 v[116:119], a[220:223], a[188:191], v[116:119]\n"
        "v_exp_f32 v101, v101\n"
        "v_mfma_f32_16x16x32_bf16 v[104:107], v[212:215], v[176:179], v[28:31]\n"
        "v_exp_f32 v102, v102\n"
        "v_mfma_f32_16x16x32_bf16 v[104:107], v[228:231], v[184:187], v[104:107]\n"
        "v_exp_f32 v103, v103\n"
        "v_mfma_f32_16x16x32_bf16 v[120:123], a[220:223], a[212:215], 0\n"
        "v_cvt_pk_bf16_f32 v70, v100, v101\n"
        "v_cvt_pk_bf16_f32 v71, v102, v103\n"
        "v_mfma_f32_16x16x32_bf16 v[120:123], a[216:219], a[208:211], v[120:123]\n"
        "v_cvt_pk_bf16_f32 v142, v116, v117\n"
        "v_cvt_pk_bf16_f32 v143, v118, v119\n"
        "v_mfma_f32_16x16x32_bf16 v[120:123], a[248:251], a[204:207], v[120:123]\n"
        "v_mov_b64 v[96:97], 0\n"
        "v_mfma_f32_16x16x32_bf16 v[120:123], a[244:247], a[200:203], v[120:123]\n"
        "v_exp_f32 v104, v104\n"
        "v_mfma_f32_16x16x32_bf16 v[120:123], a[240:243], a[196:199], v[120:123]\n"
        "v_exp_f32 v105, v105\n"
        "v_mfma_f32_16x16x32_bf16 v[120:123], a[236:239], a[192:195], v[120:123]\n"
        "v_exp_f32 v106, v106\n"
        "v_mfma_f32_16x16x32_bf16 v[108:111], v[212:215], v[196:199], v[44:47]\n"
        "v_exp_f32 v107, v107\n"
        "v_mfma_f32_16x16x32_bf16 v[108:111], v[228:231], v[204:207], v[108:111]\n"
        "v_cvt_pk_bf16_f32 v78, v104, v105\n"
        "v_cvt_pk_bf16_f32 v79, v106, v107\n"
        "v_mfma_f32_16x16x32_bf16 v[112:115], a[0:3], a[236:239], 0\n"
        "s_nop 0\n"
        "v_cvt_pk_bf16_f32 v150, v120, v121\n"
        "v_cvt_pk_bf16_f32 v151, v122, v123\n"
        "v_mfma_f32_16x16x32_bf16 v[112:115], a[4:7], a[240:243], v[112:115]\n"
        "v_mfma_f32_16x16x32_bf16 v[112:115], a[8:11], a[244:247], v[112:115]\n"
        "v_exp_f32 v108, v108\n"
        "v_mfma_f32_16x16x32_bf16 v[112:115], a[12:15], a[248:251], v[112:115]\n"
        "v_exp_f32 v109, v109\n"
        "v_mfma_f32_16x16x32_bf16 v[112:115], a[16:19], a[216:219], v[112:115]\n"
        "v_exp_f32 v110, v110\n"
        "v_mfma_f32_16x16x32_bf16 v[112:115], a[20:23], a[220:223], v[112:115]\n"
        "v_exp_f32 v111, v111\n"
        "v_smfmac_f32_16x16x64_bf16 v[96:99], a[252:255], v[64:71], v237\n"
        "v_cvt_pk_bf16_f32 v86, v108, v109\n"
        "v_mfma_f32_16x16x32_bf16 v[100:103], v[132:135], v[68:71], 0\n"
        "v_mov_b64 v[124:125], 0\n"
        "v_cvt_pk_bf16_f32 v87, v110, v111\n"
        "v_mfma_f32_16x16x32_bf16 v[100:103], v[128:131], v[64:67], v[100:103]\n"
        "s_nop 0\n"
        "v_cvt_pk_bf16_f32 v216, v112, v113\n"
        "v_cvt_pk_bf16_f32 v217, v114, v115\n"
        "v_mfma_f32_16x16x32_bf16 v[104:107], v[136:139], v[64:67], 0\n"
        "ds_read_b128 v[222:225], v251 offset:4608\n"
        "v_add_f32 v97, v96, v97\n"
        "v_mfma_f32_16x16x32_bf16 v[104:107], v[140:143], v[68:71], v[104:107]\n"
        "v_rcp_f32 v96, v97\n"
        "v_mfma_f32_16x16x32_bf16 v[108:111], v[148:151], v[68:71], 0\n"
        "v_mul_f32 v100, v96, v100\n"
        "v_mul_f32 v101, v96, v101\n"
        "v_mfma_f32_16x16x32_bf16 v[108:111], v[144:147], v[64:67], v[108:111]\n"
        "v_mul_f32 v102, v96, v102\n"
        "v_mul_f32 v103, v96, v103\n"
        "v_mfma_f32_16x16x32_bf16 v[116:119], a[44:47], a[220:223], 0\n"
        "v_cvt_pk_bf16_f32 v100, v100, v101\n"
        "v_cvt_pk_bf16_f32 v101, v102, v103\n"
        "v_mfma_f32_16x16x32_bf16 v[116:119], a[40:43], a[216:219], v[116:119]\n"
        "v_mul_f32 v104, v96, v104\n"
        "v_mul_f32 v105, v96, v105\n"
        "v_mfma_f32_16x16x32_bf16 v[116:119], a[36:39], a[248:251], v[116:119]\n"
        "v_mul_f32 v106, v96, v106\n"
        "v_mul_f32 v107, v96, v107\n"
        "v_mfma_f32_16x16x32_bf16 v[116:119], a[32:35], a[244:247], v[116:119]\n"
        "v_cvt_pk_bf16_f32 v102, v104, v105\n"
        "v_cvt_pk_bf16_f32 v103, v106, v107\n"
        "v_mfma_f32_16x16x32_bf16 v[116:119], a[28:31], a[240:243], v[116:119]\n"
        "ds_write_b128 v245, v[100:103] offset:0\n"
        "v_mfma_f32_16x16x32_bf16 v[116:119], a[24:27], a[236:239], v[116:119]\n"
        "v_mul_f32 v108, v96, v108\n"
        "v_mul_f32 v109, v96, v109\n"
        "v_smfmac_f32_16x16x64_bf16 v[124:127], a[252:255], v[72:79], v237\n"
        "v_mul_f32 v110, v96, v110\n"
        "v_mfma_f32_16x16x32_bf16 v[64:67], v[132:135], v[76:79], 0\n"
        "v_mul_f32 v111, v96, v111\n"
        "v_cvt_pk_bf16_f32 v108, v108, v109\n"
        "v_mfma_f32_16x16x32_bf16 v[64:67], v[128:131], v[72:75], v[64:67]\n"
        "ds_read_b128 v[156:159], v247 offset:0\n"
        "v_cvt_pk_bf16_f32 v218, v116, v117\n"
        "v_mfma_f32_16x16x32_bf16 v[68:71], v[136:139], v[72:75], 0\n"
        "ds_read_b128 v[160:163], v247 offset:4096\n"
        "v_cvt_pk_bf16_f32 v219, v118, v119\n"
        "v_mfma_f32_16x16x32_bf16 v[68:71], v[140:143], v[76:79], v[68:71]\n"
        "ds_read_b128 v[164:167], v247 offset:4352\n"
        "v_cvt_pk_bf16_f32 v109, v110, v111\n"
        "v_mfma_f32_16x16x32_bf16 v[120:123], v[148:151], v[76:79], 0\n"
        "ds_write_b64 v246, v[108:109] offset:0\n"
        "v_mfma_f32_16x16x32_bf16 v[120:123], v[144:147], v[72:75], v[120:123]\n"
        "v_max_u32 v236, v96, v97\n"
        "v_mov_b64 v[116:117], 0\n"
        "v_mfma_f32_16x16x32_bf16 v[112:115], a[48:51], a[236:239], 0\n"
        "v_add_f32 v125, v124, v125\n"
        "v_mfma_f32_16x16x32_bf16 v[112:115], a[52:55], a[240:243], v[112:115]\n"
        "v_rcp_f32 v124, v125\n"
        "v_mfma_f32_16x16x32_bf16 v[112:115], a[56:59], a[244:247], v[112:115]\n"
        "v_mul_f32 v64, v124, v64\n"
        "v_mul_f32 v65, v124, v65\n"
        "v_mfma_f32_16x16x32_bf16 v[112:115], a[60:63], a[248:251], v[112:115]\n"
        "v_mul_f32 v66, v124, v66\n"
        "v_mul_f32 v67, v124, v67\n"
        "v_mfma_f32_16x16x32_bf16 v[112:115], a[64:67], a[216:219], v[112:115]\n"
        "v_cvt_pk_bf16_f32 v64, v64, v65\n"
        "v_cvt_pk_bf16_f32 v65, v66, v67\n"
        "v_mfma_f32_16x16x32_bf16 v[112:115], a[68:71], a[220:223], v[112:115]\n"
        "ds_read_b128 a[216:219], v248 offset:0\n"
        "v_mul_f32 v68, v124, v68\n"
        "v_smfmac_f32_16x16x64_bf16 v[116:119], a[252:255], v[80:87], v237\n"
        "ds_read_b128 a[220:223], v248 offset:3072\n"
        "v_mfma_f32_16x16x32_bf16 v[72:75], v[132:135], v[84:87], 0\n"
        "v_mul_f32 v69, v124, v69\n"
        "v_mul_f32 v70, v124, v70\n"
        "v_mfma_f32_16x16x32_bf16 v[72:75], v[128:131], v[80:83], v[72:75]\n"
        "v_cvt_pk_bf16_f32 v220, v112, v113\n"
        "v_cvt_pk_bf16_f32 v221, v114, v115\n"
        "v_mfma_f32_16x16x32_bf16 v[76:79], v[136:139], v[80:83], 0\n"
        "ds_read_b128 a[224:227], v248 offset:32\n"
        "v_cvt_pk_bf16_f32 v226, v112, v113\n"
        "v_mfma_f32_16x16x32_bf16 v[76:79], v[140:143], v[84:87], v[76:79]\n"
        "ds_read_b128 a[228:231], v248 offset:3104\n"
        "v_cvt_pk_bf16_f32 v227, v114, v115\n"
        "v_mfma_f32_16x16x32_bf16 v[96:99], v[148:151], v[84:87], 0\n"
        "v_mul_f32 v71, v124, v71\n"
        "v_cvt_pk_bf16_f32 v66, v68, v69\n"
        "v_mfma_f32_16x16x32_bf16 v[96:99], v[144:147], v[80:83], v[96:99]\n"
        "v_cvt_pk_bf16_f32 v67, v70, v71\n"
        "v_mul_f32 v120, v124, v120\n"
        "v_mfma_f32_16x16x32_bf16 v[100:103], v[152:155], v[216:219], v[48:51]\n"
        "s_waitcnt lgkmcnt(9)\n"
        "ds_read_b128 v[152:155], v247 offset:256\n"
        "v_mul_f32 v121, v124, v121\n"
        "v_mfma_f32_16x16x32_bf16 v[100:103], v[168:171], v[220:223], v[100:103]\n"
        "ds_write_b128 v245, v[64:67] offset:256\n"
        "v_mfma_f32_16x16x32_bf16 v[104:107], v[172:175], v[216:219], v[52:55]\n"
        "v_mul_f32 v122, v124, v122\n"
        "v_mul_f32 v123, v124, v123\n"
        "v_mfma_f32_16x16x32_bf16 v[104:107], v[188:191], v[224:227], v[104:107]\n"
        "s_waitcnt lgkmcnt(5)\n"
        "v_cvt_pk_bf16_f32 v120, v120, v121\n"
        "v_cvt_pk_bf16_f32 v121, v122, v123\n"
        "v_mfma_f32_16x16x32_bf16 v[176:179], v[156:159], a[216:219], 0\n"
        "s_waitcnt lgkmcnt(4)\n"
        "ds_write_b64 v246, v[120:121] offset:256\n"
        "v_mfma_f32_16x16x32_bf16 v[176:179], v[160:163], a[220:223], v[176:179]\n"
        "ds_read_b128 v[222:225], v252 offset:4608\n"
        "v_max3_u32 v236, v236, v124, v125\n"
        "v_mfma_f32_16x16x32_bf16 v[196:199], v[164:167], a[220:223], 0\n"
        "s_waitcnt lgkmcnt(3)\n"
        "ds_read_b128 a[232:235], v254 offset:0\n"
        "v_add_f32 v117, v116, v117\n"
        "v_mfma_f32_16x16x32_bf16 v[196:199], v[152:155], a[216:219], v[196:199]\n"
        "ds_read_b128 a[220:223], v254 offset:3072\n"
        "v_mfma_f32_16x16x32_bf16 v[200:203], v[152:155], a[224:227], 0\n"
        "ds_read_b128 a[216:219], v254 offset:32\n"
        "v_mfma_f32_16x16x32_bf16 v[200:203], v[164:167], a[228:231], v[200:203]\n"
        "v_rcp_f32 v116, v117\n"
        "v_mfma_f32_16x16x32_bf16 v[180:183], v[160:163], a[228:231], 0\n"
        "ds_read_b128 a[228:231], v254 offset:3104\n"
        "v_mul_f32 v72, v116, v72\n"
        "v_mfma_f32_16x16x32_bf16 v[180:183], v[156:159], a[224:227], v[180:183]\n"
        "ds_read_b128 a[236:239], v247 offset:8192\n"
        "v_mul_f32 v73, v116, v73\n"
        "v_mfma_f32_16x16x32_bf16 v[108:111], v[192:195], v[216:219], v[56:59]\n"
        "s_waitcnt lgkmcnt(5)\n"
        "ds_read_b128 a[224:227], v248 offset:16\n"
        "v_mul_f32 v74, v116, v74\n"
        "v_mfma_f32_16x16x32_bf16 v[108:111], v[208:211], v[220:223], v[108:111]\n"
        "ds_read_b128 a[240:243], v247 offset:12288\n"
        "v_mul_f32 v75, v116, v75\n"
        "v_mfma_f32_16x16x32_bf16 v[112:115], v[212:215], v[216:219], v[60:63]\n"
        "ds_read_b128 a[244:247], v247 offset:12544\n"
        "v_cvt_pk_bf16_f32 v72, v72, v73\n"
        "v_mfma_f32_16x16x32_bf16 v[112:115], v[228:231], v[224:227], v[112:115]\n"
        "s_waitcnt lgkmcnt(7)\n"
        "ds_read_b128 a[248:251], v247 offset:8448\n"
        "v_cvt_pk_bf16_f32 v73, v74, v75\n"
        "v_mfma_f32_16x16x32_bf16 v[184:187], v[156:159], a[232:235], 0\n"
        "s_waitcnt lgkmcnt(7)\n"
        "v_exp_f32 v100, v100\n"
        "v_mfma_f32_16x16x32_bf16 v[184:187], v[160:163], a[220:223], v[184:187]\n"
        "v_exp_f32 v101, v101\n"
        "v_mfma_f32_16x16x32_bf16 v[204:207], v[164:167], a[220:223], 0\n"
        "ds_read_b128 a[220:223], v248 offset:3088\n"
        "v_mul_f32 v76, v116, v76\n"
        "v_mfma_f32_16x16x32_bf16 v[204:207], v[152:155], a[232:235], v[204:207]\n"
        "s_waitcnt lgkmcnt(7)\n"
        "ds_read_b128 a[232:235], v254 offset:16\n"
        "v_mul_f32 v77, v116, v77\n"
        "v_mfma_f32_16x16x32_bf16 v[192:195], v[152:155], a[216:219], 0\n"
        "s_waitcnt lgkmcnt(7)\n"
        "v_mul_f32 v78, v116, v78\n"
        "v_mul_f32 v79, v116, v79\n"
        "v_mfma_f32_16x16x32_bf16 v[192:195], v[164:167], a[228:231], v[192:195]\n"
        "v_exp_f32 v102, v102\n"
        "v_mfma_f32_16x16x32_bf16 v[172:175], v[160:163], a[228:231], 0\n"
        "ds_read_b128 a[228:231], v254 offset:3088\n"
        "v_cvt_pk_bf16_f32 v88, v100, v101\n"
        "v_mfma_f32_16x16x32_bf16 v[172:175], v[156:159], a[216:219], v[172:175]\n"
        "s_waitcnt lgkmcnt(6)\n"
        "ds_read_b128 a[216:219], v248 offset:6144\n"
        "v_cvt_pk_bf16_f32 v74, v76, v77\n"
        "v_mfma_f32_16x16x32_bf16 v[212:215], v[156:159], a[224:227], 0\n"
        "s_waitcnt lgkmcnt(3)\n"
        "v_exp_f32 v104, v104\n"
        "v_mfma_f32_16x16x32_bf16 v[212:215], v[160:163], a[220:223], v[212:215]\n"
        "v_exp_f32 v103, v103\n"
        "v_mfma_f32_16x16x32_bf16 v[216:219], v[164:167], a[220:223], 0\n"
        "ds_read_b128 a[220:223], v248 offset:9216\n"
        "v_cvt_pk_bf16_f32 v75, v78, v79\n"
        "v_mfma_f32_16x16x32_bf16 v[216:219], v[152:155], a[224:227], v[216:219]\n"
        "s_waitcnt lgkmcnt(3)\n"
        "ds_write_b128 v245, v[72:75] offset:512\n"
        "v_mfma_f32_16x16x32_bf16 v[224:227], v[152:155], a[232:235], 0\n"
        "s_waitcnt lgkmcnt(3)\n"
        "v_exp_f32 v105, v105\n"
        "v_mfma_f32_16x16x32_bf16 v[224:227], v[164:167], a[228:231], v[224:227]\n"
        "v_cvt_pk_bf16_f32 v89, v102, v103\n"
        "v_cvt_pk_bf16_f32 v90, v104, v105\n"
        "v_mfma_f32_16x16x32_bf16 v[220:223], v[160:163], a[228:231], 0\n"
        "ds_read_b128 a[224:227], v248 offset:6176\n"
        "ds_read_b128 a[228:231], v248 offset:9248\n"
        "v_mfma_f32_16x16x32_bf16 v[220:223], v[156:159], a[232:235], v[220:223]\n"
        "s_waitcnt lgkmcnt(4)\n"
        "ds_read_b128 a[232:235], v254 offset:6144\n"
        "v_mul_f32 v96, v116, v96\n"
        "v_mfma_f32_16x16x32_bf16 v[176:179], a[236:239], a[216:219], v[176:179]\n"
        "s_waitcnt lgkmcnt(4)\n"
        "v_exp_f32 v106, v106\n"
        "v_mfma_f32_16x16x32_bf16 v[176:179], a[240:243], a[220:223], v[176:179]\n"
        "v_mul_f32 v97, v116, v97\n"
        "v_mul_f32 v98, v116, v98\n"
        "v_mfma_f32_16x16x32_bf16 v[196:199], a[244:247], a[220:223], v[196:199]\n"
        "ds_read_b128 a[220:223], v254 offset:9216\n"
        "v_mul_f32 v99, v116, v99\n"
        "v_mfma_f32_16x16x32_bf16 v[196:199], a[248:251], a[216:219], v[196:199]\n"
        "s_waitcnt lgkmcnt(3)\n"
        "ds_read_b128 a[216:219], v254 offset:6176\n"
        "v_cvt_pk_bf16_f32 v96, v96, v97\n"
        "v_mfma_f32_16x16x32_bf16 v[200:203], a[248:251], a[224:227], v[200:203]\n"
        "s_waitcnt lgkmcnt(3)\n"
        "ds_read_b128 v[156:159], v247 offset:16384\n"
        "v_cvt_pk_bf16_f32 v97, v98, v99\n"
        "v_mfma_f32_16x16x32_bf16 v[200:203], a[244:247], a[228:231], v[200:203]\n"
        "ds_write_b64 v246, v[96:97] offset:512\n"
        "v_mfma_f32_16x16x32_bf16 v[180:183], a[240:243], a[228:231], v[180:183]\n"
        "ds_read_b128 a[228:231], v254 offset:9248\n"
        "v_max3_u32 v236, v236, v116, v117\n"
        "v_mfma_f32_16x16x32_bf16 v[180:183], a[236:239], a[224:227], v[180:183]\n"
        "s_waitcnt lgkmcnt(5)\n"
        "ds_read_b128 a[224:227], v248 offset:6160\n"
        "v_mfma_f32_16x16x32_bf16 v[184:187], a[236:239], a[232:235], v[184:187]\n"
        "s_waitcnt lgkmcnt(5)\n"
        "ds_read_b128 v[160:163], v247 offset:20480\n"
        "v_mfma_f32_16x16x32_bf16 v[184:187], a[240:243], a[220:223], v[184:187]\n"
        "ds_read_b128 v[164:167], v247 offset:20736\n"
        "v_mfma_f32_16x16x32_bf16 v[204:207], a[244:247], a[220:223], v[204:207]\n"
        "ds_read_b128 a[220:223], v248 offset:9232\n"
        "v_mfma_f32_16x16x32_bf16 v[204:207], a[248:251], a[232:235], v[204:207]\n"
        "s_waitcnt lgkmcnt(7)\n"
        "ds_read_b128 a[232:235], v254 offset:6160\n"
        "v_mfma_f32_16x16x32_bf16 v[192:195], a[248:251], a[216:219], v[192:195]\n"
        "s_waitcnt lgkmcnt(5)\n"
        "ds_read_b128 v[152:155], v247 offset:16640\n"
        "v_mfma_f32_16x16x32_bf16 v[192:195], a[244:247], a[228:231], v[192:195]\n"
        "v_exp_f32 v107, v107\n"
        "v_mfma_f32_16x16x32_bf16 v[172:175], a[240:243], a[228:231], v[172:175]\n"
        "ds_read_b128 a[228:231], v254 offset:9232\n"
        "v_cvt_pk_bf16_f32 v91, v106, v107\n"
        "v_mfma_f32_16x16x32_bf16 v[172:175], a[236:239], a[216:219], v[172:175]\n"
        "s_waitcnt lgkmcnt(6)\n"
        "ds_read_b128 a[216:219], v248 offset:12288\n"
        "v_mfma_f32_16x16x32_bf16 v[212:215], a[236:239], a[224:227], v[212:215]\n"
        "s_waitcnt lgkmcnt(4)\n"
        "v_exp_f32 v108, v108\n"
        "v_mfma_f32_16x16x32_bf16 v[212:215], a[240:243], a[220:223], v[212:215]\n"
        "v_exp_f32 v109, v109\n"
        "v_mfma_f32_16x16x32_bf16 v[216:219], a[244:247], a[220:223], v[216:219]\n"
        "ds_read_b128 a[220:223], v248 offset:15360\n"
        "v_cvt_pk_bf16_f32 v92, v108, v109\n"
        "v_mfma_f32_16x16x32_bf16 v[216:219], a[248:251], a[224:227], v[216:219]\n"
        "s_waitcnt lgkmcnt(4)\n"
        "ds_read_b128 a[224:227], v248 offset:12320\n"
        "v_mfma_f32_16x16x32_bf16 v[224:227], a[248:251], a[232:235], v[224:227]\n"
        "s_waitcnt lgkmcnt(3)\n"
        "v_exp_f32 v110, v110\n"
        "v_mfma_f32_16x16x32_bf16 v[224:227], a[244:247], a[228:231], v[224:227]\n"
        "v_exp_f32 v112, v112\n"
        "v_mfma_f32_16x16x32_bf16 v[220:223], a[240:243], a[228:231], v[220:223]\n"
        "ds_read_b128 a[228:231], v248 offset:15392\n"
        "v_mfma_f32_16x16x32_bf16 v[220:223], a[236:239], a[232:235], v[220:223]\n"
        "s_waitcnt lgkmcnt(3)\n"
        "ds_read_b128 a[232:235], v254 offset:12288\n"
        "v_mfma_f32_16x16x32_bf16 v[176:179], v[156:159], a[216:219], v[176:179]\n"
        "s_waitcnt lgkmcnt(3)\n"
        "v_exp_f32 v111, v111\n"
        "v_mfma_f32_16x16x32_bf16 v[176:179], v[160:163], a[220:223], v[176:179]\n"
        "v_exp_f32 v113, v113\n"
        "v_mfma_f32_16x16x32_bf16 v[196:199], v[164:167], a[220:223], v[196:199]\n"
        "ds_read_b128 a[220:223], v254 offset:15360\n"
        "v_cvt_pk_bf16_f32 v93, v110, v111\n"
        "v_mfma_f32_16x16x32_bf16 v[196:199], v[152:155], a[216:219], v[196:199]\n"
        "s_waitcnt lgkmcnt(3)\n"
        "ds_read_b128 a[216:219], v254 offset:12320\n"
        "v_cvt_pk_bf16_f32 v94, v112, v113\n"
        "v_mfma_f32_16x16x32_bf16 v[200:203], v[152:155], a[224:227], v[200:203]\n"
        "s_waitcnt lgkmcnt(3)\n"
        "v_exp_f32 v114, v114\n"
        "v_mfma_f32_16x16x32_bf16 v[200:203], v[164:167], a[228:231], v[200:203]\n"
        "v_exp_f32 v115, v115\n"
        "v_mfma_f32_16x16x32_bf16 v[180:183], v[160:163], a[228:231], v[180:183]\n"
        "s_waitcnt vmcnt(0)\n"
        "s_barrier\n"
        "ds_read_b128 a[228:231], v254 offset:15392\n"
        "v_cvt_pk_bf16_f32 v95, v114, v115\n"
        "v_mfma_f32_16x16x32_bf16 v[180:183], v[156:159], a[224:227], v[180:183]\n"
        "s_waitcnt lgkmcnt(3)\n"
        "ds_read_b128 a[224:227], v248 offset:12304\n"
        "v_mfma_f32_16x16x32_bf16 v[184:187], v[156:159], a[232:235], v[184:187]\n"
        "s_waitcnt lgkmcnt(3)\n"
        "v_mfma_f32_16x16x32_bf16 v[184:187], v[160:163], a[220:223], v[184:187]\n"
        "v_mfma_f32_16x16x32_bf16 v[204:207], v[164:167], a[220:223], v[204:207]\n"
        "ds_read_b128 a[220:223], v248 offset:15376\n"
        "v_mfma_f32_16x16x32_bf16 v[204:207], v[152:155], a[232:235], v[204:207]\n"
        "s_waitcnt lgkmcnt(3)\n"
        "ds_read_b128 a[232:235], v254 offset:12304\n"
        "v_mfma_f32_16x16x32_bf16 v[192:195], v[152:155], a[216:219], v[192:195]\n"
        "s_waitcnt lgkmcnt(3)\n"
        "ds_read_b128 a[236:239], v240 offset:0\n"
        "v_mfma_f32_16x16x32_bf16 v[192:195], v[164:167], a[228:231], v[192:195]\n"
        "ds_read_b128 a[240:243], v241 offset:0\n"
        "v_mfma_f32_16x16x32_bf16 v[172:175], v[160:163], a[228:231], v[172:175]\n"
        "ds_read_b128 a[228:231], v254 offset:15376\n"
        "v_mfma_f32_16x16x32_bf16 v[172:175], v[156:159], a[216:219], v[172:175]\n"
        "s_waitcnt lgkmcnt(5)\n"
        "ds_read_b128 a[244:247], v240 offset:128\n"
        "v_mfma_f32_16x16x32_bf16 v[212:215], v[156:159], a[224:227], v[212:215]\n"
        "s_waitcnt lgkmcnt(5)\n"
        "ds_read_b128 a[248:251], v241 offset:128\n"
        "v_mfma_f32_16x16x32_bf16 v[212:215], v[160:163], a[220:223], v[212:215]\n"
        "ds_read_b128 a[216:219], v240 offset:256\n"
        "v_mfma_f32_16x16x32_bf16 v[216:219], v[164:167], a[220:223], v[216:219]\n"
        "ds_read_b128 a[220:223], v241 offset:256\n"
        "v_mfma_f32_16x16x32_bf16 v[216:219], v[152:155], a[224:227], v[216:219]\n"
        "s_waitcnt lgkmcnt(7)\n"
        "v_mfma_f32_16x16x32_bf16 v[224:227], v[152:155], a[232:235], v[224:227]\n"
        "s_waitcnt lgkmcnt(4)\n"
        "v_mfma_f32_16x16x32_bf16 v[224:227], v[164:167], a[228:231], v[224:227]\n"
        "v_mfma_f32_16x16x32_bf16 v[220:223], v[160:163], a[228:231], v[220:223]\n"
        "v_mfma_f32_16x16x32_bf16 v[220:223], v[156:159], a[232:235], v[220:223]\n"
        "s_branch 10b\n"
        ".p2align 12\n"
        "51:\n"
        "s_branch 61b\n"
        "s_nop 0\n"
        "s_nop 0\n"
        "s_nop 0\n"
        "12:\n"
        "10:\n"
        "v_mfma_f32_16x16x32_bf16 v[112:115], a[72:75], a[236:239], 0\n"
        "v_mov_b64 v[124:125], 0\n"
        "v_cvt_pk_bf16_f32 v232, v176, v180\n"
        "v_mfma_f32_16x16x32_bf16 v[112:115], a[76:79], a[240:243], v[112:115]\n"
        "s_waitcnt lgkmcnt(3)\n"
        "s_add_u32 s41, s30, s40\n"
        "v_cvt_pk_bf16_f32 v233, v184, v172\n"
        "v_cvt_pk_bf16_f32 v234, v177, v181\n"
        "v_mfma_f32_16x16x32_bf16 v[112:115], a[80:83], a[244:247], v[112:115]\n"
        "s_waitcnt lgkmcnt(2)\n"
        "buffer_store_dwordx2 v[232:233], v250, s[16:19], s38 offen offset:0 sc1\n"
        "v_cvt_pk_bf16_f32 v235, v185, v173\n"
        "v_mfma_f32_16x16x32_bf16 v[112:115], a[84:87], a[248:251], v[112:115]\n"
        "s_waitcnt lgkmcnt(1)\n"
        "v_cvt_pk_bf16_f32 v232, v178, v182\n"
        "v_cvt_pk_bf16_f32 v233, v186, v174\n"
        "v_mfma_f32_16x16x32_bf16 v[112:115], a[88:91], a[216:219], v[112:115]\n"
        "s_waitcnt lgkmcnt(0)\n"
        "v_mfma_f32_16x16x32_bf16 v[112:115], a[92:95], a[220:223], v[112:115]\n"
        "s_nop 2\n"
        "v_smfmac_f32_16x16x64_bf16 v[124:127], a[252:255], v[88:95], v237\n"
        "s_nop 1\n"
        "v_mfma_f32_16x16x32_bf16 v[80:83], v[132:135], v[92:95], 0\n"
        "buffer_store_dwordx2 v[234:235], v250, s[16:19], s38 offen offset:384 sc1\n"
        "v_cvt_pk_bf16_f32 v152, v112, v113\n"
        "v_mfma_f32_16x16x32_bf16 v[80:83], v[128:131], v[88:91], v[80:83]\n"
        "v_cvt_pk_bf16_f32 v153, v114, v115\n"
        "v_cvt_pk_bf16_f32 v234, v179, v183\n"
        "v_mfma_f32_16x16x32_bf16 v[84:87], v[136:139], v[88:91], 0\n"
        "v_cvt_pk_bf16_f32 v235, v187, v175\n"
        "v_add_f32 v125, v124, v125\n"
        "v_mfma_f32_16x16x32_bf16 v[84:87], v[140:143], v[92:95], v[84:87]\n"
        "v_rcp_f32 v124, v125\n"
        "v_mfma_f32_16x16x32_bf16 v[96:99], v[148:151], v[92:95], 0\n"
        "buffer_store_dwordx2 v[232:233], v250, s[16:19], s38 offen offset:768 sc1\n"
        "v_mul_f32 v80, v124, v80\n"
        "v_mfma_f32_16x16x32_bf16 v[96:99], v[144:147], v[88:91], v[96:99]\n"
        "s_add_u32 s35, s47, 786432\n"
        "v_mul_f32 v81, v124, v81\n"
        "v_mul_f32 v82, v124, v82\n"
        "v_mfma_f32_16x16x32_bf16 v[116:119], a[116:119], a[220:223], 0\n"
        "s_sub_u32 s46, s35, 100663296\n"
        "v_mul_f32 v83, v124, v83\n"
        "v_cvt_pk_bf16_f32 v80, v80, v81\n"
        "v_mfma_f32_16x16x32_bf16 v[116:119], a[112:115], a[216:219], v[116:119]\n"
        "s_cmp_lt_u32 s46, 786432\n"
        "v_cvt_pk_bf16_f32 v81, v82, v83\n"
        "v_cvt_pk_bf16_f32 v232, v196, v200\n"
        "v_mfma_f32_16x16x32_bf16 v[116:119], a[108:111], a[248:251], v[116:119]\n"
        "buffer_store_dwordx2 v[234:235], v250, s[16:19], s38 offen offset:1152 sc1\n"
        "v_cvt_pk_bf16_f32 v233, v204, v192\n"
        "v_mfma_f32_16x16x32_bf16 v[116:119], a[104:107], a[244:247], v[116:119]\n"
        "s_cselect_b32 s35, s46, s35\n"
        "v_mul_f32 v84, v124, v84\n"
        "v_mul_f32 v85, v124, v85\n"
        "v_mfma_f32_16x16x32_bf16 v[116:119], a[100:103], a[240:243], v[116:119]\n"
        "v_mul_f32 v86, v124, v86\n"
        "v_mul_f32 v87, v124, v87\n"
        "v_mfma_f32_16x16x32_bf16 v[116:119], a[96:99], a[236:239], v[116:119]\n"
        "v_cvt_pk_bf16_f32 v82, v84, v85\n"
        "v_cvt_pk_bf16_f32 v83, v86, v87\n"
        "v_mfma_f32_16x16x32_bf16 v[120:123], a[120:123], a[236:239], 0\n"
        "ds_write_b128 v245, v[80:83] offset:768\n"
        "v_mfma_f32_16x16x32_bf16 v[120:123], a[124:127], a[240:243], v[120:123]\n"
        "buffer_store_dwordx2 v[232:233], v250, s[16:19], s39 offen offset:0 sc1\n"
        "v_mul_f32 v96, v124, v96\n"
        "v_mfma_f32_16x16x32_bf16 v[120:123], a[128:131], a[244:247], v[120:123]\n"
        "v_mul_f32 v97, v124, v97\n"
        "v_mul_f32 v98, v124, v98\n"
        "v_mfma_f32_16x16x32_bf16 v[120:123], a[132:135], a[248:251], v[120:123]\n"
        "v_mul_f32 v99, v124, v99\n"
        "v_cvt_pk_bf16_f32 v234, v197, v201\n"
        "v_mfma_f32_16x16x32_bf16 v[120:123], a[136:139], a[216:219], v[120:123]\n"
        "v_cvt_pk_bf16_f32 v235, v205, v193\n"
        "v_cvt_pk_bf16_f32 v154, v116, v117\n"
        "v_mfma_f32_16x16x32_bf16 v[120:123], a[140:143], a[220:223], v[120:123]\n"
        "buffer_store_dwordx2 v[234:235], v250, s[16:19], s39 offen offset:384 sc1\n"
        "v_cvt_pk_bf16_f32 v155, v118, v119\n"
        "v_mfma_f32_16x16x32_bf16 v[112:115], a[20:23], a[220:223], 0\n"
        "v_cvt_pk_bf16_f32 v96, v96, v97\n"
        "v_cvt_pk_bf16_f32 v97, v98, v99\n"
        "v_mfma_f32_16x16x32_bf16 v[112:115], a[16:19], a[216:219], v[112:115]\n"
        "ds_write_b64 v246, v[96:97] offset:768\n"
        "v_mfma_f32_16x16x32_bf16 v[112:115], a[12:15], a[248:251], v[112:115]\n"
        "v_max3_u32 v236, v236, v124, v125\n"
        "v_cvt_pk_bf16_f32 v232, v198, v202\n"
        "v_mfma_f32_16x16x32_bf16 v[112:115], a[8:11], a[244:247], v[112:115]\n"
        "v_cvt_pk_bf16_f32 v233, v206, v194\n"
        "v_cmp_lt_u32 vcc, s24, v236\n"
        "v_mfma_f32_16x16x32_bf16 v[112:115], a[4:7], a[240:243], v[112:115]\n"
        "buffer_store_dwordx2 v[232:233], v250, s[16:19], s39 offen offset:768 sc1\n"
        "v_cvt_pk_bf16_f32 v168, v120, v121\n"
        "v_mfma_f32_16x16x32_bf16 v[112:115], a[0:3], a[236:239], v[112:115]\n"
        "s_cmp_lg_u64 vcc, 0\n"
        "s_addc_u32 s25, s25, s25\n"
        "v_cvt_pk_bf16_f32 v169, v122, v123\n"
        "v_mfma_f32_16x16x32_bf16 v[116:119], a[24:27], a[236:239], 0\n"
        "v_cvt_pk_bf16_f32 v234, v199, v203\n"
        "v_cvt_pk_bf16_f32 v235, v207, v195\n"
        "v_mfma_f32_16x16x32_bf16 v[116:119], a[28:31], a[240:243], v[116:119]\n"
        "v_cvt_pk_bf16_f32 v232, v212, v220\n"
        "v_cvt_pk_bf16_f32 v156, v112, v113\n"
        "v_mfma_f32_16x16x32_bf16 v[116:119], a[32:35], a[244:247], v[116:119]\n"
        "buffer_store_dwordx2 v[234:235], v250, s[16:19], s39 offen offset:1152 sc1\n"
        "v_cvt_pk_bf16_f32 v157, v114, v115\n"
        "v_mfma_f32_16x16x32_bf16 v[116:119], a[36:39], a[248:251], v[116:119]\n"
        "v_cvt_pk_bf16_f32 v234, v213, v221\n"
        "v_xor_b32 v251, 0x2000, v251\n"
        "v_mfma_f32_16x16x32_bf16 v[116:119], a[40:43], a[216:219], v[116:119]\n"
        "v_xor_b32 v252, 0x2000, v252\n"
        "v_mfma_f32_16x16x32_bf16 v[116:119], a[44:47], a[220:223], v[116:119]\n"
        "s_nop 2\n"
        "v_mfma_f32_16x16x32_bf16 v[120:123], a[68:71], a[220:223], 0\n"
        "s_nop 2\n"
        "v_mfma_f32_16x16x32_bf16 v[120:123], a[64:67], a[216:219], v[120:123]\n"
        "buffer_store_dword v232, v238, s[16:19], s38 offen offset:0 sc1\n"
        "v_mfma_f32_16x16x32_bf16 v[120:123], a[60:63], a[248:251], v[120:123]\n"
        "ds_read_b128 a[224:227], v240 offset:1536\n"
        "v_cvt_pk_bf16_f32 v158, v116, v117\n"
        "v_mfma_f32_16x16x32_bf16 v[120:123], a[56:59], a[244:247], v[120:123]\n"
        "ds_read_b128 a[228:231], v241 offset:1536\n"
        "v_cvt_pk_bf16_f32 v159, v118, v119\n"
        "v_mfma_f32_16x16x32_bf16 v[120:123], a[52:55], a[240:243], v[120:123]\n"
        "ds_read_b128 a[232:235], v240 offset:1664\n"
        "v_cvt_pk_bf16_f32 v232, v214, v222\n"
        "v_mfma_f32_16x16x32_bf16 v[120:123], a[48:51], a[236:239], v[120:123]\n"
        "buffer_store_dword v234, v238, s[16:19], s38 offen offset:384 sc1\n"
        "v_mfma_f32_16x16x32_bf16 v[112:115], a[236:239], a[144:147], 0\n"
        "v_add_u32 v247, s45, v247\n"
        "v_cvt_pk_bf16_f32 v234, v215, v223\n"
        "v_mfma_f32_16x16x32_bf16 v[112:115], a[240:243], a[148:151], v[112:115]\n"
        "s_nop 2\n"
        "v_mfma_f32_16x16x32_bf16 v[112:115], a[244:247], a[152:155], v[112:115]\n"
        "v_cvt_pk_bf16_f32 v160, v120, v121\n"
        "v_cvt_pk_bf16_f32 v161, v122, v123\n"
        "v_mfma_f32_16x16x32_bf16 v[112:115], a[248:251], a[156:159], v[112:115]\n"
        "v_cvt_pk_bf16_f32 v166, v120, v121\n"
        "v_cvt_pk_bf16_f32 v167, v122, v123\n"
        "v_mfma_f32_16x16x32_bf16 v[112:115], a[216:219], a[160:163], v[112:115]\n"
        "buffer_store_dword v232, v238, s[16:19], s38 offen offset:768 sc1\n"
        "v_mfma_f32_16x16x32_bf16 v[112:115], a[220:223], a[164:167], v[112:115]\n"
        "s_nop 2\n"
        "v_mfma_f32_16x16x32_bf16 v[116:119], a[220:223], a[188:191], 0\n"
        "v_cvt_pk_bf16_f32 v232, v216, v224\n"
        "s_nop 0\n"
        "v_mfma_f32_16x16x32_bf16 v[116:119], a[216:219], a[184:187], v[116:119]\n"
        "s_nop 0\n"
        "v_cvt_pk_bf16_f32 v128, v112, v113\n"
        "v_cvt_pk_bf16_f32 v129, v114, v115\n"
        "v_mfma_f32_16x16x32_bf16 v[116:119], a[248:251], a[180:183], v[116:119]\n"
        "buffer_store_dword v234, v238, s[16:19], s38 offen offset:1152 sc1\n"
        "v_mfma_f32_16x16x32_bf16 v[116:119], a[244:247], a[176:179], v[116:119]\n"
        "s_nop 0\n"
        "v_cvt_pk_bf16_f32 v234, v217, v225\n"
        "v_mfma_f32_16x16x32_bf16 v[116:119], a[240:243], a[172:175], v[116:119]\n"
        "v_mfma_f32_16x16x32_bf16 v[116:119], a[236:239], a[168:171], v[116:119]\n"
        "s_nop 2\n"
        "v_mfma_f32_16x16x32_bf16 v[120:123], a[236:239], a[192:195], 0\n"
        "ds_read_b128 a[236:239], v241 offset:1664\n"
        "s_nop 0\n"
        "v_mfma_f32_16x16x32_bf16 v[120:123], a[240:243], a[196:199], v[120:123]\n"
        "buffer_store_dword v232, v238, s[16:19], s39 offen offset:0 sc1\n"
        "v_mfma_f32_16x16x32_bf16 v[120:123], a[244:247], a[200:203], v[120:123]\n"
        "ds_read_b128 a[240:243], v240 offset:1792\n"
        "ds_read_b128 a[244:247], v241 offset:1792\n"
        "v_mfma_f32_16x16x32_bf16 v[120:123], a[248:251], a[204:207], v[120:123]\n"
        "v_cvt_pk_bf16_f32 v136, v116, v117\n"
        "v_cvt_pk_bf16_f32 v137, v118, v119\n"
        "v_mfma_f32_16x16x32_bf16 v[120:123], a[216:219], a[208:211], v[120:123]\n"
        "v_cvt_pk_bf16_f32 v232, v218, v226\n"
        "v_mfma_f32_16x16x32_bf16 v[120:123], a[220:223], a[212:215], v[120:123]\n"
        "s_waitcnt lgkmcnt(5)\n"
        "buffer_store_dword v234, v238, s[16:19], s39 offen offset:384 sc1\n"
        "v_mfma_f32_16x16x32_bf16 v[112:115], a[72:75], a[224:227], 0\n"
        "s_waitcnt lgkmcnt(4)\n"
        "s_nop 1\n"
        "v_mfma_f32_16x16x32_bf16 v[112:115], a[76:79], a[228:231], v[112:115]\n"
        "s_waitcnt lgkmcnt(3)\n"
        "v_cvt_pk_bf16_f32 v234, v219, v227\n"
        "s_nop 0\n"
        "v_mfma_f32_16x16x32_bf16 v[112:115], a[80:83], a[232:235], v[112:115]\n"
        "s_waitcnt lgkmcnt(2)\n"
        "v_cvt_pk_bf16_f32 v144, v120, v121\n"
        "v_cvt_pk_bf16_f32 v145, v122, v123\n"
        "v_mfma_f32_16x16x32_bf16 v[112:115], a[84:87], a[236:239], v[112:115]\n"
        "s_waitcnt lgkmcnt(1)\n"
        "v_mfma_f32_16x16x32_bf16 v[112:115], a[88:91], a[240:243], v[112:115]\n"
        "s_waitcnt lgkmcnt(0)\n"
        "buffer_store_dword v232, v238, s[16:19], s39 offen offset:768 sc1\n"
        "v_mfma_f32_16x16x32_bf16 v[112:115], a[92:95], a[244:247], v[112:115]\n"
        "s_nop 2\n"
        "v_mfma_f32_16x16x32_bf16 v[116:119], a[116:119], a[244:247], 0\n"
        "s_nop 2\n"
        "v_mfma_f32_16x16x32_bf16 v[116:119], a[112:115], a[240:243], v[116:119]\n"
        "v_cvt_pk_bf16_f32 v172, v112, v113\n"
        "v_cvt_pk_bf16_f32 v173, v114, v115\n"
        "v_mfma_f32_16x16x32_bf16 v[116:119], a[108:111], a[236:239], v[116:119]\n"
        "buffer_store_dword v234, v238, s[16:19], s39 offen offset:1152 sc1\n"
        "v_mfma_f32_16x16x32_bf16 v[116:119], a[104:107], a[232:235], v[116:119]\n"
        "v_mfma_f32_16x16x32_bf16 v[116:119], a[100:103], a[228:231], v[116:119]\n"
        "v_mfma_f32_16x16x32_bf16 v[116:119], a[96:99], a[224:227], v[116:119]\n"
        "s_mov_b32 m0, s48\n"
        "s_nop 1\n"
        "v_mfma_f32_16x16x32_bf16 v[120:123], a[120:123], a[224:227], 0\n"
        "ds_read_b128 v[162:165], v251 offset:0\n"
        "s_nop 0\n"
        "v_mfma_f32_16x16x32_bf16 v[120:123], a[124:127], a[228:231], v[120:123]\n"
        "buffer_load_dwordx4 v249, s[12:15], s36 offen offset:0 lds\n"
        "v_mfma_f32_16x16x32_bf16 v[120:123], a[128:131], a[232:235], v[120:123]\n"
        "v_cvt_pk_bf16_f32 v174, v116, v117\n"
        "v_cvt_pk_bf16_f32 v175, v118, v119\n"
        "v_mfma_f32_16x16x32_bf16 v[120:123], a[132:135], a[236:239], v[120:123]\n"
        "v_mfma_f32_16x16x32_bf16 v[120:123], a[136:139], a[240:243], v[120:123]\n"
        "v_mfma_f32_16x16x32_bf16 v[120:123], a[140:143], a[244:247], v[120:123]\n"
        "s_nop 2\n"
        "v_mfma_f32_16x16x32_bf16 v[112:115], a[20:23], a[244:247], 0\n"
        "s_nop 2\n"
        "v_mfma_f32_16x16x32_bf16 v[112:115], a[16:19], a[240:243], v[112:115]\n"
        "v_cvt_pk_bf16_f32 v190, v120, v121\n"
        "v_cvt_pk_bf16_f32 v191, v122, v123\n"
        "v_mfma_f32_16x16x32_bf16 v[112:115], a[12:15], a[236:239], v[112:115]\n"
        "v_mfma_f32_16x16x32_bf16 v[112:115], a[8:11], a[232:235], v[112:115]\n"
        "v_mfma_f32_16x16x32_bf16 v[112:115], a[4:7], a[228:231], v[112:115]\n"
        "ds_read_b128 v[182:185], v251 offset:512\n"
        "v_mfma_f32_16x16x32_bf16 v[112:115], a[0:3], a[224:227], v[112:115]\n"
        "s_nop 2\n"
        "v_mfma_f32_16x16x32_bf16 v[116:119], a[24:27], a[224:227], 0\n"
        "s_nop 2\n"
        "v_mfma_f32_16x16x32_bf16 v[116:119], a[28:31], a[228:231], v[116:119]\n"
        "v_cvt_pk_bf16_f32 v176, v112, v113\n"
        "v_cvt_pk_bf16_f32 v177, v114, v115\n"
        "v_mfma_f32_16x16x32_bf16 v[116:119], a[32:35], a[232:235], v[116:119]\n"
        "v_mfma_f32_16x16x32_bf16 v[116:119], a[36:39], a[236:239], v[116:119]\n"
        "buffer_load_dwordx4 v253, s[12:15], s36 offen offset:1024 lds\n"
        "v_mfma_f32_16x16x32_bf16 v[116:119], a[40:43], a[240:243], v[116:119]\n"
        "v_mfma_f32_16x16x32_bf16 v[116:119], a[44:47], a[244:247], v[116:119]\n"
        "s_nop 2\n"
        "v_mfma_f32_16x16x32_bf16 v[120:123], a[68:71], a[244:247], 0\n"
        "s_nop 2\n"
        "v_mfma_f32_16x16x32_bf16 v[120:123], a[64:67], a[240:243], v[120:123]\n"
        "v_cvt_pk_bf16_f32 v178, v116, v117\n"
        "v_cvt_pk_bf16_f32 v179, v118, v119\n"
        "v_mfma_f32_16x16x32_bf16 v[120:123], a[60:63], a[236:239], v[120:123]\n"
        "v_mfma_f32_16x16x32_bf16 v[120:123], a[56:59], a[232:235], v[120:123]\n"
        "v_mfma_f32_16x16x32_bf16 v[120:123], a[52:55], a[228:231], v[120:123]\n"
        "v_mfma_f32_16x16x32_bf16 v[120:123], a[48:51], a[224:227], v[120:123]\n"
        "s_nop 2\n"
        "v_mfma_f32_16x16x32_bf16 v[112:115], a[224:227], a[144:147], 0\n"
        "s_nop 2\n"
        "v_mfma_f32_16x16x32_bf16 v[112:115], a[228:231], a[148:151], v[112:115]\n"
        "v_cvt_pk_bf16_f32 v180, v120, v121\n"
        "v_cvt_pk_bf16_f32 v181, v122, v123\n"
        "v_mfma_f32_16x16x32_bf16 v[112:115], a[232:235], a[152:155], v[112:115]\n"
        "v_cvt_pk_bf16_f32 v186, v120, v121\n"
        "v_cvt_pk_bf16_f32 v187, v122, v123\n"
        "v_mfma_f32_16x16x32_bf16 v[112:115], a[236:239], a[156:159], v[112:115]\n"
        "s_add_u32 m0, s41, 0\n"
        "v_mfma_f32_16x16x32_bf16 v[112:115], a[240:243], a[160:163], v[112:115]\n"
        "ds_read_b128 a[248:251], v240 offset:12288\n"
        "v_mfma_f32_16x16x32_bf16 v[112:115], a[244:247], a[164:167], v[112:115]\n"
        "buffer_load_dwordx4 v242, s[8:11], s47 offen lds\n"
        "v_mfma_f32_16x16x32_bf16 v[96:99], v[152:155], v[156:159], v[0:3]\n"
        "s_waitcnt lgkmcnt(2)\n"
        "ds_read_b128 a[216:219], v241 offset:12288\n"
        "s_nop 0\n"
        "v_mfma_f32_16x16x32_bf16 v[96:99], v[168:171], v[160:163], v[96:99]\n"
        "ds_read_b128 a[220:223], v240 offset:12416\n"
        "s_sub_u32 s31, s30, s28\n"
        "s_nop 0\n"
        "v_mfma_f32_16x16x32_bf16 v[100:103], v[172:175], v[156:159], v[4:7]\n"
        "s_sub_u32 s45, s28, s29\n"
        "v_cvt_pk_bf16_f32 v130, v112, v113\n"
        "v_cvt_pk_bf16_f32 v131, v114, v115\n"
        "v_mfma_f32_16x16x32_bf16 v[100:103], v[188:191], v[164:167], v[100:103]\n"
        "s_add_u32 s28, s28, s31\n"
        "ds_read_b128 v[162:165], v252 offset:0\n"
        "v_mfma_f32_16x16x32_bf16 v[116:119], a[244:247], a[188:191], 0\n"
        "s_mov_b32 s30, s29\n"
        "v_exp_f32 v96, v96\n"
        "v_mfma_f32_16x16x32_bf16 v[116:119], a[240:243], a[184:187], v[116:119]\n"
        "s_add_u32 s29, s29, s45\n"
        "v_exp_f32 v97, v97\n"
        "v_mfma_f32_16x16x32_bf16 v[116:119], a[236:239], a[180:183], v[116:119]\n"
        "v_exp_f32 v98, v98\n"
        "v_mfma_f32_16x16x32_bf16 v[116:119], a[232:235], a[176:179], v[116:119]\n"
        "v_exp_f32 v100, v100\n"
        "v_mfma_f32_16x16x32_bf16 v[116:119], a[228:231], a[172:175], v[116:119]\n"
        "v_exp_f32 v99, v99\n"
        "v_mfma_f32_16x16x32_bf16 v[116:119], a[224:227], a[168:171], v[116:119]\n"
        "v_exp_f32 v101, v101\n"
        "v_mfma_f32_16x16x32_bf16 v[104:107], v[152:155], v[176:179], v[16:19]\n"
        "s_waitcnt lgkmcnt(4)\n"
        "v_exp_f32 v102, v102\n"
        "v_mfma_f32_16x16x32_bf16 v[104:107], v[168:171], v[180:183], v[104:107]\n"
        "s_add_u32 m0, s41, 1024\n"
        "v_cvt_pk_bf16_f32 v64, v96, v97\n"
        "v_cvt_pk_bf16_f32 v65, v98, v99\n"
        "v_mfma_f32_16x16x32_bf16 v[108:111], v[172:175], v[176:179], v[20:23]\n"
        "v_cvt_pk_bf16_f32 v138, v116, v117\n"
        "v_cvt_pk_bf16_f32 v139, v118, v119\n"
        "v_mfma_f32_16x16x32_bf16 v[108:111], v[188:191], v[184:187], v[108:111]\n"
        "buffer_load_dwordx4 v243, s[8:11], s47 offen lds\n"
        "v_mfma_f32_16x16x32_bf16 v[120:123], a[224:227], a[192:195], 0\n"
        "ds_read_b128 a[224:227], v241 offset:12416\n"
        "v_cvt_pk_bf16_f32 v66, v100, v101\n"
        "v_mfma_f32_16x16x32_bf16 v[120:123], a[228:231], a[196:199], v[120:123]\n"
        "ds_read_b128 a[228:231], v240 offset:12544\n"
        "v_mfma_f32_16x16x32_bf16 v[120:123], a[232:235], a[200:203], v[120:123]\n"
        "ds_read_b128 a[232:235], v241 offset:12544\n"
        "v_mfma_f32_16x16x32_bf16 v[120:123], a[236:239], a[204:207], v[120:123]\n"
        "ds_read_b128 v[202:205], v251 offset:4096\n"
        "v_mfma_f32_16x16x32_bf16 v[120:123], a[240:243], a[208:211], v[120:123]\n"
        "ds_read_b128 v[182:185], v252 offset:512\n"
        "v_mfma_f32_16x16x32_bf16 v[120:123], a[244:247], a[212:215], v[120:123]\n"
        "s_waitcnt lgkmcnt(8)\n"
        "v_exp_f32 v103, v103\n"
        "v_mfma_f32_16x16x32_bf16 v[112:115], a[0:3], a[248:251], 0\n"
        "s_waitcnt lgkmcnt(7)\n"
        "v_cvt_pk_bf16_f32 v67, v102, v103\n"
        "s_nop 0\n"
        "v_mfma_f32_16x16x32_bf16 v[112:115], a[4:7], a[216:219], v[112:115]\n"
        "s_waitcnt lgkmcnt(6)\n"
        "v_exp_f32 v104, v104\n"
        "v_mfma_f32_16x16x32_bf16 v[112:115], a[8:11], a[220:223], v[112:115]\n"
        "s_waitcnt lgkmcnt(4)\n"
        "v_exp_f32 v105, v105\n"
        "v_mfma_f32_16x16x32_bf16 v[112:115], a[12:15], a[224:227], v[112:115]\n"
        "s_waitcnt lgkmcnt(3)\n"
        "v_exp_f32 v106, v106\n"
        "v_mfma_f32_16x16x32_bf16 v[112:115], a[16:19], a[228:231], v[112:115]\n"
        "s_waitcnt lgkmcnt(2)\n"
        "v_cvt_pk_bf16_f32 v72, v104, v105\n"
        "v_cvt_pk_bf16_f32 v146, v120, v121\n"
        "v_mfma_f32_16x16x32_bf16 v[112:115], a[20:23], a[232:235], v[112:115]\n"
        "s_add_u32 m0, s41, 2048\n"
        "v_exp_f32 v108, v108\n"
        "v_mfma_f32_16x16x32_bf16 v[116:119], a[44:47], a[232:235], 0\n"
        "v_cvt_pk_bf16_f32 v147, v122, v123\n"
        "v_add_u32 v245, s45, v245\n"
        "v_mfma_f32_16x16x32_bf16 v[116:119], a[40:43], a[228:231], v[116:119]\n"
        "buffer_load_dwordx4 v244, s[8:11], s47 offen lds\n"
        "v_mfma_f32_16x16x32_bf16 v[116:119], a[36:39], a[224:227], v[116:119]\n"
        "v_exp_f32 v107, v107\n"
        "v_mfma_f32_16x16x32_bf16 v[116:119], a[32:35], a[220:223], v[116:119]\n"
        "v_exp_f32 v109, v109\n"
        "v_mfma_f32_16x16x32_bf16 v[116:119], a[28:31], a[216:219], v[116:119]\n"
        "v_cvt_pk_bf16_f32 v73, v106, v107\n"
        "v_cvt_pk_bf16_f32 v74, v108, v109\n"
        "v_mfma_f32_16x16x32_bf16 v[116:119], a[24:27], a[248:251], v[116:119]\n"
        "v_exp_f32 v110, v110\n"
        "v_mfma_f32_16x16x32_bf16 v[120:123], a[48:51], a[248:251], 0\n"
        "v_exp_f32 v111, v111\n"
        "v_mfma_f32_16x16x32_bf16 v[120:123], a[52:55], a[216:219], v[120:123]\n"
        "v_cvt_pk_bf16_f32 v75, v110, v111\n"
        "v_cvt_pk_bf16_f32 v196, v112, v113\n"
        "v_mfma_f32_16x16x32_bf16 v[120:123], a[56:59], a[220:223], v[120:123]\n"
        "v_cvt_pk_bf16_f32 v197, v114, v115\n"
        "v_cvt_pk_bf16_f32 v198, v116, v117\n"
        "v_mfma_f32_16x16x32_bf16 v[120:123], a[60:63], a[224:227], v[120:123]\n"
        "v_cvt_pk_bf16_f32 v199, v118, v119\n"
        "v_add_u32 v246, s45, v246\n"
        "v_mfma_f32_16x16x32_bf16 v[120:123], a[64:67], a[228:231], v[120:123]\n"
        "v_mfma_f32_16x16x32_bf16 v[120:123], a[68:71], a[232:235], v[120:123]\n"
        "s_nop 2\n"
        "v_mfma_f32_16x16x32_bf16 v[112:115], a[92:95], a[232:235], 0\n"
        "s_nop 2\n"
        "v_mfma_f32_16x16x32_bf16 v[112:115], a[88:91], a[228:231], v[112:115]\n"
        "s_add_u32 m0, s41, 12288\n"
        "v_cvt_pk_bf16_f32 v200, v120, v121\n"
        "v_cvt_pk_bf16_f32 v201, v122, v123\n"
        "v_mfma_f32_16x16x32_bf16 v[112:115], a[84:87], a[224:227], v[112:115]\n"
        "v_cvt_pk_bf16_f32 v206, v120, v121\n"
        "v_cvt_pk_bf16_f32 v207, v122, v123\n"
        "v_mfma_f32_16x16x32_bf16 v[112:115], a[80:83], a[220:223], v[112:115]\n"
        "buffer_load_dwordx4 v242, s[8:11], s35 offen lds\n"
        "v_mfma_f32_16x16x32_bf16 v[112:115], a[76:79], a[216:219], v[112:115]\n"
        "v_mfma_f32_16x16x32_bf16 v[112:115], a[72:75], a[248:251], v[112:115]\n"
        "s_nop 2\n"
        "v_mfma_f32_16x16x32_bf16 v[96:99], v[152:155], v[196:199], v[32:35]\n"
        "s_waitcnt lgkmcnt(1)\n"
        "s_nop 1\n"
        "v_mfma_f32_16x16x32_bf16 v[96:99], v[168:171], v[200:203], v[96:99]\n"
        "s_nop 0\n"
        "v_cvt_pk_bf16_f32 v192, v112, v113\n"
        "v_cvt_pk_bf16_f32 v193, v114, v115\n"
        "v_mfma_f32_16x16x32_bf16 v[100:103], v[172:175], v[196:199], v[36:39]\n"
        "v_mfma_f32_16x16x32_bf16 v[100:103], v[188:191], v[204:207], v[100:103]\n"
        "ds_read_b128 v[202:205], v252 offset:4096\n"
        "s_cmp_eq_u32 s37, 100663296\n"
        "v_mfma_f32_16x16x32_bf16 v[116:119], a[96:99], a[248:251], 0\n"
        "s_cselect_b32 s46, 0, s37\n"
        "v_exp_f32 v96, v96\n"
        "v_mfma_f32_16x16x32_bf16 v[116:119], a[100:103], a[216:219], v[116:119]\n"
        "s_add_u32 s38, s46, s43\n"
        "v_exp_f32 v97, v97\n"
        "v_mfma_f32_16x16x32_bf16 v[116:119], a[104:107], a[220:223], v[116:119]\n"
        "s_add_u32 s39, s46, s44\n"
        "v_exp_f32 v98, v98\n"
        "v_mfma_f32_16x16x32_bf16 v[116:119], a[108:111], a[224:227], v[116:119]\n"
        "s_add_u32 s37, s37, 6291456\n"
        "v_exp_f32 v100, v100\n"
        "v_mfma_f32_16x16x32_bf16 v[116:119], a[112:115], a[228:231], v[116:119]\n"
        "v_exp_f32 v99, v99\n"
        "v_mfma_f32_16x16x32_bf16 v[116:119], a[116:119], a[232:235], v[116:119]\n"
        "s_add_u32 m0, s41, 13312\n"
        "v_exp_f32 v101, v101\n"
        "v_mfma_f32_16x16x32_bf16 v[120:123], a[140:143], a[232:235], 0\n"
        "v_exp_f32 v102, v102\n"
        "v_mfma_f32_16x16x32_bf16 v[120:123], a[136:139], a[228:231], v[120:123]\n"
        "buffer_load_dwordx4 v243, s[8:11], s35 offen lds\n"
        "v_mfma_f32_16x16x32_bf16 v[120:123], a[132:135], a[224:227], v[120:123]\n"
        "v_cvt_pk_bf16_f32 v80, v96, v97\n"
        "v_cvt_pk_bf16_f32 v194, v116, v117\n"
        "v_mfma_f32_16x16x32_bf16 v[120:123], a[128:131], a[220:223], v[120:123]\n"
        "v_cvt_pk_bf16_f32 v195, v118, v119\n"
        "v_cvt_pk_bf16_f32 v81, v98, v99\n"
        "v_mfma_f32_16x16x32_bf16 v[120:123], a[124:127], a[216:219], v[120:123]\n"
        "v_cvt_pk_bf16_f32 v82, v100, v101\n"
        "v_mfma_f32_16x16x32_bf16 v[120:123], a[120:123], a[248:251], v[120:123]\n"
        "v_exp_f32 v103, v103\n"
        "v_mfma_f32_16x16x32_bf16 v[112:115], a[248:251], a[144:147], 0\n"
        "v_cvt_pk_bf16_f32 v83, v102, v103\n"
        "s_nop 0\n"
        "v_mfma_f32_16x16x32_bf16 v[112:115], a[216:219], a[148:151], v[112:115]\n"
        "s_nop 2\n"
        "v_mfma_f32_16x16x32_bf16 v[112:115], a[220:223], a[152:155], v[112:115]\n"
        "ds_read_b128 a[236:239], v240 offset:13824\n"
        "v_cvt_pk_bf16_f32 v208, v120, v121\n"
        "v_mfma_f32_16x16x32_bf16 v[112:115], a[224:227], a[156:159], v[112:115]\n"
        "ds_read_b128 a[240:243], v241 offset:13824\n"
        "v_cvt_pk_bf16_f32 v209, v122, v123\n"
        "v_mfma_f32_16x16x32_bf16 v[112:115], a[228:231], a[160:163], v[112:115]\n"
        "ds_read_b128 a[244:247], v240 offset:13952\n"
        "v_mfma_f32_16x16x32_bf16 v[112:115], a[232:235], a[164:167], v[112:115]\n"
        "s_nop 2\n"
        "v_mfma_f32_16x16x32_bf16 v[104:107], v[192:195], v[156:159], v[8:11]\n"
        "s_nop 1\n"
        "v_mfma_f32_16x16x32_bf16 v[104:107], v[208:211], v[160:163], v[104:107]\n"
        "s_add_u32 m0, s41, 14336\n"
        "v_cvt_pk_bf16_f32 v132, v112, v113\n"
        "v_cvt_pk_bf16_f32 v133, v114, v115\n"
        "v_mfma_f32_16x16x32_bf16 v[116:119], a[232:235], a[188:191], 0\n"
        "v_mfma_f32_16x16x32_bf16 v[116:119], a[228:231], a[184:187], v[116:119]\n"
        "buffer_load_dwordx4 v244, s[8:11], s35 offen lds\n"
        "v_mfma_f32_16x16x32_bf16 v[116:119], a[224:227], a[180:183], v[116:119]\n"
        "v_mfma_f32_16x16x32_bf16 v[116:119], a[220:223], a[176:179], v[116:119]\n"
        "v_exp_f32 v104, v104\n"
        "v_mfma_f32_16x16x32_bf16 v[116:119], a[216:219], a[172:175], v[116:119]\n"
        "v_exp_f32 v105, v105\n"
        "v_mfma_f32_16x16x32_bf16 v[116:119], a[248:251], a[168:171], v[116:119]\n"
        "v_exp_f32 v106, v106\n"
        "v_mfma_f32_16x16x32_bf16 v[108:111], v[192:195], v[176:179], v[24:27]\n"
        "s_waitcnt lgkmcnt(4)\n"
        "v_exp_f32 v107, v107\n"
        "v_mfma_f32_16x16x32_bf16 v[108:111], v[208:211], v[180:183], v[108:111]\n"
        "v_cvt_pk_bf16_f32 v68, v104, v105\n"
        "v_cvt_pk_bf16_f32 v69, v106, v107\n"
        "v_mfma_f32_16x16x32_bf16 v[120:123], a[248:251], a[192:195], 0\n"
        "ds_read_b128 a[248:251], v241 offset:13952\n"
        "v_cvt_pk_bf16_f32 v140, v116, v117\n"
        "v_mfma_f32_16x16x32_bf16 v[120:123], a[216:219], a[196:199], v[120:123]\n"
        "ds_read_b128 a[216:219], v240 offset:14080\n"
        "v_cvt_pk_bf16_f32 v141, v118, v119\n"
        "v_mfma_f32_16x16x32_bf16 v[120:123], a[220:223], a[200:203], v[120:123]\n"
        "ds_read_b128 a[220:223], v241 offset:14080\n"
        "v_mfma_f32_16x16x32_bf16 v[120:123], a[224:227], a[204:207], v[120:123]\n"
        "v_exp_f32 v108, v108\n"
        "v_mfma_f32_16x16x32_bf16 v[120:123], a[228:231], a[208:211], v[120:123]\n"
        "v_exp_f32 v109, v109\n"
        "v_mfma_f32_16x16x32_bf16 v[120:123], a[232:235], a[212:215], v[120:123]\n"
        "v_exp_f32 v110, v110\n"
        "v_mfma_f32_16x16x32_bf16 v[96:99], v[192:195], v[196:199], v[40:43]\n"
        "s_waitcnt lgkmcnt(6)\n"
        "v_exp_f32 v111, v111\n"
        "v_mfma_f32_16x16x32_bf16 v[96:99], v[208:211], v[200:203], v[96:99]\n"
        "s_waitcnt lgkmcnt(5)\n"
        "v_cvt_pk_bf16_f32 v76, v108, v109\n"
        "v_cvt_pk_bf16_f32 v77, v110, v111\n"
        "v_mfma_f32_16x16x32_bf16 v[112:115], a[72:75], a[236:239], 0\n"
        "s_waitcnt lgkmcnt(4)\n"
        "s_nop 0\n"
        "v_cvt_pk_bf16_f32 v148, v120, v121\n"
        "v_mfma_f32_16x16x32_bf16 v[112:115], a[76:79], a[240:243], v[112:115]\n"
        "s_waitcnt lgkmcnt(3)\n"
        "v_cvt_pk_bf16_f32 v149, v122, v123\n"
        "v_mfma_f32_16x16x32_bf16 v[112:115], a[80:83], a[244:247], v[112:115]\n"
        "s_waitcnt lgkmcnt(2)\n"
        "v_exp_f32 v96, v96\n"
        "v_mfma_f32_16x16x32_bf16 v[112:115], a[84:87], a[248:251], v[112:115]\n"
        "s_waitcnt lgkmcnt(1)\n"
        "v_exp_f32 v97, v97\n"
        "v_mfma_f32_16x16x32_bf16 v[112:115], a[88:91], a[216:219], v[112:115]\n"
        "s_waitcnt lgkmcnt(0)\n"
        "v_exp_f32 v98, v98\n"
        "v_mfma_f32_16x16x32_bf16 v[112:115], a[92:95], a[220:223], v[112:115]\n"
        "v_exp_f32 v99, v99\n"
        "v_mfma_f32_16x16x32_bf16 v[116:119], a[116:119], a[220:223], 0\n"
        "v_cvt_pk_bf16_f32 v84, v96, v97\n"
        "v_cvt_pk_bf16_f32 v85, v98, v99\n"
        "v_mfma_f32_16x16x32_bf16 v[116:119], a[112:115], a[216:219], v[116:119]\n"
        "v_add_u32 v240, s31, v240\n"
        "v_add_u32 v241, s31, v241\n"
        "v_mfma_f32_16x16x32_bf16 v[116:119], a[108:111], a[248:251], v[116:119]\n"
        "v_cvt_pk_bf16_f32 v212, v112, v113\n"
        "v_cvt_pk_bf16_f32 v213, v114, v115\n"
        "v_mfma_f32_16x16x32_bf16 v[116:119], a[104:107], a[244:247], v[116:119]\n"
        "v_mfma_f32_16x16x32_bf16 v[116:119], a[100:103], a[240:243], v[116:119]\n"
        "v_mfma_f32_16x16x32_bf16 v[116:119], a[96:99], a[236:239], v[116:119]\n"
        "s_nop 2\n"
        "v_mfma_f32_16x16x32_bf16 v[120:123], a[120:123], a[236:239], 0\n"
        "s_nop 2\n"
        "v_mfma_f32_16x16x32_bf16 v[120:123], a[124:127], a[240:243], v[120:123]\n"
        "s_add_u32 s47, s47, 6291456\n"
        "v_cvt_pk_bf16_f32 v214, v116, v117\n"
        "v_cvt_pk_bf16_f32 v215, v118, v119\n"
        "v_mfma_f32_16x16x32_bf16 v[120:123], a[128:131], a[244:247], v[120:123]\n"
        "s_add_u32 s36, s36, 2097152\n"
        "v_mfma_f32_16x16x32_bf16 v[120:123], a[132:135], a[248:251], v[120:123]\n"
        "s_xor_b32 s48, s48, 8192\n"
        "v_mfma_f32_16x16x32_bf16 v[120:123], a[136:139], a[216:219], v[120:123]\n"
        "v_mfma_f32_16x16x32_bf16 v[120:123], a[140:143], a[220:223], v[120:123]\n"
        "s_nop 2\n"
        "v_mfma_f32_16x16x32_bf16 v[112:115], a[220:223], a[164:167], 0\n"
        "s_nop 2\n"
        "v_mfma_f32_16x16x32_bf16 v[112:115], a[216:219], a[160:163], v[112:115]\n"
        "v_cvt_pk_bf16_f32 v230, v120, v121\n"
        "v_cvt_pk_bf16_f32 v231, v122, v123\n"
        "v_mfma_f32_16x16x32_bf16 v[112:115], a[248:251], a[156:159], v[112:115]\n"
        "v_mfma_f32_16x16x32_bf16 v[112:115], a[244:247], a[152:155], v[112:115]\n"
        "v_mfma_f32_16x16x32_bf16 v[112:115], a[240:243], a[148:151], v[112:115]\n"
        "v_mfma_f32_16x16x32_bf16 v[112:115], a[236:239], a[144:147], v[112:115]\n"
        "s_barrier\n"
        "s_nop 2\n"
        "v_mfma_f32_16x16x32_bf16 v[100:103], v[212:215], v[156:159], v[12:15]\n"
        "s_nop 2\n"
        "v_mfma_f32_16x16x32_bf16 v[100:103], v[228:231], v[164:167], v[100:103]\n"
        "v_cvt_pk_bf16_f32 v134, v112, v113\n"
        "v_cvt_pk_bf16_f32 v135, v114, v115\n"
        "v_mfma_f32_16x16x32_bf16 v[116:119], a[236:239], a[168:171], 0\n"
        "s_sub_u32 s27, s27, 1\n"
        "v_mfma_f32_16x16x32_bf16 v[116:119], a[240:243], a[172:175], v[116:119]\n"
        "s_cmp_lg_u32 s27, 0\n"
        "v_mfma_f32_16x16x32_bf16 v[116:119], a[244:247], a[176:179], v[116:119]\n"
        "s_cbranch_scc0 19f\n"
        "v_mfma_f32_16x16x32_bf16 v[116:119], a[248:251], a[180:183], v[116:119]\n"
        "v_mfma_f32_16x16x32_bf16 v[116:119], a[216:219], a[184:187], v[116:119]\n"
        "s_nop 0\n"
        "v_exp_f32 v100, v100\n"
        "v_mfma_f32_16x16x32_bf16 v[116:119], a[220:223], a[188:191], v[116:119]\n"
        "v_exp_f32 v101, v101\n"
        "v_mfma_f32_16x16x32_bf16 v[104:107], v[212:215], v[176:179], v[28:31]\n"
        "v_exp_f32 v102, v102\n"
        "v_mfma_f32_16x16x32_bf16 v[104:107], v[228:231], v[184:187], v[104:107]\n"
        "v_exp_f32 v103, v103\n"
        "v_mfma_f32_16x16x32_bf16 v[120:123], a[220:223], a[212:215], 0\n"
        "v_cvt_pk_bf16_f32 v70, v100, v101\n"
        "v_cvt_pk_bf16_f32 v71, v102, v103\n"
        "v_mfma_f32_16x16x32_bf16 v[120:123], a[216:219], a[208:211], v[120:123]\n"
        "v_cvt_pk_bf16_f32 v142, v116, v117\n"
        "v_cvt_pk_bf16_f32 v143, v118, v119\n"
        "v_mfma_f32_16x16x32_bf16 v[120:123], a[248:251], a[204:207], v[120:123]\n"
        "v_mov_b64 v[96:97], 0\n"
        "v_mfma_f32_16x16x32_bf16 v[120:123], a[244:247], a[200:203], v[120:123]\n"
        "v_exp_f32 v104, v104\n"
        "v_mfma_f32_16x16x32_bf16 v[120:123], a[240:243], a[196:199], v[120:123]\n"
        "v_exp_f32 v105, v105\n"
        "v_mfma_f32_16x16x32_bf16 v[120:123], a[236:239], a[192:195], v[120:123]\n"
        "v_exp_f32 v106, v106\n"
        "v_mfma_f32_16x16x32_bf16 v[108:111], v[212:215], v[196:199], v[44:47]\n"
        "v_exp_f32 v107, v107\n"
        "v_mfma_f32_16x16x32_bf16 v[108:111], v[228:231], v[204:207], v[108:111]\n"
        "v_cvt_pk_bf16_f32 v78, v104, v105\n"
        "v_cvt_pk_bf16_f32 v79, v106, v107\n"
        "v_mfma_f32_16x16x32_bf16 v[112:115], a[0:3], a[236:239], 0\n"
        "s_nop 0\n"
        "v_cvt_pk_bf16_f32 v150, v120, v121\n"
        "v_cvt_pk_bf16_f32 v151, v122, v123\n"
        "v_mfma_f32_16x16x32_bf16 v[112:115], a[4:7], a[240:243], v[112:115]\n"
        "v_mfma_f32_16x16x32_bf16 v[112:115], a[8:11], a[244:247], v[112:115]\n"
        "v_exp_f32 v108, v108\n"
        "v_mfma_f32_16x16x32_bf16 v[112:115], a[12:15], a[248:251], v[112:115]\n"
        "v_exp_f32 v109, v109\n"
        "v_mfma_f32_16x16x32_bf16 v[112:115], a[16:19], a[216:219], v[112:115]\n"
        "v_exp_f32 v110, v110\n"
        "v_mfma_f32_16x16x32_bf16 v[112:115], a[20:23], a[220:223], v[112:115]\n"
        "v_exp_f32 v111, v111\n"
        "v_smfmac_f32_16x16x64_bf16 v[96:99], a[252:255], v[64:71], v237\n"
        "v_cvt_pk_bf16_f32 v86, v108, v109\n"
        "v_mfma_f32_16x16x32_bf16 v[100:103], v[132:135], v[68:71], 0\n"
        "v_mov_b64 v[124:125], 0\n"
        "v_cvt_pk_bf16_f32 v87, v110, v111\n"
        "v_mfma_f32_16x16x32_bf16 v[100:103], v[128:131], v[64:67], v[100:103]\n"
        "s_nop 0\n"
        "v_cvt_pk_bf16_f32 v216, v112, v113\n"
        "v_cvt_pk_bf16_f32 v217, v114, v115\n"
        "v_mfma_f32_16x16x32_bf16 v[104:107], v[136:139], v[64:67], 0\n"
        ".long 0xbec200ff\n"
        "47:\n"
        "s_branch 61b\n"
        "ds_read_b128 v[222:225], v251 offset:4608\n"
        "v_add_f32 v97, v96, v97\n"
        "v_mfma_f32_16x16x32_bf16 v[104:107], v[140:143], v[68:71], v[104:107]\n"
        "v_rcp_f32 v96, v97\n"
        "v_mfma_f32_16x16x32_bf16 v[108:111], v[148:151], v[68:71], 0\n"
        "v_mul_f32 v100, v96, v100\n"
        "v_mul_f32 v101, v96, v101\n"
        "v_mfma_f32_16x16x32_bf16 v[108:111], v[144:147], v[64:67], v[108:111]\n"
        "v_mul_f32 v102, v96, v102\n"
        "v_mul_f32 v103, v96, v103\n"
        "v_mfma_f32_16x16x32_bf16 v[116:119], a[44:47], a[220:223], 0\n"
        "v_cvt_pk_bf16_f32 v100, v100, v101\n"
        "v_cvt_pk_bf16_f32 v101, v102, v103\n"
        "v_mfma_f32_16x16x32_bf16 v[116:119], a[40:43], a[216:219], v[116:119]\n"
        "v_mul_f32 v104, v96, v104\n"
        "v_mul_f32 v105, v96, v105\n"
        "v_mfma_f32_16x16x32_bf16 v[116:119], a[36:39], a[248:251], v[116:119]\n"
        "v_mul_f32 v106, v96, v106\n"
        "v_mul_f32 v107, v96, v107\n"
        "v_mfma_f32_16x16x32_bf16 v[116:119], a[32:35], a[244:247], v[116:119]\n"
        "v_cvt_pk_bf16_f32 v102, v104, v105\n"
        "v_cvt_pk_bf16_f32 v103, v106, v107\n"
        "v_mfma_f32_16x16x32_bf16 v[116:119], a[28:31], a[240:243], v[116:119]\n"
        "ds_write_b128 v245, v[100:103] offset:0\n"
        "v_mfma_f32_16x16x32_bf16 v[116:119], a[24:27], a[236:239], v[116:119]\n"
        "v_mul_f32 v108, v96, v108\n"
        "v_mul_f32 v109, v96, v109\n"
        "v_smfmac_f32_16x16x64_bf16 v[124:127], a[252:255], v[72:79], v237\n"
        "v_mul_f32 v110, v96, v110\n"
        "v_mfma_f32_16x16x32_bf16 v[64:67], v[132:135], v[76:79], 0\n"
        "v_mul_f32 v111, v96, v111\n"
        "v_cvt_pk_bf16_f32 v108, v108, v109\n"
        "v_mfma_f32_16x16x32_bf16 v[64:67], v[128:131], v[72:75], v[64:67]\n"
        "ds_read_b128 v[156:159], v247 offset:0\n"
        "v_cvt_pk_bf16_f32 v218, v116, v117\n"
        "v_mfma_f32_16x16x32_bf16 v[68:71], v[136:139], v[72:75], 0\n"
        "ds_read_b128 v[160:163], v247 offset:4096\n"
        "v_cvt_pk_bf16_f32 v219, v118, v119\n"
        "v_mfma_f32_16x16x32_bf16 v[68:71], v[140:143], v[76:79], v[68:71]\n"
        "ds_read_b128 v[164:167], v247 offset:4352\n"
        "v_cvt_pk_bf16_f32 v109, v110, v111\n"
        "v_mfma_f32_16x16x32_bf16 v[120:123], v[148:151], v[76:79], 0\n"
        "ds_write_b64 v246, v[108:109] offset:0\n"
        "v_mfma_f32_16x16x32_bf16 v[120:123], v[144:147], v[72:75], v[120:123]\n"
        "v_max_u32 v236, v96, v97\n"
        "v_mov_b64 v[116:117], 0\n"
        "v_mfma_f32_16x16x32_bf16 v[112:115], a[48:51], a[236:239], 0\n"
        "v_add_f32 v125, v124, v125\n"
        "v_mfma_f32_16x16x32_bf16 v[112:115], a[52:55], a[240:243], v[112:115]\n"
        "v_rcp_f32 v124, v125\n"
        "v_mfma_f32_16x16x32_bf16 v[112:115], a[56:59], a[244:247], v[112:115]\n"
        "v_mul_f32 v64, v124, v64\n"
        "v_mul_f32 v65, v124, v65\n"
        "v_mfma_f32_16x16x32_bf16 v[112:115], a[60:63], a[248:251], v[112:115]\n"
        "v_mul_f32 v66, v124, v66\n"
        "v_mul_f32 v67, v124, v67\n"
        "v_mfma_f32_16x16x32_bf16 v[112:115], a[64:67], a[216:219], v[112:115]\n"
        "v_cvt_pk_bf16_f32 v64, v64, v65\n"
        "v_cvt_pk_bf16_f32 v65, v66, v67\n"
        "v_mfma_f32_16x16x32_bf16 v[112:115], a[68:71], a[220:223], v[112:115]\n"
        "ds_read_b128 a[216:219], v248 offset:0\n"
        "v_mul_f32 v68, v124, v68\n"
        "v_smfmac_f32_16x16x64_bf16 v[116:119], a[252:255], v[80:87], v237\n"
        "ds_read_b128 a[220:223], v248 offset:3072\n"
        "v_mfma_f32_16x16x32_bf16 v[72:75], v[132:135], v[84:87], 0\n"
        "v_mul_f32 v69, v124, v69\n"
        "v_mul_f32 v70, v124, v70\n"
        "v_mfma_f32_16x16x32_bf16 v[72:75], v[128:131], v[80:83], v[72:75]\n"
        "v_cvt_pk_bf16_f32 v220, v112, v113\n"
        "v_cvt_pk_bf16_f32 v221, v114, v115\n"
        "v_mfma_f32_16x16x32_bf16 v[76:79], v[136:139], v[80:83], 0\n"
        "ds_read_b128 a[224:227], v248 offset:32\n"
        "v_cvt_pk_bf16_f32 v226, v112, v113\n"
        "v_mfma_f32_16x16x32_bf16 v[76:79], v[140:143], v[84:87], v[76:79]\n"
        "ds_read_b128 a[228:231], v248 offset:3104\n"
        "v_cvt_pk_bf16_f32 v227, v114, v115\n"
        "v_mfma_f32_16x16x32_bf16 v[96:99], v[148:151], v[84:87], 0\n"
        "v_mul_f32 v71, v124, v71\n"
        "v_cvt_pk_bf16_f32 v66, v68, v69\n"
        "v_mfma_f32_16x16x32_bf16 v[96:99], v[144:147], v[80:83], v[96:99]\n"
        "v_cvt_pk_bf16_f32 v67, v70, v71\n"
        "v_mul_f32 v120, v124, v120\n"
        "v_mfma_f32_16x16x32_bf16 v[100:103], v[152:155], v[216:219], v[48:51]\n"
        "s_waitcnt lgkmcnt(9)\n"
        "ds_read_b128 v[152:155], v247 offset:256\n"
        "v_mul_f32 v121, v124, v121\n"
        "v_mfma_f32_16x16x32_bf16 v[100:103], v[168:171], v[220:223], v[100:103]\n"
        "ds_write_b128 v245, v[64:67] offset:256\n"
        "v_mfma_f32_16x16x32_bf16 v[104:107], v[172:175], v[216:219], v[52:55]\n"
        "v_mul_f32 v122, v124, v122\n"
        "v_mul_f32 v123, v124, v123\n"
        "v_mfma_f32_16x16x32_bf16 v[104:107], v[188:191], v[224:227], v[104:107]\n"
        "s_waitcnt lgkmcnt(5)\n"
        "v_cvt_pk_bf16_f32 v120, v120, v121\n"
        "v_cvt_pk_bf16_f32 v121, v122, v123\n"
        "v_mfma_f32_16x16x32_bf16 v[176:179], v[156:159], a[216:219], 0\n"
        "s_waitcnt lgkmcnt(4)\n"
        "ds_write_b64 v246, v[120:121] offset:256\n"
        "v_mfma_f32_16x16x32_bf16 v[176:179], v[160:163], a[220:223], v[176:179]\n"
        "ds_read_b128 v[222:225], v252 offset:4608\n"
        "v_max3_u32 v236, v236, v124, v125\n"
        "v_mfma_f32_16x16x32_bf16 v[196:199], v[164:167], a[220:223], 0\n"
        "s_waitcnt lgkmcnt(3)\n"
        "ds_read_b128 a[232:235], v254 offset:0\n"
        "v_add_f32 v117, v116, v117\n"
        "v_mfma_f32_16x16x32_bf16 v[196:199], v[152:155], a[216:219], v[196:199]\n"
        "ds_read_b128 a[220:223], v254 offset:3072\n"
        "v_mfma_f32_16x16x32_bf16 v[200:203], v[152:155], a[224:227], 0\n"
        "ds_read_b128 a[216:219], v254 offset:32\n"
        "v_mfma_f32_16x16x32_bf16 v[200:203], v[164:167], a[228:231], v[200:203]\n"
        "v_rcp_f32 v116, v117\n"
        "v_mfma_f32_16x16x32_bf16 v[180:183], v[160:163], a[228:231], 0\n"
        "ds_read_b128 a[228:231], v254 offset:3104\n"
        "v_mul_f32 v72, v116, v72\n"
        "v_mfma_f32_16x16x32_bf16 v[180:183], v[156:159], a[224:227], v[180:183]\n"
        "ds_read_b128 a[236:239], v247 offset:8192\n"
        "v_mul_f32 v73, v116, v73\n"
        "v_mfma_f32_16x16x32_bf16 v[108:111], v[192:195], v[216:219], v[56:59]\n"
        "s_waitcnt lgkmcnt(5)\n"
        "ds_read_b128 a[224:227], v248 offset:16\n"
        "v_mul_f32 v74, v116, v74\n"
        "v_mfma_f32_16x16x32_bf16 v[108:111], v[208:211], v[220:223], v[108:111]\n"
        "ds_read_b128 a[240:243], v247 offset:12288\n"
        "v_mul_f32 v75, v116, v75\n"
        "v_mfma_f32_16x16x32_bf16 v[112:115], v[212:215], v[216:219], v[60:63]\n"
        "ds_read_b128 a[244:247], v247 offset:12544\n"
        "v_cvt_pk_bf16_f32 v72, v72, v73\n"
        "v_mfma_f32_16x16x32_bf16 v[112:115], v[228:231], v[224:227], v[112:115]\n"
        "s_waitcnt lgkmcnt(7)\n"
        "ds_read_b128 a[248:251], v247 offset:8448\n"
        "v_cvt_pk_bf16_f32 v73, v74, v75\n"
        "v_mfma_f32_16x16x32_bf16 v[184:187], v[156:159], a[232:235], 0\n"
        "s_waitcnt lgkmcnt(7)\n"
        "v_exp_f32 v100, v100\n"
        "v_mfma_f32_16x16x32_bf16 v[184:187], v[160:163], a[220:223], v[184:187]\n"
        "v_exp_f32 v101, v101\n"
        "v_mfma_f32_16x16x32_bf16 v[204:207], v[164:167], a[220:223], 0\n"
        "ds_read_b128 a[220:223], v248 offset:3088\n"
        "v_mul_f32 v76, v116, v76\n"
        "v_mfma_f32_16x16x32_bf16 v[204:207], v[152:155], a[232:235], v[204:207]\n"
        "s_waitcnt lgkmcnt(7)\n"
        "ds_read_b128 a[232:235], v254 offset:16\n"
        "v_mul_f32 v77, v116, v77\n"
        "v_mfma_f32_16x16x32_bf16 v[192:195], v[152:155], a[216:219], 0\n"
        "s_waitcnt lgkmcnt(7)\n"
        "v_mul_f32 v78, v116, v78\n"
        "v_mul_f32 v79, v116, v79\n"
        "v_mfma_f32_16x16x32_bf16 v[192:195], v[164:167], a[228:231], v[192:195]\n"
        "v_exp_f32 v102, v102\n"
        "v_mfma_f32_16x16x32_bf16 v[172:175], v[160:163], a[228:231], 0\n"
        "ds_read_b128 a[228:231], v254 offset:3088\n"
        "v_cvt_pk_bf16_f32 v88, v100, v101\n"
        "v_mfma_f32_16x16x32_bf16 v[172:175], v[156:159], a[216:219], v[172:175]\n"
        "s_waitcnt lgkmcnt(6)\n"
        "ds_read_b128 a[216:219], v248 offset:6144\n"
        "v_cvt_pk_bf16_f32 v74, v76, v77\n"
        "v_mfma_f32_16x16x32_bf16 v[212:215], v[156:159], a[224:227], 0\n"
        "s_waitcnt lgkmcnt(3)\n"
        "v_exp_f32 v104, v104\n"
        "v_mfma_f32_16x16x32_bf16 v[212:215], v[160:163], a[220:223], v[212:215]\n"
        "v_exp_f32 v103, v103\n"
        "v_mfma_f32_16x16x32_bf16 v[216:219], v[164:167], a[220:223], 0\n"
        "ds_read_b128 a[220:223], v248 offset:9216\n"
        "v_cvt_pk_bf16_f32 v75, v78, v79\n"
        "v_mfma_f32_16x16x32_bf16 v[216:219], v[152:155], a[224:227], v[216:219]\n"
        "s_waitcnt lgkmcnt(3)\n"
        "ds_write_b128 v245, v[72:75] offset:512\n"
        "v_mfma_f32_16x16x32_bf16 v[224:227], v[152:155], a[232:235], 0\n"
        "s_waitcnt lgkmcnt(3)\n"
        "v_exp_f32 v105, v105\n"
        "v_mfma_f32_16x16x32_bf16 v[224:227], v[164:167], a[228:231], v[224:227]\n"
        "v_cvt_pk_bf16_f32 v89, v102, v103\n"
        "v_cvt_pk_bf16_f32 v90, v104, v105\n"
        "v_mfma_f32_16x16x32_bf16 v[220:223], v[160:163], a[228:231], 0\n"
        "ds_read_b128 a[224:227], v248 offset:6176\n"
        "ds_read_b128 a[228:231], v248 offset:9248\n"
        "v_mfma_f32_16x16x32_bf16 v[220:223], v[156:159], a[232:235], v[220:223]\n"
        "s_waitcnt lgkmcnt(4)\n"
        "ds_read_b128 a[232:235], v254 offset:6144\n"
        "v_mul_f32 v96, v116, v96\n"
        "v_mfma_f32_16x16x32_bf16 v[176:179], a[236:239], a[216:219], v[176:179]\n"
        "s_waitcnt lgkmcnt(4)\n"
        "v_exp_f32 v106, v106\n"
        "v_mfma_f32_16x16x32_bf16 v[176:179], a[240:243], a[220:223], v[176:179]\n"
        "v_mul_f32 v97, v116, v97\n"
        "v_mul_f32 v98, v116, v98\n"
        "v_mfma_f32_16x16x32_bf16 v[196:199], a[244:247], a[220:223], v[196:199]\n"
        "ds_read_b128 a[220:223], v254 offset:9216\n"
        "v_mul_f32 v99, v116, v99\n"
        "v_mfma_f32_16x16x32_bf16 v[196:199], a[248:251], a[216:219], v[196:199]\n"
        "s_waitcnt lgkmcnt(3)\n"
        "ds_read_b128 a[216:219], v254 offset:6176\n"
        "v_cvt_pk_bf16_f32 v96, v96, v97\n"
        "v_mfma_f32_16x16x32_bf16 v[200:203], a[248:251], a[224:227], v[200:203]\n"
        "s_waitcnt lgkmcnt(3)\n"
        "ds_read_b128 v[156:159], v247 offset:16384\n"
        "v_cvt_pk_bf16_f32 v97, v98, v99\n"
        "v_mfma_f32_16x16x32_bf16 v[200:203], a[244:247], a[228:231], v[200:203]\n"
        "ds_write_b64 v246, v[96:97] offset:512\n"
        "v_mfma_f32_16x16x32_bf16 v[180:183], a[240:243], a[228:231], v[180:183]\n"
        "ds_read_b128 a[228:231], v254 offset:9248\n"
        "v_max3_u32 v236, v236, v116, v117\n"
        "v_mfma_f32_16x16x32_bf16 v[180:183], a[236:239], a[224:227], v[180:183]\n"
        "s_waitcnt lgkmcnt(5)\n"
        "ds_read_b128 a[224:227], v248 offset:6160\n"
        "v_mfma_f32_16x16x32_bf16 v[184:187], a[236:239], a[232:235], v[184:187]\n"
        "s_waitcnt lgkmcnt(5)\n"
        "ds_read_b128 v[160:163], v247 offset:20480\n"
        "v_mfma_f32_16x16x32_bf16 v[184:187], a[240:243], a[220:223], v[184:187]\n"
        "ds_read_b128 v[164:167], v247 offset:20736\n"
        "v_mfma_f32_16x16x32_bf16 v[204:207], a[244:247], a[220:223], v[204:207]\n"
        "ds_read_b128 a[220:223], v248 offset:9232\n"
        "v_mfma_f32_16x16x32_bf16 v[204:207], a[248:251], a[232:235], v[204:207]\n"
        "s_waitcnt lgkmcnt(7)\n"
        "ds_read_b128 a[232:235], v254 offset:6160\n"
        "v_mfma_f32_16x16x32_bf16 v[192:195], a[248:251], a[216:219], v[192:195]\n"
        "s_waitcnt lgkmcnt(5)\n"
        "ds_read_b128 v[152:155], v247 offset:16640\n"
        "v_mfma_f32_16x16x32_bf16 v[192:195], a[244:247], a[228:231], v[192:195]\n"
        "v_exp_f32 v107, v107\n"
        "v_mfma_f32_16x16x32_bf16 v[172:175], a[240:243], a[228:231], v[172:175]\n"
        "ds_read_b128 a[228:231], v254 offset:9232\n"
        "v_cvt_pk_bf16_f32 v91, v106, v107\n"
        "v_mfma_f32_16x16x32_bf16 v[172:175], a[236:239], a[216:219], v[172:175]\n"
        "s_waitcnt lgkmcnt(6)\n"
        "ds_read_b128 a[216:219], v248 offset:12288\n"
        "v_mfma_f32_16x16x32_bf16 v[212:215], a[236:239], a[224:227], v[212:215]\n"
        "s_waitcnt lgkmcnt(4)\n"
        "v_exp_f32 v108, v108\n"
        "v_mfma_f32_16x16x32_bf16 v[212:215], a[240:243], a[220:223], v[212:215]\n"
        "v_exp_f32 v109, v109\n"
        "v_mfma_f32_16x16x32_bf16 v[216:219], a[244:247], a[220:223], v[216:219]\n"
        "ds_read_b128 a[220:223], v248 offset:15360\n"
        "v_cvt_pk_bf16_f32 v92, v108, v109\n"
        "v_mfma_f32_16x16x32_bf16 v[216:219], a[248:251], a[224:227], v[216:219]\n"
        "s_waitcnt lgkmcnt(4)\n"
        "ds_read_b128 a[224:227], v248 offset:12320\n"
        "v_mfma_f32_16x16x32_bf16 v[224:227], a[248:251], a[232:235], v[224:227]\n"
        "s_waitcnt lgkmcnt(3)\n"
        "v_exp_f32 v110, v110\n"
        "v_mfma_f32_16x16x32_bf16 v[224:227], a[244:247], a[228:231], v[224:227]\n"
        "v_exp_f32 v112, v112\n"
        "v_mfma_f32_16x16x32_bf16 v[220:223], a[240:243], a[228:231], v[220:223]\n"
        "ds_read_b128 a[228:231], v248 offset:15392\n"
        "v_mfma_f32_16x16x32_bf16 v[220:223], a[236:239], a[232:235], v[220:223]\n"
        "s_waitcnt lgkmcnt(3)\n"
        "ds_read_b128 a[232:235], v254 offset:12288\n"
        "v_mfma_f32_16x16x32_bf16 v[176:179], v[156:159], a[216:219], v[176:179]\n"
        "s_waitcnt lgkmcnt(3)\n"
        "v_exp_f32 v111, v111\n"
        "v_mfma_f32_16x16x32_bf16 v[176:179], v[160:163], a[220:223], v[176:179]\n"
        "v_exp_f32 v113, v113\n"
        "v_mfma_f32_16x16x32_bf16 v[196:199], v[164:167], a[220:223], v[196:199]\n"
        "ds_read_b128 a[220:223], v254 offset:15360\n"
        "v_cvt_pk_bf16_f32 v93, v110, v111\n"
        "v_mfma_f32_16x16x32_bf16 v[196:199], v[152:155], a[216:219], v[196:199]\n"
        "s_waitcnt lgkmcnt(3)\n"
        "ds_read_b128 a[216:219], v254 offset:12320\n"
        "v_cvt_pk_bf16_f32 v94, v112, v113\n"
        "v_mfma_f32_16x16x32_bf16 v[200:203], v[152:155], a[224:227], v[200:203]\n"
        "s_waitcnt lgkmcnt(3)\n"
        "v_exp_f32 v114, v114\n"
        "v_mfma_f32_16x16x32_bf16 v[200:203], v[164:167], a[228:231], v[200:203]\n"
        "v_exp_f32 v115, v115\n"
        "v_mfma_f32_16x16x32_bf16 v[180:183], v[160:163], a[228:231], v[180:183]\n"
        "s_waitcnt vmcnt(0)\n"
        "ds_read_b128 a[228:231], v254 offset:15392\n"
        "v_cvt_pk_bf16_f32 v95, v114, v115\n"
        "v_mfma_f32_16x16x32_bf16 v[180:183], v[156:159], a[224:227], v[180:183]\n"
        "s_waitcnt lgkmcnt(3)\n"
        "ds_read_b128 a[224:227], v248 offset:12304\n"
        "v_mfma_f32_16x16x32_bf16 v[184:187], v[156:159], a[232:235], v[184:187]\n"
        "s_waitcnt lgkmcnt(3)\n"
        "v_mfma_f32_16x16x32_bf16 v[184:187], v[160:163], a[220:223], v[184:187]\n"
        "s_barrier\n"
        "v_mfma_f32_16x16x32_bf16 v[204:207], v[164:167], a[220:223], v[204:207]\n"
        "ds_read_b128 a[220:223], v248 offset:15376\n"
        "v_mfma_f32_16x16x32_bf16 v[204:207], v[152:155], a[232:235], v[204:207]\n"
        "s_waitcnt lgkmcnt(3)\n"
        "ds_read_b128 a[232:235], v254 offset:12304\n"
        "v_mfma_f32_16x16x32_bf16 v[192:195], v[152:155], a[216:219], v[192:195]\n"
        "s_waitcnt lgkmcnt(3)\n"
        "ds_read_b128 a[236:239], v240 offset:0\n"
        "v_mfma_f32_16x16x32_bf16 v[192:195], v[164:167], a[228:231], v[192:195]\n"
        "ds_read_b128 a[240:243], v241 offset:0\n"
        "v_mfma_f32_16x16x32_bf16 v[172:175], v[160:163], a[228:231], v[172:175]\n"
        "ds_read_b128 a[228:231], v254 offset:15376\n"
        "v_mfma_f32_16x16x32_bf16 v[172:175], v[156:159], a[216:219], v[172:175]\n"
        "s_waitcnt lgkmcnt(5)\n"
        "ds_read_b128 a[244:247], v240 offset:128\n"
        "v_mfma_f32_16x16x32_bf16 v[212:215], v[156:159], a[224:227], v[212:215]\n"
        "s_waitcnt lgkmcnt(5)\n"
        "ds_read_b128 a[248:251], v241 offset:128\n"
        "v_mfma_f32_16x16x32_bf16 v[212:215], v[160:163], a[220:223], v[212:215]\n"
        "ds_read_b128 a[216:219], v240 offset:256\n"
        "v_mfma_f32_16x16x32_bf16 v[216:219], v[164:167], a[220:223], v[216:219]\n"
        "ds_read_b128 a[220:223], v241 offset:256\n"
        "v_mfma_f32_16x16x32_bf16 v[216:219], v[152:155], a[224:227], v[216:219]\n"
        "s_waitcnt lgkmcnt(7)\n"
        "v_mfma_f32_16x16x32_bf16 v[224:227], v[152:155], a[232:235], v[224:227]\n"
        "s_waitcnt lgkmcnt(4)\n"
        "v_mfma_f32_16x16x32_bf16 v[224:227], v[164:167], a[228:231], v[224:227]\n"
        "v_mfma_f32_16x16x32_bf16 v[220:223], v[160:163], a[228:231], v[220:223]\n"
        "v_mfma_f32_16x16x32_bf16 v[220:223], v[156:159], a[232:235], v[220:223]\n"
        "s_branch 10b\n"
        ".p2align 6\n"
        "54:\n"
        "s_branch 61b\n"
        "19:\n"
        "v_mfma_f32_16x16x32_bf16 v[116:119], a[248:251], a[180:183], v[116:119]\n"
        "ds_read_b128 a[224:227], v247 offset:0\n"
        "ds_read_b128 a[120:123], v254 offset:0\n"
        "s_and_b32 s46, s42, 1\n"
        "v_mfma_f32_16x16x32_bf16 v[116:119], a[216:219], a[184:187], v[116:119]\n"
        "ds_read_b128 a[228:231], v247 offset:4096\n"
        "ds_read_b128 a[124:127], v254 offset:3072\n"
        "v_mfma_f32_16x16x32_bf16 v[116:119], a[220:223], a[188:191], v[116:119]\n"
        "ds_read_b128 a[232:235], v247 offset:8192\n"
        "s_lshl_b32 s31, s46, 8\n"
        "v_mov_b32 v241, 0\n"
        "v_mfma_f32_16x16x32_bf16 v[104:107], v[212:215], v[176:179], v[28:31]\n"
        "ds_read_b128 a[128:131], v254 offset:6144\n"
        "s_nop 0\n"
        "v_mfma_f32_16x16x32_bf16 v[104:107], v[228:231], v[184:187], v[104:107]\n"
        "ds_read_b128 v[156:159], v247 offset:12288\n"
        "s_sub_u32 s31, s45, s31\n"
        "v_cvt_pk_bf16_f32 v142, v116, v117\n"
        "v_mfma_f32_16x16x32_bf16 v[120:123], a[220:223], a[212:215], 0\n"
        "ds_read_b128 a[132:135], v254 offset:9216\n"
        "v_cvt_pk_bf16_f32 v143, v118, v119\n"
        "v_mfma_f32_16x16x32_bf16 v[120:123], a[216:219], a[208:211], v[120:123]\n"
        "ds_read_b128 v[160:163], v247 offset:16384\n"
        "ds_read_b128 a[136:139], v254 offset:12288\n"
        "s_mul_i32 s45, s46, 786432\n"
        "v_mfma_f32_16x16x32_bf16 v[120:123], a[248:251], a[204:207], v[120:123]\n"
        "ds_read_b128 v[164:167], v247 offset:20480\n"
        "ds_read_b128 a[140:143], v254 offset:15360\n"
        "v_mfma_f32_16x16x32_bf16 v[120:123], a[244:247], a[200:203], v[120:123]\n"
        "ds_read_b128 a[92:95], v248 offset:15360\n"
        "s_sub_u32 s37, s37, s45\n"
        "v_mfma_f32_16x16x32_bf16 v[120:123], a[240:243], a[196:199], v[120:123]\n"
        "ds_read_b128 a[88:91], v248 offset:12288\n"
        "v_mfma_f32_16x16x32_bf16 v[120:123], a[236:239], a[192:195], v[120:123]\n"
        "ds_read_b128 a[84:87], v248 offset:9216\n"
        "s_cmp_eq_u32 s46, 0\n"
        "s_cselect_b32 s43, s43, s44\n"
        "v_mfma_f32_16x16x32_bf16 v[108:111], v[212:215], v[196:199], v[44:47]\n"
        "ds_read_b128 a[80:83], v248 offset:6144\n"
        "s_nop 0\n"
        "v_mfma_f32_16x16x32_bf16 v[108:111], v[228:231], v[204:207], v[108:111]\n"
        "s_waitcnt lgkmcnt(14)\n"
        "ds_read_b128 a[76:79], v248 offset:3072\n"
        "v_cvt_pk_bf16_f32 v150, v120, v121\n"
        "v_mfma_f32_16x16x32_bf16 v[8:11], a[224:227], a[120:123], 0\n"
        "s_waitcnt lgkmcnt(13)\n"
        "ds_read_b128 a[72:75], v248 offset:0\n"
        "v_cvt_pk_bf16_f32 v151, v122, v123\n"
        "v_mfma_f32_16x16x32_bf16 v[8:11], a[228:231], a[124:127], v[8:11]\n"
        "s_waitcnt lgkmcnt(12)\n"
        "v_exp_f32 v100, v100\n"
        "v_mfma_f32_16x16x32_bf16 v[8:11], a[232:235], a[128:131], v[8:11]\n"
        "s_waitcnt lgkmcnt(10)\n"
        "v_exp_f32 v101, v101\n"
        "v_mfma_f32_16x16x32_bf16 v[8:11], v[156:159], a[132:135], v[8:11]\n"
        "s_waitcnt lgkmcnt(8)\n"
        "ds_read_b128 a[96:99], v248 offset:32\n"
        "v_cvt_pk_bf16_f32 v70, v100, v101\n"
        "v_mfma_f32_16x16x32_bf16 v[8:11], v[160:163], a[136:139], v[8:11]\n"
        "s_waitcnt lgkmcnt(7)\n"
        "ds_read_b128 a[100:103], v248 offset:3104\n"
        "v_mfma_f32_16x16x32_bf16 v[8:11], v[164:167], a[140:143], v[8:11]\n"
        "s_waitcnt lgkmcnt(7)\n"
        "ds_read_b128 a[104:107], v248 offset:6176\n"
        "v_mfma_f32_16x16x32_bf16 v[0:3], v[164:167], a[92:95], 0\n"
        "s_waitcnt lgkmcnt(7)\n"
        "ds_read_b128 a[108:111], v248 offset:9248\n"
        "v_mfma_f32_16x16x32_bf16 v[0:3], v[160:163], a[88:91], v[0:3]\n"
        "s_waitcnt lgkmcnt(7)\n"
        "ds_read_b128 a[112:115], v248 offset:12320\n"
        "v_mfma_f32_16x16x32_bf16 v[0:3], v[156:159], a[84:87], v[0:3]\n"
        "s_waitcnt lgkmcnt(7)\n"
        "ds_read_b128 a[116:119], v248 offset:15392\n"
        "v_mfma_f32_16x16x32_bf16 v[0:3], a[232:235], a[80:83], v[0:3]\n"
        "s_waitcnt lgkmcnt(7)\n"
        "v_exp_f32 v102, v102\n"
        "v_mfma_f32_16x16x32_bf16 v[0:3], a[228:231], a[76:79], v[0:3]\n"
        "s_waitcnt lgkmcnt(6)\n"
        "v_exp_f32 v103, v103\n"
        "v_mfma_f32_16x16x32_bf16 v[0:3], a[224:227], a[72:75], v[0:3]\n"
        "s_add_u32 s28, s37, 786432\n"
        "v_exp_f32 v104, v104\n"
        "v_mfma_f32_16x16x32_bf16 v[116:119], a[0:3], a[236:239], 0\n"
        "s_cmp_eq_u32 s28, 100663296\n"
        "s_cselect_b32 s28, 0, s28\n"
        "v_mov_b64 v[96:97], 0\n"
        "v_mfma_f32_16x16x32_bf16 v[116:119], a[4:7], a[240:243], v[116:119]\n"
        "s_lshl_b32 s29, s42, 2\n"
        "v_exp_f32 v105, v105\n"
        "v_mfma_f32_16x16x32_bf16 v[116:119], a[8:11], a[244:247], v[116:119]\n"
        "s_add_u32 s47, s37, s43\n"
        "v_exp_f32 v106, v106\n"
        "v_mfma_f32_16x16x32_bf16 v[116:119], a[12:15], a[248:251], v[116:119]\n"
        "s_add_u32 s35, s28, s43\n"
        "v_exp_f32 v107, v107\n"
        "v_mfma_f32_16x16x32_bf16 v[116:119], a[16:19], a[216:219], v[116:119]\n"
        "ds_read_b128 a[164:167], v254 offset:15392\n"
        "v_cvt_pk_bf16_f32 v71, v102, v103\n"
        "v_mfma_f32_16x16x32_bf16 v[116:119], a[20:23], a[220:223], v[116:119]\n"
        "s_waitcnt lgkmcnt(6)\n"
        "ds_read_b128 a[160:163], v254 offset:12320\n"
        "v_cvt_pk_bf16_f32 v78, v104, v105\n"
        "v_mfma_f32_16x16x32_bf16 v[4:7], a[224:227], a[96:99], 0\n"
        "s_waitcnt lgkmcnt(6)\n"
        "ds_read_b128 a[156:159], v254 offset:9248\n"
        "v_cvt_pk_bf16_f32 v79, v106, v107\n"
        "v_mfma_f32_16x16x32_bf16 v[4:7], a[228:231], a[100:103], v[4:7]\n"
        "s_waitcnt lgkmcnt(6)\n"
        "ds_read_b128 a[152:155], v254 offset:6176\n"
        "v_add_u32 v240, s31, v247\n"
        "v_mfma_f32_16x16x32_bf16 v[4:7], a[232:235], a[104:107], v[4:7]\n"
        "s_waitcnt lgkmcnt(6)\n"
        "ds_read_b128 a[148:151], v254 offset:3104\n"
        "v_cvt_pk_bf16_f32 v216, v116, v117\n"
        "v_mfma_f32_16x16x32_bf16 v[4:7], v[156:159], a[108:111], v[4:7]\n"
        "s_waitcnt lgkmcnt(6)\n"
        "ds_read_b128 a[144:147], v254 offset:32\n"
        "v_cvt_pk_bf16_f32 v217, v118, v119\n"
        "v_mfma_f32_16x16x32_bf16 v[4:7], v[160:163], a[112:115], v[4:7]\n"
        "s_waitcnt lgkmcnt(6)\n"
        "v_exp_f32 v108, v108\n"
        "v_mfma_f32_16x16x32_bf16 v[4:7], v[164:167], a[116:119], v[4:7]\n"
        "v_exp_f32 v109, v109\n"
        "v_smfmac_f32_16x16x64_bf16 v[96:99], a[252:255], v[64:71], v237\n"
        "v_cvt_pk_bf16_f32 v86, v108, v109\n"
        "v_mfma_f32_16x16x32_bf16 v[100:103], v[132:135], v[68:71], 0\n"
        "v_mov_b64 v[124:125], 0\n"
        "v_mov_b32 v249, s29\n"
        "v_mfma_f32_16x16x32_bf16 v[100:103], v[128:131], v[64:67], v[100:103]\n"
        "v_exp_f32 v110, v110\n"
        "v_mfma_f32_16x16x32_bf16 v[104:107], v[136:139], v[64:67], 0\n"
        "v_exp_f32 v111, v111\n"
        "v_mfma_f32_16x16x32_bf16 v[104:107], v[140:143], v[68:71], v[104:107]\n"
        "v_cvt_pk_bf16_f32 v87, v110, v111\n"
        "v_add_f32 v97, v96, v97\n"
        "v_mfma_f32_16x16x32_bf16 v[108:111], v[148:151], v[68:71], 0\n"
        "v_rcp_f32 v96, v97\n"
        "v_mfma_f32_16x16x32_bf16 v[108:111], v[144:147], v[64:67], v[108:111]\n"
        "s_waitcnt lgkmcnt(5)\n"
        "v_cvt_pk_bf16_f32 v232, v0, v4\n"
        "v_mul_f32 v100, v96, v100\n"
        "v_mfma_f32_16x16x32_bf16 v[12:15], v[164:167], a[164:167], 0\n"
        "s_waitcnt lgkmcnt(4)\n"
        "v_mul_f32 v101, v96, v101\n"
        "v_mul_f32 v102, v96, v102\n"
        "v_mfma_f32_16x16x32_bf16 v[12:15], v[160:163], a[160:163], v[12:15]\n"
        "s_waitcnt lgkmcnt(3)\n"
        "ds_read_b128 v[176:179], v247 offset:256\n"
        "v_cvt_pk_bf16_f32 v234, v1, v5\n"
        "v_mfma_f32_16x16x32_bf16 v[12:15], v[156:159], a[156:159], v[12:15]\n"
        "s_waitcnt lgkmcnt(3)\n"
        "ds_read_b128 v[180:183], v247 offset:4352\n"
        "v_mul_f32 v103, v96, v103\n"
        "v_mfma_f32_16x16x32_bf16 v[12:15], a[232:235], a[152:155], v[12:15]\n"
        "s_waitcnt lgkmcnt(3)\n"
        "ds_read_b128 v[184:187], v247 offset:8448\n"
        "v_cvt_pk_bf16_f32 v100, v100, v101\n"
        "v_mfma_f32_16x16x32_bf16 v[12:15], a[228:231], a[148:151], v[12:15]\n"
        "s_waitcnt lgkmcnt(3)\n"
        "ds_read_b128 a[168:171], v248 offset:16\n"
        "v_cvt_pk_bf16_f32 v242, v2, v6\n"
        "v_mfma_f32_16x16x32_bf16 v[12:15], a[224:227], a[144:147], v[12:15]\n"
        "ds_read_b128 a[172:175], v248 offset:3088\n"
        "v_cvt_pk_bf16_f32 v101, v102, v103\n"
        "v_smfmac_f32_16x16x64_bf16 v[124:127], a[252:255], v[72:79], v237\n"
        "ds_read_b128 a[176:179], v248 offset:6160\n"
        "v_mfma_f32_16x16x32_bf16 v[64:67], v[132:135], v[76:79], 0\n"
        "ds_read_b128 a[180:183], v248 offset:9232\n"
        "v_mul_f32 v104, v96, v104\n"
        "v_mfma_f32_16x16x32_bf16 v[64:67], v[128:131], v[72:75], v[64:67]\n"
        "ds_read_b128 a[184:187], v248 offset:12304\n"
        "v_cvt_pk_bf16_f32 v233, v8, v12\n"
        "v_mfma_f32_16x16x32_bf16 v[68:71], v[136:139], v[72:75], 0\n"
        "buffer_store_dwordx2 v[232:233], v250, s[16:19], s38 offen offset:0 sc1\n"
        "ds_read_b128 a[188:191], v248 offset:15376\n"
        "v_mfma_f32_16x16x32_bf16 v[68:71], v[140:143], v[76:79], v[68:71]\n"
        "ds_read_b128 v[196:199], v247 offset:12544\n"
        "v_cvt_pk_bf16_f32 v235, v9, v13\n"
        "v_mfma_f32_16x16x32_bf16 v[112:115], v[148:151], v[76:79], 0\n"
        "ds_read_b128 v[200:203], v247 offset:16640\n"
        "v_add_f32 v125, v124, v125\n"
        "v_mfma_f32_16x16x32_bf16 v[112:115], v[144:147], v[72:75], v[112:115]\n"
        "buffer_store_dwordx2 v[234:235], v250, s[16:19], s38 offen offset:384 sc1\n"
        "ds_read_b128 v[204:207], v247 offset:20736\n"
        "v_mfma_f32_16x16x32_bf16 v[120:123], a[44:47], a[220:223], 0\n"
        "v_rcp_f32 v124, v125\n"
        "v_mfma_f32_16x16x32_bf16 v[120:123], a[40:43], a[216:219], v[120:123]\n"
        "v_cvt_pk_bf16_f32 v243, v10, v14\n"
        "v_mul_f32 v64, v124, v64\n"
        "v_mfma_f32_16x16x32_bf16 v[120:123], a[36:39], a[248:251], v[120:123]\n"
        "buffer_store_dwordx2 v[242:243], v250, s[16:19], s38 offen offset:768 sc1\n"
        "v_mul_f32 v65, v124, v65\n"
        "v_mfma_f32_16x16x32_bf16 v[120:123], a[32:35], a[244:247], v[120:123]\n"
        "ds_read_b128 a[192:195], v254 offset:16\n"
        "v_mul_f32 v66, v124, v66\n"
        "v_mfma_f32_16x16x32_bf16 v[120:123], a[28:31], a[240:243], v[120:123]\n"
        "ds_read_b128 a[196:199], v254 offset:3088\n"
        "v_mul_f32 v67, v124, v67\n"
        "v_mfma_f32_16x16x32_bf16 v[120:123], a[24:27], a[236:239], v[120:123]\n"
        "s_waitcnt lgkmcnt(10)\n"
        "ds_read_b128 a[200:203], v254 offset:6160\n"
        "v_cvt_pk_bf16_f32 v64, v64, v65\n"
        "v_mfma_f32_16x16x32_bf16 v[16:19], a[224:227], a[168:171], 0\n"
        "s_waitcnt lgkmcnt(10)\n"
        "ds_read_b128 a[204:207], v254 offset:9232\n"
        "ds_read_b128 v[222:225], v251 offset:4608\n"
        "v_mfma_f32_16x16x32_bf16 v[16:19], a[228:231], a[172:175], v[16:19]\n"
        "s_waitcnt lgkmcnt(11)\n"
        "ds_read_b128 a[208:211], v254 offset:12304\n"
        "v_cvt_pk_bf16_f32 v65, v66, v67\n"
        "v_mfma_f32_16x16x32_bf16 v[16:19], a[232:235], a[176:179], v[16:19]\n"
        "s_waitcnt lgkmcnt(11)\n"
        "ds_read_b128 a[212:215], v254 offset:15376\n"
        "v_cvt_pk_bf16_f32 v232, v3, v7\n"
        "v_mfma_f32_16x16x32_bf16 v[16:19], v[156:159], a[180:183], v[16:19]\n"
        "s_waitcnt lgkmcnt(11)\n"
        "v_cvt_pk_bf16_f32 v233, v11, v15\n"
        "v_mul_f32 v68, v124, v68\n"
        "v_mfma_f32_16x16x32_bf16 v[16:19], v[160:163], a[184:187], v[16:19]\n"
        "s_waitcnt lgkmcnt(10)\n"
        "buffer_store_dwordx2 v[232:233], v250, s[16:19], s38 offen offset:1152 sc1\n"
        "v_mul_f32 v69, v124, v69\n"
        "v_mfma_f32_16x16x32_bf16 v[16:19], v[164:167], a[188:191], v[16:19]\n"
        "s_waitcnt lgkmcnt(7)\n"
        "v_mul_f32 v70, v124, v70\n"
        "v_mul_f32 v71, v124, v71\n"
        "v_mfma_f32_16x16x32_bf16 v[24:27], v[204:207], a[92:95], 0\n"
        "v_mul_f32 v105, v96, v105\n"
        "v_mul_f32 v106, v96, v106\n"
        "v_mfma_f32_16x16x32_bf16 v[24:27], v[200:203], a[88:91], v[24:27]\n"
        "v_mul_f32 v107, v96, v107\n"
        "v_cvt_pk_bf16_f32 v66, v68, v69\n"
        "v_mfma_f32_16x16x32_bf16 v[24:27], v[196:199], a[84:87], v[24:27]\n"
        "v_cvt_pk_bf16_f32 v67, v70, v71\n"
        "v_mul_f32 v112, v124, v112\n"
        "v_mfma_f32_16x16x32_bf16 v[24:27], v[184:187], a[80:83], v[24:27]\n"
        "ds_write_b128 v245, v[64:67] offset:256\n"
        "v_mfma_f32_16x16x32_bf16 v[24:27], v[180:183], a[76:79], v[24:27]\n"
        "v_mul_f32 v113, v124, v113\n"
        "v_mul_f32 v114, v124, v114\n"
        "v_mfma_f32_16x16x32_bf16 v[24:27], v[176:179], a[72:75], v[24:27]\n"
        "s_waitcnt lgkmcnt(7)\n"
        "v_mul_f32 v115, v124, v115\n"
        "v_cvt_pk_bf16_f32 v102, v104, v105\n"
        "v_mfma_f32_16x16x32_bf16 v[20:23], a[224:227], a[192:195], 0\n"
        "s_waitcnt lgkmcnt(6)\n"
        "v_cvt_pk_bf16_f32 v103, v106, v107\n"
        "v_cvt_pk_bf16_f32 v218, v120, v121\n"
        "v_mfma_f32_16x16x32_bf16 v[20:23], a[228:231], a[196:199], v[20:23]\n"
        "s_waitcnt lgkmcnt(5)\n"
        "ds_write_b128 v245, v[100:103] offset:0\n"
        "v_mfma_f32_16x16x32_bf16 v[20:23], a[232:235], a[200:203], v[20:23]\n"
        "s_waitcnt lgkmcnt(5)\n"
        "v_cvt_pk_bf16_f32 v219, v122, v123\n"
        "v_mul_f32 v108, v96, v108\n"
        "v_mfma_f32_16x16x32_bf16 v[20:23], v[156:159], a[204:207], v[20:23]\n"
        "s_waitcnt lgkmcnt(3)\n"
        "v_mul_f32 v109, v96, v109\n"
        "v_mul_f32 v110, v96, v110\n"
        "v_mfma_f32_16x16x32_bf16 v[20:23], v[160:163], a[208:211], v[20:23]\n"
        "s_waitcnt lgkmcnt(2)\n"
        "v_mul_f32 v111, v96, v111\n"
        "v_cvt_pk_bf16_f32 v112, v112, v113\n"
        "v_mfma_f32_16x16x32_bf16 v[20:23], v[164:167], a[212:215], v[20:23]\n"
        "v_cvt_pk_bf16_f32 v113, v114, v115\n"
        "v_cvt_pk_bf16_f32 v108, v108, v109\n"
        "v_mfma_f32_16x16x32_bf16 v[116:119], a[48:51], a[236:239], 0\n"
        "ds_write_b64 v246, v[112:113] offset:256\n"
        "v_mfma_f32_16x16x32_bf16 v[116:119], a[52:55], a[240:243], v[116:119]\n"
        "v_cvt_pk_bf16_f32 v109, v110, v111\n"
        "v_max_u32 v236, v96, v97\n"
        "v_mfma_f32_16x16x32_bf16 v[116:119], a[56:59], a[244:247], v[116:119]\n"
        "ds_write_b64 v246, v[108:109] offset:0\n"
        "v_mfma_f32_16x16x32_bf16 v[116:119], a[60:63], a[248:251], v[116:119]\n"
        "v_max3_u32 v236, v236, v124, v125\n"
        "v_cvt_pk_bf16_f32 v244, v16, v20\n"
        "v_mfma_f32_16x16x32_bf16 v[116:119], a[64:67], a[216:219], v[116:119]\n"
        "buffer_store_dword v244, v238, s[16:19], s38 offen offset:0 sc1\n"
        "v_mfma_f32_16x16x32_bf16 v[116:119], a[68:71], a[220:223], v[116:119]\n"
        "v_cvt_pk_bf16_f32 v239, v17, v21\n"
        "v_cvt_pk_bf16_f32 v255, v18, v22\n"
        "v_mfma_f32_16x16x32_bf16 v[32:35], v[204:207], a[140:143], 0\n"
        "v_cvt_pk_bf16_f32 v244, v19, v23\n"
        "s_nop 0\n"
        "v_mfma_f32_16x16x32_bf16 v[32:35], v[200:203], a[136:139], v[32:35]\n"
        "buffer_store_dword v239, v238, s[16:19], s38 offen offset:384 sc1\n"
        "v_mfma_f32_16x16x32_bf16 v[32:35], v[196:199], a[132:135], v[32:35]\n"
        "v_cvt_pk_bf16_f32 v220, v116, v117\n"
        "v_cvt_pk_bf16_f32 v221, v118, v119\n"
        "v_mfma_f32_16x16x32_bf16 v[32:35], v[184:187], a[128:131], v[32:35]\n"
        "v_cvt_pk_bf16_f32 v226, v116, v117\n"
        "v_cvt_pk_bf16_f32 v227, v118, v119\n"
        "v_mfma_f32_16x16x32_bf16 v[32:35], v[180:183], a[124:127], v[32:35]\n"
        "v_mfma_f32_16x16x32_bf16 v[32:35], v[176:179], a[120:123], v[32:35]\n"
        "buffer_store_dword v255, v238, s[16:19], s38 offen offset:768 sc1\n"
        "v_mfma_f32_16x16x32_bf16 v[0:3], v[152:155], v[216:219], v[48:51]\n"
        ".long 0xbec200ff\n"
        "48:\n"
        "s_branch 62b\n"
        "v_mfma_f32_16x16x32_bf16 v[0:3], v[168:171], v[220:223], v[0:3]\n"
        "s_nop 2\n"
        "v_mfma_f32_16x16x32_bf16 v[4:7], v[172:175], v[216:219], v[52:55]\n"
        "s_nop 2\n"
        "v_mfma_f32_16x16x32_bf16 v[4:7], v[188:191], v[224:227], v[4:7]\n"
        "buffer_store_dword v244, v238, s[16:19], s38 offen offset:1152 sc1\n"
        "v_mfma_f32_16x16x32_bf16 v[28:31], v[176:179], a[96:99], 0\n"
        "ds_read_b128 v[222:225], v252 offset:4608\n"
        "s_nop 0\n"
        "v_mfma_f32_16x16x32_bf16 v[28:31], v[180:183], a[100:103], v[28:31]\n"
        "v_exp_f32 v0, v0\n"
        "v_mfma_f32_16x16x32_bf16 v[28:31], v[184:187], a[104:107], v[28:31]\n"
        "v_exp_f32 v1, v1\n"
        "v_mfma_f32_16x16x32_bf16 v[28:31], v[196:199], a[108:111], v[28:31]\n"
        "v_exp_f32 v2, v2\n"
        "v_mfma_f32_16x16x32_bf16 v[28:31], v[200:203], a[112:115], v[28:31]\n"
        "v_cvt_pk_bf16_f32 v88, v0, v1\n"
        "v_mfma_f32_16x16x32_bf16 v[28:31], v[204:207], a[116:119], v[28:31]\n"
        "v_exp_f32 v4, v4\n"
        "v_mfma_f32_16x16x32_bf16 v[36:39], v[204:207], a[164:167], 0\n"
        "v_exp_f32 v3, v3\n"
        "v_mfma_f32_16x16x32_bf16 v[36:39], v[200:203], a[160:163], v[36:39]\n"
        "v_exp_f32 v5, v5\n"
        "v_mfma_f32_16x16x32_bf16 v[36:39], v[196:199], a[156:159], v[36:39]\n"
        "v_cvt_pk_bf16_f32 v89, v2, v3\n"
        "v_cvt_pk_bf16_f32 v90, v4, v5\n"
        "v_mfma_f32_16x16x32_bf16 v[36:39], v[184:187], a[152:155], v[36:39]\n"
        "v_exp_f32 v6, v6\n"
        "v_mfma_f32_16x16x32_bf16 v[36:39], v[180:183], a[148:151], v[36:39]\n"
        "v_exp_f32 v7, v7\n"
        "v_mfma_f32_16x16x32_bf16 v[36:39], v[176:179], a[144:147], v[36:39]\n"
        "v_cvt_pk_bf16_f32 v91, v6, v7\n"
        "v_mov_b64 v[96:97], 0\n"
        "v_mfma_f32_16x16x32_bf16 v[8:11], v[192:195], v[216:219], v[56:59]\n"
        "s_waitcnt lgkmcnt(0)\n"
        "v_cvt_pk_bf16_f32 v234, v24, v28\n"
        "v_cvt_pk_bf16_f32 v242, v25, v29\n"
        "v_mfma_f32_16x16x32_bf16 v[8:11], v[208:211], v[220:223], v[8:11]\n"
        "v_cvt_pk_bf16_f32 v232, v26, v30\n"
        "s_nop 0\n"
        "v_mfma_f32_16x16x32_bf16 v[44:47], v[176:179], a[192:195], 0\n"
        "v_cvt_pk_bf16_f32 v235, v32, v36\n"
        "v_cvt_pk_bf16_f32 v243, v33, v37\n"
        "v_mfma_f32_16x16x32_bf16 v[44:47], v[180:183], a[196:199], v[44:47]\n"
        "buffer_store_dwordx2 v[234:235], v250, s[16:19], s39 offen offset:0 sc1\n"
        "v_cvt_pk_bf16_f32 v233, v34, v38\n"
        "v_mfma_f32_16x16x32_bf16 v[44:47], v[184:187], a[200:203], v[44:47]\n"
        "v_exp_f32 v8, v8\n"
        "v_mfma_f32_16x16x32_bf16 v[44:47], v[196:199], a[204:207], v[44:47]\n"
        "v_exp_f32 v9, v9\n"
        "v_mfma_f32_16x16x32_bf16 v[44:47], v[200:203], a[208:211], v[44:47]\n"
        "buffer_store_dwordx2 v[242:243], v250, s[16:19], s39 offen offset:384 sc1\n"
        "v_cvt_pk_bf16_f32 v92, v8, v9\n"
        "v_mfma_f32_16x16x32_bf16 v[44:47], v[204:207], a[212:215], v[44:47]\n"
        "v_exp_f32 v10, v10\n"
        "v_mfma_f32_16x16x32_bf16 v[12:15], v[212:215], v[216:219], v[60:63]\n"
        "v_exp_f32 v11, v11\n"
        "v_mfma_f32_16x16x32_bf16 v[12:15], v[228:231], v[224:227], v[12:15]\n"
        "buffer_store_dwordx2 v[232:233], v250, s[16:19], s39 offen offset:768 sc1\n"
        "v_cvt_pk_bf16_f32 v234, v27, v31\n"
        "v_smfmac_f32_16x16x64_bf16 v[96:99], a[252:255], v[80:87], v237\n"
        "v_cvt_pk_bf16_f32 v235, v35, v39\n"
        "v_mfma_f32_16x16x32_bf16 v[72:75], v[132:135], v[84:87], 0\n"
        "s_barrier\n"
        "ds_read_b128 a[0:3], v240 offset:0\n"
        "ds_read_b128 a[4:7], v240 offset:4096\n"
        "v_mfma_f32_16x16x32_bf16 v[72:75], v[128:131], v[80:83], v[72:75]\n"
        "buffer_store_dwordx2 v[234:235], v250, s[16:19], s39 offen offset:1152 sc1\n"
        "ds_read_b128 a[8:11], v240 offset:8192\n"
        "v_mfma_f32_16x16x32_bf16 v[76:79], v[136:139], v[80:83], 0\n"
        "ds_read_b128 a[12:15], v240 offset:12288\n"
        "ds_read_b128 a[16:19], v240 offset:16384\n"
        "v_mfma_f32_16x16x32_bf16 v[76:79], v[140:143], v[84:87], v[76:79]\n"
        "ds_read_b128 a[20:23], v240 offset:20480\n"
        "v_cvt_pk_bf16_f32 v93, v10, v11\n"
        "v_mfma_f32_16x16x32_bf16 v[100:103], v[148:151], v[84:87], 0\n"
        "v_exp_f32 v12, v12\n"
        "v_mfma_f32_16x16x32_bf16 v[100:103], v[144:147], v[80:83], v[100:103]\n"
        "v_exp_f32 v13, v13\n"
        "v_mfma_f32_16x16x32_bf16 v[40:43], v[204:207], a[188:191], 0\n"
        "v_exp_f32 v14, v14\n"
        "v_mfma_f32_16x16x32_bf16 v[40:43], v[200:203], a[184:187], v[40:43]\n"
        "v_cvt_pk_bf16_f32 v94, v12, v13\n"
        "v_mov_b64 v[124:125], 0\n"
        "v_mfma_f32_16x16x32_bf16 v[40:43], v[196:199], a[180:183], v[40:43]\n"
        "v_exp_f32 v15, v15\n"
        "v_mfma_f32_16x16x32_bf16 v[40:43], v[184:187], a[176:179], v[40:43]\n"
        "v_cvt_pk_bf16_f32 v95, v14, v15\n"
        "v_add_f32 v97, v96, v97\n"
        "v_mfma_f32_16x16x32_bf16 v[40:43], v[180:183], a[172:175], v[40:43]\n"
        "v_rcp_f32 v96, v97\n"
        "v_mfma_f32_16x16x32_bf16 v[40:43], v[176:179], a[168:171], v[40:43]\n"
        "s_waitcnt lgkmcnt(5)\n"
        "v_mul_f32 v72, v96, v72\n"
        "v_mul_f32 v73, v96, v73\n"
        "v_mfma_f32_16x16x32_bf16 v[156:159], a[0:3], a[72:75], 0\n"
        "s_waitcnt lgkmcnt(4)\n"
        "v_mul_f32 v74, v96, v74\n"
        "v_mul_f32 v75, v96, v75\n"
        "v_mfma_f32_16x16x32_bf16 v[156:159], a[4:7], a[76:79], v[156:159]\n"
        "s_waitcnt lgkmcnt(3)\n"
        "v_cvt_pk_bf16_f32 v72, v72, v73\n"
        "v_cvt_pk_bf16_f32 v73, v74, v75\n"
        "v_mfma_f32_16x16x32_bf16 v[156:159], a[8:11], a[80:83], v[156:159]\n"
        "s_waitcnt lgkmcnt(2)\n"
        "v_cvt_pk_bf16_f32 v239, v40, v44\n"
        "v_cvt_pk_bf16_f32 v255, v41, v45\n"
        "v_mfma_f32_16x16x32_bf16 v[156:159], a[12:15], a[84:87], v[156:159]\n"
        "s_waitcnt lgkmcnt(1)\n"
        "buffer_store_dword v239, v238, s[16:19], s39 offen offset:0 sc1\n"
        "v_mfma_f32_16x16x32_bf16 v[156:159], a[16:19], a[88:91], v[156:159]\n"
        "s_waitcnt lgkmcnt(0)\n"
        "v_cvt_pk_bf16_f32 v244, v42, v46\n"
        "v_cvt_pk_bf16_f32 v239, v43, v47\n"
        "v_mfma_f32_16x16x32_bf16 v[156:159], a[20:23], a[92:95], v[156:159]\n"
        "buffer_store_dword v255, v238, s[16:19], s39 offen offset:384 sc1\n"
        "v_smfmac_f32_16x16x64_bf16 v[124:127], a[252:255], v[88:95], v237\n"
        "v_mul_f32 v76, v96, v76\n"
        "v_mfma_f32_16x16x32_bf16 v[104:107], v[132:135], v[92:95], 0\n"
        "buffer_store_dword v244, v238, s[16:19], s39 offen offset:768 sc1\n"
        "v_mfma_f32_16x16x32_bf16 v[104:107], v[128:131], v[88:91], v[104:107]\n"
        "v_mul_f32 v77, v96, v77\n"
        "v_mul_f32 v78, v96, v78\n"
        "v_mfma_f32_16x16x32_bf16 v[108:111], v[136:139], v[88:91], 0\n"
        "buffer_store_dword v239, v238, s[16:19], s39 offen offset:1152 sc1\n"
        "v_mfma_f32_16x16x32_bf16 v[108:111], v[140:143], v[92:95], v[108:111]\n"
        "v_mul_f32 v79, v96, v79\n"
        "v_add_f32 v125, v124, v125\n"
        "v_mfma_f32_16x16x32_bf16 v[112:115], v[148:151], v[92:95], 0\n"
        "v_cvt_pk_bf16_f32 v74, v76, v77\n"
        "v_cvt_pk_bf16_f32 v75, v78, v79\n"
        "v_mfma_f32_16x16x32_bf16 v[112:115], v[144:147], v[88:91], v[112:115]\n"
        "ds_write_b128 v245, v[72:75] offset:512\n"
        "v_mfma_f32_16x16x32_bf16 v[176:179], a[20:23], a[164:167], 0\n"
        "v_rcp_f32 v124, v125\n"
        "v_mfma_f32_16x16x32_bf16 v[176:179], a[16:19], a[160:163], v[176:179]\n"
        "v_mul_f32 v100, v96, v100\n"
        "v_mul_f32 v101, v96, v101\n"
        "v_mfma_f32_16x16x32_bf16 v[176:179], a[12:15], a[156:159], v[176:179]\n"
        "v_mul_f32 v102, v96, v102\n"
        "v_mul_f32 v103, v96, v103\n"
        "v_mfma_f32_16x16x32_bf16 v[176:179], a[8:11], a[152:155], v[176:179]\n"
        "v_mul_f32 v104, v124, v104\n"
        "v_mul_f32 v105, v124, v105\n"
        "v_mfma_f32_16x16x32_bf16 v[176:179], a[4:7], a[148:151], v[176:179]\n"
        "v_mul_f32 v106, v124, v106\n"
        "v_mul_f32 v107, v124, v107\n"
        "v_mfma_f32_16x16x32_bf16 v[176:179], a[0:3], a[144:147], v[176:179]\n"
        "v_cvt_pk_bf16_f32 v100, v100, v101\n"
        "v_cvt_pk_bf16_f32 v101, v102, v103\n"
        "v_mfma_f32_16x16x32_bf16 v[160:163], a[0:3], a[96:99], 0\n"
        "ds_write_b64 v246, v[100:101] offset:512\n"
        "v_mfma_f32_16x16x32_bf16 v[160:163], a[4:7], a[100:103], v[160:163]\n"
        "v_cvt_pk_bf16_f32 v104, v104, v105\n"
        "v_cvt_pk_bf16_f32 v105, v106, v107\n"
        "v_mfma_f32_16x16x32_bf16 v[160:163], a[8:11], a[104:107], v[160:163]\n"
        "v_mul_f32 v108, v124, v108\n"
        "v_mul_f32 v109, v124, v109\n"
        "v_mfma_f32_16x16x32_bf16 v[160:163], a[12:15], a[108:111], v[160:163]\n"
        "v_mul_f32 v110, v124, v110\n"
        "v_mul_f32 v111, v124, v111\n"
        "v_mfma_f32_16x16x32_bf16 v[160:163], a[16:19], a[112:115], v[160:163]\n"
        "v_max3_u32 v236, v236, v96, v97\n"
        "v_cvt_pk_bf16_f32 v106, v108, v109\n"
        "v_mfma_f32_16x16x32_bf16 v[160:163], a[20:23], a[116:119], v[160:163]\n"
        "v_cvt_pk_bf16_f32 v107, v110, v111\n"
        "v_mul_f32 v112, v124, v112\n"
        "v_mfma_f32_16x16x32_bf16 v[164:167], a[20:23], a[140:143], 0\n"
        "ds_write_b128 v245, v[104:107] offset:768\n"
        "v_mfma_f32_16x16x32_bf16 v[164:167], a[16:19], a[136:139], v[164:167]\n"
        "v_mul_f32 v113, v124, v113\n"
        "v_mul_f32 v114, v124, v114\n"
        "v_mfma_f32_16x16x32_bf16 v[164:167], a[12:15], a[132:135], v[164:167]\n"
        "v_mul_f32 v115, v124, v115\n"
        "v_cvt_pk_bf16_f32 v112, v112, v113\n"
        "v_mfma_f32_16x16x32_bf16 v[164:167], a[8:11], a[128:131], v[164:167]\n"
        "v_cvt_pk_bf16_f32 v113, v114, v115\n"
        "v_max3_u32 v236, v236, v124, v125\n"
        "v_mfma_f32_16x16x32_bf16 v[164:167], a[4:7], a[124:127], v[164:167]\n"
        "ds_write_b64 v246, v[112:113] offset:768\n"
        "v_mfma_f32_16x16x32_bf16 v[164:167], a[0:3], a[120:123], v[164:167]\n"
        "v_cmp_lt_u32 vcc, s24, v236\n"
        "v_cvt_pk_bf16_f32 v242, v156, v160\n"
        "v_mfma_f32_16x16x32_bf16 v[180:183], a[0:3], a[168:171], 0\n"
        "s_cmp_lg_u64 vcc, 0\n"
        "s_addc_u32 s25, s25, s25\n"
        "v_mov_b32 v253, s25\n"
        "v_mfma_f32_16x16x32_bf16 v[180:183], a[4:7], a[172:175], v[180:183]\n"
        "ds_write_b32 v249, v253\n"
        "v_mfma_f32_16x16x32_bf16 v[180:183], a[8:11], a[176:179], v[180:183]\n"
        "v_cvt_pk_bf16_f32 v243, v164, v176\n"
        "v_cvt_pk_bf16_f32 v232, v157, v161\n"
        "v_mfma_f32_16x16x32_bf16 v[180:183], a[12:15], a[180:183], v[180:183]\n"
        "buffer_store_dwordx2 v[242:243], v250, s[16:19], s47 offen offset:0 sc1\n"
        "v_cvt_pk_bf16_f32 v233, v165, v177\n"
        "v_mfma_f32_16x16x32_bf16 v[180:183], a[16:19], a[184:187], v[180:183]\n"
        "v_cvt_pk_bf16_f32 v234, v158, v162\n"
        "v_cvt_pk_bf16_f32 v235, v166, v178\n"
        "v_mfma_f32_16x16x32_bf16 v[180:183], a[20:23], a[188:191], v[180:183]\n"
        "s_waitcnt lgkmcnt(0)\n"
        "s_barrier\n"
        "ds_read_b128 a[24:27], v240 offset:512\n"
        "ds_read_b128 a[28:31], v240 offset:4608\n"
        "v_mfma_f32_16x16x32_bf16 v[184:187], a[20:23], a[212:215], 0\n"
        "buffer_store_dwordx2 v[232:233], v250, s[16:19], s47 offen offset:384 sc1\n"
        "ds_read_b128 a[32:35], v240 offset:8704\n"
        "v_mfma_f32_16x16x32_bf16 v[184:187], a[16:19], a[208:211], v[184:187]\n"
        "ds_read_b128 a[36:39], v240 offset:12800\n"
        "ds_read_b128 a[40:43], v240 offset:16896\n"
        "v_mfma_f32_16x16x32_bf16 v[184:187], a[12:15], a[204:207], v[184:187]\n"
        "ds_read_b128 a[44:47], v240 offset:20992\n"
        "v_cvt_pk_bf16_f32 v242, v159, v163\n"
        "v_mfma_f32_16x16x32_bf16 v[184:187], a[8:11], a[200:203], v[184:187]\n"
        "buffer_store_dwordx2 v[234:235], v250, s[16:19], s47 offen offset:768 sc1\n"
        "v_cvt_pk_bf16_f32 v243, v167, v179\n"
        "v_mfma_f32_16x16x32_bf16 v[184:187], a[4:7], a[196:199], v[184:187]\n"
        "v_mfma_f32_16x16x32_bf16 v[184:187], a[0:3], a[192:195], v[184:187]\n"
        "s_waitcnt lgkmcnt(5)\n"
        "s_nop 1\n"
        "v_mfma_f32_16x16x32_bf16 v[52:55], a[24:27], a[168:171], 0\n"
        "s_waitcnt lgkmcnt(4)\n"
        "buffer_store_dwordx2 v[242:243], v250, s[16:19], s47 offen offset:1152 sc1\n"
        "s_nop 0\n"
        "v_mfma_f32_16x16x32_bf16 v[52:55], a[28:31], a[172:175], v[52:55]\n"
        "s_waitcnt lgkmcnt(3)\n"
        "ds_read_b128 v[60:63], v241\n"
        "s_nop 0\n"
        "v_mfma_f32_16x16x32_bf16 v[52:55], a[32:35], a[176:179], v[52:55]\n"
        "s_waitcnt lgkmcnt(3)\n"
        "v_cvt_pk_bf16_f32 v255, v180, v184\n"
        "v_cvt_pk_bf16_f32 v244, v181, v185\n"
        "v_mfma_f32_16x16x32_bf16 v[52:55], a[36:39], a[180:183], v[52:55]\n"
        "s_waitcnt lgkmcnt(2)\n"
        "buffer_store_dword v255, v238, s[16:19], s47 offen offset:0 sc1\n"
        "v_mfma_f32_16x16x32_bf16 v[52:55], a[40:43], a[184:187], v[52:55]\n"
        "s_waitcnt lgkmcnt(1)\n"
        "v_cvt_pk_bf16_f32 v239, v182, v186\n"
        "v_cvt_pk_bf16_f32 v255, v183, v187\n"
        "v_mfma_f32_16x16x32_bf16 v[52:55], a[44:47], a[188:191], v[52:55]\n"
        "v_mfma_f32_16x16x32_bf16 v[196:199], a[44:47], a[92:95], 0\n"
        "buffer_store_dword v244, v238, s[16:19], s47 offen offset:384 sc1\n"
        "v_mfma_f32_16x16x32_bf16 v[196:199], a[40:43], a[88:91], v[196:199]\n"
        "v_mfma_f32_16x16x32_bf16 v[196:199], a[36:39], a[84:87], v[196:199]\n"
        "v_mfma_f32_16x16x32_bf16 v[196:199], a[32:35], a[80:83], v[196:199]\n"
        "buffer_store_dword v239, v238, s[16:19], s47 offen offset:768 sc1\n"
        "v_mfma_f32_16x16x32_bf16 v[196:199], a[28:31], a[76:79], v[196:199]\n"
        "v_mfma_f32_16x16x32_bf16 v[196:199], a[24:27], a[72:75], v[196:199]\n"
        "v_mfma_f32_16x16x32_bf16 v[56:59], a[24:27], a[192:195], 0\n"
        "buffer_store_dword v255, v238, s[16:19], s47 offen offset:1152 sc1\n"
        "v_mfma_f32_16x16x32_bf16 v[56:59], a[28:31], a[196:199], v[56:59]\n"
        "v_mfma_f32_16x16x32_bf16 v[56:59], a[32:35], a[200:203], v[56:59]\n"
        "v_mfma_f32_16x16x32_bf16 v[56:59], a[36:39], a[204:207], v[56:59]\n"
        "s_waitcnt lgkmcnt(0)\n"
        "v_or3_b32 v60, v60, v61, v62\n"
        "v_or_b32 v60, v60, v63\n"
        "v_mfma_f32_16x16x32_bf16 v[56:59], a[40:43], a[208:211], v[56:59]\n"
        "s_nop 0\n"
        "v_readfirstlane_b32 s25, v60\n"
        "v_mfma_f32_16x16x32_bf16 v[56:59], a[44:47], a[212:215], v[56:59]\n"
        "s_nop 2\n"
        "v_mfma_f32_16x16x32_bf16 v[204:207], a[44:47], a[140:143], 0\n"
        "s_nop 2\n"
        "v_mfma_f32_16x16x32_bf16 v[204:207], a[40:43], a[136:139], v[204:207]\n"
        "v_cvt_pk_bf16_f32 v88, v52, v56\n"
        "v_cvt_pk_bf16_f32 v90, v53, v57\n"
        "v_mfma_f32_16x16x32_bf16 v[204:207], a[36:39], a[132:135], v[204:207]\n"
        "v_cvt_pk_bf16_f32 v92, v54, v58\n"
        "v_cvt_pk_bf16_f32 v94, v55, v59\n"
        "v_mfma_f32_16x16x32_bf16 v[204:207], a[32:35], a[128:131], v[204:207]\n"
        "buffer_store_dword v88, v238, s[16:19], s35 offen offset:0 sc1\n"
        "v_mfma_f32_16x16x32_bf16 v[204:207], a[28:31], a[124:127], v[204:207]\n"
        "v_mfma_f32_16x16x32_bf16 v[204:207], a[24:27], a[120:123], v[204:207]\n"
        "buffer_store_dword v90, v238, s[16:19], s35 offen offset:384 sc1\n"
        "v_mfma_f32_16x16x32_bf16 v[200:203], a[24:27], a[96:99], 0\n"
        "v_mfma_f32_16x16x32_bf16 v[200:203], a[28:31], a[100:103], v[200:203]\n"
        "buffer_store_dword v92, v238, s[16:19], s35 offen offset:768 sc1\n"
        "v_mfma_f32_16x16x32_bf16 v[200:203], a[32:35], a[104:107], v[200:203]\n"
        "v_mfma_f32_16x16x32_bf16 v[200:203], a[36:39], a[108:111], v[200:203]\n"
        "buffer_store_dword v94, v238, s[16:19], s35 offen offset:1152 sc1\n"
        "v_mfma_f32_16x16x32_bf16 v[200:203], a[40:43], a[112:115], v[200:203]\n"
        "v_mfma_f32_16x16x32_bf16 v[200:203], a[44:47], a[116:119], v[200:203]\n"
        "v_mfma_f32_16x16x32_bf16 v[48:51], a[44:47], a[164:167], 0\n"
        "v_mfma_f32_16x16x32_bf16 v[48:51], a[40:43], a[160:163], v[48:51]\n"
        "s_nop 2\n"
        "v_mfma_f32_16x16x32_bf16 v[48:51], a[36:39], a[156:159], v[48:51]\n"
        "s_nop 1\n"
        "v_cvt_pk_bf16_f32 v80, v196, v200\n"
        "v_mfma_f32_16x16x32_bf16 v[48:51], a[32:35], a[152:155], v[48:51]\n"
        "v_cvt_pk_bf16_f32 v82, v197, v201\n"
        "v_mfma_f32_16x16x32_bf16 v[48:51], a[28:31], a[148:151], v[48:51]\n"
        "v_cvt_pk_bf16_f32 v84, v198, v202\n"
        "v_mfma_f32_16x16x32_bf16 v[48:51], a[24:27], a[144:147], v[48:51]\n"
        "s_nop 2\n"
        "v_cvt_pk_bf16_f32 v86, v199, v203\n"
        "s_nop 3\n"
        "v_cvt_pk_bf16_f32 v81, v204, v48\n"
        "buffer_store_dwordx2 v[80:81], v250, s[16:19], s35 offen offset:0 sc1\n"
        "v_cvt_pk_bf16_f32 v83, v205, v49\n"
        "buffer_store_dwordx2 v[82:83], v250, s[16:19], s35 offen offset:384 sc1\n"
        "v_cvt_pk_bf16_f32 v85, v206, v50\n"
        "buffer_store_dwordx2 v[84:85], v250, s[16:19], s35 offen offset:768 sc1\n"
        "v_cvt_pk_bf16_f32 v87, v207, v51\n"
        "buffer_store_dwordx2 v[86:87], v250, s[16:19], s35 offen offset:1152 sc1\n"
        "s_cmp_lg_u32 s25, 0\n"
        "s_cbranch_scc1 30f\n"
        "s_endpgm\n"
        "30:\n"
        : "={s25}"(flg), "={s42}"(w), "+{s[0:1]}"(ka), "+{s2}"(bid), "+{v0}"(tid)
        : "s"(ldsb)
        : "v1", "v2", "v3", "v4", "v5", "v6", "v7", "v8", "v9", "v10", "v11", "v12", "v13", "v14", "v15", "v16", "v17", "v18", "v19", "v20", "v21", "v22", "v23", "v24", "v25", "v26", "v27", "v28", "v29", "v30", "v31", "v32", "v33", "v34", "v35", "v36", "v37", "v38", "v39", "v40", "v41", "v42", "v43", "v44", "v45", "v46", "v47", "v48", "v49", "v50", "v51", "v52", "v53", "v54", "v55", "v56", "v57", "v58", "v59", "v60", "v61", "v62", "v63", "v64", "v65", "v66", "v67", "v68", "v69", "v70", "v71", "v72", "v73", "v74", "v75", "v76", "v77", "v78", "v79", "v80", "v81", "v82", "v83", "v84", "v85", "v86", "v87", "v88", "v89", "v90", "v91", "v92", "v93", "v94", "v95", "v96", "v97", "v98", "v99", "v100", "v101", "v102", "v103", "v104", "v105", "v106", "v107", "v108", "v109", "v110", "v111", "v112", "v113", "v114", "v115", "v116", "v117", "v118", "v119", "v120", "v121", "v122", "v123", "v124", "v125", "v126", "v127", "v128", "v129", "v130", "v131", "v132", "v133", "v134", "v135", "v136", "v137", "v138", "v139", "v140", "v141", "v142", "v143", "v144", "v145", "v146", "v147", "v148", "v149", "v150", "v151", "v152", "v153", "v154", "v155", "v156", "v157", "v158", "v159", "v160", "v161", "v162", "v163", "v164", "v165", "v166", "v167", "v168", "v169", "v170", "v171", "v172", "v173", "v174", "v175", "v176", "v177", "v178", "v179", "v180", "v181", "v182", "v183", "v184", "v185", "v186", "v187", "v188", "v189", "v190", "v191", "v192", "v193", "v194", "v195", "v196", "v197", "v198", "v199", "v200", "v201", "v202", "v203", "v204", "v205", "v206", "v207", "v208", "v209", "v210", "v211", "v212", "v213", "v214", "v215", "v216", "v217", "v218", "v219", "v220", "v221", "v222", "v223", "v224", "v225", "v226", "v227", "v228", "v229", "v230", "v231", "v232", "v233", "v234", "v235", "v236", "v237", "v238", "v239", "v240", "v241", "v242", "v243", "v244", "v245", "v246", "v247", "v248", "v249", "v250", "v251", "v252", "v253", "v254", "v255", "a0", "a1", "a2", "a3", "a4", "a5", "a6", "a7", "a8", "a9", "a10", "a11", "a12", "a13", "a14", "a15", "a16", "a17", "a18", "a19", "a20", "a21", "a22", "a23", "a24", "a25", "a26", "a27", "a28", "a29", "a30", "a31", "a32", "a33", "a34", "a35", "a36", "a37", "a38", "a39", "a40", "a41", "a42", "a43", "a44", "a45", "a46", "a47", "a48", "a49", "a50", "a51", "a52", "a53", "a54", "a55", "a56", "a57", "a58", "a59", "a60", "a61", "a62", "a63", "a64", "a65", "a66", "a67", "a68", "a69", "a70", "a71", "a72", "a73", "a74", "a75", "a76", "a77", "a78", "a79", "a80", "a81", "a82", "a83", "a84", "a85", "a86", "a87", "a88", "a89", "a90", "a91", "a92", "a93", "a94", "a95", "a96", "a97", "a98", "a99", "a100", "a101", "a102", "a103", "a104", "a105", "a106", "a107", "a108", "a109", "a110", "a111", "a112", "a113", "a114", "a115", "a116", "a117", "a118", "a119", "a120", "a121", "a122", "a123", "a124", "a125", "a126", "a127", "a128", "a129", "a130", "a131", "a132", "a133", "a134", "a135", "a136", "a137", "a138", "a139", "a140", "a141", "a142", "a143", "a144", "a145", "a146", "a147", "a148", "a149", "a150", "a151", "a152", "a153", "a154", "a155", "a156", "a157", "a158", "a159", "a160", "a161", "a162", "a163", "a164", "a165", "a166", "a167", "a168", "a169", "a170", "a171", "a172", "a173", "a174", "a175", "a176", "a177", "a178", "a179", "a180", "a181", "a182", "a183", "a184", "a185", "a186", "a187", "a188", "a189", "a190", "a191", "a192", "a193", "a194", "a195", "a196", "a197", "a198", "a199", "a200", "a201", "a202", "a203", "a204", "a205", "a206", "a207", "a208", "a209", "a210", "a211", "a212", "a213", "a214", "a215", "a216", "a217", "a218", "a219", "a220", "a221", "a222", "a223", "a224", "a225", "a226", "a227", "a228", "a229", "a230", "a231", "a232", "a233", "a234", "a235", "a236", "a237", "a238", "a239", "a240", "a241", "a242", "a243", "a244", "a245", "a246", "a247", "a248", "a249", "a250", "a251", "a252", "a253", "a254", "a255", "s8", "s9", "s10", "s11", "s12", "s13", "s14", "s15", "s16", "s17", "s18", "s19", "s20", "s21", "s22", "s23", "s24", "s26", "s27", "s28", "s29", "s30", "s31", "s34", "s35", "s36", "s37", "s38", "s39", "s40", "s41", "s43", "s44", "s45", "s46", "s47", "s48", "s49", "s50", "s51", "s52", "s53", "s54", "s55", "s56", "s57", "s58", "s59", "s60", "s61", "s62", "s63", "s64", "s65", "s66", "s67", "s68", "s69", "s70", "s71", "s72", "s73", "s74", "s75", "s76", "s77", "s78", "s79", "s80", "s81", "s82", "s83", "vcc", "scc", "memory");
    if (flg) {
        unsigned ln;
        asm volatile("s_waitcnt vmcnt(0)\n v_mbcnt_lo_u32_b32 %0, -1, 0\n v_mbcnt_hi_u32_b32 %0, -1, %0" : "=v"(ln) :: "memory");
        const unsigned t = w * 64u + ln, wx = bid & 63u, wy0 = bid >> 6;
        typedef const char* __attribute__((address_space(4))) const* kargp;
        const char *x = ((kargp)ka)[0], *wqkv = ((kargp)ka)[1], *wo = ((kargp)ka)[2], *rpb = ((kargp)ka)[3], *rpi = ((kargp)ka)[4],
                   *mask = ((kargp)ka)[5];
        char* out = (char*)((kargp)ka)[6];
        unsigned short* xs = (unsigned short*)smem;
        unsigned short* qs = xs + 64 * 192;
        const unsigned short* W1 = (const unsigned short*)wqkv;
        const unsigned short* W2 = (const unsigned short*)wo;
        for (unsigned i = 0; i < 16u; ++i) {
            if (!((flg >> (15u - i)) & 1u)) continue;
            const unsigned wy = wy0 + 4u * i;
            __syncthreads();
            for (unsigned e = t; e < 64u * 96u; e += 256u) {
                const unsigned tok = e / 96u, c2 = e % 96u;
                const unsigned pix = ((8u * wy + 4u + (tok >> 3)) & 511u) * 512u + ((8u * wx + 4u + (tok & 7u)) & 511u);
                ((unsigned*)xs)[e] = ((const unsigned*)x)[pix * 96u + c2];
            }
            __syncthreads();
            for (unsigned col = t; col < 576u; col += 256u)
                for (unsigned tb = 0; tb < 64u; tb += 8u) {
                    float a[8] = {0.f, 0.f, 0.f, 0.f, 0.f, 0.f, 0.f, 0.f};
                    for (unsigned c = 0; c < 192u; ++c) {
                        const float wt = bf2f(W1[c * 576u + col]);
#pragma unroll
                        for (int j = 0; j < 8; ++j) a[j] += bf2f(xs[(tb + j) * 192u + c]) * wt;
                    }
#pragma unroll
                    for (int j = 0; j < 8; ++j) qs[(tb + j) * 578u + col] = f2bf(a[j]);
                }
            __syncthreads();
            {
                float qv[48], o[48];
#pragma unroll
                for (int d = 0; d < 48; ++d) { qv[d] = bf2f(qs[ln * 578u + w * 48u + d]); o[d] = 0.f; }
                const unsigned short* mrow = (const unsigned short*)mask + (size_t)(wy * 64u + wx) * 4096u + ln * 64u;
                float mx = -__builtin_inff(), l = 0.f;
                for (int pass = 0; pass < 2; ++pass)
                    for (unsigned k = 0; k < 64u; ++k) {
                        float s = 0.f;
#pragma unroll
                        for (int d = 0; d < 48; ++d) s += qv[d] * bf2f(qs[k * 578u + 192u + w * 48u + d]);
                        s = s * 0.14433756729740643f + bf2f(((const unsigned short*)rpb)[((const unsigned*)rpi)[ln * 64u + k] * 4u + w]) + bf2f(mrow[k]);
                        if (pass == 0) mx = fmaxf(mx, s);
                        else {
                            const float p = __expf(s - mx);
                            l += p;
#pragma unroll
                            for (int d = 0; d < 48; ++d) o[d] += p * bf2f(qs[k * 578u + 384u + w * 48u + d]);
                        }
                    }
#pragma unroll
                for (int d = 0; d < 48; ++d) xs[ln * 192u + w * 48u + d] = f2bf(o[d] / l);
            }
            __syncthreads();
            if (t < 192u)
                for (unsigned tb = 0; tb < 64u; tb += 8u) {
                    float a[8] = {0.f, 0.f, 0.f, 0.f, 0.f, 0.f, 0.f, 0.f};
                    for (unsigned c = 0; c < 192u; ++c) {
                        const float wt = bf2f(W2[c * 192u + t]);
#pragma unroll
                        for (int j = 0; j < 8; ++j) a[j] += bf2f(xs[(tb + j) * 192u + c]) * wt;
                    }
                    const unsigned row = (8u * wy + 4u + (tb >> 3)) & 511u;
#pragma unroll
                    for (int j = 0; j < 8; ++j)
                        ((unsigned short*)out)[(row * 512u + ((8u * wx + 4u + j) & 511u)) * 192u + t] = f2bf(a[j]);
                }
        }
    }
}


extern "C" void kernel_launch(void* const* d_in, const int* in_sizes, int n_in, void* d_out, int out_size, void* d_ws,
                              size_t ws_size, hipStream_t stream) {
    win_attn_kernel<<<256, 256, 0, stream>>>((const char*)d_in[0], (const char*)d_in[1], (const char*)d_in[2],
                                             (const char*)d_in[3], (const char*)d_in[4], (const char*)d_in[5],
                                             (char*)d_out, (char*)d_ws);

}
```

```cpp
#include <hip/hip_runtime.h>
#include <hip/hip_bf16.h>

typedef unsigned int u32x4 __attribute__((ext_vector_type(4)));

static __device__ __forceinline__ float bf2f(unsigned short u) { return __uint_as_float((unsigned)u << 16); }
static __device__ __forceinline__ unsigned short f2bf(float f) { return __builtin_bit_cast(unsigned short, (__bf16)f); }

__global__ __launch_bounds__(256, 1) __attribute__((aligned(4096))) void win_attn_kernel(const char* x_, const char* wqkv_, const char* wo_, const char* rpb_,
                                                          const char* rpi_, const char* mask_, char* out_, char* ws) {
    __shared__ __attribute__((aligned(16))) char smem[163840];
    const void* ka = (const void*)__builtin_amdgcn_kernarg_segment_ptr();
    unsigned bid = blockIdx.x, tid = threadIdx.x, flg, w;
    unsigned ldsb = (unsigned)(size_t)smem;
    asm volatile(
        "s_load_dwordx16 s[68:83], s[0:1], 0\n"
        "v_readfirstlane_b32 s42, v0\n"
        "v_and_b32 v1, 63, v0\n"
        "s_lshr_b32 s31, s2, 1\n"
        "s_lshr_b32 s42, s42, 6\n"
        "s_and_b32 s31, s31, 0x7c\n"
        "v_lshlrev_b32 v2, 4, v1\n"
        "s_add_u32 s31, s31, s42\n"
        "s_mul_i32 s35, s31, 320\n"
        "v_min_u32 v10, 4, v1\n"
        "s_getpc_b64 s[66:67]\n"
        "v_lshl_add_u32 v10, v10, 6, s35\n"
        "global_load_dword v60, v10, s[66:67]\n"
        "s_lshl_b32 s50, s31, 6\n"
        "s_mul_i32 s50, s50, 27\n"
        "v_min_u32 v136, 26, v1\n"
        "v_lshl_add_u32 v136, v136, 6, s50\n"
        "v_mul_u32_u24 v11, 171, v1\n"
        "v_lshrrev_b32 v4, 10, v11\n"
        "v_bfe_u32 v7, v11, 13, 1\n"
        "v_add_u32 v11, 10944, v11\n"
        "v_lshrrev_b32 v5, 10, v11\n"
        "v_bfe_u32 v8, v11, 13, 1\n"
        "v_add_u32 v11, 10944, v11\n"
        "v_lshrrev_b32 v6, 10, v11\n"
        "v_bfe_u32 v9, v11, 13, 1\n"
        "s_mul_i32 s40, s42, 3072\n"
        "s_mov_b32 s10, 100663296\n"
        "s_mov_b32 s11, 0x20000\n"
        "s_mov_b32 s58, 16384\n"
        "s_mov_b32 s59, 0x20000\n"
        "s_mov_b32 s62, 1800\n"
        "s_mov_b32 s63, 0x20000\n"
        "s_mov_b32 s22, 221184\n"
        "s_mov_b32 s23, 0x20000\n"
        "s_and_b32 s51, s42, 1\n"
        "v_lshlrev_b32 v134, 3, v0\n"
        "s_lshl_b32 s35, s51, 7\n"
        "v_and_b32 v10, 48, v1\n"
        "v_xor_b32 v131, s35, v2\n"
        "s_lshl_b32 s46, s42, 12\n"
        "v_xor_b32 v131, v131, v10\n"
        "v_xor_b32 v132, 64, v131\n"
        "s_and_b32 s44, s2, 63\n"
        "s_lshl3_add_u32 s44, s44, 4\n"
        "s_lshl_b32 s35, s42, 5\n"
        "s_movk_i32 s41, 384\n"
        "s_movk_i32 s45, -384\n"
        "v_lshrrev_b32 v10, 2, v4\n"
        "v_mad_i32_i24 v11, v10, s45, v2\n"
        "v_add_u32 v10, s44, v10\n"
        "v_lshl_or_b32 v242, v7, 4, s35\n"
        "v_and_b32 v10, 511, v10\n"
        "v_xor_b32 v11, v11, v242\n"
        "v_mad_u32_u24 v242, v10, s41, v11\n"
        "v_lshrrev_b32 v10, 2, v5\n"
        "v_mad_i32_i24 v11, v10, s45, v2\n"
        "v_add_u32 v10, s44, v10\n"
        "v_add_u32 v11, 1024, v11\n"
        "v_lshl_or_b32 v243, v8, 4, s35\n"
        "v_and_b32 v10, 511, v10\n"
        "v_xor_b32 v11, v11, v243\n"
        "v_mad_u32_u24 v243, v10, s41, v11\n"
        "v_lshrrev_b32 v10, 2, v6\n"
        "v_mad_i32_i24 v11, v10, s45, v2\n"
        "v_add_u32 v10, s44, v10\n"
        "v_add_u32 v11, 2048, v11\n"
        "v_lshl_or_b32 v244, v9, 4, s35\n"
        "v_and_b32 v10, 511, v10\n"
        "v_xor_b32 v11, v11, v244\n"
        "v_mad_u32_u24 v244, v10, s41, v11\n"
        "s_mul_i32 s43, s44, 384\n"
        "s_add_u32 s44, s44, 4\n"
        "s_and_b32 s44, s44, 511\n"
        "s_mul_i32 s44, s44, 384\n"
        "s_lshr_b32 s37, s2, 6\n"
        "s_lshl3_add_u32 s47, s37, s42\n"
        "s_add_u32 s47, s47, 68\n"
        "s_mul_i32 s47, s47, 196608\n"
        "s_lshl1_add_u32 s37, s37, s51\n"
        "s_add_u32 s37, s37, 1\n"
        "s_mul_i32 s37, s37, 786432\n"
        "s_mul_i32 s65, s42, 18432\n"
        "s_mov_b32 s14, 33554432\n"
        "s_mov_b32 s15, 0x20000\n"
        "s_mul_i32 s35, s42, 96\n"
        "s_movk_i32 s31, 1056\n"
        "s_movk_i32 s45, 4608\n"
        "v_add_u32 v3, s35, v2\n"
        "s_waitcnt lgkmcnt(0)\n"
        "s_mov_b64 s[60:61], s[74:75]\n"
        "s_mov_b64 s[20:21], s[70:71]\n"
        "s_mov_b64 s[56:57], s[76:77]\n"
        "s_mov_b64 s[8:9], s[68:69]\n"
        "buffer_load_dwordx2 v[144:145], v134, s[60:63], 0 offen\n"
        "s_add_u32 m0, s46, 122880\n"
        "s_mov_b32 s64, s46\n"
        "buffer_load_dword v136, s[20:23], 0 offen lds\n"
        "s_mov_b64 s[52:53], s[72:73]\n"
        "s_mov_b64 s[12:13], s[78:79]\n"
        "buffer_load_dwordx4 v131, s[56:59], s64 offen lds\n"
        "s_mov_b64 s[16:17], s[80:81]\n"
        "v_bfe_u32 v12, v4, 2, 1\n"
        "v_mad_u32_u24 v128, v4, s31, v3\n"
        "v_sub_u32 v12, v12, v7\n"
        "v_mad_i32_i24 v128, v12, s45, v128\n"
        "v_bfe_u32 v13, v5, 2, 1\n"
        "buffer_load_dwordx4 v131, s[56:59], s64 offen offset:1024 lds\n"
        "v_mad_u32_u24 v129, v5, s31, v3\n"
        "v_sub_u32 v13, v13, v8\n"
        "v_mad_i32_i24 v129, v13, s45, v129\n"
        "v_bfe_u32 v14, v6, 2, 1\n"
        "v_mad_u32_u24 v130, v6, s31, v3\n"
        "v_sub_u32 v14, v14, v9\n"
        "v_mad_i32_i24 v130, v14, s45, v130\n"
        "s_lshl_b32 s48, s42, 11\n"
        "s_lshl_b32 s36, s2, 13\n"
        "v_and_b32 v19, 16, v1\n"
        "buffer_load_dwordx4 v132, s[56:59], s64 offen offset:2048 lds\n"
        "s_add_u32 s48, s48, 155648\n"
        "s_add_u32 s36, s36, s48\n"
        "s_add_u32 s36, s36, 4038656\n"
        "s_lshl_b32 s35, s51, 6\n"
        "v_xor_b32 v249, v2, v19\n"
        "v_xor_b32 v249, s35, v249\n"
        "v_xor_b32 v253, 32, v249\n"
        "v_and_b32 v15, 15, v1\n"
        "v_lshrrev_b32 v16, 4, v1\n"
        "v_and_b32 v17, 12, v1\n"
        "buffer_load_dwordx4 v132, s[56:59], s64 offen offset:3072 lds\n"
        "v_and_b32 v18, 0xf0, v2\n"
        "v_add_u32 v17, v17, v15\n"
        "v_bfe_u32 v10, v1, 2, 3\n"
        "v_lshrrev_b32 v11, 5, v1\n"
        "v_lshl_or_b32 v10, v11, 4, v10\n"
        "v_and_b32 v255, 3, v1\n"
        "v_mul_u32_u24 v10, 96, v10\n"
        "v_lshl_add_u32 v255, v255, 3, s65\n"
        "v_add_u32 v255, v255, v10\n"
        "v_lshrrev_b32 v20, 1, v15\n"
        "s_add_u32 m0, s40, 73728\n"
        "s_sub_u32 s64, s47, 12582912\n"
        "buffer_load_dwordx4 v242, s[8:11], s64 offen lds\n"
        "v_xor_b32 v20, v20, v16\n"
        "v_lshlrev_b32 v20, 4, v20\n"
        "v_mad_u32_u24 v240, v17, s41, v20\n"
        "v_lshl_or_b32 v251, v17, 7, v20\n"
        "v_add_u32 v240, 73728, v240\n"
        "v_add_u32 v251, 155648, v251\n"
        "v_xor_b32 v241, 64, v240\n"
        "v_xor_b32 v252, 64, v251\n"
        "s_lshl_b32 s67, s42, 10\n"
        "s_add_u32 s67, s67, 139264\n"
        "s_add_u32 m0, s40, 74752\n"
        "s_nop 0\n"
        "buffer_load_dwordx4 v243, s[8:11], s64 offen lds\n"
        "v_lshlrev_b32 v135, 2, v0\n"
        "v_add_u32 v135, 139264, v135\n"
        "v_lshlrev_b32 v11, 5, v16\n"
        "v_xor_b32 v11, v11, v18\n"
        "v_lshl_or_b32 v11, v17, 8, v11\n"
        "v_add_u32 v140, 122880, v11\n"
        "v_xor_b32 v141, 16, v140\n"
        "v_xor_b32 v142, 128, v140\n"
        "v_xor_b32 v143, 144, v140\n"
        "v_lshrrev_b32 v10, 2, v15\n"
        "s_add_u32 m0, s40, 75776\n"
        "s_nop 0\n"
        "buffer_load_dwordx4 v244, s[8:11], s64 offen lds\n"
        "v_cmp_eq_u32 vcc, v10, v16\n"
        "v_mov_b32 v11, 0x3fb9\n"
        "v_mov_b32 v189, 0\n"
        "v_and_b32 v10, 48, v2\n"
        "v_cndmask_b32 v188, 0, v11, vcc\n"
        "v_lshlrev_b64 v[188:189], v10, v[188:189]\n"
        "v_cmp_gt_u32 vcc, 32, v1\n"
        "v_mov_b32 v11, 15957\n"
        "v_lshrrev_b32 v149, 2, v15\n"
        "v_lshrrev_b32 v151, 3, v19\n"
        "v_mov_b32 v148, 14702\n"
        "v_cndmask_b32 v11, v148, v11, vcc\n"
        "v_xor_b32 v151, v149, v151\n"
        "v_cmp_eq_u32 vcc, 0, v151\n"
        "v_mov_b32 v149, 0\n"
        "v_cndmask_b32 v148, 0, v11, vcc\n"
        "v_lshlrev_b64 v[148:149], v10, v[148:149]\n"
        "v_cmp_eq_u32 vcc, 1, v151\n"
        "v_mov_b32 v151, 0\n"
        "v_cndmask_b32 v150, 0, v11, vcc\n"
        "v_lshlrev_b64 v[150:151], v10, v[150:151]\n"
        "v_and_b32 v10, 8, v1\n"
        "v_add_u32 v10, v10, v15\n"
        "v_mul_u32_u24 v10, 96, v10\n"
        "v_add3_u32 v137, v10, v19, s65\n"
        "v_and_b32 v237, 1, v1\n"
        "v_mul_u32_u24 v237, 0xaaaa, v237\n"
        "v_mov_b64 v[170:171], v[188:189]\n"
        "v_mov_b64 v[210:211], v[188:189]\n"
        "v_mov_b64 v[228:229], v[188:189]\n"
        "v_add_u32 v237, 0x4444, v237\n"
        "v_mov_b32 v11, 0x3f803f80\n"
        "v_accvgpr_write_b32 a252, v11\n"
        "v_accvgpr_write_b32 a253, v11\n"
        "v_accvgpr_write_b32 a254, v11\n"
        "v_accvgpr_write_b32 a255, v11\n"
        "v_lshl_or_b32 v10, v16, 10, v18\n"
        "s_lshl_b32 s31, s42, 12\n"
        "s_add_u32 s31, s31, 73728\n"
        "s_lshl_b32 s35, s51, 9\n"
        "s_add_u32 s35, s35, 73728\n"
        "v_add_u32 v245, s31, v10\n"
        "s_sub_u32 s50, s35, s31\n"
        "v_lshrrev_b32 v10, 5, v1\n"
        "v_lshrrev_b32 v11, 1, v19\n"
        "s_sub_u32 s31, s48, 65536\n"
        "v_lshl_or_b32 v10, v10, 10, v18\n"
        "v_add3_u32 v246, v10, v11, s31\n"
        "v_mul_u32_u24 v10, 384, v16\n"
        "s_movk_i32 s45, 1728\n"
        "v_lshrrev_b32 v235, 2, v15\n"
        "v_lshrrev_b32 v254, 1, v15\n"
        "v_and_b32 v11, 1, v1\n"
        "v_mad_u32_u24 v235, v235, 48, v10\n"
        "v_and_b32 v248, 1, v254\n"
        "v_mad_u32_u24 v235, v248, s45, v235\n"
        "v_mul_u32_u24 v248, 36, v11\n"
        "v_lshlrev_b32 v11, 5, v11\n"
        "v_xor_b32 v254, v254, v248\n"
        "v_add3_u32 v235, v235, v11, s65\n"
        "v_mad_u32_u24 v254, v254, 48, v10\n"
        "v_add3_u32 v254, v254, 16, s65\n"
        "s_lshr_b32 s31, s42, 1\n"
        "s_mul_i32 s35, s31, 36864\n"
        "s_movk_i32 s45, 18048\n"
        "v_mad_u32_u24 v10, v15, 48, v10\n"
        "v_lshrrev_b32 v248, 3, v15\n"
        "v_mad_u32_u24 v10, v248, s45, v10\n"
        "v_add_u32 v248, s35, v10\n"
        "s_lshr_b32 s49, s42, 1\n"
        "s_mul_i32 s49, s49, -192\n"
        "s_add_u32 s49, s49, 128\n"
        "s_mul_i32 s31, s31, 256\n"
        "v_mul_u32_u24 v10, 196608, v16\n"
        "v_mul_u32_u24 v11, 8, v15\n"
        "v_add3_u32 v250, v10, v11, s31\n"
        "s_lshl_b32 s35, s51, 6\n"
        "s_add_u32 s66, s31, s35\n"
        "s_lshl_b32 s35, s42, 5\n"
        "s_sub_u32 s35, s35, s66\n"
        "s_add_u32 s27, s35, 64\n"
        "s_movk_i32 s31, 288\n"
        "s_movk_i32 s35, 3072\n"
        "s_movk_i32 s41, 1536\n"
        "s_movk_i32 s45, 12288\n"
        "v_mad_u32_u24 v10, v4, s31, v2\n"
        "v_bfe_u32 v11, v10, 6, 1\n"
        "v_add_u32 v10, s66, v10\n"
        "v_mad_i32_i24 v10, v11, s27, v10\n"
        "v_lshrrev_b32 v11, 4, v4\n"
        "v_mad_i32_i24 v238, v12, s41, v10\n"
        "v_sub_u32 v7, v7, v11\n"
        "v_mad_u32_u24 v238, v11, s45, v238\n"
        "v_mad_i32_i24 v232, v7, s35, v10\n"
        "v_mad_u32_u24 v10, v5, s31, v2\n"
        "v_bfe_u32 v11, v10, 6, 1\n"
        "v_add_u32 v10, s66, v10\n"
        "v_mad_i32_i24 v10, v11, s27, v10\n"
        "v_lshrrev_b32 v11, 4, v5\n"
        "v_mad_i32_i24 v239, v13, s41, v10\n"
        "v_sub_u32 v8, v8, v11\n"
        "v_mad_u32_u24 v239, v11, s45, v239\n"
        "v_mad_i32_i24 v233, v8, s35, v10\n"
        "v_mad_u32_u24 v10, v6, s31, v2\n"
        "v_bfe_u32 v11, v10, 6, 1\n"
        "v_add_u32 v10, s66, v10\n"
        "v_mad_i32_i24 v10, v11, s27, v10\n"
        "v_lshrrev_b32 v11, 4, v6\n"
        "v_mad_i32_i24 v247, v14, s41, v10\n"
        "v_sub_u32 v9, v9, v11\n"
        "v_mad_u32_u24 v247, v11, s45, v247\n"
        "v_mad_i32_i24 v234, v9, s35, v10\n"
        "s_mov_b32 s26, 0x3fb8aa3b\n"
        "s_mov_b32 s24, 0x5d800000\n"
        "s_mov_b32 s27, 14\n"
        "s_mov_b32 s28, 98304\n"
        "s_mov_b32 s29, 73728\n"
        "s_mov_b32 s30, 122880\n"
        "s_mov_b32 s45, s50\n"
        "s_mov_b32 s25, 0\n"
        "s_mov_b32 s54, 73728\n"
        "s_mov_b32 s55, 0x20000\n"
        "s_mov_b32 s18, 100663296\n"
        "s_mov_b32 s19, 0x20000\n"
        "s_mov_b32 s56, 36864\n"
        "s_mov_b32 s57, 73728\n"
        "s_mov_b32 s58, 110592\n"
        "s_mov_b32 s59, 147456\n"
        "s_mov_b32 s60, 184320\n"
        "s_mov_b32 s61, 18432\n"
        "s_mov_b32 s62, 55296\n"
        "s_mov_b32 s63, 12288\n"
        "s_mov_b32 s34, 49152\n"
        "s_add_u32 m0, s40, 86016\n"
        "s_sub_u32 s64, s47, 11796480\n"
        "buffer_load_dwordx4 v242, s[8:11], s64 offen lds\n"
        "s_waitcnt vmcnt(9)\n"
        "v_and_b32 v146, 0xffff0000, v144\n"
        "v_and_b32 v147, 0xffff0000, v145\n"
        "v_lshlrev_b32 v144, 16, v144\n"
        "v_lshlrev_b32 v145, 16, v145\n"
        "s_add_u32 m0, s40, 87040\n"
        "v_mul_f32 v146, s26, v146\n"
        "buffer_load_dwordx4 v243, s[8:11], s64 offen lds\n"
        "v_mul_f32 v147, s26, v147\n"
        "v_mul_f32 v144, s26, v144\n"
        "v_mul_f32 v145, s26, v145\n"
        "ds_write2st64_b32 v135, v144, v146 offset0:0 offset1:4\n"
        "ds_write2st64_b32 v135, v145, v147 offset0:8 offset1:12\n"
        "s_cmp_eq_u32 s42, 0\n"
        "s_cbranch_scc1 41f\n"
        "s_cmp_eq_u32 s42, 1\n"
        "s_cbranch_scc1 42f\n"
        "s_cmp_eq_u32 s42, 2\n"
        "s_cbranch_scc1 43f\n"
        "s_cmp_eq_u32 s42, 3\n"
        "s_cbranch_scc1 44f\n"
        "60:\n"
        "s_add_u32 m0, s40, 88064\n"
        "s_nop 0\n"
        "buffer_load_dwordx4 v244, s[8:11], s64 offen lds\n"
        "s_waitcnt lgkmcnt(0)\n"
        "s_waitcnt vmcnt(6)\n"
        "s_barrier\n"
        "s_add_u32 m0, s65, 0\n"
        "ds_read_b128 v[0:3], v140 offset:0\n"
        "buffer_load_dwordx4 v128, s[20:23], 0 offen lds\n"
        "buffer_load_dwordx4 v129, s[20:23], 0 offen offset:1024 lds\n"
        "ds_read_b128 v[4:7], v141 offset:0\n"
        "ds_read_b128 v[16:19], v140 offset:1024\n"
        "ds_read_b128 v[20:23], v141 offset:1024\n"
        "ds_read_b128 v[8:11], v142 offset:0\n"
        "ds_read_b128 v[24:27], v142 offset:1024\n"
        "ds_read_b128 v[12:15], v143 offset:0\n"
        "ds_read_b128 v[28:31], v143 offset:1024\n"
        "ds_read_b128 v[32:35], v140 offset:8192\n"
        "ds_read_b128 v[36:39], v141 offset:8192\n"
        "ds_read_b128 v[40:43], v142 offset:8192\n"
        "s_cmp_eq_u32 s42, 0\n"
        "s_cbranch_scc1 51f\n"
        "s_cmp_eq_u32 s42, 1\n"
        "s_cbranch_scc1 48f\n"
        "s_cmp_eq_u32 s42, 2\n"
        "s_cbranch_scc1 45f\n"
        "s_cmp_eq_u32 s42, 3\n"
        "s_cbranch_scc1 47f\n"
        "61:\n"
        "buffer_load_dwordx4 v130, s[20:23], 0 offen offset:2048 lds\n"
        "s_add_u32 m0, s65, 2688\n"
        "s_waitcnt lgkmcnt(10)\n"
        "v_lshl_add_u32 v0, v0, 2, s67\n"
        "buffer_load_dwordx4 v128, s[20:23], 0 offen offset:384 lds\n"
        "v_lshl_add_u32 v1, v1, 2, s67\n"
        "v_lshl_add_u32 v2, v2, 2, s67\n"
        "v_lshl_add_u32 v3, v3, 2, s67\n"
        "s_waitcnt lgkmcnt(9)\n"
        "v_lshl_add_u32 v4, v4, 2, s67\n"
        "v_lshl_add_u32 v5, v5, 2, s67\n"
        "v_lshl_add_u32 v6, v6, 2, s67\n"
        "v_lshl_add_u32 v7, v7, 2, s67\n"
        "s_waitcnt lgkmcnt(8)\n"
        "v_lshl_add_u32 v16, v16, 2, s67\n"
        "v_lshl_add_u32 v17, v17, 2, s67\n"
        "v_lshl_add_u32 v18, v18, 2, s67\n"
        "v_lshl_add_u32 v19, v19, 2, s67\n"
        "buffer_load_dwordx4 v129, s[20:23], 0 offen offset:1408 lds\n"
        "ds_read_b32 v0, v0\n"
        "ds_read_b32 v1, v1\n"
        "ds_read_b32 v2, v2\n"
        "ds_read_b32 v3, v3\n"
        "ds_read_b32 v4, v4\n"
        "ds_read_b32 v5, v5\n"
        "ds_read_b32 v6, v6\n"
        "ds_read_b32 v7, v7\n"
        "ds_read_b32 v16, v16\n"
        "ds_read_b32 v17, v17\n"
        "ds_read_b32 v18, v18\n"
        "ds_read_b32 v19, v19\n"
        "buffer_load_dwordx4 v130, s[20:23], 0 offen offset:2432 lds\n"
        "s_waitcnt lgkmcnt(15)\n"
        "v_lshl_add_u32 v20, v20, 2, s67\n"
        "v_lshl_add_u32 v21, v21, 2, s67\n"
        "v_lshl_add_u32 v22, v22, 2, s67\n"
        "v_lshl_add_u32 v23, v23, 2, s67\n"
        "v_lshl_add_u32 v8, v8, 2, s67\n"
        "v_lshl_add_u32 v9, v9, 2, s67\n"
        "v_lshl_add_u32 v10, v10, 2, s67\n"
        "v_lshl_add_u32 v11, v11, 2, s67\n"
        "v_lshl_add_u32 v24, v24, 2, s67\n"
        "v_lshl_add_u32 v25, v25, 2, s67\n"
        "v_lshl_add_u32 v26, v26, 2, s67\n"
        "v_lshl_add_u32 v27, v27, 2, s67\n"
        "ds_read_b32 v20, v20\n"
        "ds_read_b32 v21, v21\n"
        "ds_read_b32 v22, v22\n"
        "ds_read_b32 v23, v23\n"
        "ds_read_b32 v8, v8\n"
        "ds_read_b32 v9, v9\n"
        "ds_read_b32 v10, v10\n"
        "ds_read_b32 v11, v11\n"
        "ds_read_b32 v24, v24\n"
        "ds_read_b32 v25, v25\n"
        "ds_read_b32 v26, v26\n"
        "ds_read_b32 v27, v27\n"
        "s_add_u32 m0, s65, 6144\n"
        "v_lshl_add_u32 v12, v12, 2, s67\n"
        "buffer_load_dwordx4 v128, s[20:23], s56 offen lds\n"
        "v_lshl_add_u32 v13, v13, 2, s67\n"
        "v_lshl_add_u32 v14, v14, 2, s67\n"
        "v_lshl_add_u32 v15, v15, 2, s67\n"
        "v_lshl_add_u32 v28, v28, 2, s67\n"
        "v_lshl_add_u32 v29, v29, 2, s67\n"
        "v_lshl_add_u32 v30, v30, 2, s67\n"
        "v_lshl_add_u32 v31, v31, 2, s67\n"
        "s_waitcnt lgkmcnt(15)\n"
        "v_lshl_add_u32 v32, v32, 2, s67\n"
        "v_lshl_add_u32 v33, v33, 2, s67\n"
        "v_lshl_add_u32 v34, v34, 2, s67\n"
        "v_lshl_add_u32 v35, v35, 2, s67\n"
        "ds_read_b32 v12, v12\n"
        "ds_read_b32 v13, v13\n"
        "ds_read_b32 v14, v14\n"
        "ds_read_b32 v15, v15\n"
        "ds_read_b32 v28, v28\n"
        "ds_read_b32 v29, v29\n"
        "ds_read_b32 v30, v30\n"
        "ds_read_b32 v31, v31\n"
        "ds_read_b32 v32, v32\n"
        "ds_read_b32 v33, v33\n"
        "ds_read_b32 v34, v34\n"
        "ds_read_b32 v35, v35\n"
        "buffer_load_dwordx4 v129, s[20:23], s56 offen offset:1024 lds\n"
        "v_lshl_add_u32 v36, v36, 2, s67\n"
        "v_lshl_add_u32 v37, v37, 2, s67\n"
        "v_lshl_add_u32 v38, v38, 2, s67\n"
        "v_lshl_add_u32 v39, v39, 2, s67\n"
        "v_lshl_add_u32 v40, v40, 2, s67\n"
        "v_lshl_add_u32 v41, v41, 2, s67\n"
        "v_lshl_add_u32 v42, v42, 2, s67\n"
        "v_lshl_add_u32 v43, v43, 2, s67\n"
        "ds_read_b32 v36, v36\n"
        "ds_read_b32 v37, v37\n"
        "ds_read_b32 v38, v38\n"
        "ds_read_b32 v39, v39\n"
        "ds_read_b32 v40, v40\n"
        "ds_read_b32 v41, v41\n"
        "ds_read_b32 v42, v42\n"
        "ds_read_b32 v43, v43\n"
        "buffer_load_dwordx4 v130, s[20:23], s56 offen offset:2048 lds\n"
        "s_waitcnt vmcnt(9)\n"
        "s_barrier\n"
        "s_waitcnt vmcnt(6)\n"
        "ds_read_b128 a[212:215], v137 offset:0\n"
        "ds_read_b128 a[208:211], v137 offset:768\n"
        "ds_read_b128 a[204:207], v137 offset:32\n"
        "ds_read_b128 a[200:203], v137 offset:800\n"
        "ds_read_b128 a[196:199], v137 offset:64\n"
        "ds_read_b128 a[192:195], v137 offset:832\n"
        "s_add_u32 m0, s65, 8832\n"
        "s_nop 0\n"
        "buffer_load_dwordx4 v128, s[20:23], s56 offen offset:384 lds\n"
        "s_waitcnt lgkmcnt(5)\n"
        "v_mfma_f32_16x16x32_bf16 v[112:115], a[212:215], v[148:151], 0\n"
        "s_waitcnt lgkmcnt(4)\n"
        "ds_read_b128 a[216:219], v240 offset:0\n"
        "ds_read_b128 a[220:223], v240 offset:1536\n"
        "ds_read_b128 a[224:227], v240 offset:12288\n"
        "ds_read_b128 a[228:231], v240 offset:13824\n"
        "v_mfma_f32_16x16x32_bf16 v[116:119], a[208:211], v[148:151], 0\n"
        "s_waitcnt lgkmcnt(7)\n"
        "v_mfma_f32_16x16x32_bf16 v[120:123], a[204:207], v[148:151], 0\n"
        "s_waitcnt lgkmcnt(6)\n"
        "v_mfma_f32_16x16x32_bf16 v[124:127], a[200:203], v[148:151], 0\n"
        "s_waitcnt lgkmcnt(5)\n"
        "buffer_load_dwordx4 v129, s[20:23], s56 offen offset:1408 lds\n"
        "v_mfma_f32_16x16x32_bf16 v[96:99], a[196:199], v[148:151], 0\n"
        "s_waitcnt lgkmcnt(4)\n"
        "ds_read_b128 v[44:47], v143 offset:8192\n"
        "v_cvt_pk_bf16_f32 v112, v112, v113\n"
        "v_mfma_f32_16x16x32_bf16 v[100:103], a[192:195], v[148:151], 0\n"
        "s_waitcnt lgkmcnt(4)\n"
        "s_waitcnt vmcnt(5)\n"
        "ds_read_b64_tr_b16 a[72:73], v255 offset:3072\n"
        "ds_read_b64_tr_b16 a[74:75], v255 offset:3840\n"
        "v_accvgpr_write_b32 a0, v112\n"
        "v_cvt_pk_bf16_f32 v114, v114, v115\n"
        "v_accvgpr_write_b32 a1, v114\n"
        "v_cvt_pk_bf16_f32 v116, v116, v117\n"
        "v_accvgpr_write_b32 a2, v116\n"
        "v_cvt_pk_bf16_f32 v118, v118, v119\n"
        "v_accvgpr_write_b32 a3, v118\n"
        "s_nop 1\n"
        "v_mfma_f32_16x16x32_bf16 v[156:159], a[0:3], a[216:219], 0\n"
        "s_waitcnt lgkmcnt(5)\n"
        "ds_read_b64_tr_b16 a[96:97], v255 offset:3104\n"
        "ds_read_b64_tr_b16 a[98:99], v255 offset:3872\n"
        "v_mfma_f32_16x16x32_bf16 v[176:179], a[0:3], a[220:223], 0\n"
        "s_waitcnt lgkmcnt(5)\n"
        "buffer_load_dwordx4 v130, s[20:23], s56 offen offset:2432 lds\n"
        "ds_read_b64_tr_b16 a[120:121], v255 offset:3136\n"
        "ds_read_b64_tr_b16 a[122:123], v255 offset:3904\n"
        "v_mfma_f32_16x16x32_bf16 v[196:199], a[0:3], a[224:227], 0\n"
        "s_add_u32 m0, s65, 12288\n"
        "v_cvt_pk_bf16_f32 v120, v120, v121\n"
        "v_accvgpr_write_b32 a24, v120\n"
        "v_mfma_f32_16x16x32_bf16 v[216:219], a[0:3], a[228:231], 0\n"
        "s_waitcnt lgkmcnt(4)\n"
        "v_cvt_pk_bf16_f32 v122, v122, v123\n"
        "v_accvgpr_write_b32 a25, v122\n"
        "v_mfma_f32_16x16x32_bf16 v[152:155], a[72:75], a[216:219], 0\n"
        "buffer_load_dwordx4 v128, s[20:23], s57 offen lds\n"
        "s_waitcnt vmcnt(4)\n"
        "ds_read_b128 a[188:191], v137 offset:6144\n"
        "v_mfma_f32_16x16x32_bf16 v[172:175], a[72:75], a[220:223], 0\n"
        "ds_read_b128 a[184:187], v137 offset:6912\n"
        "ds_read_b128 a[180:183], v137 offset:6176\n"
        "v_cvt_pk_bf16_f32 v124, v124, v125\n"
        "v_accvgpr_write_b32 a26, v124\n"
        "v_cvt_pk_bf16_f32 v126, v126, v127\n"
        "v_accvgpr_write_b32 a27, v126\n"
        "s_nop 1\n"
        "v_mfma_f32_16x16x32_bf16 v[224:227], a[24:27], a[228:231], 0\n"
        "buffer_load_dwordx4 v129, s[20:23], s57 offen offset:1024 lds\n"
        "ds_read_b128 a[176:179], v137 offset:6944\n"
        "ds_read_b128 a[172:175], v137 offset:6208\n"
        "v_mfma_f32_16x16x32_bf16 v[204:207], a[24:27], a[224:227], 0\n"
        "ds_read_b128 a[168:171], v137 offset:6976\n"
        "ds_read_b128 a[232:235], v241 offset:0\n"
        "v_mfma_f32_16x16x32_bf16 v[184:187], a[24:27], a[220:223], 0\n"
        "ds_read_b128 a[236:239], v241 offset:1536\n"
        "ds_read_b128 a[240:243], v241 offset:12288\n"
        "v_mfma_f32_16x16x32_bf16 v[164:167], a[24:27], a[216:219], 0\n"
        "buffer_load_dwordx4 v130, s[20:23], s57 offen offset:2048 lds\n"
        "v_mfma_f32_16x16x32_bf16 v[192:195], a[72:75], a[224:227], 0\n"
        "s_add_u32 m0, s65, 14976\n"
        "ds_read_b128 a[244:247], v241 offset:13824\n"
        "v_cvt_pk_bf16_f32 v96, v96, v97\n"
        "v_mfma_f32_16x16x32_bf16 v[212:215], a[72:75], a[228:231], 0\n"
        "v_accvgpr_write_b32 a48, v96\n"
        "v_cvt_pk_bf16_f32 v98, v98, v99\n"
        "v_accvgpr_write_b32 a49, v98\n"
        "v_cvt_pk_bf16_f32 v100, v100, v101\n"
        "v_accvgpr_write_b32 a50, v100\n"
        "v_cvt_pk_bf16_f32 v102, v102, v103\n"
        "v_accvgpr_write_b32 a51, v102\n"
        "s_nop 1\n"
        "v_mfma_f32_16x16x32_bf16 v[160:163], a[48:51], a[216:219], 0\n"
        "v_lshl_add_u32 v44, v44, 2, s67\n"
        "v_lshl_add_u32 v45, v45, 2, s67\n"
        "v_mfma_f32_16x16x32_bf16 v[180:183], a[48:51], a[220:223], 0\n"
        "s_waitcnt lgkmcnt(5)\n"
        "ds_read_b32 v44, v44\n"
        "ds_read_b32 v45, v45\n"
        "v_mfma_f32_16x16x32_bf16 v[104:107], a[188:191], v[148:151], 0\n"
        "buffer_load_dwordx4 v128, s[20:23], s57 offen offset:384 lds\n"
        "v_mfma_f32_16x16x32_bf16 v[200:203], a[48:51], a[224:227], 0\n"
        "v_lshl_add_u32 v46, v46, 2, s67\n"
        "v_lshl_add_u32 v47, v47, 2, s67\n"
        "v_mfma_f32_16x16x32_bf16 v[108:111], a[184:187], v[148:151], 0\n"
        "ds_read_b32 v46, v46\n"
        "ds_read_b32 v47, v47\n"
        "v_mfma_f32_16x16x32_bf16 v[220:223], a[48:51], a[228:231], 0\n"
        "buffer_load_dwordx4 v129, s[20:23], s57 offen offset:1408 lds\n"
        "v_mfma_f32_16x16x32_bf16 v[88:91], a[96:99], a[228:231], 0\n"
        "ds_read_b128 v[48:51], v140 offset:9216\n"
        "v_cvt_pk_bf16_f32 v104, v104, v105\n"
        "v_mfma_f32_16x16x32_bf16 v[80:83], a[96:99], a[224:227], 0\n"
        "s_waitcnt vmcnt(5)\n"
        "ds_read_b64_tr_b16 a[76:77], v255 offset:9216\n"
        "ds_read_b64_tr_b16 a[78:79], v255 offset:9984\n"
        "v_mfma_f32_16x16x32_bf16 v[68:71], a[96:99], a[220:223], 0\n"
        "ds_read_b64_tr_b16 a[100:101], v255 offset:9248\n"
        "ds_read_b64_tr_b16 a[102:103], v255 offset:10016\n"
        "v_mfma_f32_16x16x32_bf16 v[64:67], a[96:99], a[216:219], 0\n"
        "buffer_load_dwordx4 v130, s[20:23], s57 offen offset:2432 lds\n"
        "ds_read_b64_tr_b16 a[124:125], v255 offset:9280\n"
        "ds_read_b64_tr_b16 a[126:127], v255 offset:10048\n"
        "v_mfma_f32_16x16x32_bf16 v[112:115], a[180:183], v[148:151], 0\n"
        "s_add_u32 m0, s65, 0\n"
        "v_accvgpr_write_b32 a4, v104\n"
        "v_cvt_pk_bf16_f32 v106, v106, v107\n"
        "v_mfma_f32_16x16x32_bf16 v[116:119], a[176:179], v[148:151], 0\n"
        "v_accvgpr_write_b32 a5, v106\n"
        "v_cvt_pk_bf16_f32 v108, v108, v109\n"
        "v_mfma_f32_16x16x32_bf16 v[72:75], a[120:123], a[216:219], 0\n"
        "buffer_load_dwordx4 v128, s[20:23], s58 offen lds\n"
        "v_mfma_f32_16x16x32_bf16 v[76:79], a[120:123], a[220:223], 0\n"
        "v_accvgpr_write_b32 a6, v108\n"
        "v_cvt_pk_bf16_f32 v110, v110, v111\n"
        "v_mfma_f32_16x16x32_bf16 v[84:87], a[120:123], a[224:227], 0\n"
        "v_accvgpr_write_b32 a7, v110\n"
        "v_cvt_pk_bf16_f32 v112, v112, v113\n"
        "v_mfma_f32_16x16x32_bf16 v[92:95], a[120:123], a[228:231], 0\n"
        "s_waitcnt lgkmcnt(2)\n"
        "v_accvgpr_write_b32 a28, v112\n"
        "v_cvt_pk_bf16_f32 v114, v114, v115\n"
        "v_mfma_f32_16x16x32_bf16 v[156:159], a[4:7], a[232:235], v[156:159]\n"
        "buffer_load_dwordx4 v129, s[20:23], s58 offen offset:1024 lds\n"
        "v_mfma_f32_16x16x32_bf16 v[176:179], a[4:7], a[236:239], v[176:179]\n"
        "v_lshl_add_u32 v48, v48, 2, s67\n"
        "v_lshl_add_u32 v49, v49, 2, s67\n"
        "v_mfma_f32_16x16x32_bf16 v[196:199], a[4:7], a[240:243], v[196:199]\n"
        "ds_read_b32 v48, v48\n"
        "ds_read_b32 v49, v49\n"
        "v_mfma_f32_16x16x32_bf16 v[216:219], a[4:7], a[244:247], v[216:219]\n"
        ".long 0xbec200ff\n"
        "41:\n"
        "s_branch 60b\n"
        "v_lshl_add_u32 v50, v50, 2, s67\n"
        "v_lshl_add_u32 v51, v51, 2, s67\n"
        "v_mfma_f32_16x16x32_bf16 v[120:123], a[172:175], v[148:151], 0\n"
        "buffer_load_dwordx4 v130, s[20:23], s58 offen offset:2048 lds\n"
        "v_mfma_f32_16x16x32_bf16 v[124:127], a[168:171], v[148:151], 0\n"
        "s_add_u32 m0, s65, 2688\n"
        "ds_read_b32 v50, v50\n"
        "ds_read_b32 v51, v51\n"
        "v_mfma_f32_16x16x32_bf16 v[152:155], a[76:79], a[232:235], v[152:155]\n"
        "ds_read_b128 a[248:251], v240 offset:128\n"
        "v_accvgpr_write_b32 a29, v114\n"
        "v_mfma_f32_16x16x32_bf16 v[172:175], a[76:79], a[236:239], v[172:175]\n"
        "s_waitcnt vmcnt(6)\n"
        "ds_read_b128 a[212:215], v137 offset:12288\n"
        "ds_read_b128 a[208:211], v137 offset:13056\n"
        "v_mfma_f32_16x16x32_bf16 v[192:195], a[76:79], a[240:243], v[192:195]\n"
        "buffer_load_dwordx4 v128, s[20:23], s58 offen offset:384 lds\n"
        "ds_read_b128 a[204:207], v137 offset:12320\n"
        "v_cvt_pk_bf16_f32 v116, v116, v117\n"
        "v_mfma_f32_16x16x32_bf16 v[212:215], a[76:79], a[244:247], v[212:215]\n"
        "ds_read_b128 a[200:203], v137 offset:13088\n"
        "ds_read_b128 a[196:199], v137 offset:12352\n"
        "v_accvgpr_write_b32 a30, v116\n"
        "v_cvt_pk_bf16_f32 v118, v118, v119\n"
        "v_accvgpr_write_b32 a31, v118\n"
        "s_nop 1\n"
        "v_mfma_f32_16x16x32_bf16 v[224:227], a[28:31], a[244:247], v[224:227]\n"
        "ds_read_b128 a[192:195], v137 offset:13120\n"
        "ds_read_b128 a[216:219], v240 offset:1664\n"
        "v_mfma_f32_16x16x32_bf16 v[204:207], a[28:31], a[240:243], v[204:207]\n"
        "buffer_load_dwordx4 v129, s[20:23], s58 offen offset:1408 lds\n"
        "v_mfma_f32_16x16x32_bf16 v[184:187], a[28:31], a[236:239], v[184:187]\n"
        "ds_read_b128 a[220:223], v240 offset:12416\n"
        "ds_read_b128 a[224:227], v240 offset:13952\n"
        "v_mfma_f32_16x16x32_bf16 v[164:167], a[28:31], a[232:235], v[164:167]\n"
        "s_waitcnt lgkmcnt(8)\n"
        "v_cvt_pk_bf16_f32 v120, v120, v121\n"
        "v_accvgpr_write_b32 a52, v120\n"
        "v_mfma_f32_16x16x32_bf16 v[96:99], a[212:215], v[148:151], 0\n"
        "s_waitcnt lgkmcnt(7)\n"
        "v_cvt_pk_bf16_f32 v122, v122, v123\n"
        "v_accvgpr_write_b32 a53, v122\n"
        "v_mfma_f32_16x16x32_bf16 v[100:103], a[208:211], v[148:151], 0\n"
        "buffer_load_dwordx4 v130, s[20:23], s58 offen offset:2432 lds\n"
        "v_mfma_f32_16x16x32_bf16 v[88:91], a[100:103], a[244:247], v[88:91]\n"
        "s_add_u32 m0, s65, 6144\n"
        "v_cvt_pk_bf16_f32 v124, v124, v125\n"
        "v_accvgpr_write_b32 a54, v124\n"
        "v_mfma_f32_16x16x32_bf16 v[80:83], a[100:103], a[240:243], v[80:83]\n"
        "v_cvt_pk_bf16_f32 v126, v126, v127\n"
        "v_accvgpr_write_b32 a55, v126\n"
        "v_mfma_f32_16x16x32_bf16 v[68:71], a[100:103], a[236:239], v[68:71]\n"
        "v_cvt_pk_bf16_f32 v96, v96, v97\n"
        "v_accvgpr_write_b32 a8, v96\n"
        "v_mfma_f32_16x16x32_bf16 v[64:67], a[100:103], a[232:235], v[64:67]\n"
        "buffer_load_dwordx4 v128, s[20:23], s59 offen lds\n"
        "v_mfma_f32_16x16x32_bf16 v[160:163], a[52:55], a[232:235], v[160:163]\n"
        "s_waitcnt vmcnt(7)\n"
        "ds_read_b64_tr_b16 a[80:81], v255 offset:15360\n"
        "ds_read_b64_tr_b16 a[82:83], v255 offset:16128\n"
        "v_mfma_f32_16x16x32_bf16 v[180:183], a[52:55], a[236:239], v[180:183]\n"
        "ds_read_b64_tr_b16 a[104:105], v255 offset:15392\n"
        "ds_read_b64_tr_b16 a[106:107], v255 offset:16160\n"
        "v_mfma_f32_16x16x32_bf16 v[200:203], a[52:55], a[240:243], v[200:203]\n"
        "ds_read_b64_tr_b16 a[128:129], v255 offset:15424\n"
        "ds_read_b64_tr_b16 a[130:131], v255 offset:16192\n"
        "v_mfma_f32_16x16x32_bf16 v[220:223], a[52:55], a[244:247], v[220:223]\n"
        "s_waitcnt lgkmcnt(6)\n"
        "v_cvt_pk_bf16_f32 v98, v98, v99\n"
        "v_accvgpr_write_b32 a9, v98\n"
        "v_mfma_f32_16x16x32_bf16 v[104:107], a[204:207], v[148:151], 0\n"
        "buffer_load_dwordx4 v129, s[20:23], s59 offen offset:1024 lds\n"
        "v_mfma_f32_16x16x32_bf16 v[108:111], a[200:203], v[148:151], 0\n"
        "v_cvt_pk_bf16_f32 v100, v100, v101\n"
        "v_accvgpr_write_b32 a10, v100\n"
        "v_mfma_f32_16x16x32_bf16 v[72:75], a[124:127], a[232:235], v[72:75]\n"
        "v_cvt_pk_bf16_f32 v102, v102, v103\n"
        "v_accvgpr_write_b32 a11, v102\n"
        "v_mfma_f32_16x16x32_bf16 v[76:79], a[124:127], a[236:239], v[76:79]\n"
        "buffer_load_dwordx4 v130, s[20:23], s59 offen offset:2048 lds\n"
        "v_mfma_f32_16x16x32_bf16 v[84:87], a[124:127], a[240:243], v[84:87]\n"
        "s_add_u32 m0, s65, 8832\n"
        "v_cvt_pk_bf16_f32 v104, v104, v105\n"
        "v_accvgpr_write_b32 a32, v104\n"
        "v_mfma_f32_16x16x32_bf16 v[92:95], a[124:127], a[244:247], v[92:95]\n"
        "s_waitcnt vmcnt(6)\n"
        "ds_read_b128 a[188:191], v137 offset:0\n"
        "ds_read_b128 a[184:187], v137 offset:768\n"
        "v_mfma_f32_16x16x32_bf16 v[156:159], a[8:11], a[248:251], v[156:159]\n"
        "buffer_load_dwordx4 v128, s[20:23], s59 offen offset:384 lds\n"
        "ds_read_b128 a[180:183], v137 offset:32\n"
        "v_mfma_f32_16x16x32_bf16 v[176:179], a[8:11], a[216:219], v[176:179]\n"
        "ds_read_b128 a[176:179], v137 offset:800\n"
        "ds_read_b128 a[172:175], v137 offset:64\n"
        "v_mfma_f32_16x16x32_bf16 v[196:199], a[8:11], a[220:223], v[196:199]\n"
        "ds_read_b128 a[168:171], v137 offset:832\n"
        "v_cvt_pk_bf16_f32 v106, v106, v107\n"
        "v_mfma_f32_16x16x32_bf16 v[216:219], a[8:11], a[224:227], v[216:219]\n"
        "v_accvgpr_write_b32 a33, v106\n"
        "v_cvt_pk_bf16_f32 v108, v108, v109\n"
        "v_mfma_f32_16x16x32_bf16 v[112:115], a[196:199], v[148:151], 0\n"
        "buffer_load_dwordx4 v129, s[20:23], s59 offen offset:1408 lds\n"
        "v_mfma_f32_16x16x32_bf16 v[116:119], a[192:195], v[148:151], 0\n"
        "s_waitcnt lgkmcnt(6)\n"
        "v_accvgpr_write_b32 a34, v108\n"
        "v_cvt_pk_bf16_f32 v110, v110, v111\n"
        "v_mfma_f32_16x16x32_bf16 v[152:155], a[80:83], a[248:251], v[152:155]\n"
        "ds_read_b128 a[228:231], v241 offset:128\n"
        "v_accvgpr_write_b32 a35, v110\n"
        "v_mfma_f32_16x16x32_bf16 v[172:175], a[80:83], a[216:219], v[172:175]\n"
        "ds_read_b128 a[232:235], v241 offset:1664\n"
        "v_cvt_pk_bf16_f32 v112, v112, v113\n"
        "v_mfma_f32_16x16x32_bf16 v[192:195], a[80:83], a[220:223], v[192:195]\n"
        "buffer_load_dwordx4 v130, s[20:23], s59 offen offset:2432 lds\n"
        "v_mfma_f32_16x16x32_bf16 v[212:215], a[80:83], a[224:227], v[212:215]\n"
        "s_add_u32 m0, s65, 12288\n"
        "ds_read_b128 a[236:239], v241 offset:12416\n"
        "ds_read_b128 a[240:243], v241 offset:13952\n"
        "v_mfma_f32_16x16x32_bf16 v[224:227], a[32:35], a[224:227], v[224:227]\n"
        "ds_read_b128 v[52:55], v141 offset:9216\n"
        "v_accvgpr_write_b32 a56, v112\n"
        "v_mfma_f32_16x16x32_bf16 v[204:207], a[32:35], a[220:223], v[204:207]\n"
        "v_cvt_pk_bf16_f32 v114, v114, v115\n"
        "v_accvgpr_write_b32 a57, v114\n"
        "v_mfma_f32_16x16x32_bf16 v[184:187], a[32:35], a[216:219], v[184:187]\n"
        "buffer_load_dwordx4 v128, s[20:23], s60 offen lds\n"
        "v_mfma_f32_16x16x32_bf16 v[164:167], a[32:35], a[248:251], v[164:167]\n"
        "s_waitcnt lgkmcnt(4)\n"
        "v_cvt_pk_bf16_f32 v116, v116, v117\n"
        "v_accvgpr_write_b32 a58, v116\n"
        "v_mfma_f32_16x16x32_bf16 v[120:123], a[188:191], v[148:151], 0\n"
        "s_waitcnt vmcnt(7)\n"
        "ds_read_b64_tr_b16 a[84:85], v255 offset:3072\n"
        "ds_read_b64_tr_b16 a[86:87], v255 offset:3840\n"
        "v_mfma_f32_16x16x32_bf16 v[124:127], a[184:187], v[148:151], 0\n"
        "ds_read_b64_tr_b16 a[108:109], v255 offset:3104\n"
        "ds_read_b64_tr_b16 a[110:111], v255 offset:3872\n"
        "v_mfma_f32_16x16x32_bf16 v[88:91], a[104:107], a[224:227], v[88:91]\n"
        "buffer_load_dwordx4 v129, s[20:23], s60 offen offset:1024 lds\n"
        "ds_read_b64_tr_b16 a[132:133], v255 offset:3136\n"
        "ds_read_b64_tr_b16 a[134:135], v255 offset:3904\n"
        "v_mfma_f32_16x16x32_bf16 v[80:83], a[104:107], a[220:223], v[80:83]\n"
        "v_cvt_pk_bf16_f32 v118, v118, v119\n"
        "v_accvgpr_write_b32 a59, v118\n"
        "v_mfma_f32_16x16x32_bf16 v[68:71], a[104:107], a[216:219], v[68:71]\n"
        "v_cvt_pk_bf16_f32 v120, v120, v121\n"
        "v_accvgpr_write_b32 a12, v120\n"
        "v_mfma_f32_16x16x32_bf16 v[64:67], a[104:107], a[248:251], v[64:67]\n"
        "buffer_load_dwordx4 v130, s[20:23], s60 offen offset:2048 lds\n"
        "v_mfma_f32_16x16x32_bf16 v[160:163], a[56:59], a[248:251], v[160:163]\n"
        "s_add_u32 m0, s65, 14976\n"
        "v_cvt_pk_bf16_f32 v122, v122, v123\n"
        "v_accvgpr_write_b32 a13, v122\n"
        "v_mfma_f32_16x16x32_bf16 v[180:183], a[56:59], a[216:219], v[180:183]\n"
        "s_waitcnt lgkmcnt(4)\n"
        "v_lshl_add_u32 v52, v52, 2, s67\n"
        "v_lshl_add_u32 v53, v53, 2, s67\n"
        "v_mfma_f32_16x16x32_bf16 v[200:203], a[56:59], a[220:223], v[200:203]\n"
        "ds_read_b32 v52, v52\n"
        "ds_read_b32 v53, v53\n"
        "v_mfma_f32_16x16x32_bf16 v[220:223], a[56:59], a[224:227], v[220:223]\n"
        "buffer_load_dwordx4 v128, s[20:23], s60 offen offset:384 lds\n"
        "v_mfma_f32_16x16x32_bf16 v[96:99], a[180:183], v[148:151], 0\n"
        "v_lshl_add_u32 v54, v54, 2, s67\n"
        "v_lshl_add_u32 v55, v55, 2, s67\n"
        "v_mfma_f32_16x16x32_bf16 v[100:103], a[176:179], v[148:151], 0\n"
        "ds_read_b32 v54, v54\n"
        "ds_read_b32 v55, v55\n"
        "v_mfma_f32_16x16x32_bf16 v[72:75], a[128:131], a[248:251], v[72:75]\n"
        "v_cvt_pk_bf16_f32 v124, v124, v125\n"
        "v_accvgpr_write_b32 a14, v124\n"
        "v_mfma_f32_16x16x32_bf16 v[76:79], a[128:131], a[216:219], v[76:79]\n"
        "buffer_load_dwordx4 v129, s[20:23], s60 offen offset:1408 lds\n"
        "v_mfma_f32_16x16x32_bf16 v[84:87], a[128:131], a[220:223], v[84:87]\n"
        "v_cvt_pk_bf16_f32 v126, v126, v127\n"
        "v_accvgpr_write_b32 a15, v126\n"
        "v_mfma_f32_16x16x32_bf16 v[92:95], a[128:131], a[224:227], v[92:95]\n"
        "v_cvt_pk_bf16_f32 v96, v96, v97\n"
        "v_accvgpr_write_b32 a36, v96\n"
        "v_mfma_f32_16x16x32_bf16 v[156:159], a[12:15], a[228:231], v[156:159]\n"
        "s_waitcnt vmcnt(8)\n"
        "ds_read_b128 a[212:215], v137 offset:6144\n"
        "ds_read_b128 a[208:211], v137 offset:6912\n"
        "v_mfma_f32_16x16x32_bf16 v[176:179], a[12:15], a[232:235], v[176:179]\n"
        "buffer_load_dwordx4 v130, s[20:23], s60 offen offset:2432 lds\n"
        "ds_read_b128 a[204:207], v137 offset:6176\n"
        "v_mfma_f32_16x16x32_bf16 v[196:199], a[12:15], a[236:239], v[196:199]\n"
        "ds_read_b128 a[200:203], v137 offset:6944\n"
        "ds_read_b128 a[196:199], v137 offset:6208\n"
        "v_mfma_f32_16x16x32_bf16 v[216:219], a[12:15], a[240:243], v[216:219]\n"
        "ds_read_b128 a[192:195], v137 offset:6976\n"
        "v_cvt_pk_bf16_f32 v98, v98, v99\n"
        "v_mfma_f32_16x16x32_bf16 v[104:107], a[172:175], v[148:151], 0\n"
        "v_accvgpr_write_b32 a37, v98\n"
        "v_cvt_pk_bf16_f32 v100, v100, v101\n"
        "v_mfma_f32_16x16x32_bf16 v[108:111], a[168:171], v[148:151], 0\n"
        "v_accvgpr_write_b32 a38, v100\n"
        "v_cvt_pk_bf16_f32 v102, v102, v103\n"
        "v_mfma_f32_16x16x32_bf16 v[152:155], a[84:87], a[228:231], v[152:155]\n"
        "ds_read_b128 a[244:247], v240 offset:256\n"
        "v_accvgpr_write_b32 a39, v102\n"
        "v_mfma_f32_16x16x32_bf16 v[172:175], a[84:87], a[232:235], v[172:175]\n"
        "s_mov_b32 m0, s48\n"
        "ds_read_b128 a[248:251], v240 offset:1792\n"
        "v_cvt_pk_bf16_f32 v104, v104, v105\n"
        "v_mfma_f32_16x16x32_bf16 v[192:195], a[84:87], a[236:239], v[192:195]\n"
        "s_sub_u32 s64, s36, 4194304\n"
        "ds_read_b128 a[216:219], v240 offset:12544\n"
        "v_accvgpr_write_b32 a60, v104\n"
        "v_mfma_f32_16x16x32_bf16 v[212:215], a[84:87], a[240:243], v[212:215]\n"
        "buffer_load_dwordx4 v249, s[12:15], s64 offen lds\n"
        "v_mfma_f32_16x16x32_bf16 v[224:227], a[36:39], a[240:243], v[224:227]\n"
        "ds_read_b128 a[220:223], v240 offset:14080\n"
        "v_cvt_pk_bf16_f32 v106, v106, v107\n"
        "v_mfma_f32_16x16x32_bf16 v[204:207], a[36:39], a[236:239], v[204:207]\n"
        "ds_read_b128 v[56:59], v142 offset:9216\n"
        "v_accvgpr_write_b32 a61, v106\n"
        "v_mfma_f32_16x16x32_bf16 v[184:187], a[36:39], a[232:235], v[184:187]\n"
        "s_waitcnt vmcnt(7)\n"
        "ds_read_b64_tr_b16 a[88:89], v255 offset:9216\n"
        "ds_read_b64_tr_b16 a[90:91], v255 offset:9984\n"
        "v_mfma_f32_16x16x32_bf16 v[164:167], a[36:39], a[228:231], v[164:167]\n"
        "s_waitcnt lgkmcnt(6)\n"
        "ds_read_b64_tr_b16 a[112:113], v255 offset:9248\n"
        "ds_read_b64_tr_b16 a[114:115], v255 offset:10016\n"
        "v_mfma_f32_16x16x32_bf16 v[112:115], a[212:215], v[148:151], 0\n"
        "buffer_load_dwordx4 v253, s[12:15], s64 offen offset:1024 lds\n"
        "ds_read_b64_tr_b16 a[136:137], v255 offset:9280\n"
        "v_mfma_f32_16x16x32_bf16 v[116:119], a[208:211], v[148:151], 0\n"
        "ds_read_b64_tr_b16 a[138:139], v255 offset:10048\n"
        "s_add_u32 m0, s65, 0\n"
        "v_cvt_pk_bf16_f32 v108, v108, v109\n"
        "v_mfma_f32_16x16x32_bf16 v[88:91], a[108:111], a[240:243], v[88:91]\n"
        "s_mov_b32 s64, 768\n"
        "v_accvgpr_write_b32 a62, v108\n"
        "v_cvt_pk_bf16_f32 v110, v110, v111\n"
        "v_mfma_f32_16x16x32_bf16 v[80:83], a[108:111], a[236:239], v[80:83]\n"
        "buffer_load_dwordx4 v128, s[20:23], s64 offen lds\n"
        "v_mfma_f32_16x16x32_bf16 v[68:71], a[108:111], a[232:235], v[68:71]\n"
        "v_accvgpr_write_b32 a63, v110\n"
        "v_cvt_pk_bf16_f32 v112, v112, v113\n"
        "v_mfma_f32_16x16x32_bf16 v[64:67], a[108:111], a[228:231], v[64:67]\n"
        "v_accvgpr_write_b32 a16, v112\n"
        "v_cvt_pk_bf16_f32 v114, v114, v115\n"
        "v_mfma_f32_16x16x32_bf16 v[160:163], a[60:63], a[228:231], v[160:163]\n"
        "v_accvgpr_write_b32 a17, v114\n"
        "v_cvt_pk_bf16_f32 v116, v116, v117\n"
        "v_mfma_f32_16x16x32_bf16 v[180:183], a[60:63], a[232:235], v[180:183]\n"
        "buffer_load_dwordx4 v129, s[20:23], s64 offen offset:1024 lds\n"
        "v_mfma_f32_16x16x32_bf16 v[200:203], a[60:63], a[236:239], v[200:203]\n"
        "ds_read_b128 v[60:63], v143 offset:9216\n"
        "v_accvgpr_write_b32 a18, v116\n"
        "v_mfma_f32_16x16x32_bf16 v[220:223], a[60:63], a[240:243], v[220:223]\n"
        "v_cvt_pk_bf16_f32 v118, v118, v119\n"
        "v_accvgpr_write_b32 a19, v118\n"
        "v_mfma_f32_16x16x32_bf16 v[120:123], a[204:207], v[148:151], 0\n"
        "s_waitcnt lgkmcnt(1)\n"
        "v_lshl_add_u32 v56, v56, 2, s67\n"
        "v_lshl_add_u32 v57, v57, 2, s67\n"
        "v_mfma_f32_16x16x32_bf16 v[124:127], a[200:203], v[148:151], 0\n"
        "buffer_load_dwordx4 v130, s[20:23], s64 offen offset:2048 lds\n"
        "v_mfma_f32_16x16x32_bf16 v[72:75], a[132:135], a[228:231], v[72:75]\n"
        "s_add_u32 m0, s65, 2304\n"
        "ds_read_b32 v56, v56\n"
        "ds_read_b32 v57, v57\n"
        "v_mfma_f32_16x16x32_bf16 v[76:79], a[132:135], a[232:235], v[76:79]\n"
        "v_lshl_add_u32 v58, v58, 2, s67\n"
        "v_lshl_add_u32 v59, v59, 2, s67\n"
        "v_mfma_f32_16x16x32_bf16 v[84:87], a[132:135], a[236:239], v[84:87]\n"
        "ds_read_b32 v58, v58\n"
        "ds_read_b32 v59, v59\n"
        "v_mfma_f32_16x16x32_bf16 v[92:95], a[132:135], a[240:243], v[92:95]\n"
        "buffer_load_dwordx4 v128, s[20:23], s56 offen offset:768 lds\n"
        "v_mfma_f32_16x16x32_bf16 v[156:159], a[16:19], a[244:247], v[156:159]\n"
        "v_cvt_pk_bf16_f32 v120, v120, v121\n"
        "v_accvgpr_write_b32 a40, v120\n"
        "v_mfma_f32_16x16x32_bf16 v[176:179], a[16:19], a[248:251], v[176:179]\n"
        "v_cvt_pk_bf16_f32 v122, v122, v123\n"
        "v_accvgpr_write_b32 a41, v122\n"
        "v_mfma_f32_16x16x32_bf16 v[196:199], a[16:19], a[216:219], v[196:199]\n"
        "v_cvt_pk_bf16_f32 v124, v124, v125\n"
        "v_accvgpr_write_b32 a42, v124\n"
        "v_mfma_f32_16x16x32_bf16 v[216:219], a[16:19], a[220:223], v[216:219]\n"
        "buffer_load_dwordx4 v129, s[20:23], s56 offen offset:1792 lds\n"
        "v_mfma_f32_16x16x32_bf16 v[96:99], a[196:199], v[148:151], 0\n"
        "ds_read_b128 a[224:227], v241 offset:256\n"
        "v_cvt_pk_bf16_f32 v126, v126, v127\n"
        "v_mfma_f32_16x16x32_bf16 v[100:103], a[192:195], v[148:151], 0\n"
        "s_waitcnt vmcnt(10)\n"
        "ds_read_b128 a[188:191], v137 offset:12288\n"
        "ds_read_b128 a[184:187], v137 offset:13056\n"
        "v_mfma_f32_16x16x32_bf16 v[152:155], a[88:91], a[244:247], v[152:155]\n"
        "ds_read_b128 a[180:183], v137 offset:12320\n"
        "ds_read_b128 a[176:179], v137 offset:13088\n"
        "v_mfma_f32_16x16x32_bf16 v[172:175], a[88:91], a[248:251], v[172:175]\n"
        "buffer_load_dwordx4 v130, s[20:23], s56 offen offset:2816 lds\n"
        "ds_read_b128 a[172:175], v137 offset:12352\n"
        "v_mfma_f32_16x16x32_bf16 v[192:195], a[88:91], a[216:219], v[192:195]\n"
        "ds_read_b128 a[168:171], v137 offset:13120\n"
        "s_add_u32 m0, s65, 5376\n"
        "ds_read_b128 a[228:231], v241 offset:1792\n"
        "v_accvgpr_write_b32 a43, v126\n"
        "v_mfma_f32_16x16x32_bf16 v[212:215], a[88:91], a[220:223], v[212:215]\n"
        "ds_read_b128 a[232:235], v241 offset:12544\n"
        "ds_read_b128 a[236:239], v241 offset:14080\n"
        "v_mfma_f32_16x16x32_bf16 v[224:227], a[40:43], a[220:223], v[224:227]\n"
        "s_waitcnt lgkmcnt(10)\n"
        "v_lshl_add_u32 v60, v60, 2, s67\n"
        "v_lshl_add_u32 v61, v61, 2, s67\n"
        "v_lshl_add_u32 v62, v62, 2, s67\n"
        "v_mfma_f32_16x16x32_bf16 v[204:207], a[40:43], a[216:219], v[204:207]\n"
        "buffer_load_dwordx4 v128, s[20:23], s57 offen offset:768 lds\n"
        "v_lshl_add_u32 v63, v63, 2, s67\n"
        "v_mfma_f32_16x16x32_bf16 v[184:187], a[40:43], a[248:251], v[184:187]\n"
        "ds_read_b32 v60, v60\n"
        "ds_read_b32 v61, v61\n"
        "v_mfma_f32_16x16x32_bf16 v[164:167], a[40:43], a[244:247], v[164:167]\n"
        "s_waitcnt lgkmcnt(7)\n"
        "ds_read_b32 v62, v62\n"
        "ds_read_b32 v63, v63\n"
        "v_mfma_f32_16x16x32_bf16 v[104:107], a[188:191], v[148:151], 0\n"
        "v_cvt_pk_bf16_f32 v96, v96, v97\n"
        "v_accvgpr_write_b32 a64, v96\n"
        "v_mfma_f32_16x16x32_bf16 v[108:111], a[184:187], v[148:151], 0\n"
        "buffer_load_dwordx4 v129, s[20:23], s57 offen offset:1792 lds\n"
        "v_mfma_f32_16x16x32_bf16 v[88:91], a[112:115], a[220:223], v[88:91]\n"
        "v_cvt_pk_bf16_f32 v98, v98, v99\n"
        "v_accvgpr_write_b32 a65, v98\n"
        "v_mfma_f32_16x16x32_bf16 v[80:83], a[112:115], a[216:219], v[80:83]\n"
        "v_cvt_pk_bf16_f32 v100, v100, v101\n"
        "v_accvgpr_write_b32 a66, v100\n"
        "v_mfma_f32_16x16x32_bf16 v[68:71], a[112:115], a[248:251], v[68:71]\n"
        "v_cvt_pk_bf16_f32 v102, v102, v103\n"
        "v_accvgpr_write_b32 a67, v102\n"
        "v_mfma_f32_16x16x32_bf16 v[64:67], a[112:115], a[244:247], v[64:67]\n"
        "s_waitcnt vmcnt(10)\n"
        "ds_read_b64_tr_b16 a[92:93], v255 offset:15360\n"
        "ds_read_b64_tr_b16 a[94:95], v255 offset:16128\n"
        "v_mfma_f32_16x16x32_bf16 v[160:163], a[64:67], a[244:247], v[160:163]\n"
        "ds_read_b64_tr_b16 a[116:117], v255 offset:15392\n"
        "ds_read_b64_tr_b16 a[118:119], v255 offset:16160\n"
        "v_mfma_f32_16x16x32_bf16 v[180:183], a[64:67], a[248:251], v[180:183]\n"
        "ds_read_b64_tr_b16 a[140:141], v255 offset:15424\n"
        "ds_read_b64_tr_b16 a[142:143], v255 offset:16192\n"
        "v_mfma_f32_16x16x32_bf16 v[200:203], a[64:67], a[216:219], v[200:203]\n"
        "v_cvt_pk_bf16_f32 v104, v104, v105\n"
        "v_accvgpr_write_b32 a20, v104\n"
        "v_mfma_f32_16x16x32_bf16 v[220:223], a[64:67], a[220:223], v[220:223]\n"
        "v_cvt_pk_bf16_f32 v106, v106, v107\n"
        "v_accvgpr_write_b32 a21, v106\n"
        "v_mfma_f32_16x16x32_bf16 v[112:115], a[180:183], v[148:151], 0\n"
        "v_cvt_pk_bf16_f32 v108, v108, v109\n"
        "v_accvgpr_write_b32 a22, v108\n"
        "v_mfma_f32_16x16x32_bf16 v[116:119], a[176:179], v[148:151], 0\n"
        "v_cvt_pk_bf16_f32 v110, v110, v111\n"
        "v_accvgpr_write_b32 a23, v110\n"
        "v_mfma_f32_16x16x32_bf16 v[72:75], a[136:139], a[244:247], v[72:75]\n"
        "v_mfma_f32_16x16x32_bf16 v[76:79], a[136:139], a[248:251], v[76:79]\n"
        "v_mfma_f32_16x16x32_bf16 v[156:159], a[20:23], a[224:227], v[156:159]\n"
        "s_waitcnt lgkmcnt(0)\n"
        "v_cvt_pk_bf16_f32 v112, v112, v113\n"
        "v_accvgpr_write_b32 a44, v112\n"
        "v_mfma_f32_16x16x32_bf16 v[176:179], a[20:23], a[228:231], v[176:179]\n"
        "v_cvt_pk_bf16_f32 v114, v114, v115\n"
        "v_accvgpr_write_b32 a45, v114\n"
        "v_mfma_f32_16x16x32_bf16 v[196:199], a[20:23], a[232:235], v[196:199]\n"
        "v_cvt_pk_bf16_f32 v116, v116, v117\n"
        "v_accvgpr_write_b32 a46, v116\n"
        "v_cvt_pk_bf16_f32 v156, v156, v157\n"
        "v_mfma_f32_16x16x32_bf16 v[216:219], a[20:23], a[236:239], v[216:219]\n"
        "v_cvt_pk_bf16_f32 v157, v158, v159\n"
        "v_cvt_pk_bf16_f32 v176, v176, v177\n"
        "v_cvt_pk_bf16_f32 v177, v178, v179\n"
        "v_mfma_f32_16x16x32_bf16 v[120:123], a[172:175], v[148:151], 0\n"
        "v_cvt_pk_bf16_f32 v196, v196, v197\n"
        "v_cvt_pk_bf16_f32 v197, v198, v199\n"
        "v_mfma_f32_16x16x32_bf16 v[124:127], a[168:171], v[148:151], 0\n"
        "v_cvt_pk_bf16_f32 v118, v118, v119\n"
        "v_cvt_pk_bf16_f32 v216, v216, v217\n"
        "v_cvt_pk_bf16_f32 v217, v218, v219\n"
        "v_mfma_f32_16x16x32_bf16 v[84:87], a[136:139], a[216:219], v[84:87]\n"
        "v_accvgpr_write_b32 a47, v118\n"
        "v_cvt_pk_bf16_f32 v120, v120, v121\n"
        "v_mfma_f32_16x16x32_bf16 v[92:95], a[136:139], a[220:223], v[92:95]\n"
        "v_accvgpr_write_b32 a68, v120\n"
        "v_cvt_pk_bf16_f32 v122, v122, v123\n"
        "v_mfma_f32_16x16x32_bf16 v[224:227], a[44:47], a[236:239], v[224:227]\n"
        "v_accvgpr_write_b32 a69, v122\n"
        "v_cvt_pk_bf16_f32 v124, v124, v125\n"
        "v_mfma_f32_16x16x32_bf16 v[204:207], a[44:47], a[232:235], v[204:207]\n"
        "v_accvgpr_write_b32 a70, v124\n"
        "v_cvt_pk_bf16_f32 v126, v126, v127\n"
        "v_mfma_f32_16x16x32_bf16 v[184:187], a[44:47], a[228:231], v[184:187]\n"
        "v_accvgpr_write_b32 a71, v126\n"
        "s_nop 0\n"
        "v_cvt_pk_bf16_f32 v218, v224, v225\n"
        "v_mfma_f32_16x16x32_bf16 v[164:167], a[44:47], a[224:227], v[164:167]\n"
        "v_cvt_pk_bf16_f32 v219, v226, v227\n"
        "v_cvt_pk_bf16_f32 v198, v204, v205\n"
        "v_cvt_pk_bf16_f32 v199, v206, v207\n"
        "v_mfma_f32_16x16x32_bf16 v[152:155], a[92:95], a[224:227], v[152:155]\n"
        "v_cvt_pk_bf16_f32 v178, v184, v185\n"
        "v_cvt_pk_bf16_f32 v179, v186, v187\n"
        "v_mfma_f32_16x16x32_bf16 v[64:67], a[116:119], a[224:227], v[64:67]\n"
        "buffer_load_dwordx4 v130, s[20:23], s57 offen offset:2816 lds\n"
        "v_cvt_pk_bf16_f32 v158, v164, v165\n"
        "v_mfma_f32_16x16x32_bf16 v[72:75], a[140:143], a[224:227], v[72:75]\n"
        "s_add_u32 m0, s65, 8448\n"
        "v_cvt_pk_bf16_f32 v159, v166, v167\n"
        "v_cvt_pk_bf16_f32 v152, v152, v153\n"
        "v_mfma_f32_16x16x32_bf16 v[172:175], a[92:95], a[228:231], v[172:175]\n"
        "v_cvt_pk_bf16_f32 v153, v154, v155\n"
        "v_cvt_pk_bf16_f32 v154, v64, v65\n"
        "v_mfma_f32_16x16x32_bf16 v[160:163], a[68:71], a[224:227], v[160:163]\n"
        "v_cvt_pk_bf16_f32 v155, v66, v67\n"
        "v_cvt_pk_bf16_f32 v168, v72, v73\n"
        "v_mfma_f32_16x16x32_bf16 v[180:183], a[68:71], a[228:231], v[180:183]\n"
        "buffer_load_dwordx4 v128, s[20:23], s58 offen offset:768 lds\n"
        "v_mfma_f32_16x16x32_bf16 v[200:203], a[68:71], a[232:235], v[200:203]\n"
        "v_cvt_pk_bf16_f32 v169, v74, v75\n"
        "v_cvt_pk_bf16_f32 v172, v172, v173\n"
        "v_mfma_f32_16x16x32_bf16 v[220:223], a[68:71], a[236:239], v[220:223]\n"
        "v_cvt_pk_bf16_f32 v166, v160, v161\n"
        "v_cvt_pk_bf16_f32 v160, v160, v161\n"
        "v_cvt_pk_bf16_f32 v161, v162, v163\n"
        "v_cvt_pk_bf16_f32 v167, v162, v163\n"
        "v_cvt_pk_bf16_f32 v186, v180, v181\n"
        "v_cvt_pk_bf16_f32 v180, v180, v181\n"
        "v_mfma_f32_16x16x32_bf16 v[68:71], a[116:119], a[228:231], v[68:71]\n"
        "buffer_load_dwordx4 v129, s[20:23], s58 offen offset:1792 lds\n"
        "v_cvt_pk_bf16_f32 v181, v182, v183\n"
        "v_cvt_pk_bf16_f32 v187, v182, v183\n"
        "v_cvt_pk_bf16_f32 v206, v200, v201\n"
        "v_cvt_pk_bf16_f32 v200, v200, v201\n"
        "v_mfma_f32_16x16x32_bf16 v[76:79], a[140:143], a[228:231], v[76:79]\n"
        "s_waitcnt vmcnt(11)\n"
        "s_barrier\n"
        "ds_read_b128 v[162:165], v251 offset:0\n"
        "ds_read_b128 v[182:185], v251 offset:512\n"
        "v_cvt_pk_bf16_f32 v201, v202, v203\n"
        "v_cvt_pk_bf16_f32 v207, v202, v203\n"
        "v_cvt_pk_bf16_f32 v226, v220, v221\n"
        "v_cvt_pk_bf16_f32 v220, v220, v221\n"
        "v_mfma_f32_16x16x32_bf16 v[192:195], a[92:95], a[232:235], v[192:195]\n"
        "ds_read_b128 v[202:205], v251 offset:4096\n"
        "ds_read_b128 a[240:243], v240 offset:0\n"
        "v_cvt_pk_bf16_f32 v221, v222, v223\n"
        "v_cvt_pk_bf16_f32 v227, v222, v223\n"
        "v_cvt_pk_bf16_f32 v173, v174, v175\n"
        "v_mfma_f32_16x16x32_bf16 v[80:83], a[116:119], a[232:235], v[80:83]\n"
        "buffer_load_dwordx4 v130, s[20:23], s58 offen offset:2816 lds\n"
        "v_mfma_f32_16x16x32_bf16 v[84:87], a[140:143], a[232:235], v[84:87]\n"
        "s_add_u32 m0, s65, 11520\n"
        "ds_read_b128 a[244:247], v241 offset:0\n"
        "ds_read_b128 a[248:251], v240 offset:128\n"
        "v_mfma_f32_16x16x32_bf16 v[212:215], a[92:95], a[236:239], v[212:215]\n"
        "v_cvt_pk_bf16_f32 v174, v68, v69\n"
        "v_cvt_pk_bf16_f32 v175, v70, v71\n"
        "v_mfma_f32_16x16x32_bf16 v[88:91], a[116:119], a[236:239], v[88:91]\n"
        "buffer_load_dwordx4 v128, s[20:23], s59 offen offset:768 lds\n"
        "v_mfma_f32_16x16x32_bf16 v[92:95], a[140:143], a[236:239], v[92:95]\n"
        "s_waitcnt vmcnt(10)\n"
        "ds_read_b64_tr_b16 a[144:145], v255 offset:0\n"
        "ds_read_b64_tr_b16 a[146:147], v255 offset:768\n"
        "v_mfma_f32_16x16x32_bf16 v[64:67], v[152:155], v[156:159], v[0:3]\n"
        "s_waitcnt lgkmcnt(6)\n"
        "ds_read_b64_tr_b16 a[168:169], v255 offset:32\n"
        "ds_read_b64_tr_b16 a[170:171], v255 offset:800\n"
        "v_cvt_pk_bf16_f32 v190, v76, v77\n"
        "v_cvt_pk_bf16_f32 v191, v78, v79\n"
        "v_mfma_f32_16x16x32_bf16 v[64:67], v[168:171], v[160:163], v[64:67]\n"
        "ds_read_b64_tr_b16 a[192:193], v255 offset:64\n"
        "ds_read_b64_tr_b16 a[194:195], v255 offset:832\n"
        "v_cvt_pk_bf16_f32 v192, v192, v193\n"
        "v_cvt_pk_bf16_f32 v193, v194, v195\n"
        "v_mfma_f32_16x16x32_bf16 v[96:99], v[172:175], v[156:159], v[4:7]\n"
        "buffer_load_dwordx4 v129, s[20:23], s59 offen offset:1792 lds\n"
        "v_cvt_pk_bf16_f32 v194, v80, v81\n"
        "v_cvt_pk_bf16_f32 v195, v82, v83\n"
        "v_exp_f32 v64, v64\n"
        "v_mfma_f32_16x16x32_bf16 v[96:99], v[188:191], v[164:167], v[96:99]\n"
        "ds_read_b128 v[162:165], v252 offset:0\n"
        "v_exp_f32 v65, v65\n"
        "v_exp_f32 v66, v66\n"
        "v_cvt_pk_bf16_f32 v64, v64, v65\n"
        "v_mfma_f32_16x16x32_bf16 v[72:75], v[152:155], v[176:179], v[16:19]\n"
        "buffer_load_dwordx4 v130, s[20:23], s59 offen offset:2816 lds\n"
        "v_cvt_pk_bf16_f32 v208, v84, v85\n"
        "v_cvt_pk_bf16_f32 v209, v86, v87\n"
        "v_exp_f32 v96, v96\n"
        "v_mfma_f32_16x16x32_bf16 v[72:75], v[168:171], v[180:183], v[72:75]\n"
        "s_add_u32 m0, s65, 14592\n"
        "s_waitcnt vmcnt(9)\n"
        "ds_read_b64_tr_b16 a[148:149], v255 offset:3072\n"
        "ds_read_b64_tr_b16 a[150:151], v255 offset:3840\n"
        "v_exp_f32 v67, v67\n"
        "v_exp_f32 v97, v97\n"
        "v_mfma_f32_16x16x32_bf16 v[100:103], v[172:175], v[176:179], v[20:23]\n"
        "buffer_load_dwordx4 v128, s[20:23], s60 offen offset:768 lds\n"
        "ds_read_b64_tr_b16 a[172:173], v255 offset:3104\n"
        "ds_read_b64_tr_b16 a[174:175], v255 offset:3872\n"
        "v_cvt_pk_bf16_f32 v212, v212, v213\n"
        "v_cvt_pk_bf16_f32 v213, v214, v215\n"
        "v_mfma_f32_16x16x32_bf16 v[100:103], v[188:191], v[184:187], v[100:103]\n"
        "ds_read_b128 v[182:185], v252 offset:512\n"
        "ds_read_b64_tr_b16 a[196:197], v255 offset:3136\n"
        "ds_read_b64_tr_b16 a[198:199], v255 offset:3904\n"
        "v_cvt_pk_bf16_f32 v65, v66, v67\n"
        "v_exp_f32 v98, v98\n"
        "v_mfma_f32_16x16x32_bf16 v[68:71], v[192:195], v[156:159], v[8:11]\n"
        "s_waitcnt lgkmcnt(7)\n"
        "ds_read_b128 a[216:219], v240 offset:1536\n"
        "ds_read_b128 a[220:223], v241 offset:1536\n"
        "v_cvt_pk_bf16_f32 v66, v96, v97\n"
        "v_exp_f32 v72, v72\n"
        "v_cvt_pk_bf16_f32 v214, v88, v89\n"
        "v_mfma_f32_16x16x32_bf16 v[68:71], v[208:211], v[160:163], v[68:71]\n"
        "buffer_load_dwordx4 v129, s[20:23], s60 offen offset:1792 lds\n"
        "s_waitcnt vmcnt(8)\n"
        "ds_read_b64_tr_b16 a[152:153], v255 offset:6144\n"
        "ds_read_b64_tr_b16 a[154:155], v255 offset:6912\n"
        "v_cvt_pk_bf16_f32 v215, v90, v91\n"
        "v_cvt_pk_bf16_f32 v230, v92, v93\n"
        "v_mfma_f32_16x16x32_bf16 v[76:79], v[192:195], v[176:179], v[24:27]\n"
        "s_waitcnt lgkmcnt(6)\n"
        "ds_read_b64_tr_b16 a[176:177], v255 offset:6176\n"
        "ds_read_b64_tr_b16 a[178:179], v255 offset:6944\n"
        "v_exp_f32 v99, v99\n"
        "v_exp_f32 v73, v73\n"
        "v_mfma_f32_16x16x32_bf16 v[76:79], v[208:211], v[180:183], v[76:79]\n"
        "buffer_load_dwordx4 v130, s[20:23], s60 offen offset:2816 lds\n"
        "ds_read_b64_tr_b16 a[200:201], v255 offset:6208\n"
        "ds_read_b64_tr_b16 a[202:203], v255 offset:6976\n"
        "v_cvt_pk_bf16_f32 v231, v94, v95\n"
        "v_cvt_pk_bf16_f32 v67, v98, v99\n"
        "v_mfma_f32_16x16x32_bf16 v[104:107], v[212:215], v[156:159], v[12:15]\n"
        ".long 0xbec200ff\n"
        "42:\n"
        "s_branch 60b\n"
        "s_add_u32 m0, s40, 98304\n"
        "ds_read_b128 a[224:227], v240 offset:1664\n"
        "v_exp_f32 v74, v74\n"
        "v_mfma_f32_16x16x32_bf16 v[104:107], v[228:231], v[164:167], v[104:107]\n"
        "s_sub_u32 s64, s47, 6291456\n"
        "v_cvt_pk_bf16_f32 v72, v72, v73\n"
        "v_exp_f32 v100, v100\n"
        "v_mfma_f32_16x16x32_bf16 v[108:111], v[212:215], v[176:179], v[28:31]\n"
        "buffer_load_dwordx4 v242, s[8:11], s64 offen lds\n"
        "v_mfma_f32_16x16x32_bf16 v[108:111], v[228:231], v[184:187], v[108:111]\n"
        "s_add_u32 m0, s40, 99328\n"
        "v_exp_f32 v75, v75\n"
        "v_mfma_f32_16x16x32_bf16 v[80:83], v[152:155], v[196:199], v[32:35]\n"
        "ds_read_b128 a[228:231], v240 offset:12288\n"
        "v_exp_f32 v101, v101\n"
        "v_mfma_f32_16x16x32_bf16 v[80:83], v[168:171], v[200:203], v[80:83]\n"
        "buffer_load_dwordx4 v243, s[8:11], s64 offen lds\n"
        "v_cvt_pk_bf16_f32 v73, v74, v75\n"
        "v_mfma_f32_16x16x32_bf16 v[112:115], a[240:243], a[144:147], 0\n"
        "s_add_u32 m0, s40, 100352\n"
        "ds_read_b128 a[232:235], v241 offset:12288\n"
        "ds_read_b128 a[236:239], v240 offset:12416\n"
        "v_cvt_pk_bf16_f32 v74, v100, v101\n"
        "v_mfma_f32_16x16x32_bf16 v[112:115], a[244:247], a[148:151], v[112:115]\n"
        "s_waitcnt lgkmcnt(3)\n"
        "v_exp_f32 v102, v102\n"
        "v_mfma_f32_16x16x32_bf16 v[112:115], a[248:251], a[152:155], v[112:115]\n"
        "buffer_load_dwordx4 v244, s[8:11], s64 offen lds\n"
        "v_mfma_f32_16x16x32_bf16 v[116:119], a[248:251], a[176:179], 0\n"
        "s_add_u32 m0, s40, 110592\n"
        "v_exp_f32 v68, v68\n"
        "v_mfma_f32_16x16x32_bf16 v[116:119], a[244:247], a[172:175], v[116:119]\n"
        "s_sub_u32 s64, s47, 5505024\n"
        "v_exp_f32 v103, v103\n"
        "v_mfma_f32_16x16x32_bf16 v[116:119], a[240:243], a[168:171], v[116:119]\n"
        "buffer_load_dwordx4 v242, s[8:11], s64 offen lds\n"
        "v_cvt_pk_bf16_f32 v75, v102, v103\n"
        "v_mfma_f32_16x16x32_bf16 v[96:99], v[172:175], v[196:199], v[36:39]\n"
        "s_add_u32 m0, s40, 111616\n"
        "s_waitcnt vmcnt(10)\n"
        "ds_read_b64_tr_b16 a[156:157], v255 offset:9216\n"
        "ds_read_b64_tr_b16 a[158:159], v255 offset:9984\n"
        "v_mfma_f32_16x16x32_bf16 v[96:99], v[188:191], v[204:207], v[96:99]\n"
        "ds_read_b64_tr_b16 a[180:181], v255 offset:9248\n"
        "ds_read_b64_tr_b16 a[182:183], v255 offset:10016\n"
        "ds_read_b128 v[202:205], v252 offset:4096\n"
        "v_mfma_f32_16x16x32_bf16 v[120:123], a[240:243], a[192:195], 0\n"
        "buffer_load_dwordx4 v243, s[8:11], s64 offen lds\n"
        "ds_read_b64_tr_b16 a[204:205], v255 offset:9280\n"
        "v_mfma_f32_16x16x32_bf16 v[120:123], a[244:247], a[196:199], v[120:123]\n"
        "ds_read_b64_tr_b16 a[206:207], v255 offset:10048\n"
        "s_add_u32 m0, s40, 112640\n"
        "ds_read_b128 a[240:243], v240 offset:13824\n"
        "v_mfma_f32_16x16x32_bf16 v[120:123], a[248:251], a[200:203], v[120:123]\n"
        "s_waitcnt vmcnt(8)\n"
        "ds_read_b64_tr_b16 a[160:161], v255 offset:12288\n"
        "ds_read_b64_tr_b16 a[162:163], v255 offset:13056\n"
        "v_mfma_f32_16x16x32_bf16 v[124:127], a[216:219], a[144:147], 0\n"
        "buffer_load_dwordx4 v244, s[8:11], s64 offen lds\n"
        "ds_read_b64_tr_b16 a[184:185], v255 offset:12320\n"
        "v_mfma_f32_16x16x32_bf16 v[124:127], a[220:223], a[148:151], v[124:127]\n"
        "ds_read_b64_tr_b16 a[186:187], v255 offset:13088\n"
        "s_xor_b32 m0, s48, 8192\n"
        "ds_read_b64_tr_b16 a[208:209], v255 offset:12352\n"
        "ds_read_b64_tr_b16 a[210:211], v255 offset:13120\n"
        "v_mfma_f32_16x16x32_bf16 v[124:127], a[224:227], a[152:155], v[124:127]\n"
        "s_sub_u32 s64, s36, 2097152\n"
        "ds_read_b128 a[244:247], v241 offset:13824\n"
        "ds_read_b128 a[248:251], v240 offset:13952\n"
        "v_mfma_f32_16x16x32_bf16 v[156:159], a[224:227], a[176:179], 0\n"
        "buffer_load_dwordx4 v249, s[12:15], s64 offen lds\n"
        "v_mfma_f32_16x16x32_bf16 v[156:159], a[220:223], a[172:175], v[156:159]\n"
        "ds_read_b128 v[222:225], v251 offset:4608\n"
        "v_exp_f32 v69, v69\n"
        "v_mfma_f32_16x16x32_bf16 v[156:159], a[216:219], a[168:171], v[156:159]\n"
        "v_exp_f32 v70, v70\n"
        "v_cvt_pk_bf16_f32 v68, v68, v69\n"
        "v_mfma_f32_16x16x32_bf16 v[84:87], v[192:195], v[196:199], v[40:43]\n"
        "s_waitcnt lgkmcnt(7)\n"
        "v_exp_f32 v76, v76\n"
        "v_mfma_f32_16x16x32_bf16 v[84:87], v[208:211], v[200:203], v[84:87]\n"
        "buffer_load_dwordx4 v253, s[12:15], s64 offen offset:1024 lds\n"
        "v_mfma_f32_16x16x32_bf16 v[160:163], a[216:219], a[192:195], 0\n"
        "s_add_u32 m0, s65, 12288\n"
        "v_exp_f32 v71, v71\n"
        "v_mfma_f32_16x16x32_bf16 v[160:163], a[220:223], a[196:199], v[160:163]\n"
        "ds_read_b128 a[216:219], v241 offset:128\n"
        "v_exp_f32 v77, v77\n"
        "v_mfma_f32_16x16x32_bf16 v[160:163], a[224:227], a[200:203], v[160:163]\n"
        "ds_read_b128 a[220:223], v240 offset:256\n"
        "v_cvt_pk_bf16_f32 v69, v70, v71\n"
        "v_cvt_pk_bf16_f32 v76, v76, v77\n"
        "v_mfma_f32_16x16x32_bf16 v[164:167], a[228:231], a[144:147], 0\n"
        "s_waitcnt lgkmcnt(2)\n"
        "buffer_load_dword v136, s[52:55], 0 offen lds\n"
        "v_mfma_f32_16x16x32_bf16 v[164:167], a[232:235], a[148:151], v[164:167]\n"
        "ds_read_b128 a[224:227], v241 offset:256\n"
        "v_exp_f32 v78, v78\n"
        "v_mfma_f32_16x16x32_bf16 v[164:167], a[236:239], a[152:155], v[164:167]\n"
        "v_exp_f32 v104, v104\n"
        "v_mfma_f32_16x16x32_bf16 v[100:103], v[212:215], v[196:199], v[44:47]\n"
        "v_exp_f32 v79, v79\n"
        "v_mfma_f32_16x16x32_bf16 v[100:103], v[228:231], v[204:207], v[100:103]\n"
        "v_exp_f32 v105, v105\n"
        "v_cvt_pk_bf16_f32 v77, v78, v79\n"
        "v_mfma_f32_16x16x32_bf16 v[176:179], a[236:239], a[176:179], 0\n"
        "v_exp_f32 v106, v106\n"
        "v_cvt_pk_bf16_f32 v70, v104, v105\n"
        "v_mfma_f32_16x16x32_bf16 v[176:179], a[232:235], a[172:175], v[176:179]\n"
        "v_exp_f32 v108, v108\n"
        "v_mfma_f32_16x16x32_bf16 v[176:179], a[228:231], a[168:171], v[176:179]\n"
        "s_waitcnt vmcnt(9)\n"
        "ds_read_b64_tr_b16 a[164:165], v255 offset:15360\n"
        "ds_read_b64_tr_b16 a[166:167], v255 offset:16128\n"
        "v_mfma_f32_16x16x32_bf16 v[180:183], a[228:231], a[192:195], 0\n"
        "ds_read_b64_tr_b16 a[188:189], v255 offset:15392\n"
        "ds_read_b64_tr_b16 a[190:191], v255 offset:16160\n"
        "v_mfma_f32_16x16x32_bf16 v[180:183], a[232:235], a[196:199], v[180:183]\n"
        "ds_read_b64_tr_b16 a[212:213], v255 offset:15424\n"
        "ds_read_b64_tr_b16 a[214:215], v255 offset:16192\n"
        "v_mfma_f32_16x16x32_bf16 v[180:183], a[236:239], a[200:203], v[180:183]\n"
        "ds_read_b128 a[228:231], v241 offset:1664\n"
        "ds_read_b128 a[232:235], v240 offset:1792\n"
        "v_mfma_f32_16x16x32_bf16 v[184:187], a[240:243], a[144:147], 0\n"
        "ds_read_b128 a[236:239], v241 offset:1792\n"
        "v_exp_f32 v107, v107\n"
        "v_mfma_f32_16x16x32_bf16 v[184:187], a[244:247], a[148:151], v[184:187]\n"
        "v_exp_f32 v109, v109\n"
        "v_cvt_pk_bf16_f32 v71, v106, v107\n"
        "v_mfma_f32_16x16x32_bf16 v[184:187], a[248:251], a[152:155], v[184:187]\n"
        "v_exp_f32 v110, v110\n"
        "v_cvt_pk_bf16_f32 v78, v108, v109\n"
        "v_mfma_f32_16x16x32_bf16 v[196:199], a[248:251], a[176:179], 0\n"
        "v_exp_f32 v111, v111\n"
        "v_mfma_f32_16x16x32_bf16 v[196:199], a[244:247], a[172:175], v[196:199]\n"
        "v_cvt_pk_bf16_f32 v79, v110, v111\n"
        "v_exp_f32 v80, v80\n"
        "v_mfma_f32_16x16x32_bf16 v[196:199], a[240:243], a[168:171], v[196:199]\n"
        "v_exp_f32 v81, v81\n"
        "v_mfma_f32_16x16x32_bf16 v[200:203], a[240:243], a[192:195], 0\n"
        "v_exp_f32 v82, v82\n"
        "v_cvt_pk_bf16_f32 v80, v80, v81\n"
        "v_mfma_f32_16x16x32_bf16 v[200:203], a[244:247], a[196:199], v[200:203]\n"
        "ds_read_b128 a[240:243], v241 offset:12416\n"
        "v_exp_f32 v83, v83\n"
        "v_mfma_f32_16x16x32_bf16 v[200:203], a[248:251], a[200:203], v[200:203]\n"
        "ds_read_b128 a[244:247], v240 offset:12544\n"
        "v_cvt_pk_bf16_f32 v81, v82, v83\n"
        "v_mfma_f32_16x16x32_bf16 v[88:91], v[152:155], v[216:219], v[48:51]\n"
        "ds_read_b128 a[248:251], v241 offset:12544\n"
        "v_exp_f32 v96, v96\n"
        "v_mfma_f32_16x16x32_bf16 v[88:91], v[168:171], v[220:223], v[88:91]\n"
        "s_waitcnt lgkmcnt(3)\n"
        "v_exp_f32 v97, v97\n"
        "v_mfma_f32_16x16x32_bf16 v[112:115], a[216:219], a[156:159], v[112:115]\n"
        "v_exp_f32 v98, v98\n"
        "v_cvt_pk_bf16_f32 v82, v96, v97\n"
        "v_mfma_f32_16x16x32_bf16 v[112:115], a[220:223], a[160:163], v[112:115]\n"
        "v_exp_f32 v99, v99\n"
        "v_mfma_f32_16x16x32_bf16 v[112:115], a[224:227], a[164:167], v[112:115]\n"
        "v_cvt_pk_bf16_f32 v83, v98, v99\n"
        "v_exp_f32 v84, v84\n"
        "v_mfma_f32_16x16x32_bf16 v[116:119], a[224:227], a[188:191], v[116:119]\n"
        "v_exp_f32 v85, v85\n"
        "v_mfma_f32_16x16x32_bf16 v[116:119], a[220:223], a[184:187], v[116:119]\n"
        "v_exp_f32 v86, v86\n"
        "v_cvt_pk_bf16_f32 v84, v84, v85\n"
        "v_mfma_f32_16x16x32_bf16 v[116:119], a[216:219], a[180:183], v[116:119]\n"
        "v_exp_f32 v87, v87\n"
        "v_mfma_f32_16x16x32_bf16 v[120:123], a[216:219], a[204:207], v[120:123]\n"
        "v_cvt_pk_bf16_f32 v85, v86, v87\n"
        "v_exp_f32 v100, v100\n"
        "v_mfma_f32_16x16x32_bf16 v[120:123], a[220:223], a[208:211], v[120:123]\n"
        "ds_read_b128 a[216:219], v241 offset:13952\n"
        "v_exp_f32 v101, v101\n"
        "v_mfma_f32_16x16x32_bf16 v[120:123], a[224:227], a[212:215], v[120:123]\n"
        "ds_read_b128 a[220:223], v240 offset:14080\n"
        "v_exp_f32 v102, v102\n"
        "v_mfma_f32_16x16x32_bf16 v[124:127], a[228:231], a[156:159], v[124:127]\n"
        "ds_read_b128 a[224:227], v241 offset:14080\n"
        "v_cvt_pk_bf16_f32 v86, v100, v101\n"
        "v_cvt_pk_bf16_f32 v128, v112, v113\n"
        "v_mfma_f32_16x16x32_bf16 v[124:127], a[232:235], a[160:163], v[124:127]\n"
        "v_exp_f32 v103, v103\n"
        "v_cvt_pk_bf16_f32 v129, v114, v115\n"
        "v_mfma_f32_16x16x32_bf16 v[124:127], a[236:239], a[164:167], v[124:127]\n"
        "v_cvt_pk_bf16_f32 v87, v102, v103\n"
        "v_cvt_pk_bf16_f32 v136, v116, v117\n"
        "v_mfma_f32_16x16x32_bf16 v[104:107], v[172:175], v[216:219], v[52:55]\n"
        "v_cvt_pk_bf16_f32 v137, v118, v119\n"
        "v_cvt_pk_bf16_f32 v144, v120, v121\n"
        "v_mfma_f32_16x16x32_bf16 v[104:107], v[188:191], v[224:227], v[104:107]\n"
        "ds_read_b128 v[222:225], v252 offset:4608\n"
        "v_cvt_pk_bf16_f32 v145, v122, v123\n"
        "v_mfma_f32_16x16x32_bf16 v[156:159], a[236:239], a[188:191], v[156:159]\n"
        "v_exp_f32 v88, v88\n"
        "v_mfma_f32_16x16x32_bf16 v[156:159], a[232:235], a[184:187], v[156:159]\n"
        "v_cvt_pk_bf16_f32 v130, v124, v125\n"
        "v_cvt_pk_bf16_f32 v131, v126, v127\n"
        "v_mfma_f32_16x16x32_bf16 v[156:159], a[228:231], a[180:183], v[156:159]\n"
        "v_exp_f32 v89, v89\n"
        "v_mfma_f32_16x16x32_bf16 v[160:163], a[228:231], a[204:207], v[160:163]\n"
        "v_exp_f32 v90, v90\n"
        "v_mfma_f32_16x16x32_bf16 v[160:163], a[232:235], a[208:211], v[160:163]\n"
        "v_exp_f32 v91, v91\n"
        "v_mfma_f32_16x16x32_bf16 v[160:163], a[236:239], a[212:215], v[160:163]\n"
        "s_waitcnt lgkmcnt(1)\n"
        "v_exp_f32 v104, v104\n"
        "v_mfma_f32_16x16x32_bf16 v[164:167], a[240:243], a[156:159], v[164:167]\n"
        "v_cvt_pk_bf16_f32 v138, v156, v157\n"
        "v_cvt_pk_bf16_f32 v139, v158, v159\n"
        "v_mfma_f32_16x16x32_bf16 v[164:167], a[244:247], a[160:163], v[164:167]\n"
        "v_cvt_pk_bf16_f32 v88, v88, v89\n"
        "v_cvt_pk_bf16_f32 v89, v90, v91\n"
        "v_mfma_f32_16x16x32_bf16 v[164:167], a[248:251], a[164:167], v[164:167]\n"
        "v_exp_f32 v105, v105\n"
        "v_mfma_f32_16x16x32_bf16 v[92:95], v[192:195], v[216:219], v[56:59]\n"
        "s_waitcnt lgkmcnt(0)\n"
        "v_cvt_pk_bf16_f32 v146, v160, v161\n"
        "v_cvt_pk_bf16_f32 v147, v162, v163\n"
        "v_mfma_f32_16x16x32_bf16 v[92:95], v[208:211], v[220:223], v[92:95]\n"
        "v_exp_f32 v106, v106\n"
        "v_mfma_f32_16x16x32_bf16 v[176:179], a[248:251], a[188:191], v[176:179]\n"
        "v_exp_f32 v107, v107\n"
        "v_mfma_f32_16x16x32_bf16 v[176:179], a[244:247], a[184:187], v[176:179]\n"
        "v_cvt_pk_bf16_f32 v132, v164, v165\n"
        "v_cvt_pk_bf16_f32 v133, v166, v167\n"
        "v_mfma_f32_16x16x32_bf16 v[176:179], a[240:243], a[180:183], v[176:179]\n"
        "v_cvt_pk_bf16_f32 v90, v104, v105\n"
        "v_cvt_pk_bf16_f32 v91, v106, v107\n"
        "v_mfma_f32_16x16x32_bf16 v[180:183], a[240:243], a[204:207], v[180:183]\n"
        "v_exp_f32 v92, v92\n"
        "v_mfma_f32_16x16x32_bf16 v[180:183], a[244:247], a[208:211], v[180:183]\n"
        "v_exp_f32 v93, v93\n"
        "v_mfma_f32_16x16x32_bf16 v[180:183], a[248:251], a[212:215], v[180:183]\n"
        "v_mov_b64 v[96:97], 0\n"
        "v_cvt_pk_bf16_f32 v140, v176, v177\n"
        "v_mfma_f32_16x16x32_bf16 v[184:187], a[216:219], a[156:159], v[184:187]\n"
        "v_cvt_pk_bf16_f32 v141, v178, v179\n"
        "v_cvt_pk_bf16_f32 v92, v92, v93\n"
        "v_mfma_f32_16x16x32_bf16 v[184:187], a[220:223], a[160:163], v[184:187]\n"
        "v_exp_f32 v94, v94\n"
        "v_mfma_f32_16x16x32_bf16 v[184:187], a[224:227], a[164:167], v[184:187]\n"
        "v_exp_f32 v95, v95\n"
        "v_mfma_f32_16x16x32_bf16 v[108:111], v[212:215], v[216:219], v[60:63]\n"
        "v_cvt_pk_bf16_f32 v148, v180, v181\n"
        "v_cvt_pk_bf16_f32 v149, v182, v183\n"
        "v_mfma_f32_16x16x32_bf16 v[108:111], v[228:231], v[224:227], v[108:111]\n"
        "v_add_u32 v240, 24576, v240\n"
        "v_add_u32 v241, 24576, v241\n"
        "v_mfma_f32_16x16x32_bf16 v[196:199], a[224:227], a[188:191], v[196:199]\n"
        "v_cvt_pk_bf16_f32 v134, v184, v185\n"
        "v_cvt_pk_bf16_f32 v135, v186, v187\n"
        "v_mfma_f32_16x16x32_bf16 v[196:199], a[220:223], a[184:187], v[196:199]\n"
        "v_mov_b64 v[172:173], 0\n"
        "v_cvt_pk_bf16_f32 v93, v94, v95\n"
        "v_mfma_f32_16x16x32_bf16 v[196:199], a[216:219], a[180:183], v[196:199]\n"
        "v_exp_f32 v108, v108\n"
        "v_mfma_f32_16x16x32_bf16 v[200:203], a[216:219], a[204:207], v[200:203]\n"
        "v_exp_f32 v109, v109\n"
        "v_mfma_f32_16x16x32_bf16 v[200:203], a[220:223], a[208:211], v[200:203]\n"
        "v_exp_f32 v110, v110\n"
        "v_mfma_f32_16x16x32_bf16 v[200:203], a[224:227], a[212:215], v[200:203]\n"
        "v_exp_f32 v111, v111\n"
        "v_smfmac_f32_16x16x64_bf16 v[96:99], a[252:255], v[64:71], v237\n"
        "v_cvt_pk_bf16_f32 v142, v196, v197\n"
        "v_mfma_f32_16x16x32_bf16 v[100:103], v[132:135], v[68:71], 0\n"
        "v_cvt_pk_bf16_f32 v143, v198, v199\n"
        "v_cvt_pk_bf16_f32 v94, v108, v109\n"
        "v_mfma_f32_16x16x32_bf16 v[100:103], v[128:131], v[64:67], v[100:103]\n"
        "v_mov_b64 v[192:193], 0\n"
        "v_cvt_pk_bf16_f32 v150, v200, v201\n"
        "v_mfma_f32_16x16x32_bf16 v[104:107], v[136:139], v[64:67], 0\n"
        "v_cvt_pk_bf16_f32 v151, v202, v203\n"
        "v_add_f32 v97, v96, v97\n"
        "v_mfma_f32_16x16x32_bf16 v[104:107], v[140:143], v[68:71], v[104:107]\n"
        "v_rcp_f32 v96, v97\n"
        "v_cvt_pk_bf16_f32 v95, v110, v111\n"
        "v_mfma_f32_16x16x32_bf16 v[108:111], v[148:151], v[68:71], 0\n"
        "v_mul_f32 v100, v96, v100\n"
        "v_mul_f32 v101, v96, v101\n"
        "v_mfma_f32_16x16x32_bf16 v[108:111], v[144:147], v[64:67], v[108:111]\n"
        "v_mul_f32 v102, v96, v102\n"
        "v_mul_f32 v103, v96, v103\n"
        "v_smfmac_f32_16x16x64_bf16 v[172:175], a[252:255], v[72:79], v237\n"
        "s_waitcnt vmcnt(6)\n"
        "s_barrier\n"
        "ds_read_b128 a[236:239], v240 offset:0\n"
        "v_mfma_f32_16x16x32_bf16 v[176:179], v[132:135], v[76:79], 0\n"
        "ds_read_b128 a[240:243], v241 offset:0\n"
        "ds_read_b128 a[244:247], v240 offset:128\n"
        "v_mfma_f32_16x16x32_bf16 v[176:179], v[128:131], v[72:75], v[176:179]\n"
        "ds_read_b128 a[248:251], v241 offset:128\n"
        "ds_read_b128 a[216:219], v240 offset:256\n"
        "v_mfma_f32_16x16x32_bf16 v[180:183], v[136:139], v[72:75], 0\n"
        "ds_read_b128 a[220:223], v241 offset:256\n"
        "v_cvt_pk_bf16_f32 v100, v100, v101\n"
        "v_mfma_f32_16x16x32_bf16 v[180:183], v[140:143], v[76:79], v[180:183]\n"
        "v_cvt_pk_bf16_f32 v101, v102, v103\n"
        "v_mul_f32 v104, v96, v104\n"
        "v_mfma_f32_16x16x32_bf16 v[184:187], v[148:151], v[76:79], 0\n"
        "v_mul_f32 v105, v96, v105\n"
        "v_mul_f32 v106, v96, v106\n"
        "v_mfma_f32_16x16x32_bf16 v[184:187], v[144:147], v[72:75], v[184:187]\n"
        "v_mul_f32 v107, v96, v107\n"
        "v_cvt_pk_bf16_f32 v102, v104, v105\n"
        "v_smfmac_f32_16x16x64_bf16 v[192:195], a[252:255], v[80:87], v237\n"
        "v_cvt_pk_bf16_f32 v103, v106, v107\n"
        "v_mfma_f32_16x16x32_bf16 v[196:199], v[132:135], v[84:87], 0\n"
        "ds_write_b128 v245, v[100:103] offset:0\n"
        "v_mfma_f32_16x16x32_bf16 v[196:199], v[128:131], v[80:83], v[196:199]\n"
        "buffer_load_dwordx4 v238, s[52:55], s63 offen lds\n"
        "v_mfma_f32_16x16x32_bf16 v[200:203], v[136:139], v[80:83], 0\n"
        "v_mov_b64 v[124:125], 0\n"
        "v_mul_f32 v108, v96, v108\n"
        "v_mfma_f32_16x16x32_bf16 v[200:203], v[140:143], v[84:87], v[200:203]\n"
        "v_mul_f32 v109, v96, v109\n"
        "v_mul_f32 v110, v96, v110\n"
        "v_mul_f32 v111, v96, v111\n"
        "v_mfma_f32_16x16x32_bf16 v[204:207], v[148:151], v[84:87], 0\n"
        "v_cvt_pk_bf16_f32 v108, v108, v109\n"
        "v_cvt_pk_bf16_f32 v109, v110, v111\n"
        "v_mfma_f32_16x16x32_bf16 v[204:207], v[144:147], v[80:83], v[204:207]\n"
        "s_waitcnt lgkmcnt(1)\n"
        "ds_write_b64 v246, v[108:109] offset:0\n"
        "v_mfma_f32_16x16x32_bf16 v[112:115], a[72:75], a[236:239], 0\n"
        "v_max_u32 v236, v96, v97\n"
        "v_add_f32 v173, v172, v173\n"
        "v_mfma_f32_16x16x32_bf16 v[112:115], a[76:79], a[240:243], v[112:115]\n"
        "v_rcp_f32 v172, v173\n"
        "v_mfma_f32_16x16x32_bf16 v[112:115], a[80:83], a[244:247], v[112:115]\n"
        "v_mul_f32 v176, v172, v176\n"
        "v_mul_f32 v177, v172, v177\n"
        "v_mfma_f32_16x16x32_bf16 v[112:115], a[84:87], a[248:251], v[112:115]\n"
        "buffer_load_dwordx4 v239, s[52:55], s63 offen offset:1024 lds\n"
        "v_mfma_f32_16x16x32_bf16 v[112:115], a[88:91], a[216:219], v[112:115]\n"
        "v_mul_f32 v178, v172, v178\n"
        "v_mul_f32 v179, v172, v179\n"
        "v_mfma_f32_16x16x32_bf16 v[112:115], a[92:95], a[220:223], v[112:115]\n"
        "v_cvt_pk_bf16_f32 v176, v176, v177\n"
        "v_cvt_pk_bf16_f32 v177, v178, v179\n"
        "v_smfmac_f32_16x16x64_bf16 v[124:127], a[252:255], v[88:95], v237\n"
        "v_mul_f32 v180, v172, v180\n"
        "v_mfma_f32_16x16x32_bf16 v[80:83], v[132:135], v[92:95], 0\n"
        "v_mul_f32 v181, v172, v181\n"
        "v_mul_f32 v182, v172, v182\n"
        "v_mfma_f32_16x16x32_bf16 v[80:83], v[128:131], v[88:91], v[80:83]\n"
        "v_cvt_pk_bf16_f32 v152, v112, v113\n"
        "v_cvt_pk_bf16_f32 v153, v114, v115\n"
        "v_mfma_f32_16x16x32_bf16 v[84:87], v[136:139], v[88:91], 0\n"
        "v_mul_f32 v183, v172, v183\n"
        "v_cvt_pk_bf16_f32 v178, v180, v181\n"
        "v_mfma_f32_16x16x32_bf16 v[84:87], v[140:143], v[92:95], v[84:87]\n"
        "v_cvt_pk_bf16_f32 v179, v182, v183\n"
        "v_mul_f32 v184, v172, v184\n"
        "v_mfma_f32_16x16x32_bf16 v[96:99], v[148:151], v[92:95], 0\n"
        "buffer_load_dwordx4 v247, s[52:55], s63 offen offset:2048 lds\n"
        "v_mfma_f32_16x16x32_bf16 v[96:99], v[144:147], v[88:91], v[96:99]\n"
        "ds_write_b128 v245, v[176:179] offset:256\n"
        "v_mfma_f32_16x16x32_bf16 v[116:119], a[116:119], a[220:223], 0\n"
        "v_mul_f32 v185, v172, v185\n"
        "v_mul_f32 v186, v172, v186\n"
        "v_mfma_f32_16x16x32_bf16 v[116:119], a[112:115], a[216:219], v[116:119]\n"
        "v_mul_f32 v187, v172, v187\n"
        "v_add_f32 v125, v124, v125\n"
        "v_mfma_f32_16x16x32_bf16 v[116:119], a[108:111], a[248:251], v[116:119]\n"
        "v_rcp_f32 v124, v125\n"
        "v_mfma_f32_16x16x32_bf16 v[116:119], a[104:107], a[244:247], v[116:119]\n"
        "s_add_u32 m0, s65, 15360\n"
        "v_cvt_pk_bf16_f32 v184, v184, v185\n"
        "v_cvt_pk_bf16_f32 v185, v186, v187\n"
        "v_mfma_f32_16x16x32_bf16 v[116:119], a[100:103], a[240:243], v[116:119]\n"
        "ds_write_b64 v246, v[184:185] offset:256\n"
        "v_mfma_f32_16x16x32_bf16 v[116:119], a[96:99], a[236:239], v[116:119]\n"
        "v_mul_f32 v80, v124, v80\n"
        "v_mul_f32 v81, v124, v81\n"
        "v_mfma_f32_16x16x32_bf16 v[120:123], a[120:123], a[236:239], 0\n"
        "buffer_load_dwordx4 v238, s[52:55], s34 offen lds\n"
        "v_mfma_f32_16x16x32_bf16 v[120:123], a[124:127], a[240:243], v[120:123]\n"
        "v_mul_f32 v82, v124, v82\n"
        "v_mul_f32 v83, v124, v83\n"
        "v_mfma_f32_16x16x32_bf16 v[120:123], a[128:131], a[244:247], v[120:123]\n"
        "v_max3_u32 v236, v236, v172, v173\n"
        "v_cvt_pk_bf16_f32 v80, v80, v81\n"
        "v_mfma_f32_16x16x32_bf16 v[120:123], a[132:135], a[248:251], v[120:123]\n"
        "v_cvt_pk_bf16_f32 v81, v82, v83\n"
        "v_mul_f32 v84, v124, v84\n"
        "v_mfma_f32_16x16x32_bf16 v[120:123], a[136:139], a[216:219], v[120:123]\n"
        "v_mul_f32 v85, v124, v85\n"
        "v_mul_f32 v86, v124, v86\n"
        "v_mfma_f32_16x16x32_bf16 v[120:123], a[140:143], a[220:223], v[120:123]\n"
        "v_mul_f32 v87, v124, v87\n"
        "v_cvt_pk_bf16_f32 v82, v84, v85\n"
        "v_mfma_f32_16x16x32_bf16 v[112:115], a[20:23], a[220:223], 0\n"
        "v_cvt_pk_bf16_f32 v83, v86, v87\n"
        "v_mul_f32 v96, v124, v96\n"
        "v_mfma_f32_16x16x32_bf16 v[112:115], a[16:19], a[216:219], v[112:115]\n"
        "ds_write_b128 v245, v[80:83] offset:768\n"
        "v_mfma_f32_16x16x32_bf16 v[112:115], a[12:15], a[248:251], v[112:115]\n"
        "buffer_load_dwordx4 v239, s[52:55], s34 offen offset:1024 lds\n"
        "v_mfma_f32_16x16x32_bf16 v[112:115], a[8:11], a[244:247], v[112:115]\n"
        "v_mul_f32 v97, v124, v97\n"
        "v_mul_f32 v98, v124, v98\n"
        "v_mfma_f32_16x16x32_bf16 v[112:115], a[4:7], a[240:243], v[112:115]\n"
        "v_mul_f32 v99, v124, v99\n"
        "v_cvt_pk_bf16_f32 v154, v116, v117\n"
        "v_mfma_f32_16x16x32_bf16 v[112:115], a[0:3], a[236:239], v[112:115]\n"
        "v_cvt_pk_bf16_f32 v155, v118, v119\n"
        "v_cvt_pk_bf16_f32 v96, v96, v97\n"
        "v_mfma_f32_16x16x32_bf16 v[116:119], a[24:27], a[236:239], 0\n"
        "v_cvt_pk_bf16_f32 v97, v98, v99\n"
        "v_cvt_pk_bf16_f32 v168, v120, v121\n"
        "v_mfma_f32_16x16x32_bf16 v[116:119], a[28:31], a[240:243], v[116:119]\n"
        "ds_write_b64 v246, v[96:97] offset:768\n"
        "v_mfma_f32_16x16x32_bf16 v[116:119], a[32:35], a[244:247], v[116:119]\n"
        "v_cvt_pk_bf16_f32 v169, v122, v123\n"
        "v_add_f32 v193, v192, v193\n"
        "v_mfma_f32_16x16x32_bf16 v[116:119], a[36:39], a[248:251], v[116:119]\n"
        "v_rcp_f32 v192, v193\n"
        "v_mfma_f32_16x16x32_bf16 v[116:119], a[40:43], a[216:219], v[116:119]\n"
        "buffer_load_dwordx4 v247, s[52:55], s34 offen offset:2048 lds\n"
        "v_mfma_f32_16x16x32_bf16 v[116:119], a[44:47], a[220:223], v[116:119]\n"
        "v_mul_f32 v196, v192, v196\n"
        "v_mul_f32 v197, v192, v197\n"
        "v_mfma_f32_16x16x32_bf16 v[120:123], a[68:71], a[220:223], 0\n"
        "v_mul_f32 v198, v192, v198\n"
        "v_mul_f32 v199, v192, v199\n"
        "v_mfma_f32_16x16x32_bf16 v[120:123], a[64:67], a[216:219], v[120:123]\n"
        "v_cvt_pk_bf16_f32 v156, v112, v113\n"
        "v_cvt_pk_bf16_f32 v157, v114, v115\n"
        "v_mfma_f32_16x16x32_bf16 v[120:123], a[60:63], a[248:251], v[120:123]\n"
        "ds_read_b128 a[224:227], v240 offset:1536\n"
        "v_cvt_pk_bf16_f32 v196, v196, v197\n"
        "v_mfma_f32_16x16x32_bf16 v[120:123], a[56:59], a[244:247], v[120:123]\n"
        "s_add_u32 m0, s65, 0\n"
        "ds_read_b128 a[228:231], v241 offset:1536\n"
        "v_cvt_pk_bf16_f32 v197, v198, v199\n"
        "v_mfma_f32_16x16x32_bf16 v[120:123], a[52:55], a[240:243], v[120:123]\n"
        "ds_read_b128 a[232:235], v240 offset:1664\n"
        "v_mul_f32 v200, v192, v200\n"
        "v_mfma_f32_16x16x32_bf16 v[120:123], a[48:51], a[236:239], v[120:123]\n"
        "v_mul_f32 v201, v192, v201\n"
        "v_mul_f32 v202, v192, v202\n"
        "v_mfma_f32_16x16x32_bf16 v[112:115], a[236:239], a[144:147], 0\n"
        "buffer_load_dwordx4 v232, s[52:55], 0 offen lds\n"
        "v_mfma_f32_16x16x32_bf16 v[112:115], a[240:243], a[148:151], v[112:115]\n"
        "v_mul_f32 v203, v192, v203\n"
        "v_cvt_pk_bf16_f32 v198, v200, v201\n"
        "v_mfma_f32_16x16x32_bf16 v[112:115], a[244:247], a[152:155], v[112:115]\n"
        "v_cvt_pk_bf16_f32 v199, v202, v203\n"
        "v_mul_f32 v204, v192, v204\n"
        "v_mfma_f32_16x16x32_bf16 v[112:115], a[248:251], a[156:159], v[112:115]\n"
        "ds_write_b128 v245, v[196:199] offset:512\n"
        "v_mfma_f32_16x16x32_bf16 v[112:115], a[216:219], a[160:163], v[112:115]\n"
        "v_mul_f32 v205, v192, v205\n"
        "v_mul_f32 v206, v192, v206\n"
        "v_mfma_f32_16x16x32_bf16 v[112:115], a[220:223], a[164:167], v[112:115]\n"
        "v_mul_f32 v207, v192, v207\n"
        "v_cvt_pk_bf16_f32 v158, v116, v117\n"
        "v_cvt_pk_bf16_f32 v159, v118, v119\n"
        "v_mfma_f32_16x16x32_bf16 v[116:119], a[220:223], a[188:191], 0\n"
        "v_cvt_pk_bf16_f32 v204, v204, v205\n"
        "v_cvt_pk_bf16_f32 v205, v206, v207\n"
        "v_mfma_f32_16x16x32_bf16 v[116:119], a[216:219], a[184:187], v[116:119]\n"
        "ds_write_b64 v246, v[204:205] offset:512\n"
        "v_mfma_f32_16x16x32_bf16 v[116:119], a[248:251], a[180:183], v[116:119]\n"
        "buffer_load_dwordx4 v233, s[52:55], 0 offen offset:1024 lds\n"
        "v_mfma_f32_16x16x32_bf16 v[116:119], a[244:247], a[176:179], v[116:119]\n"
        "s_waitcnt vmcnt(5)\n"
        "ds_read_b64_tr_b16 v[64:65], v255 offset:12288\n"
        "ds_read_b64_tr_b16 v[66:67], v255 offset:13056\n"
        "v_mfma_f32_16x16x32_bf16 v[116:119], a[240:243], a[172:175], v[116:119]\n"
        "v_max3_u32 v236, v236, v192, v193\n"
        "v_max3_u32 v236, v236, v124, v125\n"
        "v_mfma_f32_16x16x32_bf16 v[116:119], a[236:239], a[168:171], v[116:119]\n"
        "ds_read_b64_tr_b16 v[68:69], v255 offset:12320\n"
        "ds_read_b64_tr_b16 v[70:71], v255 offset:13088\n"
        "v_cvt_pk_bf16_f32 v160, v120, v121\n"
        "v_cvt_pk_bf16_f32 v161, v122, v123\n"
        "v_cvt_pk_bf16_f32 v166, v120, v121\n"
        "v_cvt_pk_bf16_f32 v167, v122, v123\n"
        "v_mfma_f32_16x16x32_bf16 v[120:123], a[236:239], a[192:195], 0\n"
        "ds_read_b128 a[236:239], v241 offset:1664\n"
        "v_cmp_lt_u32 vcc, s24, v236\n"
        "v_mfma_f32_16x16x32_bf16 v[120:123], a[240:243], a[196:199], v[120:123]\n"
        "ds_read_b128 a[240:243], v240 offset:1792\n"
        "s_cmp_lg_u64 vcc, 0\n"
        "s_addc_u32 s25, s25, s25\n"
        "v_mfma_f32_16x16x32_bf16 v[120:123], a[244:247], a[200:203], v[120:123]\n"
        "ds_read_b64_tr_b16 v[72:73], v255 offset:12352\n"
        "ds_read_b64_tr_b16 v[74:75], v255 offset:13120\n"
        "v_mfma_f32_16x16x32_bf16 v[120:123], a[248:251], a[204:207], v[120:123]\n"
        "ds_read_b128 a[244:247], v241 offset:1792\n"
        "v_cvt_pk_bf16_f32 v128, v112, v113\n"
        "v_mfma_f32_16x16x32_bf16 v[120:123], a[216:219], a[208:211], v[120:123]\n"
        "s_waitcnt lgkmcnt(7)\n"
        "ds_write_b128 v235, v[64:67] offset:12288\n"
        "v_mfma_f32_16x16x32_bf16 v[120:123], a[220:223], a[212:215], v[120:123]\n"
        "v_cvt_pk_bf16_f32 v129, v114, v115\n"
        "v_and_b32 v239, 32, v245\n"
        "v_mfma_f32_16x16x32_bf16 v[112:115], a[72:75], a[224:227], 0\n"
        "buffer_load_dwordx4 v234, s[52:55], 0 offen offset:2048 lds\n"
        "v_mfma_f32_16x16x32_bf16 v[112:115], a[76:79], a[228:231], v[112:115]\n"
        "s_waitcnt vmcnt(3)\n"
        "ds_read_b64_tr_b16 v[76:77], v255 offset:15360\n"
        "ds_read_b64_tr_b16 v[78:79], v255 offset:16128\n"
        "v_mfma_f32_16x16x32_bf16 v[112:115], a[80:83], a[232:235], v[112:115]\n"
        "s_waitcnt lgkmcnt(6)\n"
        "v_cvt_pk_bf16_f32 v136, v116, v117\n"
        "v_cvt_pk_bf16_f32 v137, v118, v119\n"
        "v_mfma_f32_16x16x32_bf16 v[112:115], a[84:87], a[236:239], v[112:115]\n"
        "s_add_u32 m0, s65, 3072\n"
        "v_xor_b32 v251, 0x2000, v251\n"
        "v_xor_b32 v252, 0x2000, v252\n"
        "v_mfma_f32_16x16x32_bf16 v[112:115], a[88:91], a[240:243], v[112:115]\n"
        "s_waitcnt lgkmcnt(3)\n"
        "v_mad_i32_i24 v239, v239, -12, v235\n"
        "v_cvt_pk_bf16_f32 v144, v120, v121\n"
        "v_mfma_f32_16x16x32_bf16 v[112:115], a[92:95], a[244:247], v[112:115]\n"
        "ds_write_b128 v239, v[68:71] offset:12480\n"
        "s_barrier\n"
        "v_mfma_f32_16x16x32_bf16 v[116:119], a[116:119], a[244:247], 0\n"
        "buffer_load_dwordx4 v232, s[52:55], s61 offen lds\n"
        "v_mfma_f32_16x16x32_bf16 v[116:119], a[112:115], a[240:243], v[116:119]\n"
        "ds_read_b64_tr_b16 v[88:89], v255 offset:15392\n"
        "ds_read_b64_tr_b16 v[90:91], v255 offset:16160\n"
        "v_mfma_f32_16x16x32_bf16 v[116:119], a[108:111], a[236:239], v[116:119]\n"
        "v_cvt_pk_bf16_f32 v145, v122, v123\n"
        "v_cvt_pk_bf16_f32 v172, v112, v113\n"
        "v_mfma_f32_16x16x32_bf16 v[116:119], a[104:107], a[232:235], v[116:119]\n"
        "v_cvt_pk_bf16_f32 v173, v114, v115\n"
        "v_add_u32 v247, s45, v245\n"
        "v_mfma_f32_16x16x32_bf16 v[116:119], a[100:103], a[228:231], v[116:119]\n"
        "v_mfma_f32_16x16x32_bf16 v[116:119], a[96:99], a[224:227], v[116:119]\n"
        "ds_write_b128 v254, v[72:75] offset:12288\n"
        "v_mfma_f32_16x16x32_bf16 v[120:123], a[120:123], a[224:227], 0\n"
        "s_nop 2\n"
        "v_mfma_f32_16x16x32_bf16 v[120:123], a[124:127], a[228:231], v[120:123]\n"
        "ds_read_b128 v[162:165], v251 offset:0\n"
        "s_nop 0\n"
        "v_mfma_f32_16x16x32_bf16 v[120:123], a[128:131], a[232:235], v[120:123]\n"
        "buffer_load_dwordx4 v233, s[52:55], s61 offen offset:1024 lds\n"
        "v_mfma_f32_16x16x32_bf16 v[120:123], a[132:135], a[236:239], v[120:123]\n"
        "ds_read_b64_tr_b16 v[92:93], v255 offset:15424\n"
        "ds_read_b64_tr_b16 v[94:95], v255 offset:16192\n"
        "v_mfma_f32_16x16x32_bf16 v[120:123], a[136:139], a[240:243], v[120:123]\n"
        "v_cvt_pk_bf16_f32 v174, v116, v117\n"
        "v_cvt_pk_bf16_f32 v175, v118, v119\n"
        "v_mfma_f32_16x16x32_bf16 v[120:123], a[140:143], a[244:247], v[120:123]\n"
        "s_nop 2\n"
        "v_mfma_f32_16x16x32_bf16 v[112:115], a[20:23], a[244:247], 0\n"
        "s_nop 2\n"
        "v_mfma_f32_16x16x32_bf16 v[112:115], a[16:19], a[240:243], v[112:115]\n"
        "v_cvt_pk_bf16_f32 v190, v120, v121\n"
        "v_cvt_pk_bf16_f32 v191, v122, v123\n"
        "v_mfma_f32_16x16x32_bf16 v[112:115], a[12:15], a[236:239], v[112:115]\n"
        "v_mfma_f32_16x16x32_bf16 v[112:115], a[8:11], a[232:235], v[112:115]\n"
        "s_waitcnt vmcnt(2)\n"
        "ds_read_b64_tr_b16 v[212:213], v255 offset:0\n"
        "ds_read_b64_tr_b16 v[214:215], v255 offset:768\n"
        "v_mfma_f32_16x16x32_bf16 v[112:115], a[4:7], a[228:231], v[112:115]\n"
        "buffer_load_dwordx4 v234, s[52:55], s61 offen offset:2048 lds\n"
        "v_mfma_f32_16x16x32_bf16 v[112:115], a[0:3], a[224:227], v[112:115]\n"
        "ds_read_b128 v[182:185], v251 offset:512\n"
        "s_nop 0\n"
        "v_mfma_f32_16x16x32_bf16 v[116:119], a[24:27], a[224:227], 0\n"
        "s_nop 2\n"
        "v_mfma_f32_16x16x32_bf16 v[116:119], a[28:31], a[228:231], v[116:119]\n"
        "s_waitcnt lgkmcnt(3)\n"
        "ds_write_b128 v235, v[76:79] offset:15360\n"
        "v_mfma_f32_16x16x32_bf16 v[116:119], a[32:35], a[232:235], v[116:119]\n"
        "v_cvt_pk_bf16_f32 v176, v112, v113\n"
        "v_cvt_pk_bf16_f32 v177, v114, v115\n"
        "v_mfma_f32_16x16x32_bf16 v[116:119], a[36:39], a[236:239], v[116:119]\n"
        "s_add_u32 m0, s65, 6144\n"
        "v_mfma_f32_16x16x32_bf16 v[116:119], a[40:43], a[240:243], v[116:119]\n"
        "ds_read_b64_tr_b16 v[216:217], v255 offset:32\n"
        "ds_read_b64_tr_b16 v[218:219], v255 offset:800\n"
        "v_mfma_f32_16x16x32_bf16 v[116:119], a[44:47], a[244:247], v[116:119]\n"
        "s_nop 2\n"
        "v_mfma_f32_16x16x32_bf16 v[120:123], a[68:71], a[244:247], 0\n"
        ".long 0xbec200ff\n"
        "43:\n"
        "s_branch 60b\n"
        "buffer_load_dwordx4 v232, s[52:55], s56 offen lds\n"
        "v_mfma_f32_16x16x32_bf16 v[120:123], a[64:67], a[240:243], v[120:123]\n"
        "s_nop 1\n"
        "v_cvt_pk_bf16_f32 v178, v116, v117\n"
        "v_mfma_f32_16x16x32_bf16 v[120:123], a[60:63], a[236:239], v[120:123]\n"
        "ds_write_b128 v239, v[88:91] offset:15552\n"
        "v_mfma_f32_16x16x32_bf16 v[120:123], a[56:59], a[232:235], v[120:123]\n"
        "v_cvt_pk_bf16_f32 v179, v118, v119\n"
        "v_mfma_f32_16x16x32_bf16 v[120:123], a[52:55], a[228:231], v[120:123]\n"
        "v_mfma_f32_16x16x32_bf16 v[120:123], a[48:51], a[224:227], v[120:123]\n"
        "ds_read_b64_tr_b16 v[220:221], v255 offset:64\n"
        "ds_read_b64_tr_b16 v[222:223], v255 offset:832\n"
        "v_mfma_f32_16x16x32_bf16 v[112:115], a[224:227], a[144:147], 0\n"
        "s_nop 2\n"
        "v_mfma_f32_16x16x32_bf16 v[112:115], a[228:231], a[148:151], v[112:115]\n"
        "s_nop 0\n"
        "v_cvt_pk_bf16_f32 v180, v120, v121\n"
        "v_cvt_pk_bf16_f32 v181, v122, v123\n"
        "v_mfma_f32_16x16x32_bf16 v[112:115], a[232:235], a[152:155], v[112:115]\n"
        "buffer_load_dwordx4 v233, s[52:55], s56 offen offset:1024 lds\n"
        "v_mfma_f32_16x16x32_bf16 v[112:115], a[236:239], a[156:159], v[112:115]\n"
        "ds_write_b128 v254, v[92:95] offset:15360\n"
        "v_mfma_f32_16x16x32_bf16 v[112:115], a[240:243], a[160:163], v[112:115]\n"
        "v_cvt_pk_bf16_f32 v186, v120, v121\n"
        "v_cvt_pk_bf16_f32 v187, v122, v123\n"
        "v_mfma_f32_16x16x32_bf16 v[112:115], a[244:247], a[164:167], v[112:115]\n"
        "ds_read_b128 a[248:251], v240 offset:12288\n"
        "s_nop 0\n"
        "v_mfma_f32_16x16x32_bf16 v[96:99], v[152:155], v[156:159], v[0:3]\n"
        "s_waitcnt vmcnt(2)\n"
        "ds_read_b64_tr_b16 v[224:225], v255 offset:3072\n"
        "ds_read_b64_tr_b16 v[226:227], v255 offset:3840\n"
        "v_mfma_f32_16x16x32_bf16 v[96:99], v[168:171], v[160:163], v[96:99]\n"
        "ds_read_b128 a[216:219], v241 offset:12288\n"
        "s_sub_u32 s31, s30, s28\n"
        "v_cvt_pk_bf16_f32 v130, v112, v113\n"
        "v_mfma_f32_16x16x32_bf16 v[100:103], v[172:175], v[156:159], v[4:7]\n"
        "ds_read_b128 a[220:223], v240 offset:12416\n"
        "s_sub_u32 s45, s28, s29\n"
        "v_cvt_pk_bf16_f32 v131, v114, v115\n"
        "v_mfma_f32_16x16x32_bf16 v[100:103], v[188:191], v[164:167], v[100:103]\n"
        "s_add_u32 s28, s28, s31\n"
        "ds_read_b128 v[162:165], v252 offset:0\n"
        "v_mfma_f32_16x16x32_bf16 v[116:119], a[244:247], a[188:191], 0\n"
        "buffer_load_dwordx4 v234, s[52:55], s56 offen offset:2048 lds\n"
        "v_mfma_f32_16x16x32_bf16 v[116:119], a[240:243], a[184:187], v[116:119]\n"
        "s_waitcnt lgkmcnt(7)\n"
        "ds_write_b128 v235, v[212:215] offset:0\n"
        "v_mfma_f32_16x16x32_bf16 v[116:119], a[236:239], a[180:183], v[116:119]\n"
        "v_exp_f32 v96, v96\n"
        "v_mfma_f32_16x16x32_bf16 v[116:119], a[232:235], a[176:179], v[116:119]\n"
        "ds_read_b64_tr_b16 v[88:89], v255 offset:3104\n"
        "ds_read_b64_tr_b16 v[90:91], v255 offset:3872\n"
        "v_mfma_f32_16x16x32_bf16 v[116:119], a[228:231], a[172:175], v[116:119]\n"
        "v_exp_f32 v97, v97\n"
        "v_mfma_f32_16x16x32_bf16 v[116:119], a[224:227], a[168:171], v[116:119]\n"
        "s_add_u32 m0, s65, 9216\n"
        "v_exp_f32 v98, v98\n"
        "v_mfma_f32_16x16x32_bf16 v[104:107], v[152:155], v[176:179], v[16:19]\n"
        "v_exp_f32 v100, v100\n"
        "v_mfma_f32_16x16x32_bf16 v[104:107], v[168:171], v[180:183], v[104:107]\n"
        "ds_write_b128 v239, v[216:219] offset:192\n"
        "v_mfma_f32_16x16x32_bf16 v[108:111], v[172:175], v[176:179], v[20:23]\n"
        "buffer_load_dwordx4 v232, s[52:55], s62 offen lds\n"
        "v_mfma_f32_16x16x32_bf16 v[108:111], v[188:191], v[184:187], v[108:111]\n"
        "ds_read_b128 v[182:185], v252 offset:512\n"
        "v_cvt_pk_bf16_f32 v64, v96, v97\n"
        "v_mfma_f32_16x16x32_bf16 v[120:123], a[224:227], a[192:195], 0\n"
        "ds_read_b128 a[224:227], v241 offset:12416\n"
        "ds_read_b64_tr_b16 v[92:93], v255 offset:3136\n"
        "v_mfma_f32_16x16x32_bf16 v[120:123], a[228:231], a[196:199], v[120:123]\n"
        "ds_read_b64_tr_b16 v[94:95], v255 offset:3904\n"
        "ds_read_b128 a[228:231], v240 offset:12544\n"
        "v_mfma_f32_16x16x32_bf16 v[120:123], a[232:235], a[200:203], v[120:123]\n"
        "ds_read_b128 a[232:235], v241 offset:12544\n"
        "v_cvt_pk_bf16_f32 v138, v116, v117\n"
        "v_mfma_f32_16x16x32_bf16 v[120:123], a[236:239], a[204:207], v[120:123]\n"
        "ds_read_b128 v[202:205], v251 offset:4096\n"
        "v_cvt_pk_bf16_f32 v139, v118, v119\n"
        "v_mfma_f32_16x16x32_bf16 v[120:123], a[240:243], a[208:211], v[120:123]\n"
        "ds_write_b128 v254, v[220:223] offset:0\n"
        "v_mfma_f32_16x16x32_bf16 v[120:123], a[244:247], a[212:215], v[120:123]\n"
        "s_waitcnt lgkmcnt(9)\n"
        "v_exp_f32 v99, v99\n"
        "v_mfma_f32_16x16x32_bf16 v[112:115], a[0:3], a[248:251], 0\n"
        "buffer_load_dwordx4 v233, s[52:55], s62 offen offset:1024 lds\n"
        "v_mfma_f32_16x16x32_bf16 v[112:115], a[4:7], a[216:219], v[112:115]\n"
        "s_waitcnt vmcnt(2)\n"
        "ds_read_b64_tr_b16 v[212:213], v255 offset:6144\n"
        "ds_read_b64_tr_b16 v[214:215], v255 offset:6912\n"
        "v_mfma_f32_16x16x32_bf16 v[112:115], a[8:11], a[220:223], v[112:115]\n"
        "s_waitcnt lgkmcnt(5)\n"
        "v_exp_f32 v101, v101\n"
        "v_mfma_f32_16x16x32_bf16 v[112:115], a[12:15], a[224:227], v[112:115]\n"
        "v_cvt_pk_bf16_f32 v65, v98, v99\n"
        "v_cvt_pk_bf16_f32 v66, v100, v101\n"
        "v_mfma_f32_16x16x32_bf16 v[112:115], a[16:19], a[228:231], v[112:115]\n"
        "s_waitcnt lgkmcnt(3)\n"
        "v_exp_f32 v102, v102\n"
        "v_mfma_f32_16x16x32_bf16 v[112:115], a[20:23], a[232:235], v[112:115]\n"
        "ds_write_b128 v235, v[224:227] offset:3072\n"
        "v_mfma_f32_16x16x32_bf16 v[116:119], a[44:47], a[232:235], 0\n"
        "v_exp_f32 v103, v103\n"
        "v_mfma_f32_16x16x32_bf16 v[116:119], a[40:43], a[228:231], v[116:119]\n"
        "v_cvt_pk_bf16_f32 v67, v102, v103\n"
        "v_cvt_pk_bf16_f32 v146, v120, v121\n"
        "v_mfma_f32_16x16x32_bf16 v[116:119], a[36:39], a[224:227], v[116:119]\n"
        "buffer_load_dwordx4 v234, s[52:55], s62 offen offset:2048 lds\n"
        "s_add_u32 s41, s30, s40\n"
        "s_add_u32 s35, s47, 786432\n"
        "s_sub_u32 s46, s35, 100663296\n"
        "s_cmp_lt_u32 s46, 786432\n"
        "s_mov_b32 s30, s29\n"
        "v_mfma_f32_16x16x32_bf16 v[116:119], a[32:35], a[220:223], v[116:119]\n"
        "s_cselect_b32 s35, s46, s35\n"
        "s_add_u32 s29, s29, s45\n"
        "ds_read_b64_tr_b16 v[216:217], v255 offset:6176\n"
        "v_mfma_f32_16x16x32_bf16 v[116:119], a[28:31], a[216:219], v[116:119]\n"
        "ds_read_b64_tr_b16 v[218:219], v255 offset:6944\n"
        "v_cvt_pk_bf16_f32 v147, v122, v123\n"
        "v_mfma_f32_16x16x32_bf16 v[116:119], a[24:27], a[248:251], v[116:119]\n"
        "v_exp_f32 v104, v104\n"
        "v_mfma_f32_16x16x32_bf16 v[120:123], a[48:51], a[248:251], 0\n"
        "ds_write_b128 v239, v[88:91] offset:3264\n"
        "v_mfma_f32_16x16x32_bf16 v[120:123], a[52:55], a[216:219], v[120:123]\n"
        "v_exp_f32 v105, v105\n"
        "v_mfma_f32_16x16x32_bf16 v[120:123], a[56:59], a[220:223], v[120:123]\n"
        "s_mov_b32 m0, s48\n"
        "v_exp_f32 v106, v106\n"
        "v_mfma_f32_16x16x32_bf16 v[120:123], a[60:63], a[224:227], v[120:123]\n"
        "ds_read_b64_tr_b16 v[220:221], v255 offset:6208\n"
        "ds_read_b64_tr_b16 v[222:223], v255 offset:6976\n"
        "v_mfma_f32_16x16x32_bf16 v[120:123], a[64:67], a[228:231], v[120:123]\n"
        "buffer_load_dwordx4 v249, s[12:15], s36 offen offset:0 lds\n"
        "v_mfma_f32_16x16x32_bf16 v[120:123], a[68:71], a[232:235], v[120:123]\n"
        "v_cvt_pk_bf16_f32 v72, v104, v105\n"
        "v_cvt_pk_bf16_f32 v196, v112, v113\n"
        "v_cvt_pk_bf16_f32 v197, v114, v115\n"
        "v_mfma_f32_16x16x32_bf16 v[112:115], a[92:95], a[232:235], 0\n"
        "v_exp_f32 v108, v108\n"
        "v_mfma_f32_16x16x32_bf16 v[112:115], a[88:91], a[228:231], v[112:115]\n"
        "ds_write_b128 v254, v[92:95] offset:3072\n"
        "v_mfma_f32_16x16x32_bf16 v[112:115], a[84:87], a[224:227], v[112:115]\n"
        "v_exp_f32 v107, v107\n"
        "v_mfma_f32_16x16x32_bf16 v[112:115], a[80:83], a[220:223], v[112:115]\n"
        "v_exp_f32 v109, v109\n"
        "v_mfma_f32_16x16x32_bf16 v[112:115], a[76:79], a[216:219], v[112:115]\n"
        "s_waitcnt vmcnt(1)\n"
        "ds_read_b64_tr_b16 v[224:225], v255 offset:9216\n"
        "ds_read_b64_tr_b16 v[226:227], v255 offset:9984\n"
        "v_mfma_f32_16x16x32_bf16 v[112:115], a[72:75], a[248:251], v[112:115]\n"
        "buffer_load_dwordx4 v253, s[12:15], s36 offen offset:1024 lds\n"
        "v_cvt_pk_bf16_f32 v198, v116, v117\n"
        "v_cvt_pk_bf16_f32 v199, v118, v119\n"
        "s_nop 1\n"
        "v_mfma_f32_16x16x32_bf16 v[96:99], v[152:155], v[196:199], v[32:35]\n"
        "v_cvt_pk_bf16_f32 v73, v106, v107\n"
        "v_cvt_pk_bf16_f32 v74, v108, v109\n"
        "v_cvt_pk_bf16_f32 v200, v120, v121\n"
        "v_cvt_pk_bf16_f32 v201, v122, v123\n"
        "s_nop 1\n"
        "v_mfma_f32_16x16x32_bf16 v[96:99], v[168:171], v[200:203], v[96:99]\n"
        "v_exp_f32 v110, v110\n"
        "v_mfma_f32_16x16x32_bf16 v[100:103], v[172:175], v[196:199], v[36:39]\n"
        "s_waitcnt lgkmcnt(3)\n"
        "ds_write_b128 v235, v[212:215] offset:6144\n"
        "v_cvt_pk_bf16_f32 v206, v120, v121\n"
        "v_cvt_pk_bf16_f32 v207, v122, v123\n"
        "s_nop 1\n"
        "v_mfma_f32_16x16x32_bf16 v[100:103], v[188:191], v[204:207], v[100:103]\n"
        "s_cmp_eq_u32 s37, 100663296\n"
        "v_exp_f32 v111, v111\n"
        "v_mfma_f32_16x16x32_bf16 v[116:119], a[96:99], a[248:251], 0\n"
        "s_cselect_b32 s46, 0, s37\n"
        "ds_read_b128 v[202:205], v252 offset:4096\n"
        "v_cvt_pk_bf16_f32 v75, v110, v111\n"
        "v_mfma_f32_16x16x32_bf16 v[116:119], a[100:103], a[216:219], v[116:119]\n"
        "ds_read_b64_tr_b16 v[88:89], v255 offset:9248\n"
        "ds_read_b64_tr_b16 v[90:91], v255 offset:10016\n"
        "s_add_u32 m0, s41, 0\n"
        "v_mfma_f32_16x16x32_bf16 v[116:119], a[104:107], a[220:223], v[116:119]\n"
        "buffer_load_dwordx4 v242, s[8:11], s47 offen lds\n"
        "v_mfma_f32_16x16x32_bf16 v[116:119], a[108:111], a[224:227], v[116:119]\n"
        "s_add_u32 s38, s46, s43\n"
        "v_cvt_pk_bf16_f32 v192, v112, v113\n"
        "v_cvt_pk_bf16_f32 v193, v114, v115\n"
        "v_mfma_f32_16x16x32_bf16 v[116:119], a[112:115], a[228:231], v[116:119]\n"
        "s_add_u32 s39, s46, s44\n"
        "v_exp_f32 v96, v96\n"
        "v_mfma_f32_16x16x32_bf16 v[116:119], a[116:119], a[232:235], v[116:119]\n"
        "ds_write_b128 v239, v[216:219] offset:6336\n"
        "s_add_u32 s37, s37, 6291456\n"
        "v_mfma_f32_16x16x32_bf16 v[120:123], a[140:143], a[232:235], 0\n"
        "v_exp_f32 v97, v97\n"
        "v_mfma_f32_16x16x32_bf16 v[120:123], a[136:139], a[228:231], v[120:123]\n"
        "s_add_u32 m0, s41, 1024\n"
        "v_exp_f32 v98, v98\n"
        "v_mfma_f32_16x16x32_bf16 v[120:123], a[132:135], a[224:227], v[120:123]\n"
        "ds_read_b64_tr_b16 v[92:93], v255 offset:9280\n"
        "ds_read_b64_tr_b16 v[94:95], v255 offset:10048\n"
        "v_mfma_f32_16x16x32_bf16 v[120:123], a[128:131], a[220:223], v[120:123]\n"
        "buffer_load_dwordx4 v243, s[8:11], s47 offen lds\n"
        "v_mfma_f32_16x16x32_bf16 v[120:123], a[124:127], a[216:219], v[120:123]\n"
        "v_cvt_pk_bf16_f32 v80, v96, v97\n"
        "v_cvt_pk_bf16_f32 v194, v116, v117\n"
        "v_mfma_f32_16x16x32_bf16 v[120:123], a[120:123], a[248:251], v[120:123]\n"
        "v_exp_f32 v100, v100\n"
        "v_mfma_f32_16x16x32_bf16 v[112:115], a[248:251], a[144:147], 0\n"
        "ds_write_b128 v254, v[220:223] offset:6144\n"
        "v_mfma_f32_16x16x32_bf16 v[112:115], a[216:219], a[148:151], v[112:115]\n"
        "v_cvt_pk_bf16_f32 v195, v118, v119\n"
        "v_add_u32 v245, s45, v245\n"
        "v_mfma_f32_16x16x32_bf16 v[112:115], a[220:223], a[152:155], v[112:115]\n"
        "s_add_u32 m0, s41, 2048\n"
        "ds_read_b128 a[236:239], v240 offset:13824\n"
        "v_cvt_pk_bf16_f32 v208, v120, v121\n"
        "v_mfma_f32_16x16x32_bf16 v[112:115], a[224:227], a[156:159], v[112:115]\n"
        "ds_read_b128 a[240:243], v241 offset:13824\n"
        "v_cvt_pk_bf16_f32 v209, v122, v123\n"
        "v_mfma_f32_16x16x32_bf16 v[112:115], a[228:231], a[160:163], v[112:115]\n"
        "buffer_load_dwordx4 v244, s[8:11], s47 offen lds\n"
        "v_mfma_f32_16x16x32_bf16 v[112:115], a[232:235], a[164:167], v[112:115]\n"
        "ds_read_b128 a[244:247], v240 offset:13952\n"
        "v_add_u32 v246, s45, v246\n"
        "v_mfma_f32_16x16x32_bf16 v[104:107], v[192:195], v[156:159], v[8:11]\n"
        "v_exp_f32 v99, v99\n"
        "v_mfma_f32_16x16x32_bf16 v[104:107], v[208:211], v[160:163], v[104:107]\n"
        "s_waitcnt lgkmcnt(4)\n"
        "ds_write_b128 v235, v[224:227] offset:9216\n"
        "v_mfma_f32_16x16x32_bf16 v[116:119], a[232:235], a[188:191], 0\n"
        "v_exp_f32 v101, v101\n"
        "v_mfma_f32_16x16x32_bf16 v[116:119], a[228:231], a[184:187], v[116:119]\n"
        "s_add_u32 m0, s41, 12288\n"
        "v_cvt_pk_bf16_f32 v81, v98, v99\n"
        "v_cvt_pk_bf16_f32 v82, v100, v101\n"
        "v_mfma_f32_16x16x32_bf16 v[116:119], a[224:227], a[180:183], v[116:119]\n"
        "v_exp_f32 v102, v102\n"
        "v_mfma_f32_16x16x32_bf16 v[116:119], a[220:223], a[176:179], v[116:119]\n"
        "buffer_load_dwordx4 v242, s[8:11], s35 offen lds\n"
        "v_mfma_f32_16x16x32_bf16 v[116:119], a[216:219], a[172:175], v[116:119]\n"
        "v_exp_f32 v103, v103\n"
        "v_mfma_f32_16x16x32_bf16 v[116:119], a[248:251], a[168:171], v[116:119]\n"
        "v_cvt_pk_bf16_f32 v83, v102, v103\n"
        "v_cvt_pk_bf16_f32 v132, v112, v113\n"
        "v_mfma_f32_16x16x32_bf16 v[108:111], v[192:195], v[176:179], v[24:27]\n"
        "ds_write_b128 v239, v[88:91] offset:9408\n"
        "v_mfma_f32_16x16x32_bf16 v[108:111], v[208:211], v[180:183], v[108:111]\n"
        "v_cvt_pk_bf16_f32 v133, v114, v115\n"
        "s_nop 0\n"
        "v_mfma_f32_16x16x32_bf16 v[120:123], a[248:251], a[192:195], 0\n"
        "ds_read_b128 a[248:251], v241 offset:13952\n"
        "s_add_u32 m0, s41, 13312\n"
        "v_cvt_pk_bf16_f32 v140, v116, v117\n"
        "v_mfma_f32_16x16x32_bf16 v[120:123], a[216:219], a[196:199], v[120:123]\n"
        "ds_read_b128 a[216:219], v240 offset:14080\n"
        "v_cvt_pk_bf16_f32 v141, v118, v119\n"
        "v_mfma_f32_16x16x32_bf16 v[120:123], a[220:223], a[200:203], v[120:123]\n"
        "buffer_load_dwordx4 v243, s[8:11], s35 offen lds\n"
        "v_mfma_f32_16x16x32_bf16 v[120:123], a[224:227], a[204:207], v[120:123]\n"
        "ds_read_b128 a[220:223], v241 offset:14080\n"
        "v_mfma_f32_16x16x32_bf16 v[120:123], a[228:231], a[208:211], v[120:123]\n"
        "v_exp_f32 v104, v104\n"
        "v_mfma_f32_16x16x32_bf16 v[120:123], a[232:235], a[212:215], v[120:123]\n"
        "ds_write_b128 v254, v[92:95] offset:9216\n"
        "v_mfma_f32_16x16x32_bf16 v[96:99], v[192:195], v[196:199], v[40:43]\n"
        "v_exp_f32 v105, v105\n"
        "v_mfma_f32_16x16x32_bf16 v[96:99], v[208:211], v[200:203], v[96:99]\n"
        "s_waitcnt lgkmcnt(5)\n"
        "s_add_u32 m0, s41, 14336\n"
        "v_exp_f32 v106, v106\n"
        "v_mfma_f32_16x16x32_bf16 v[112:115], a[72:75], a[236:239], 0\n"
        "v_cvt_pk_bf16_f32 v68, v104, v105\n"
        "v_cvt_pk_bf16_f32 v148, v120, v121\n"
        "v_mfma_f32_16x16x32_bf16 v[112:115], a[76:79], a[240:243], v[112:115]\n"
        "buffer_load_dwordx4 v244, s[8:11], s35 offen lds\n"
        "v_mfma_f32_16x16x32_bf16 v[112:115], a[80:83], a[244:247], v[112:115]\n"
        "s_waitcnt lgkmcnt(1)\n"
        "v_exp_f32 v107, v107\n"
        "v_mfma_f32_16x16x32_bf16 v[112:115], a[84:87], a[248:251], v[112:115]\n"
        "v_cvt_pk_bf16_f32 v69, v106, v107\n"
        "v_cvt_pk_bf16_f32 v149, v122, v123\n"
        "v_mfma_f32_16x16x32_bf16 v[112:115], a[88:91], a[216:219], v[112:115]\n"
        "v_exp_f32 v108, v108\n"
        "v_mfma_f32_16x16x32_bf16 v[112:115], a[92:95], a[220:223], v[112:115]\n"
        "v_exp_f32 v109, v109\n"
        "v_mfma_f32_16x16x32_bf16 v[116:119], a[116:119], a[220:223], 0\n"
        "v_exp_f32 v110, v110\n"
        "v_mfma_f32_16x16x32_bf16 v[116:119], a[112:115], a[216:219], v[116:119]\n"
        "v_cvt_pk_bf16_f32 v76, v108, v109\n"
        "v_add_u32 v240, s31, v240\n"
        "v_mfma_f32_16x16x32_bf16 v[116:119], a[108:111], a[248:251], v[116:119]\n"
        "v_exp_f32 v111, v111\n"
        "v_mfma_f32_16x16x32_bf16 v[116:119], a[104:107], a[244:247], v[116:119]\n"
        "v_cvt_pk_bf16_f32 v212, v112, v113\n"
        "v_cvt_pk_bf16_f32 v213, v114, v115\n"
        "v_mfma_f32_16x16x32_bf16 v[116:119], a[100:103], a[240:243], v[116:119]\n"
        "v_cvt_pk_bf16_f32 v77, v110, v111\n"
        "v_add_u32 v241, s31, v241\n"
        "v_mfma_f32_16x16x32_bf16 v[116:119], a[96:99], a[236:239], v[116:119]\n"
        "v_exp_f32 v96, v96\n"
        "v_mfma_f32_16x16x32_bf16 v[120:123], a[120:123], a[236:239], 0\n"
        "v_exp_f32 v97, v97\n"
        "v_mfma_f32_16x16x32_bf16 v[120:123], a[124:127], a[240:243], v[120:123]\n"
        "s_add_u32 s47, s47, 6291456\n"
        "v_exp_f32 v98, v98\n"
        "v_mfma_f32_16x16x32_bf16 v[120:123], a[128:131], a[244:247], v[120:123]\n"
        "s_add_u32 s36, s36, 2097152\n"
        "v_cvt_pk_bf16_f32 v214, v116, v117\n"
        "v_cvt_pk_bf16_f32 v215, v118, v119\n"
        "v_mfma_f32_16x16x32_bf16 v[120:123], a[132:135], a[248:251], v[120:123]\n"
        "s_xor_b32 s48, s48, 8192\n"
        "v_cvt_pk_bf16_f32 v84, v96, v97\n"
        "v_and_b32 v254, 32, v250\n"
        "v_mfma_f32_16x16x32_bf16 v[120:123], a[136:139], a[216:219], v[120:123]\n"
        "v_exp_f32 v99, v99\n"
        "v_mfma_f32_16x16x32_bf16 v[120:123], a[140:143], a[220:223], v[120:123]\n"
        "v_cvt_pk_bf16_f32 v85, v98, v99\n"
        "v_mad_i32_i24 v254, v254, -12, v248\n"
        "v_mfma_f32_16x16x32_bf16 v[112:115], a[220:223], a[164:167], 0\n"
        "v_add_u32 v254, 1728, v254\n"
        "s_nop 0\n"
        "v_mfma_f32_16x16x32_bf16 v[112:115], a[216:219], a[160:163], v[112:115]\n"
        "s_nop 1\n"
        "v_cvt_pk_bf16_f32 v230, v120, v121\n"
        "v_mfma_f32_16x16x32_bf16 v[112:115], a[248:251], a[156:159], v[112:115]\n"
        "s_waitcnt lgkmcnt(0)\n"
        "s_barrier\n"
        "v_cvt_pk_bf16_f32 v231, v122, v123\n"
        "v_mfma_f32_16x16x32_bf16 v[112:115], a[244:247], a[152:155], v[112:115]\n"
        "v_mfma_f32_16x16x32_bf16 v[112:115], a[240:243], a[148:151], v[112:115]\n"
        "v_mfma_f32_16x16x32_bf16 v[112:115], a[236:239], a[144:147], v[112:115]\n"
        "s_nop 2\n"
        "v_mfma_f32_16x16x32_bf16 v[100:103], v[212:215], v[156:159], v[12:15]\n"
        "s_nop 2\n"
        "v_mfma_f32_16x16x32_bf16 v[100:103], v[228:231], v[164:167], v[100:103]\n"
        "v_cvt_pk_bf16_f32 v134, v112, v113\n"
        "v_cvt_pk_bf16_f32 v135, v114, v115\n"
        "v_mfma_f32_16x16x32_bf16 v[116:119], a[236:239], a[168:171], 0\n"
        "v_mfma_f32_16x16x32_bf16 v[116:119], a[240:243], a[172:175], v[116:119]\n"
        "v_mfma_f32_16x16x32_bf16 v[116:119], a[244:247], a[176:179], v[116:119]\n"
        "v_mfma_f32_16x16x32_bf16 v[116:119], a[248:251], a[180:183], v[116:119]\n"
        "v_mfma_f32_16x16x32_bf16 v[116:119], a[216:219], a[184:187], v[116:119]\n"
        "s_nop 0\n"
        "v_exp_f32 v100, v100\n"
        "v_mfma_f32_16x16x32_bf16 v[116:119], a[220:223], a[188:191], v[116:119]\n"
        "v_exp_f32 v101, v101\n"
        "v_mfma_f32_16x16x32_bf16 v[104:107], v[212:215], v[176:179], v[28:31]\n"
        "v_exp_f32 v102, v102\n"
        "v_mfma_f32_16x16x32_bf16 v[104:107], v[228:231], v[184:187], v[104:107]\n"
        "v_exp_f32 v103, v103\n"
        "v_mfma_f32_16x16x32_bf16 v[120:123], a[220:223], a[212:215], 0\n"
        "v_cvt_pk_bf16_f32 v70, v100, v101\n"
        "v_cvt_pk_bf16_f32 v71, v102, v103\n"
        "v_mfma_f32_16x16x32_bf16 v[120:123], a[216:219], a[208:211], v[120:123]\n"
        "v_cvt_pk_bf16_f32 v142, v116, v117\n"
        "v_cvt_pk_bf16_f32 v143, v118, v119\n"
        "v_mfma_f32_16x16x32_bf16 v[120:123], a[248:251], a[204:207], v[120:123]\n"
        "v_mov_b64 v[96:97], 0\n"
        "v_mfma_f32_16x16x32_bf16 v[120:123], a[244:247], a[200:203], v[120:123]\n"
        "v_exp_f32 v104, v104\n"
        "v_mfma_f32_16x16x32_bf16 v[120:123], a[240:243], a[196:199], v[120:123]\n"
        "v_exp_f32 v105, v105\n"
        "v_mfma_f32_16x16x32_bf16 v[120:123], a[236:239], a[192:195], v[120:123]\n"
        "v_exp_f32 v106, v106\n"
        "v_mfma_f32_16x16x32_bf16 v[108:111], v[212:215], v[196:199], v[44:47]\n"
        "v_exp_f32 v107, v107\n"
        "v_mfma_f32_16x16x32_bf16 v[108:111], v[228:231], v[204:207], v[108:111]\n"
        "v_cvt_pk_bf16_f32 v78, v104, v105\n"
        "v_cvt_pk_bf16_f32 v79, v106, v107\n"
        "v_mfma_f32_16x16x32_bf16 v[112:115], a[0:3], a[236:239], 0\n"
        "s_nop 0\n"
        "v_cvt_pk_bf16_f32 v150, v120, v121\n"
        "v_cvt_pk_bf16_f32 v151, v122, v123\n"
        "v_mfma_f32_16x16x32_bf16 v[112:115], a[4:7], a[240:243], v[112:115]\n"
        "v_mfma_f32_16x16x32_bf16 v[112:115], a[8:11], a[244:247], v[112:115]\n"
        "v_exp_f32 v108, v108\n"
        "v_mfma_f32_16x16x32_bf16 v[112:115], a[12:15], a[248:251], v[112:115]\n"
        "v_exp_f32 v109, v109\n"
        "v_mfma_f32_16x16x32_bf16 v[112:115], a[16:19], a[216:219], v[112:115]\n"
        "v_exp_f32 v110, v110\n"
        "v_mfma_f32_16x16x32_bf16 v[112:115], a[20:23], a[220:223], v[112:115]\n"
        "v_exp_f32 v111, v111\n"
        "v_smfmac_f32_16x16x64_bf16 v[96:99], a[252:255], v[64:71], v237\n"
        "v_cvt_pk_bf16_f32 v86, v108, v109\n"
        "v_mfma_f32_16x16x32_bf16 v[100:103], v[132:135], v[68:71], 0\n"
        "v_mov_b64 v[124:125], 0\n"
        "v_cvt_pk_bf16_f32 v87, v110, v111\n"
        "v_mfma_f32_16x16x32_bf16 v[100:103], v[128:131], v[64:67], v[100:103]\n"
        "s_nop 0\n"
        "v_cvt_pk_bf16_f32 v216, v112, v113\n"
        "v_cvt_pk_bf16_f32 v217, v114, v115\n"
        "v_mfma_f32_16x16x32_bf16 v[104:107], v[136:139], v[64:67], 0\n"
        "ds_read_b128 v[222:225], v251 offset:4608\n"
        "v_add_f32 v97, v96, v97\n"
        "v_mfma_f32_16x16x32_bf16 v[104:107], v[140:143], v[68:71], v[104:107]\n"
        "v_rcp_f32 v96, v97\n"
        "v_mfma_f32_16x16x32_bf16 v[108:111], v[148:151], v[68:71], 0\n"
        "v_mul_f32 v100, v96, v100\n"
        "v_mul_f32 v101, v96, v101\n"
        "v_mfma_f32_16x16x32_bf16 v[108:111], v[144:147], v[64:67], v[108:111]\n"
        "v_mul_f32 v102, v96, v102\n"
        "v_mul_f32 v103, v96, v103\n"
        "v_mfma_f32_16x16x32_bf16 v[116:119], a[44:47], a[220:223], 0\n"
        "v_cvt_pk_bf16_f32 v100, v100, v101\n"
        "v_cvt_pk_bf16_f32 v101, v102, v103\n"
        "v_mfma_f32_16x16x32_bf16 v[116:119], a[40:43], a[216:219], v[116:119]\n"
        "v_mul_f32 v104, v96, v104\n"
        "v_mul_f32 v105, v96, v105\n"
        "v_mfma_f32_16x16x32_bf16 v[116:119], a[36:39], a[248:251], v[116:119]\n"
        "v_mul_f32 v106, v96, v106\n"
        "v_mul_f32 v107, v96, v107\n"
        "v_mfma_f32_16x16x32_bf16 v[116:119], a[32:35], a[244:247], v[116:119]\n"
        "v_cvt_pk_bf16_f32 v102, v104, v105\n"
        "v_cvt_pk_bf16_f32 v103, v106, v107\n"
        "v_mfma_f32_16x16x32_bf16 v[116:119], a[28:31], a[240:243], v[116:119]\n"
        "ds_write_b128 v245, v[100:103] offset:0\n"
        "v_mfma_f32_16x16x32_bf16 v[116:119], a[24:27], a[236:239], v[116:119]\n"
        "v_mul_f32 v108, v96, v108\n"
        "v_mul_f32 v109, v96, v109\n"
        "v_smfmac_f32_16x16x64_bf16 v[124:127], a[252:255], v[72:79], v237\n"
        "v_mul_f32 v110, v96, v110\n"
        "v_mul_f32 v111, v96, v111\n"
        "v_mfma_f32_16x16x32_bf16 v[64:67], v[132:135], v[76:79], 0\n"
        "v_cvt_pk_bf16_f32 v108, v108, v109\n"
        "v_cvt_pk_bf16_f32 v109, v110, v111\n"
        "v_mfma_f32_16x16x32_bf16 v[64:67], v[128:131], v[72:75], v[64:67]\n"
        "ds_read_b128 v[156:159], v247 offset:0\n"
        "v_cvt_pk_bf16_f32 v218, v116, v117\n"
        "v_mfma_f32_16x16x32_bf16 v[68:71], v[136:139], v[72:75], 0\n"
        "ds_write_b64 v246, v[108:109] offset:0\n"
        "v_mfma_f32_16x16x32_bf16 v[68:71], v[140:143], v[76:79], v[68:71]\n"
        "ds_read_b128 v[160:163], v247 offset:4096\n"
        "v_cvt_pk_bf16_f32 v219, v118, v119\n"
        "v_mfma_f32_16x16x32_bf16 v[120:123], v[148:151], v[76:79], 0\n"
        "ds_read_b128 v[164:167], v247 offset:4352\n"
        "v_max_u32 v236, v96, v97\n"
        "v_mfma_f32_16x16x32_bf16 v[120:123], v[144:147], v[72:75], v[120:123]\n"
        "v_mov_b64 v[116:117], 0\n"
        "v_add_f32 v125, v124, v125\n"
        "v_mfma_f32_16x16x32_bf16 v[112:115], a[48:51], a[236:239], 0\n"
        "v_rcp_f32 v124, v125\n"
        "v_mfma_f32_16x16x32_bf16 v[112:115], a[52:55], a[240:243], v[112:115]\n"
        "v_mul_f32 v64, v124, v64\n"
        "v_mul_f32 v65, v124, v65\n"
        "v_mfma_f32_16x16x32_bf16 v[112:115], a[56:59], a[244:247], v[112:115]\n"
        "v_mul_f32 v66, v124, v66\n"
        "v_mul_f32 v67, v124, v67\n"
        "v_mfma_f32_16x16x32_bf16 v[112:115], a[60:63], a[248:251], v[112:115]\n"
        "v_cvt_pk_bf16_f32 v64, v64, v65\n"
        "v_cvt_pk_bf16_f32 v65, v66, v67\n"
        "v_mfma_f32_16x16x32_bf16 v[112:115], a[64:67], a[216:219], v[112:115]\n"
        "v_mul_f32 v68, v124, v68\n"
        "v_mul_f32 v69, v124, v69\n"
        "v_mfma_f32_16x16x32_bf16 v[112:115], a[68:71], a[220:223], v[112:115]\n"
        "ds_read_b128 a[216:219], v248 offset:0\n"
        "v_mul_f32 v70, v124, v70\n"
        "v_smfmac_f32_16x16x64_bf16 v[116:119], a[252:255], v[80:87], v237\n"
        "ds_read_b128 a[220:223], v248 offset:3072\n"
        "v_mul_f32 v71, v124, v71\n"
        "v_mfma_f32_16x16x32_bf16 v[72:75], v[132:135], v[84:87], 0\n"
        "v_cvt_pk_bf16_f32 v66, v68, v69\n"
        "v_cvt_pk_bf16_f32 v67, v70, v71\n"
        "v_mfma_f32_16x16x32_bf16 v[72:75], v[128:131], v[80:83], v[72:75]\n"
        "ds_write_b128 v245, v[64:67] offset:256\n"
        "v_mfma_f32_16x16x32_bf16 v[76:79], v[136:139], v[80:83], 0\n"
        "v_cvt_pk_bf16_f32 v220, v112, v113\n"
        "v_cvt_pk_bf16_f32 v221, v114, v115\n"
        "v_mfma_f32_16x16x32_bf16 v[76:79], v[140:143], v[84:87], v[76:79]\n"
        "ds_read_b128 a[224:227], v248 offset:32\n"
        "v_cvt_pk_bf16_f32 v226, v112, v113\n"
        "v_mfma_f32_16x16x32_bf16 v[96:99], v[148:151], v[84:87], 0\n"
        "ds_read_b128 a[228:231], v248 offset:3104\n"
        "v_cvt_pk_bf16_f32 v227, v114, v115\n"
        "v_mfma_f32_16x16x32_bf16 v[96:99], v[144:147], v[80:83], v[96:99]\n"
        "v_mul_f32 v120, v124, v120\n"
        "v_mul_f32 v121, v124, v121\n"
        "v_mfma_f32_16x16x32_bf16 v[100:103], v[152:155], v[216:219], v[48:51]\n"
        "s_waitcnt lgkmcnt(3)\n"
        "ds_read_b128 v[152:155], v247 offset:256\n"
        "v_mul_f32 v122, v124, v122\n"
        "v_mfma_f32_16x16x32_bf16 v[100:103], v[168:171], v[220:223], v[100:103]\n"
        "v_mul_f32 v123, v124, v123\n"
        "v_cvt_pk_bf16_f32 v120, v120, v121\n"
        "v_mfma_f32_16x16x32_bf16 v[104:107], v[172:175], v[216:219], v[52:55]\n"
        "v_cvt_pk_bf16_f32 v121, v122, v123\n"
        "v_max3_u32 v236, v236, v124, v125\n"
        "v_mfma_f32_16x16x32_bf16 v[104:107], v[188:191], v[224:227], v[104:107]\n"
        "ds_write_b64 v246, v[120:121] offset:256\n"
        "v_mfma_f32_16x16x32_bf16 v[176:179], v[156:159], a[216:219], 0\n"
        "ds_read_b128 v[222:225], v252 offset:4608\n"
        "v_add_f32 v117, v116, v117\n"
        "v_mfma_f32_16x16x32_bf16 v[176:179], v[160:163], a[220:223], v[176:179]\n"
        "ds_read_b128 a[232:235], v254 offset:0\n"
        "v_mfma_f32_16x16x32_bf16 v[196:199], v[164:167], a[220:223], 0\n"
        "s_waitcnt lgkmcnt(3)\n"
        "ds_read_b128 a[220:223], v254 offset:3072\n"
        "v_mfma_f32_16x16x32_bf16 v[196:199], v[152:155], a[216:219], v[196:199]\n"
        "v_rcp_f32 v116, v117\n"
        "v_mfma_f32_16x16x32_bf16 v[200:203], v[152:155], a[224:227], 0\n"
        "ds_read_b128 a[216:219], v254 offset:32\n"
        "v_mul_f32 v72, v116, v72\n"
        "v_mfma_f32_16x16x32_bf16 v[200:203], v[164:167], a[228:231], v[200:203]\n"
        "v_exp_f32 v100, v100\n"
        "v_mfma_f32_16x16x32_bf16 v[180:183], v[160:163], a[228:231], 0\n"
        "ds_read_b128 a[228:231], v254 offset:3104\n"
        "v_mul_f32 v73, v116, v73\n"
        "v_mfma_f32_16x16x32_bf16 v[180:183], v[156:159], a[224:227], v[180:183]\n"
        "ds_read_b128 a[236:239], v247 offset:8192\n"
        "v_mul_f32 v74, v116, v74\n"
        "v_mfma_f32_16x16x32_bf16 v[108:111], v[192:195], v[216:219], v[56:59]\n"
        "s_waitcnt lgkmcnt(4)\n"
        "ds_read_b128 a[224:227], v248 offset:16\n"
        "v_mul_f32 v75, v116, v75\n"
        "v_mfma_f32_16x16x32_bf16 v[108:111], v[208:211], v[220:223], v[108:111]\n"
        "ds_read_b128 a[240:243], v247 offset:12288\n"
        "v_cvt_pk_bf16_f32 v72, v72, v73\n"
        "v_mfma_f32_16x16x32_bf16 v[112:115], v[212:215], v[216:219], v[60:63]\n"
        "ds_read_b128 a[244:247], v247 offset:12544\n"
        "v_cvt_pk_bf16_f32 v73, v74, v75\n"
        "v_mfma_f32_16x16x32_bf16 v[112:115], v[228:231], v[224:227], v[112:115]\n"
        "ds_read_b128 a[248:251], v247 offset:8448\n"
        "v_mul_f32 v76, v116, v76\n"
        "v_mfma_f32_16x16x32_bf16 v[184:187], v[156:159], a[232:235], 0\n"
        "s_waitcnt lgkmcnt(6)\n"
        "v_exp_f32 v101, v101\n"
        "v_mfma_f32_16x16x32_bf16 v[184:187], v[160:163], a[220:223], v[184:187]\n"
        "v_mul_f32 v77, v116, v77\n"
        "v_mul_f32 v78, v116, v78\n"
        "v_mfma_f32_16x16x32_bf16 v[204:207], v[164:167], a[220:223], 0\n"
        "ds_read_b128 a[220:223], v248 offset:3088\n"
        "v_mul_f32 v79, v116, v79\n"
        "v_mfma_f32_16x16x32_bf16 v[204:207], v[152:155], a[232:235], v[204:207]\n"
        "ds_read_b128 a[232:235], v254 offset:16\n"
        "v_cvt_pk_bf16_f32 v88, v100, v101\n"
        "v_mfma_f32_16x16x32_bf16 v[192:195], v[152:155], a[216:219], 0\n"
        "s_waitcnt lgkmcnt(4)\n"
        "v_exp_f32 v102, v102\n"
        "v_mfma_f32_16x16x32_bf16 v[192:195], v[164:167], a[228:231], v[192:195]\n"
        "v_exp_f32 v104, v104\n"
        "v_mfma_f32_16x16x32_bf16 v[172:175], v[160:163], a[228:231], 0\n"
        "ds_read_b128 a[228:231], v254 offset:3088\n"
        "v_cvt_pk_bf16_f32 v74, v76, v77\n"
        "v_mfma_f32_16x16x32_bf16 v[172:175], v[156:159], a[216:219], v[172:175]\n"
        "ds_read_b128 a[216:219], v248 offset:6144\n"
        "v_cvt_pk_bf16_f32 v75, v78, v79\n"
        "v_mfma_f32_16x16x32_bf16 v[212:215], v[156:159], a[224:227], 0\n"
        "s_waitcnt lgkmcnt(3)\n"
        "ds_write_b128 v245, v[72:75] offset:512\n"
        "v_mfma_f32_16x16x32_bf16 v[212:215], v[160:163], a[220:223], v[212:215]\n"
        "v_exp_f32 v103, v103\n"
        "v_mfma_f32_16x16x32_bf16 v[216:219], v[164:167], a[220:223], 0\n"
        "ds_read_b128 a[220:223], v248 offset:9216\n"
        "v_cvt_pk_bf16_f32 v89, v102, v103\n"
        "v_mfma_f32_16x16x32_bf16 v[216:219], v[152:155], a[224:227], v[216:219]\n"
        "s_waitcnt lgkmcnt(4)\n"
        "ds_read_b128 a[224:227], v248 offset:6176\n"
        "v_mul_f32 v96, v116, v96\n"
        "v_mfma_f32_16x16x32_bf16 v[224:227], v[152:155], a[232:235], 0\n"
        "s_waitcnt lgkmcnt(4)\n"
        "v_exp_f32 v105, v105\n"
        "v_mfma_f32_16x16x32_bf16 v[224:227], v[164:167], a[228:231], v[224:227]\n"
        "v_exp_f32 v106, v106\n"
        "v_mfma_f32_16x16x32_bf16 v[220:223], v[160:163], a[228:231], 0\n"
        "ds_read_b128 a[228:231], v248 offset:9248\n"
        "v_cvt_pk_bf16_f32 v90, v104, v105\n"
        "v_mfma_f32_16x16x32_bf16 v[220:223], v[156:159], a[232:235], v[220:223]\n"
        "s_waitcnt lgkmcnt(4)\n"
        "ds_read_b128 a[232:235], v254 offset:6144\n"
        "v_mul_f32 v97, v116, v97\n"
        "v_mfma_f32_16x16x32_bf16 v[176:179], a[236:239], a[216:219], v[176:179]\n"
        "s_waitcnt lgkmcnt(3)\n"
        "v_mul_f32 v98, v116, v98\n"
        "v_mul_f32 v99, v116, v99\n"
        "v_mfma_f32_16x16x32_bf16 v[176:179], a[240:243], a[220:223], v[176:179]\n"
        "v_exp_f32 v107, v107\n"
        "v_mfma_f32_16x16x32_bf16 v[196:199], a[244:247], a[220:223], v[196:199]\n"
        "ds_read_b128 a[220:223], v254 offset:9216\n"
        "v_cvt_pk_bf16_f32 v91, v106, v107\n"
        "v_mfma_f32_16x16x32_bf16 v[196:199], a[248:251], a[216:219], v[196:199]\n"
        "s_waitcnt lgkmcnt(3)\n"
        "ds_read_b128 a[216:219], v254 offset:6176\n"
        "v_cvt_pk_bf16_f32 v96, v96, v97\n"
        "v_mfma_f32_16x16x32_bf16 v[200:203], a[248:251], a[224:227], v[200:203]\n"
        "s_waitcnt lgkmcnt(3)\n"
        "ds_read_b128 v[156:159], v247 offset:16384\n"
        "v_cvt_pk_bf16_f32 v97, v98, v99\n"
        "v_mfma_f32_16x16x32_bf16 v[200:203], a[244:247], a[228:231], v[200:203]\n"
        "ds_write_b64 v246, v[96:97] offset:512\n"
        "v_mfma_f32_16x16x32_bf16 v[180:183], a[240:243], a[228:231], v[180:183]\n"
        "ds_read_b128 a[228:231], v254 offset:9248\n"
        "v_max3_u32 v236, v236, v116, v117\n"
        "v_mfma_f32_16x16x32_bf16 v[180:183], a[236:239], a[224:227], v[180:183]\n"
        "s_waitcnt lgkmcnt(5)\n"
        "ds_read_b128 a[224:227], v248 offset:6160\n"
        "v_bfe_u32 v238, v250, 3, 4\n"
        "v_mfma_f32_16x16x32_bf16 v[184:187], a[236:239], a[232:235], v[184:187]\n"
        ".long 0xbec200ff\n"
        "44:\n"
        "s_branch 60b\n"
        "s_waitcnt lgkmcnt(5)\n"
        "ds_read_b128 v[160:163], v247 offset:20480\n"
        "v_mad_i32_i24 v238, v238, -4, v250\n"
        "v_mfma_f32_16x16x32_bf16 v[184:187], a[240:243], a[220:223], v[184:187]\n"
        "ds_read_b128 v[164:167], v247 offset:20736\n"
        "v_add_u32 v238, s49, v238\n"
        "v_mfma_f32_16x16x32_bf16 v[204:207], a[244:247], a[220:223], v[204:207]\n"
        "ds_read_b128 a[220:223], v248 offset:9232\n"
        "v_mfma_f32_16x16x32_bf16 v[204:207], a[248:251], a[232:235], v[204:207]\n"
        "s_waitcnt lgkmcnt(6)\n"
        "ds_read_b128 a[232:235], v254 offset:6160\n"
        "v_mfma_f32_16x16x32_bf16 v[192:195], a[248:251], a[216:219], v[192:195]\n"
        "s_waitcnt lgkmcnt(5)\n"
        "ds_read_b128 v[152:155], v247 offset:16640\n"
        "v_mfma_f32_16x16x32_bf16 v[192:195], a[244:247], a[228:231], v[192:195]\n"
        "v_exp_f32 v108, v108\n"
        "v_mfma_f32_16x16x32_bf16 v[172:175], a[240:243], a[228:231], v[172:175]\n"
        "ds_read_b128 a[228:231], v254 offset:9232\n"
        "v_mfma_f32_16x16x32_bf16 v[172:175], a[236:239], a[216:219], v[172:175]\n"
        "s_waitcnt lgkmcnt(5)\n"
        "ds_read_b128 a[216:219], v248 offset:12288\n"
        "v_mfma_f32_16x16x32_bf16 v[212:215], a[236:239], a[224:227], v[212:215]\n"
        "s_waitcnt lgkmcnt(4)\n"
        "v_exp_f32 v109, v109\n"
        "v_mfma_f32_16x16x32_bf16 v[212:215], a[240:243], a[220:223], v[212:215]\n"
        "v_exp_f32 v110, v110\n"
        "v_mfma_f32_16x16x32_bf16 v[216:219], a[244:247], a[220:223], v[216:219]\n"
        "ds_read_b128 a[220:223], v248 offset:15360\n"
        "v_cvt_pk_bf16_f32 v92, v108, v109\n"
        "v_mfma_f32_16x16x32_bf16 v[216:219], a[248:251], a[224:227], v[216:219]\n"
        "s_waitcnt lgkmcnt(3)\n"
        "ds_read_b128 a[224:227], v248 offset:12320\n"
        "v_mfma_f32_16x16x32_bf16 v[224:227], a[248:251], a[232:235], v[224:227]\n"
        "s_waitcnt lgkmcnt(3)\n"
        "v_exp_f32 v112, v112\n"
        "v_mfma_f32_16x16x32_bf16 v[224:227], a[244:247], a[228:231], v[224:227]\n"
        "v_exp_f32 v111, v111\n"
        "v_mfma_f32_16x16x32_bf16 v[220:223], a[240:243], a[228:231], v[220:223]\n"
        "ds_read_b128 a[228:231], v248 offset:15392\n"
        "v_cvt_pk_bf16_f32 v93, v110, v111\n"
        "v_mfma_f32_16x16x32_bf16 v[220:223], a[236:239], a[232:235], v[220:223]\n"
        "s_waitcnt lgkmcnt(3)\n"
        "ds_read_b128 a[232:235], v254 offset:12288\n"
        "v_mfma_f32_16x16x32_bf16 v[176:179], v[156:159], a[216:219], v[176:179]\n"
        "s_waitcnt lgkmcnt(3)\n"
        "v_exp_f32 v113, v113\n"
        "v_mfma_f32_16x16x32_bf16 v[176:179], v[160:163], a[220:223], v[176:179]\n"
        "v_exp_f32 v114, v114\n"
        "v_mfma_f32_16x16x32_bf16 v[196:199], v[164:167], a[220:223], v[196:199]\n"
        "ds_read_b128 a[220:223], v254 offset:15360\n"
        "v_cvt_pk_bf16_f32 v94, v112, v113\n"
        "v_mfma_f32_16x16x32_bf16 v[196:199], v[152:155], a[216:219], v[196:199]\n"
        "s_waitcnt lgkmcnt(3)\n"
        "ds_read_b128 a[216:219], v254 offset:12320\n"
        "v_mfma_f32_16x16x32_bf16 v[200:203], v[152:155], a[224:227], v[200:203]\n"
        "s_waitcnt lgkmcnt(3)\n"
        "v_exp_f32 v115, v115\n"
        "v_mfma_f32_16x16x32_bf16 v[200:203], v[164:167], a[228:231], v[200:203]\n"
        "v_cvt_pk_bf16_f32 v95, v114, v115\n"
        "v_mfma_f32_16x16x32_bf16 v[180:183], v[160:163], a[228:231], v[180:183]\n"
        "s_waitcnt vmcnt(0)\n"
        "s_barrier\n"
        "ds_read_b128 a[228:231], v254 offset:15392\n"
        "v_mfma_f32_16x16x32_bf16 v[180:183], v[156:159], a[224:227], v[180:183]\n"
        "s_waitcnt lgkmcnt(3)\n"
        "ds_read_b128 a[224:227], v248 offset:12304\n"
        "v_mfma_f32_16x16x32_bf16 v[184:187], v[156:159], a[232:235], v[184:187]\n"
        "s_waitcnt lgkmcnt(3)\n"
        "v_mfma_f32_16x16x32_bf16 v[184:187], v[160:163], a[220:223], v[184:187]\n"
        "v_mfma_f32_16x16x32_bf16 v[204:207], v[164:167], a[220:223], v[204:207]\n"
        "ds_read_b128 a[220:223], v248 offset:15376\n"
        "v_mfma_f32_16x16x32_bf16 v[204:207], v[152:155], a[232:235], v[204:207]\n"
        "s_waitcnt lgkmcnt(3)\n"
        "ds_read_b128 a[232:235], v254 offset:12304\n"
        "v_mfma_f32_16x16x32_bf16 v[192:195], v[152:155], a[216:219], v[192:195]\n"
        "s_waitcnt lgkmcnt(3)\n"
        "ds_read_b128 a[236:239], v240 offset:0\n"
        "v_mfma_f32_16x16x32_bf16 v[192:195], v[164:167], a[228:231], v[192:195]\n"
        "ds_read_b128 a[240:243], v241 offset:0\n"
        "v_mfma_f32_16x16x32_bf16 v[172:175], v[160:163], a[228:231], v[172:175]\n"
        "ds_read_b128 a[228:231], v254 offset:15376\n"
        "v_mfma_f32_16x16x32_bf16 v[172:175], v[156:159], a[216:219], v[172:175]\n"
        "s_waitcnt lgkmcnt(5)\n"
        "ds_read_b128 a[244:247], v240 offset:128\n"
        "v_mfma_f32_16x16x32_bf16 v[212:215], v[156:159], a[224:227], v[212:215]\n"
        "s_waitcnt lgkmcnt(5)\n"
        "ds_read_b128 a[248:251], v241 offset:128\n"
        "v_mfma_f32_16x16x32_bf16 v[212:215], v[160:163], a[220:223], v[212:215]\n"
        "ds_read_b128 a[216:219], v240 offset:256\n"
        "v_mfma_f32_16x16x32_bf16 v[216:219], v[164:167], a[220:223], v[216:219]\n"
        "ds_read_b128 a[220:223], v241 offset:256\n"
        "v_mfma_f32_16x16x32_bf16 v[216:219], v[152:155], a[224:227], v[216:219]\n"
        "s_waitcnt lgkmcnt(6)\n"
        "v_mfma_f32_16x16x32_bf16 v[224:227], v[152:155], a[232:235], v[224:227]\n"
        "s_waitcnt lgkmcnt(4)\n"
        "v_mfma_f32_16x16x32_bf16 v[224:227], v[164:167], a[228:231], v[224:227]\n"
        "v_mfma_f32_16x16x32_bf16 v[220:223], v[160:163], a[228:231], v[220:223]\n"
        "v_mfma_f32_16x16x32_bf16 v[220:223], v[156:159], a[232:235], v[220:223]\n"
        "s_waitcnt lgkmcnt(0)\n"
        "s_cmp_eq_u32 s42, 2\n"
        "s_cbranch_scc1 12f\n"
        "s_cmp_eq_u32 s42, 3\n"
        "s_cbranch_scc1 12f\n"
        ".p2align 6\n"
        "s_nop 0\n"
        "s_nop 0\n"
        "s_nop 0\n"
        "s_nop 0\n"
        "11:\n"
        "10:\n"
        "v_mfma_f32_16x16x32_bf16 v[112:115], a[72:75], a[236:239], 0\n"
        "v_mov_b64 v[124:125], 0\n"
        "v_cvt_pk_bf16_f32 v232, v176, v180\n"
        "v_mfma_f32_16x16x32_bf16 v[112:115], a[76:79], a[240:243], v[112:115]\n"
        "s_waitcnt lgkmcnt(3)\n"
        "s_add_u32 s41, s30, s40\n"
        "v_cvt_pk_bf16_f32 v233, v184, v172\n"
        "v_cvt_pk_bf16_f32 v234, v177, v181\n"
        "v_mfma_f32_16x16x32_bf16 v[112:115], a[80:83], a[244:247], v[112:115]\n"
        "s_waitcnt lgkmcnt(2)\n"
        "buffer_store_dwordx2 v[232:233], v250, s[16:19], s38 offen offset:0 sc1\n"
        "v_cvt_pk_bf16_f32 v235, v185, v173\n"
        "v_mfma_f32_16x16x32_bf16 v[112:115], a[84:87], a[248:251], v[112:115]\n"
        "s_waitcnt lgkmcnt(1)\n"
        "v_cvt_pk_bf16_f32 v232, v178, v182\n"
        "v_cvt_pk_bf16_f32 v233, v186, v174\n"
        "v_mfma_f32_16x16x32_bf16 v[112:115], a[88:91], a[216:219], v[112:115]\n"
        "s_waitcnt lgkmcnt(0)\n"
        "v_mfma_f32_16x16x32_bf16 v[112:115], a[92:95], a[220:223], v[112:115]\n"
        "s_nop 2\n"
        "v_smfmac_f32_16x16x64_bf16 v[124:127], a[252:255], v[88:95], v237\n"
        "s_nop 1\n"
        "v_mfma_f32_16x16x32_bf16 v[80:83], v[132:135], v[92:95], 0\n"
        "buffer_store_dwordx2 v[234:235], v250, s[16:19], s38 offen offset:384 sc1\n"
        "v_cvt_pk_bf16_f32 v152, v112, v113\n"
        "v_mfma_f32_16x16x32_bf16 v[80:83], v[128:131], v[88:91], v[80:83]\n"
        "v_cvt_pk_bf16_f32 v153, v114, v115\n"
        "v_cvt_pk_bf16_f32 v234, v179, v183\n"
        "v_mfma_f32_16x16x32_bf16 v[84:87], v[136:139], v[88:91], 0\n"
        "v_cvt_pk_bf16_f32 v235, v187, v175\n"
        "v_add_f32 v125, v124, v125\n"
        "v_mfma_f32_16x16x32_bf16 v[84:87], v[140:143], v[92:95], v[84:87]\n"
        "v_rcp_f32 v124, v125\n"
        "v_mfma_f32_16x16x32_bf16 v[96:99], v[148:151], v[92:95], 0\n"
        "buffer_store_dwordx2 v[232:233], v250, s[16:19], s38 offen offset:768 sc1\n"
        "v_mul_f32 v80, v124, v80\n"
        "v_mfma_f32_16x16x32_bf16 v[96:99], v[144:147], v[88:91], v[96:99]\n"
        "s_add_u32 s35, s47, 786432\n"
        "v_mul_f32 v81, v124, v81\n"
        "v_mul_f32 v82, v124, v82\n"
        "v_mfma_f32_16x16x32_bf16 v[116:119], a[116:119], a[220:223], 0\n"
        "s_sub_u32 s46, s35, 100663296\n"
        "v_mul_f32 v83, v124, v83\n"
        "v_cvt_pk_bf16_f32 v80, v80, v81\n"
        "v_mfma_f32_16x16x32_bf16 v[116:119], a[112:115], a[216:219], v[116:119]\n"
        "s_cmp_lt_u32 s46, 786432\n"
        "v_cvt_pk_bf16_f32 v81, v82, v83\n"
        "v_cvt_pk_bf16_f32 v232, v196, v200\n"
        "v_mfma_f32_16x16x32_bf16 v[116:119], a[108:111], a[248:251], v[116:119]\n"
        "buffer_store_dwordx2 v[234:235], v250, s[16:19], s38 offen offset:1152 sc1\n"
        "v_cvt_pk_bf16_f32 v233, v204, v192\n"
        "v_mfma_f32_16x16x32_bf16 v[116:119], a[104:107], a[244:247], v[116:119]\n"
        "s_cselect_b32 s35, s46, s35\n"
        "v_mul_f32 v84, v124, v84\n"
        "v_mul_f32 v85, v124, v85\n"
        "v_mfma_f32_16x16x32_bf16 v[116:119], a[100:103], a[240:243], v[116:119]\n"
        "v_mul_f32 v86, v124, v86\n"
        "v_mul_f32 v87, v124, v87\n"
        "v_mfma_f32_16x16x32_bf16 v[116:119], a[96:99], a[236:239], v[116:119]\n"
        "v_cvt_pk_bf16_f32 v82, v84, v85\n"
        "v_cvt_pk_bf16_f32 v83, v86, v87\n"
        "v_mfma_f32_16x16x32_bf16 v[120:123], a[120:123], a[236:239], 0\n"
        "ds_write_b128 v245, v[80:83] offset:768\n"
        "v_mfma_f32_16x16x32_bf16 v[120:123], a[124:127], a[240:243], v[120:123]\n"
        "buffer_store_dwordx2 v[232:233], v250, s[16:19], s39 offen offset:0 sc1\n"
        "v_mul_f32 v96, v124, v96\n"
        "v_mfma_f32_16x16x32_bf16 v[120:123], a[128:131], a[244:247], v[120:123]\n"
        "v_mul_f32 v97, v124, v97\n"
        "v_mul_f32 v98, v124, v98\n"
        "v_mfma_f32_16x16x32_bf16 v[120:123], a[132:135], a[248:251], v[120:123]\n"
        "v_mul_f32 v99, v124, v99\n"
        "v_cvt_pk_bf16_f32 v234, v197, v201\n"
        "v_mfma_f32_16x16x32_bf16 v[120:123], a[136:139], a[216:219], v[120:123]\n"
        "v_cvt_pk_bf16_f32 v235, v205, v193\n"
        "v_cvt_pk_bf16_f32 v154, v116, v117\n"
        "v_mfma_f32_16x16x32_bf16 v[120:123], a[140:143], a[220:223], v[120:123]\n"
        "buffer_store_dwordx2 v[234:235], v250, s[16:19], s39 offen offset:384 sc1\n"
        "v_cvt_pk_bf16_f32 v155, v118, v119\n"
        "v_mfma_f32_16x16x32_bf16 v[112:115], a[20:23], a[220:223], 0\n"
        "v_cvt_pk_bf16_f32 v96, v96, v97\n"
        "v_cvt_pk_bf16_f32 v97, v98, v99\n"
        "v_mfma_f32_16x16x32_bf16 v[112:115], a[16:19], a[216:219], v[112:115]\n"
        "ds_write_b64 v246, v[96:97] offset:768\n"
        "v_mfma_f32_16x16x32_bf16 v[112:115], a[12:15], a[248:251], v[112:115]\n"
        "v_max3_u32 v236, v236, v124, v125\n"
        "v_cvt_pk_bf16_f32 v232, v198, v202\n"
        "v_mfma_f32_16x16x32_bf16 v[112:115], a[8:11], a[244:247], v[112:115]\n"
        "v_cvt_pk_bf16_f32 v233, v206, v194\n"
        "v_cmp_lt_u32 vcc, s24, v236\n"
        "v_mfma_f32_16x16x32_bf16 v[112:115], a[4:7], a[240:243], v[112:115]\n"
        "buffer_store_dwordx2 v[232:233], v250, s[16:19], s39 offen offset:768 sc1\n"
        "v_cvt_pk_bf16_f32 v168, v120, v121\n"
        "v_mfma_f32_16x16x32_bf16 v[112:115], a[0:3], a[236:239], v[112:115]\n"
        "s_cmp_lg_u64 vcc, 0\n"
        "s_addc_u32 s25, s25, s25\n"
        "v_cvt_pk_bf16_f32 v169, v122, v123\n"
        "v_mfma_f32_16x16x32_bf16 v[116:119], a[24:27], a[236:239], 0\n"
        "v_cvt_pk_bf16_f32 v234, v199, v203\n"
        "v_cvt_pk_bf16_f32 v235, v207, v195\n"
        "v_mfma_f32_16x16x32_bf16 v[116:119], a[28:31], a[240:243], v[116:119]\n"
        "v_cvt_pk_bf16_f32 v232, v212, v220\n"
        "v_cvt_pk_bf16_f32 v156, v112, v113\n"
        "v_mfma_f32_16x16x32_bf16 v[116:119], a[32:35], a[244:247], v[116:119]\n"
        "buffer_store_dwordx2 v[234:235], v250, s[16:19], s39 offen offset:1152 sc1\n"
        "v_cvt_pk_bf16_f32 v157, v114, v115\n"
        "v_mfma_f32_16x16x32_bf16 v[116:119], a[36:39], a[248:251], v[116:119]\n"
        "v_cvt_pk_bf16_f32 v234, v213, v221\n"
        "v_xor_b32 v251, 0x2000, v251\n"
        "v_mfma_f32_16x16x32_bf16 v[116:119], a[40:43], a[216:219], v[116:119]\n"
        "v_xor_b32 v252, 0x2000, v252\n"
        "v_mfma_f32_16x16x32_bf16 v[116:119], a[44:47], a[220:223], v[116:119]\n"
        "s_nop 2\n"
        "v_mfma_f32_16x16x32_bf16 v[120:123], a[68:71], a[220:223], 0\n"
        "s_nop 2\n"
        "v_mfma_f32_16x16x32_bf16 v[120:123], a[64:67], a[216:219], v[120:123]\n"
        "buffer_store_dword v232, v238, s[16:19], s38 offen offset:0 sc1\n"
        "v_mfma_f32_16x16x32_bf16 v[120:123], a[60:63], a[248:251], v[120:123]\n"
        "ds_read_b128 a[224:227], v240 offset:1536\n"
        "v_cvt_pk_bf16_f32 v158, v116, v117\n"
        "v_mfma_f32_16x16x32_bf16 v[120:123], a[56:59], a[244:247], v[120:123]\n"
        "ds_read_b128 a[228:231], v241 offset:1536\n"
        "v_cvt_pk_bf16_f32 v159, v118, v119\n"
        "v_mfma_f32_16x16x32_bf16 v[120:123], a[52:55], a[240:243], v[120:123]\n"
        "ds_read_b128 a[232:235], v240 offset:1664\n"
        "v_cvt_pk_bf16_f32 v232, v214, v222\n"
        "v_mfma_f32_16x16x32_bf16 v[120:123], a[48:51], a[236:239], v[120:123]\n"
        "buffer_store_dword v234, v238, s[16:19], s38 offen offset:384 sc1\n"
        "v_mfma_f32_16x16x32_bf16 v[112:115], a[236:239], a[144:147], 0\n"
        "v_add_u32 v247, s45, v247\n"
        "v_cvt_pk_bf16_f32 v234, v215, v223\n"
        "v_mfma_f32_16x16x32_bf16 v[112:115], a[240:243], a[148:151], v[112:115]\n"
        "s_nop 2\n"
        "v_mfma_f32_16x16x32_bf16 v[112:115], a[244:247], a[152:155], v[112:115]\n"
        "v_cvt_pk_bf16_f32 v160, v120, v121\n"
        "v_cvt_pk_bf16_f32 v161, v122, v123\n"
        "v_mfma_f32_16x16x32_bf16 v[112:115], a[248:251], a[156:159], v[112:115]\n"
        "v_cvt_pk_bf16_f32 v166, v120, v121\n"
        "v_cvt_pk_bf16_f32 v167, v122, v123\n"
        "v_mfma_f32_16x16x32_bf16 v[112:115], a[216:219], a[160:163], v[112:115]\n"
        "buffer_store_dword v232, v238, s[16:19], s38 offen offset:768 sc1\n"
        "v_mfma_f32_16x16x32_bf16 v[112:115], a[220:223], a[164:167], v[112:115]\n"
        "s_nop 2\n"
        "v_mfma_f32_16x16x32_bf16 v[116:119], a[220:223], a[188:191], 0\n"
        "v_cvt_pk_bf16_f32 v232, v216, v224\n"
        "s_nop 0\n"
        "v_mfma_f32_16x16x32_bf16 v[116:119], a[216:219], a[184:187], v[116:119]\n"
        "s_nop 0\n"
        "v_cvt_pk_bf16_f32 v128, v112, v113\n"
        "v_cvt_pk_bf16_f32 v129, v114, v115\n"
        "v_mfma_f32_16x16x32_bf16 v[116:119], a[248:251], a[180:183], v[116:119]\n"
        "buffer_store_dword v234, v238, s[16:19], s38 offen offset:1152 sc1\n"
        "v_mfma_f32_16x16x32_bf16 v[116:119], a[244:247], a[176:179], v[116:119]\n"
        "s_nop 0\n"
        "v_cvt_pk_bf16_f32 v234, v217, v225\n"
        "v_mfma_f32_16x16x32_bf16 v[116:119], a[240:243], a[172:175], v[116:119]\n"
        "v_mfma_f32_16x16x32_bf16 v[116:119], a[236:239], a[168:171], v[116:119]\n"
        "s_nop 2\n"
        "v_mfma_f32_16x16x32_bf16 v[120:123], a[236:239], a[192:195], 0\n"
        "ds_read_b128 a[236:239], v241 offset:1664\n"
        "s_nop 0\n"
        "v_mfma_f32_16x16x32_bf16 v[120:123], a[240:243], a[196:199], v[120:123]\n"
        "buffer_store_dword v232, v238, s[16:19], s39 offen offset:0 sc1\n"
        "v_mfma_f32_16x16x32_bf16 v[120:123], a[244:247], a[200:203], v[120:123]\n"
        "ds_read_b128 a[240:243], v240 offset:1792\n"
        "ds_read_b128 a[244:247], v241 offset:1792\n"
        "v_mfma_f32_16x16x32_bf16 v[120:123], a[248:251], a[204:207], v[120:123]\n"
        "v_cvt_pk_bf16_f32 v136, v116, v117\n"
        "v_cvt_pk_bf16_f32 v137, v118, v119\n"
        "v_mfma_f32_16x16x32_bf16 v[120:123], a[216:219], a[208:211], v[120:123]\n"
        "v_cvt_pk_bf16_f32 v232, v218, v226\n"
        "v_mfma_f32_16x16x32_bf16 v[120:123], a[220:223], a[212:215], v[120:123]\n"
        "s_waitcnt lgkmcnt(5)\n"
        "buffer_store_dword v234, v238, s[16:19], s39 offen offset:384 sc1\n"
        "v_mfma_f32_16x16x32_bf16 v[112:115], a[72:75], a[224:227], 0\n"
        "s_waitcnt lgkmcnt(4)\n"
        "s_nop 1\n"
        "v_mfma_f32_16x16x32_bf16 v[112:115], a[76:79], a[228:231], v[112:115]\n"
        "s_waitcnt lgkmcnt(3)\n"
        "v_cvt_pk_bf16_f32 v234, v219, v227\n"
        "s_nop 0\n"
        "v_mfma_f32_16x16x32_bf16 v[112:115], a[80:83], a[232:235], v[112:115]\n"
        "s_waitcnt lgkmcnt(2)\n"
        "v_cvt_pk_bf16_f32 v144, v120, v121\n"
        "v_cvt_pk_bf16_f32 v145, v122, v123\n"
        "v_mfma_f32_16x16x32_bf16 v[112:115], a[84:87], a[236:239], v[112:115]\n"
        "s_waitcnt lgkmcnt(1)\n"
        "v_mfma_f32_16x16x32_bf16 v[112:115], a[88:91], a[240:243], v[112:115]\n"
        "s_waitcnt lgkmcnt(0)\n"
        "buffer_store_dword v232, v238, s[16:19], s39 offen offset:768 sc1\n"
        "v_mfma_f32_16x16x32_bf16 v[112:115], a[92:95], a[244:247], v[112:115]\n"
        "s_nop 2\n"
        "v_mfma_f32_16x16x32_bf16 v[116:119], a[116:119], a[244:247], 0\n"
        "s_nop 2\n"
        "v_mfma_f32_16x16x32_bf16 v[116:119], a[112:115], a[240:243], v[116:119]\n"
        "v_cvt_pk_bf16_f32 v172, v112, v113\n"
        "v_cvt_pk_bf16_f32 v173, v114, v115\n"
        "v_mfma_f32_16x16x32_bf16 v[116:119], a[108:111], a[236:239], v[116:119]\n"
        "buffer_store_dword v234, v238, s[16:19], s39 offen offset:1152 sc1\n"
        "v_mfma_f32_16x16x32_bf16 v[116:119], a[104:107], a[232:235], v[116:119]\n"
        "v_mfma_f32_16x16x32_bf16 v[116:119], a[100:103], a[228:231], v[116:119]\n"
        "v_mfma_f32_16x16x32_bf16 v[116:119], a[96:99], a[224:227], v[116:119]\n"
        "s_mov_b32 m0, s48\n"
        "s_nop 1\n"
        "v_mfma_f32_16x16x32_bf16 v[120:123], a[120:123], a[224:227], 0\n"
        "ds_read_b128 v[162:165], v251 offset:0\n"
        "s_nop 0\n"
        "v_mfma_f32_16x16x32_bf16 v[120:123], a[124:127], a[228:231], v[120:123]\n"
        "buffer_load_dwordx4 v249, s[12:15], s36 offen offset:0 lds\n"
        "v_mfma_f32_16x16x32_bf16 v[120:123], a[128:131], a[232:235], v[120:123]\n"
        "v_cvt_pk_bf16_f32 v174, v116, v117\n"
        "v_cvt_pk_bf16_f32 v175, v118, v119\n"
        "v_mfma_f32_16x16x32_bf16 v[120:123], a[132:135], a[236:239], v[120:123]\n"
        "v_mfma_f32_16x16x32_bf16 v[120:123], a[136:139], a[240:243], v[120:123]\n"
        "v_mfma_f32_16x16x32_bf16 v[120:123], a[140:143], a[244:247], v[120:123]\n"
        "s_nop 2\n"
        "v_mfma_f32_16x16x32_bf16 v[112:115], a[20:23], a[244:247], 0\n"
        "s_nop 2\n"
        "v_mfma_f32_16x16x32_bf16 v[112:115], a[16:19], a[240:243], v[112:115]\n"
        "v_cvt_pk_bf16_f32 v190, v120, v121\n"
        "v_cvt_pk_bf16_f32 v191, v122, v123\n"
        "v_mfma_f32_16x16x32_bf16 v[112:115], a[12:15], a[236:239], v[112:115]\n"
        "v_mfma_f32_16x16x32_bf16 v[112:115], a[8:11], a[232:235], v[112:115]\n"
        "v_mfma_f32_16x16x32_bf16 v[112:115], a[4:7], a[228:231], v[112:115]\n"
        "ds_read_b128 v[182:185], v251 offset:512\n"
        "v_mfma_f32_16x16x32_bf16 v[112:115], a[0:3], a[224:227], v[112:115]\n"
        "s_nop 2\n"
        "v_mfma_f32_16x16x32_bf16 v[116:119], a[24:27], a[224:227], 0\n"
        "s_nop 2\n"
        "v_mfma_f32_16x16x32_bf16 v[116:119], a[28:31], a[228:231], v[116:119]\n"
        "v_cvt_pk_bf16_f32 v176, v112, v113\n"
        "v_cvt_pk_bf16_f32 v177, v114, v115\n"
        "v_mfma_f32_16x16x32_bf16 v[116:119], a[32:35], a[232:235], v[116:119]\n"
        "v_mfma_f32_16x16x32_bf16 v[116:119], a[36:39], a[236:239], v[116:119]\n"
        "buffer_load_dwordx4 v253, s[12:15], s36 offen offset:1024 lds\n"
        "v_mfma_f32_16x16x32_bf16 v[116:119], a[40:43], a[240:243], v[116:119]\n"
        "v_mfma_f32_16x16x32_bf16 v[116:119], a[44:47], a[244:247], v[116:119]\n"
        "s_nop 2\n"
        "v_mfma_f32_16x16x32_bf16 v[120:123], a[68:71], a[244:247], 0\n"
        "s_nop 2\n"
        "v_mfma_f32_16x16x32_bf16 v[120:123], a[64:67], a[240:243], v[120:123]\n"
        "v_cvt_pk_bf16_f32 v178, v116, v117\n"
        "v_cvt_pk_bf16_f32 v179, v118, v119\n"
        "v_mfma_f32_16x16x32_bf16 v[120:123], a[60:63], a[236:239], v[120:123]\n"
        "v_mfma_f32_16x16x32_bf16 v[120:123], a[56:59], a[232:235], v[120:123]\n"
        "v_mfma_f32_16x16x32_bf16 v[120:123], a[52:55], a[228:231], v[120:123]\n"
        "v_mfma_f32_16x16x32_bf16 v[120:123], a[48:51], a[224:227], v[120:123]\n"
        "s_nop 2\n"
        "v_mfma_f32_16x16x32_bf16 v[112:115], a[224:227], a[144:147], 0\n"
        "s_nop 2\n"
        "v_mfma_f32_16x16x32_bf16 v[112:115], a[228:231], a[148:151], v[112:115]\n"
        "v_cvt_pk_bf16_f32 v180, v120, v121\n"
        "v_cvt_pk_bf16_f32 v181, v122, v123\n"
        "v_mfma_f32_16x16x32_bf16 v[112:115], a[232:235], a[152:155], v[112:115]\n"
        "v_cvt_pk_bf16_f32 v186, v120, v121\n"
        "v_cvt_pk_bf16_f32 v187, v122, v123\n"
        "v_mfma_f32_16x16x32_bf16 v[112:115], a[236:239], a[156:159], v[112:115]\n"
        "s_add_u32 m0, s41, 0\n"
        "v_mfma_f32_16x16x32_bf16 v[112:115], a[240:243], a[160:163], v[112:115]\n"
        "ds_read_b128 a[248:251], v240 offset:12288\n"
        "v_mfma_f32_16x16x32_bf16 v[112:115], a[244:247], a[164:167], v[112:115]\n"
        "buffer_load_dwordx4 v242, s[8:11], s47 offen lds\n"
        "v_mfma_f32_16x16x32_bf16 v[96:99], v[152:155], v[156:159], v[0:3]\n"
        "s_waitcnt lgkmcnt(2)\n"
        "ds_read_b128 a[216:219], v241 offset:12288\n"
        "s_nop 0\n"
        "v_mfma_f32_16x16x32_bf16 v[96:99], v[168:171], v[160:163], v[96:99]\n"
        "ds_read_b128 a[220:223], v240 offset:12416\n"
        "s_sub_u32 s31, s30, s28\n"
        "s_nop 0\n"
        "v_mfma_f32_16x16x32_bf16 v[100:103], v[172:175], v[156:159], v[4:7]\n"
        "s_sub_u32 s45, s28, s29\n"
        "v_cvt_pk_bf16_f32 v130, v112, v113\n"
        "v_cvt_pk_bf16_f32 v131, v114, v115\n"
        "v_mfma_f32_16x16x32_bf16 v[100:103], v[188:191], v[164:167], v[100:103]\n"
        "s_add_u32 s28, s28, s31\n"
        "ds_read_b128 v[162:165], v252 offset:0\n"
        "v_mfma_f32_16x16x32_bf16 v[116:119], a[244:247], a[188:191], 0\n"
        "s_mov_b32 s30, s29\n"
        "v_exp_f32 v96, v96\n"
        "v_mfma_f32_16x16x32_bf16 v[116:119], a[240:243], a[184:187], v[116:119]\n"
        "s_add_u32 s29, s29, s45\n"
        "v_exp_f32 v97, v97\n"
        "v_mfma_f32_16x16x32_bf16 v[116:119], a[236:239], a[180:183], v[116:119]\n"
        "v_exp_f32 v98, v98\n"
        "v_mfma_f32_16x16x32_bf16 v[116:119], a[232:235], a[176:179], v[116:119]\n"
        "v_exp_f32 v100, v100\n"
        "v_mfma_f32_16x16x32_bf16 v[116:119], a[228:231], a[172:175], v[116:119]\n"
        "v_exp_f32 v99, v99\n"
        "v_mfma_f32_16x16x32_bf16 v[116:119], a[224:227], a[168:171], v[116:119]\n"
        "v_exp_f32 v101, v101\n"
        "v_mfma_f32_16x16x32_bf16 v[104:107], v[152:155], v[176:179], v[16:19]\n"
        "s_waitcnt lgkmcnt(4)\n"
        "v_exp_f32 v102, v102\n"
        "v_mfma_f32_16x16x32_bf16 v[104:107], v[168:171], v[180:183], v[104:107]\n"
        "s_add_u32 m0, s41, 1024\n"
        "v_cvt_pk_bf16_f32 v64, v96, v97\n"
        "v_cvt_pk_bf16_f32 v65, v98, v99\n"
        "v_mfma_f32_16x16x32_bf16 v[108:111], v[172:175], v[176:179], v[20:23]\n"
        "v_cvt_pk_bf16_f32 v138, v116, v117\n"
        "v_cvt_pk_bf16_f32 v139, v118, v119\n"
        "v_mfma_f32_16x16x32_bf16 v[108:111], v[188:191], v[184:187], v[108:111]\n"
        "buffer_load_dwordx4 v243, s[8:11], s47 offen lds\n"
        "v_mfma_f32_16x16x32_bf16 v[120:123], a[224:227], a[192:195], 0\n"
        "ds_read_b128 a[224:227], v241 offset:12416\n"
        "v_cvt_pk_bf16_f32 v66, v100, v101\n"
        "v_mfma_f32_16x16x32_bf16 v[120:123], a[228:231], a[196:199], v[120:123]\n"
        "ds_read_b128 a[228:231], v240 offset:12544\n"
        "v_mfma_f32_16x16x32_bf16 v[120:123], a[232:235], a[200:203], v[120:123]\n"
        "ds_read_b128 a[232:235], v241 offset:12544\n"
        "v_mfma_f32_16x16x32_bf16 v[120:123], a[236:239], a[204:207], v[120:123]\n"
        "ds_read_b128 v[202:205], v251 offset:4096\n"
        "v_mfma_f32_16x16x32_bf16 v[120:123], a[240:243], a[208:211], v[120:123]\n"
        "ds_read_b128 v[182:185], v252 offset:512\n"
        "v_mfma_f32_16x16x32_bf16 v[120:123], a[244:247], a[212:215], v[120:123]\n"
        "s_waitcnt lgkmcnt(8)\n"
        "v_exp_f32 v103, v103\n"
        "v_mfma_f32_16x16x32_bf16 v[112:115], a[0:3], a[248:251], 0\n"
        "s_waitcnt lgkmcnt(7)\n"
        "v_cvt_pk_bf16_f32 v67, v102, v103\n"
        "s_nop 0\n"
        "v_mfma_f32_16x16x32_bf16 v[112:115], a[4:7], a[216:219], v[112:115]\n"
        "s_waitcnt lgkmcnt(6)\n"
        "v_exp_f32 v104, v104\n"
        "v_mfma_f32_16x16x32_bf16 v[112:115], a[8:11], a[220:223], v[112:115]\n"
        "s_waitcnt lgkmcnt(4)\n"
        "v_exp_f32 v105, v105\n"
        "v_mfma_f32_16x16x32_bf16 v[112:115], a[12:15], a[224:227], v[112:115]\n"
        "s_waitcnt lgkmcnt(3)\n"
        "v_exp_f32 v106, v106\n"
        "v_mfma_f32_16x16x32_bf16 v[112:115], a[16:19], a[228:231], v[112:115]\n"
        "s_waitcnt lgkmcnt(2)\n"
        "v_cvt_pk_bf16_f32 v72, v104, v105\n"
        "v_cvt_pk_bf16_f32 v146, v120, v121\n"
        "v_mfma_f32_16x16x32_bf16 v[112:115], a[20:23], a[232:235], v[112:115]\n"
        "s_add_u32 m0, s41, 2048\n"
        "v_exp_f32 v108, v108\n"
        "v_mfma_f32_16x16x32_bf16 v[116:119], a[44:47], a[232:235], 0\n"
        "v_cvt_pk_bf16_f32 v147, v122, v123\n"
        "v_add_u32 v245, s45, v245\n"
        "v_mfma_f32_16x16x32_bf16 v[116:119], a[40:43], a[228:231], v[116:119]\n"
        "buffer_load_dwordx4 v244, s[8:11], s47 offen lds\n"
        "v_mfma_f32_16x16x32_bf16 v[116:119], a[36:39], a[224:227], v[116:119]\n"
        "v_exp_f32 v107, v107\n"
        "v_mfma_f32_16x16x32_bf16 v[116:119], a[32:35], a[220:223], v[116:119]\n"
        "v_exp_f32 v109, v109\n"
        "v_mfma_f32_16x16x32_bf16 v[116:119], a[28:31], a[216:219], v[116:119]\n"
        "v_cvt_pk_bf16_f32 v73, v106, v107\n"
        "v_cvt_pk_bf16_f32 v74, v108, v109\n"
        "v_mfma_f32_16x16x32_bf16 v[116:119], a[24:27], a[248:251], v[116:119]\n"
        "v_exp_f32 v110, v110\n"
        "v_mfma_f32_16x16x32_bf16 v[120:123], a[48:51], a[248:251], 0\n"
        "v_exp_f32 v111, v111\n"
        "v_mfma_f32_16x16x32_bf16 v[120:123], a[52:55], a[216:219], v[120:123]\n"
        "v_cvt_pk_bf16_f32 v75, v110, v111\n"
        "v_cvt_pk_bf16_f32 v196, v112, v113\n"
        "v_mfma_f32_16x16x32_bf16 v[120:123], a[56:59], a[220:223], v[120:123]\n"
        "v_cvt_pk_bf16_f32 v197, v114, v115\n"
        "v_cvt_pk_bf16_f32 v198, v116, v117\n"
        "v_mfma_f32_16x16x32_bf16 v[120:123], a[60:63], a[224:227], v[120:123]\n"
        "v_cvt_pk_bf16_f32 v199, v118, v119\n"
        "v_add_u32 v246, s45, v246\n"
        "v_mfma_f32_16x16x32_bf16 v[120:123], a[64:67], a[228:231], v[120:123]\n"
        "v_mfma_f32_16x16x32_bf16 v[120:123], a[68:71], a[232:235], v[120:123]\n"
        "s_nop 2\n"
        "v_mfma_f32_16x16x32_bf16 v[112:115], a[92:95], a[232:235], 0\n"
        "s_nop 2\n"
        "v_mfma_f32_16x16x32_bf16 v[112:115], a[88:91], a[228:231], v[112:115]\n"
        "s_add_u32 m0, s41, 12288\n"
        "v_cvt_pk_bf16_f32 v200, v120, v121\n"
        "v_cvt_pk_bf16_f32 v201, v122, v123\n"
        "v_mfma_f32_16x16x32_bf16 v[112:115], a[84:87], a[224:227], v[112:115]\n"
        "v_cvt_pk_bf16_f32 v206, v120, v121\n"
        "v_cvt_pk_bf16_f32 v207, v122, v123\n"
        "v_mfma_f32_16x16x32_bf16 v[112:115], a[80:83], a[220:223], v[112:115]\n"
        "buffer_load_dwordx4 v242, s[8:11], s35 offen lds\n"
        "v_mfma_f32_16x16x32_bf16 v[112:115], a[76:79], a[216:219], v[112:115]\n"
        "v_mfma_f32_16x16x32_bf16 v[112:115], a[72:75], a[248:251], v[112:115]\n"
        "s_nop 2\n"
        "v_mfma_f32_16x16x32_bf16 v[96:99], v[152:155], v[196:199], v[32:35]\n"
        "s_waitcnt lgkmcnt(1)\n"
        "s_nop 1\n"
        "v_mfma_f32_16x16x32_bf16 v[96:99], v[168:171], v[200:203], v[96:99]\n"
        "s_nop 0\n"
        "v_cvt_pk_bf16_f32 v192, v112, v113\n"
        "v_cvt_pk_bf16_f32 v193, v114, v115\n"
        "v_mfma_f32_16x16x32_bf16 v[100:103], v[172:175], v[196:199], v[36:39]\n"
        "v_mfma_f32_16x16x32_bf16 v[100:103], v[188:191], v[204:207], v[100:103]\n"
        "ds_read_b128 v[202:205], v252 offset:4096\n"
        "s_cmp_eq_u32 s37, 100663296\n"
        "v_mfma_f32_16x16x32_bf16 v[116:119], a[96:99], a[248:251], 0\n"
        "s_cselect_b32 s46, 0, s37\n"
        "v_exp_f32 v96, v96\n"
        "v_mfma_f32_16x16x32_bf16 v[116:119], a[100:103], a[216:219], v[116:119]\n"
        "s_add_u32 s38, s46, s43\n"
        "v_exp_f32 v97, v97\n"
        "v_mfma_f32_16x16x32_bf16 v[116:119], a[104:107], a[220:223], v[116:119]\n"
        "s_add_u32 s39, s46, s44\n"
        "v_exp_f32 v98, v98\n"
        "v_mfma_f32_16x16x32_bf16 v[116:119], a[108:111], a[224:227], v[116:119]\n"
        "s_add_u32 s37, s37, 6291456\n"
        "v_exp_f32 v100, v100\n"
        "v_mfma_f32_16x16x32_bf16 v[116:119], a[112:115], a[228:231], v[116:119]\n"
        "v_exp_f32 v99, v99\n"
        "v_mfma_f32_16x16x32_bf16 v[116:119], a[116:119], a[232:235], v[116:119]\n"
        "s_add_u32 m0, s41, 13312\n"
        "v_exp_f32 v101, v101\n"
        "v_mfma_f32_16x16x32_bf16 v[120:123], a[140:143], a[232:235], 0\n"
        "v_exp_f32 v102, v102\n"
        "v_mfma_f32_16x16x32_bf16 v[120:123], a[136:139], a[228:231], v[120:123]\n"
        "buffer_load_dwordx4 v243, s[8:11], s35 offen lds\n"
        "v_mfma_f32_16x16x32_bf16 v[120:123], a[132:135], a[224:227], v[120:123]\n"
        "v_cvt_pk_bf16_f32 v80, v96, v97\n"
        "v_cvt_pk_bf16_f32 v194, v116, v117\n"
        "v_mfma_f32_16x16x32_bf16 v[120:123], a[128:131], a[220:223], v[120:123]\n"
        "v_cvt_pk_bf16_f32 v195, v118, v119\n"
        "v_cvt_pk_bf16_f32 v81, v98, v99\n"
        "v_mfma_f32_16x16x32_bf16 v[120:123], a[124:127], a[216:219], v[120:123]\n"
        "v_cvt_pk_bf16_f32 v82, v100, v101\n"
        "v_mfma_f32_16x16x32_bf16 v[120:123], a[120:123], a[248:251], v[120:123]\n"
        "v_exp_f32 v103, v103\n"
        "v_mfma_f32_16x16x32_bf16 v[112:115], a[248:251], a[144:147], 0\n"
        "v_cvt_pk_bf16_f32 v83, v102, v103\n"
        "s_nop 0\n"
        "v_mfma_f32_16x16x32_bf16 v[112:115], a[216:219], a[148:151], v[112:115]\n"
        "s_nop 2\n"
        "v_mfma_f32_16x16x32_bf16 v[112:115], a[220:223], a[152:155], v[112:115]\n"
        "ds_read_b128 a[236:239], v240 offset:13824\n"
        "v_cvt_pk_bf16_f32 v208, v120, v121\n"
        "v_mfma_f32_16x16x32_bf16 v[112:115], a[224:227], a[156:159], v[112:115]\n"
        "ds_read_b128 a[240:243], v241 offset:13824\n"
        "v_cvt_pk_bf16_f32 v209, v122, v123\n"
        "v_mfma_f32_16x16x32_bf16 v[112:115], a[228:231], a[160:163], v[112:115]\n"
        "ds_read_b128 a[244:247], v240 offset:13952\n"
        "v_mfma_f32_16x16x32_bf16 v[112:115], a[232:235], a[164:167], v[112:115]\n"
        "s_nop 2\n"
        "v_mfma_f32_16x16x32_bf16 v[104:107], v[192:195], v[156:159], v[8:11]\n"
        "s_nop 1\n"
        "v_mfma_f32_16x16x32_bf16 v[104:107], v[208:211], v[160:163], v[104:107]\n"
        "s_add_u32 m0, s41, 14336\n"
        "v_cvt_pk_bf16_f32 v132, v112, v113\n"
        "v_cvt_pk_bf16_f32 v133, v114, v115\n"
        "v_mfma_f32_16x16x32_bf16 v[116:119], a[232:235], a[188:191], 0\n"
        "v_mfma_f32_16x16x32_bf16 v[116:119], a[228:231], a[184:187], v[116:119]\n"
        "buffer_load_dwordx4 v244, s[8:11], s35 offen lds\n"
        "v_mfma_f32_16x16x32_bf16 v[116:119], a[224:227], a[180:183], v[116:119]\n"
        "v_mfma_f32_16x16x32_bf16 v[116:119], a[220:223], a[176:179], v[116:119]\n"
        "v_exp_f32 v104, v104\n"
        "v_mfma_f32_16x16x32_bf16 v[116:119], a[216:219], a[172:175], v[116:119]\n"
        "v_exp_f32 v105, v105\n"
        "v_mfma_f32_16x16x32_bf16 v[116:119], a[248:251], a[168:171], v[116:119]\n"
        "v_exp_f32 v106, v106\n"
        "v_mfma_f32_16x16x32_bf16 v[108:111], v[192:195], v[176:179], v[24:27]\n"
        "s_waitcnt lgkmcnt(4)\n"
        "v_exp_f32 v107, v107\n"
        "v_mfma_f32_16x16x32_bf16 v[108:111], v[208:211], v[180:183], v[108:111]\n"
        "v_cvt_pk_bf16_f32 v68, v104, v105\n"
        "v_cvt_pk_bf16_f32 v69, v106, v107\n"
        "v_mfma_f32_16x16x32_bf16 v[120:123], a[248:251], a[192:195], 0\n"
        "ds_read_b128 a[248:251], v241 offset:13952\n"
        "v_cvt_pk_bf16_f32 v140, v116, v117\n"
        "v_mfma_f32_16x16x32_bf16 v[120:123], a[216:219], a[196:199], v[120:123]\n"
        "ds_read_b128 a[216:219], v240 offset:14080\n"
        "v_cvt_pk_bf16_f32 v141, v118, v119\n"
        "v_mfma_f32_16x16x32_bf16 v[120:123], a[220:223], a[200:203], v[120:123]\n"
        "ds_read_b128 a[220:223], v241 offset:14080\n"
        "v_mfma_f32_16x16x32_bf16 v[120:123], a[224:227], a[204:207], v[120:123]\n"
        "v_exp_f32 v108, v108\n"
        "v_mfma_f32_16x16x32_bf16 v[120:123], a[228:231], a[208:211], v[120:123]\n"
        "v_exp_f32 v109, v109\n"
        "v_mfma_f32_16x16x32_bf16 v[120:123], a[232:235], a[212:215], v[120:123]\n"
        "v_exp_f32 v110, v110\n"
        "v_mfma_f32_16x16x32_bf16 v[96:99], v[192:195], v[196:199], v[40:43]\n"
        "s_waitcnt lgkmcnt(6)\n"
        "v_exp_f32 v111, v111\n"
        "v_mfma_f32_16x16x32_bf16 v[96:99], v[208:211], v[200:203], v[96:99]\n"
        "s_waitcnt lgkmcnt(5)\n"
        "v_cvt_pk_bf16_f32 v76, v108, v109\n"
        "v_cvt_pk_bf16_f32 v77, v110, v111\n"
        "v_mfma_f32_16x16x32_bf16 v[112:115], a[72:75], a[236:239], 0\n"
        "s_waitcnt lgkmcnt(4)\n"
        "s_nop 0\n"
        "v_cvt_pk_bf16_f32 v148, v120, v121\n"
        "v_mfma_f32_16x16x32_bf16 v[112:115], a[76:79], a[240:243], v[112:115]\n"
        ".long 0xbec200ff\n"
        "45:\n"
        "s_branch 61b\n"
        "s_waitcnt lgkmcnt(3)\n"
        "v_cvt_pk_bf16_f32 v149, v122, v123\n"
        "v_mfma_f32_16x16x32_bf16 v[112:115], a[80:83], a[244:247], v[112:115]\n"
        "s_waitcnt lgkmcnt(2)\n"
        "v_exp_f32 v96, v96\n"
        "v_mfma_f32_16x16x32_bf16 v[112:115], a[84:87], a[248:251], v[112:115]\n"
        "s_waitcnt lgkmcnt(1)\n"
        "v_exp_f32 v97, v97\n"
        "v_mfma_f32_16x16x32_bf16 v[112:115], a[88:91], a[216:219], v[112:115]\n"
        "s_waitcnt lgkmcnt(0)\n"
        "v_exp_f32 v98, v98\n"
        "v_mfma_f32_16x16x32_bf16 v[112:115], a[92:95], a[220:223], v[112:115]\n"
        "v_exp_f32 v99, v99\n"
        "v_mfma_f32_16x16x32_bf16 v[116:119], a[116:119], a[220:223], 0\n"
        "v_cvt_pk_bf16_f32 v84, v96, v97\n"
        "v_cvt_pk_bf16_f32 v85, v98, v99\n"
        "v_mfma_f32_16x16x32_bf16 v[116:119], a[112:115], a[216:219], v[116:119]\n"
        "v_add_u32 v240, s31, v240\n"
        "v_add_u32 v241, s31, v241\n"
        "v_mfma_f32_16x16x32_bf16 v[116:119], a[108:111], a[248:251], v[116:119]\n"
        "v_cvt_pk_bf16_f32 v212, v112, v113\n"
        "v_cvt_pk_bf16_f32 v213, v114, v115\n"
        "v_mfma_f32_16x16x32_bf16 v[116:119], a[104:107], a[244:247], v[116:119]\n"
        "v_mfma_f32_16x16x32_bf16 v[116:119], a[100:103], a[240:243], v[116:119]\n"
        "v_mfma_f32_16x16x32_bf16 v[116:119], a[96:99], a[236:239], v[116:119]\n"
        "s_nop 2\n"
        "v_mfma_f32_16x16x32_bf16 v[120:123], a[120:123], a[236:239], 0\n"
        "s_nop 2\n"
        "v_mfma_f32_16x16x32_bf16 v[120:123], a[124:127], a[240:243], v[120:123]\n"
        "s_add_u32 s47, s47, 6291456\n"
        "v_cvt_pk_bf16_f32 v214, v116, v117\n"
        "v_cvt_pk_bf16_f32 v215, v118, v119\n"
        "v_mfma_f32_16x16x32_bf16 v[120:123], a[128:131], a[244:247], v[120:123]\n"
        "s_add_u32 s36, s36, 2097152\n"
        "v_mfma_f32_16x16x32_bf16 v[120:123], a[132:135], a[248:251], v[120:123]\n"
        "s_xor_b32 s48, s48, 8192\n"
        "v_mfma_f32_16x16x32_bf16 v[120:123], a[136:139], a[216:219], v[120:123]\n"
        "v_mfma_f32_16x16x32_bf16 v[120:123], a[140:143], a[220:223], v[120:123]\n"
        "s_nop 2\n"
        "v_mfma_f32_16x16x32_bf16 v[112:115], a[220:223], a[164:167], 0\n"
        "s_nop 2\n"
        "v_mfma_f32_16x16x32_bf16 v[112:115], a[216:219], a[160:163], v[112:115]\n"
        "v_cvt_pk_bf16_f32 v230, v120, v121\n"
        "v_cvt_pk_bf16_f32 v231, v122, v123\n"
        "v_mfma_f32_16x16x32_bf16 v[112:115], a[248:251], a[156:159], v[112:115]\n"
        "s_barrier\n"
        "v_mfma_f32_16x16x32_bf16 v[112:115], a[244:247], a[152:155], v[112:115]\n"
        "v_mfma_f32_16x16x32_bf16 v[112:115], a[240:243], a[148:151], v[112:115]\n"
        "v_mfma_f32_16x16x32_bf16 v[112:115], a[236:239], a[144:147], v[112:115]\n"
        "s_nop 2\n"
        "v_mfma_f32_16x16x32_bf16 v[100:103], v[212:215], v[156:159], v[12:15]\n"
        "s_nop 2\n"
        "v_mfma_f32_16x16x32_bf16 v[100:103], v[228:231], v[164:167], v[100:103]\n"
        "v_cvt_pk_bf16_f32 v134, v112, v113\n"
        "v_cvt_pk_bf16_f32 v135, v114, v115\n"
        "v_mfma_f32_16x16x32_bf16 v[116:119], a[236:239], a[168:171], 0\n"
        "s_sub_u32 s27, s27, 1\n"
        "v_mfma_f32_16x16x32_bf16 v[116:119], a[240:243], a[172:175], v[116:119]\n"
        "s_cmp_lg_u32 s27, 0\n"
        "v_mfma_f32_16x16x32_bf16 v[116:119], a[244:247], a[176:179], v[116:119]\n"
        "s_cbranch_scc0 19f\n"
        "v_mfma_f32_16x16x32_bf16 v[116:119], a[248:251], a[180:183], v[116:119]\n"
        "v_mfma_f32_16x16x32_bf16 v[116:119], a[216:219], a[184:187], v[116:119]\n"
        "s_nop 0\n"
        "v_exp_f32 v100, v100\n"
        "v_mfma_f32_16x16x32_bf16 v[116:119], a[220:223], a[188:191], v[116:119]\n"
        "v_exp_f32 v101, v101\n"
        "v_mfma_f32_16x16x32_bf16 v[104:107], v[212:215], v[176:179], v[28:31]\n"
        "v_exp_f32 v102, v102\n"
        "v_mfma_f32_16x16x32_bf16 v[104:107], v[228:231], v[184:187], v[104:107]\n"
        "v_exp_f32 v103, v103\n"
        "v_mfma_f32_16x16x32_bf16 v[120:123], a[220:223], a[212:215], 0\n"
        "v_cvt_pk_bf16_f32 v70, v100, v101\n"
        "v_cvt_pk_bf16_f32 v71, v102, v103\n"
        "v_mfma_f32_16x16x32_bf16 v[120:123], a[216:219], a[208:211], v[120:123]\n"
        "v_cvt_pk_bf16_f32 v142, v116, v117\n"
        "v_cvt_pk_bf16_f32 v143, v118, v119\n"
        "v_mfma_f32_16x16x32_bf16 v[120:123], a[248:251], a[204:207], v[120:123]\n"
        "v_mov_b64 v[96:97], 0\n"
        "v_mfma_f32_16x16x32_bf16 v[120:123], a[244:247], a[200:203], v[120:123]\n"
        "v_exp_f32 v104, v104\n"
        "v_mfma_f32_16x16x32_bf16 v[120:123], a[240:243], a[196:199], v[120:123]\n"
        "v_exp_f32 v105, v105\n"
        "v_mfma_f32_16x16x32_bf16 v[120:123], a[236:239], a[192:195], v[120:123]\n"
        "v_exp_f32 v106, v106\n"
        "v_mfma_f32_16x16x32_bf16 v[108:111], v[212:215], v[196:199], v[44:47]\n"
        "v_exp_f32 v107, v107\n"
        "v_mfma_f32_16x16x32_bf16 v[108:111], v[228:231], v[204:207], v[108:111]\n"
        "v_cvt_pk_bf16_f32 v78, v104, v105\n"
        "v_cvt_pk_bf16_f32 v79, v106, v107\n"
        "v_mfma_f32_16x16x32_bf16 v[112:115], a[0:3], a[236:239], 0\n"
        "s_nop 0\n"
        "v_cvt_pk_bf16_f32 v150, v120, v121\n"
        "v_cvt_pk_bf16_f32 v151, v122, v123\n"
        "v_mfma_f32_16x16x32_bf16 v[112:115], a[4:7], a[240:243], v[112:115]\n"
        "v_mfma_f32_16x16x32_bf16 v[112:115], a[8:11], a[244:247], v[112:115]\n"
        "v_exp_f32 v108, v108\n"
        "v_mfma_f32_16x16x32_bf16 v[112:115], a[12:15], a[248:251], v[112:115]\n"
        "v_exp_f32 v109, v109\n"
        "v_mfma_f32_16x16x32_bf16 v[112:115], a[16:19], a[216:219], v[112:115]\n"
        "v_exp_f32 v110, v110\n"
        "v_mfma_f32_16x16x32_bf16 v[112:115], a[20:23], a[220:223], v[112:115]\n"
        "v_exp_f32 v111, v111\n"
        "v_smfmac_f32_16x16x64_bf16 v[96:99], a[252:255], v[64:71], v237\n"
        "v_cvt_pk_bf16_f32 v86, v108, v109\n"
        "v_mfma_f32_16x16x32_bf16 v[100:103], v[132:135], v[68:71], 0\n"
        "v_mov_b64 v[124:125], 0\n"
        "v_cvt_pk_bf16_f32 v87, v110, v111\n"
        "v_mfma_f32_16x16x32_bf16 v[100:103], v[128:131], v[64:67], v[100:103]\n"
        "s_nop 0\n"
        "v_cvt_pk_bf16_f32 v216, v112, v113\n"
        "v_cvt_pk_bf16_f32 v217, v114, v115\n"
        "v_mfma_f32_16x16x32_bf16 v[104:107], v[136:139], v[64:67], 0\n"
        "ds_read_b128 v[222:225], v251 offset:4608\n"
        "v_add_f32 v97, v96, v97\n"
        "v_mfma_f32_16x16x32_bf16 v[104:107], v[140:143], v[68:71], v[104:107]\n"
        "v_rcp_f32 v96, v97\n"
        "v_mfma_f32_16x16x32_bf16 v[108:111], v[148:151], v[68:71], 0\n"
        "v_mul_f32 v100, v96, v100\n"
        "v_mul_f32 v101, v96, v101\n"
        "v_mfma_f32_16x16x32_bf16 v[108:111], v[144:147], v[64:67], v[108:111]\n"
        "v_mul_f32 v102, v96, v102\n"
        "v_mul_f32 v103, v96, v103\n"
        "v_mfma_f32_16x16x32_bf16 v[116:119], a[44:47], a[220:223], 0\n"
        "v_cvt_pk_bf16_f32 v100, v100, v101\n"
        "v_cvt_pk_bf16_f32 v101, v102, v103\n"
        "v_mfma_f32_16x16x32_bf16 v[116:119], a[40:43], a[216:219], v[116:119]\n"
        "v_mul_f32 v104, v96, v104\n"
        "v_mul_f32 v105, v96, v105\n"
        "v_mfma_f32_16x16x32_bf16 v[116:119], a[36:39], a[248:251], v[116:119]\n"
        "v_mul_f32 v106, v96, v106\n"
        "v_mul_f32 v107, v96, v107\n"
        "v_mfma_f32_16x16x32_bf16 v[116:119], a[32:35], a[244:247], v[116:119]\n"
        "v_cvt_pk_bf16_f32 v102, v104, v105\n"
        "v_cvt_pk_bf16_f32 v103, v106, v107\n"
        "v_mfma_f32_16x16x32_bf16 v[116:119], a[28:31], a[240:243], v[116:119]\n"
        "ds_write_b128 v245, v[100:103] offset:0\n"
        "v_mfma_f32_16x16x32_bf16 v[116:119], a[24:27], a[236:239], v[116:119]\n"
        "v_mul_f32 v108, v96, v108\n"
        "v_mul_f32 v109, v96, v109\n"
        "v_smfmac_f32_16x16x64_bf16 v[124:127], a[252:255], v[72:79], v237\n"
        "v_mul_f32 v110, v96, v110\n"
        "v_mfma_f32_16x16x32_bf16 v[64:67], v[132:135], v[76:79], 0\n"
        "v_mul_f32 v111, v96, v111\n"
        "v_cvt_pk_bf16_f32 v108, v108, v109\n"
        "v_mfma_f32_16x16x32_bf16 v[64:67], v[128:131], v[72:75], v[64:67]\n"
        "ds_read_b128 v[156:159], v247 offset:0\n"
        "v_cvt_pk_bf16_f32 v218, v116, v117\n"
        "v_mfma_f32_16x16x32_bf16 v[68:71], v[136:139], v[72:75], 0\n"
        "ds_read_b128 v[160:163], v247 offset:4096\n"
        "v_cvt_pk_bf16_f32 v219, v118, v119\n"
        "v_mfma_f32_16x16x32_bf16 v[68:71], v[140:143], v[76:79], v[68:71]\n"
        "ds_read_b128 v[164:167], v247 offset:4352\n"
        "v_cvt_pk_bf16_f32 v109, v110, v111\n"
        "v_mfma_f32_16x16x32_bf16 v[120:123], v[148:151], v[76:79], 0\n"
        "ds_write_b64 v246, v[108:109] offset:0\n"
        "v_mfma_f32_16x16x32_bf16 v[120:123], v[144:147], v[72:75], v[120:123]\n"
        "v_max_u32 v236, v96, v97\n"
        "v_mov_b64 v[116:117], 0\n"
        "v_mfma_f32_16x16x32_bf16 v[112:115], a[48:51], a[236:239], 0\n"
        "v_add_f32 v125, v124, v125\n"
        "v_mfma_f32_16x16x32_bf16 v[112:115], a[52:55], a[240:243], v[112:115]\n"
        "v_rcp_f32 v124, v125\n"
        "v_mfma_f32_16x16x32_bf16 v[112:115], a[56:59], a[244:247], v[112:115]\n"
        "v_mul_f32 v64, v124, v64\n"
        "v_mul_f32 v65, v124, v65\n"
        "v_mfma_f32_16x16x32_bf16 v[112:115], a[60:63], a[248:251], v[112:115]\n"
        "v_mul_f32 v66, v124, v66\n"
        "v_mul_f32 v67, v124, v67\n"
        "v_mfma_f32_16x16x32_bf16 v[112:115], a[64:67], a[216:219], v[112:115]\n"
        "v_cvt_pk_bf16_f32 v64, v64, v65\n"
        "v_cvt_pk_bf16_f32 v65, v66, v67\n"
        "v_mfma_f32_16x16x32_bf16 v[112:115], a[68:71], a[220:223], v[112:115]\n"
        "ds_read_b128 a[216:219], v248 offset:0\n"
        "v_mul_f32 v68, v124, v68\n"
        "v_smfmac_f32_16x16x64_bf16 v[116:119], a[252:255], v[80:87], v237\n"
        "ds_read_b128 a[220:223], v248 offset:3072\n"
        "v_mfma_f32_16x16x32_bf16 v[72:75], v[132:135], v[84:87], 0\n"
        "v_mul_f32 v69, v124, v69\n"
        "v_mul_f32 v70, v124, v70\n"
        "v_mfma_f32_16x16x32_bf16 v[72:75], v[128:131], v[80:83], v[72:75]\n"
        "v_cvt_pk_bf16_f32 v220, v112, v113\n"
        "v_cvt_pk_bf16_f32 v221, v114, v115\n"
        "v_mfma_f32_16x16x32_bf16 v[76:79], v[136:139], v[80:83], 0\n"
        "ds_read_b128 a[224:227], v248 offset:32\n"
        "v_cvt_pk_bf16_f32 v226, v112, v113\n"
        "v_mfma_f32_16x16x32_bf16 v[76:79], v[140:143], v[84:87], v[76:79]\n"
        "ds_read_b128 a[228:231], v248 offset:3104\n"
        "v_cvt_pk_bf16_f32 v227, v114, v115\n"
        "v_mfma_f32_16x16x32_bf16 v[96:99], v[148:151], v[84:87], 0\n"
        "v_mul_f32 v71, v124, v71\n"
        "v_cvt_pk_bf16_f32 v66, v68, v69\n"
        "v_mfma_f32_16x16x32_bf16 v[96:99], v[144:147], v[80:83], v[96:99]\n"
        "v_cvt_pk_bf16_f32 v67, v70, v71\n"
        "v_mul_f32 v120, v124, v120\n"
        "v_mfma_f32_16x16x32_bf16 v[100:103], v[152:155], v[216:219], v[48:51]\n"
        "s_waitcnt lgkmcnt(9)\n"
        "ds_read_b128 v[152:155], v247 offset:256\n"
        "v_mul_f32 v121, v124, v121\n"
        "v_mfma_f32_16x16x32_bf16 v[100:103], v[168:171], v[220:223], v[100:103]\n"
        "ds_write_b128 v245, v[64:67] offset:256\n"
        "v_mfma_f32_16x16x32_bf16 v[104:107], v[172:175], v[216:219], v[52:55]\n"
        "v_mul_f32 v122, v124, v122\n"
        "v_mul_f32 v123, v124, v123\n"
        "v_mfma_f32_16x16x32_bf16 v[104:107], v[188:191], v[224:227], v[104:107]\n"
        "s_waitcnt lgkmcnt(5)\n"
        "v_cvt_pk_bf16_f32 v120, v120, v121\n"
        "v_cvt_pk_bf16_f32 v121, v122, v123\n"
        "v_mfma_f32_16x16x32_bf16 v[176:179], v[156:159], a[216:219], 0\n"
        "s_waitcnt lgkmcnt(4)\n"
        "ds_write_b64 v246, v[120:121] offset:256\n"
        "v_mfma_f32_16x16x32_bf16 v[176:179], v[160:163], a[220:223], v[176:179]\n"
        "ds_read_b128 v[222:225], v252 offset:4608\n"
        "v_max3_u32 v236, v236, v124, v125\n"
        "v_mfma_f32_16x16x32_bf16 v[196:199], v[164:167], a[220:223], 0\n"
        "s_waitcnt lgkmcnt(3)\n"
        "ds_read_b128 a[232:235], v254 offset:0\n"
        "v_add_f32 v117, v116, v117\n"
        "v_mfma_f32_16x16x32_bf16 v[196:199], v[152:155], a[216:219], v[196:199]\n"
        "ds_read_b128 a[220:223], v254 offset:3072\n"
        "v_mfma_f32_16x16x32_bf16 v[200:203], v[152:155], a[224:227], 0\n"
        "ds_read_b128 a[216:219], v254 offset:32\n"
        "v_mfma_f32_16x16x32_bf16 v[200:203], v[164:167], a[228:231], v[200:203]\n"
        "v_rcp_f32 v116, v117\n"
        "v_mfma_f32_16x16x32_bf16 v[180:183], v[160:163], a[228:231], 0\n"
        "ds_read_b128 a[228:231], v254 offset:3104\n"
        "v_mul_f32 v72, v116, v72\n"
        "v_mfma_f32_16x16x32_bf16 v[180:183], v[156:159], a[224:227], v[180:183]\n"
        "ds_read_b128 a[236:239], v247 offset:8192\n"
        "v_mul_f32 v73, v116, v73\n"
        "v_mfma_f32_16x16x32_bf16 v[108:111], v[192:195], v[216:219], v[56:59]\n"
        "s_waitcnt lgkmcnt(5)\n"
        "ds_read_b128 a[224:227], v248 offset:16\n"
        "v_mul_f32 v74, v116, v74\n"
        "v_mfma_f32_16x16x32_bf16 v[108:111], v[208:211], v[220:223], v[108:111]\n"
        "ds_read_b128 a[240:243], v247 offset:12288\n"
        "v_mul_f32 v75, v116, v75\n"
        "v_mfma_f32_16x16x32_bf16 v[112:115], v[212:215], v[216:219], v[60:63]\n"
        "ds_read_b128 a[244:247], v247 offset:12544\n"
        "v_cvt_pk_bf16_f32 v72, v72, v73\n"
        "v_mfma_f32_16x16x32_bf16 v[112:115], v[228:231], v[224:227], v[112:115]\n"
        "s_waitcnt lgkmcnt(7)\n"
        "ds_read_b128 a[248:251], v247 offset:8448\n"
        "v_cvt_pk_bf16_f32 v73, v74, v75\n"
        "v_mfma_f32_16x16x32_bf16 v[184:187], v[156:159], a[232:235], 0\n"
        "s_waitcnt lgkmcnt(7)\n"
        "v_exp_f32 v100, v100\n"
        "v_mfma_f32_16x16x32_bf16 v[184:187], v[160:163], a[220:223], v[184:187]\n"
        "v_exp_f32 v101, v101\n"
        "v_mfma_f32_16x16x32_bf16 v[204:207], v[164:167], a[220:223], 0\n"
        "ds_read_b128 a[220:223], v248 offset:3088\n"
        "v_mul_f32 v76, v116, v76\n"
        "v_mfma_f32_16x16x32_bf16 v[204:207], v[152:155], a[232:235], v[204:207]\n"
        "s_waitcnt lgkmcnt(7)\n"
        "ds_read_b128 a[232:235], v254 offset:16\n"
        "v_mul_f32 v77, v116, v77\n"
        "v_mfma_f32_16x16x32_bf16 v[192:195], v[152:155], a[216:219], 0\n"
        "s_waitcnt lgkmcnt(7)\n"
        "v_mul_f32 v78, v116, v78\n"
        "v_mul_f32 v79, v116, v79\n"
        "v_mfma_f32_16x16x32_bf16 v[192:195], v[164:167], a[228:231], v[192:195]\n"
        "v_exp_f32 v102, v102\n"
        "v_mfma_f32_16x16x32_bf16 v[172:175], v[160:163], a[228:231], 0\n"
        "ds_read_b128 a[228:231], v254 offset:3088\n"
        "v_cvt_pk_bf16_f32 v88, v100, v101\n"
        "v_mfma_f32_16x16x32_bf16 v[172:175], v[156:159], a[216:219], v[172:175]\n"
        "s_waitcnt lgkmcnt(6)\n"
        "ds_read_b128 a[216:219], v248 offset:6144\n"
        "v_cvt_pk_bf16_f32 v74, v76, v77\n"
        "v_mfma_f32_16x16x32_bf16 v[212:215], v[156:159], a[224:227], 0\n"
        "s_waitcnt lgkmcnt(3)\n"
        "v_exp_f32 v104, v104\n"
        "v_mfma_f32_16x16x32_bf16 v[212:215], v[160:163], a[220:223], v[212:215]\n"
        "v_exp_f32 v103, v103\n"
        "v_mfma_f32_16x16x32_bf16 v[216:219], v[164:167], a[220:223], 0\n"
        "ds_read_b128 a[220:223], v248 offset:9216\n"
        "v_cvt_pk_bf16_f32 v75, v78, v79\n"
        "v_mfma_f32_16x16x32_bf16 v[216:219], v[152:155], a[224:227], v[216:219]\n"
        "s_waitcnt lgkmcnt(3)\n"
        "ds_write_b128 v245, v[72:75] offset:512\n"
        "v_mfma_f32_16x16x32_bf16 v[224:227], v[152:155], a[232:235], 0\n"
        "s_waitcnt lgkmcnt(3)\n"
        "v_exp_f32 v105, v105\n"
        "v_mfma_f32_16x16x32_bf16 v[224:227], v[164:167], a[228:231], v[224:227]\n"
        "v_cvt_pk_bf16_f32 v89, v102, v103\n"
        "v_cvt_pk_bf16_f32 v90, v104, v105\n"
        "v_mfma_f32_16x16x32_bf16 v[220:223], v[160:163], a[228:231], 0\n"
        "ds_read_b128 a[224:227], v248 offset:6176\n"
        "ds_read_b128 a[228:231], v248 offset:9248\n"
        "v_mfma_f32_16x16x32_bf16 v[220:223], v[156:159], a[232:235], v[220:223]\n"
        "s_waitcnt lgkmcnt(4)\n"
        "ds_read_b128 a[232:235], v254 offset:6144\n"
        "v_mul_f32 v96, v116, v96\n"
        "v_mfma_f32_16x16x32_bf16 v[176:179], a[236:239], a[216:219], v[176:179]\n"
        "s_waitcnt lgkmcnt(4)\n"
        "v_exp_f32 v106, v106\n"
        "v_mfma_f32_16x16x32_bf16 v[176:179], a[240:243], a[220:223], v[176:179]\n"
        "v_mul_f32 v97, v116, v97\n"
        "v_mul_f32 v98, v116, v98\n"
        "v_mfma_f32_16x16x32_bf16 v[196:199], a[244:247], a[220:223], v[196:199]\n"
        "ds_read_b128 a[220:223], v254 offset:9216\n"
        "v_mul_f32 v99, v116, v99\n"
        "v_mfma_f32_16x16x32_bf16 v[196:199], a[248:251], a[216:219], v[196:199]\n"
        "s_waitcnt lgkmcnt(3)\n"
        "ds_read_b128 a[216:219], v254 offset:6176\n"
        "v_cvt_pk_bf16_f32 v96, v96, v97\n"
        "v_mfma_f32_16x16x32_bf16 v[200:203], a[248:251], a[224:227], v[200:203]\n"
        "s_waitcnt lgkmcnt(3)\n"
        "ds_read_b128 v[156:159], v247 offset:16384\n"
        "v_cvt_pk_bf16_f32 v97, v98, v99\n"
        "v_mfma_f32_16x16x32_bf16 v[200:203], a[244:247], a[228:231], v[200:203]\n"
        "ds_write_b64 v246, v[96:97] offset:512\n"
        "v_mfma_f32_16x16x32_bf16 v[180:183], a[240:243], a[228:231], v[180:183]\n"
        "ds_read_b128 a[228:231], v254 offset:9248\n"
        "v_max3_u32 v236, v236, v116, v117\n"
        "v_mfma_f32_16x16x32_bf16 v[180:183], a[236:239], a[224:227], v[180:183]\n"
        "s_waitcnt lgkmcnt(5)\n"
        "ds_read_b128 a[224:227], v248 offset:6160\n"
        "v_mfma_f32_16x16x32_bf16 v[184:187], a[236:239], a[232:235], v[184:187]\n"
        "s_waitcnt lgkmcnt(5)\n"
        "ds_read_b128 v[160:163], v247 offset:20480\n"
        "v_mfma_f32_16x16x32_bf16 v[184:187], a[240:243], a[220:223], v[184:187]\n"
        "ds_read_b128 v[164:167], v247 offset:20736\n"
        "v_mfma_f32_16x16x32_bf16 v[204:207], a[244:247], a[220:223], v[204:207]\n"
        "ds_read_b128 a[220:223], v248 offset:9232\n"
        "v_mfma_f32_16x16x32_bf16 v[204:207], a[248:251], a[232:235], v[204:207]\n"
        "s_waitcnt lgkmcnt(7)\n"
        "ds_read_b128 a[232:235], v254 offset:6160\n"
        "v_mfma_f32_16x16x32_bf16 v[192:195], a[248:251], a[216:219], v[192:195]\n"
        "s_waitcnt lgkmcnt(5)\n"
        "ds_read_b128 v[152:155], v247 offset:16640\n"
        "v_mfma_f32_16x16x32_bf16 v[192:195], a[244:247], a[228:231], v[192:195]\n"
        "v_exp_f32 v107, v107\n"
        "v_mfma_f32_16x16x32_bf16 v[172:175], a[240:243], a[228:231], v[172:175]\n"
        "ds_read_b128 a[228:231], v254 offset:9232\n"
        "v_cvt_pk_bf16_f32 v91, v106, v107\n"
        "v_mfma_f32_16x16x32_bf16 v[172:175], a[236:239], a[216:219], v[172:175]\n"
        "s_waitcnt lgkmcnt(6)\n"
        "ds_read_b128 a[216:219], v248 offset:12288\n"
        "v_mfma_f32_16x16x32_bf16 v[212:215], a[236:239], a[224:227], v[212:215]\n"
        "s_waitcnt lgkmcnt(4)\n"
        "v_exp_f32 v108, v108\n"
        "v_mfma_f32_16x16x32_bf16 v[212:215], a[240:243], a[220:223], v[212:215]\n"
        "v_exp_f32 v109, v109\n"
        "v_mfma_f32_16x16x32_bf16 v[216:219], a[244:247], a[220:223], v[216:219]\n"
        "ds_read_b128 a[220:223], v248 offset:15360\n"
        "v_cvt_pk_bf16_f32 v92, v108, v109\n"
        "v_mfma_f32_16x16x32_bf16 v[216:219], a[248:251], a[224:227], v[216:219]\n"
        "s_waitcnt lgkmcnt(4)\n"
        "ds_read_b128 a[224:227], v248 offset:12320\n"
        "v_mfma_f32_16x16x32_bf16 v[224:227], a[248:251], a[232:235], v[224:227]\n"
        "s_waitcnt lgkmcnt(3)\n"
        "v_exp_f32 v110, v110\n"
        "v_mfma_f32_16x16x32_bf16 v[224:227], a[244:247], a[228:231], v[224:227]\n"
        "v_exp_f32 v112, v112\n"
        "v_mfma_f32_16x16x32_bf16 v[220:223], a[240:243], a[228:231], v[220:223]\n"
        "ds_read_b128 a[228:231], v248 offset:15392\n"
        "v_mfma_f32_16x16x32_bf16 v[220:223], a[236:239], a[232:235], v[220:223]\n"
        "s_waitcnt lgkmcnt(3)\n"
        "ds_read_b128 a[232:235], v254 offset:12288\n"
        "v_mfma_f32_16x16x32_bf16 v[176:179], v[156:159], a[216:219], v[176:179]\n"
        "s_waitcnt lgkmcnt(3)\n"
        "v_exp_f32 v111, v111\n"
        "v_mfma_f32_16x16x32_bf16 v[176:179], v[160:163], a[220:223], v[176:179]\n"
        "v_exp_f32 v113, v113\n"
        "v_mfma_f32_16x16x32_bf16 v[196:199], v[164:167], a[220:223], v[196:199]\n"
        "ds_read_b128 a[220:223], v254 offset:15360\n"
        "v_cvt_pk_bf16_f32 v93, v110, v111\n"
        "v_mfma_f32_16x16x32_bf16 v[196:199], v[152:155], a[216:219], v[196:199]\n"
        "s_waitcnt lgkmcnt(3)\n"
        "ds_read_b128 a[216:219], v254 offset:12320\n"
        "v_cvt_pk_bf16_f32 v94, v112, v113\n"
        "v_mfma_f32_16x16x32_bf16 v[200:203], v[152:155], a[224:227], v[200:203]\n"
        "s_waitcnt lgkmcnt(3)\n"
        "v_exp_f32 v114, v114\n"
        "v_mfma_f32_16x16x32_bf16 v[200:203], v[164:167], a[228:231], v[200:203]\n"
        "v_exp_f32 v115, v115\n"
        "v_mfma_f32_16x16x32_bf16 v[180:183], v[160:163], a[228:231], v[180:183]\n"
        "s_waitcnt vmcnt(0)\n"
        "s_barrier\n"
        "ds_read_b128 a[228:231], v254 offset:15392\n"
        "v_cvt_pk_bf16_f32 v95, v114, v115\n"
        "v_mfma_f32_16x16x32_bf16 v[180:183], v[156:159], a[224:227], v[180:183]\n"
        "s_waitcnt lgkmcnt(3)\n"
        "ds_read_b128 a[224:227], v248 offset:12304\n"
        "v_mfma_f32_16x16x32_bf16 v[184:187], v[156:159], a[232:235], v[184:187]\n"
        "s_waitcnt lgkmcnt(3)\n"
        "v_mfma_f32_16x16x32_bf16 v[184:187], v[160:163], a[220:223], v[184:187]\n"
        "v_mfma_f32_16x16x32_bf16 v[204:207], v[164:167], a[220:223], v[204:207]\n"
        "ds_read_b128 a[220:223], v248 offset:15376\n"
        "v_mfma_f32_16x16x32_bf16 v[204:207], v[152:155], a[232:235], v[204:207]\n"
        "s_waitcnt lgkmcnt(3)\n"
        "ds_read_b128 a[232:235], v254 offset:12304\n"
        "v_mfma_f32_16x16x32_bf16 v[192:195], v[152:155], a[216:219], v[192:195]\n"
        "s_waitcnt lgkmcnt(3)\n"
        "ds_read_b128 a[236:239], v240 offset:0\n"
        "v_mfma_f32_16x16x32_bf16 v[192:195], v[164:167], a[228:231], v[192:195]\n"
        "ds_read_b128 a[240:243], v241 offset:0\n"
        "v_mfma_f32_16x16x32_bf16 v[172:175], v[160:163], a[228:231], v[172:175]\n"
        "ds_read_b128 a[228:231], v254 offset:15376\n"
        "v_mfma_f32_16x16x32_bf16 v[172:175], v[156:159], a[216:219], v[172:175]\n"
        "s_waitcnt lgkmcnt(5)\n"
        "ds_read_b128 a[244:247], v240 offset:128\n"
        "v_mfma_f32_16x16x32_bf16 v[212:215], v[156:159], a[224:227], v[212:215]\n"
        "s_waitcnt lgkmcnt(5)\n"
        "ds_read_b128 a[248:251], v241 offset:128\n"
        "v_mfma_f32_16x16x32_bf16 v[212:215], v[160:163], a[220:223], v[212:215]\n"
        "ds_read_b128 a[216:219], v240 offset:256\n"
        "v_mfma_f32_16x16x32_bf16 v[216:219], v[164:167], a[220:223], v[216:219]\n"
        "ds_read_b128 a[220:223], v241 offset:256\n"
        "v_mfma_f32_16x16x32_bf16 v[216:219], v[152:155], a[224:227], v[216:219]\n"
        "s_waitcnt lgkmcnt(7)\n"
        "v_mfma_f32_16x16x32_bf16 v[224:227], v[152:155], a[232:235], v[224:227]\n"
        "s_waitcnt lgkmcnt(4)\n"
        "v_mfma_f32_16x16x32_bf16 v[224:227], v[164:167], a[228:231], v[224:227]\n"
        "v_mfma_f32_16x16x32_bf16 v[220:223], v[160:163], a[228:231], v[220:223]\n"
        "v_mfma_f32_16x16x32_bf16 v[220:223], v[156:159], a[232:235], v[220:223]\n"
        "s_branch 10b\n"
        ".p2align 12\n"
        "51:\n"
        "s_branch 61b\n"
        "s_nop 0\n"
        "s_nop 0\n"
        "s_nop 0\n"
        "12:\n"
        "10:\n"
        "v_mfma_f32_16x16x32_bf16 v[112:115], a[72:75], a[236:239], 0\n"
        "v_mov_b64 v[124:125], 0\n"
        "v_cvt_pk_bf16_f32 v232, v176, v180\n"
        "v_mfma_f32_16x16x32_bf16 v[112:115], a[76:79], a[240:243], v[112:115]\n"
        "s_waitcnt lgkmcnt(3)\n"
        "s_add_u32 s41, s30, s40\n"
        "v_cvt_pk_bf16_f32 v233, v184, v172\n"
        "v_cvt_pk_bf16_f32 v234, v177, v181\n"
        "v_mfma_f32_16x16x32_bf16 v[112:115], a[80:83], a[244:247], v[112:115]\n"
        "s_waitcnt lgkmcnt(2)\n"
        "buffer_store_dwordx2 v[232:233], v250, s[16:19], s38 offen offset:0 sc1\n"
        "v_cvt_pk_bf16_f32 v235, v185, v173\n"
        "v_mfma_f32_16x16x32_bf16 v[112:115], a[84:87], a[248:251], v[112:115]\n"
        "s_waitcnt lgkmcnt(1)\n"
        "v_cvt_pk_bf16_f32 v232, v178, v182\n"
        "v_cvt_pk_bf16_f32 v233, v186, v174\n"
        "v_mfma_f32_16x16x32_bf16 v[112:115], a[88:91], a[216:219], v[112:115]\n"
        "s_waitcnt lgkmcnt(0)\n"
        "v_mfma_f32_16x16x32_bf16 v[112:115], a[92:95], a[220:223], v[112:115]\n"
        "s_nop 2\n"
        "v_smfmac_f32_16x16x64_bf16 v[124:127], a[252:255], v[88:95], v237\n"
        "s_nop 1\n"
        "v_mfma_f32_16x16x32_bf16 v[80:83], v[132:135], v[92:95], 0\n"
        "buffer_store_dwordx2 v[234:235], v250, s[16:19], s38 offen offset:384 sc1\n"
        "v_cvt_pk_bf16_f32 v152, v112, v113\n"
        "v_mfma_f32_16x16x32_bf16 v[80:83], v[128:131], v[88:91], v[80:83]\n"
        "v_cvt_pk_bf16_f32 v153, v114, v115\n"
        "v_cvt_pk_bf16_f32 v234, v179, v183\n"
        "v_mfma_f32_16x16x32_bf16 v[84:87], v[136:139], v[88:91], 0\n"
        "v_cvt_pk_bf16_f32 v235, v187, v175\n"
        "v_add_f32 v125, v124, v125\n"
        "v_mfma_f32_16x16x32_bf16 v[84:87], v[140:143], v[92:95], v[84:87]\n"
        "v_rcp_f32 v124, v125\n"
        "v_mfma_f32_16x16x32_bf16 v[96:99], v[148:151], v[92:95], 0\n"
        "buffer_store_dwordx2 v[232:233], v250, s[16:19], s38 offen offset:768 sc1\n"
        "v_mul_f32 v80, v124, v80\n"
        "v_mfma_f32_16x16x32_bf16 v[96:99], v[144:147], v[88:91], v[96:99]\n"
        "s_add_u32 s35, s47, 786432\n"
        "v_mul_f32 v81, v124, v81\n"
        "v_mul_f32 v82, v124, v82\n"
        "v_mfma_f32_16x16x32_bf16 v[116:119], a[116:119], a[220:223], 0\n"
        "s_sub_u32 s46, s35, 100663296\n"
        "v_mul_f32 v83, v124, v83\n"
        "v_cvt_pk_bf16_f32 v80, v80, v81\n"
        "v_mfma_f32_16x16x32_bf16 v[116:119], a[112:115], a[216:219], v[116:119]\n"
        "s_cmp_lt_u32 s46, 786432\n"
        "v_cvt_pk_bf16_f32 v81, v82, v83\n"
        "v_cvt_pk_bf16_f32 v232, v196, v200\n"
        "v_mfma_f32_16x16x32_bf16 v[116:119], a[108:111], a[248:251], v[116:119]\n"
        "buffer_store_dwordx2 v[234:235], v250, s[16:19], s38 offen offset:1152 sc1\n"
        "v_cvt_pk_bf16_f32 v233, v204, v192\n"
        "v_mfma_f32_16x16x32_bf16 v[116:119], a[104:107], a[244:247], v[116:119]\n"
        "s_cselect_b32 s35, s46, s35\n"
        "v_mul_f32 v84, v124, v84\n"
        "v_mul_f32 v85, v124, v85\n"
        "v_mfma_f32_16x16x32_bf16 v[116:119], a[100:103], a[240:243], v[116:119]\n"
        "v_mul_f32 v86, v124, v86\n"
        "v_mul_f32 v87, v124, v87\n"
        "v_mfma_f32_16x16x32_bf16 v[116:119], a[96:99], a[236:239], v[116:119]\n"
        "v_cvt_pk_bf16_f32 v82, v84, v85\n"
        "v_cvt_pk_bf16_f32 v83, v86, v87\n"
        "v_mfma_f32_16x16x32_bf16 v[120:123], a[120:123], a[236:239], 0\n"
        "ds_write_b128 v245, v[80:83] offset:768\n"
        "v_mfma_f32_16x16x32_bf16 v[120:123], a[124:127], a[240:243], v[120:123]\n"
        "buffer_store_dwordx2 v[232:233], v250, s[16:19], s39 offen offset:0 sc1\n"
        "v_mul_f32 v96, v124, v96\n"
        "v_mfma_f32_16x16x32_bf16 v[120:123], a[128:131], a[244:247], v[120:123]\n"
        "v_mul_f32 v97, v124, v97\n"
        "v_mul_f32 v98, v124, v98\n"
        "v_mfma_f32_16x16x32_bf16 v[120:123], a[132:135], a[248:251], v[120:123]\n"
        "v_mul_f32 v99, v124, v99\n"
        "v_cvt_pk_bf16_f32 v234, v197, v201\n"
        "v_mfma_f32_16x16x32_bf16 v[120:123], a[136:139], a[216:219], v[120:123]\n"
        "v_cvt_pk_bf16_f32 v235, v205, v193\n"
        "v_cvt_pk_bf16_f32 v154, v116, v117\n"
        "v_mfma_f32_16x16x32_bf16 v[120:123], a[140:143], a[220:223], v[120:123]\n"
        "buffer_store_dwordx2 v[234:235], v250, s[16:19], s39 offen offset:384 sc1\n"
        "v_cvt_pk_bf16_f32 v155, v118, v119\n"
        "v_mfma_f32_16x16x32_bf16 v[112:115], a[20:23], a[220:223], 0\n"
        "v_cvt_pk_bf16_f32 v96, v96, v97\n"
        "v_cvt_pk_bf16_f32 v97, v98, v99\n"
        "v_mfma_f32_16x16x32_bf16 v[112:115], a[16:19], a[216:219], v[112:115]\n"
        "ds_write_b64 v246, v[96:97] offset:768\n"
        "v_mfma_f32_16x16x32_bf16 v[112:115], a[12:15], a[248:251], v[112:115]\n"
        "v_max3_u32 v236, v236, v124, v125\n"
        "v_cvt_pk_bf16_f32 v232, v198, v202\n"
        "v_mfma_f32_16x16x32_bf16 v[112:115], a[8:11], a[244:247], v[112:115]\n"
        "v_cvt_pk_bf16_f32 v233, v206, v194\n"
        "v_cmp_lt_u32 vcc, s24, v236\n"
        "v_mfma_f32_16x16x32_bf16 v[112:115], a[4:7], a[240:243], v[112:115]\n"
        "buffer_store_dwordx2 v[232:233], v250, s[16:19], s39 offen offset:768 sc1\n"
        "v_cvt_pk_bf16_f32 v168, v120, v121\n"
        "v_mfma_f32_16x16x32_bf16 v[112:115], a[0:3], a[236:239], v[112:115]\n"
        "s_cmp_lg_u64 vcc, 0\n"
        "s_addc_u32 s25, s25, s25\n"
        "v_cvt_pk_bf16_f32 v169, v122, v123\n"
        "v_mfma_f32_16x16x32_bf16 v[116:119], a[24:27], a[236:239], 0\n"
        "v_cvt_pk_bf16_f32 v234, v199, v203\n"
        "v_cvt_pk_bf16_f32 v235, v207, v195\n"
        "v_mfma_f32_16x16x32_bf16 v[116:119], a[28:31], a[240:243], v[116:119]\n"
        "v_cvt_pk_bf16_f32 v232, v212, v220\n"
        "v_cvt_pk_bf16_f32 v156, v112, v113\n"
        "v_mfma_f32_16x16x32_bf16 v[116:119], a[32:35], a[244:247], v[116:119]\n"
        "buffer_store_dwordx2 v[234:235], v250, s[16:19], s39 offen offset:1152 sc1\n"
        "v_cvt_pk_bf16_f32 v157, v114, v115\n"
        "v_mfma_f32_16x16x32_bf16 v[116:119], a[36:39], a[248:251], v[116:119]\n"
        "v_cvt_pk_bf16_f32 v234, v213, v221\n"
        "v_xor_b32 v251, 0x2000, v251\n"
        "v_mfma_f32_16x16x32_bf16 v[116:119], a[40:43], a[216:219], v[116:119]\n"
        "v_xor_b32 v252, 0x2000, v252\n"
        "v_mfma_f32_16x16x32_bf16 v[116:119], a[44:47], a[220:223], v[116:119]\n"
        "s_nop 2\n"
        "v_mfma_f32_16x16x32_bf16 v[120:123], a[68:71], a[220:223], 0\n"
        "s_nop 2\n"
        "v_mfma_f32_16x16x32_bf16 v[120:123], a[64:67], a[216:219], v[120:123]\n"
        "buffer_store_dword v232, v238, s[16:19], s38 offen offset:0 sc1\n"
        "v_mfma_f32_16x16x32_bf16 v[120:123], a[60:63], a[248:251], v[120:123]\n"
        "ds_read_b128 a[224:227], v240 offset:1536\n"
        "v_cvt_pk_bf16_f32 v158, v116, v117\n"
        "v_mfma_f32_16x16x32_bf16 v[120:123], a[56:59], a[244:247], v[120:123]\n"
        "ds_read_b128 a[228:231], v241 offset:1536\n"
        "v_cvt_pk_bf16_f32 v159, v118, v119\n"
        "v_mfma_f32_16x16x32_bf16 v[120:123], a[52:55], a[240:243], v[120:123]\n"
        "ds_read_b128 a[232:235], v240 offset:1664\n"
        "v_cvt_pk_bf16_f32 v232, v214, v222\n"
        "v_mfma_f32_16x16x32_bf16 v[120:123], a[48:51], a[236:239], v[120:123]\n"
        "buffer_store_dword v234, v238, s[16:19], s38 offen offset:384 sc1\n"
        "v_mfma_f32_16x16x32_bf16 v[112:115], a[236:239], a[144:147], 0\n"
        "v_add_u32 v247, s45, v247\n"
        "v_cvt_pk_bf16_f32 v234, v215, v223\n"
        "v_mfma_f32_16x16x32_bf16 v[112:115], a[240:243], a[148:151], v[112:115]\n"
        "s_nop 2\n"
        "v_mfma_f32_16x16x32_bf16 v[112:115], a[244:247], a[152:155], v[112:115]\n"
        "v_cvt_pk_bf16_f32 v160, v120, v121\n"
        "v_cvt_pk_bf16_f32 v161, v122, v123\n"
        "v_mfma_f32_16x16x32_bf16 v[112:115], a[248:251], a[156:159], v[112:115]\n"
        "v_cvt_pk_bf16_f32 v166, v120, v121\n"
        "v_cvt_pk_bf16_f32 v167, v122, v123\n"
        "v_mfma_f32_16x16x32_bf16 v[112:115], a[216:219], a[160:163], v[112:115]\n"
        "buffer_store_dword v232, v238, s[16:19], s38 offen offset:768 sc1\n"
        "v_mfma_f32_16x16x32_bf16 v[112:115], a[220:223], a[164:167], v[112:115]\n"
        "s_nop 2\n"
        "v_mfma_f32_16x16x32_bf16 v[116:119], a[220:223], a[188:191], 0\n"
        "v_cvt_pk_bf16_f32 v232, v216, v224\n"
        "s_nop 0\n"
        "v_mfma_f32_16x16x32_bf16 v[116:119], a[216:219], a[184:187], v[116:119]\n"
        "s_nop 0\n"
        "v_cvt_pk_bf16_f32 v128, v112, v113\n"
        "v_cvt_pk_bf16_f32 v129, v114, v115\n"
        "v_mfma_f32_16x16x32_bf16 v[116:119], a[248:251], a[180:183], v[116:119]\n"
        "buffer_store_dword v234, v238, s[16:19], s38 offen offset:1152 sc1\n"
        "v_mfma_f32_16x16x32_bf16 v[116:119], a[244:247], a[176:179], v[116:119]\n"
        "s_nop 0\n"
        "v_cvt_pk_bf16_f32 v234, v217, v225\n"
        "v_mfma_f32_16x16x32_bf16 v[116:119], a[240:243], a[172:175], v[116:119]\n"
        "v_mfma_f32_16x16x32_bf16 v[116:119], a[236:239], a[168:171], v[116:119]\n"
        "s_nop 2\n"
        "v_mfma_f32_16x16x32_bf16 v[120:123], a[236:239], a[192:195], 0\n"
        "ds_read_b128 a[236:239], v241 offset:1664\n"
        "s_nop 0\n"
        "v_mfma_f32_16x16x32_bf16 v[120:123], a[240:243], a[196:199], v[120:123]\n"
        "buffer_store_dword v232, v238, s[16:19], s39 offen offset:0 sc1\n"
        "v_mfma_f32_16x16x32_bf16 v[120:123], a[244:247], a[200:203], v[120:123]\n"
        "ds_read_b128 a[240:243], v240 offset:1792\n"
        "ds_read_b128 a[244:247], v241 offset:1792\n"
        "v_mfma_f32_16x16x32_bf16 v[120:123], a[248:251], a[204:207], v[120:123]\n"
        "v_cvt_pk_bf16_f32 v136, v116, v117\n"
        "v_cvt_pk_bf16_f32 v137, v118, v119\n"
        "v_mfma_f32_16x16x32_bf16 v[120:123], a[216:219], a[208:211], v[120:123]\n"
        "v_cvt_pk_bf16_f32 v232, v218, v226\n"
        "v_mfma_f32_16x16x32_bf16 v[120:123], a[220:223], a[212:215], v[120:123]\n"
        "s_waitcnt lgkmcnt(5)\n"
        "buffer_store_dword v234, v238, s[16:19], s39 offen offset:384 sc1\n"
        "v_mfma_f32_16x16x32_bf16 v[112:115], a[72:75], a[224:227], 0\n"
        "s_waitcnt lgkmcnt(4)\n"
        "s_nop 1\n"
        "v_mfma_f32_16x16x32_bf16 v[112:115], a[76:79], a[228:231], v[112:115]\n"
        "s_waitcnt lgkmcnt(3)\n"
        "v_cvt_pk_bf16_f32 v234, v219, v227\n"
        "s_nop 0\n"
        "v_mfma_f32_16x16x32_bf16 v[112:115], a[80:83], a[232:235], v[112:115]\n"
        "s_waitcnt lgkmcnt(2)\n"
        "v_cvt_pk_bf16_f32 v144, v120, v121\n"
        "v_cvt_pk_bf16_f32 v145, v122, v123\n"
        "v_mfma_f32_16x16x32_bf16 v[112:115], a[84:87], a[236:239], v[112:115]\n"
        "s_waitcnt lgkmcnt(1)\n"
        "v_mfma_f32_16x16x32_bf16 v[112:115], a[88:91], a[240:243], v[112:115]\n"
        "s_waitcnt lgkmcnt(0)\n"
        "buffer_store_dword v232, v238, s[16:19], s39 offen offset:768 sc1\n"
        "v_mfma_f32_16x16x32_bf16 v[112:115], a[92:95], a[244:247], v[112:115]\n"
        "s_nop 2\n"
        "v_mfma_f32_16x16x32_bf16 v[116:119], a[116:119], a[244:247], 0\n"
        "s_nop 2\n"
        "v_mfma_f32_16x16x32_bf16 v[116:119], a[112:115], a[240:243], v[116:119]\n"
        "v_cvt_pk_bf16_f32 v172, v112, v113\n"
        "v_cvt_pk_bf16_f32 v173, v114, v115\n"
        "v_mfma_f32_16x16x32_bf16 v[116:119], a[108:111], a[236:239], v[116:119]\n"
        "buffer_store_dword v234, v238, s[16:19], s39 offen offset:1152 sc1\n"
        "v_mfma_f32_16x16x32_bf16 v[116:119], a[104:107], a[232:235], v[116:119]\n"
        "v_mfma_f32_16x16x32_bf16 v[116:119], a[100:103], a[228:231], v[116:119]\n"
        "v_mfma_f32_16x16x32_bf16 v[116:119], a[96:99], a[224:227], v[116:119]\n"
        "s_mov_b32 m0, s48\n"
        "s_nop 1\n"
        "v_mfma_f32_16x16x32_bf16 v[120:123], a[120:123], a[224:227], 0\n"
        "ds_read_b128 v[162:165], v251 offset:0\n"
        "s_nop 0\n"
        "v_mfma_f32_16x16x32_bf16 v[120:123], a[124:127], a[228:231], v[120:123]\n"
        "buffer_load_dwordx4 v249, s[12:15], s36 offen offset:0 lds\n"
        "v_mfma_f32_16x16x32_bf16 v[120:123], a[128:131], a[232:235], v[120:123]\n"
        "v_cvt_pk_bf16_f32 v174, v116, v117\n"
        "v_cvt_pk_bf16_f32 v175, v118, v119\n"
        "v_mfma_f32_16x16x32_bf16 v[120:123], a[132:135], a[236:239], v[120:123]\n"
        "v_mfma_f32_16x16x32_bf16 v[120:123], a[136:139], a[240:243], v[120:123]\n"
        "v_mfma_f32_16x16x32_bf16 v[120:123], a[140:143], a[244:247], v[120:123]\n"
        "s_nop 2\n"
        "v_mfma_f32_16x16x32_bf16 v[112:115], a[20:23], a[244:247], 0\n"
        "s_nop 2\n"
        "v_mfma_f32_16x16x32_bf16 v[112:115], a[16:19], a[240:243], v[112:115]\n"
        "v_cvt_pk_bf16_f32 v190, v120, v121\n"
        "v_cvt_pk_bf16_f32 v191, v122, v123\n"
        "v_mfma_f32_16x16x32_bf16 v[112:115], a[12:15], a[236:239], v[112:115]\n"
        "v_mfma_f32_16x16x32_bf16 v[112:115], a[8:11], a[232:235], v[112:115]\n"
        "v_mfma_f32_16x16x32_bf16 v[112:115], a[4:7], a[228:231], v[112:115]\n"
        "ds_read_b128 v[182:185], v251 offset:512\n"
        "v_mfma_f32_16x16x32_bf16 v[112:115], a[0:3], a[224:227], v[112:115]\n"
        "s_nop 2\n"
        "v_mfma_f32_16x16x32_bf16 v[116:119], a[24:27], a[224:227], 0\n"
        "s_nop 2\n"
        "v_mfma_f32_16x16x32_bf16 v[116:119], a[28:31], a[228:231], v[116:119]\n"
        "v_cvt_pk_bf16_f32 v176, v112, v113\n"
        "v_cvt_pk_bf16_f32 v177, v114, v115\n"
        "v_mfma_f32_16x16x32_bf16 v[116:119], a[32:35], a[232:235], v[116:119]\n"
        "v_mfma_f32_16x16x32_bf16 v[116:119], a[36:39], a[236:239], v[116:119]\n"
        "buffer_load_dwordx4 v253, s[12:15], s36 offen offset:1024 lds\n"
        "v_mfma_f32_16x16x32_bf16 v[116:119], a[40:43], a[240:243], v[116:119]\n"
        "v_mfma_f32_16x16x32_bf16 v[116:119], a[44:47], a[244:247], v[116:119]\n"
        "s_nop 2\n"
        "v_mfma_f32_16x16x32_bf16 v[120:123], a[68:71], a[244:247], 0\n"
        "s_nop 2\n"
        "v_mfma_f32_16x16x32_bf16 v[120:123], a[64:67], a[240:243], v[120:123]\n"
        "v_cvt_pk_bf16_f32 v178, v116, v117\n"
        "v_cvt_pk_bf16_f32 v179, v118, v119\n"
        "v_mfma_f32_16x16x32_bf16 v[120:123], a[60:63], a[236:239], v[120:123]\n"
        "v_mfma_f32_16x16x32_bf16 v[120:123], a[56:59], a[232:235], v[120:123]\n"
        "v_mfma_f32_16x16x32_bf16 v[120:123], a[52:55], a[228:231], v[120:123]\n"
        "v_mfma_f32_16x16x32_bf16 v[120:123], a[48:51], a[224:227], v[120:123]\n"
        "s_nop 2\n"
        "v_mfma_f32_16x16x32_bf16 v[112:115], a[224:227], a[144:147], 0\n"
        "s_nop 2\n"
        "v_mfma_f32_16x16x32_bf16 v[112:115], a[228:231], a[148:151], v[112:115]\n"
        "v_cvt_pk_bf16_f32 v180, v120, v121\n"
        "v_cvt_pk_bf16_f32 v181, v122, v123\n"
        "v_mfma_f32_16x16x32_bf16 v[112:115], a[232:235], a[152:155], v[112:115]\n"
        "v_cvt_pk_bf16_f32 v186, v120, v121\n"
        "v_cvt_pk_bf16_f32 v187, v122, v123\n"
        "v_mfma_f32_16x16x32_bf16 v[112:115], a[236:239], a[156:159], v[112:115]\n"
        "s_add_u32 m0, s41, 0\n"
        "v_mfma_f32_16x16x32_bf16 v[112:115], a[240:243], a[160:163], v[112:115]\n"
        "ds_read_b128 a[248:251], v240 offset:12288\n"
        "v_mfma_f32_16x16x32_bf16 v[112:115], a[244:247], a[164:167], v[112:115]\n"
        "buffer_load_dwordx4 v242, s[8:11], s47 offen lds\n"
        "v_mfma_f32_16x16x32_bf16 v[96:99], v[152:155], v[156:159], v[0:3]\n"
        "s_waitcnt lgkmcnt(2)\n"
        "ds_read_b128 a[216:219], v241 offset:12288\n"
        "s_nop 0\n"
        "v_mfma_f32_16x16x32_bf16 v[96:99], v[168:171], v[160:163], v[96:99]\n"
        "ds_read_b128 a[220:223], v240 offset:12416\n"
        "s_sub_u32 s31, s30, s28\n"
        "s_nop 0\n"
        "v_mfma_f32_16x16x32_bf16 v[100:103], v[172:175], v[156:159], v[4:7]\n"
        "s_sub_u32 s45, s28, s29\n"
        "v_cvt_pk_bf16_f32 v130, v112, v113\n"
        "v_cvt_pk_bf16_f32 v131, v114, v115\n"
        "v_mfma_f32_16x16x32_bf16 v[100:103], v[188:191], v[164:167], v[100:103]\n"
        "s_add_u32 s28, s28, s31\n"
        "ds_read_b128 v[162:165], v252 offset:0\n"
        "v_mfma_f32_16x16x32_bf16 v[116:119], a[244:247], a[188:191], 0\n"
        "s_mov_b32 s30, s29\n"
        "v_exp_f32 v96, v96\n"
        "v_mfma_f32_16x16x32_bf16 v[116:119], a[240:243], a[184:187], v[116:119]\n"
        "s_add_u32 s29, s29, s45\n"
        "v_exp_f32 v97, v97\n"
        "v_mfma_f32_16x16x32_bf16 v[116:119], a[236:239], a[180:183], v[116:119]\n"
        "v_exp_f32 v98, v98\n"
        "v_mfma_f32_16x16x32_bf16 v[116:119], a[232:235], a[176:179], v[116:119]\n"
        "v_exp_f32 v100, v100\n"
        "v_mfma_f32_16x16x32_bf16 v[116:119], a[228:231], a[172:175], v[116:119]\n"
        "v_exp_f32 v99, v99\n"
        "v_mfma_f32_16x16x32_bf16 v[116:119], a[224:227], a[168:171], v[116:119]\n"
        "v_exp_f32 v101, v101\n"
        "v_mfma_f32_16x16x32_bf16 v[104:107], v[152:155], v[176:179], v[16:19]\n"
        "s_waitcnt lgkmcnt(4)\n"
        "v_exp_f32 v102, v102\n"
        "v_mfma_f32_16x16x32_bf16 v[104:107], v[168:171], v[180:183], v[104:107]\n"
        "s_add_u32 m0, s41, 1024\n"
        "v_cvt_pk_bf16_f32 v64, v96, v97\n"
        "v_cvt_pk_bf16_f32 v65, v98, v99\n"
        "v_mfma_f32_16x16x32_bf16 v[108:111], v[172:175], v[176:179], v[20:23]\n"
        "v_cvt_pk_bf16_f32 v138, v116, v117\n"
        "v_cvt_pk_bf16_f32 v139, v118, v119\n"
        "v_mfma_f32_16x16x32_bf16 v[108:111], v[188:191], v[184:187], v[108:111]\n"
        "buffer_load_dwordx4 v243, s[8:11], s47 offen lds\n"
        "v_mfma_f32_16x16x32_bf16 v[120:123], a[224:227], a[192:195], 0\n"
        "ds_read_b128 a[224:227], v241 offset:12416\n"
        "v_cvt_pk_bf16_f32 v66, v100, v101\n"
        "v_mfma_f32_16x16x32_bf16 v[120:123], a[228:231], a[196:199], v[120:123]\n"
        "ds_read_b128 a[228:231], v240 offset:12544\n"
        "v_mfma_f32_16x16x32_bf16 v[120:123], a[232:235], a[200:203], v[120:123]\n"
        "ds_read_b128 a[232:235], v241 offset:12544\n"
        "v_mfma_f32_16x16x32_bf16 v[120:123], a[236:239], a[204:207], v[120:123]\n"
        "ds_read_b128 v[202:205], v251 offset:4096\n"
        "v_mfma_f32_16x16x32_bf16 v[120:123], a[240:243], a[208:211], v[120:123]\n"
        "ds_read_b128 v[182:185], v252 offset:512\n"
        "v_mfma_f32_16x16x32_bf16 v[120:123], a[244:247], a[212:215], v[120:123]\n"
        "s_waitcnt lgkmcnt(8)\n"
        "v_exp_f32 v103, v103\n"
        "v_mfma_f32_16x16x32_bf16 v[112:115], a[0:3], a[248:251], 0\n"
        "s_waitcnt lgkmcnt(7)\n"
        "v_cvt_pk_bf16_f32 v67, v102, v103\n"
        "s_nop 0\n"
        "v_mfma_f32_16x16x32_bf16 v[112:115], a[4:7], a[216:219], v[112:115]\n"
        "s_waitcnt lgkmcnt(6)\n"
        "v_exp_f32 v104, v104\n"
        "v_mfma_f32_16x16x32_bf16 v[112:115], a[8:11], a[220:223], v[112:115]\n"
        "s_waitcnt lgkmcnt(4)\n"
        "v_exp_f32 v105, v105\n"
        "v_mfma_f32_16x16x32_bf16 v[112:115], a[12:15], a[224:227], v[112:115]\n"
        "s_waitcnt lgkmcnt(3)\n"
        "v_exp_f32 v106, v106\n"
        "v_mfma_f32_16x16x32_bf16 v[112:115], a[16:19], a[228:231], v[112:115]\n"
        "s_waitcnt lgkmcnt(2)\n"
        "v_cvt_pk_bf16_f32 v72, v104, v105\n"
        "v_cvt_pk_bf16_f32 v146, v120, v121\n"
        "v_mfma_f32_16x16x32_bf16 v[112:115], a[20:23], a[232:235], v[112:115]\n"
        "s_add_u32 m0, s41, 2048\n"
        "v_exp_f32 v108, v108\n"
        "v_mfma_f32_16x16x32_bf16 v[116:119], a[44:47], a[232:235], 0\n"
        "v_cvt_pk_bf16_f32 v147, v122, v123\n"
        "v_add_u32 v245, s45, v245\n"
        "v_mfma_f32_16x16x32_bf16 v[116:119], a[40:43], a[228:231], v[116:119]\n"
        "buffer_load_dwordx4 v244, s[8:11], s47 offen lds\n"
        "v_mfma_f32_16x16x32_bf16 v[116:119], a[36:39], a[224:227], v[116:119]\n"
        "v_exp_f32 v107, v107\n"
        "v_mfma_f32_16x16x32_bf16 v[116:119], a[32:35], a[220:223], v[116:119]\n"
        "v_exp_f32 v109, v109\n"
        "v_mfma_f32_16x16x32_bf16 v[116:119], a[28:31], a[216:219], v[116:119]\n"
        "v_cvt_pk_bf16_f32 v73, v106, v107\n"
        "v_cvt_pk_bf16_f32 v74, v108, v109\n"
        "v_mfma_f32_16x16x32_bf16 v[116:119], a[24:27], a[248:251], v[116:119]\n"
        "v_exp_f32 v110, v110\n"
        "v_mfma_f32_16x16x32_bf16 v[120:123], a[48:51], a[248:251], 0\n"
        "v_exp_f32 v111, v111\n"
        "v_mfma_f32_16x16x32_bf16 v[120:123], a[52:55], a[216:219], v[120:123]\n"
        "v_cvt_pk_bf16_f32 v75, v110, v111\n"
        "v_cvt_pk_bf16_f32 v196, v112, v113\n"
        "v_mfma_f32_16x16x32_bf16 v[120:123], a[56:59], a[220:223], v[120:123]\n"
        "v_cvt_pk_bf16_f32 v197, v114, v115\n"
        "v_cvt_pk_bf16_f32 v198, v116, v117\n"
        "v_mfma_f32_16x16x32_bf16 v[120:123], a[60:63], a[224:227], v[120:123]\n"
        "v_cvt_pk_bf16_f32 v199, v118, v119\n"
        "v_add_u32 v246, s45, v246\n"
        "v_mfma_f32_16x16x32_bf16 v[120:123], a[64:67], a[228:231], v[120:123]\n"
        "v_mfma_f32_16x16x32_bf16 v[120:123], a[68:71], a[232:235], v[120:123]\n"
        "s_nop 2\n"
        "v_mfma_f32_16x16x32_bf16 v[112:115], a[92:95], a[232:235], 0\n"
        "s_nop 2\n"
        "v_mfma_f32_16x16x32_bf16 v[112:115], a[88:91], a[228:231], v[112:115]\n"
        "s_add_u32 m0, s41, 12288\n"
        "v_cvt_pk_bf16_f32 v200, v120, v121\n"
        "v_cvt_pk_bf16_f32 v201, v122, v123\n"
        "v_mfma_f32_16x16x32_bf16 v[112:115], a[84:87], a[224:227], v[112:115]\n"
        "v_cvt_pk_bf16_f32 v206, v120, v121\n"
        "v_cvt_pk_bf16_f32 v207, v122, v123\n"
        "v_mfma_f32_16x16x32_bf16 v[112:115], a[80:83], a[220:223], v[112:115]\n"
        "buffer_load_dwordx4 v242, s[8:11], s35 offen lds\n"
        "v_mfma_f32_16x16x32_bf16 v[112:115], a[76:79], a[216:219], v[112:115]\n"
        "v_mfma_f32_16x16x32_bf16 v[112:115], a[72:75], a[248:251], v[112:115]\n"
        "s_nop 2\n"
        "v_mfma_f32_16x16x32_bf16 v[96:99], v[152:155], v[196:199], v[32:35]\n"
        "s_waitcnt lgkmcnt(1)\n"
        "s_nop 1\n"
        "v_mfma_f32_16x16x32_bf16 v[96:99], v[168:171], v[200:203], v[96:99]\n"
        "s_nop 0\n"
        "v_cvt_pk_bf16_f32 v192, v112, v113\n"
        "v_cvt_pk_bf16_f32 v193, v114, v115\n"
        "v_mfma_f32_16x16x32_bf16 v[100:103], v[172:175], v[196:199], v[36:39]\n"
        "v_mfma_f32_16x16x32_bf16 v[100:103], v[188:191], v[204:207], v[100:103]\n"
        "ds_read_b128 v[202:205], v252 offset:4096\n"
        "s_cmp_eq_u32 s37, 100663296\n"
        "v_mfma_f32_16x16x32_bf16 v[116:119], a[96:99], a[248:251], 0\n"
        "s_cselect_b32 s46, 0, s37\n"
        "v_exp_f32 v96, v96\n"
        "v_mfma_f32_16x16x32_bf16 v[116:119], a[100:103], a[216:219], v[116:119]\n"
        "s_add_u32 s38, s46, s43\n"
        "v_exp_f32 v97, v97\n"
        "v_mfma_f32_16x16x32_bf16 v[116:119], a[104:107], a[220:223], v[116:119]\n"
        "s_add_u32 s39, s46, s44\n"
        "v_exp_f32 v98, v98\n"
        "v_mfma_f32_16x16x32_bf16 v[116:119], a[108:111], a[224:227], v[116:119]\n"
        "s_add_u32 s37, s37, 6291456\n"
        "v_exp_f32 v100, v100\n"
        "v_mfma_f32_16x16x32_bf16 v[116:119], a[112:115], a[228:231], v[116:119]\n"
        "v_exp_f32 v99, v99\n"
        "v_mfma_f32_16x16x32_bf16 v[116:119], a[116:119], a[232:235], v[116:119]\n"
        "s_add_u32 m0, s41, 13312\n"
        "v_exp_f32 v101, v101\n"
        "v_mfma_f32_16x16x32_bf16 v[120:123], a[140:143], a[232:235], 0\n"
        "v_exp_f32 v102, v102\n"
        "v_mfma_f32_16x16x32_bf16 v[120:123], a[136:139], a[228:231], v[120:123]\n"
        "buffer_load_dwordx4 v243, s[8:11], s35 offen lds\n"
        "v_mfma_f32_16x16x32_bf16 v[120:123], a[132:135], a[224:227], v[120:123]\n"
        "v_cvt_pk_bf16_f32 v80, v96, v97\n"
        "v_cvt_pk_bf16_f32 v194, v116, v117\n"
        "v_mfma_f32_16x16x32_bf16 v[120:123], a[128:131], a[220:223], v[120:123]\n"
        "v_cvt_pk_bf16_f32 v195, v118, v119\n"
        "v_cvt_pk_bf16_f32 v81, v98, v99\n"
        "v_mfma_f32_16x16x32_bf16 v[120:123], a[124:127], a[216:219], v[120:123]\n"
        "v_cvt_pk_bf16_f32 v82, v100, v101\n"
        "v_mfma_f32_16x16x32_bf16 v[120:123], a[120:123], a[248:251], v[120:123]\n"
        "v_exp_f32 v103, v103\n"
        "v_mfma_f32_16x16x32_bf16 v[112:115], a[248:251], a[144:147], 0\n"
        "v_cvt_pk_bf16_f32 v83, v102, v103\n"
        "s_nop 0\n"
        "v_mfma_f32_16x16x32_bf16 v[112:115], a[216:219], a[148:151], v[112:115]\n"
        "s_nop 2\n"
        "v_mfma_f32_16x16x32_bf16 v[112:115], a[220:223], a[152:155], v[112:115]\n"
        "ds_read_b128 a[236:239], v240 offset:13824\n"
        "v_cvt_pk_bf16_f32 v208, v120, v121\n"
        "v_mfma_f32_16x16x32_bf16 v[112:115], a[224:227], a[156:159], v[112:115]\n"
        "ds_read_b128 a[240:243], v241 offset:13824\n"
        "v_cvt_pk_bf16_f32 v209, v122, v123\n"
        "v_mfma_f32_16x16x32_bf16 v[112:115], a[228:231], a[160:163], v[112:115]\n"
        "ds_read_b128 a[244:247], v240 offset:13952\n"
        "v_mfma_f32_16x16x32_bf16 v[112:115], a[232:235], a[164:167], v[112:115]\n"
        "s_nop 2\n"
        "v_mfma_f32_16x16x32_bf16 v[104:107], v[192:195], v[156:159], v[8:11]\n"
        "s_nop 1\n"
        "v_mfma_f32_16x16x32_bf16 v[104:107], v[208:211], v[160:163], v[104:107]\n"
        "s_add_u32 m0, s41, 14336\n"
        "v_cvt_pk_bf16_f32 v132, v112, v113\n"
        "v_cvt_pk_bf16_f32 v133, v114, v115\n"
        "v_mfma_f32_16x16x32_bf16 v[116:119], a[232:235], a[188:191], 0\n"
        "v_mfma_f32_16x16x32_bf16 v[116:119], a[228:231], a[184:187], v[116:119]\n"
        "buffer_load_dwordx4 v244, s[8:11], s35 offen lds\n"
        "v_mfma_f32_16x16x32_bf16 v[116:119], a[224:227], a[180:183], v[116:119]\n"
        "v_mfma_f32_16x16x32_bf16 v[116:119], a[220:223], a[176:179], v[116:119]\n"
        "v_exp_f32 v104, v104\n"
        "v_mfma_f32_16x16x32_bf16 v[116:119], a[216:219], a[172:175], v[116:119]\n"
        "v_exp_f32 v105, v105\n"
        "v_mfma_f32_16x16x32_bf16 v[116:119], a[248:251], a[168:171], v[116:119]\n"
        "v_exp_f32 v106, v106\n"
        "v_mfma_f32_16x16x32_bf16 v[108:111], v[192:195], v[176:179], v[24:27]\n"
        "s_waitcnt lgkmcnt(4)\n"
        "v_exp_f32 v107, v107\n"
        "v_mfma_f32_16x16x32_bf16 v[108:111], v[208:211], v[180:183], v[108:111]\n"
        "v_cvt_pk_bf16_f32 v68, v104, v105\n"
        "v_cvt_pk_bf16_f32 v69, v106, v107\n"
        "v_mfma_f32_16x16x32_bf16 v[120:123], a[248:251], a[192:195], 0\n"
        "ds_read_b128 a[248:251], v241 offset:13952\n"
        "v_cvt_pk_bf16_f32 v140, v116, v117\n"
        "v_mfma_f32_16x16x32_bf16 v[120:123], a[216:219], a[196:199], v[120:123]\n"
        "ds_read_b128 a[216:219], v240 offset:14080\n"
        "v_cvt_pk_bf16_f32 v141, v118, v119\n"
        "v_mfma_f32_16x16x32_bf16 v[120:123], a[220:223], a[200:203], v[120:123]\n"
        "ds_read_b128 a[220:223], v241 offset:14080\n"
        "v_mfma_f32_16x16x32_bf16 v[120:123], a[224:227], a[204:207], v[120:123]\n"
        "v_exp_f32 v108, v108\n"
        "v_mfma_f32_16x16x32_bf16 v[120:123], a[228:231], a[208:211], v[120:123]\n"
        "v_exp_f32 v109, v109\n"
        "v_mfma_f32_16x16x32_bf16 v[120:123], a[232:235], a[212:215], v[120:123]\n"
        "v_exp_f32 v110, v110\n"
        "v_mfma_f32_16x16x32_bf16 v[96:99], v[192:195], v[196:199], v[40:43]\n"
        "s_waitcnt lgkmcnt(6)\n"
        "v_exp_f32 v111, v111\n"
        "v_mfma_f32_16x16x32_bf16 v[96:99], v[208:211], v[200:203], v[96:99]\n"
        "s_waitcnt lgkmcnt(5)\n"
        "v_cvt_pk_bf16_f32 v76, v108, v109\n"
        "v_cvt_pk_bf16_f32 v77, v110, v111\n"
        "v_mfma_f32_16x16x32_bf16 v[112:115], a[72:75], a[236:239], 0\n"
        "s_waitcnt lgkmcnt(4)\n"
        "s_nop 0\n"
        "v_cvt_pk_bf16_f32 v148, v120, v121\n"
        "v_mfma_f32_16x16x32_bf16 v[112:115], a[76:79], a[240:243], v[112:115]\n"
        "s_waitcnt lgkmcnt(3)\n"
        "v_cvt_pk_bf16_f32 v149, v122, v123\n"
        "v_mfma_f32_16x16x32_bf16 v[112:115], a[80:83], a[244:247], v[112:115]\n"
        "s_waitcnt lgkmcnt(2)\n"
        "v_exp_f32 v96, v96\n"
        "v_mfma_f32_16x16x32_bf16 v[112:115], a[84:87], a[248:251], v[112:115]\n"
        "s_waitcnt lgkmcnt(1)\n"
        "v_exp_f32 v97, v97\n"
        "v_mfma_f32_16x16x32_bf16 v[112:115], a[88:91], a[216:219], v[112:115]\n"
        "s_waitcnt lgkmcnt(0)\n"
        "v_exp_f32 v98, v98\n"
        "v_mfma_f32_16x16x32_bf16 v[112:115], a[92:95], a[220:223], v[112:115]\n"
        "v_exp_f32 v99, v99\n"
        "v_mfma_f32_16x16x32_bf16 v[116:119], a[116:119], a[220:223], 0\n"
        "v_cvt_pk_bf16_f32 v84, v96, v97\n"
        "v_cvt_pk_bf16_f32 v85, v98, v99\n"
        "v_mfma_f32_16x16x32_bf16 v[116:119], a[112:115], a[216:219], v[116:119]\n"
        "v_add_u32 v240, s31, v240\n"
        "v_add_u32 v241, s31, v241\n"
        "v_mfma_f32_16x16x32_bf16 v[116:119], a[108:111], a[248:251], v[116:119]\n"
        "v_cvt_pk_bf16_f32 v212, v112, v113\n"
        "v_cvt_pk_bf16_f32 v213, v114, v115\n"
        "v_mfma_f32_16x16x32_bf16 v[116:119], a[104:107], a[244:247], v[116:119]\n"
        "v_mfma_f32_16x16x32_bf16 v[116:119], a[100:103], a[240:243], v[116:119]\n"
        "v_mfma_f32_16x16x32_bf16 v[116:119], a[96:99], a[236:239], v[116:119]\n"
        "s_nop 2\n"
        "v_mfma_f32_16x16x32_bf16 v[120:123], a[120:123], a[236:239], 0\n"
        "s_nop 2\n"
        "v_mfma_f32_16x16x32_bf16 v[120:123], a[124:127], a[240:243], v[120:123]\n"
        "s_add_u32 s47, s47, 6291456\n"
        "v_cvt_pk_bf16_f32 v214, v116, v117\n"
        "v_cvt_pk_bf16_f32 v215, v118, v119\n"
        "v_mfma_f32_16x16x32_bf16 v[120:123], a[128:131], a[244:247], v[120:123]\n"
        "s_add_u32 s36, s36, 2097152\n"
        "v_mfma_f32_16x16x32_bf16 v[120:123], a[132:135], a[248:251], v[120:123]\n"
        "s_xor_b32 s48, s48, 8192\n"
        "v_mfma_f32_16x16x32_bf16 v[120:123], a[136:139], a[216:219], v[120:123]\n"
        "v_mfma_f32_16x16x32_bf16 v[120:123], a[140:143], a[220:223], v[120:123]\n"
        "s_nop 2\n"
        "v_mfma_f32_16x16x32_bf16 v[112:115], a[220:223], a[164:167], 0\n"
        "s_nop 2\n"
        "v_mfma_f32_16x16x32_bf16 v[112:115], a[216:219], a[160:163], v[112:115]\n"
        "v_cvt_pk_bf16_f32 v230, v120, v121\n"
        "v_cvt_pk_bf16_f32 v231, v122, v123\n"
        "v_mfma_f32_16x16x32_bf16 v[112:115], a[248:251], a[156:159], v[112:115]\n"
        "v_mfma_f32_16x16x32_bf16 v[112:115], a[244:247], a[152:155], v[112:115]\n"
        "v_mfma_f32_16x16x32_bf16 v[112:115], a[240:243], a[148:151], v[112:115]\n"
        "v_mfma_f32_16x16x32_bf16 v[112:115], a[236:239], a[144:147], v[112:115]\n"
        "s_barrier\n"
        "s_nop 2\n"
        "v_mfma_f32_16x16x32_bf16 v[100:103], v[212:215], v[156:159], v[12:15]\n"
        "s_nop 2\n"
        "v_mfma_f32_16x16x32_bf16 v[100:103], v[228:231], v[164:167], v[100:103]\n"
        "v_cvt_pk_bf16_f32 v134, v112, v113\n"
        "v_cvt_pk_bf16_f32 v135, v114, v115\n"
        "v_mfma_f32_16x16x32_bf16 v[116:119], a[236:239], a[168:171], 0\n"
        "s_sub_u32 s27, s27, 1\n"
        "v_mfma_f32_16x16x32_bf16 v[116:119], a[240:243], a[172:175], v[116:119]\n"
        "s_cmp_lg_u32 s27, 0\n"
        "v_mfma_f32_16x16x32_bf16 v[116:119], a[244:247], a[176:179], v[116:119]\n"
        "s_cbranch_scc0 19f\n"
        "v_mfma_f32_16x16x32_bf16 v[116:119], a[248:251], a[180:183], v[116:119]\n"
        "v_mfma_f32_16x16x32_bf16 v[116:119], a[216:219], a[184:187], v[116:119]\n"
        "s_nop 0\n"
        "v_exp_f32 v100, v100\n"
        "v_mfma_f32_16x16x32_bf16 v[116:119], a[220:223], a[188:191], v[116:119]\n"
        "v_exp_f32 v101, v101\n"
        "v_mfma_f32_16x16x32_bf16 v[104:107], v[212:215], v[176:179], v[28:31]\n"
        "v_exp_f32 v102, v102\n"
        "v_mfma_f32_16x16x32_bf16 v[104:107], v[228:231], v[184:187], v[104:107]\n"
        "v_exp_f32 v103, v103\n"
        "v_mfma_f32_16x16x32_bf16 v[120:123], a[220:223], a[212:215], 0\n"
        "v_cvt_pk_bf16_f32 v70, v100, v101\n"
        "v_cvt_pk_bf16_f32 v71, v102, v103\n"
        "v_mfma_f32_16x16x32_bf16 v[120:123], a[216:219], a[208:211], v[120:123]\n"
        "v_cvt_pk_bf16_f32 v142, v116, v117\n"
        "v_cvt_pk_bf16_f32 v143, v118, v119\n"
        "v_mfma_f32_16x16x32_bf16 v[120:123], a[248:251], a[204:207], v[120:123]\n"
        "v_mov_b64 v[96:97], 0\n"
        "v_mfma_f32_16x16x32_bf16 v[120:123], a[244:247], a[200:203], v[120:123]\n"
        "v_exp_f32 v104, v104\n"
        "v_mfma_f32_16x16x32_bf16 v[120:123], a[240:243], a[196:199], v[120:123]\n"
        "v_exp_f32 v105, v105\n"
        "v_mfma_f32_16x16x32_bf16 v[120:123], a[236:239], a[192:195], v[120:123]\n"
        "v_exp_f32 v106, v106\n"
        "v_mfma_f32_16x16x32_bf16 v[108:111], v[212:215], v[196:199], v[44:47]\n"
        "v_exp_f32 v107, v107\n"
        "v_mfma_f32_16x16x32_bf16 v[108:111], v[228:231], v[204:207], v[108:111]\n"
        "v_cvt_pk_bf16_f32 v78, v104, v105\n"
        "v_cvt_pk_bf16_f32 v79, v106, v107\n"
        "v_mfma_f32_16x16x32_bf16 v[112:115], a[0:3], a[236:239], 0\n"
        "s_nop 0\n"
        "v_cvt_pk_bf16_f32 v150, v120, v121\n"
        "v_cvt_pk_bf16_f32 v151, v122, v123\n"
        "v_mfma_f32_16x16x32_bf16 v[112:115], a[4:7], a[240:243], v[112:115]\n"
        "v_mfma_f32_16x16x32_bf16 v[112:115], a[8:11], a[244:247], v[112:115]\n"
        "v_exp_f32 v108, v108\n"
        "v_mfma_f32_16x16x32_bf16 v[112:115], a[12:15], a[248:251], v[112:115]\n"
        "v_exp_f32 v109, v109\n"
        "v_mfma_f32_16x16x32_bf16 v[112:115], a[16:19], a[216:219], v[112:115]\n"
        "v_exp_f32 v110, v110\n"
        "v_mfma_f32_16x16x32_bf16 v[112:115], a[20:23], a[220:223], v[112:115]\n"
        "v_exp_f32 v111, v111\n"
        "v_smfmac_f32_16x16x64_bf16 v[96:99], a[252:255], v[64:71], v237\n"
        "v_cvt_pk_bf16_f32 v86, v108, v109\n"
        "v_mfma_f32_16x16x32_bf16 v[100:103], v[132:135], v[68:71], 0\n"
        "v_mov_b64 v[124:125], 0\n"
        "v_cvt_pk_bf16_f32 v87, v110, v111\n"
        "v_mfma_f32_16x16x32_bf16 v[100:103], v[128:131], v[64:67], v[100:103]\n"
        "s_nop 0\n"
        "v_cvt_pk_bf16_f32 v216, v112, v113\n"
        "v_cvt_pk_bf16_f32 v217, v114, v115\n"
        "v_mfma_f32_16x16x32_bf16 v[104:107], v[136:139], v[64:67], 0\n"
        ".long 0xbec200ff\n"
        "47:\n"
        "s_branch 61b\n"
        "ds_read_b128 v[222:225], v251 offset:4608\n"
        "v_add_f32 v97, v96, v97\n"
        "v_mfma_f32_16x16x32_bf16 v[104:107], v[140:143], v[68:71], v[104:107]\n"
        "v_rcp_f32 v96, v97\n"
        "v_mfma_f32_16x16x32_bf16 v[108:111], v[148:151], v[68:71], 0\n"
        "v_mul_f32 v100, v96, v100\n"
        "v_mul_f32 v101, v96, v101\n"
        "v_mfma_f32_16x16x32_bf16 v[108:111], v[144:147], v[64:67], v[108:111]\n"
        "v_mul_f32 v102, v96, v102\n"
        "v_mul_f32 v103, v96, v103\n"
        "v_mfma_f32_16x16x32_bf16 v[116:119], a[44:47], a[220:223], 0\n"
        "v_cvt_pk_bf16_f32 v100, v100, v101\n"
        "v_cvt_pk_bf16_f32 v101, v102, v103\n"
        "v_mfma_f32_16x16x32_bf16 v[116:119], a[40:43], a[216:219], v[116:119]\n"
        "v_mul_f32 v104, v96, v104\n"
        "v_mul_f32 v105, v96, v105\n"
        "v_mfma_f32_16x16x32_bf16 v[116:119], a[36:39], a[248:251], v[116:119]\n"
        "v_mul_f32 v106, v96, v106\n"
        "v_mul_f32 v107, v96, v107\n"
        "v_mfma_f32_16x16x32_bf16 v[116:119], a[32:35], a[244:247], v[116:119]\n"
        "v_cvt_pk_bf16_f32 v102, v104, v105\n"
        "v_cvt_pk_bf16_f32 v103, v106, v107\n"
        "v_mfma_f32_16x16x32_bf16 v[116:119], a[28:31], a[240:243], v[116:119]\n"
        "ds_write_b128 v245, v[100:103] offset:0\n"
        "v_mfma_f32_16x16x32_bf16 v[116:119], a[24:27], a[236:239], v[116:119]\n"
        "v_mul_f32 v108, v96, v108\n"
        "v_mul_f32 v109, v96, v109\n"
        "v_smfmac_f32_16x16x64_bf16 v[124:127], a[252:255], v[72:79], v237\n"
        "v_mul_f32 v110, v96, v110\n"
        "v_mfma_f32_16x16x32_bf16 v[64:67], v[132:135], v[76:79], 0\n"
        "v_mul_f32 v111, v96, v111\n"
        "v_cvt_pk_bf16_f32 v108, v108, v109\n"
        "v_mfma_f32_16x16x32_bf16 v[64:67], v[128:131], v[72:75], v[64:67]\n"
        "ds_read_b128 v[156:159], v247 offset:0\n"
        "v_cvt_pk_bf16_f32 v218, v116, v117\n"
        "v_mfma_f32_16x16x32_bf16 v[68:71], v[136:139], v[72:75], 0\n"
        "ds_read_b128 v[160:163], v247 offset:4096\n"
        "v_cvt_pk_bf16_f32 v219, v118, v119\n"
        "v_mfma_f32_16x16x32_bf16 v[68:71], v[140:143], v[76:79], v[68:71]\n"
        "ds_read_b128 v[164:167], v247 offset:4352\n"
        "v_cvt_pk_bf16_f32 v109, v110, v111\n"
        "v_mfma_f32_16x16x32_bf16 v[120:123], v[148:151], v[76:79], 0\n"
        "ds_write_b64 v246, v[108:109] offset:0\n"
        "v_mfma_f32_16x16x32_bf16 v[120:123], v[144:147], v[72:75], v[120:123]\n"
        "v_max_u32 v236, v96, v97\n"
        "v_mov_b64 v[116:117], 0\n"
        "v_mfma_f32_16x16x32_bf16 v[112:115], a[48:51], a[236:239], 0\n"
        "v_add_f32 v125, v124, v125\n"
        "v_mfma_f32_16x16x32_bf16 v[112:115], a[52:55], a[240:243], v[112:115]\n"
        "v_rcp_f32 v124, v125\n"
        "v_mfma_f32_16x16x32_bf16 v[112:115], a[56:59], a[244:247], v[112:115]\n"
        "v_mul_f32 v64, v124, v64\n"
        "v_mul_f32 v65, v124, v65\n"
        "v_mfma_f32_16x16x32_bf16 v[112:115], a[60:63], a[248:251], v[112:115]\n"
        "v_mul_f32 v66, v124, v66\n"
        "v_mul_f32 v67, v124, v67\n"
        "v_mfma_f32_16x16x32_bf16 v[112:115], a[64:67], a[216:219], v[112:115]\n"
        "v_cvt_pk_bf16_f32 v64, v64, v65\n"
        "v_cvt_pk_bf16_f32 v65, v66, v67\n"
        "v_mfma_f32_16x16x32_bf16 v[112:115], a[68:71], a[220:223], v[112:115]\n"
        "ds_read_b128 a[216:219], v248 offset:0\n"
        "v_mul_f32 v68, v124, v68\n"
        "v_smfmac_f32_16x16x64_bf16 v[116:119], a[252:255], v[80:87], v237\n"
        "ds_read_b128 a[220:223], v248 offset:3072\n"
        "v_mfma_f32_16x16x32_bf16 v[72:75], v[132:135], v[84:87], 0\n"
        "v_mul_f32 v69, v124, v69\n"
        "v_mul_f32 v70, v124, v70\n"
        "v_mfma_f32_16x16x32_bf16 v[72:75], v[128:131], v[80:83], v[72:75]\n"
        "v_cvt_pk_bf16_f32 v220, v112, v113\n"
        "v_cvt_pk_bf16_f32 v221, v114, v115\n"
        "v_mfma_f32_16x16x32_bf16 v[76:79], v[136:139], v[80:83], 0\n"
        "ds_read_b128 a[224:227], v248 offset:32\n"
        "v_cvt_pk_bf16_f32 v226, v112, v113\n"
        "v_mfma_f32_16x16x32_bf16 v[76:79], v[140:143], v[84:87], v[76:79]\n"
        "ds_read_b128 a[228:231], v248 offset:3104\n"
        "v_cvt_pk_bf16_f32 v227, v114, v115\n"
        "v_mfma_f32_16x16x32_bf16 v[96:99], v[148:151], v[84:87], 0\n"
        "v_mul_f32 v71, v124, v71\n"
        "v_cvt_pk_bf16_f32 v66, v68, v69\n"
        "v_mfma_f32_16x16x32_bf16 v[96:99], v[144:147], v[80:83], v[96:99]\n"
        "v_cvt_pk_bf16_f32 v67, v70, v71\n"
        "v_mul_f32 v120, v124, v120\n"
        "v_mfma_f32_16x16x32_bf16 v[100:103], v[152:155], v[216:219], v[48:51]\n"
        "s_waitcnt lgkmcnt(9)\n"
        "ds_read_b128 v[152:155], v247 offset:256\n"
        "v_mul_f32 v121, v124, v121\n"
        "v_mfma_f32_16x16x32_bf16 v[100:103], v[168:171], v[220:223], v[100:103]\n"
        "ds_write_b128 v245, v[64:67] offset:256\n"
        "v_mfma_f32_16x16x32_bf16 v[104:107], v[172:175], v[216:219], v[52:55]\n"
        "v_mul_f32 v122, v124, v122\n"
        "v_mul_f32 v123, v124, v123\n"
        "v_mfma_f32_16x16x32_bf16 v[104:107], v[188:191], v[224:227], v[104:107]\n"
        "s_waitcnt lgkmcnt(5)\n"
        "v_cvt_pk_bf16_f32 v120, v120, v121\n"
        "v_cvt_pk_bf16_f32 v121, v122, v123\n"
        "v_mfma_f32_16x16x32_bf16 v[176:179], v[156:159], a[216:219], 0\n"
        "s_waitcnt lgkmcnt(4)\n"
        "ds_write_b64 v246, v[120:121] offset:256\n"
        "v_mfma_f32_16x16x32_bf16 v[176:179], v[160:163], a[220:223], v[176:179]\n"
        "ds_read_b128 v[222:225], v252 offset:4608\n"
        "v_max3_u32 v236, v236, v124, v125\n"
        "v_mfma_f32_16x16x32_bf16 v[196:199], v[164:167], a[220:223], 0\n"
        "s_waitcnt lgkmcnt(3)\n"
        "ds_read_b128 a[232:235], v254 offset:0\n"
        "v_add_f32 v117, v116, v117\n"
        "v_mfma_f32_16x16x32_bf16 v[196:199], v[152:155], a[216:219], v[196:199]\n"
        "ds_read_b128 a[220:223], v254 offset:3072\n"
        "v_mfma_f32_16x16x32_bf16 v[200:203], v[152:155], a[224:227], 0\n"
        "ds_read_b128 a[216:219], v254 offset:32\n"
        "v_mfma_f32_16x16x32_bf16 v[200:203], v[164:167], a[228:231], v[200:203]\n"
        "v_rcp_f32 v116, v117\n"
        "v_mfma_f32_16x16x32_bf16 v[180:183], v[160:163], a[228:231], 0\n"
        "ds_read_b128 a[228:231], v254 offset:3104\n"
        "v_mul_f32 v72, v116, v72\n"
        "v_mfma_f32_16x16x32_bf16 v[180:183], v[156:159], a[224:227], v[180:183]\n"
        "ds_read_b128 a[236:239], v247 offset:8192\n"
        "v_mul_f32 v73, v116, v73\n"
        "v_mfma_f32_16x16x32_bf16 v[108:111], v[192:195], v[216:219], v[56:59]\n"
        "s_waitcnt lgkmcnt(5)\n"
        "ds_read_b128 a[224:227], v248 offset:16\n"
        "v_mul_f32 v74, v116, v74\n"
        "v_mfma_f32_16x16x32_bf16 v[108:111], v[208:211], v[220:223], v[108:111]\n"
        "ds_read_b128 a[240:243], v247 offset:12288\n"
        "v_mul_f32 v75, v116, v75\n"
        "v_mfma_f32_16x16x32_bf16 v[112:115], v[212:215], v[216:219], v[60:63]\n"
        "ds_read_b128 a[244:247], v247 offset:12544\n"
        "v_cvt_pk_bf16_f32 v72, v72, v73\n"
        "v_mfma_f32_16x16x32_bf16 v[112:115], v[228:231], v[224:227], v[112:115]\n"
        "s_waitcnt lgkmcnt(7)\n"
        "ds_read_b128 a[248:251], v247 offset:8448\n"
        "v_cvt_pk_bf16_f32 v73, v74, v75\n"
        "v_mfma_f32_16x16x32_bf16 v[184:187], v[156:159], a[232:235], 0\n"
        "s_waitcnt lgkmcnt(7)\n"
        "v_exp_f32 v100, v100\n"
        "v_mfma_f32_16x16x32_bf16 v[184:187], v[160:163], a[220:223], v[184:187]\n"
        "v_exp_f32 v101, v101\n"
        "v_mfma_f32_16x16x32_bf16 v[204:207], v[164:167], a[220:223], 0\n"
        "ds_read_b128 a[220:223], v248 offset:3088\n"
        "v_mul_f32 v76, v116, v76\n"
        "v_mfma_f32_16x16x32_bf16 v[204:207], v[152:155], a[232:235], v[204:207]\n"
        "s_waitcnt lgkmcnt(7)\n"
        "ds_read_b128 a[232:235], v254 offset:16\n"
        "v_mul_f32 v77, v116, v77\n"
        "v_mfma_f32_16x16x32_bf16 v[192:195], v[152:155], a[216:219], 0\n"
        "s_waitcnt lgkmcnt(7)\n"
        "v_mul_f32 v78, v116, v78\n"
        "v_mul_f32 v79, v116, v79\n"
        "v_mfma_f32_16x16x32_bf16 v[192:195], v[164:167], a[228:231], v[192:195]\n"
        "v_exp_f32 v102, v102\n"
        "v_mfma_f32_16x16x32_bf16 v[172:175], v[160:163], a[228:231], 0\n"
        "ds_read_b128 a[228:231], v254 offset:3088\n"
        "v_cvt_pk_bf16_f32 v88, v100, v101\n"
        "v_mfma_f32_16x16x32_bf16 v[172:175], v[156:159], a[216:219], v[172:175]\n"
        "s_waitcnt lgkmcnt(6)\n"
        "ds_read_b128 a[216:219], v248 offset:6144\n"
        "v_cvt_pk_bf16_f32 v74, v76, v77\n"
        "v_mfma_f32_16x16x32_bf16 v[212:215], v[156:159], a[224:227], 0\n"
        "s_waitcnt lgkmcnt(3)\n"
        "v_exp_f32 v104, v104\n"
        "v_mfma_f32_16x16x32_bf16 v[212:215], v[160:163], a[220:223], v[212:215]\n"
        "v_exp_f32 v103, v103\n"
        "v_mfma_f32_16x16x32_bf16 v[216:219], v[164:167], a[220:223], 0\n"
        "ds_read_b128 a[220:223], v248 offset:9216\n"
        "v_cvt_pk_bf16_f32 v75, v78, v79\n"
        "v_mfma_f32_16x16x32_bf16 v[216:219], v[152:155], a[224:227], v[216:219]\n"
        "s_waitcnt lgkmcnt(3)\n"
        "ds_write_b128 v245, v[72:75] offset:512\n"
        "v_mfma_f32_16x16x32_bf16 v[224:227], v[152:155], a[232:235], 0\n"
        "s_waitcnt lgkmcnt(3)\n"
        "v_exp_f32 v105, v105\n"
        "v_mfma_f32_16x16x32_bf16 v[224:227], v[164:167], a[228:231], v[224:227]\n"
        "v_cvt_pk_bf16_f32 v89, v102, v103\n"
        "v_cvt_pk_bf16_f32 v90, v104, v105\n"
        "v_mfma_f32_16x16x32_bf16 v[220:223], v[160:163], a[228:231], 0\n"
        "ds_read_b128 a[224:227], v248 offset:6176\n"
        "ds_read_b128 a[228:231], v248 offset:9248\n"
        "v_mfma_f32_16x16x32_bf16 v[220:223], v[156:159], a[232:235], v[220:223]\n"
        "s_waitcnt lgkmcnt(4)\n"
        "ds_read_b128 a[232:235], v254 offset:6144\n"
        "v_mul_f32 v96, v116, v96\n"
        "v_mfma_f32_16x16x32_bf16 v[176:179], a[236:239], a[216:219], v[176:179]\n"
        "s_waitcnt lgkmcnt(4)\n"
        "v_exp_f32 v106, v106\n"
        "v_mfma_f32_16x16x32_bf16 v[176:179], a[240:243], a[220:223], v[176:179]\n"
        "v_mul_f32 v97, v116, v97\n"
        "v_mul_f32 v98, v116, v98\n"
        "v_mfma_f32_16x16x32_bf16 v[196:199], a[244:247], a[220:223], v[196:199]\n"
        "ds_read_b128 a[220:223], v254 offset:9216\n"
        "v_mul_f32 v99, v116, v99\n"
        "v_mfma_f32_16x16x32_bf16 v[196:199], a[248:251], a[216:219], v[196:199]\n"
        "s_waitcnt lgkmcnt(3)\n"
        "ds_read_b128 a[216:219], v254 offset:6176\n"
        "v_cvt_pk_bf16_f32 v96, v96, v97\n"
        "v_mfma_f32_16x16x32_bf16 v[200:203], a[248:251], a[224:227], v[200:203]\n"
        "s_waitcnt lgkmcnt(3)\n"
        "ds_read_b128 v[156:159], v247 offset:16384\n"
        "v_cvt_pk_bf16_f32 v97, v98, v99\n"
        "v_mfma_f32_16x16x32_bf16 v[200:203], a[244:247], a[228:231], v[200:203]\n"
        "ds_write_b64 v246, v[96:97] offset:512\n"
        "v_mfma_f32_16x16x32_bf16 v[180:183], a[240:243], a[228:231], v[180:183]\n"
        "ds_read_b128 a[228:231], v254 offset:9248\n"
        "v_max3_u32 v236, v236, v116, v117\n"
        "v_mfma_f32_16x16x32_bf16 v[180:183], a[236:239], a[224:227], v[180:183]\n"
        "s_waitcnt lgkmcnt(5)\n"
        "ds_read_b128 a[224:227], v248 offset:6160\n"
        "v_mfma_f32_16x16x32_bf16 v[184:187], a[236:239], a[232:235], v[184:187]\n"
        "s_waitcnt lgkmcnt(5)\n"
        "ds_read_b128 v[160:163], v247 offset:20480\n"
        "v_mfma_f32_16x16x32_bf16 v[184:187], a[240:243], a[220:223], v[184:187]\n"
        "ds_read_b128 v[164:167], v247 offset:20736\n"
        "v_mfma_f32_16x16x32_bf16 v[204:207], a[244:247], a[220:223], v[204:207]\n"
        "ds_read_b128 a[220:223], v248 offset:9232\n"
        "v_mfma_f32_16x16x32_bf16 v[204:207], a[248:251], a[232:235], v[204:207]\n"
        "s_waitcnt lgkmcnt(7)\n"
        "ds_read_b128 a[232:235], v254 offset:6160\n"
        "v_mfma_f32_16x16x32_bf16 v[192:195], a[248:251], a[216:219], v[192:195]\n"
        "s_waitcnt lgkmcnt(5)\n"
        "ds_read_b128 v[152:155], v247 offset:16640\n"
        "v_mfma_f32_16x16x32_bf16 v[192:195], a[244:247], a[228:231], v[192:195]\n"
        "v_exp_f32 v107, v107\n"
        "v_mfma_f32_16x16x32_bf16 v[172:175], a[240:243], a[228:231], v[172:175]\n"
        "ds_read_b128 a[228:231], v254 offset:9232\n"
        "v_cvt_pk_bf16_f32 v91, v106, v107\n"
        "v_mfma_f32_16x16x32_bf16 v[172:175], a[236:239], a[216:219], v[172:175]\n"
        "s_waitcnt lgkmcnt(6)\n"
        "ds_read_b128 a[216:219], v248 offset:12288\n"
        "v_mfma_f32_16x16x32_bf16 v[212:215], a[236:239], a[224:227], v[212:215]\n"
        "s_waitcnt lgkmcnt(4)\n"
        "v_exp_f32 v108, v108\n"
        "v_mfma_f32_16x16x32_bf16 v[212:215], a[240:243], a[220:223], v[212:215]\n"
        "v_exp_f32 v109, v109\n"
        "v_mfma_f32_16x16x32_bf16 v[216:219], a[244:247], a[220:223], v[216:219]\n"
        "ds_read_b128 a[220:223], v248 offset:15360\n"
        "v_cvt_pk_bf16_f32 v92, v108, v109\n"
        "v_mfma_f32_16x16x32_bf16 v[216:219], a[248:251], a[224:227], v[216:219]\n"
        "s_waitcnt lgkmcnt(4)\n"
        "ds_read_b128 a[224:227], v248 offset:12320\n"
        "v_mfma_f32_16x16x32_bf16 v[224:227], a[248:251], a[232:235], v[224:227]\n"
        "s_waitcnt lgkmcnt(3)\n"
        "v_exp_f32 v110, v110\n"
        "v_mfma_f32_16x16x32_bf16 v[224:227], a[244:247], a[228:231], v[224:227]\n"
        "v_exp_f32 v112, v112\n"
        "v_mfma_f32_16x16x32_bf16 v[220:223], a[240:243], a[228:231], v[220:223]\n"
        "ds_read_b128 a[228:231], v248 offset:15392\n"
        "v_mfma_f32_16x16x32_bf16 v[220:223], a[236:239], a[232:235], v[220:223]\n"
        "s_waitcnt lgkmcnt(3)\n"
        "ds_read_b128 a[232:235], v254 offset:12288\n"
        "v_mfma_f32_16x16x32_bf16 v[176:179], v[156:159], a[216:219], v[176:179]\n"
        "s_waitcnt lgkmcnt(3)\n"
        "v_exp_f32 v111, v111\n"
        "v_mfma_f32_16x16x32_bf16 v[176:179], v[160:163], a[220:223], v[176:179]\n"
        "v_exp_f32 v113, v113\n"
        "v_mfma_f32_16x16x32_bf16 v[196:199], v[164:167], a[220:223], v[196:199]\n"
        "ds_read_b128 a[220:223], v254 offset:15360\n"
        "v_cvt_pk_bf16_f32 v93, v110, v111\n"
        "v_mfma_f32_16x16x32_bf16 v[196:199], v[152:155], a[216:219], v[196:199]\n"
        "s_waitcnt lgkmcnt(3)\n"
        "ds_read_b128 a[216:219], v254 offset:12320\n"
        "v_cvt_pk_bf16_f32 v94, v112, v113\n"
        "v_mfma_f32_16x16x32_bf16 v[200:203], v[152:155], a[224:227], v[200:203]\n"
        "s_waitcnt lgkmcnt(3)\n"
        "v_exp_f32 v114, v114\n"
        "v_mfma_f32_16x16x32_bf16 v[200:203], v[164:167], a[228:231], v[200:203]\n"
        "v_exp_f32 v115, v115\n"
        "v_mfma_f32_16x16x32_bf16 v[180:183], v[160:163], a[228:231], v[180:183]\n"
        "s_waitcnt vmcnt(0)\n"
        "ds_read_b128 a[228:231], v254 offset:15392\n"
        "v_cvt_pk_bf16_f32 v95, v114, v115\n"
        "v_mfma_f32_16x16x32_bf16 v[180:183], v[156:159], a[224:227], v[180:183]\n"
        "s_waitcnt lgkmcnt(3)\n"
        "ds_read_b128 a[224:227], v248 offset:12304\n"
        "v_mfma_f32_16x16x32_bf16 v[184:187], v[156:159], a[232:235], v[184:187]\n"
        "s_waitcnt lgkmcnt(3)\n"
        "v_mfma_f32_16x16x32_bf16 v[184:187], v[160:163], a[220:223], v[184:187]\n"
        "s_barrier\n"
        "v_mfma_f32_16x16x32_bf16 v[204:207], v[164:167], a[220:223], v[204:207]\n"
        "ds_read_b128 a[220:223], v248 offset:15376\n"
        "v_mfma_f32_16x16x32_bf16 v[204:207], v[152:155], a[232:235], v[204:207]\n"
        "s_waitcnt lgkmcnt(3)\n"
        "ds_read_b128 a[232:235], v254 offset:12304\n"
        "v_mfma_f32_16x16x32_bf16 v[192:195], v[152:155], a[216:219], v[192:195]\n"
        "s_waitcnt lgkmcnt(3)\n"
        "ds_read_b128 a[236:239], v240 offset:0\n"
        "v_mfma_f32_16x16x32_bf16 v[192:195], v[164:167], a[228:231], v[192:195]\n"
        "ds_read_b128 a[240:243], v241 offset:0\n"
        "v_mfma_f32_16x16x32_bf16 v[172:175], v[160:163], a[228:231], v[172:175]\n"
        "ds_read_b128 a[228:231], v254 offset:15376\n"
        "v_mfma_f32_16x16x32_bf16 v[172:175], v[156:159], a[216:219], v[172:175]\n"
        "s_waitcnt lgkmcnt(5)\n"
        "ds_read_b128 a[244:247], v240 offset:128\n"
        "v_mfma_f32_16x16x32_bf16 v[212:215], v[156:159], a[224:227], v[212:215]\n"
        "s_waitcnt lgkmcnt(5)\n"
        "ds_read_b128 a[248:251], v241 offset:128\n"
        "v_mfma_f32_16x16x32_bf16 v[212:215], v[160:163], a[220:223], v[212:215]\n"
        "ds_read_b128 a[216:219], v240 offset:256\n"
        "v_mfma_f32_16x16x32_bf16 v[216:219], v[164:167], a[220:223], v[216:219]\n"
        "ds_read_b128 a[220:223], v241 offset:256\n"
        "v_mfma_f32_16x16x32_bf16 v[216:219], v[152:155], a[224:227], v[216:219]\n"
        "s_waitcnt lgkmcnt(7)\n"
        "v_mfma_f32_16x16x32_bf16 v[224:227], v[152:155], a[232:235], v[224:227]\n"
        "s_waitcnt lgkmcnt(4)\n"
        "v_mfma_f32_16x16x32_bf16 v[224:227], v[164:167], a[228:231], v[224:227]\n"
        "v_mfma_f32_16x16x32_bf16 v[220:223], v[160:163], a[228:231], v[220:223]\n"
        "v_mfma_f32_16x16x32_bf16 v[220:223], v[156:159], a[232:235], v[220:223]\n"
        "s_branch 10b\n"
        ".p2align 6\n"
        "s_nop 0\n"
        "19:\n"
        "v_mfma_f32_16x16x32_bf16 v[116:119], a[248:251], a[180:183], v[116:119]\n"
        "ds_read_b128 a[224:227], v247 offset:0\n"
        "ds_read_b128 a[120:123], v254 offset:0\n"
        "s_and_b32 s46, s42, 1\n"
        "v_mfma_f32_16x16x32_bf16 v[116:119], a[216:219], a[184:187], v[116:119]\n"
        "ds_read_b128 a[228:231], v247 offset:4096\n"
        "ds_read_b128 a[124:127], v254 offset:3072\n"
        "v_mfma_f32_16x16x32_bf16 v[116:119], a[220:223], a[188:191], v[116:119]\n"
        "ds_read_b128 a[232:235], v247 offset:8192\n"
        "s_lshl_b32 s31, s46, 8\n"
        "v_mov_b32 v241, 0\n"
        "v_mfma_f32_16x16x32_bf16 v[104:107], v[212:215], v[176:179], v[28:31]\n"
        "ds_read_b128 a[128:131], v254 offset:6144\n"
        "s_nop 0\n"
        "v_mfma_f32_16x16x32_bf16 v[104:107], v[228:231], v[184:187], v[104:107]\n"
        "ds_read_b128 v[156:159], v247 offset:12288\n"
        "s_sub_u32 s31, s45, s31\n"
        "v_cvt_pk_bf16_f32 v142, v116, v117\n"
        "v_mfma_f32_16x16x32_bf16 v[120:123], a[220:223], a[212:215], 0\n"
        "ds_read_b128 a[132:135], v254 offset:9216\n"
        "v_cvt_pk_bf16_f32 v143, v118, v119\n"
        "v_mfma_f32_16x16x32_bf16 v[120:123], a[216:219], a[208:211], v[120:123]\n"
        "ds_read_b128 v[160:163], v247 offset:16384\n"
        "ds_read_b128 a[136:139], v254 offset:12288\n"
        "s_mul_i32 s45, s46, 786432\n"
        "v_mfma_f32_16x16x32_bf16 v[120:123], a[248:251], a[204:207], v[120:123]\n"
        "ds_read_b128 v[164:167], v247 offset:20480\n"
        "ds_read_b128 a[140:143], v254 offset:15360\n"
        "v_mfma_f32_16x16x32_bf16 v[120:123], a[244:247], a[200:203], v[120:123]\n"
        "ds_read_b128 a[92:95], v248 offset:15360\n"
        "s_sub_u32 s37, s37, s45\n"
        "v_mfma_f32_16x16x32_bf16 v[120:123], a[240:243], a[196:199], v[120:123]\n"
        "ds_read_b128 a[88:91], v248 offset:12288\n"
        "v_mfma_f32_16x16x32_bf16 v[120:123], a[236:239], a[192:195], v[120:123]\n"
        "ds_read_b128 a[84:87], v248 offset:9216\n"
        "s_cmp_eq_u32 s46, 0\n"
        "s_cselect_b32 s43, s43, s44\n"
        "v_mfma_f32_16x16x32_bf16 v[108:111], v[212:215], v[196:199], v[44:47]\n"
        "ds_read_b128 a[80:83], v248 offset:6144\n"
        "s_nop 0\n"
        "v_mfma_f32_16x16x32_bf16 v[108:111], v[228:231], v[204:207], v[108:111]\n"
        "s_waitcnt lgkmcnt(14)\n"
        "ds_read_b128 a[76:79], v248 offset:3072\n"
        "v_cvt_pk_bf16_f32 v150, v120, v121\n"
        "v_mfma_f32_16x16x32_bf16 v[8:11], a[224:227], a[120:123], 0\n"
        "s_waitcnt lgkmcnt(13)\n"
        "ds_read_b128 a[72:75], v248 offset:0\n"
        "v_cvt_pk_bf16_f32 v151, v122, v123\n"
        "v_mfma_f32_16x16x32_bf16 v[8:11], a[228:231], a[124:127], v[8:11]\n"
        "s_waitcnt lgkmcnt(12)\n"
        "v_exp_f32 v100, v100\n"
        "v_mfma_f32_16x16x32_bf16 v[8:11], a[232:235], a[128:131], v[8:11]\n"
        "s_waitcnt lgkmcnt(10)\n"
        "v_exp_f32 v101, v101\n"
        "v_mfma_f32_16x16x32_bf16 v[8:11], v[156:159], a[132:135], v[8:11]\n"
        "s_waitcnt lgkmcnt(8)\n"
        "ds_read_b128 a[96:99], v248 offset:32\n"
        "v_cvt_pk_bf16_f32 v70, v100, v101\n"
        "v_mfma_f32_16x16x32_bf16 v[8:11], v[160:163], a[136:139], v[8:11]\n"
        "s_waitcnt lgkmcnt(7)\n"
        "ds_read_b128 a[100:103], v248 offset:3104\n"
        "v_mfma_f32_16x16x32_bf16 v[8:11], v[164:167], a[140:143], v[8:11]\n"
        "s_waitcnt lgkmcnt(7)\n"
        "ds_read_b128 a[104:107], v248 offset:6176\n"
        "v_mfma_f32_16x16x32_bf16 v[0:3], v[164:167], a[92:95], 0\n"
        "s_waitcnt lgkmcnt(7)\n"
        "ds_read_b128 a[108:111], v248 offset:9248\n"
        "v_mfma_f32_16x16x32_bf16 v[0:3], v[160:163], a[88:91], v[0:3]\n"
        "s_waitcnt lgkmcnt(7)\n"
        "ds_read_b128 a[112:115], v248 offset:12320\n"
        "v_mfma_f32_16x16x32_bf16 v[0:3], v[156:159], a[84:87], v[0:3]\n"
        "s_waitcnt lgkmcnt(7)\n"
        "ds_read_b128 a[116:119], v248 offset:15392\n"
        "v_mfma_f32_16x16x32_bf16 v[0:3], a[232:235], a[80:83], v[0:3]\n"
        "s_waitcnt lgkmcnt(7)\n"
        "v_exp_f32 v102, v102\n"
        "v_mfma_f32_16x16x32_bf16 v[0:3], a[228:231], a[76:79], v[0:3]\n"
        "s_waitcnt lgkmcnt(6)\n"
        "v_exp_f32 v103, v103\n"
        "v_mfma_f32_16x16x32_bf16 v[0:3], a[224:227], a[72:75], v[0:3]\n"
        "s_add_u32 s28, s37, 786432\n"
        "v_exp_f32 v104, v104\n"
        "v_mfma_f32_16x16x32_bf16 v[116:119], a[0:3], a[236:239], 0\n"
        "s_cmp_eq_u32 s28, 100663296\n"
        "s_cselect_b32 s28, 0, s28\n"
        "v_mov_b64 v[96:97], 0\n"
        "v_mfma_f32_16x16x32_bf16 v[116:119], a[4:7], a[240:243], v[116:119]\n"
        "s_lshl_b32 s29, s42, 2\n"
        "v_exp_f32 v105, v105\n"
        "v_mfma_f32_16x16x32_bf16 v[116:119], a[8:11], a[244:247], v[116:119]\n"
        "s_add_u32 s47, s37, s43\n"
        "v_exp_f32 v106, v106\n"
        "v_mfma_f32_16x16x32_bf16 v[116:119], a[12:15], a[248:251], v[116:119]\n"
        "s_add_u32 s35, s28, s43\n"
        "v_exp_f32 v107, v107\n"
        "v_mfma_f32_16x16x32_bf16 v[116:119], a[16:19], a[216:219], v[116:119]\n"
        "ds_read_b128 a[164:167], v254 offset:15392\n"
        "v_cvt_pk_bf16_f32 v71, v102, v103\n"
        "v_mfma_f32_16x16x32_bf16 v[116:119], a[20:23], a[220:223], v[116:119]\n"
        "s_waitcnt lgkmcnt(6)\n"
        "ds_read_b128 a[160:163], v254 offset:12320\n"
        "v_cvt_pk_bf16_f32 v78, v104, v105\n"
        "v_mfma_f32_16x16x32_bf16 v[4:7], a[224:227], a[96:99], 0\n"
        "s_waitcnt lgkmcnt(6)\n"
        "ds_read_b128 a[156:159], v254 offset:9248\n"
        "v_cvt_pk_bf16_f32 v79, v106, v107\n"
        "v_mfma_f32_16x16x32_bf16 v[4:7], a[228:231], a[100:103], v[4:7]\n"
        "s_waitcnt lgkmcnt(6)\n"
        "ds_read_b128 a[152:155], v254 offset:6176\n"
        "v_add_u32 v240, s31, v247\n"
        "v_mfma_f32_16x16x32_bf16 v[4:7], a[232:235], a[104:107], v[4:7]\n"
        "s_waitcnt lgkmcnt(6)\n"
        "ds_read_b128 a[148:151], v254 offset:3104\n"
        "v_cvt_pk_bf16_f32 v216, v116, v117\n"
        "v_mfma_f32_16x16x32_bf16 v[4:7], v[156:159], a[108:111], v[4:7]\n"
        "s_waitcnt lgkmcnt(6)\n"
        "ds_read_b128 a[144:147], v254 offset:32\n"
        "v_cvt_pk_bf16_f32 v217, v118, v119\n"
        "v_mfma_f32_16x16x32_bf16 v[4:7], v[160:163], a[112:115], v[4:7]\n"
        "s_waitcnt lgkmcnt(6)\n"
        "v_exp_f32 v108, v108\n"
        "v_mfma_f32_16x16x32_bf16 v[4:7], v[164:167], a[116:119], v[4:7]\n"
        "v_exp_f32 v109, v109\n"
        "v_smfmac_f32_16x16x64_bf16 v[96:99], a[252:255], v[64:71], v237\n"
        "v_cvt_pk_bf16_f32 v86, v108, v109\n"
        "v_mfma_f32_16x16x32_bf16 v[100:103], v[132:135], v[68:71], 0\n"
        "v_mov_b64 v[124:125], 0\n"
        "v_mov_b32 v249, s29\n"
        "v_mfma_f32_16x16x32_bf16 v[100:103], v[128:131], v[64:67], v[100:103]\n"
        "v_exp_f32 v110, v110\n"
        "v_mfma_f32_16x16x32_bf16 v[104:107], v[136:139], v[64:67], 0\n"
        "v_exp_f32 v111, v111\n"
        "v_mfma_f32_16x16x32_bf16 v[104:107], v[140:143], v[68:71], v[104:107]\n"
        "v_cvt_pk_bf16_f32 v87, v110, v111\n"
        "v_add_f32 v97, v96, v97\n"
        "v_mfma_f32_16x16x32_bf16 v[108:111], v[148:151], v[68:71], 0\n"
        "v_rcp_f32 v96, v97\n"
        "v_mfma_f32_16x16x32_bf16 v[108:111], v[144:147], v[64:67], v[108:111]\n"
        "s_waitcnt lgkmcnt(5)\n"
        "v_cvt_pk_bf16_f32 v232, v0, v4\n"
        "v_mul_f32 v100, v96, v100\n"
        "v_mfma_f32_16x16x32_bf16 v[12:15], v[164:167], a[164:167], 0\n"
        "s_waitcnt lgkmcnt(4)\n"
        "v_mul_f32 v101, v96, v101\n"
        "v_mul_f32 v102, v96, v102\n"
        "v_mfma_f32_16x16x32_bf16 v[12:15], v[160:163], a[160:163], v[12:15]\n"
        "s_waitcnt lgkmcnt(3)\n"
        "ds_read_b128 v[176:179], v247 offset:256\n"
        "v_cvt_pk_bf16_f32 v234, v1, v5\n"
        "v_mfma_f32_16x16x32_bf16 v[12:15], v[156:159], a[156:159], v[12:15]\n"
        "s_waitcnt lgkmcnt(3)\n"
        "ds_read_b128 v[180:183], v247 offset:4352\n"
        "v_mul_f32 v103, v96, v103\n"
        "v_mfma_f32_16x16x32_bf16 v[12:15], a[232:235], a[152:155], v[12:15]\n"
        "s_waitcnt lgkmcnt(3)\n"
        "ds_read_b128 v[184:187], v247 offset:8448\n"
        "v_cvt_pk_bf16_f32 v100, v100, v101\n"
        "v_mfma_f32_16x16x32_bf16 v[12:15], a[228:231], a[148:151], v[12:15]\n"
        "s_waitcnt lgkmcnt(3)\n"
        "ds_read_b128 a[168:171], v248 offset:16\n"
        "v_cvt_pk_bf16_f32 v242, v2, v6\n"
        "v_mfma_f32_16x16x32_bf16 v[12:15], a[224:227], a[144:147], v[12:15]\n"
        "ds_read_b128 a[172:175], v248 offset:3088\n"
        "v_cvt_pk_bf16_f32 v101, v102, v103\n"
        "v_smfmac_f32_16x16x64_bf16 v[124:127], a[252:255], v[72:79], v237\n"
        "ds_read_b128 a[176:179], v248 offset:6160\n"
        "v_mfma_f32_16x16x32_bf16 v[64:67], v[132:135], v[76:79], 0\n"
        "ds_read_b128 a[180:183], v248 offset:9232\n"
        "v_mul_f32 v104, v96, v104\n"
        "v_mfma_f32_16x16x32_bf16 v[64:67], v[128:131], v[72:75], v[64:67]\n"
        "ds_read_b128 a[184:187], v248 offset:12304\n"
        "v_cvt_pk_bf16_f32 v233, v8, v12\n"
        "v_mfma_f32_16x16x32_bf16 v[68:71], v[136:139], v[72:75], 0\n"
        "buffer_store_dwordx2 v[232:233], v250, s[16:19], s38 offen offset:0 sc1\n"
        "ds_read_b128 a[188:191], v248 offset:15376\n"
        "v_mfma_f32_16x16x32_bf16 v[68:71], v[140:143], v[76:79], v[68:71]\n"
        "ds_read_b128 v[196:199], v247 offset:12544\n"
        "v_cvt_pk_bf16_f32 v235, v9, v13\n"
        "v_mfma_f32_16x16x32_bf16 v[112:115], v[148:151], v[76:79], 0\n"
        "ds_read_b128 v[200:203], v247 offset:16640\n"
        "v_add_f32 v125, v124, v125\n"
        "v_mfma_f32_16x16x32_bf16 v[112:115], v[144:147], v[72:75], v[112:115]\n"
        "buffer_store_dwordx2 v[234:235], v250, s[16:19], s38 offen offset:384 sc1\n"
        "ds_read_b128 v[204:207], v247 offset:20736\n"
        "v_mfma_f32_16x16x32_bf16 v[120:123], a[44:47], a[220:223], 0\n"
        "v_rcp_f32 v124, v125\n"
        "v_mfma_f32_16x16x32_bf16 v[120:123], a[40:43], a[216:219], v[120:123]\n"
        "v_cvt_pk_bf16_f32 v243, v10, v14\n"
        "v_mul_f32 v64, v124, v64\n"
        "v_mfma_f32_16x16x32_bf16 v[120:123], a[36:39], a[248:251], v[120:123]\n"
        "buffer_store_dwordx2 v[242:243], v250, s[16:19], s38 offen offset:768 sc1\n"
        "v_mul_f32 v65, v124, v65\n"
        "v_mfma_f32_16x16x32_bf16 v[120:123], a[32:35], a[244:247], v[120:123]\n"
        "ds_read_b128 a[192:195], v254 offset:16\n"
        "v_mul_f32 v66, v124, v66\n"
        "v_mfma_f32_16x16x32_bf16 v[120:123], a[28:31], a[240:243], v[120:123]\n"
        "ds_read_b128 a[196:199], v254 offset:3088\n"
        "v_mul_f32 v67, v124, v67\n"
        "v_mfma_f32_16x16x32_bf16 v[120:123], a[24:27], a[236:239], v[120:123]\n"
        "s_waitcnt lgkmcnt(10)\n"
        "ds_read_b128 a[200:203], v254 offset:6160\n"
        "v_cvt_pk_bf16_f32 v64, v64, v65\n"
        "v_mfma_f32_16x16x32_bf16 v[16:19], a[224:227], a[168:171], 0\n"
        "s_waitcnt lgkmcnt(10)\n"
        "ds_read_b128 a[204:207], v254 offset:9232\n"
        "ds_read_b128 v[222:225], v251 offset:4608\n"
        "v_mfma_f32_16x16x32_bf16 v[16:19], a[228:231], a[172:175], v[16:19]\n"
        "s_waitcnt lgkmcnt(11)\n"
        "ds_read_b128 a[208:211], v254 offset:12304\n"
        "v_cvt_pk_bf16_f32 v65, v66, v67\n"
        "v_mfma_f32_16x16x32_bf16 v[16:19], a[232:235], a[176:179], v[16:19]\n"
        "s_waitcnt lgkmcnt(11)\n"
        "ds_read_b128 a[212:215], v254 offset:15376\n"
        "v_cvt_pk_bf16_f32 v232, v3, v7\n"
        "v_mfma_f32_16x16x32_bf16 v[16:19], v[156:159], a[180:183], v[16:19]\n"
        "s_waitcnt lgkmcnt(11)\n"
        "v_cvt_pk_bf16_f32 v233, v11, v15\n"
        "v_mul_f32 v68, v124, v68\n"
        "v_mfma_f32_16x16x32_bf16 v[16:19], v[160:163], a[184:187], v[16:19]\n"
        "s_waitcnt lgkmcnt(10)\n"
        "buffer_store_dwordx2 v[232:233], v250, s[16:19], s38 offen offset:1152 sc1\n"
        "v_mul_f32 v69, v124, v69\n"
        "v_mfma_f32_16x16x32_bf16 v[16:19], v[164:167], a[188:191], v[16:19]\n"
        "s_waitcnt lgkmcnt(7)\n"
        "v_mul_f32 v70, v124, v70\n"
        "v_mul_f32 v71, v124, v71\n"
        "v_mfma_f32_16x16x32_bf16 v[24:27], v[204:207], a[92:95], 0\n"
        "v_mul_f32 v105, v96, v105\n"
        "v_mul_f32 v106, v96, v106\n"
        "v_mfma_f32_16x16x32_bf16 v[24:27], v[200:203], a[88:91], v[24:27]\n"
        "v_mul_f32 v107, v96, v107\n"
        "v_cvt_pk_bf16_f32 v66, v68, v69\n"
        "v_mfma_f32_16x16x32_bf16 v[24:27], v[196:199], a[84:87], v[24:27]\n"
        "v_cvt_pk_bf16_f32 v67, v70, v71\n"
        "v_mul_f32 v112, v124, v112\n"
        "v_mfma_f32_16x16x32_bf16 v[24:27], v[184:187], a[80:83], v[24:27]\n"
        "ds_write_b128 v245, v[64:67] offset:256\n"
        "v_mfma_f32_16x16x32_bf16 v[24:27], v[180:183], a[76:79], v[24:27]\n"
        "v_mul_f32 v113, v124, v113\n"
        "v_mul_f32 v114, v124, v114\n"
        "v_mfma_f32_16x16x32_bf16 v[24:27], v[176:179], a[72:75], v[24:27]\n"
        "s_waitcnt lgkmcnt(7)\n"
        "v_mul_f32 v115, v124, v115\n"
        "v_cvt_pk_bf16_f32 v102, v104, v105\n"
        "v_mfma_f32_16x16x32_bf16 v[20:23], a[224:227], a[192:195], 0\n"
        "s_waitcnt lgkmcnt(6)\n"
        "v_cvt_pk_bf16_f32 v103, v106, v107\n"
        "v_cvt_pk_bf16_f32 v218, v120, v121\n"
        "v_mfma_f32_16x16x32_bf16 v[20:23], a[228:231], a[196:199], v[20:23]\n"
        "s_waitcnt lgkmcnt(5)\n"
        "ds_write_b128 v245, v[100:103] offset:0\n"
        "v_mfma_f32_16x16x32_bf16 v[20:23], a[232:235], a[200:203], v[20:23]\n"
        "s_waitcnt lgkmcnt(5)\n"
        "v_cvt_pk_bf16_f32 v219, v122, v123\n"
        "v_mul_f32 v108, v96, v108\n"
        "v_mfma_f32_16x16x32_bf16 v[20:23], v[156:159], a[204:207], v[20:23]\n"
        "s_waitcnt lgkmcnt(3)\n"
        "v_mul_f32 v109, v96, v109\n"
        "v_mul_f32 v110, v96, v110\n"
        "v_mfma_f32_16x16x32_bf16 v[20:23], v[160:163], a[208:211], v[20:23]\n"
        "s_waitcnt lgkmcnt(2)\n"
        "v_mul_f32 v111, v96, v111\n"
        "v_cvt_pk_bf16_f32 v112, v112, v113\n"
        "v_mfma_f32_16x16x32_bf16 v[20:23], v[164:167], a[212:215], v[20:23]\n"
        "v_cvt_pk_bf16_f32 v113, v114, v115\n"
        "v_cvt_pk_bf16_f32 v108, v108, v109\n"
        "v_mfma_f32_16x16x32_bf16 v[116:119], a[48:51], a[236:239], 0\n"
        "ds_write_b64 v246, v[112:113] offset:256\n"
        "v_mfma_f32_16x16x32_bf16 v[116:119], a[52:55], a[240:243], v[116:119]\n"
        "v_cvt_pk_bf16_f32 v109, v110, v111\n"
        "v_max_u32 v236, v96, v97\n"
        "v_mfma_f32_16x16x32_bf16 v[116:119], a[56:59], a[244:247], v[116:119]\n"
        "ds_write_b64 v246, v[108:109] offset:0\n"
        "v_mfma_f32_16x16x32_bf16 v[116:119], a[60:63], a[248:251], v[116:119]\n"
        "v_max3_u32 v236, v236, v124, v125\n"
        "v_cvt_pk_bf16_f32 v244, v16, v20\n"
        "v_mfma_f32_16x16x32_bf16 v[116:119], a[64:67], a[216:219], v[116:119]\n"
        "buffer_store_dword v244, v238, s[16:19], s38 offen offset:0 sc1\n"
        "v_mfma_f32_16x16x32_bf16 v[116:119], a[68:71], a[220:223], v[116:119]\n"
        "v_cvt_pk_bf16_f32 v239, v17, v21\n"
        "v_cvt_pk_bf16_f32 v255, v18, v22\n"
        "v_mfma_f32_16x16x32_bf16 v[32:35], v[204:207], a[140:143], 0\n"
        "v_cvt_pk_bf16_f32 v244, v19, v23\n"
        "s_nop 0\n"
        "v_mfma_f32_16x16x32_bf16 v[32:35], v[200:203], a[136:139], v[32:35]\n"
        "buffer_store_dword v239, v238, s[16:19], s38 offen offset:384 sc1\n"
        "v_mfma_f32_16x16x32_bf16 v[32:35], v[196:199], a[132:135], v[32:35]\n"
        "v_cvt_pk_bf16_f32 v220, v116, v117\n"
        "v_cvt_pk_bf16_f32 v221, v118, v119\n"
        "v_mfma_f32_16x16x32_bf16 v[32:35], v[184:187], a[128:131], v[32:35]\n"
        "v_cvt_pk_bf16_f32 v226, v116, v117\n"
        "v_cvt_pk_bf16_f32 v227, v118, v119\n"
        "v_mfma_f32_16x16x32_bf16 v[32:35], v[180:183], a[124:127], v[32:35]\n"
        "v_mfma_f32_16x16x32_bf16 v[32:35], v[176:179], a[120:123], v[32:35]\n"
        "buffer_store_dword v255, v238, s[16:19], s38 offen offset:768 sc1\n"
        "v_mfma_f32_16x16x32_bf16 v[0:3], v[152:155], v[216:219], v[48:51]\n"
        ".long 0xbec200ff\n"
        "48:\n"
        "s_branch 61b\n"
        "v_mfma_f32_16x16x32_bf16 v[0:3], v[168:171], v[220:223], v[0:3]\n"
        "s_nop 2\n"
        "v_mfma_f32_16x16x32_bf16 v[4:7], v[172:175], v[216:219], v[52:55]\n"
        "s_nop 2\n"
        "v_mfma_f32_16x16x32_bf16 v[4:7], v[188:191], v[224:227], v[4:7]\n"
        "buffer_store_dword v244, v238, s[16:19], s38 offen offset:1152 sc1\n"
        "v_mfma_f32_16x16x32_bf16 v[28:31], v[176:179], a[96:99], 0\n"
        "ds_read_b128 v[222:225], v252 offset:4608\n"
        "s_nop 0\n"
        "v_mfma_f32_16x16x32_bf16 v[28:31], v[180:183], a[100:103], v[28:31]\n"
        "v_exp_f32 v0, v0\n"
        "v_mfma_f32_16x16x32_bf16 v[28:31], v[184:187], a[104:107], v[28:31]\n"
        "v_exp_f32 v1, v1\n"
        "v_mfma_f32_16x16x32_bf16 v[28:31], v[196:199], a[108:111], v[28:31]\n"
        "v_exp_f32 v2, v2\n"
        "v_mfma_f32_16x16x32_bf16 v[28:31], v[200:203], a[112:115], v[28:31]\n"
        "v_cvt_pk_bf16_f32 v88, v0, v1\n"
        "v_mfma_f32_16x16x32_bf16 v[28:31], v[204:207], a[116:119], v[28:31]\n"
        "v_exp_f32 v4, v4\n"
        "v_mfma_f32_16x16x32_bf16 v[36:39], v[204:207], a[164:167], 0\n"
        "v_exp_f32 v3, v3\n"
        "v_mfma_f32_16x16x32_bf16 v[36:39], v[200:203], a[160:163], v[36:39]\n"
        "v_exp_f32 v5, v5\n"
        "v_mfma_f32_16x16x32_bf16 v[36:39], v[196:199], a[156:159], v[36:39]\n"
        "v_cvt_pk_bf16_f32 v89, v2, v3\n"
        "v_cvt_pk_bf16_f32 v90, v4, v5\n"
        "v_mfma_f32_16x16x32_bf16 v[36:39], v[184:187], a[152:155], v[36:39]\n"
        "v_exp_f32 v6, v6\n"
        "v_mfma_f32_16x16x32_bf16 v[36:39], v[180:183], a[148:151], v[36:39]\n"
        "v_exp_f32 v7, v7\n"
        "v_mfma_f32_16x16x32_bf16 v[36:39], v[176:179], a[144:147], v[36:39]\n"
        "v_cvt_pk_bf16_f32 v91, v6, v7\n"
        "v_mov_b64 v[96:97], 0\n"
        "v_mfma_f32_16x16x32_bf16 v[8:11], v[192:195], v[216:219], v[56:59]\n"
        "s_waitcnt lgkmcnt(0)\n"
        "v_cvt_pk_bf16_f32 v234, v24, v28\n"
        "v_cvt_pk_bf16_f32 v242, v25, v29\n"
        "v_mfma_f32_16x16x32_bf16 v[8:11], v[208:211], v[220:223], v[8:11]\n"
        "v_cvt_pk_bf16_f32 v232, v26, v30\n"
        "s_nop 0\n"
        "v_mfma_f32_16x16x32_bf16 v[44:47], v[176:179], a[192:195], 0\n"
        "v_cvt_pk_bf16_f32 v235, v32, v36\n"
        "v_cvt_pk_bf16_f32 v243, v33, v37\n"
        "v_mfma_f32_16x16x32_bf16 v[44:47], v[180:183], a[196:199], v[44:47]\n"
        "buffer_store_dwordx2 v[234:235], v250, s[16:19], s39 offen offset:0 sc1\n"
        "v_cvt_pk_bf16_f32 v233, v34, v38\n"
        "v_mfma_f32_16x16x32_bf16 v[44:47], v[184:187], a[200:203], v[44:47]\n"
        "v_exp_f32 v8, v8\n"
        "v_mfma_f32_16x16x32_bf16 v[44:47], v[196:199], a[204:207], v[44:47]\n"
        "v_exp_f32 v9, v9\n"
        "v_mfma_f32_16x16x32_bf16 v[44:47], v[200:203], a[208:211], v[44:47]\n"
        "buffer_store_dwordx2 v[242:243], v250, s[16:19], s39 offen offset:384 sc1\n"
        "v_cvt_pk_bf16_f32 v92, v8, v9\n"
        "v_mfma_f32_16x16x32_bf16 v[44:47], v[204:207], a[212:215], v[44:47]\n"
        "v_exp_f32 v10, v10\n"
        "v_mfma_f32_16x16x32_bf16 v[12:15], v[212:215], v[216:219], v[60:63]\n"
        "v_exp_f32 v11, v11\n"
        "v_mfma_f32_16x16x32_bf16 v[12:15], v[228:231], v[224:227], v[12:15]\n"
        "buffer_store_dwordx2 v[232:233], v250, s[16:19], s39 offen offset:768 sc1\n"
        "v_cvt_pk_bf16_f32 v234, v27, v31\n"
        "v_smfmac_f32_16x16x64_bf16 v[96:99], a[252:255], v[80:87], v237\n"
        "v_cvt_pk_bf16_f32 v235, v35, v39\n"
        "v_mfma_f32_16x16x32_bf16 v[72:75], v[132:135], v[84:87], 0\n"
        "s_barrier\n"
        "ds_read_b128 a[0:3], v240 offset:0\n"
        "ds_read_b128 a[4:7], v240 offset:4096\n"
        "v_mfma_f32_16x16x32_bf16 v[72:75], v[128:131], v[80:83], v[72:75]\n"
        "buffer_store_dwordx2 v[234:235], v250, s[16:19], s39 offen offset:1152 sc1\n"
        "ds_read_b128 a[8:11], v240 offset:8192\n"
        "v_mfma_f32_16x16x32_bf16 v[76:79], v[136:139], v[80:83], 0\n"
        "ds_read_b128 a[12:15], v240 offset:12288\n"
        "ds_read_b128 a[16:19], v240 offset:16384\n"
        "v_mfma_f32_16x16x32_bf16 v[76:79], v[140:143], v[84:87], v[76:79]\n"
        "ds_read_b128 a[20:23], v240 offset:20480\n"
        "v_cvt_pk_bf16_f32 v93, v10, v11\n"
        "v_mfma_f32_16x16x32_bf16 v[100:103], v[148:151], v[84:87], 0\n"
        "v_exp_f32 v12, v12\n"
        "v_mfma_f32_16x16x32_bf16 v[100:103], v[144:147], v[80:83], v[100:103]\n"
        "v_exp_f32 v13, v13\n"
        "v_mfma_f32_16x16x32_bf16 v[40:43], v[204:207], a[188:191], 0\n"
        "v_exp_f32 v14, v14\n"
        "v_mfma_f32_16x16x32_bf16 v[40:43], v[200:203], a[184:187], v[40:43]\n"
        "v_cvt_pk_bf16_f32 v94, v12, v13\n"
        "v_mov_b64 v[124:125], 0\n"
        "v_mfma_f32_16x16x32_bf16 v[40:43], v[196:199], a[180:183], v[40:43]\n"
        "v_exp_f32 v15, v15\n"
        "v_mfma_f32_16x16x32_bf16 v[40:43], v[184:187], a[176:179], v[40:43]\n"
        "v_cvt_pk_bf16_f32 v95, v14, v15\n"
        "v_add_f32 v97, v96, v97\n"
        "v_mfma_f32_16x16x32_bf16 v[40:43], v[180:183], a[172:175], v[40:43]\n"
        "v_rcp_f32 v96, v97\n"
        "v_mfma_f32_16x16x32_bf16 v[40:43], v[176:179], a[168:171], v[40:43]\n"
        "s_waitcnt lgkmcnt(5)\n"
        "v_mul_f32 v72, v96, v72\n"
        "v_mul_f32 v73, v96, v73\n"
        "v_mfma_f32_16x16x32_bf16 v[156:159], a[0:3], a[72:75], 0\n"
        "s_waitcnt lgkmcnt(4)\n"
        "v_mul_f32 v74, v96, v74\n"
        "v_mul_f32 v75, v96, v75\n"
        "v_mfma_f32_16x16x32_bf16 v[156:159], a[4:7], a[76:79], v[156:159]\n"
        "s_waitcnt lgkmcnt(3)\n"
        "v_cvt_pk_bf16_f32 v72, v72, v73\n"
        "v_cvt_pk_bf16_f32 v73, v74, v75\n"
        "v_mfma_f32_16x16x32_bf16 v[156:159], a[8:11], a[80:83], v[156:159]\n"
        "s_waitcnt lgkmcnt(2)\n"
        "v_cvt_pk_bf16_f32 v239, v40, v44\n"
        "v_cvt_pk_bf16_f32 v255, v41, v45\n"
        "v_mfma_f32_16x16x32_bf16 v[156:159], a[12:15], a[84:87], v[156:159]\n"
        "s_waitcnt lgkmcnt(1)\n"
        "buffer_store_dword v239, v238, s[16:19], s39 offen offset:0 sc1\n"
        "v_mfma_f32_16x16x32_bf16 v[156:159], a[16:19], a[88:91], v[156:159]\n"
        "s_waitcnt lgkmcnt(0)\n"
        "v_cvt_pk_bf16_f32 v244, v42, v46\n"
        "v_cvt_pk_bf16_f32 v239, v43, v47\n"
        "v_mfma_f32_16x16x32_bf16 v[156:159], a[20:23], a[92:95], v[156:159]\n"
        "buffer_store_dword v255, v238, s[16:19], s39 offen offset:384 sc1\n"
        "v_smfmac_f32_16x16x64_bf16 v[124:127], a[252:255], v[88:95], v237\n"
        "v_mul_f32 v76, v96, v76\n"
        "v_mfma_f32_16x16x32_bf16 v[104:107], v[132:135], v[92:95], 0\n"
        "buffer_store_dword v244, v238, s[16:19], s39 offen offset:768 sc1\n"
        "v_mfma_f32_16x16x32_bf16 v[104:107], v[128:131], v[88:91], v[104:107]\n"
        "v_mul_f32 v77, v96, v77\n"
        "v_mul_f32 v78, v96, v78\n"
        "v_mfma_f32_16x16x32_bf16 v[108:111], v[136:139], v[88:91], 0\n"
        "buffer_store_dword v239, v238, s[16:19], s39 offen offset:1152 sc1\n"
        "v_mfma_f32_16x16x32_bf16 v[108:111], v[140:143], v[92:95], v[108:111]\n"
        "v_mul_f32 v79, v96, v79\n"
        "v_add_f32 v125, v124, v125\n"
        "v_mfma_f32_16x16x32_bf16 v[112:115], v[148:151], v[92:95], 0\n"
        "v_cvt_pk_bf16_f32 v74, v76, v77\n"
        "v_cvt_pk_bf16_f32 v75, v78, v79\n"
        "v_mfma_f32_16x16x32_bf16 v[112:115], v[144:147], v[88:91], v[112:115]\n"
        "ds_write_b128 v245, v[72:75] offset:512\n"
        "v_mfma_f32_16x16x32_bf16 v[176:179], a[20:23], a[164:167], 0\n"
        "v_rcp_f32 v124, v125\n"
        "v_mfma_f32_16x16x32_bf16 v[176:179], a[16:19], a[160:163], v[176:179]\n"
        "v_mul_f32 v100, v96, v100\n"
        "v_mul_f32 v101, v96, v101\n"
        "v_mfma_f32_16x16x32_bf16 v[176:179], a[12:15], a[156:159], v[176:179]\n"
        "v_mul_f32 v102, v96, v102\n"
        "v_mul_f32 v103, v96, v103\n"
        "v_mfma_f32_16x16x32_bf16 v[176:179], a[8:11], a[152:155], v[176:179]\n"
        "v_mul_f32 v104, v124, v104\n"
        "v_mul_f32 v105, v124, v105\n"
        "v_mfma_f32_16x16x32_bf16 v[176:179], a[4:7], a[148:151], v[176:179]\n"
        "v_mul_f32 v106, v124, v106\n"
        "v_mul_f32 v107, v124, v107\n"
        "v_mfma_f32_16x16x32_bf16 v[176:179], a[0:3], a[144:147], v[176:179]\n"
        "v_cvt_pk_bf16_f32 v100, v100, v101\n"
        "v_cvt_pk_bf16_f32 v101, v102, v103\n"
        "v_mfma_f32_16x16x32_bf16 v[160:163], a[0:3], a[96:99], 0\n"
        "ds_write_b64 v246, v[100:101] offset:512\n"
        "v_mfma_f32_16x16x32_bf16 v[160:163], a[4:7], a[100:103], v[160:163]\n"
        "v_cvt_pk_bf16_f32 v104, v104, v105\n"
        "v_cvt_pk_bf16_f32 v105, v106, v107\n"
        "v_mfma_f32_16x16x32_bf16 v[160:163], a[8:11], a[104:107], v[160:163]\n"
        "v_mul_f32 v108, v124, v108\n"
        "v_mul_f32 v109, v124, v109\n"
        "v_mfma_f32_16x16x32_bf16 v[160:163], a[12:15], a[108:111], v[160:163]\n"
        "v_mul_f32 v110, v124, v110\n"
        "v_mul_f32 v111, v124, v111\n"
        "v_mfma_f32_16x16x32_bf16 v[160:163], a[16:19], a[112:115], v[160:163]\n"
        "v_max3_u32 v236, v236, v96, v97\n"
        "v_cvt_pk_bf16_f32 v106, v108, v109\n"
        "v_mfma_f32_16x16x32_bf16 v[160:163], a[20:23], a[116:119], v[160:163]\n"
        "v_cvt_pk_bf16_f32 v107, v110, v111\n"
        "v_mul_f32 v112, v124, v112\n"
        "v_mfma_f32_16x16x32_bf16 v[164:167], a[20:23], a[140:143], 0\n"
        "ds_write_b128 v245, v[104:107] offset:768\n"
        "v_mfma_f32_16x16x32_bf16 v[164:167], a[16:19], a[136:139], v[164:167]\n"
        "v_mul_f32 v113, v124, v113\n"
        "v_mul_f32 v114, v124, v114\n"
        "v_mfma_f32_16x16x32_bf16 v[164:167], a[12:15], a[132:135], v[164:167]\n"
        "v_mul_f32 v115, v124, v115\n"
        "v_cvt_pk_bf16_f32 v112, v112, v113\n"
        "v_mfma_f32_16x16x32_bf16 v[164:167], a[8:11], a[128:131], v[164:167]\n"
        "v_cvt_pk_bf16_f32 v113, v114, v115\n"
        "v_max3_u32 v236, v236, v124, v125\n"
        "v_mfma_f32_16x16x32_bf16 v[164:167], a[4:7], a[124:127], v[164:167]\n"
        "ds_write_b64 v246, v[112:113] offset:768\n"
        "v_mfma_f32_16x16x32_bf16 v[164:167], a[0:3], a[120:123], v[164:167]\n"
        "v_cmp_lt_u32 vcc, s24, v236\n"
        "v_cvt_pk_bf16_f32 v242, v156, v160\n"
        "v_mfma_f32_16x16x32_bf16 v[180:183], a[0:3], a[168:171], 0\n"
        "s_cmp_lg_u64 vcc, 0\n"
        "s_addc_u32 s25, s25, s25\n"
        "v_mov_b32 v253, s25\n"
        "v_mfma_f32_16x16x32_bf16 v[180:183], a[4:7], a[172:175], v[180:183]\n"
        "ds_write_b32 v249, v253\n"
        "v_mfma_f32_16x16x32_bf16 v[180:183], a[8:11], a[176:179], v[180:183]\n"
        "v_cvt_pk_bf16_f32 v243, v164, v176\n"
        "v_cvt_pk_bf16_f32 v232, v157, v161\n"
        "v_mfma_f32_16x16x32_bf16 v[180:183], a[12:15], a[180:183], v[180:183]\n"
        "buffer_store_dwordx2 v[242:243], v250, s[16:19], s47 offen offset:0 sc1\n"
        "v_cvt_pk_bf16_f32 v233, v165, v177\n"
        "v_mfma_f32_16x16x32_bf16 v[180:183], a[16:19], a[184:187], v[180:183]\n"
        "v_cvt_pk_bf16_f32 v234, v158, v162\n"
        "v_cvt_pk_bf16_f32 v235, v166, v178\n"
        "v_mfma_f32_16x16x32_bf16 v[180:183], a[20:23], a[188:191], v[180:183]\n"
        "s_waitcnt lgkmcnt(0)\n"
        "s_barrier\n"
        "ds_read_b128 a[24:27], v240 offset:512\n"
        "ds_read_b128 a[28:31], v240 offset:4608\n"
        "v_mfma_f32_16x16x32_bf16 v[184:187], a[20:23], a[212:215], 0\n"
        "buffer_store_dwordx2 v[232:233], v250, s[16:19], s47 offen offset:384 sc1\n"
        "ds_read_b128 a[32:35], v240 offset:8704\n"
        "v_mfma_f32_16x16x32_bf16 v[184:187], a[16:19], a[208:211], v[184:187]\n"
        "ds_read_b128 a[36:39], v240 offset:12800\n"
        "ds_read_b128 a[40:43], v240 offset:16896\n"
        "v_mfma_f32_16x16x32_bf16 v[184:187], a[12:15], a[204:207], v[184:187]\n"
        "ds_read_b128 a[44:47], v240 offset:20992\n"
        "v_cvt_pk_bf16_f32 v242, v159, v163\n"
        "v_mfma_f32_16x16x32_bf16 v[184:187], a[8:11], a[200:203], v[184:187]\n"
        "buffer_store_dwordx2 v[234:235], v250, s[16:19], s47 offen offset:768 sc1\n"
        "v_cvt_pk_bf16_f32 v243, v167, v179\n"
        "v_mfma_f32_16x16x32_bf16 v[184:187], a[4:7], a[196:199], v[184:187]\n"
        "v_mfma_f32_16x16x32_bf16 v[184:187], a[0:3], a[192:195], v[184:187]\n"
        "s_waitcnt lgkmcnt(5)\n"
        "s_nop 1\n"
        "v_mfma_f32_16x16x32_bf16 v[52:55], a[24:27], a[168:171], 0\n"
        "s_waitcnt lgkmcnt(4)\n"
        "buffer_store_dwordx2 v[242:243], v250, s[16:19], s47 offen offset:1152 sc1\n"
        "s_nop 0\n"
        "v_mfma_f32_16x16x32_bf16 v[52:55], a[28:31], a[172:175], v[52:55]\n"
        "s_waitcnt lgkmcnt(3)\n"
        "ds_read_b128 v[60:63], v241\n"
        "s_nop 0\n"
        "v_mfma_f32_16x16x32_bf16 v[52:55], a[32:35], a[176:179], v[52:55]\n"
        "s_waitcnt lgkmcnt(3)\n"
        "v_cvt_pk_bf16_f32 v255, v180, v184\n"
        "v_cvt_pk_bf16_f32 v244, v181, v185\n"
        "v_mfma_f32_16x16x32_bf16 v[52:55], a[36:39], a[180:183], v[52:55]\n"
        "s_waitcnt lgkmcnt(2)\n"
        "buffer_store_dword v255, v238, s[16:19], s47 offen offset:0 sc1\n"
        "v_mfma_f32_16x16x32_bf16 v[52:55], a[40:43], a[184:187], v[52:55]\n"
        "s_waitcnt lgkmcnt(1)\n"
        "v_cvt_pk_bf16_f32 v239, v182, v186\n"
        "v_cvt_pk_bf16_f32 v255, v183, v187\n"
        "v_mfma_f32_16x16x32_bf16 v[52:55], a[44:47], a[188:191], v[52:55]\n"
        "v_mfma_f32_16x16x32_bf16 v[196:199], a[44:47], a[92:95], 0\n"
        "buffer_store_dword v244, v238, s[16:19], s47 offen offset:384 sc1\n"
        "v_mfma_f32_16x16x32_bf16 v[196:199], a[40:43], a[88:91], v[196:199]\n"
        "v_mfma_f32_16x16x32_bf16 v[196:199], a[36:39], a[84:87], v[196:199]\n"
        "v_mfma_f32_16x16x32_bf16 v[196:199], a[32:35], a[80:83], v[196:199]\n"
        "buffer_store_dword v239, v238, s[16:19], s47 offen offset:768 sc1\n"
        "v_mfma_f32_16x16x32_bf16 v[196:199], a[28:31], a[76:79], v[196:199]\n"
        "v_mfma_f32_16x16x32_bf16 v[196:199], a[24:27], a[72:75], v[196:199]\n"
        "v_mfma_f32_16x16x32_bf16 v[56:59], a[24:27], a[192:195], 0\n"
        "buffer_store_dword v255, v238, s[16:19], s47 offen offset:1152 sc1\n"
        "v_mfma_f32_16x16x32_bf16 v[56:59], a[28:31], a[196:199], v[56:59]\n"
        "v_mfma_f32_16x16x32_bf16 v[56:59], a[32:35], a[200:203], v[56:59]\n"
        "v_mfma_f32_16x16x32_bf16 v[56:59], a[36:39], a[204:207], v[56:59]\n"
        "s_waitcnt lgkmcnt(0)\n"
        "v_or3_b32 v60, v60, v61, v62\n"
        "v_or_b32 v60, v60, v63\n"
        "v_mfma_f32_16x16x32_bf16 v[56:59], a[40:43], a[208:211], v[56:59]\n"
        "s_nop 0\n"
        "v_readfirstlane_b32 s25, v60\n"
        "v_mfma_f32_16x16x32_bf16 v[56:59], a[44:47], a[212:215], v[56:59]\n"
        "s_nop 2\n"
        "v_mfma_f32_16x16x32_bf16 v[204:207], a[44:47], a[140:143], 0\n"
        "s_nop 2\n"
        "v_mfma_f32_16x16x32_bf16 v[204:207], a[40:43], a[136:139], v[204:207]\n"
        "v_cvt_pk_bf16_f32 v88, v52, v56\n"
        "v_cvt_pk_bf16_f32 v90, v53, v57\n"
        "v_mfma_f32_16x16x32_bf16 v[204:207], a[36:39], a[132:135], v[204:207]\n"
        "v_cvt_pk_bf16_f32 v92, v54, v58\n"
        "v_cvt_pk_bf16_f32 v94, v55, v59\n"
        "v_mfma_f32_16x16x32_bf16 v[204:207], a[32:35], a[128:131], v[204:207]\n"
        "buffer_store_dword v88, v238, s[16:19], s35 offen offset:0 sc1\n"
        "v_mfma_f32_16x16x32_bf16 v[204:207], a[28:31], a[124:127], v[204:207]\n"
        "v_mfma_f32_16x16x32_bf16 v[204:207], a[24:27], a[120:123], v[204:207]\n"
        "buffer_store_dword v90, v238, s[16:19], s35 offen offset:384 sc1\n"
        "v_mfma_f32_16x16x32_bf16 v[200:203], a[24:27], a[96:99], 0\n"
        "v_mfma_f32_16x16x32_bf16 v[200:203], a[28:31], a[100:103], v[200:203]\n"
        "buffer_store_dword v92, v238, s[16:19], s35 offen offset:768 sc1\n"
        "v_mfma_f32_16x16x32_bf16 v[200:203], a[32:35], a[104:107], v[200:203]\n"
        "v_mfma_f32_16x16x32_bf16 v[200:203], a[36:39], a[108:111], v[200:203]\n"
        "buffer_store_dword v94, v238, s[16:19], s35 offen offset:1152 sc1\n"
        "v_mfma_f32_16x16x32_bf16 v[200:203], a[40:43], a[112:115], v[200:203]\n"
        "v_mfma_f32_16x16x32_bf16 v[200:203], a[44:47], a[116:119], v[200:203]\n"
        "v_mfma_f32_16x16x32_bf16 v[48:51], a[44:47], a[164:167], 0\n"
        "v_mfma_f32_16x16x32_bf16 v[48:51], a[40:43], a[160:163], v[48:51]\n"
        "s_nop 2\n"
        "v_mfma_f32_16x16x32_bf16 v[48:51], a[36:39], a[156:159], v[48:51]\n"
        "s_nop 1\n"
        "v_cvt_pk_bf16_f32 v80, v196, v200\n"
        "v_mfma_f32_16x16x32_bf16 v[48:51], a[32:35], a[152:155], v[48:51]\n"
        "v_cvt_pk_bf16_f32 v82, v197, v201\n"
        "v_mfma_f32_16x16x32_bf16 v[48:51], a[28:31], a[148:151], v[48:51]\n"
        "v_cvt_pk_bf16_f32 v84, v198, v202\n"
        "v_mfma_f32_16x16x32_bf16 v[48:51], a[24:27], a[144:147], v[48:51]\n"
        "s_nop 2\n"
        "v_cvt_pk_bf16_f32 v86, v199, v203\n"
        "s_nop 3\n"
        "v_cvt_pk_bf16_f32 v81, v204, v48\n"
        "buffer_store_dwordx2 v[80:81], v250, s[16:19], s35 offen offset:0 sc1\n"
        "v_cvt_pk_bf16_f32 v83, v205, v49\n"
        "buffer_store_dwordx2 v[82:83], v250, s[16:19], s35 offen offset:384 sc1\n"
        "v_cvt_pk_bf16_f32 v85, v206, v50\n"
        "buffer_store_dwordx2 v[84:85], v250, s[16:19], s35 offen offset:768 sc1\n"
        "v_cvt_pk_bf16_f32 v87, v207, v51\n"
        "buffer_store_dwordx2 v[86:87], v250, s[16:19], s35 offen offset:1152 sc1\n"
        "s_cmp_lg_u32 s25, 0\n"
        "s_cbranch_scc1 30f\n"
        "s_endpgm\n"
        "30:\n"
        : "={s25}"(flg), "={s42}"(w), "+{s[0:1]}"(ka), "+{s2}"(bid), "+{v0}"(tid)
        : "s"(ldsb)
        : "v1", "v2", "v3", "v4", "v5", "v6", "v7", "v8", "v9", "v10", "v11", "v12", "v13", "v14", "v15", "v16", "v17", "v18", "v19", "v20", "v21", "v22", "v23", "v24", "v25", "v26", "v27", "v28", "v29", "v30", "v31", "v32", "v33", "v34", "v35", "v36", "v37", "v38", "v39", "v40", "v41", "v42", "v43", "v44", "v45", "v46", "v47", "v48", "v49", "v50", "v51", "v52", "v53", "v54", "v55", "v56", "v57", "v58", "v59", "v60", "v61", "v62", "v63", "v64", "v65", "v66", "v67", "v68", "v69", "v70", "v71", "v72", "v73", "v74", "v75", "v76", "v77", "v78", "v79", "v80", "v81", "v82", "v83", "v84", "v85", "v86", "v87", "v88", "v89", "v90", "v91", "v92", "v93", "v94", "v95", "v96", "v97", "v98", "v99", "v100", "v101", "v102", "v103", "v104", "v105", "v106", "v107", "v108", "v109", "v110", "v111", "v112", "v113", "v114", "v115", "v116", "v117", "v118", "v119", "v120", "v121", "v122", "v123", "v124", "v125", "v126", "v127", "v128", "v129", "v130", "v131", "v132", "v133", "v134", "v135", "v136", "v137", "v138", "v139", "v140", "v141", "v142", "v143", "v144", "v145", "v146", "v147", "v148", "v149", "v150", "v151", "v152", "v153", "v154", "v155", "v156", "v157", "v158", "v159", "v160", "v161", "v162", "v163", "v164", "v165", "v166", "v167", "v168", "v169", "v170", "v171", "v172", "v173", "v174", "v175", "v176", "v177", "v178", "v179", "v180", "v181", "v182", "v183", "v184", "v185", "v186", "v187", "v188", "v189", "v190", "v191", "v192", "v193", "v194", "v195", "v196", "v197", "v198", "v199", "v200", "v201", "v202", "v203", "v204", "v205", "v206", "v207", "v208", "v209", "v210", "v211", "v212", "v213", "v214", "v215", "v216", "v217", "v218", "v219", "v220", "v221", "v222", "v223", "v224", "v225", "v226", "v227", "v228", "v229", "v230", "v231", "v232", "v233", "v234", "v235", "v236", "v237", "v238", "v239", "v240", "v241", "v242", "v243", "v244", "v245", "v246", "v247", "v248", "v249", "v250", "v251", "v252", "v253", "v254", "v255", "a0", "a1", "a2", "a3", "a4", "a5", "a6", "a7", "a8", "a9", "a10", "a11", "a12", "a13", "a14", "a15", "a16", "a17", "a18", "a19", "a20", "a21", "a22", "a23", "a24", "a25", "a26", "a27", "a28", "a29", "a30", "a31", "a32", "a33", "a34", "a35", "a36", "a37", "a38", "a39", "a40", "a41", "a42", "a43", "a44", "a45", "a46", "a47", "a48", "a49", "a50", "a51", "a52", "a53", "a54", "a55", "a56", "a57", "a58", "a59", "a60", "a61", "a62", "a63", "a64", "a65", "a66", "a67", "a68", "a69", "a70", "a71", "a72", "a73", "a74", "a75", "a76", "a77", "a78", "a79", "a80", "a81", "a82", "a83", "a84", "a85", "a86", "a87", "a88", "a89", "a90", "a91", "a92", "a93", "a94", "a95", "a96", "a97", "a98", "a99", "a100", "a101", "a102", "a103", "a104", "a105", "a106", "a107", "a108", "a109", "a110", "a111", "a112", "a113", "a114", "a115", "a116", "a117", "a118", "a119", "a120", "a121", "a122", "a123", "a124", "a125", "a126", "a127", "a128", "a129", "a130", "a131", "a132", "a133", "a134", "a135", "a136", "a137", "a138", "a139", "a140", "a141", "a142", "a143", "a144", "a145", "a146", "a147", "a148", "a149", "a150", "a151", "a152", "a153", "a154", "a155", "a156", "a157", "a158", "a159", "a160", "a161", "a162", "a163", "a164", "a165", "a166", "a167", "a168", "a169", "a170", "a171", "a172", "a173", "a174", "a175", "a176", "a177", "a178", "a179", "a180", "a181", "a182", "a183", "a184", "a185", "a186", "a187", "a188", "a189", "a190", "a191", "a192", "a193", "a194", "a195", "a196", "a197", "a198", "a199", "a200", "a201", "a202", "a203", "a204", "a205", "a206", "a207", "a208", "a209", "a210", "a211", "a212", "a213", "a214", "a215", "a216", "a217", "a218", "a219", "a220", "a221", "a222", "a223", "a224", "a225", "a226", "a227", "a228", "a229", "a230", "a231", "a232", "a233", "a234", "a235", "a236", "a237", "a238", "a239", "a240", "a241", "a242", "a243", "a244", "a245", "a246", "a247", "a248", "a249", "a250", "a251", "a252", "a253", "a254", "a255", "s4", "s5", "s6", "s7", "s8", "s9", "s10", "s11", "s12", "s13", "s14", "s15", "s16", "s17", "s18", "s19", "s20", "s21", "s22", "s23", "s24", "s26", "s27", "s28", "s29", "s30", "s31", "s34", "s35", "s36", "s37", "s38", "s39", "s40", "s41", "s43", "s44", "s45", "s46", "s47", "s48", "s49", "s50", "s51", "s52", "s53", "s54", "s55", "s56", "s57", "s58", "s59", "s60", "s61", "s62", "s63", "s64", "s65", "s66", "s67", "s68", "s69", "s70", "s71", "s72", "s73", "s74", "s75", "s76", "s77", "s78", "s79", "s80", "s81", "s82", "s83", "vcc", "scc", "memory");
    if (flg) {
        unsigned ln;
        asm volatile("s_waitcnt vmcnt(0)\n v_mbcnt_lo_u32_b32 %0, -1, 0\n v_mbcnt_hi_u32_b32 %0, -1, %0" : "=v"(ln) :: "memory");
        const unsigned t = w * 64u + ln, wx = bid & 63u, wy0 = bid >> 6;
        typedef const char* __attribute__((address_space(4))) const* kargp;
        const char *x = ((kargp)ka)[0], *wqkv = ((kargp)ka)[1], *wo = ((kargp)ka)[2], *rpb = ((kargp)ka)[3], *rpi = ((kargp)ka)[4],
                   *mask = ((kargp)ka)[5];
        char* out = (char*)((kargp)ka)[6];
        unsigned short* xs = (unsigned short*)smem;
        unsigned short* qs = xs + 64 * 192;
        const unsigned short* W1 = (const unsigned short*)wqkv;
        const unsigned short* W2 = (const unsigned short*)wo;
        for (unsigned i = 0; i < 16u; ++i) {
            if (!((flg >> (15u - i)) & 1u)) continue;
            const unsigned wy = wy0 + 4u * i;
            __syncthreads();
            for (unsigned e = t; e < 64u * 96u; e += 256u) {
                const unsigned tok = e / 96u, c2 = e % 96u;
                const unsigned pix = ((8u * wy + 4u + (tok >> 3)) & 511u) * 512u + ((8u * wx + 4u + (tok & 7u)) & 511u);
                ((unsigned*)xs)[e] = ((const unsigned*)x)[pix * 96u + c2];
            }
            __syncthreads();
            for (unsigned col = t; col < 576u; col += 256u)
                for (unsigned tb = 0; tb < 64u; tb += 8u) {
                    float a[8] = {0.f, 0.f, 0.f, 0.f, 0.f, 0.f, 0.f, 0.f};
                    for (unsigned c = 0; c < 192u; ++c) {
                        const float wt = bf2f(W1[c * 576u + col]);
#pragma unroll
                        for (int j = 0; j < 8; ++j) a[j] += bf2f(xs[(tb + j) * 192u + c]) * wt;
                    }
#pragma unroll
                    for (int j = 0; j < 8; ++j) qs[(tb + j) * 578u + col] = f2bf(a[j]);
                }
            __syncthreads();
            {
                float qv[48], o[48];
#pragma unroll
                for (int d = 0; d < 48; ++d) { qv[d] = bf2f(qs[ln * 578u + w * 48u + d]); o[d] = 0.f; }
                const unsigned short* mrow = (const unsigned short*)mask + (size_t)(wy * 64u + wx) * 4096u + ln * 64u;
                float mx = -__builtin_inff(), l = 0.f;
                for (int pass = 0; pass < 2; ++pass)
                    for (unsigned k = 0; k < 64u; ++k) {
                        float s = 0.f;
#pragma unroll
                        for (int d = 0; d < 48; ++d) s += qv[d] * bf2f(qs[k * 578u + 192u + w * 48u + d]);
                        s = s * 0.14433756729740643f + bf2f(((const unsigned short*)rpb)[((const unsigned*)rpi)[ln * 64u + k] * 4u + w]) + bf2f(mrow[k]);
                        if (pass == 0) mx = fmaxf(mx, s);
                        else {
                            const float p = __expf(s - mx);
                            l += p;
#pragma unroll
                            for (int d = 0; d < 48; ++d) o[d] += p * bf2f(qs[k * 578u + 384u + w * 48u + d]);
                        }
                    }
#pragma unroll
                for (int d = 0; d < 48; ++d) xs[ln * 192u + w * 48u + d] = f2bf(o[d] / l);
            }
            __syncthreads();
            if (t < 192u)
                for (unsigned tb = 0; tb < 64u; tb += 8u) {
                    float a[8] = {0.f, 0.f, 0.f, 0.f, 0.f, 0.f, 0.f, 0.f};
                    for (unsigned c = 0; c < 192u; ++c) {
                        const float wt = bf2f(W2[c * 192u + t]);
#pragma unroll
                        for (int j = 0; j < 8; ++j) a[j] += bf2f(xs[(tb + j) * 192u + c]) * wt;
                    }
                    const unsigned row = (8u * wy + 4u + (tb >> 3)) & 511u;
#pragma unroll
                    for (int j = 0; j < 8; ++j)
                        ((unsigned short*)out)[(row * 512u + ((8u * wx + 4u + j) & 511u)) * 192u + t] = f2bf(a[j]);
                }
        }
    }
}


extern "C" void kernel_launch(void* const* d_in, const int* in_sizes, int n_in, void* d_out, int out_size, void* d_ws,
                              size_t ws_size, hipStream_t stream) {
    win_attn_kernel<<<256, 256, 0, stream>>>((const char*)d_in[0], (const char*)d_in[1], (const char*)d_in[2],
                                             (const char*)d_in[3], (const char*)d_in[4], (const char*)d_in[5],
                                             (char*)d_out, (char*)d_ws);

}
```

```cpp
#include <hip/hip_runtime.h>
#include <hip/hip_bf16.h>

typedef unsigned int u32x4 __attribute__((ext_vector_type(4)));

static __device__ __forceinline__ float bf2f(unsigned short u) { return __uint_as_float((unsigned)u << 16); }
static __device__ __forceinline__ unsigned short f2bf(float f) { return __builtin_bit_cast(unsigned short, (__bf16)f); }

__global__ __launch_bounds__(256, 1) __attribute__((aligned(4096))) void win_attn_kernel(const char* x_, const char* wqkv_, const char* wo_, const char* rpb_,
                                                          const char* rpi_, const char* mask_, char* out_, char* ws) {
    __shared__ __attribute__((aligned(16))) char smem[163840];
    const void* ka = (const void*)__builtin_amdgcn_kernarg_segment_ptr();
    unsigned bid = blockIdx.x, tid = threadIdx.x, flg, w;
    unsigned ldsb = (unsigned)(size_t)smem;
    asm volatile(
        "s_load_dwordx16 s[68:83], s[0:1], 0\n"
        "v_readfirstlane_b32 s42, v0\n"
        "v_and_b32 v1, 63, v0\n"
        "s_lshr_b32 s31, s2, 1\n"
        "s_lshr_b32 s42, s42, 6\n"
        "s_and_b32 s31, s31, 0x7c\n"
        "v_lshlrev_b32 v2, 4, v1\n"
        "s_add_u32 s31, s31, s42\n"
        "s_mul_i32 s35, s31, 320\n"
        "v_min_u32 v10, 4, v1\n"
        "s_getpc_b64 s[66:67]\n"
        "v_lshl_add_u32 v10, v10, 6, s35\n"
        "global_load_dword v60, v10, s[66:67]\n"
        "s_lshl_b32 s50, s31, 6\n"
        "s_mul_i32 s50, s50, 27\n"
        "v_min_u32 v136, 26, v1\n"
        "v_lshl_add_u32 v136, v136, 6, s50\n"
        "v_mul_u32_u24 v11, 171, v1\n"
        "v_lshrrev_b32 v4, 10, v11\n"
        "v_bfe_u32 v7, v11, 13, 1\n"
        "v_add_u32 v11, 10944, v11\n"
        "v_lshrrev_b32 v5, 10, v11\n"
        "v_bfe_u32 v8, v11, 13, 1\n"
        "v_add_u32 v11, 10944, v11\n"
        "v_lshrrev_b32 v6, 10, v11\n"
        "v_bfe_u32 v9, v11, 13, 1\n"
        "s_mul_i32 s40, s42, 3072\n"
        "s_mov_b32 s10, 100663296\n"
        "s_mov_b32 s11, 0x20000\n"
        "s_mov_b32 s6, 16384\n"
        "s_mov_b32 s7, 0x20000\n"
        "s_mov_b32 s62, 1800\n"
        "s_mov_b32 s63, 0x20000\n"
        "s_mov_b32 s22, 221184\n"
        "s_mov_b32 s23, 0x20000\n"
        "s_and_b32 s51, s42, 1\n"
        "v_lshlrev_b32 v134, 3, v0\n"
        "s_lshl_b32 s35, s51, 7\n"
        "v_and_b32 v10, 48, v1\n"
        "v_xor_b32 v131, s35, v2\n"
        "s_lshl_b32 s46, s42, 12\n"
        "v_xor_b32 v131, v131, v10\n"
        "v_xor_b32 v132, 64, v131\n"
        "s_and_b32 s44, s2, 63\n"
        "s_lshl3_add_u32 s44, s44, 4\n"
        "s_lshl_b32 s35, s42, 5\n"
        "s_movk_i32 s41, 384\n"
        "s_movk_i32 s45, -384\n"
        "v_lshrrev_b32 v10, 2, v4\n"
        "v_mad_i32_i24 v11, v10, s45, v2\n"
        "v_add_u32 v10, s44, v10\n"
        "v_lshl_or_b32 v242, v7, 4, s35\n"
        "v_and_b32 v10, 511, v10\n"
        "v_xor_b32 v11, v11, v242\n"
        "v_mad_u32_u24 v242, v10, s41, v11\n"
        "v_lshrrev_b32 v10, 2, v5\n"
        "v_mad_i32_i24 v11, v10, s45, v2\n"
        "v_add_u32 v10, s44, v10\n"
        "v_add_u32 v11, 1024, v11\n"
        "v_lshl_or_b32 v243, v8, 4, s35\n"
        "v_and_b32 v10, 511, v10\n"
        "v_xor_b32 v11, v11, v243\n"
        "v_mad_u32_u24 v243, v10, s41, v11\n"
        "v_lshrrev_b32 v10, 2, v6\n"
        "v_mad_i32_i24 v11, v10, s45, v2\n"
        "v_add_u32 v10, s44, v10\n"
        "v_add_u32 v11, 2048, v11\n"
        "v_lshl_or_b32 v244, v9, 4, s35\n"
        "v_and_b32 v10, 511, v10\n"
        "v_xor_b32 v11, v11, v244\n"
        "v_mad_u32_u24 v244, v10, s41, v11\n"
        "s_mul_i32 s43, s44, 384\n"
        "s_add_u32 s44, s44, 4\n"
        "s_and_b32 s44, s44, 511\n"
        "s_mul_i32 s44, s44, 384\n"
        "s_lshr_b32 s37, s2, 6\n"
        "s_lshl3_add_u32 s47, s37, s42\n"
        "s_add_u32 s47, s47, 68\n"
        "s_mul_i32 s47, s47, 196608\n"
        "s_lshl1_add_u32 s37, s37, s51\n"
        "s_add_u32 s37, s37, 1\n"
        "s_mul_i32 s37, s37, 786432\n"
        "s_mul_i32 s65, s42, 18432\n"
        "s_mov_b32 s14, 33554432\n"
        "s_mov_b32 s15, 0x20000\n"
        "s_mul_i32 s35, s42, 96\n"
        "s_movk_i32 s31, 1056\n"
        "s_movk_i32 s45, 4608\n"
        "v_add_u32 v3, s35, v2\n"
        "s_waitcnt lgkmcnt(0)\n"
        "s_mov_b64 s[60:61], s[74:75]\n"
        "s_mov_b64 s[20:21], s[70:71]\n"
        "s_mov_b64 s[4:5], s[76:77]\n"
        "s_mov_b64 s[8:9], s[68:69]\n"
        "buffer_load_dwordx2 v[144:145], v134, s[60:63], 0 offen\n"
        "s_add_u32 m0, s46, 122880\n"
        "s_mov_b32 s64, s46\n"
        "buffer_load_dword v136, s[20:23], 0 offen lds\n"
        "s_mov_b64 s[52:53], s[72:73]\n"
        "s_mov_b64 s[12:13], s[78:79]\n"
        "buffer_load_dwordx4 v131, s[4:7], s64 offen lds\n"
        "s_mov_b64 s[16:17], s[80:81]\n"
        "v_bfe_u32 v12, v4, 2, 1\n"
        "v_mad_u32_u24 v128, v4, s31, v3\n"
        "v_sub_u32 v12, v12, v7\n"
        "v_mad_i32_i24 v128, v12, s45, v128\n"
        "v_bfe_u32 v13, v5, 2, 1\n"
        "buffer_load_dwordx4 v131, s[4:7], s64 offen offset:1024 lds\n"
        "v_mad_u32_u24 v129, v5, s31, v3\n"
        "v_sub_u32 v13, v13, v8\n"
        "v_mad_i32_i24 v129, v13, s45, v129\n"
        "v_bfe_u32 v14, v6, 2, 1\n"
        "v_mad_u32_u24 v130, v6, s31, v3\n"
        "v_sub_u32 v14, v14, v9\n"
        "v_mad_i32_i24 v130, v14, s45, v130\n"
        "s_lshl_b32 s48, s42, 11\n"
        "s_lshl_b32 s36, s2, 13\n"
        "v_and_b32 v19, 16, v1\n"
        "buffer_load_dwordx4 v132, s[4:7], s64 offen offset:2048 lds\n"
        "s_add_u32 s48, s48, 155648\n"
        "s_add_u32 s36, s36, s48\n"
        "s_add_u32 s36, s36, 4038656\n"
        "s_lshl_b32 s35, s51, 6\n"
        "v_xor_b32 v249, v2, v19\n"
        "v_xor_b32 v249, s35, v249\n"
        "v_xor_b32 v253, 32, v249\n"
        "v_and_b32 v15, 15, v1\n"
        "v_lshrrev_b32 v16, 4, v1\n"
        "v_and_b32 v17, 12, v1\n"
        "buffer_load_dwordx4 v132, s[4:7], s64 offen offset:3072 lds\n"
        "v_and_b32 v18, 0xf0, v2\n"
        "v_add_u32 v17, v17, v15\n"
        "v_bfe_u32 v10, v1, 2, 3\n"
        "v_lshrrev_b32 v11, 5, v1\n"
        "v_lshl_or_b32 v10, v11, 4, v10\n"
        "v_and_b32 v255, 3, v1\n"
        "v_mul_u32_u24 v10, 96, v10\n"
        "v_lshl_add_u32 v255, v255, 3, s65\n"
        "v_add_u32 v255, v255, v10\n"
        "v_lshrrev_b32 v20, 1, v15\n"
        "s_add_u32 m0, s40, 73728\n"
        "s_sub_u32 s64, s47, 12582912\n"
        "buffer_load_dwordx4 v242, s[8:11], s64 offen lds\n"
        "v_xor_b32 v20, v20, v16\n"
        "v_lshlrev_b32 v20, 4, v20\n"
        "v_mad_u32_u24 v240, v17, s41, v20\n"
        "v_lshl_or_b32 v251, v17, 7, v20\n"
        "v_add_u32 v240, 73728, v240\n"
        "v_add_u32 v251, 155648, v251\n"
        "v_xor_b32 v241, 64, v240\n"
        "v_xor_b32 v252, 64, v251\n"
        "s_lshl_b32 s67, s42, 10\n"
        "s_add_u32 s67, s67, 139264\n"
        "s_add_u32 m0, s40, 74752\n"
        "s_nop 0\n"
        "buffer_load_dwordx4 v243, s[8:11], s64 offen lds\n"
        "v_lshlrev_b32 v135, 2, v0\n"
        "v_add_u32 v135, 139264, v135\n"
        "v_lshlrev_b32 v11, 5, v16\n"
        "v_xor_b32 v11, v11, v18\n"
        "v_lshl_or_b32 v11, v17, 8, v11\n"
        "v_add_u32 v140, 122880, v11\n"
        "v_xor_b32 v141, 16, v140\n"
        "v_xor_b32 v142, 128, v140\n"
        "v_xor_b32 v143, 144, v140\n"
        "v_lshrrev_b32 v10, 2, v15\n"
        "s_add_u32 m0, s40, 75776\n"
        "s_nop 0\n"
        "buffer_load_dwordx4 v244, s[8:11], s64 offen lds\n"
        "v_cmp_eq_u32 vcc, v10, v16\n"
        "v_mov_b32 v11, 0x3fb9\n"
        "v_mov_b32 v189, 0\n"
        "v_and_b32 v10, 48, v2\n"
        "v_cndmask_b32 v188, 0, v11, vcc\n"
        "v_lshlrev_b64 v[188:189], v10, v[188:189]\n"
        "v_cmp_gt_u32 vcc, 32, v1\n"
        "v_mov_b32 v11, 15957\n"
        "v_lshrrev_b32 v149, 2, v15\n"
        "v_lshrrev_b32 v151, 3, v19\n"
        "v_mov_b32 v148, 14702\n"
        "v_cndmask_b32 v11, v148, v11, vcc\n"
        "v_xor_b32 v151, v149, v151\n"
        "v_cmp_eq_u32 vcc, 0, v151\n"
        "v_mov_b32 v149, 0\n"
        "v_cndmask_b32 v148, 0, v11, vcc\n"
        "v_lshlrev_b64 v[148:149], v10, v[148:149]\n"
        "v_cmp_eq_u32 vcc, 1, v151\n"
        "v_mov_b32 v151, 0\n"
        "v_cndmask_b32 v150, 0, v11, vcc\n"
        "v_lshlrev_b64 v[150:151], v10, v[150:151]\n"
        "v_and_b32 v10, 8, v1\n"
        "v_add_u32 v10, v10, v15\n"
        "v_mul_u32_u24 v10, 96, v10\n"
        "v_add3_u32 v137, v10, v19, s65\n"
        "v_and_b32 v237, 1, v1\n"
        "v_mul_u32_u24 v237, 0xaaaa, v237\n"
        "v_mov_b64 v[170:171], v[188:189]\n"
        "v_mov_b64 v[210:211], v[188:189]\n"
        "v_mov_b64 v[228:229], v[188:189]\n"
        "v_add_u32 v237, 0x4444, v237\n"
        "v_mov_b32 v11, 0x3f803f80\n"
        "v_accvgpr_write_b32 a252, v11\n"
        "v_accvgpr_write_b32 a253, v11\n"
        "v_accvgpr_write_b32 a254, v11\n"
        "v_accvgpr_write_b32 a255, v11\n"
        "v_lshl_or_b32 v10, v16, 10, v18\n"
        "s_lshl_b32 s31, s42, 12\n"
        "s_add_u32 s31, s31, 73728\n"
        "s_lshl_b32 s35, s51, 9\n"
        "s_add_u32 s35, s35, 73728\n"
        "v_add_u32 v245, s31, v10\n"
        "s_sub_u32 s50, s35, s31\n"
        "v_lshrrev_b32 v10, 5, v1\n"
        "v_lshrrev_b32 v11, 1, v19\n"
        "s_sub_u32 s31, s48, 65536\n"
        "v_lshl_or_b32 v10, v10, 10, v18\n"
        "v_add3_u32 v246, v10, v11, s31\n"
        "v_mul_u32_u24 v10, 384, v16\n"
        "s_movk_i32 s45, 1728\n"
        "v_lshrrev_b32 v235, 2, v15\n"
        "v_lshrrev_b32 v254, 1, v15\n"
        "v_and_b32 v11, 1, v1\n"
        "v_mad_u32_u24 v235, v235, 48, v10\n"
        "v_and_b32 v248, 1, v254\n"
        "v_mad_u32_u24 v235, v248, s45, v235\n"
        "v_mul_u32_u24 v248, 36, v11\n"
        "v_lshlrev_b32 v11, 5, v11\n"
        "v_xor_b32 v254, v254, v248\n"
        "v_add3_u32 v235, v235, v11, s65\n"
        "v_mad_u32_u24 v254, v254, 48, v10\n"
        "v_add3_u32 v254, v254, 16, s65\n"
        "s_lshr_b32 s31, s42, 1\n"
        "s_mul_i32 s35, s31, 36864\n"
        "s_movk_i32 s45, 18048\n"
        "v_mad_u32_u24 v10, v15, 48, v10\n"
        "v_lshrrev_b32 v248, 3, v15\n"
        "v_mad_u32_u24 v10, v248, s45, v10\n"
        "v_add_u32 v248, s35, v10\n"
        "s_lshr_b32 s49, s42, 1\n"
        "s_mul_i32 s49, s49, -192\n"
        "s_add_u32 s49, s49, 128\n"
        "s_mul_i32 s31, s31, 256\n"
        "v_mul_u32_u24 v10, 196608, v16\n"
        "v_mul_u32_u24 v11, 8, v15\n"
        "v_add3_u32 v250, v10, v11, s31\n"
        "s_lshl_b32 s35, s51, 6\n"
        "s_add_u32 s66, s31, s35\n"
        "s_lshl_b32 s35, s42, 5\n"
        "s_sub_u32 s35, s35, s66\n"
        "s_add_u32 s27, s35, 64\n"
        "s_movk_i32 s31, 288\n"
        "s_movk_i32 s35, 3072\n"
        "s_movk_i32 s41, 1536\n"
        "s_movk_i32 s45, 12288\n"
        "v_mad_u32_u24 v10, v4, s31, v2\n"
        "v_bfe_u32 v11, v10, 6, 1\n"
        "v_add_u32 v10, s66, v10\n"
        "v_mad_i32_i24 v10, v11, s27, v10\n"
        "v_lshrrev_b32 v11, 4, v4\n"
        "v_mad_i32_i24 v238, v12, s41, v10\n"
        "v_sub_u32 v7, v7, v11\n"
        "v_mad_u32_u24 v238, v11, s45, v238\n"
        "v_mad_i32_i24 v232, v7, s35, v10\n"
        "v_mad_u32_u24 v10, v5, s31, v2\n"
        "v_bfe_u32 v11, v10, 6, 1\n"
        "v_add_u32 v10, s66, v10\n"
        "v_mad_i32_i24 v10, v11, s27, v10\n"
        "v_lshrrev_b32 v11, 4, v5\n"
        "v_mad_i32_i24 v239, v13, s41, v10\n"
        "v_sub_u32 v8, v8, v11\n"
        "v_mad_u32_u24 v239, v11, s45, v239\n"
        "v_mad_i32_i24 v233, v8, s35, v10\n"
        "v_mad_u32_u24 v10, v6, s31, v2\n"
        "v_bfe_u32 v11, v10, 6, 1\n"
        "v_add_u32 v10, s66, v10\n"
        "v_mad_i32_i24 v10, v11, s27, v10\n"
        "v_lshrrev_b32 v11, 4, v6\n"
        "v_mad_i32_i24 v247, v14, s41, v10\n"
        "v_sub_u32 v9, v9, v11\n"
        "v_mad_u32_u24 v247, v11, s45, v247\n"
        "v_mad_i32_i24 v234, v9, s35, v10\n"
        "s_mov_b32 s26, 0x3fb8aa3b\n"
        "s_mov_b32 s24, 0x5d800000\n"
        "s_mov_b32 s27, 14\n"
        "s_mov_b32 s28, 98304\n"
        "s_mov_b32 s29, 73728\n"
        "s_mov_b32 s30, 122880\n"
        "s_mov_b32 s45, s50\n"
        "s_mov_b32 s25, 0\n"
        "s_mov_b32 s54, 73728\n"
        "s_mov_b32 s55, 0x20000\n"
        "s_mov_b32 s18, 100663296\n"
        "s_mov_b32 s19, 0x20000\n"
        "s_mov_b32 s56, 36864\n"
        "s_mov_b32 s57, 73728\n"
        "s_mov_b32 s58, 110592\n"
        "s_mov_b32 s59, 147456\n"
        "s_mov_b32 s60, 184320\n"
        "s_mov_b32 s61, 18432\n"
        "s_mov_b32 s62, 55296\n"
        "s_mov_b32 s63, 12288\n"
        "s_mov_b32 s34, 49152\n"
        "s_add_u32 m0, s40, 86016\n"
        "s_sub_u32 s64, s47, 11796480\n"
        "buffer_load_dwordx4 v242, s[8:11], s64 offen lds\n"
        "s_waitcnt vmcnt(9)\n"
        "v_and_b32 v146, 0xffff0000, v144\n"
        "v_and_b32 v147, 0xffff0000, v145\n"
        "v_lshlrev_b32 v144, 16, v144\n"
        "v_lshlrev_b32 v145, 16, v145\n"
        "s_add_u32 m0, s40, 87040\n"
        "v_mul_f32 v146, s26, v146\n"
        "buffer_load_dwordx4 v243, s[8:11], s64 offen lds\n"
        "v_mul_f32 v147, s26, v147\n"
        "v_mul_f32 v144, s26, v144\n"
        "v_mul_f32 v145, s26, v145\n"
        "ds_write2st64_b32 v135, v144, v146 offset0:0 offset1:4\n"
        "ds_write2st64_b32 v135, v145, v147 offset0:8 offset1:12\n"
        "s_cmp_eq_u32 s42, 0\n"
        "s_cbranch_scc1 41f\n"
        "s_cmp_eq_u32 s42, 1\n"
        "s_cbranch_scc1 42f\n"
        "s_cmp_eq_u32 s42, 2\n"
        "s_cbranch_scc1 43f\n"
        "s_cmp_eq_u32 s42, 3\n"
        "s_cbranch_scc1 44f\n"
        "60:\n"
        "s_add_u32 m0, s40, 88064\n"
        "s_nop 0\n"
        "buffer_load_dwordx4 v244, s[8:11], s64 offen lds\n"
        "s_waitcnt lgkmcnt(0)\n"
        "s_waitcnt vmcnt(6)\n"
        "s_barrier\n"
        "s_add_u32 m0, s65, 0\n"
        "ds_read_b128 v[0:3], v140 offset:0\n"
        "buffer_load_dwordx4 v128, s[20:23], 0 offen lds\n"
        "buffer_load_dwordx4 v129, s[20:23], 0 offen offset:1024 lds\n"
        "ds_read_b128 v[4:7], v141 offset:0\n"
        "ds_read_b128 v[16:19], v140 offset:1024\n"
        "ds_read_b128 v[20:23], v141 offset:1024\n"
        "ds_read_b128 v[8:11], v142 offset:0\n"
        "ds_read_b128 v[24:27], v142 offset:1024\n"
        "ds_read_b128 v[12:15], v143 offset:0\n"
        "ds_read_b128 v[28:31], v143 offset:1024\n"
        "ds_read_b128 v[32:35], v140 offset:8192\n"
        "ds_read_b128 v[36:39], v141 offset:8192\n"
        "ds_read_b128 v[40:43], v142 offset:8192\n"
        "s_cmp_eq_u32 s42, 0\n"
        "s_cbranch_scc1 51f\n"
        "s_cmp_eq_u32 s42, 1\n"
        "s_cbranch_scc1 48f\n"
        "s_cmp_eq_u32 s42, 2\n"
        "s_cbranch_scc1 45f\n"
        "s_cmp_eq_u32 s42, 3\n"
        "s_cbranch_scc1 47f\n"
        "61:\n"
        "buffer_load_dwordx4 v130, s[20:23], 0 offen offset:2048 lds\n"
        "s_add_u32 m0, s65, 2688\n"
        "s_waitcnt lgkmcnt(10)\n"
        "v_lshl_add_u32 v0, v0, 2, s67\n"
        "buffer_load_dwordx4 v128, s[20:23], 0 offen offset:384 lds\n"
        "v_lshl_add_u32 v1, v1, 2, s67\n"
        "v_lshl_add_u32 v2, v2, 2, s67\n"
        "v_lshl_add_u32 v3, v3, 2, s67\n"
        "s_waitcnt lgkmcnt(9)\n"
        "v_lshl_add_u32 v4, v4, 2, s67\n"
        "v_lshl_add_u32 v5, v5, 2, s67\n"
        "v_lshl_add_u32 v6, v6, 2, s67\n"
        "v_lshl_add_u32 v7, v7, 2, s67\n"
        "s_waitcnt lgkmcnt(8)\n"
        "v_lshl_add_u32 v16, v16, 2, s67\n"
        "v_lshl_add_u32 v17, v17, 2, s67\n"
        "v_lshl_add_u32 v18, v18, 2, s67\n"
        "v_lshl_add_u32 v19, v19, 2, s67\n"
        "buffer_load_dwordx4 v129, s[20:23], 0 offen offset:1408 lds\n"
        "ds_read_b32 v0, v0\n"
        "ds_read_b32 v1, v1\n"
        "ds_read_b32 v2, v2\n"
        "ds_read_b32 v3, v3\n"
        "ds_read_b32 v4, v4\n"
        "ds_read_b32 v5, v5\n"
        "ds_read_b32 v6, v6\n"
        "ds_read_b32 v7, v7\n"
        "ds_read_b32 v16, v16\n"
        "ds_read_b32 v17, v17\n"
        "ds_read_b32 v18, v18\n"
        "ds_read_b32 v19, v19\n"
        "buffer_load_dwordx4 v130, s[20:23], 0 offen offset:2432 lds\n"
        "s_waitcnt lgkmcnt(15)\n"
        "v_lshl_add_u32 v20, v20, 2, s67\n"
        "v_lshl_add_u32 v21, v21, 2, s67\n"
        "v_lshl_add_u32 v22, v22, 2, s67\n"
        "v_lshl_add_u32 v23, v23, 2, s67\n"
        "v_lshl_add_u32 v8, v8, 2, s67\n"
        "v_lshl_add_u32 v9, v9, 2, s67\n"
        "v_lshl_add_u32 v10, v10, 2, s67\n"
        "v_lshl_add_u32 v11, v11, 2, s67\n"
        "v_lshl_add_u32 v24, v24, 2, s67\n"
        "v_lshl_add_u32 v25, v25, 2, s67\n"
        "v_lshl_add_u32 v26, v26, 2, s67\n"
        "v_lshl_add_u32 v27, v27, 2, s67\n"
        "ds_read_b32 v20, v20\n"
        "ds_read_b32 v21, v21\n"
        "ds_read_b32 v22, v22\n"
        "ds_read_b32 v23, v23\n"
        "ds_read_b32 v8, v8\n"
        "ds_read_b32 v9, v9\n"
        "ds_read_b32 v10, v10\n"
        "ds_read_b32 v11, v11\n"
        "ds_read_b32 v24, v24\n"
        "ds_read_b32 v25, v25\n"
        "ds_read_b32 v26, v26\n"
        "ds_read_b32 v27, v27\n"
        "s_add_u32 m0, s65, 6144\n"
        "v_lshl_add_u32 v12, v12, 2, s67\n"
        "buffer_load_dwordx4 v128, s[20:23], s56 offen lds\n"
        "v_lshl_add_u32 v13, v13, 2, s67\n"
        "v_lshl_add_u32 v14, v14, 2, s67\n"
        "v_lshl_add_u32 v15, v15, 2, s67\n"
        "v_lshl_add_u32 v28, v28, 2, s67\n"
        "v_lshl_add_u32 v29, v29, 2, s67\n"
        "v_lshl_add_u32 v30, v30, 2, s67\n"
        "v_lshl_add_u32 v31, v31, 2, s67\n"
        "s_waitcnt lgkmcnt(15)\n"
        "v_lshl_add_u32 v32, v32, 2, s67\n"
        "v_lshl_add_u32 v33, v33, 2, s67\n"
        "v_lshl_add_u32 v34, v34, 2, s67\n"
        "v_lshl_add_u32 v35, v35, 2, s67\n"
        "ds_read_b32 v12, v12\n"
        "ds_read_b32 v13, v13\n"
        "ds_read_b32 v14, v14\n"
        "ds_read_b32 v15, v15\n"
        "ds_read_b32 v28, v28\n"
        "ds_read_b32 v29, v29\n"
        "ds_read_b32 v30, v30\n"
        "ds_read_b32 v31, v31\n"
        "ds_read_b32 v32, v32\n"
        "ds_read_b32 v33, v33\n"
        "ds_read_b32 v34, v34\n"
        "ds_read_b32 v35, v35\n"
        "buffer_load_dwordx4 v129, s[20:23], s56 offen offset:1024 lds\n"
        "v_lshl_add_u32 v36, v36, 2, s67\n"
        "v_lshl_add_u32 v37, v37, 2, s67\n"
        "v_lshl_add_u32 v38, v38, 2, s67\n"
        "v_lshl_add_u32 v39, v39, 2, s67\n"
        "v_lshl_add_u32 v40, v40, 2, s67\n"
        "v_lshl_add_u32 v41, v41, 2, s67\n"
        "v_lshl_add_u32 v42, v42, 2, s67\n"
        "v_lshl_add_u32 v43, v43, 2, s67\n"
        "ds_read_b32 v36, v36\n"
        "ds_read_b32 v37, v37\n"
        "ds_read_b32 v38, v38\n"
        "ds_read_b32 v39, v39\n"
        "ds_read_b32 v40, v40\n"
        "ds_read_b32 v41, v41\n"
        "ds_read_b32 v42, v42\n"
        "ds_read_b32 v43, v43\n"
        "buffer_load_dwordx4 v130, s[20:23], s56 offen offset:2048 lds\n"
        "s_waitcnt vmcnt(9)\n"
        "s_barrier\n"
        "s_waitcnt vmcnt(6)\n"
        "ds_read_b128 a[212:215], v137 offset:0\n"
        "ds_read_b128 a[208:211], v137 offset:768\n"
        "ds_read_b128 a[204:207], v137 offset:32\n"
        "ds_read_b128 a[200:203], v137 offset:800\n"
        "ds_read_b128 a[196:199], v137 offset:64\n"
        "ds_read_b128 a[192:195], v137 offset:832\n"
        "s_add_u32 m0, s65, 8832\n"
        "s_nop 0\n"
        "buffer_load_dwordx4 v128, s[20:23], s56 offen offset:384 lds\n"
        "s_waitcnt lgkmcnt(5)\n"
        "v_mfma_f32_16x16x32_bf16 v[112:115], a[212:215], v[148:151], 0\n"
        "s_waitcnt lgkmcnt(4)\n"
        "ds_read_b128 a[216:219], v240 offset:0\n"
        "ds_read_b128 a[220:223], v240 offset:1536\n"
        "ds_read_b128 a[224:227], v240 offset:12288\n"
        "ds_read_b128 a[228:231], v240 offset:13824\n"
        "v_mfma_f32_16x16x32_bf16 v[116:119], a[208:211], v[148:151], 0\n"
        "s_waitcnt lgkmcnt(7)\n"
        "v_mfma_f32_16x16x32_bf16 v[120:123], a[204:207], v[148:151], 0\n"
        "s_waitcnt lgkmcnt(6)\n"
        "v_mfma_f32_16x16x32_bf16 v[124:127], a[200:203], v[148:151], 0\n"
        "s_waitcnt lgkmcnt(5)\n"
        "buffer_load_dwordx4 v129, s[20:23], s56 offen offset:1408 lds\n"
        "v_mfma_f32_16x16x32_bf16 v[96:99], a[196:199], v[148:151], 0\n"
        "s_waitcnt lgkmcnt(4)\n"
        "ds_read_b128 v[44:47], v143 offset:8192\n"
        "v_cvt_pk_bf16_f32 v112, v112, v113\n"
        "v_mfma_f32_16x16x32_bf16 v[100:103], a[192:195], v[148:151], 0\n"
        "s_waitcnt lgkmcnt(4)\n"
        "s_waitcnt vmcnt(5)\n"
        "ds_read_b64_tr_b16 a[72:73], v255 offset:3072\n"
        "ds_read_b64_tr_b16 a[74:75], v255 offset:3840\n"
        "v_accvgpr_write_b32 a0, v112\n"
        "v_cvt_pk_bf16_f32 v114, v114, v115\n"
        "v_accvgpr_write_b32 a1, v114\n"
        "v_cvt_pk_bf16_f32 v116, v116, v117\n"
        "v_accvgpr_write_b32 a2, v116\n"
        "v_cvt_pk_bf16_f32 v118, v118, v119\n"
        "v_accvgpr_write_b32 a3, v118\n"
        "s_nop 1\n"
        "v_mfma_f32_16x16x32_bf16 v[156:159], a[0:3], a[216:219], 0\n"
        "s_waitcnt lgkmcnt(5)\n"
        "ds_read_b64_tr_b16 a[96:97], v255 offset:3104\n"
        "ds_read_b64_tr_b16 a[98:99], v255 offset:3872\n"
        "v_mfma_f32_16x16x32_bf16 v[176:179], a[0:3], a[220:223], 0\n"
        "s_waitcnt lgkmcnt(5)\n"
        "buffer_load_dwordx4 v130, s[20:23], s56 offen offset:2432 lds\n"
        "ds_read_b64_tr_b16 a[120:121], v255 offset:3136\n"
        "ds_read_b64_tr_b16 a[122:123], v255 offset:3904\n"
        "v_mfma_f32_16x16x32_bf16 v[196:199], a[0:3], a[224:227], 0\n"
        "s_add_u32 m0, s65, 12288\n"
        "v_cvt_pk_bf16_f32 v120, v120, v121\n"
        "v_accvgpr_write_b32 a24, v120\n"
        "v_mfma_f32_16x16x32_bf16 v[216:219], a[0:3], a[228:231], 0\n"
        "s_waitcnt lgkmcnt(4)\n"
        "v_cvt_pk_bf16_f32 v122, v122, v123\n"
        "v_accvgpr_write_b32 a25, v122\n"
        "v_mfma_f32_16x16x32_bf16 v[152:155], a[72:75], a[216:219], 0\n"
        "buffer_load_dwordx4 v128, s[20:23], s57 offen lds\n"
        "s_waitcnt vmcnt(4)\n"
        "ds_read_b128 a[188:191], v137 offset:6144\n"
        "v_mfma_f32_16x16x32_bf16 v[172:175], a[72:75], a[220:223], 0\n"
        "ds_read_b128 a[184:187], v137 offset:6912\n"
        "ds_read_b128 a[180:183], v137 offset:6176\n"
        "v_cvt_pk_bf16_f32 v124, v124, v125\n"
        "v_accvgpr_write_b32 a26, v124\n"
        "v_cvt_pk_bf16_f32 v126, v126, v127\n"
        "v_accvgpr_write_b32 a27, v126\n"
        "s_nop 1\n"
        "v_mfma_f32_16x16x32_bf16 v[224:227], a[24:27], a[228:231], 0\n"
        "buffer_load_dwordx4 v129, s[20:23], s57 offen offset:1024 lds\n"
        "ds_read_b128 a[176:179], v137 offset:6944\n"
        "ds_read_b128 a[172:175], v137 offset:6208\n"
        "v_mfma_f32_16x16x32_bf16 v[204:207], a[24:27], a[224:227], 0\n"
        "ds_read_b128 a[168:171], v137 offset:6976\n"
        "ds_read_b128 a[232:235], v241 offset:0\n"
        "v_mfma_f32_16x16x32_bf16 v[184:187], a[24:27], a[220:223], 0\n"
        "ds_read_b128 a[236:239], v241 offset:1536\n"
        "ds_read_b128 a[240:243], v241 offset:12288\n"
        "v_mfma_f32_16x16x32_bf16 v[164:167], a[24:27], a[216:219], 0\n"
        "buffer_load_dwordx4 v130, s[20:23], s57 offen offset:2048 lds\n"
        "v_mfma_f32_16x16x32_bf16 v[192:195], a[72:75], a[224:227], 0\n"
        "s_add_u32 m0, s65, 14976\n"
        "ds_read_b128 a[244:247], v241 offset:13824\n"
        "v_cvt_pk_bf16_f32 v96, v96, v97\n"
        "v_mfma_f32_16x16x32_bf16 v[212:215], a[72:75], a[228:231], 0\n"
        "v_accvgpr_write_b32 a48, v96\n"
        "v_cvt_pk_bf16_f32 v98, v98, v99\n"
        "v_accvgpr_write_b32 a49, v98\n"
        "v_cvt_pk_bf16_f32 v100, v100, v101\n"
        "v_accvgpr_write_b32 a50, v100\n"
        "v_cvt_pk_bf16_f32 v102, v102, v103\n"
        "v_accvgpr_write_b32 a51, v102\n"
        "s_nop 1\n"
        "v_mfma_f32_16x16x32_bf16 v[160:163], a[48:51], a[216:219], 0\n"
        "v_lshl_add_u32 v44, v44, 2, s67\n"
        "v_lshl_add_u32 v45, v45, 2, s67\n"
        "v_mfma_f32_16x16x32_bf16 v[180:183], a[48:51], a[220:223], 0\n"
        "s_waitcnt lgkmcnt(5)\n"
        "ds_read_b32 v44, v44\n"
        "ds_read_b32 v45, v45\n"
        "v_mfma_f32_16x16x32_bf16 v[104:107], a[188:191], v[148:151], 0\n"
        "buffer_load_dwordx4 v128, s[20:23], s57 offen offset:384 lds\n"
        "v_mfma_f32_16x16x32_bf16 v[200:203], a[48:51], a[224:227], 0\n"
        "v_lshl_add_u32 v46, v46, 2, s67\n"
        "v_lshl_add_u32 v47, v47, 2, s67\n"
        "v_mfma_f32_16x16x32_bf16 v[108:111], a[184:187], v[148:151], 0\n"
        "ds_read_b32 v46, v46\n"
        "ds_read_b32 v47, v47\n"
        "v_mfma_f32_16x16x32_bf16 v[220:223], a[48:51], a[228:231], 0\n"
        "buffer_load_dwordx4 v129, s[20:23], s57 offen offset:1408 lds\n"
        "v_mfma_f32_16x16x32_bf16 v[88:91], a[96:99], a[228:231], 0\n"
        "ds_read_b128 v[48:51], v140 offset:9216\n"
        "v_cvt_pk_bf16_f32 v104, v104, v105\n"
        "v_mfma_f32_16x16x32_bf16 v[80:83], a[96:99], a[224:227], 0\n"
        "s_waitcnt vmcnt(5)\n"
        "ds_read_b64_tr_b16 a[76:77], v255 offset:9216\n"
        "ds_read_b64_tr_b16 a[78:79], v255 offset:9984\n"
        "v_mfma_f32_16x16x32_bf16 v[68:71], a[96:99], a[220:223], 0\n"
        "ds_read_b64_tr_b16 a[100:101], v255 offset:9248\n"
        "ds_read_b64_tr_b16 a[102:103], v255 offset:10016\n"
        "v_mfma_f32_16x16x32_bf16 v[64:67], a[96:99], a[216:219], 0\n"
        "buffer_load_dwordx4 v130, s[20:23], s57 offen offset:2432 lds\n"
        "ds_read_b64_tr_b16 a[124:125], v255 offset:9280\n"
        "ds_read_b64_tr_b16 a[126:127], v255 offset:10048\n"
        "v_mfma_f32_16x16x32_bf16 v[112:115], a[180:183], v[148:151], 0\n"
        "s_add_u32 m0, s65, 0\n"
        "v_accvgpr_write_b32 a4, v104\n"
        "v_cvt_pk_bf16_f32 v106, v106, v107\n"
        "v_mfma_f32_16x16x32_bf16 v[116:119], a[176:179], v[148:151], 0\n"
        "v_accvgpr_write_b32 a5, v106\n"
        "v_cvt_pk_bf16_f32 v108, v108, v109\n"
        "v_mfma_f32_16x16x32_bf16 v[72:75], a[120:123], a[216:219], 0\n"
        "buffer_load_dwordx4 v128, s[20:23], s58 offen lds\n"
        "v_mfma_f32_16x16x32_bf16 v[76:79], a[120:123], a[220:223], 0\n"
        "v_accvgpr_write_b32 a6, v108\n"
        "v_cvt_pk_bf16_f32 v110, v110, v111\n"
        "v_mfma_f32_16x16x32_bf16 v[84:87], a[120:123], a[224:227], 0\n"
        "v_accvgpr_write_b32 a7, v110\n"
        "v_cvt_pk_bf16_f32 v112, v112, v113\n"
        "v_mfma_f32_16x16x32_bf16 v[92:95], a[120:123], a[228:231], 0\n"
        "s_waitcnt lgkmcnt(2)\n"
        "v_accvgpr_write_b32 a28, v112\n"
        "v_cvt_pk_bf16_f32 v114, v114, v115\n"
        "v_mfma_f32_16x16x32_bf16 v[156:159], a[4:7], a[232:235], v[156:159]\n"
        "buffer_load_dwordx4 v129, s[20:23], s58 offen offset:1024 lds\n"
        "v_mfma_f32_16x16x32_bf16 v[176:179], a[4:7], a[236:239], v[176:179]\n"
        "v_lshl_add_u32 v48, v48, 2, s67\n"
        "v_lshl_add_u32 v49, v49, 2, s67\n"
        "v_mfma_f32_16x16x32_bf16 v[196:199], a[4:7], a[240:243], v[196:199]\n"
        "ds_read_b32 v48, v48\n"
        "ds_read_b32 v49, v49\n"
        "v_mfma_f32_16x16x32_bf16 v[216:219], a[4:7], a[244:247], v[216:219]\n"
        ".long 0xbec200ff\n"
        "41:\n"
        "s_branch 60b\n"
        "v_lshl_add_u32 v50, v50, 2, s67\n"
        "v_lshl_add_u32 v51, v51, 2, s67\n"
        "v_mfma_f32_16x16x32_bf16 v[120:123], a[172:175], v[148:151], 0\n"
        "buffer_load_dwordx4 v130, s[20:23], s58 offen offset:2048 lds\n"
        "v_mfma_f32_16x16x32_bf16 v[124:127], a[168:171], v[148:151], 0\n"
        "s_add_u32 m0, s65, 2688\n"
        "ds_read_b32 v50, v50\n"
        "ds_read_b32 v51, v51\n"
        "v_mfma_f32_16x16x32_bf16 v[152:155], a[76:79], a[232:235], v[152:155]\n"
        "ds_read_b128 a[248:251], v240 offset:128\n"
        "v_accvgpr_write_b32 a29, v114\n"
        "v_mfma_f32_16x16x32_bf16 v[172:175], a[76:79], a[236:239], v[172:175]\n"
        "s_waitcnt vmcnt(6)\n"
        "ds_read_b128 a[212:215], v137 offset:12288\n"
        "ds_read_b128 a[208:211], v137 offset:13056\n"
        "v_mfma_f32_16x16x32_bf16 v[192:195], a[76:79], a[240:243], v[192:195]\n"
        "buffer_load_dwordx4 v128, s[20:23], s58 offen offset:384 lds\n"
        "ds_read_b128 a[204:207], v137 offset:12320\n"
        "v_cvt_pk_bf16_f32 v116, v116, v117\n"
        "v_mfma_f32_16x16x32_bf16 v[212:215], a[76:79], a[244:247], v[212:215]\n"
        "ds_read_b128 a[200:203], v137 offset:13088\n"
        "ds_read_b128 a[196:199], v137 offset:12352\n"
        "v_accvgpr_write_b32 a30, v116\n"
        "v_cvt_pk_bf16_f32 v118, v118, v119\n"
        "v_accvgpr_write_b32 a31, v118\n"
        "s_nop 1\n"
        "v_mfma_f32_16x16x32_bf16 v[224:227], a[28:31], a[244:247], v[224:227]\n"
        "ds_read_b128 a[192:195], v137 offset:13120\n"
        "ds_read_b128 a[216:219], v240 offset:1664\n"
        "v_mfma_f32_16x16x32_bf16 v[204:207], a[28:31], a[240:243], v[204:207]\n"
        "buffer_load_dwordx4 v129, s[20:23], s58 offen offset:1408 lds\n"
        "v_mfma_f32_16x16x32_bf16 v[184:187], a[28:31], a[236:239], v[184:187]\n"
        "ds_read_b128 a[220:223], v240 offset:12416\n"
        "ds_read_b128 a[224:227], v240 offset:13952\n"
        "v_mfma_f32_16x16x32_bf16 v[164:167], a[28:31], a[232:235], v[164:167]\n"
        "s_waitcnt lgkmcnt(8)\n"
        "v_cvt_pk_bf16_f32 v120, v120, v121\n"
        "v_accvgpr_write_b32 a52, v120\n"
        "v_mfma_f32_16x16x32_bf16 v[96:99], a[212:215], v[148:151], 0\n"
        "s_waitcnt lgkmcnt(7)\n"
        "v_cvt_pk_bf16_f32 v122, v122, v123\n"
        "v_accvgpr_write_b32 a53, v122\n"
        "v_mfma_f32_16x16x32_bf16 v[100:103], a[208:211], v[148:151], 0\n"
        "buffer_load_dwordx4 v130, s[20:23], s58 offen offset:2432 lds\n"
        "v_mfma_f32_16x16x32_bf16 v[88:91], a[100:103], a[244:247], v[88:91]\n"
        "s_add_u32 m0, s65, 6144\n"
        "v_cvt_pk_bf16_f32 v124, v124, v125\n"
        "v_accvgpr_write_b32 a54, v124\n"
        "v_mfma_f32_16x16x32_bf16 v[80:83], a[100:103], a[240:243], v[80:83]\n"
        "v_cvt_pk_bf16_f32 v126, v126, v127\n"
        "v_accvgpr_write_b32 a55, v126\n"
        "v_mfma_f32_16x16x32_bf16 v[68:71], a[100:103], a[236:239], v[68:71]\n"
        "v_cvt_pk_bf16_f32 v96, v96, v97\n"
        "v_accvgpr_write_b32 a8, v96\n"
        "v_mfma_f32_16x16x32_bf16 v[64:67], a[100:103], a[232:235], v[64:67]\n"
        "buffer_load_dwordx4 v128, s[20:23], s59 offen lds\n"
        "v_mfma_f32_16x16x32_bf16 v[160:163], a[52:55], a[232:235], v[160:163]\n"
        "s_waitcnt vmcnt(7)\n"
        "ds_read_b64_tr_b16 a[80:81], v255 offset:15360\n"
        "ds_read_b64_tr_b16 a[82:83], v255 offset:16128\n"
        "v_mfma_f32_16x16x32_bf16 v[180:183], a[52:55], a[236:239], v[180:183]\n"
        "ds_read_b64_tr_b16 a[104:105], v255 offset:15392\n"
        "ds_read_b64_tr_b16 a[106:107], v255 offset:16160\n"
        "v_mfma_f32_16x16x32_bf16 v[200:203], a[52:55], a[240:243], v[200:203]\n"
        "ds_read_b64_tr_b16 a[128:129], v255 offset:15424\n"
        "ds_read_b64_tr_b16 a[130:131], v255 offset:16192\n"
        "v_mfma_f32_16x16x32_bf16 v[220:223], a[52:55], a[244:247], v[220:223]\n"
        "s_waitcnt lgkmcnt(6)\n"
        "v_cvt_pk_bf16_f32 v98, v98, v99\n"
        "v_accvgpr_write_b32 a9, v98\n"
        "v_mfma_f32_16x16x32_bf16 v[104:107], a[204:207], v[148:151], 0\n"
        "buffer_load_dwordx4 v129, s[20:23], s59 offen offset:1024 lds\n"
        "v_mfma_f32_16x16x32_bf16 v[108:111], a[200:203], v[148:151], 0\n"
        "v_cvt_pk_bf16_f32 v100, v100, v101\n"
        "v_accvgpr_write_b32 a10, v100\n"
        "v_mfma_f32_16x16x32_bf16 v[72:75], a[124:127], a[232:235], v[72:75]\n"
        "v_cvt_pk_bf16_f32 v102, v102, v103\n"
        "v_accvgpr_write_b32 a11, v102\n"
        "v_mfma_f32_16x16x32_bf16 v[76:79], a[124:127], a[236:239], v[76:79]\n"
        "buffer_load_dwordx4 v130, s[20:23], s59 offen offset:2048 lds\n"
        "v_mfma_f32_16x16x32_bf16 v[84:87], a[124:127], a[240:243], v[84:87]\n"
        "s_add_u32 m0, s65, 8832\n"
        "v_cvt_pk_bf16_f32 v104, v104, v105\n"
        "v_accvgpr_write_b32 a32, v104\n"
        "v_mfma_f32_16x16x32_bf16 v[92:95], a[124:127], a[244:247], v[92:95]\n"
        "s_waitcnt vmcnt(6)\n"
        "ds_read_b128 a[188:191], v137 offset:0\n"
        "ds_read_b128 a[184:187], v137 offset:768\n"
        "v_mfma_f32_16x16x32_bf16 v[156:159], a[8:11], a[248:251], v[156:159]\n"
        "buffer_load_dwordx4 v128, s[20:23], s59 offen offset:384 lds\n"
        "ds_read_b128 a[180:183], v137 offset:32\n"
        "v_mfma_f32_16x16x32_bf16 v[176:179], a[8:11], a[216:219], v[176:179]\n"
        "ds_read_b128 a[176:179], v137 offset:800\n"
        "ds_read_b128 a[172:175], v137 offset:64\n"
        "v_mfma_f32_16x16x32_bf16 v[196:199], a[8:11], a[220:223], v[196:199]\n"
        "ds_read_b128 a[168:171], v137 offset:832\n"
        "v_cvt_pk_bf16_f32 v106, v106, v107\n"
        "v_mfma_f32_16x16x32_bf16 v[216:219], a[8:11], a[224:227], v[216:219]\n"
        "v_accvgpr_write_b32 a33, v106\n"
        "v_cvt_pk_bf16_f32 v108, v108, v109\n"
        "v_mfma_f32_16x16x32_bf16 v[112:115], a[196:199], v[148:151], 0\n"
        "buffer_load_dwordx4 v129, s[20:23], s59 offen offset:1408 lds\n"
        "v_mfma_f32_16x16x32_bf16 v[116:119], a[192:195], v[148:151], 0\n"
        "s_waitcnt lgkmcnt(6)\n"
        "v_accvgpr_write_b32 a34, v108\n"
        "v_cvt_pk_bf16_f32 v110, v110, v111\n"
        "v_mfma_f32_16x16x32_bf16 v[152:155], a[80:83], a[248:251], v[152:155]\n"
        "ds_read_b128 a[228:231], v241 offset:128\n"
        "v_accvgpr_write_b32 a35, v110\n"
        "v_mfma_f32_16x16x32_bf16 v[172:175], a[80:83], a[216:219], v[172:175]\n"
        "ds_read_b128 a[232:235], v241 offset:1664\n"
        "v_cvt_pk_bf16_f32 v112, v112, v113\n"
        "v_mfma_f32_16x16x32_bf16 v[192:195], a[80:83], a[220:223], v[192:195]\n"
        "buffer_load_dwordx4 v130, s[20:23], s59 offen offset:2432 lds\n"
        "v_mfma_f32_16x16x32_bf16 v[212:215], a[80:83], a[224:227], v[212:215]\n"
        "s_add_u32 m0, s65, 12288\n"
        "ds_read_b128 a[236:239], v241 offset:12416\n"
        "ds_read_b128 a[240:243], v241 offset:13952\n"
        "v_mfma_f32_16x16x32_bf16 v[224:227], a[32:35], a[224:227], v[224:227]\n"
        "ds_read_b128 v[52:55], v141 offset:9216\n"
        "v_accvgpr_write_b32 a56, v112\n"
        "v_mfma_f32_16x16x32_bf16 v[204:207], a[32:35], a[220:223], v[204:207]\n"
        "v_cvt_pk_bf16_f32 v114, v114, v115\n"
        "v_accvgpr_write_b32 a57, v114\n"
        "v_mfma_f32_16x16x32_bf16 v[184:187], a[32:35], a[216:219], v[184:187]\n"
        "buffer_load_dwordx4 v128, s[20:23], s60 offen lds\n"
        "v_mfma_f32_16x16x32_bf16 v[164:167], a[32:35], a[248:251], v[164:167]\n"
        "s_waitcnt lgkmcnt(4)\n"
        "v_cvt_pk_bf16_f32 v116, v116, v117\n"
        "v_accvgpr_write_b32 a58, v116\n"
        "v_mfma_f32_16x16x32_bf16 v[120:123], a[188:191], v[148:151], 0\n"
        "s_waitcnt vmcnt(7)\n"
        "ds_read_b64_tr_b16 a[84:85], v255 offset:3072\n"
        "ds_read_b64_tr_b16 a[86:87], v255 offset:3840\n"
        "v_mfma_f32_16x16x32_bf16 v[124:127], a[184:187], v[148:151], 0\n"
        "ds_read_b64_tr_b16 a[108:109], v255 offset:3104\n"
        "ds_read_b64_tr_b16 a[110:111], v255 offset:3872\n"
        "v_mfma_f32_16x16x32_bf16 v[88:91], a[104:107], a[224:227], v[88:91]\n"
        "buffer_load_dwordx4 v129, s[20:23], s60 offen offset:1024 lds\n"
        "ds_read_b64_tr_b16 a[132:133], v255 offset:3136\n"
        "ds_read_b64_tr_b16 a[134:135], v255 offset:3904\n"
        "v_mfma_f32_16x16x32_bf16 v[80:83], a[104:107], a[220:223], v[80:83]\n"
        "v_cvt_pk_bf16_f32 v118, v118, v119\n"
        "v_accvgpr_write_b32 a59, v118\n"
        "v_mfma_f32_16x16x32_bf16 v[68:71], a[104:107], a[216:219], v[68:71]\n"
        "v_cvt_pk_bf16_f32 v120, v120, v121\n"
        "v_accvgpr_write_b32 a12, v120\n"
        "v_mfma_f32_16x16x32_bf16 v[64:67], a[104:107], a[248:251], v[64:67]\n"
        "buffer_load_dwordx4 v130, s[20:23], s60 offen offset:2048 lds\n"
        "v_mfma_f32_16x16x32_bf16 v[160:163], a[56:59], a[248:251], v[160:163]\n"
        "s_add_u32 m0, s65, 14976\n"
        "v_cvt_pk_bf16_f32 v122, v122, v123\n"
        "v_accvgpr_write_b32 a13, v122\n"
        "v_mfma_f32_16x16x32_bf16 v[180:183], a[56:59], a[216:219], v[180:183]\n"
        "s_waitcnt lgkmcnt(4)\n"
        "v_lshl_add_u32 v52, v52, 2, s67\n"
        "v_lshl_add_u32 v53, v53, 2, s67\n"
        "v_mfma_f32_16x16x32_bf16 v[200:203], a[56:59], a[220:223], v[200:203]\n"
        "ds_read_b32 v52, v52\n"
        "ds_read_b32 v53, v53\n"
        "v_mfma_f32_16x16x32_bf16 v[220:223], a[56:59], a[224:227], v[220:223]\n"
        "buffer_load_dwordx4 v128, s[20:23], s60 offen offset:384 lds\n"
        "v_mfma_f32_16x16x32_bf16 v[96:99], a[180:183], v[148:151], 0\n"
        "v_lshl_add_u32 v54, v54, 2, s67\n"
        "v_lshl_add_u32 v55, v55, 2, s67\n"
        "v_mfma_f32_16x16x32_bf16 v[100:103], a[176:179], v[148:151], 0\n"
        "ds_read_b32 v54, v54\n"
        "ds_read_b32 v55, v55\n"
        "v_mfma_f32_16x16x32_bf16 v[72:75], a[128:131], a[248:251], v[72:75]\n"
        "v_cvt_pk_bf16_f32 v124, v124, v125\n"
        "v_accvgpr_write_b32 a14, v124\n"
        "v_mfma_f32_16x16x32_bf16 v[76:79], a[128:131], a[216:219], v[76:79]\n"
        "buffer_load_dwordx4 v129, s[20:23], s60 offen offset:1408 lds\n"
        "v_mfma_f32_16x16x32_bf16 v[84:87], a[128:131], a[220:223], v[84:87]\n"
        "v_cvt_pk_bf16_f32 v126, v126, v127\n"
        "v_accvgpr_write_b32 a15, v126\n"
        "v_mfma_f32_16x16x32_bf16 v[92:95], a[128:131], a[224:227], v[92:95]\n"
        "v_cvt_pk_bf16_f32 v96, v96, v97\n"
        "v_accvgpr_write_b32 a36, v96\n"
        "v_mfma_f32_16x16x32_bf16 v[156:159], a[12:15], a[228:231], v[156:159]\n"
        "s_waitcnt vmcnt(8)\n"
        "ds_read_b128 a[212:215], v137 offset:6144\n"
        "ds_read_b128 a[208:211], v137 offset:6912\n"
        "v_mfma_f32_16x16x32_bf16 v[176:179], a[12:15], a[232:235], v[176:179]\n"
        "buffer_load_dwordx4 v130, s[20:23], s60 offen offset:2432 lds\n"
        "ds_read_b128 a[204:207], v137 offset:6176\n"
        "v_mfma_f32_16x16x32_bf16 v[196:199], a[12:15], a[236:239], v[196:199]\n"
        "ds_read_b128 a[200:203], v137 offset:6944\n"
        "ds_read_b128 a[196:199], v137 offset:6208\n"
        "v_mfma_f32_16x16x32_bf16 v[216:219], a[12:15], a[240:243], v[216:219]\n"
        "ds_read_b128 a[192:195], v137 offset:6976\n"
        "v_cvt_pk_bf16_f32 v98, v98, v99\n"
        "v_mfma_f32_16x16x32_bf16 v[104:107], a[172:175], v[148:151], 0\n"
        "v_accvgpr_write_b32 a37, v98\n"
        "v_cvt_pk_bf16_f32 v100, v100, v101\n"
        "v_mfma_f32_16x16x32_bf16 v[108:111], a[168:171], v[148:151], 0\n"
        "v_accvgpr_write_b32 a38, v100\n"
        "v_cvt_pk_bf16_f32 v102, v102, v103\n"
        "v_mfma_f32_16x16x32_bf16 v[152:155], a[84:87], a[228:231], v[152:155]\n"
        "ds_read_b128 a[244:247], v240 offset:256\n"
        "v_accvgpr_write_b32 a39, v102\n"
        "v_mfma_f32_16x16x32_bf16 v[172:175], a[84:87], a[232:235], v[172:175]\n"
        "s_mov_b32 m0, s48\n"
        "ds_read_b128 a[248:251], v240 offset:1792\n"
        "v_cvt_pk_bf16_f32 v104, v104, v105\n"
        "v_mfma_f32_16x16x32_bf16 v[192:195], a[84:87], a[236:239], v[192:195]\n"
        "s_sub_u32 s64, s36, 4194304\n"
        "ds_read_b128 a[216:219], v240 offset:12544\n"
        "v_accvgpr_write_b32 a60, v104\n"
        "v_mfma_f32_16x16x32_bf16 v[212:215], a[84:87], a[240:243], v[212:215]\n"
        "buffer_load_dwordx4 v249, s[12:15], s64 offen lds\n"
        "v_mfma_f32_16x16x32_bf16 v[224:227], a[36:39], a[240:243], v[224:227]\n"
        "ds_read_b128 a[220:223], v240 offset:14080\n"
        "v_cvt_pk_bf16_f32 v106, v106, v107\n"
        "v_mfma_f32_16x16x32_bf16 v[204:207], a[36:39], a[236:239], v[204:207]\n"
        "ds_read_b128 v[56:59], v142 offset:9216\n"
        "v_accvgpr_write_b32 a61, v106\n"
        "v_mfma_f32_16x16x32_bf16 v[184:187], a[36:39], a[232:235], v[184:187]\n"
        "s_waitcnt vmcnt(7)\n"
        "ds_read_b64_tr_b16 a[88:89], v255 offset:9216\n"
        "ds_read_b64_tr_b16 a[90:91], v255 offset:9984\n"
        "v_mfma_f32_16x16x32_bf16 v[164:167], a[36:39], a[228:231], v[164:167]\n"
        "s_waitcnt lgkmcnt(6)\n"
        "ds_read_b64_tr_b16 a[112:113], v255 offset:9248\n"
        "ds_read_b64_tr_b16 a[114:115], v255 offset:10016\n"
        "v_mfma_f32_16x16x32_bf16 v[112:115], a[212:215], v[148:151], 0\n"
        "buffer_load_dwordx4 v253, s[12:15], s64 offen offset:1024 lds\n"
        "ds_read_b64_tr_b16 a[136:137], v255 offset:9280\n"
        "v_mfma_f32_16x16x32_bf16 v[116:119], a[208:211], v[148:151], 0\n"
        "ds_read_b64_tr_b16 a[138:139], v255 offset:10048\n"
        "s_add_u32 m0, s65, 0\n"
        "v_cvt_pk_bf16_f32 v108, v108, v109\n"
        "v_mfma_f32_16x16x32_bf16 v[88:91], a[108:111], a[240:243], v[88:91]\n"
        "s_mov_b32 s64, 768\n"
        "v_accvgpr_write_b32 a62, v108\n"
        "v_cvt_pk_bf16_f32 v110, v110, v111\n"
        "v_mfma_f32_16x16x32_bf16 v[80:83], a[108:111], a[236:239], v[80:83]\n"
        "buffer_load_dwordx4 v128, s[20:23], s64 offen lds\n"
        "v_mfma_f32_16x16x32_bf16 v[68:71], a[108:111], a[232:235], v[68:71]\n"
        "v_accvgpr_write_b32 a63, v110\n"
        "v_cvt_pk_bf16_f32 v112, v112, v113\n"
        "v_mfma_f32_16x16x32_bf16 v[64:67], a[108:111], a[228:231], v[64:67]\n"
        "v_accvgpr_write_b32 a16, v112\n"
        "v_cvt_pk_bf16_f32 v114, v114, v115\n"
        "v_mfma_f32_16x16x32_bf16 v[160:163], a[60:63], a[228:231], v[160:163]\n"
        "v_accvgpr_write_b32 a17, v114\n"
        "v_cvt_pk_bf16_f32 v116, v116, v117\n"
        "v_mfma_f32_16x16x32_bf16 v[180:183], a[60:63], a[232:235], v[180:183]\n"
        "buffer_load_dwordx4 v129, s[20:23], s64 offen offset:1024 lds\n"
        "v_mfma_f32_16x16x32_bf16 v[200:203], a[60:63], a[236:239], v[200:203]\n"
        "ds_read_b128 v[60:63], v143 offset:9216\n"
        "v_accvgpr_write_b32 a18, v116\n"
        "v_mfma_f32_16x16x32_bf16 v[220:223], a[60:63], a[240:243], v[220:223]\n"
        "v_cvt_pk_bf16_f32 v118, v118, v119\n"
        "v_accvgpr_write_b32 a19, v118\n"
        "v_mfma_f32_16x16x32_bf16 v[120:123], a[204:207], v[148:151], 0\n"
        "s_waitcnt lgkmcnt(1)\n"
        "v_lshl_add_u32 v56, v56, 2, s67\n"
        "v_lshl_add_u32 v57, v57, 2, s67\n"
        "v_mfma_f32_16x16x32_bf16 v[124:127], a[200:203], v[148:151], 0\n"
        "buffer_load_dwordx4 v130, s[20:23], s64 offen offset:2048 lds\n"
        "v_mfma_f32_16x16x32_bf16 v[72:75], a[132:135], a[228:231], v[72:75]\n"
        "s_add_u32 m0, s65, 2304\n"
        "ds_read_b32 v56, v56\n"
        "ds_read_b32 v57, v57\n"
        "v_mfma_f32_16x16x32_bf16 v[76:79], a[132:135], a[232:235], v[76:79]\n"
        "v_lshl_add_u32 v58, v58, 2, s67\n"
        "v_lshl_add_u32 v59, v59, 2, s67\n"
        "v_mfma_f32_16x16x32_bf16 v[84:87], a[132:135], a[236:239], v[84:87]\n"
        "ds_read_b32 v58, v58\n"
        "ds_read_b32 v59, v59\n"
        "v_mfma_f32_16x16x32_bf16 v[92:95], a[132:135], a[240:243], v[92:95]\n"
        "buffer_load_dwordx4 v128, s[20:23], s56 offen offset:768 lds\n"
        "v_mfma_f32_16x16x32_bf16 v[156:159], a[16:19], a[244:247], v[156:159]\n"
        "v_cvt_pk_bf16_f32 v120, v120, v121\n"
        "v_accvgpr_write_b32 a40, v120\n"
        "v_mfma_f32_16x16x32_bf16 v[176:179], a[16:19], a[248:251], v[176:179]\n"
        "v_cvt_pk_bf16_f32 v122, v122, v123\n"
        "v_accvgpr_write_b32 a41, v122\n"
        "v_mfma_f32_16x16x32_bf16 v[196:199], a[16:19], a[216:219], v[196:199]\n"
        "v_cvt_pk_bf16_f32 v124, v124, v125\n"
        "v_accvgpr_write_b32 a42, v124\n"
        "v_mfma_f32_16x16x32_bf16 v[216:219], a[16:19], a[220:223], v[216:219]\n"
        "buffer_load_dwordx4 v129, s[20:23], s56 offen offset:1792 lds\n"
        "v_mfma_f32_16x16x32_bf16 v[96:99], a[196:199], v[148:151], 0\n"
        "ds_read_b128 a[224:227], v241 offset:256\n"
        "v_cvt_pk_bf16_f32 v126, v126, v127\n"
        "v_mfma_f32_16x16x32_bf16 v[100:103], a[192:195], v[148:151], 0\n"
        "s_waitcnt vmcnt(10)\n"
        "ds_read_b128 a[188:191], v137 offset:12288\n"
        "ds_read_b128 a[184:187], v137 offset:13056\n"
        "v_mfma_f32_16x16x32_bf16 v[152:155], a[88:91], a[244:247], v[152:155]\n"
        "ds_read_b128 a[180:183], v137 offset:12320\n"
        "ds_read_b128 a[176:179], v137 offset:13088\n"
        "v_mfma_f32_16x16x32_bf16 v[172:175], a[88:91], a[248:251], v[172:175]\n"
        "buffer_load_dwordx4 v130, s[20:23], s56 offen offset:2816 lds\n"
        "ds_read_b128 a[172:175], v137 offset:12352\n"
        "v_mfma_f32_16x16x32_bf16 v[192:195], a[88:91], a[216:219], v[192:195]\n"
        "ds_read_b128 a[168:171], v137 offset:13120\n"
        "s_add_u32 m0, s65, 5376\n"
        "ds_read_b128 a[228:231], v241 offset:1792\n"
        "v_accvgpr_write_b32 a43, v126\n"
        "v_mfma_f32_16x16x32_bf16 v[212:215], a[88:91], a[220:223], v[212:215]\n"
        "ds_read_b128 a[232:235], v241 offset:12544\n"
        "ds_read_b128 a[236:239], v241 offset:14080\n"
        "v_mfma_f32_16x16x32_bf16 v[224:227], a[40:43], a[220:223], v[224:227]\n"
        "s_waitcnt lgkmcnt(10)\n"
        "v_lshl_add_u32 v60, v60, 2, s67\n"
        "v_lshl_add_u32 v61, v61, 2, s67\n"
        "v_lshl_add_u32 v62, v62, 2, s67\n"
        "v_mfma_f32_16x16x32_bf16 v[204:207], a[40:43], a[216:219], v[204:207]\n"
        "buffer_load_dwordx4 v128, s[20:23], s57 offen offset:768 lds\n"
        "v_lshl_add_u32 v63, v63, 2, s67\n"
        "v_mfma_f32_16x16x32_bf16 v[184:187], a[40:43], a[248:251], v[184:187]\n"
        "ds_read_b32 v60, v60\n"
        "ds_read_b32 v61, v61\n"
        "v_mfma_f32_16x16x32_bf16 v[164:167], a[40:43], a[244:247], v[164:167]\n"
        "s_waitcnt lgkmcnt(7)\n"
        "ds_read_b32 v62, v62\n"
        "ds_read_b32 v63, v63\n"
        "v_mfma_f32_16x16x32_bf16 v[104:107], a[188:191], v[148:151], 0\n"
        "v_cvt_pk_bf16_f32 v96, v96, v97\n"
        "v_accvgpr_write_b32 a64, v96\n"
        "v_mfma_f32_16x16x32_bf16 v[108:111], a[184:187], v[148:151], 0\n"
        "buffer_load_dwordx4 v129, s[20:23], s57 offen offset:1792 lds\n"
        "v_mfma_f32_16x16x32_bf16 v[88:91], a[112:115], a[220:223], v[88:91]\n"
        "v_cvt_pk_bf16_f32 v98, v98, v99\n"
        "v_accvgpr_write_b32 a65, v98\n"
        "v_mfma_f32_16x16x32_bf16 v[80:83], a[112:115], a[216:219], v[80:83]\n"
        "v_cvt_pk_bf16_f32 v100, v100, v101\n"
        "v_accvgpr_write_b32 a66, v100\n"
        "v_mfma_f32_16x16x32_bf16 v[68:71], a[112:115], a[248:251], v[68:71]\n"
        "v_cvt_pk_bf16_f32 v102, v102, v103\n"
        "v_accvgpr_write_b32 a67, v102\n"
        "v_mfma_f32_16x16x32_bf16 v[64:67], a[112:115], a[244:247], v[64:67]\n"
        "s_waitcnt vmcnt(10)\n"
        "ds_read_b64_tr_b16 a[92:93], v255 offset:15360\n"
        "ds_read_b64_tr_b16 a[94:95], v255 offset:16128\n"
        "v_mfma_f32_16x16x32_bf16 v[160:163], a[64:67], a[244:247], v[160:163]\n"
        "ds_read_b64_tr_b16 a[116:117], v255 offset:15392\n"
        "ds_read_b64_tr_b16 a[118:119], v255 offset:16160\n"
        "v_mfma_f32_16x16x32_bf16 v[180:183], a[64:67], a[248:251], v[180:183]\n"
        "ds_read_b64_tr_b16 a[140:141], v255 offset:15424\n"
        "ds_read_b64_tr_b16 a[142:143], v255 offset:16192\n"
        "v_mfma_f32_16x16x32_bf16 v[200:203], a[64:67], a[216:219], v[200:203]\n"
        "v_cvt_pk_bf16_f32 v104, v104, v105\n"
        "v_accvgpr_write_b32 a20, v104\n"
        "v_mfma_f32_16x16x32_bf16 v[220:223], a[64:67], a[220:223], v[220:223]\n"
        "v_cvt_pk_bf16_f32 v106, v106, v107\n"
        "v_accvgpr_write_b32 a21, v106\n"
        "v_mfma_f32_16x16x32_bf16 v[112:115], a[180:183], v[148:151], 0\n"
        "v_cvt_pk_bf16_f32 v108, v108, v109\n"
        "v_accvgpr_write_b32 a22, v108\n"
        "v_mfma_f32_16x16x32_bf16 v[116:119], a[176:179], v[148:151], 0\n"
        "v_cvt_pk_bf16_f32 v110, v110, v111\n"
        "v_accvgpr_write_b32 a23, v110\n"
        "v_mfma_f32_16x16x32_bf16 v[72:75], a[136:139], a[244:247], v[72:75]\n"
        "v_mfma_f32_16x16x32_bf16 v[76:79], a[136:139], a[248:251], v[76:79]\n"
        "v_mfma_f32_16x16x32_bf16 v[156:159], a[20:23], a[224:227], v[156:159]\n"
        "s_waitcnt lgkmcnt(0)\n"
        "v_cvt_pk_bf16_f32 v112, v112, v113\n"
        "v_accvgpr_write_b32 a44, v112\n"
        "v_mfma_f32_16x16x32_bf16 v[176:179], a[20:23], a[228:231], v[176:179]\n"
        "v_cvt_pk_bf16_f32 v114, v114, v115\n"
        "v_accvgpr_write_b32 a45, v114\n"
        "v_mfma_f32_16x16x32_bf16 v[196:199], a[20:23], a[232:235], v[196:199]\n"
        "v_cvt_pk_bf16_f32 v116, v116, v117\n"
        "v_accvgpr_write_b32 a46, v116\n"
        "v_cvt_pk_bf16_f32 v156, v156, v157\n"
        "v_mfma_f32_16x16x32_bf16 v[216:219], a[20:23], a[236:239], v[216:219]\n"
        "v_cvt_pk_bf16_f32 v157, v158, v159\n"
        "v_cvt_pk_bf16_f32 v176, v176, v177\n"
        "v_cvt_pk_bf16_f32 v177, v178, v179\n"
        "v_mfma_f32_16x16x32_bf16 v[120:123], a[172:175], v[148:151], 0\n"
        "v_cvt_pk_bf16_f32 v196, v196, v197\n"
        "v_cvt_pk_bf16_f32 v197, v198, v199\n"
        "v_mfma_f32_16x16x32_bf16 v[124:127], a[168:171], v[148:151], 0\n"
        "v_cvt_pk_bf16_f32 v118, v118, v119\n"
        "v_cvt_pk_bf16_f32 v216, v216, v217\n"
        "v_cvt_pk_bf16_f32 v217, v218, v219\n"
        "v_mfma_f32_16x16x32_bf16 v[84:87], a[136:139], a[216:219], v[84:87]\n"
        "v_accvgpr_write_b32 a47, v118\n"
        "v_cvt_pk_bf16_f32 v120, v120, v121\n"
        "v_mfma_f32_16x16x32_bf16 v[92:95], a[136:139], a[220:223], v[92:95]\n"
        "v_accvgpr_write_b32 a68, v120\n"
        "v_cvt_pk_bf16_f32 v122, v122, v123\n"
        "v_mfma_f32_16x16x32_bf16 v[224:227], a[44:47], a[236:239], v[224:227]\n"
        "v_accvgpr_write_b32 a69, v122\n"
        "v_cvt_pk_bf16_f32 v124, v124, v125\n"
        "v_mfma_f32_16x16x32_bf16 v[204:207], a[44:47], a[232:235], v[204:207]\n"
        "v_accvgpr_write_b32 a70, v124\n"
        "v_cvt_pk_bf16_f32 v126, v126, v127\n"
        "v_mfma_f32_16x16x32_bf16 v[184:187], a[44:47], a[228:231], v[184:187]\n"
        "v_accvgpr_write_b32 a71, v126\n"
        "s_nop 0\n"
        "v_cvt_pk_bf16_f32 v218, v224, v225\n"
        "v_mfma_f32_16x16x32_bf16 v[164:167], a[44:47], a[224:227], v[164:167]\n"
        "v_cvt_pk_bf16_f32 v219, v226, v227\n"
        "v_cvt_pk_bf16_f32 v198, v204, v205\n"
        "v_cvt_pk_bf16_f32 v199, v206, v207\n"
        "v_mfma_f32_16x16x32_bf16 v[152:155], a[92:95], a[224:227], v[152:155]\n"
        "v_cvt_pk_bf16_f32 v178, v184, v185\n"
        "v_cvt_pk_bf16_f32 v179, v186, v187\n"
        "v_mfma_f32_16x16x32_bf16 v[64:67], a[116:119], a[224:227], v[64:67]\n"
        "buffer_load_dwordx4 v130, s[20:23], s57 offen offset:2816 lds\n"
        "v_cvt_pk_bf16_f32 v158, v164, v165\n"
        "v_mfma_f32_16x16x32_bf16 v[72:75], a[140:143], a[224:227], v[72:75]\n"
        "s_add_u32 m0, s65, 8448\n"
        "v_cvt_pk_bf16_f32 v159, v166, v167\n"
        "v_cvt_pk_bf16_f32 v152, v152, v153\n"
        "v_mfma_f32_16x16x32_bf16 v[172:175], a[92:95], a[228:231], v[172:175]\n"
        "v_cvt_pk_bf16_f32 v153, v154, v155\n"
        "v_cvt_pk_bf16_f32 v154, v64, v65\n"
        "v_mfma_f32_16x16x32_bf16 v[160:163], a[68:71], a[224:227], v[160:163]\n"
        "v_cvt_pk_bf16_f32 v155, v66, v67\n"
        "v_cvt_pk_bf16_f32 v168, v72, v73\n"
        "v_mfma_f32_16x16x32_bf16 v[180:183], a[68:71], a[228:231], v[180:183]\n"
        "buffer_load_dwordx4 v128, s[20:23], s58 offen offset:768 lds\n"
        "v_mfma_f32_16x16x32_bf16 v[200:203], a[68:71], a[232:235], v[200:203]\n"
        "v_cvt_pk_bf16_f32 v169, v74, v75\n"
        "v_cvt_pk_bf16_f32 v172, v172, v173\n"
        "v_mfma_f32_16x16x32_bf16 v[220:223], a[68:71], a[236:239], v[220:223]\n"
        "v_cvt_pk_bf16_f32 v166, v160, v161\n"
        "v_cvt_pk_bf16_f32 v160, v160, v161\n"
        "v_cvt_pk_bf16_f32 v161, v162, v163\n"
        "v_cvt_pk_bf16_f32 v167, v162, v163\n"
        "v_cvt_pk_bf16_f32 v186, v180, v181\n"
        "v_cvt_pk_bf16_f32 v180, v180, v181\n"
        "v_mfma_f32_16x16x32_bf16 v[68:71], a[116:119], a[228:231], v[68:71]\n"
        "buffer_load_dwordx4 v129, s[20:23], s58 offen offset:1792 lds\n"
        "v_cvt_pk_bf16_f32 v181, v182, v183\n"
        "v_cvt_pk_bf16_f32 v187, v182, v183\n"
        "v_cvt_pk_bf16_f32 v206, v200, v201\n"
        "v_cvt_pk_bf16_f32 v200, v200, v201\n"
        "v_mfma_f32_16x16x32_bf16 v[76:79], a[140:143], a[228:231], v[76:79]\n"
        "s_waitcnt vmcnt(11)\n"
        "s_barrier\n"
        "ds_read_b128 v[162:165], v251 offset:0\n"
        "ds_read_b128 v[182:185], v251 offset:512\n"
        "v_cvt_pk_bf16_f32 v201, v202, v203\n"
        "v_cvt_pk_bf16_f32 v207, v202, v203\n"
        "v_cvt_pk_bf16_f32 v226, v220, v221\n"
        "v_cvt_pk_bf16_f32 v220, v220, v221\n"
        "v_mfma_f32_16x16x32_bf16 v[192:195], a[92:95], a[232:235], v[192:195]\n"
        "ds_read_b128 v[202:205], v251 offset:4096\n"
        "ds_read_b128 a[240:243], v240 offset:0\n"
        "v_cvt_pk_bf16_f32 v221, v222, v223\n"
        "v_cvt_pk_bf16_f32 v227, v222, v223\n"
        "v_cvt_pk_bf16_f32 v173, v174, v175\n"
        "v_mfma_f32_16x16x32_bf16 v[80:83], a[116:119], a[232:235], v[80:83]\n"
        "buffer_load_dwordx4 v130, s[20:23], s58 offen offset:2816 lds\n"
        "v_mfma_f32_16x16x32_bf16 v[84:87], a[140:143], a[232:235], v[84:87]\n"
        "s_add_u32 m0, s65, 11520\n"
        "ds_read_b128 a[244:247], v241 offset:0\n"
        "ds_read_b128 a[248:251], v240 offset:128\n"
        "v_mfma_f32_16x16x32_bf16 v[212:215], a[92:95], a[236:239], v[212:215]\n"
        "v_cvt_pk_bf16_f32 v174, v68, v69\n"
        "v_cvt_pk_bf16_f32 v175, v70, v71\n"
        "v_mfma_f32_16x16x32_bf16 v[88:91], a[116:119], a[236:239], v[88:91]\n"
        "buffer_load_dwordx4 v128, s[20:23], s59 offen offset:768 lds\n"
        "v_mfma_f32_16x16x32_bf16 v[92:95], a[140:143], a[236:239], v[92:95]\n"
        "s_waitcnt vmcnt(10)\n"
        "ds_read_b64_tr_b16 a[144:145], v255 offset:0\n"
        "ds_read_b64_tr_b16 a[146:147], v255 offset:768\n"
        "v_mfma_f32_16x16x32_bf16 v[64:67], v[152:155], v[156:159], v[0:3]\n"
        "s_waitcnt lgkmcnt(6)\n"
        "ds_read_b64_tr_b16 a[168:169], v255 offset:32\n"
        "ds_read_b64_tr_b16 a[170:171], v255 offset:800\n"
        "v_cvt_pk_bf16_f32 v190, v76, v77\n"
        "v_cvt_pk_bf16_f32 v191, v78, v79\n"
        "v_mfma_f32_16x16x32_bf16 v[64:67], v[168:171], v[160:163], v[64:67]\n"
        "ds_read_b64_tr_b16 a[192:193], v255 offset:64\n"
        "ds_read_b64_tr_b16 a[194:195], v255 offset:832\n"
        "v_cvt_pk_bf16_f32 v192, v192, v193\n"
        "v_cvt_pk_bf16_f32 v193, v194, v195\n"
        "v_mfma_f32_16x16x32_bf16 v[96:99], v[172:175], v[156:159], v[4:7]\n"
        "buffer_load_dwordx4 v129, s[20:23], s59 offen offset:1792 lds\n"
        "v_cvt_pk_bf16_f32 v194, v80, v81\n"
        "v_cvt_pk_bf16_f32 v195, v82, v83\n"
        "v_exp_f32 v64, v64\n"
        "v_mfma_f32_16x16x32_bf16 v[96:99], v[188:191], v[164:167], v[96:99]\n"
        "ds_read_b128 v[162:165], v252 offset:0\n"
        "v_exp_f32 v65, v65\n"
        "v_exp_f32 v66, v66\n"
        "v_cvt_pk_bf16_f32 v64, v64, v65\n"
        "v_mfma_f32_16x16x32_bf16 v[72:75], v[152:155], v[176:179], v[16:19]\n"
        "buffer_load_dwordx4 v130, s[20:23], s59 offen offset:2816 lds\n"
        "v_cvt_pk_bf16_f32 v208, v84, v85\n"
        "v_cvt_pk_bf16_f32 v209, v86, v87\n"
        "v_exp_f32 v96, v96\n"
        "v_mfma_f32_16x16x32_bf16 v[72:75], v[168:171], v[180:183], v[72:75]\n"
        "s_add_u32 m0, s65, 14592\n"
        "s_waitcnt vmcnt(9)\n"
        "ds_read_b64_tr_b16 a[148:149], v255 offset:3072\n"
        "ds_read_b64_tr_b16 a[150:151], v255 offset:3840\n"
        "v_exp_f32 v67, v67\n"
        "v_exp_f32 v97, v97\n"
        "v_mfma_f32_16x16x32_bf16 v[100:103], v[172:175], v[176:179], v[20:23]\n"
        "buffer_load_dwordx4 v128, s[20:23], s60 offen offset:768 lds\n"
        "ds_read_b64_tr_b16 a[172:173], v255 offset:3104\n"
        "ds_read_b64_tr_b16 a[174:175], v255 offset:3872\n"
        "v_cvt_pk_bf16_f32 v212, v212, v213\n"
        "v_cvt_pk_bf16_f32 v213, v214, v215\n"
        "v_mfma_f32_16x16x32_bf16 v[100:103], v[188:191], v[184:187], v[100:103]\n"
        "ds_read_b128 v[182:185], v252 offset:512\n"
        "ds_read_b64_tr_b16 a[196:197], v255 offset:3136\n"
        "ds_read_b64_tr_b16 a[198:199], v255 offset:3904\n"
        "v_cvt_pk_bf16_f32 v65, v66, v67\n"
        "v_exp_f32 v98, v98\n"
        "v_mfma_f32_16x16x32_bf16 v[68:71], v[192:195], v[156:159], v[8:11]\n"
        "s_waitcnt lgkmcnt(7)\n"
        "ds_read_b128 a[216:219], v240 offset:1536\n"
        "ds_read_b128 a[220:223], v241 offset:1536\n"
        "v_cvt_pk_bf16_f32 v66, v96, v97\n"
        "v_exp_f32 v72, v72\n"
        "v_cvt_pk_bf16_f32 v214, v88, v89\n"
        "v_mfma_f32_16x16x32_bf16 v[68:71], v[208:211], v[160:163], v[68:71]\n"
        "buffer_load_dwordx4 v129, s[20:23], s60 offen offset:1792 lds\n"
        "s_waitcnt vmcnt(8)\n"
        "ds_read_b64_tr_b16 a[152:153], v255 offset:6144\n"
        "ds_read_b64_tr_b16 a[154:155], v255 offset:6912\n"
        "v_cvt_pk_bf16_f32 v215, v90, v91\n"
        "v_cvt_pk_bf16_f32 v230, v92, v93\n"
        "v_mfma_f32_16x16x32_bf16 v[76:79], v[192:195], v[176:179], v[24:27]\n"
        "s_waitcnt lgkmcnt(6)\n"
        "ds_read_b64_tr_b16 a[176:177], v255 offset:6176\n"
        "ds_read_b64_tr_b16 a[178:179], v255 offset:6944\n"
        "v_exp_f32 v99, v99\n"
        "v_exp_f32 v73, v73\n"
        "v_mfma_f32_16x16x32_bf16 v[76:79], v[208:211], v[180:183], v[76:79]\n"
        "buffer_load_dwordx4 v130, s[20:23], s60 offen offset:2816 lds\n"
        "ds_read_b64_tr_b16 a[200:201], v255 offset:6208\n"
        "ds_read_b64_tr_b16 a[202:203], v255 offset:6976\n"
        "v_cvt_pk_bf16_f32 v231, v94, v95\n"
        "v_cvt_pk_bf16_f32 v67, v98, v99\n"
        "v_mfma_f32_16x16x32_bf16 v[104:107], v[212:215], v[156:159], v[12:15]\n"
        ".long 0xbec200ff\n"
        "42:\n"
        "s_branch 60b\n"
        "s_add_u32 m0, s40, 98304\n"
        "ds_read_b128 a[224:227], v240 offset:1664\n"
        "v_exp_f32 v74, v74\n"
        "v_mfma_f32_16x16x32_bf16 v[104:107], v[228:231], v[164:167], v[104:107]\n"
        "s_sub_u32 s64, s47, 6291456\n"
        "v_cvt_pk_bf16_f32 v72, v72, v73\n"
        "v_exp_f32 v100, v100\n"
        "v_mfma_f32_16x16x32_bf16 v[108:111], v[212:215], v[176:179], v[28:31]\n"
        "buffer_load_dwordx4 v242, s[8:11], s64 offen lds\n"
        "v_mfma_f32_16x16x32_bf16 v[108:111], v[228:231], v[184:187], v[108:111]\n"
        "s_add_u32 m0, s40, 99328\n"
        "v_exp_f32 v75, v75\n"
        "v_mfma_f32_16x16x32_bf16 v[80:83], v[152:155], v[196:199], v[32:35]\n"
        "ds_read_b128 a[228:231], v240 offset:12288\n"
        "v_exp_f32 v101, v101\n"
        "v_mfma_f32_16x16x32_bf16 v[80:83], v[168:171], v[200:203], v[80:83]\n"
        "buffer_load_dwordx4 v243, s[8:11], s64 offen lds\n"
        "v_cvt_pk_bf16_f32 v73, v74, v75\n"
        "v_mfma_f32_16x16x32_bf16 v[112:115], a[240:243], a[144:147], 0\n"
        "s_add_u32 m0, s40, 100352\n"
        "ds_read_b128 a[232:235], v241 offset:12288\n"
        "ds_read_b128 a[236:239], v240 offset:12416\n"
        "v_cvt_pk_bf16_f32 v74, v100, v101\n"
        "v_mfma_f32_16x16x32_bf16 v[112:115], a[244:247], a[148:151], v[112:115]\n"
        "s_waitcnt lgkmcnt(3)\n"
        "v_exp_f32 v102, v102\n"
        "v_mfma_f32_16x16x32_bf16 v[112:115], a[248:251], a[152:155], v[112:115]\n"
        "buffer_load_dwordx4 v244, s[8:11], s64 offen lds\n"
        "v_mfma_f32_16x16x32_bf16 v[116:119], a[248:251], a[176:179], 0\n"
        "s_add_u32 m0, s40, 110592\n"
        "v_exp_f32 v68, v68\n"
        "v_mfma_f32_16x16x32_bf16 v[116:119], a[244:247], a[172:175], v[116:119]\n"
        "s_sub_u32 s64, s47, 5505024\n"
        "v_exp_f32 v103, v103\n"
        "v_mfma_f32_16x16x32_bf16 v[116:119], a[240:243], a[168:171], v[116:119]\n"
        "buffer_load_dwordx4 v242, s[8:11], s64 offen lds\n"
        "v_cvt_pk_bf16_f32 v75, v102, v103\n"
        "v_mfma_f32_16x16x32_bf16 v[96:99], v[172:175], v[196:199], v[36:39]\n"
        "s_add_u32 m0, s40, 111616\n"
        "s_waitcnt vmcnt(10)\n"
        "ds_read_b64_tr_b16 a[156:157], v255 offset:9216\n"
        "ds_read_b64_tr_b16 a[158:159], v255 offset:9984\n"
        "v_mfma_f32_16x16x32_bf16 v[96:99], v[188:191], v[204:207], v[96:99]\n"
        "ds_read_b64_tr_b16 a[180:181], v255 offset:9248\n"
        "ds_read_b64_tr_b16 a[182:183], v255 offset:10016\n"
        "ds_read_b128 v[202:205], v252 offset:4096\n"
        "v_mfma_f32_16x16x32_bf16 v[120:123], a[240:243], a[192:195], 0\n"
        "buffer_load_dwordx4 v243, s[8:11], s64 offen lds\n"
        "ds_read_b64_tr_b16 a[204:205], v255 offset:9280\n"
        "v_mfma_f32_16x16x32_bf16 v[120:123], a[244:247], a[196:199], v[120:123]\n"
        "ds_read_b64_tr_b16 a[206:207], v255 offset:10048\n"
        "s_add_u32 m0, s40, 112640\n"
        "ds_read_b128 a[240:243], v240 offset:13824\n"
        "v_mfma_f32_16x16x32_bf16 v[120:123], a[248:251], a[200:203], v[120:123]\n"
        "s_waitcnt vmcnt(8)\n"
        "ds_read_b64_tr_b16 a[160:161], v255 offset:12288\n"
        "ds_read_b64_tr_b16 a[162:163], v255 offset:13056\n"
        "v_mfma_f32_16x16x32_bf16 v[124:127], a[216:219], a[144:147], 0\n"
        "buffer_load_dwordx4 v244, s[8:11], s64 offen lds\n"
        "ds_read_b64_tr_b16 a[184:185], v255 offset:12320\n"
        "v_mfma_f32_16x16x32_bf16 v[124:127], a[220:223], a[148:151], v[124:127]\n"
        "ds_read_b64_tr_b16 a[186:187], v255 offset:13088\n"
        "s_xor_b32 m0, s48, 8192\n"
        "ds_read_b64_tr_b16 a[208:209], v255 offset:12352\n"
        "ds_read_b64_tr_b16 a[210:211], v255 offset:13120\n"
        "v_mfma_f32_16x16x32_bf16 v[124:127], a[224:227], a[152:155], v[124:127]\n"
        "s_sub_u32 s64, s36, 2097152\n"
        "ds_read_b128 a[244:247], v241 offset:13824\n"
        "ds_read_b128 a[248:251], v240 offset:13952\n"
        "v_mfma_f32_16x16x32_bf16 v[156:159], a[224:227], a[176:179], 0\n"
        "buffer_load_dwordx4 v249, s[12:15], s64 offen lds\n"
        "v_mfma_f32_16x16x32_bf16 v[156:159], a[220:223], a[172:175], v[156:159]\n"
        "ds_read_b128 v[222:225], v251 offset:4608\n"
        "v_exp_f32 v69, v69\n"
        "v_mfma_f32_16x16x32_bf16 v[156:159], a[216:219], a[168:171], v[156:159]\n"
        "v_exp_f32 v70, v70\n"
        "v_cvt_pk_bf16_f32 v68, v68, v69\n"
        "v_mfma_f32_16x16x32_bf16 v[84:87], v[192:195], v[196:199], v[40:43]\n"
        "s_waitcnt lgkmcnt(7)\n"
        "v_exp_f32 v76, v76\n"
        "v_mfma_f32_16x16x32_bf16 v[84:87], v[208:211], v[200:203], v[84:87]\n"
        "buffer_load_dwordx4 v253, s[12:15], s64 offen offset:1024 lds\n"
        "v_mfma_f32_16x16x32_bf16 v[160:163], a[216:219], a[192:195], 0\n"
        "s_add_u32 m0, s65, 12288\n"
        "v_exp_f32 v71, v71\n"
        "v_mfma_f32_16x16x32_bf16 v[160:163], a[220:223], a[196:199], v[160:163]\n"
        "ds_read_b128 a[216:219], v241 offset:128\n"
        "v_exp_f32 v77, v77\n"
        "v_mfma_f32_16x16x32_bf16 v[160:163], a[224:227], a[200:203], v[160:163]\n"
        "ds_read_b128 a[220:223], v240 offset:256\n"
        "v_cvt_pk_bf16_f32 v69, v70, v71\n"
        "v_cvt_pk_bf16_f32 v76, v76, v77\n"
        "v_mfma_f32_16x16x32_bf16 v[164:167], a[228:231], a[144:147], 0\n"
        "s_waitcnt lgkmcnt(2)\n"
        "buffer_load_dword v136, s[52:55], 0 offen lds\n"
        "v_mfma_f32_16x16x32_bf16 v[164:167], a[232:235], a[148:151], v[164:167]\n"
        "ds_read_b128 a[224:227], v241 offset:256\n"
        "v_exp_f32 v78, v78\n"
        "v_mfma_f32_16x16x32_bf16 v[164:167], a[236:239], a[152:155], v[164:167]\n"
        "v_exp_f32 v104, v104\n"
        "v_mfma_f32_16x16x32_bf16 v[100:103], v[212:215], v[196:199], v[44:47]\n"
        "v_exp_f32 v79, v79\n"
        "v_mfma_f32_16x16x32_bf16 v[100:103], v[228:231], v[204:207], v[100:103]\n"
        "v_exp_f32 v105, v105\n"
        "v_cvt_pk_bf16_f32 v77, v78, v79\n"
        "v_mfma_f32_16x16x32_bf16 v[176:179], a[236:239], a[176:179], 0\n"
        "v_exp_f32 v106, v106\n"
        "v_cvt_pk_bf16_f32 v70, v104, v105\n"
        "v_mfma_f32_16x16x32_bf16 v[176:179], a[232:235], a[172:175], v[176:179]\n"
        "v_exp_f32 v108, v108\n"
        "v_mfma_f32_16x16x32_bf16 v[176:179], a[228:231], a[168:171], v[176:179]\n"
        "s_waitcnt vmcnt(9)\n"
        "ds_read_b64_tr_b16 a[164:165], v255 offset:15360\n"
        "ds_read_b64_tr_b16 a[166:167], v255 offset:16128\n"
        "v_mfma_f32_16x16x32_bf16 v[180:183], a[228:231], a[192:195], 0\n"
        "ds_read_b64_tr_b16 a[188:189], v255 offset:15392\n"
        "ds_read_b64_tr_b16 a[190:191], v255 offset:16160\n"
        "v_mfma_f32_16x16x32_bf16 v[180:183], a[232:235], a[196:199], v[180:183]\n"
        "ds_read_b64_tr_b16 a[212:213], v255 offset:15424\n"
        "ds_read_b64_tr_b16 a[214:215], v255 offset:16192\n"
        "v_mfma_f32_16x16x32_bf16 v[180:183], a[236:239], a[200:203], v[180:183]\n"
        "ds_read_b128 a[228:231], v241 offset:1664\n"
        "ds_read_b128 a[232:235], v240 offset:1792\n"
        "v_mfma_f32_16x16x32_bf16 v[184:187], a[240:243], a[144:147], 0\n"
        "ds_read_b128 a[236:239], v241 offset:1792\n"
        "v_exp_f32 v107, v107\n"
        "v_mfma_f32_16x16x32_bf16 v[184:187], a[244:247], a[148:151], v[184:187]\n"
        "v_exp_f32 v109, v109\n"
        "v_cvt_pk_bf16_f32 v71, v106, v107\n"
        "v_mfma_f32_16x16x32_bf16 v[184:187], a[248:251], a[152:155], v[184:187]\n"
        "v_exp_f32 v110, v110\n"
        "v_cvt_pk_bf16_f32 v78, v108, v109\n"
        "v_mfma_f32_16x16x32_bf16 v[196:199], a[248:251], a[176:179], 0\n"
        "v_exp_f32 v111, v111\n"
        "v_mfma_f32_16x16x32_bf16 v[196:199], a[244:247], a[172:175], v[196:199]\n"
        "v_cvt_pk_bf16_f32 v79, v110, v111\n"
        "v_exp_f32 v80, v80\n"
        "v_mfma_f32_16x16x32_bf16 v[196:199], a[240:243], a[168:171], v[196:199]\n"
        "v_exp_f32 v81, v81\n"
        "v_mfma_f32_16x16x32_bf16 v[200:203], a[240:243], a[192:195], 0\n"
        "v_exp_f32 v82, v82\n"
        "v_cvt_pk_bf16_f32 v80, v80, v81\n"
        "v_mfma_f32_16x16x32_bf16 v[200:203], a[244:247], a[196:199], v[200:203]\n"
        "ds_read_b128 a[240:243], v241 offset:12416\n"
        "v_exp_f32 v83, v83\n"
        "v_mfma_f32_16x16x32_bf16 v[200:203], a[248:251], a[200:203], v[200:203]\n"
        "ds_read_b128 a[244:247], v240 offset:12544\n"
        "v_cvt_pk_bf16_f32 v81, v82, v83\n"
        "v_mfma_f32_16x16x32_bf16 v[88:91], v[152:155], v[216:219], v[48:51]\n"
        "ds_read_b128 a[248:251], v241 offset:12544\n"
        "v_exp_f32 v96, v96\n"
        "v_mfma_f32_16x16x32_bf16 v[88:91], v[168:171], v[220:223], v[88:91]\n"
        "s_waitcnt lgkmcnt(3)\n"
        "v_exp_f32 v97, v97\n"
        "v_mfma_f32_16x16x32_bf16 v[112:115], a[216:219], a[156:159], v[112:115]\n"
        "v_exp_f32 v98, v98\n"
        "v_cvt_pk_bf16_f32 v82, v96, v97\n"
        "v_mfma_f32_16x16x32_bf16 v[112:115], a[220:223], a[160:163], v[112:115]\n"
        "v_exp_f32 v99, v99\n"
        "v_mfma_f32_16x16x32_bf16 v[112:115], a[224:227], a[164:167], v[112:115]\n"
        "v_cvt_pk_bf16_f32 v83, v98, v99\n"
        "v_exp_f32 v84, v84\n"
        "v_mfma_f32_16x16x32_bf16 v[116:119], a[224:227], a[188:191], v[116:119]\n"
        "v_exp_f32 v85, v85\n"
        "v_mfma_f32_16x16x32_bf16 v[116:119], a[220:223], a[184:187], v[116:119]\n"
        "v_exp_f32 v86, v86\n"
        "v_cvt_pk_bf16_f32 v84, v84, v85\n"
        "v_mfma_f32_16x16x32_bf16 v[116:119], a[216:219], a[180:183], v[116:119]\n"
        "v_exp_f32 v87, v87\n"
        "v_mfma_f32_16x16x32_bf16 v[120:123], a[216:219], a[204:207], v[120:123]\n"
        "v_cvt_pk_bf16_f32 v85, v86, v87\n"
        "v_exp_f32 v100, v100\n"
        "v_mfma_f32_16x16x32_bf16 v[120:123], a[220:223], a[208:211], v[120:123]\n"
        "ds_read_b128 a[216:219], v241 offset:13952\n"
        "v_exp_f32 v101, v101\n"
        "v_mfma_f32_16x16x32_bf16 v[120:123], a[224:227], a[212:215], v[120:123]\n"
        "ds_read_b128 a[220:223], v240 offset:14080\n"
        "v_exp_f32 v102, v102\n"
        "v_mfma_f32_16x16x32_bf16 v[124:127], a[228:231], a[156:159], v[124:127]\n"
        "ds_read_b128 a[224:227], v241 offset:14080\n"
        "v_cvt_pk_bf16_f32 v86, v100, v101\n"
        "v_cvt_pk_bf16_f32 v128, v112, v113\n"
        "v_mfma_f32_16x16x32_bf16 v[124:127], a[232:235], a[160:163], v[124:127]\n"
        "v_exp_f32 v103, v103\n"
        "v_cvt_pk_bf16_f32 v129, v114, v115\n"
        "v_mfma_f32_16x16x32_bf16 v[124:127], a[236:239], a[164:167], v[124:127]\n"
        "v_cvt_pk_bf16_f32 v87, v102, v103\n"
        "v_cvt_pk_bf16_f32 v136, v116, v117\n"
        "v_mfma_f32_16x16x32_bf16 v[104:107], v[172:175], v[216:219], v[52:55]\n"
        "v_cvt_pk_bf16_f32 v137, v118, v119\n"
        "v_cvt_pk_bf16_f32 v144, v120, v121\n"
        "v_mfma_f32_16x16x32_bf16 v[104:107], v[188:191], v[224:227], v[104:107]\n"
        "ds_read_b128 v[222:225], v252 offset:4608\n"
        "v_cvt_pk_bf16_f32 v145, v122, v123\n"
        "v_mfma_f32_16x16x32_bf16 v[156:159], a[236:239], a[188:191], v[156:159]\n"
        "v_exp_f32 v88, v88\n"
        "v_mfma_f32_16x16x32_bf16 v[156:159], a[232:235], a[184:187], v[156:159]\n"
        "v_cvt_pk_bf16_f32 v130, v124, v125\n"
        "v_cvt_pk_bf16_f32 v131, v126, v127\n"
        "v_mfma_f32_16x16x32_bf16 v[156:159], a[228:231], a[180:183], v[156:159]\n"
        "v_exp_f32 v89, v89\n"
        "v_mfma_f32_16x16x32_bf16 v[160:163], a[228:231], a[204:207], v[160:163]\n"
        "v_exp_f32 v90, v90\n"
        "v_mfma_f32_16x16x32_bf16 v[160:163], a[232:235], a[208:211], v[160:163]\n"
        "v_exp_f32 v91, v91\n"
        "v_mfma_f32_16x16x32_bf16 v[160:163], a[236:239], a[212:215], v[160:163]\n"
        "s_waitcnt lgkmcnt(1)\n"
        "v_exp_f32 v104, v104\n"
        "v_mfma_f32_16x16x32_bf16 v[164:167], a[240:243], a[156:159], v[164:167]\n"
        "v_cvt_pk_bf16_f32 v138, v156, v157\n"
        "v_cvt_pk_bf16_f32 v139, v158, v159\n"
        "v_mfma_f32_16x16x32_bf16 v[164:167], a[244:247], a[160:163], v[164:167]\n"
        "v_cvt_pk_bf16_f32 v88, v88, v89\n"
        "v_cvt_pk_bf16_f32 v89, v90, v91\n"
        "v_mfma_f32_16x16x32_bf16 v[164:167], a[248:251], a[164:167], v[164:167]\n"
        "v_exp_f32 v105, v105\n"
        "v_mfma_f32_16x16x32_bf16 v[92:95], v[192:195], v[216:219], v[56:59]\n"
        "s_waitcnt lgkmcnt(0)\n"
        "v_cvt_pk_bf16_f32 v146, v160, v161\n"
        "v_cvt_pk_bf16_f32 v147, v162, v163\n"
        "v_mfma_f32_16x16x32_bf16 v[92:95], v[208:211], v[220:223], v[92:95]\n"
        "v_exp_f32 v106, v106\n"
        "v_mfma_f32_16x16x32_bf16 v[176:179], a[248:251], a[188:191], v[176:179]\n"
        "v_exp_f32 v107, v107\n"
        "v_mfma_f32_16x16x32_bf16 v[176:179], a[244:247], a[184:187], v[176:179]\n"
        "v_cvt_pk_bf16_f32 v132, v164, v165\n"
        "v_cvt_pk_bf16_f32 v133, v166, v167\n"
        "v_mfma_f32_16x16x32_bf16 v[176:179], a[240:243], a[180:183], v[176:179]\n"
        "v_cvt_pk_bf16_f32 v90, v104, v105\n"
        "v_cvt_pk_bf16_f32 v91, v106, v107\n"
        "v_mfma_f32_16x16x32_bf16 v[180:183], a[240:243], a[204:207], v[180:183]\n"
        "v_exp_f32 v92, v92\n"
        "v_mfma_f32_16x16x32_bf16 v[180:183], a[244:247], a[208:211], v[180:183]\n"
        "v_exp_f32 v93, v93\n"
        "v_mfma_f32_16x16x32_bf16 v[180:183], a[248:251], a[212:215], v[180:183]\n"
        "v_mov_b64 v[96:97], 0\n"
        "v_cvt_pk_bf16_f32 v140, v176, v177\n"
        "v_mfma_f32_16x16x32_bf16 v[184:187], a[216:219], a[156:159], v[184:187]\n"
        "v_cvt_pk_bf16_f32 v141, v178, v179\n"
        "v_cvt_pk_bf16_f32 v92, v92, v93\n"
        "v_mfma_f32_16x16x32_bf16 v[184:187], a[220:223], a[160:163], v[184:187]\n"
        "v_exp_f32 v94, v94\n"
        "v_mfma_f32_16x16x32_bf16 v[184:187], a[224:227], a[164:167], v[184:187]\n"
        "v_exp_f32 v95, v95\n"
        "v_mfma_f32_16x16x32_bf16 v[108:111], v[212:215], v[216:219], v[60:63]\n"
        "v_cvt_pk_bf16_f32 v148, v180, v181\n"
        "v_cvt_pk_bf16_f32 v149, v182, v183\n"
        "v_mfma_f32_16x16x32_bf16 v[108:111], v[228:231], v[224:227], v[108:111]\n"
        "v_add_u32 v240, 24576, v240\n"
        "v_add_u32 v241, 24576, v241\n"
        "v_mfma_f32_16x16x32_bf16 v[196:199], a[224:227], a[188:191], v[196:199]\n"
        "v_cvt_pk_bf16_f32 v134, v184, v185\n"
        "v_cvt_pk_bf16_f32 v135, v186, v187\n"
        "v_mfma_f32_16x16x32_bf16 v[196:199], a[220:223], a[184:187], v[196:199]\n"
        "v_mov_b64 v[172:173], 0\n"
        "v_cvt_pk_bf16_f32 v93, v94, v95\n"
        "v_mfma_f32_16x16x32_bf16 v[196:199], a[216:219], a[180:183], v[196:199]\n"
        "v_exp_f32 v108, v108\n"
        "v_mfma_f32_16x16x32_bf16 v[200:203], a[216:219], a[204:207], v[200:203]\n"
        "v_exp_f32 v109, v109\n"
        "v_mfma_f32_16x16x32_bf16 v[200:203], a[220:223], a[208:211], v[200:203]\n"
        "v_exp_f32 v110, v110\n"
        "v_mfma_f32_16x16x32_bf16 v[200:203], a[224:227], a[212:215], v[200:203]\n"
        "v_exp_f32 v111, v111\n"
        "v_smfmac_f32_16x16x64_bf16 v[96:99], a[252:255], v[64:71], v237\n"
        "v_cvt_pk_bf16_f32 v142, v196, v197\n"
        "v_mfma_f32_16x16x32_bf16 v[100:103], v[132:135], v[68:71], 0\n"
        "v_cvt_pk_bf16_f32 v143, v198, v199\n"
        "v_cvt_pk_bf16_f32 v94, v108, v109\n"
        "v_mfma_f32_16x16x32_bf16 v[100:103], v[128:131], v[64:67], v[100:103]\n"
        "v_mov_b64 v[192:193], 0\n"
        "v_cvt_pk_bf16_f32 v150, v200, v201\n"
        "v_mfma_f32_16x16x32_bf16 v[104:107], v[136:139], v[64:67], 0\n"
        "v_cvt_pk_bf16_f32 v151, v202, v203\n"
        "v_add_f32 v97, v96, v97\n"
        "v_mfma_f32_16x16x32_bf16 v[104:107], v[140:143], v[68:71], v[104:107]\n"
        "v_rcp_f32 v96, v97\n"
        "v_cvt_pk_bf16_f32 v95, v110, v111\n"
        "v_mfma_f32_16x16x32_bf16 v[108:111], v[148:151], v[68:71], 0\n"
        "v_mul_f32 v100, v96, v100\n"
        "v_mul_f32 v101, v96, v101\n"
        "v_mfma_f32_16x16x32_bf16 v[108:111], v[144:147], v[64:67], v[108:111]\n"
        "v_mul_f32 v102, v96, v102\n"
        "v_mul_f32 v103, v96, v103\n"
        "v_smfmac_f32_16x16x64_bf16 v[172:175], a[252:255], v[72:79], v237\n"
        "s_waitcnt vmcnt(6)\n"
        "s_barrier\n"
        "ds_read_b128 a[236:239], v240 offset:0\n"
        "v_mfma_f32_16x16x32_bf16 v[176:179], v[132:135], v[76:79], 0\n"
        "ds_read_b128 a[240:243], v241 offset:0\n"
        "ds_read_b128 a[244:247], v240 offset:128\n"
        "v_mfma_f32_16x16x32_bf16 v[176:179], v[128:131], v[72:75], v[176:179]\n"
        "ds_read_b128 a[248:251], v241 offset:128\n"
        "ds_read_b128 a[216:219], v240 offset:256\n"
        "v_mfma_f32_16x16x32_bf16 v[180:183], v[136:139], v[72:75], 0\n"
        "ds_read_b128 a[220:223], v241 offset:256\n"
        "v_cvt_pk_bf16_f32 v100, v100, v101\n"
        "v_mfma_f32_16x16x32_bf16 v[180:183], v[140:143], v[76:79], v[180:183]\n"
        "v_cvt_pk_bf16_f32 v101, v102, v103\n"
        "v_mul_f32 v104, v96, v104\n"
        "v_mfma_f32_16x16x32_bf16 v[184:187], v[148:151], v[76:79], 0\n"
        "v_mul_f32 v105, v96, v105\n"
        "v_mul_f32 v106, v96, v106\n"
        "v_mfma_f32_16x16x32_bf16 v[184:187], v[144:147], v[72:75], v[184:187]\n"
        "v_mul_f32 v107, v96, v107\n"
        "v_cvt_pk_bf16_f32 v102, v104, v105\n"
        "v_smfmac_f32_16x16x64_bf16 v[192:195], a[252:255], v[80:87], v237\n"
        "v_cvt_pk_bf16_f32 v103, v106, v107\n"
        "v_mfma_f32_16x16x32_bf16 v[196:199], v[132:135], v[84:87], 0\n"
        "ds_write_b128 v245, v[100:103] offset:0\n"
        "v_mfma_f32_16x16x32_bf16 v[196:199], v[128:131], v[80:83], v[196:199]\n"
        "buffer_load_dwordx4 v238, s[52:55], s63 offen lds\n"
        "v_mfma_f32_16x16x32_bf16 v[200:203], v[136:139], v[80:83], 0\n"
        "v_mov_b64 v[124:125], 0\n"
        "v_mul_f32 v108, v96, v108\n"
        "v_mfma_f32_16x16x32_bf16 v[200:203], v[140:143], v[84:87], v[200:203]\n"
        "v_mul_f32 v109, v96, v109\n"
        "v_mul_f32 v110, v96, v110\n"
        "v_mul_f32 v111, v96, v111\n"
        "v_mfma_f32_16x16x32_bf16 v[204:207], v[148:151], v[84:87], 0\n"
        "v_cvt_pk_bf16_f32 v108, v108, v109\n"
        "v_cvt_pk_bf16_f32 v109, v110, v111\n"
        "v_mfma_f32_16x16x32_bf16 v[204:207], v[144:147], v[80:83], v[204:207]\n"
        "s_waitcnt lgkmcnt(1)\n"
        "ds_write_b64 v246, v[108:109] offset:0\n"
        "v_mfma_f32_16x16x32_bf16 v[112:115], a[72:75], a[236:239], 0\n"
        "v_max_u32 v236, v96, v97\n"
        "v_add_f32 v173, v172, v173\n"
        "v_mfma_f32_16x16x32_bf16 v[112:115], a[76:79], a[240:243], v[112:115]\n"
        "v_rcp_f32 v172, v173\n"
        "v_mfma_f32_16x16x32_bf16 v[112:115], a[80:83], a[244:247], v[112:115]\n"
        "v_mul_f32 v176, v172, v176\n"
        "v_mul_f32 v177, v172, v177\n"
        "v_mfma_f32_16x16x32_bf16 v[112:115], a[84:87], a[248:251], v[112:115]\n"
        "buffer_load_dwordx4 v239, s[52:55], s63 offen offset:1024 lds\n"
        "v_mfma_f32_16x16x32_bf16 v[112:115], a[88:91], a[216:219], v[112:115]\n"
        "v_mul_f32 v178, v172, v178\n"
        "v_mul_f32 v179, v172, v179\n"
        "v_mfma_f32_16x16x32_bf16 v[112:115], a[92:95], a[220:223], v[112:115]\n"
        "v_cvt_pk_bf16_f32 v176, v176, v177\n"
        "v_cvt_pk_bf16_f32 v177, v178, v179\n"
        "v_smfmac_f32_16x16x64_bf16 v[124:127], a[252:255], v[88:95], v237\n"
        "v_mul_f32 v180, v172, v180\n"
        "v_mfma_f32_16x16x32_bf16 v[80:83], v[132:135], v[92:95], 0\n"
        "v_mul_f32 v181, v172, v181\n"
        "v_mul_f32 v182, v172, v182\n"
        "v_mfma_f32_16x16x32_bf16 v[80:83], v[128:131], v[88:91], v[80:83]\n"
        "v_cvt_pk_bf16_f32 v152, v112, v113\n"
        "v_cvt_pk_bf16_f32 v153, v114, v115\n"
        "v_mfma_f32_16x16x32_bf16 v[84:87], v[136:139], v[88:91], 0\n"
        "v_mul_f32 v183, v172, v183\n"
        "v_cvt_pk_bf16_f32 v178, v180, v181\n"
        "v_mfma_f32_16x16x32_bf16 v[84:87], v[140:143], v[92:95], v[84:87]\n"
        "v_cvt_pk_bf16_f32 v179, v182, v183\n"
        "v_mul_f32 v184, v172, v184\n"
        "v_mfma_f32_16x16x32_bf16 v[96:99], v[148:151], v[92:95], 0\n"
        "buffer_load_dwordx4 v247, s[52:55], s63 offen offset:2048 lds\n"
        "v_mfma_f32_16x16x32_bf16 v[96:99], v[144:147], v[88:91], v[96:99]\n"
        "ds_write_b128 v245, v[176:179] offset:256\n"
        "v_mfma_f32_16x16x32_bf16 v[116:119], a[116:119], a[220:223], 0\n"
        "v_mul_f32 v185, v172, v185\n"
        "v_mul_f32 v186, v172, v186\n"
        "v_mfma_f32_16x16x32_bf16 v[116:119], a[112:115], a[216:219], v[116:119]\n"
        "v_mul_f32 v187, v172, v187\n"
        "v_add_f32 v125, v124, v125\n"
        "v_mfma_f32_16x16x32_bf16 v[116:119], a[108:111], a[248:251], v[116:119]\n"
        "v_rcp_f32 v124, v125\n"
        "v_mfma_f32_16x16x32_bf16 v[116:119], a[104:107], a[244:247], v[116:119]\n"
        "s_add_u32 m0, s65, 15360\n"
        "v_cvt_pk_bf16_f32 v184, v184, v185\n"
        "v_cvt_pk_bf16_f32 v185, v186, v187\n"
        "v_mfma_f32_16x16x32_bf16 v[116:119], a[100:103], a[240:243], v[116:119]\n"
        "ds_write_b64 v246, v[184:185] offset:256\n"
        "v_mfma_f32_16x16x32_bf16 v[116:119], a[96:99], a[236:239], v[116:119]\n"
        "v_mul_f32 v80, v124, v80\n"
        "v_mul_f32 v81, v124, v81\n"
        "v_mfma_f32_16x16x32_bf16 v[120:123], a[120:123], a[236:239], 0\n"
        "buffer_load_dwordx4 v238, s[52:55], s34 offen lds\n"
        "v_mfma_f32_16x16x32_bf16 v[120:123], a[124:127], a[240:243], v[120:123]\n"
        "v_mul_f32 v82, v124, v82\n"
        "v_mul_f32 v83, v124, v83\n"
        "v_mfma_f32_16x16x32_bf16 v[120:123], a[128:131], a[244:247], v[120:123]\n"
        "v_max3_u32 v236, v236, v172, v173\n"
        "v_cvt_pk_bf16_f32 v80, v80, v81\n"
        "v_mfma_f32_16x16x32_bf16 v[120:123], a[132:135], a[248:251], v[120:123]\n"
        "v_cvt_pk_bf16_f32 v81, v82, v83\n"
        "v_mul_f32 v84, v124, v84\n"
        "v_mfma_f32_16x16x32_bf16 v[120:123], a[136:139], a[216:219], v[120:123]\n"
        "v_mul_f32 v85, v124, v85\n"
        "v_mul_f32 v86, v124, v86\n"
        "v_mfma_f32_16x16x32_bf16 v[120:123], a[140:143], a[220:223], v[120:123]\n"
        "v_mul_f32 v87, v124, v87\n"
        "v_cvt_pk_bf16_f32 v82, v84, v85\n"
        "v_mfma_f32_16x16x32_bf16 v[112:115], a[20:23], a[220:223], 0\n"
        "v_cvt_pk_bf16_f32 v83, v86, v87\n"
        "v_mul_f32 v96, v124, v96\n"
        "v_mfma_f32_16x16x32_bf16 v[112:115], a[16:19], a[216:219], v[112:115]\n"
        "ds_write_b128 v245, v[80:83] offset:768\n"
        "v_mfma_f32_16x16x32_bf16 v[112:115], a[12:15], a[248:251], v[112:115]\n"
        "buffer_load_dwordx4 v239, s[52:55], s34 offen offset:1024 lds\n"
        "v_mfma_f32_16x16x32_bf16 v[112:115], a[8:11], a[244:247], v[112:115]\n"
        "v_mul_f32 v97, v124, v97\n"
        "v_mul_f32 v98, v124, v98\n"
        "v_mfma_f32_16x16x32_bf16 v[112:115], a[4:7], a[240:243], v[112:115]\n"
        "v_mul_f32 v99, v124, v99\n"
        "v_cvt_pk_bf16_f32 v154, v116, v117\n"
        "v_mfma_f32_16x16x32_bf16 v[112:115], a[0:3], a[236:239], v[112:115]\n"
        "v_cvt_pk_bf16_f32 v155, v118, v119\n"
        "v_cvt_pk_bf16_f32 v96, v96, v97\n"
        "v_mfma_f32_16x16x32_bf16 v[116:119], a[24:27], a[236:239], 0\n"
        "v_cvt_pk_bf16_f32 v97, v98, v99\n"
        "v_cvt_pk_bf16_f32 v168, v120, v121\n"
        "v_mfma_f32_16x16x32_bf16 v[116:119], a[28:31], a[240:243], v[116:119]\n"
        "ds_write_b64 v246, v[96:97] offset:768\n"
        "v_mfma_f32_16x16x32_bf16 v[116:119], a[32:35], a[244:247], v[116:119]\n"
        "v_cvt_pk_bf16_f32 v169, v122, v123\n"
        "v_add_f32 v193, v192, v193\n"
        "v_mfma_f32_16x16x32_bf16 v[116:119], a[36:39], a[248:251], v[116:119]\n"
        "v_rcp_f32 v192, v193\n"
        "v_mfma_f32_16x16x32_bf16 v[116:119], a[40:43], a[216:219], v[116:119]\n"
        "buffer_load_dwordx4 v247, s[52:55], s34 offen offset:2048 lds\n"
        "v_mfma_f32_16x16x32_bf16 v[116:119], a[44:47], a[220:223], v[116:119]\n"
        "v_mul_f32 v196, v192, v196\n"
        "v_mul_f32 v197, v192, v197\n"
        "v_mfma_f32_16x16x32_bf16 v[120:123], a[68:71], a[220:223], 0\n"
        "v_mul_f32 v198, v192, v198\n"
        "v_mul_f32 v199, v192, v199\n"
        "v_mfma_f32_16x16x32_bf16 v[120:123], a[64:67], a[216:219], v[120:123]\n"
        "v_cvt_pk_bf16_f32 v156, v112, v113\n"
        "v_cvt_pk_bf16_f32 v157, v114, v115\n"
        "v_mfma_f32_16x16x32_bf16 v[120:123], a[60:63], a[248:251], v[120:123]\n"
        "ds_read_b128 a[224:227], v240 offset:1536\n"
        "v_cvt_pk_bf16_f32 v196, v196, v197\n"
        "v_mfma_f32_16x16x32_bf16 v[120:123], a[56:59], a[244:247], v[120:123]\n"
        "s_add_u32 m0, s65, 0\n"
        "ds_read_b128 a[228:231], v241 offset:1536\n"
        "v_cvt_pk_bf16_f32 v197, v198, v199\n"
        "v_mfma_f32_16x16x32_bf16 v[120:123], a[52:55], a[240:243], v[120:123]\n"
        "ds_read_b128 a[232:235], v240 offset:1664\n"
        "v_mul_f32 v200, v192, v200\n"
        "v_mfma_f32_16x16x32_bf16 v[120:123], a[48:51], a[236:239], v[120:123]\n"
        "v_mul_f32 v201, v192, v201\n"
        "v_mul_f32 v202, v192, v202\n"
        "v_mfma_f32_16x16x32_bf16 v[112:115], a[236:239], a[144:147], 0\n"
        "buffer_load_dwordx4 v232, s[52:55], 0 offen lds\n"
        "v_mfma_f32_16x16x32_bf16 v[112:115], a[240:243], a[148:151], v[112:115]\n"
        "v_mul_f32 v203, v192, v203\n"
        "v_cvt_pk_bf16_f32 v198, v200, v201\n"
        "v_mfma_f32_16x16x32_bf16 v[112:115], a[244:247], a[152:155], v[112:115]\n"
        "v_cvt_pk_bf16_f32 v199, v202, v203\n"
        "v_mul_f32 v204, v192, v204\n"
        "v_mfma_f32_16x16x32_bf16 v[112:115], a[248:251], a[156:159], v[112:115]\n"
        "ds_write_b128 v245, v[196:199] offset:512\n"
        "v_mfma_f32_16x16x32_bf16 v[112:115], a[216:219], a[160:163], v[112:115]\n"
        "v_mul_f32 v205, v192, v205\n"
        "v_mul_f32 v206, v192, v206\n"
        "v_mfma_f32_16x16x32_bf16 v[112:115], a[220:223], a[164:167], v[112:115]\n"
        "v_mul_f32 v207, v192, v207\n"
        "v_cvt_pk_bf16_f32 v158, v116, v117\n"
        "v_cvt_pk_bf16_f32 v159, v118, v119\n"
        "v_mfma_f32_16x16x32_bf16 v[116:119], a[220:223], a[188:191], 0\n"
        "v_cvt_pk_bf16_f32 v204, v204, v205\n"
        "v_cvt_pk_bf16_f32 v205, v206, v207\n"
        "v_mfma_f32_16x16x32_bf16 v[116:119], a[216:219], a[184:187], v[116:119]\n"
        "ds_write_b64 v246, v[204:205] offset:512\n"
        "v_mfma_f32_16x16x32_bf16 v[116:119], a[248:251], a[180:183], v[116:119]\n"
        "buffer_load_dwordx4 v233, s[52:55], 0 offen offset:1024 lds\n"
        "v_mfma_f32_16x16x32_bf16 v[116:119], a[244:247], a[176:179], v[116:119]\n"
        "s_waitcnt vmcnt(5)\n"
        "ds_read_b64_tr_b16 v[64:65], v255 offset:12288\n"
        "ds_read_b64_tr_b16 v[66:67], v255 offset:13056\n"
        "v_mfma_f32_16x16x32_bf16 v[116:119], a[240:243], a[172:175], v[116:119]\n"
        "v_max3_u32 v236, v236, v192, v193\n"
        "v_max3_u32 v236, v236, v124, v125\n"
        "v_mfma_f32_16x16x32_bf16 v[116:119], a[236:239], a[168:171], v[116:119]\n"
        "ds_read_b64_tr_b16 v[68:69], v255 offset:12320\n"
        "ds_read_b64_tr_b16 v[70:71], v255 offset:13088\n"
        "v_cvt_pk_bf16_f32 v160, v120, v121\n"
        "v_cvt_pk_bf16_f32 v161, v122, v123\n"
        "v_cvt_pk_bf16_f32 v166, v120, v121\n"
        "v_cvt_pk_bf16_f32 v167, v122, v123\n"
        "v_mfma_f32_16x16x32_bf16 v[120:123], a[236:239], a[192:195], 0\n"
        "ds_read_b128 a[236:239], v241 offset:1664\n"
        "v_cmp_lt_u32 vcc, s24, v236\n"
        "v_mfma_f32_16x16x32_bf16 v[120:123], a[240:243], a[196:199], v[120:123]\n"
        "ds_read_b128 a[240:243], v240 offset:1792\n"
        "s_cmp_lg_u64 vcc, 0\n"
        "s_addc_u32 s25, s25, s25\n"
        "v_mfma_f32_16x16x32_bf16 v[120:123], a[244:247], a[200:203], v[120:123]\n"
        "ds_read_b64_tr_b16 v[72:73], v255 offset:12352\n"
        "ds_read_b64_tr_b16 v[74:75], v255 offset:13120\n"
        "v_mfma_f32_16x16x32_bf16 v[120:123], a[248:251], a[204:207], v[120:123]\n"
        "ds_read_b128 a[244:247], v241 offset:1792\n"
        "v_cvt_pk_bf16_f32 v128, v112, v113\n"
        "v_mfma_f32_16x16x32_bf16 v[120:123], a[216:219], a[208:211], v[120:123]\n"
        "s_waitcnt lgkmcnt(7)\n"
        "ds_write_b128 v235, v[64:67] offset:12288\n"
        "v_mfma_f32_16x16x32_bf16 v[120:123], a[220:223], a[212:215], v[120:123]\n"
        "v_cvt_pk_bf16_f32 v129, v114, v115\n"
        "v_and_b32 v239, 32, v245\n"
        "v_mfma_f32_16x16x32_bf16 v[112:115], a[72:75], a[224:227], 0\n"
        "buffer_load_dwordx4 v234, s[52:55], 0 offen offset:2048 lds\n"
        "v_mfma_f32_16x16x32_bf16 v[112:115], a[76:79], a[228:231], v[112:115]\n"
        "s_waitcnt vmcnt(3)\n"
        "ds_read_b64_tr_b16 v[76:77], v255 offset:15360\n"
        "ds_read_b64_tr_b16 v[78:79], v255 offset:16128\n"
        "v_mfma_f32_16x16x32_bf16 v[112:115], a[80:83], a[232:235], v[112:115]\n"
        "s_waitcnt lgkmcnt(6)\n"
        "v_cvt_pk_bf16_f32 v136, v116, v117\n"
        "v_cvt_pk_bf16_f32 v137, v118, v119\n"
        "v_mfma_f32_16x16x32_bf16 v[112:115], a[84:87], a[236:239], v[112:115]\n"
        "s_add_u32 m0, s65, 3072\n"
        "v_xor_b32 v251, 0x2000, v251\n"
        "v_xor_b32 v252, 0x2000, v252\n"
        "v_mfma_f32_16x16x32_bf16 v[112:115], a[88:91], a[240:243], v[112:115]\n"
        "s_waitcnt lgkmcnt(3)\n"
        "v_mad_i32_i24 v239, v239, -12, v235\n"
        "v_cvt_pk_bf16_f32 v144, v120, v121\n"
        "v_mfma_f32_16x16x32_bf16 v[112:115], a[92:95], a[244:247], v[112:115]\n"
        "ds_write_b128 v239, v[68:71] offset:12480\n"
        "s_barrier\n"
        "v_mfma_f32_16x16x32_bf16 v[116:119], a[116:119], a[244:247], 0\n"
        "buffer_load_dwordx4 v232, s[52:55], s61 offen lds\n"
        "v_mfma_f32_16x16x32_bf16 v[116:119], a[112:115], a[240:243], v[116:119]\n"
        "ds_read_b64_tr_b16 v[88:89], v255 offset:15392\n"
        "ds_read_b64_tr_b16 v[90:91], v255 offset:16160\n"
        "v_mfma_f32_16x16x32_bf16 v[116:119], a[108:111], a[236:239], v[116:119]\n"
        "v_cvt_pk_bf16_f32 v145, v122, v123\n"
        "v_cvt_pk_bf16_f32 v172, v112, v113\n"
        "v_mfma_f32_16x16x32_bf16 v[116:119], a[104:107], a[232:235], v[116:119]\n"
        "v_cvt_pk_bf16_f32 v173, v114, v115\n"
        "v_add_u32 v247, s45, v245\n"
        "v_mfma_f32_16x16x32_bf16 v[116:119], a[100:103], a[228:231], v[116:119]\n"
        "v_mfma_f32_16x16x32_bf16 v[116:119], a[96:99], a[224:227], v[116:119]\n"
        "ds_write_b128 v254, v[72:75] offset:12288\n"
        "v_mfma_f32_16x16x32_bf16 v[120:123], a[120:123], a[224:227], 0\n"
        "s_nop 2\n"
        "v_mfma_f32_16x16x32_bf16 v[120:123], a[124:127], a[228:231], v[120:123]\n"
        "ds_read_b128 v[162:165], v251 offset:0\n"
        "s_nop 0\n"
        "v_mfma_f32_16x16x32_bf16 v[120:123], a[128:131], a[232:235], v[120:123]\n"
        "buffer_load_dwordx4 v233, s[52:55], s61 offen offset:1024 lds\n"
        "v_mfma_f32_16x16x32_bf16 v[120:123], a[132:135], a[236:239], v[120:123]\n"
        "ds_read_b64_tr_b16 v[92:93], v255 offset:15424\n"
        "ds_read_b64_tr_b16 v[94:95], v255 offset:16192\n"
        "v_mfma_f32_16x16x32_bf16 v[120:123], a[136:139], a[240:243], v[120:123]\n"
        "v_cvt_pk_bf16_f32 v174, v116, v117\n"
        "v_cvt_pk_bf16_f32 v175, v118, v119\n"
        "v_mfma_f32_16x16x32_bf16 v[120:123], a[140:143], a[244:247], v[120:123]\n"
        "s_nop 2\n"
        "v_mfma_f32_16x16x32_bf16 v[112:115], a[20:23], a[244:247], 0\n"
        "s_nop 2\n"
        "v_mfma_f32_16x16x32_bf16 v[112:115], a[16:19], a[240:243], v[112:115]\n"
        "v_cvt_pk_bf16_f32 v190, v120, v121\n"
        "v_cvt_pk_bf16_f32 v191, v122, v123\n"
        "v_mfma_f32_16x16x32_bf16 v[112:115], a[12:15], a[236:239], v[112:115]\n"
        "v_mfma_f32_16x16x32_bf16 v[112:115], a[8:11], a[232:235], v[112:115]\n"
        "s_waitcnt vmcnt(2)\n"
        "ds_read_b64_tr_b16 v[212:213], v255 offset:0\n"
        "ds_read_b64_tr_b16 v[214:215], v255 offset:768\n"
        "v_mfma_f32_16x16x32_bf16 v[112:115], a[4:7], a[228:231], v[112:115]\n"
        "buffer_load_dwordx4 v234, s[52:55], s61 offen offset:2048 lds\n"
        "v_mfma_f32_16x16x32_bf16 v[112:115], a[0:3], a[224:227], v[112:115]\n"
        "ds_read_b128 v[182:185], v251 offset:512\n"
        "s_nop 0\n"
        "v_mfma_f32_16x16x32_bf16 v[116:119], a[24:27], a[224:227], 0\n"
        "s_nop 2\n"
        "v_mfma_f32_16x16x32_bf16 v[116:119], a[28:31], a[228:231], v[116:119]\n"
        "s_waitcnt lgkmcnt(3)\n"
        "ds_write_b128 v235, v[76:79] offset:15360\n"
        "v_mfma_f32_16x16x32_bf16 v[116:119], a[32:35], a[232:235], v[116:119]\n"
        "v_cvt_pk_bf16_f32 v176, v112, v113\n"
        "v_cvt_pk_bf16_f32 v177, v114, v115\n"
        "v_mfma_f32_16x16x32_bf16 v[116:119], a[36:39], a[236:239], v[116:119]\n"
        "s_add_u32 m0, s65, 6144\n"
        "v_mfma_f32_16x16x32_bf16 v[116:119], a[40:43], a[240:243], v[116:119]\n"
        "ds_read_b64_tr_b16 v[216:217], v255 offset:32\n"
        "ds_read_b64_tr_b16 v[218:219], v255 offset:800\n"
        "v_mfma_f32_16x16x32_bf16 v[116:119], a[44:47], a[244:247], v[116:119]\n"
        "s_nop 2\n"
        "v_mfma_f32_16x16x32_bf16 v[120:123], a[68:71], a[244:247], 0\n"
        ".long 0xbec200ff\n"
        "43:\n"
        "s_branch 60b\n"
        "buffer_load_dwordx4 v232, s[52:55], s56 offen lds\n"
        "v_mfma_f32_16x16x32_bf16 v[120:123], a[64:67], a[240:243], v[120:123]\n"
        "s_nop 1\n"
        "v_cvt_pk_bf16_f32 v178, v116, v117\n"
        "v_mfma_f32_16x16x32_bf16 v[120:123], a[60:63], a[236:239], v[120:123]\n"
        "ds_write_b128 v239, v[88:91] offset:15552\n"
        "v_mfma_f32_16x16x32_bf16 v[120:123], a[56:59], a[232:235], v[120:123]\n"
        "v_cvt_pk_bf16_f32 v179, v118, v119\n"
        "v_mfma_f32_16x16x32_bf16 v[120:123], a[52:55], a[228:231], v[120:123]\n"
        "v_mfma_f32_16x16x32_bf16 v[120:123], a[48:51], a[224:227], v[120:123]\n"
        "ds_read_b64_tr_b16 v[220:221], v255 offset:64\n"
        "ds_read_b64_tr_b16 v[222:223], v255 offset:832\n"
        "v_mfma_f32_16x16x32_bf16 v[112:115], a[224:227], a[144:147], 0\n"
        "s_nop 2\n"
        "v_mfma_f32_16x16x32_bf16 v[112:115], a[228:231], a[148:151], v[112:115]\n"
        "s_nop 0\n"
        "v_cvt_pk_bf16_f32 v180, v120, v121\n"
        "v_cvt_pk_bf16_f32 v181, v122, v123\n"
        "v_mfma_f32_16x16x32_bf16 v[112:115], a[232:235], a[152:155], v[112:115]\n"
        "buffer_load_dwordx4 v233, s[52:55], s56 offen offset:1024 lds\n"
        "v_mfma_f32_16x16x32_bf16 v[112:115], a[236:239], a[156:159], v[112:115]\n"
        "ds_write_b128 v254, v[92:95] offset:15360\n"
        "v_mfma_f32_16x16x32_bf16 v[112:115], a[240:243], a[160:163], v[112:115]\n"
        "v_cvt_pk_bf16_f32 v186, v120, v121\n"
        "v_cvt_pk_bf16_f32 v187, v122, v123\n"
        "v_mfma_f32_16x16x32_bf16 v[112:115], a[244:247], a[164:167], v[112:115]\n"
        "ds_read_b128 a[248:251], v240 offset:12288\n"
        "s_nop 0\n"
        "v_mfma_f32_16x16x32_bf16 v[96:99], v[152:155], v[156:159], v[0:3]\n"
        "s_waitcnt vmcnt(2)\n"
        "ds_read_b64_tr_b16 v[224:225], v255 offset:3072\n"
        "ds_read_b64_tr_b16 v[226:227], v255 offset:3840\n"
        "v_mfma_f32_16x16x32_bf16 v[96:99], v[168:171], v[160:163], v[96:99]\n"
        "ds_read_b128 a[216:219], v241 offset:12288\n"
        "s_sub_u32 s31, s30, s28\n"
        "v_cvt_pk_bf16_f32 v130, v112, v113\n"
        "v_mfma_f32_16x16x32_bf16 v[100:103], v[172:175], v[156:159], v[4:7]\n"
        "ds_read_b128 a[220:223], v240 offset:12416\n"
        "s_sub_u32 s45, s28, s29\n"
        "v_cvt_pk_bf16_f32 v131, v114, v115\n"
        "v_mfma_f32_16x16x32_bf16 v[100:103], v[188:191], v[164:167], v[100:103]\n"
        "s_add_u32 s28, s28, s31\n"
        "ds_read_b128 v[162:165], v252 offset:0\n"
        "v_mfma_f32_16x16x32_bf16 v[116:119], a[244:247], a[188:191], 0\n"
        "buffer_load_dwordx4 v234, s[52:55], s56 offen offset:2048 lds\n"
        "v_mfma_f32_16x16x32_bf16 v[116:119], a[240:243], a[184:187], v[116:119]\n"
        "s_waitcnt lgkmcnt(7)\n"
        "ds_write_b128 v235, v[212:215] offset:0\n"
        "v_mfma_f32_16x16x32_bf16 v[116:119], a[236:239], a[180:183], v[116:119]\n"
        "v_exp_f32 v96, v96\n"
        "v_mfma_f32_16x16x32_bf16 v[116:119], a[232:235], a[176:179], v[116:119]\n"
        "ds_read_b64_tr_b16 v[88:89], v255 offset:3104\n"
        "ds_read_b64_tr_b16 v[90:91], v255 offset:3872\n"
        "v_mfma_f32_16x16x32_bf16 v[116:119], a[228:231], a[172:175], v[116:119]\n"
        "v_exp_f32 v97, v97\n"
        "v_mfma_f32_16x16x32_bf16 v[116:119], a[224:227], a[168:171], v[116:119]\n"
        "s_add_u32 m0, s65, 9216\n"
        "v_exp_f32 v98, v98\n"
        "v_mfma_f32_16x16x32_bf16 v[104:107], v[152:155], v[176:179], v[16:19]\n"
        "v_exp_f32 v100, v100\n"
        "v_mfma_f32_16x16x32_bf16 v[104:107], v[168:171], v[180:183], v[104:107]\n"
        "ds_write_b128 v239, v[216:219] offset:192\n"
        "v_mfma_f32_16x16x32_bf16 v[108:111], v[172:175], v[176:179], v[20:23]\n"
        "buffer_load_dwordx4 v232, s[52:55], s62 offen lds\n"
        "v_mfma_f32_16x16x32_bf16 v[108:111], v[188:191], v[184:187], v[108:111]\n"
        "ds_read_b128 v[182:185], v252 offset:512\n"
        "v_cvt_pk_bf16_f32 v64, v96, v97\n"
        "v_mfma_f32_16x16x32_bf16 v[120:123], a[224:227], a[192:195], 0\n"
        "ds_read_b128 a[224:227], v241 offset:12416\n"
        "ds_read_b64_tr_b16 v[92:93], v255 offset:3136\n"
        "v_mfma_f32_16x16x32_bf16 v[120:123], a[228:231], a[196:199], v[120:123]\n"
        "ds_read_b64_tr_b16 v[94:95], v255 offset:3904\n"
        "ds_read_b128 a[228:231], v240 offset:12544\n"
        "v_mfma_f32_16x16x32_bf16 v[120:123], a[232:235], a[200:203], v[120:123]\n"
        "ds_read_b128 a[232:235], v241 offset:12544\n"
        "v_cvt_pk_bf16_f32 v138, v116, v117\n"
        "v_mfma_f32_16x16x32_bf16 v[120:123], a[236:239], a[204:207], v[120:123]\n"
        "ds_read_b128 v[202:205], v251 offset:4096\n"
        "v_cvt_pk_bf16_f32 v139, v118, v119\n"
        "v_mfma_f32_16x16x32_bf16 v[120:123], a[240:243], a[208:211], v[120:123]\n"
        "ds_write_b128 v254, v[220:223] offset:0\n"
        "v_mfma_f32_16x16x32_bf16 v[120:123], a[244:247], a[212:215], v[120:123]\n"
        "s_waitcnt lgkmcnt(9)\n"
        "v_exp_f32 v99, v99\n"
        "v_mfma_f32_16x16x32_bf16 v[112:115], a[0:3], a[248:251], 0\n"
        "buffer_load_dwordx4 v233, s[52:55], s62 offen offset:1024 lds\n"
        "v_mfma_f32_16x16x32_bf16 v[112:115], a[4:7], a[216:219], v[112:115]\n"
        "s_waitcnt vmcnt(2)\n"
        "ds_read_b64_tr_b16 v[212:213], v255 offset:6144\n"
        "ds_read_b64_tr_b16 v[214:215], v255 offset:6912\n"
        "v_mfma_f32_16x16x32_bf16 v[112:115], a[8:11], a[220:223], v[112:115]\n"
        "s_waitcnt lgkmcnt(5)\n"
        "v_exp_f32 v101, v101\n"
        "v_mfma_f32_16x16x32_bf16 v[112:115], a[12:15], a[224:227], v[112:115]\n"
        "v_cvt_pk_bf16_f32 v65, v98, v99\n"
        "v_cvt_pk_bf16_f32 v66, v100, v101\n"
        "v_mfma_f32_16x16x32_bf16 v[112:115], a[16:19], a[228:231], v[112:115]\n"
        "s_waitcnt lgkmcnt(3)\n"
        "v_exp_f32 v102, v102\n"
        "v_mfma_f32_16x16x32_bf16 v[112:115], a[20:23], a[232:235], v[112:115]\n"
        "ds_write_b128 v235, v[224:227] offset:3072\n"
        "v_mfma_f32_16x16x32_bf16 v[116:119], a[44:47], a[232:235], 0\n"
        "v_exp_f32 v103, v103\n"
        "v_mfma_f32_16x16x32_bf16 v[116:119], a[40:43], a[228:231], v[116:119]\n"
        "v_cvt_pk_bf16_f32 v67, v102, v103\n"
        "v_cvt_pk_bf16_f32 v146, v120, v121\n"
        "v_mfma_f32_16x16x32_bf16 v[116:119], a[36:39], a[224:227], v[116:119]\n"
        "buffer_load_dwordx4 v234, s[52:55], s62 offen offset:2048 lds\n"
        "s_add_u32 s41, s30, s40\n"
        "s_add_u32 s35, s47, 786432\n"
        "s_sub_u32 s46, s35, 100663296\n"
        "s_cmp_lt_u32 s46, 786432\n"
        "s_mov_b32 s30, s29\n"
        "v_mfma_f32_16x16x32_bf16 v[116:119], a[32:35], a[220:223], v[116:119]\n"
        "s_cselect_b32 s35, s46, s35\n"
        "s_add_u32 s29, s29, s45\n"
        "ds_read_b64_tr_b16 v[216:217], v255 offset:6176\n"
        "v_mfma_f32_16x16x32_bf16 v[116:119], a[28:31], a[216:219], v[116:119]\n"
        "ds_read_b64_tr_b16 v[218:219], v255 offset:6944\n"
        "v_cvt_pk_bf16_f32 v147, v122, v123\n"
        "v_mfma_f32_16x16x32_bf16 v[116:119], a[24:27], a[248:251], v[116:119]\n"
        "v_exp_f32 v104, v104\n"
        "v_mfma_f32_16x16x32_bf16 v[120:123], a[48:51], a[248:251], 0\n"
        "ds_write_b128 v239, v[88:91] offset:3264\n"
        "v_mfma_f32_16x16x32_bf16 v[120:123], a[52:55], a[216:219], v[120:123]\n"
        "v_exp_f32 v105, v105\n"
        "v_mfma_f32_16x16x32_bf16 v[120:123], a[56:59], a[220:223], v[120:123]\n"
        "s_mov_b32 m0, s48\n"
        "v_exp_f32 v106, v106\n"
        "v_mfma_f32_16x16x32_bf16 v[120:123], a[60:63], a[224:227], v[120:123]\n"
        "ds_read_b64_tr_b16 v[220:221], v255 offset:6208\n"
        "ds_read_b64_tr_b16 v[222:223], v255 offset:6976\n"
        "v_mfma_f32_16x16x32_bf16 v[120:123], a[64:67], a[228:231], v[120:123]\n"
        "buffer_load_dwordx4 v249, s[12:15], s36 offen offset:0 lds\n"
        "v_mfma_f32_16x16x32_bf16 v[120:123], a[68:71], a[232:235], v[120:123]\n"
        "v_cvt_pk_bf16_f32 v72, v104, v105\n"
        "v_cvt_pk_bf16_f32 v196, v112, v113\n"
        "v_cvt_pk_bf16_f32 v197, v114, v115\n"
        "v_mfma_f32_16x16x32_bf16 v[112:115], a[92:95], a[232:235], 0\n"
        "v_exp_f32 v108, v108\n"
        "v_mfma_f32_16x16x32_bf16 v[112:115], a[88:91], a[228:231], v[112:115]\n"
        "ds_write_b128 v254, v[92:95] offset:3072\n"
        "v_mfma_f32_16x16x32_bf16 v[112:115], a[84:87], a[224:227], v[112:115]\n"
        "v_exp_f32 v107, v107\n"
        "v_mfma_f32_16x16x32_bf16 v[112:115], a[80:83], a[220:223], v[112:115]\n"
        "v_exp_f32 v109, v109\n"
        "v_mfma_f32_16x16x32_bf16 v[112:115], a[76:79], a[216:219], v[112:115]\n"
        "s_waitcnt vmcnt(1)\n"
        "ds_read_b64_tr_b16 v[224:225], v255 offset:9216\n"
        "ds_read_b64_tr_b16 v[226:227], v255 offset:9984\n"
        "v_mfma_f32_16x16x32_bf16 v[112:115], a[72:75], a[248:251], v[112:115]\n"
        "buffer_load_dwordx4 v253, s[12:15], s36 offen offset:1024 lds\n"
        "v_cvt_pk_bf16_f32 v198, v116, v117\n"
        "v_cvt_pk_bf16_f32 v199, v118, v119\n"
        "s_nop 1\n"
        "v_mfma_f32_16x16x32_bf16 v[96:99], v[152:155], v[196:199], v[32:35]\n"
        "v_cvt_pk_bf16_f32 v73, v106, v107\n"
        "v_cvt_pk_bf16_f32 v74, v108, v109\n"
        "v_cvt_pk_bf16_f32 v200, v120, v121\n"
        "v_cvt_pk_bf16_f32 v201, v122, v123\n"
        "s_nop 1\n"
        "v_mfma_f32_16x16x32_bf16 v[96:99], v[168:171], v[200:203], v[96:99]\n"
        "v_exp_f32 v110, v110\n"
        "v_mfma_f32_16x16x32_bf16 v[100:103], v[172:175], v[196:199], v[36:39]\n"
        "s_waitcnt lgkmcnt(3)\n"
        "ds_write_b128 v235, v[212:215] offset:6144\n"
        "v_cvt_pk_bf16_f32 v206, v120, v121\n"
        "v_cvt_pk_bf16_f32 v207, v122, v123\n"
        "s_nop 1\n"
        "v_mfma_f32_16x16x32_bf16 v[100:103], v[188:191], v[204:207], v[100:103]\n"
        "s_cmp_eq_u32 s37, 100663296\n"
        "v_exp_f32 v111, v111\n"
        "v_mfma_f32_16x16x32_bf16 v[116:119], a[96:99], a[248:251], 0\n"
        "s_cselect_b32 s46, 0, s37\n"
        "ds_read_b128 v[202:205], v252 offset:4096\n"
        "v_cvt_pk_bf16_f32 v75, v110, v111\n"
        "v_mfma_f32_16x16x32_bf16 v[116:119], a[100:103], a[216:219], v[116:119]\n"
        "ds_read_b64_tr_b16 v[88:89], v255 offset:9248\n"
        "ds_read_b64_tr_b16 v[90:91], v255 offset:10016\n"
        "s_add_u32 m0, s41, 0\n"
        "v_mfma_f32_16x16x32_bf16 v[116:119], a[104:107], a[220:223], v[116:119]\n"
        "buffer_load_dwordx4 v242, s[8:11], s47 offen lds\n"
        "v_mfma_f32_16x16x32_bf16 v[116:119], a[108:111], a[224:227], v[116:119]\n"
        "s_add_u32 s38, s46, s43\n"
        "v_cvt_pk_bf16_f32 v192, v112, v113\n"
        "v_cvt_pk_bf16_f32 v193, v114, v115\n"
        "v_mfma_f32_16x16x32_bf16 v[116:119], a[112:115], a[228:231], v[116:119]\n"
        "s_add_u32 s39, s46, s44\n"
        "v_exp_f32 v96, v96\n"
        "v_mfma_f32_16x16x32_bf16 v[116:119], a[116:119], a[232:235], v[116:119]\n"
        "ds_write_b128 v239, v[216:219] offset:6336\n"
        "s_add_u32 s37, s37, 6291456\n"
        "v_mfma_f32_16x16x32_bf16 v[120:123], a[140:143], a[232:235], 0\n"
        "v_exp_f32 v97, v97\n"
        "v_mfma_f32_16x16x32_bf16 v[120:123], a[136:139], a[228:231], v[120:123]\n"
        "s_add_u32 m0, s41, 1024\n"
        "v_exp_f32 v98, v98\n"
        "v_mfma_f32_16x16x32_bf16 v[120:123], a[132:135], a[224:227], v[120:123]\n"
        "ds_read_b64_tr_b16 v[92:93], v255 offset:9280\n"
        "ds_read_b64_tr_b16 v[94:95], v255 offset:10048\n"
        "v_mfma_f32_16x16x32_bf16 v[120:123], a[128:131], a[220:223], v[120:123]\n"
        "buffer_load_dwordx4 v243, s[8:11], s47 offen lds\n"
        "v_mfma_f32_16x16x32_bf16 v[120:123], a[124:127], a[216:219], v[120:123]\n"
        "v_cvt_pk_bf16_f32 v80, v96, v97\n"
        "v_cvt_pk_bf16_f32 v194, v116, v117\n"
        "v_mfma_f32_16x16x32_bf16 v[120:123], a[120:123], a[248:251], v[120:123]\n"
        "v_exp_f32 v100, v100\n"
        "v_mfma_f32_16x16x32_bf16 v[112:115], a[248:251], a[144:147], 0\n"
        "ds_write_b128 v254, v[220:223] offset:6144\n"
        "v_mfma_f32_16x16x32_bf16 v[112:115], a[216:219], a[148:151], v[112:115]\n"
        "v_cvt_pk_bf16_f32 v195, v118, v119\n"
        "v_add_u32 v245, s45, v245\n"
        "v_mfma_f32_16x16x32_bf16 v[112:115], a[220:223], a[152:155], v[112:115]\n"
        "s_add_u32 m0, s41, 2048\n"
        "ds_read_b128 a[236:239], v240 offset:13824\n"
        "v_cvt_pk_bf16_f32 v208, v120, v121\n"
        "v_mfma_f32_16x16x32_bf16 v[112:115], a[224:227], a[156:159], v[112:115]\n"
        "ds_read_b128 a[240:243], v241 offset:13824\n"
        "v_cvt_pk_bf16_f32 v209, v122, v123\n"
        "v_mfma_f32_16x16x32_bf16 v[112:115], a[228:231], a[160:163], v[112:115]\n"
        "buffer_load_dwordx4 v244, s[8:11], s47 offen lds\n"
        "v_mfma_f32_16x16x32_bf16 v[112:115], a[232:235], a[164:167], v[112:115]\n"
        "ds_read_b128 a[244:247], v240 offset:13952\n"
        "v_add_u32 v246, s45, v246\n"
        "v_mfma_f32_16x16x32_bf16 v[104:107], v[192:195], v[156:159], v[8:11]\n"
        "v_exp_f32 v99, v99\n"
        "v_mfma_f32_16x16x32_bf16 v[104:107], v[208:211], v[160:163], v[104:107]\n"
        "s_waitcnt lgkmcnt(4)\n"
        "ds_write_b128 v235, v[224:227] offset:9216\n"
        "v_mfma_f32_16x16x32_bf16 v[116:119], a[232:235], a[188:191], 0\n"
        "v_exp_f32 v101, v101\n"
        "v_mfma_f32_16x16x32_bf16 v[116:119], a[228:231], a[184:187], v[116:119]\n"
        "s_add_u32 m0, s41, 12288\n"
        "v_cvt_pk_bf16_f32 v81, v98, v99\n"
        "v_cvt_pk_bf16_f32 v82, v100, v101\n"
        "v_mfma_f32_16x16x32_bf16 v[116:119], a[224:227], a[180:183], v[116:119]\n"
        "v_exp_f32 v102, v102\n"
        "v_mfma_f32_16x16x32_bf16 v[116:119], a[220:223], a[176:179], v[116:119]\n"
        "buffer_load_dwordx4 v242, s[8:11], s35 offen lds\n"
        "v_mfma_f32_16x16x32_bf16 v[116:119], a[216:219], a[172:175], v[116:119]\n"
        "v_exp_f32 v103, v103\n"
        "v_mfma_f32_16x16x32_bf16 v[116:119], a[248:251], a[168:171], v[116:119]\n"
        "v_cvt_pk_bf16_f32 v83, v102, v103\n"
        "v_cvt_pk_bf16_f32 v132, v112, v113\n"
        "v_mfma_f32_16x16x32_bf16 v[108:111], v[192:195], v[176:179], v[24:27]\n"
        "ds_write_b128 v239, v[88:91] offset:9408\n"
        "v_mfma_f32_16x16x32_bf16 v[108:111], v[208:211], v[180:183], v[108:111]\n"
        "v_cvt_pk_bf16_f32 v133, v114, v115\n"
        "s_nop 0\n"
        "v_mfma_f32_16x16x32_bf16 v[120:123], a[248:251], a[192:195], 0\n"
        "ds_read_b128 a[248:251], v241 offset:13952\n"
        "s_add_u32 m0, s41, 13312\n"
        "v_cvt_pk_bf16_f32 v140, v116, v117\n"
        "v_mfma_f32_16x16x32_bf16 v[120:123], a[216:219], a[196:199], v[120:123]\n"
        "ds_read_b128 a[216:219], v240 offset:14080\n"
        "v_cvt_pk_bf16_f32 v141, v118, v119\n"
        "v_mfma_f32_16x16x32_bf16 v[120:123], a[220:223], a[200:203], v[120:123]\n"
        "buffer_load_dwordx4 v243, s[8:11], s35 offen lds\n"
        "v_mfma_f32_16x16x32_bf16 v[120:123], a[224:227], a[204:207], v[120:123]\n"
        "ds_read_b128 a[220:223], v241 offset:14080\n"
        "v_mfma_f32_16x16x32_bf16 v[120:123], a[228:231], a[208:211], v[120:123]\n"
        "v_exp_f32 v104, v104\n"
        "v_mfma_f32_16x16x32_bf16 v[120:123], a[232:235], a[212:215], v[120:123]\n"
        "ds_write_b128 v254, v[92:95] offset:9216\n"
        "v_mfma_f32_16x16x32_bf16 v[96:99], v[192:195], v[196:199], v[40:43]\n"
        "v_exp_f32 v105, v105\n"
        "v_mfma_f32_16x16x32_bf16 v[96:99], v[208:211], v[200:203], v[96:99]\n"
        "s_waitcnt lgkmcnt(5)\n"
        "s_add_u32 m0, s41, 14336\n"
        "v_exp_f32 v106, v106\n"
        "v_mfma_f32_16x16x32_bf16 v[112:115], a[72:75], a[236:239], 0\n"
        "v_cvt_pk_bf16_f32 v68, v104, v105\n"
        "v_cvt_pk_bf16_f32 v148, v120, v121\n"
        "v_mfma_f32_16x16x32_bf16 v[112:115], a[76:79], a[240:243], v[112:115]\n"
        "buffer_load_dwordx4 v244, s[8:11], s35 offen lds\n"
        "v_mfma_f32_16x16x32_bf16 v[112:115], a[80:83], a[244:247], v[112:115]\n"
        "s_waitcnt lgkmcnt(1)\n"
        "v_exp_f32 v107, v107\n"
        "v_mfma_f32_16x16x32_bf16 v[112:115], a[84:87], a[248:251], v[112:115]\n"
        "v_cvt_pk_bf16_f32 v69, v106, v107\n"
        "v_cvt_pk_bf16_f32 v149, v122, v123\n"
        "v_mfma_f32_16x16x32_bf16 v[112:115], a[88:91], a[216:219], v[112:115]\n"
        "v_exp_f32 v108, v108\n"
        "v_mfma_f32_16x16x32_bf16 v[112:115], a[92:95], a[220:223], v[112:115]\n"
        "v_exp_f32 v109, v109\n"
        "v_mfma_f32_16x16x32_bf16 v[116:119], a[116:119], a[220:223], 0\n"
        "v_exp_f32 v110, v110\n"
        "v_mfma_f32_16x16x32_bf16 v[116:119], a[112:115], a[216:219], v[116:119]\n"
        "v_cvt_pk_bf16_f32 v76, v108, v109\n"
        "v_add_u32 v240, s31, v240\n"
        "v_mfma_f32_16x16x32_bf16 v[116:119], a[108:111], a[248:251], v[116:119]\n"
        "v_exp_f32 v111, v111\n"
        "v_mfma_f32_16x16x32_bf16 v[116:119], a[104:107], a[244:247], v[116:119]\n"
        "v_cvt_pk_bf16_f32 v212, v112, v113\n"
        "v_cvt_pk_bf16_f32 v213, v114, v115\n"
        "v_mfma_f32_16x16x32_bf16 v[116:119], a[100:103], a[240:243], v[116:119]\n"
        "v_cvt_pk_bf16_f32 v77, v110, v111\n"
        "v_add_u32 v241, s31, v241\n"
        "v_mfma_f32_16x16x32_bf16 v[116:119], a[96:99], a[236:239], v[116:119]\n"
        "v_exp_f32 v96, v96\n"
        "v_mfma_f32_16x16x32_bf16 v[120:123], a[120:123], a[236:239], 0\n"
        "v_exp_f32 v97, v97\n"
        "v_mfma_f32_16x16x32_bf16 v[120:123], a[124:127], a[240:243], v[120:123]\n"
        "s_add_u32 s47, s47, 6291456\n"
        "v_exp_f32 v98, v98\n"
        "v_mfma_f32_16x16x32_bf16 v[120:123], a[128:131], a[244:247], v[120:123]\n"
        "s_add_u32 s36, s36, 2097152\n"
        "v_cvt_pk_bf16_f32 v214, v116, v117\n"
        "v_cvt_pk_bf16_f32 v215, v118, v119\n"
        "v_mfma_f32_16x16x32_bf16 v[120:123], a[132:135], a[248:251], v[120:123]\n"
        "s_xor_b32 s48, s48, 8192\n"
        "v_cvt_pk_bf16_f32 v84, v96, v97\n"
        "v_and_b32 v254, 32, v250\n"
        "v_mfma_f32_16x16x32_bf16 v[120:123], a[136:139], a[216:219], v[120:123]\n"
        "v_exp_f32 v99, v99\n"
        "v_mfma_f32_16x16x32_bf16 v[120:123], a[140:143], a[220:223], v[120:123]\n"
        "v_cvt_pk_bf16_f32 v85, v98, v99\n"
        "v_mad_i32_i24 v254, v254, -12, v248\n"
        "v_mfma_f32_16x16x32_bf16 v[112:115], a[220:223], a[164:167], 0\n"
        "v_add_u32 v254, 1728, v254\n"
        "s_nop 0\n"
        "v_mfma_f32_16x16x32_bf16 v[112:115], a[216:219], a[160:163], v[112:115]\n"
        "s_nop 1\n"
        "v_cvt_pk_bf16_f32 v230, v120, v121\n"
        "v_mfma_f32_16x16x32_bf16 v[112:115], a[248:251], a[156:159], v[112:115]\n"
        "s_waitcnt lgkmcnt(0)\n"
        "s_barrier\n"
        "v_cvt_pk_bf16_f32 v231, v122, v123\n"
        "v_mfma_f32_16x16x32_bf16 v[112:115], a[244:247], a[152:155], v[112:115]\n"
        "v_mfma_f32_16x16x32_bf16 v[112:115], a[240:243], a[148:151], v[112:115]\n"
        "v_mfma_f32_16x16x32_bf16 v[112:115], a[236:239], a[144:147], v[112:115]\n"
        "s_nop 2\n"
        "v_mfma_f32_16x16x32_bf16 v[100:103], v[212:215], v[156:159], v[12:15]\n"
        "s_nop 2\n"
        "v_mfma_f32_16x16x32_bf16 v[100:103], v[228:231], v[164:167], v[100:103]\n"
        "v_cvt_pk_bf16_f32 v134, v112, v113\n"
        "v_cvt_pk_bf16_f32 v135, v114, v115\n"
        "v_mfma_f32_16x16x32_bf16 v[116:119], a[236:239], a[168:171], 0\n"
        "v_mfma_f32_16x16x32_bf16 v[116:119], a[240:243], a[172:175], v[116:119]\n"
        "v_mfma_f32_16x16x32_bf16 v[116:119], a[244:247], a[176:179], v[116:119]\n"
        "v_mfma_f32_16x16x32_bf16 v[116:119], a[248:251], a[180:183], v[116:119]\n"
        "v_mfma_f32_16x16x32_bf16 v[116:119], a[216:219], a[184:187], v[116:119]\n"
        "s_nop 0\n"
        "v_exp_f32 v100, v100\n"
        "v_mfma_f32_16x16x32_bf16 v[116:119], a[220:223], a[188:191], v[116:119]\n"
        "v_exp_f32 v101, v101\n"
        "v_mfma_f32_16x16x32_bf16 v[104:107], v[212:215], v[176:179], v[28:31]\n"
        "v_exp_f32 v102, v102\n"
        "v_mfma_f32_16x16x32_bf16 v[104:107], v[228:231], v[184:187], v[104:107]\n"
        "v_exp_f32 v103, v103\n"
        "v_mfma_f32_16x16x32_bf16 v[120:123], a[220:223], a[212:215], 0\n"
        "v_cvt_pk_bf16_f32 v70, v100, v101\n"
        "v_cvt_pk_bf16_f32 v71, v102, v103\n"
        "v_mfma_f32_16x16x32_bf16 v[120:123], a[216:219], a[208:211], v[120:123]\n"
        "v_cvt_pk_bf16_f32 v142, v116, v117\n"
        "v_cvt_pk_bf16_f32 v143, v118, v119\n"
        "v_mfma_f32_16x16x32_bf16 v[120:123], a[248:251], a[204:207], v[120:123]\n"
        "v_mov_b64 v[96:97], 0\n"
        "v_mfma_f32_16x16x32_bf16 v[120:123], a[244:247], a[200:203], v[120:123]\n"
        "v_exp_f32 v104, v104\n"
        "v_mfma_f32_16x16x32_bf16 v[120:123], a[240:243], a[196:199], v[120:123]\n"
        "v_exp_f32 v105, v105\n"
        "v_mfma_f32_16x16x32_bf16 v[120:123], a[236:239], a[192:195], v[120:123]\n"
        "v_exp_f32 v106, v106\n"
        "v_mfma_f32_16x16x32_bf16 v[108:111], v[212:215], v[196:199], v[44:47]\n"
        "v_exp_f32 v107, v107\n"
        "v_mfma_f32_16x16x32_bf16 v[108:111], v[228:231], v[204:207], v[108:111]\n"
        "v_cvt_pk_bf16_f32 v78, v104, v105\n"
        "v_cvt_pk_bf16_f32 v79, v106, v107\n"
        "v_mfma_f32_16x16x32_bf16 v[112:115], a[0:3], a[236:239], 0\n"
        "s_nop 0\n"
        "v_cvt_pk_bf16_f32 v150, v120, v121\n"
        "v_cvt_pk_bf16_f32 v151, v122, v123\n"
        "v_mfma_f32_16x16x32_bf16 v[112:115], a[4:7], a[240:243], v[112:115]\n"
        "v_mfma_f32_16x16x32_bf16 v[112:115], a[8:11], a[244:247], v[112:115]\n"
        "v_exp_f32 v108, v108\n"
        "v_mfma_f32_16x16x32_bf16 v[112:115], a[12:15], a[248:251], v[112:115]\n"
        "v_exp_f32 v109, v109\n"
        "v_mfma_f32_16x16x32_bf16 v[112:115], a[16:19], a[216:219], v[112:115]\n"
        "v_exp_f32 v110, v110\n"
        "v_mfma_f32_16x16x32_bf16 v[112:115], a[20:23], a[220:223], v[112:115]\n"
        "v_exp_f32 v111, v111\n"
        "v_smfmac_f32_16x16x64_bf16 v[96:99], a[252:255], v[64:71], v237\n"
        "v_cvt_pk_bf16_f32 v86, v108, v109\n"
        "v_mfma_f32_16x16x32_bf16 v[100:103], v[132:135], v[68:71], 0\n"
        "v_mov_b64 v[124:125], 0\n"
        "v_cvt_pk_bf16_f32 v87, v110, v111\n"
        "v_mfma_f32_16x16x32_bf16 v[100:103], v[128:131], v[64:67], v[100:103]\n"
        "s_nop 0\n"
        "v_cvt_pk_bf16_f32 v216, v112, v113\n"
        "v_cvt_pk_bf16_f32 v217, v114, v115\n"
        "v_mfma_f32_16x16x32_bf16 v[104:107], v[136:139], v[64:67], 0\n"
        "ds_read_b128 v[222:225], v251 offset:4608\n"
        "v_add_f32 v97, v96, v97\n"
        "v_mfma_f32_16x16x32_bf16 v[104:107], v[140:143], v[68:71], v[104:107]\n"
        "v_rcp_f32 v96, v97\n"
        "v_mfma_f32_16x16x32_bf16 v[108:111], v[148:151], v[68:71], 0\n"
        "v_mul_f32 v100, v96, v100\n"
        "v_mul_f32 v101, v96, v101\n"
        "v_mfma_f32_16x16x32_bf16 v[108:111], v[144:147], v[64:67], v[108:111]\n"
        "v_mul_f32 v102, v96, v102\n"
        "v_mul_f32 v103, v96, v103\n"
        "v_mfma_f32_16x16x32_bf16 v[116:119], a[44:47], a[220:223], 0\n"
        "v_cvt_pk_bf16_f32 v100, v100, v101\n"
        "v_cvt_pk_bf16_f32 v101, v102, v103\n"
        "v_mfma_f32_16x16x32_bf16 v[116:119], a[40:43], a[216:219], v[116:119]\n"
        "v_mul_f32 v104, v96, v104\n"
        "v_mul_f32 v105, v96, v105\n"
        "v_mfma_f32_16x16x32_bf16 v[116:119], a[36:39], a[248:251], v[116:119]\n"
        "v_mul_f32 v106, v96, v106\n"
        "v_mul_f32 v107, v96, v107\n"
        "v_mfma_f32_16x16x32_bf16 v[116:119], a[32:35], a[244:247], v[116:119]\n"
        "v_cvt_pk_bf16_f32 v102, v104, v105\n"
        "v_cvt_pk_bf16_f32 v103, v106, v107\n"
        "v_mfma_f32_16x16x32_bf16 v[116:119], a[28:31], a[240:243], v[116:119]\n"
        "ds_write_b128 v245, v[100:103] offset:0\n"
        "v_mfma_f32_16x16x32_bf16 v[116:119], a[24:27], a[236:239], v[116:119]\n"
        "v_mul_f32 v108, v96, v108\n"
        "v_mul_f32 v109, v96, v109\n"
        "v_smfmac_f32_16x16x64_bf16 v[124:127], a[252:255], v[72:79], v237\n"
        "v_mul_f32 v110, v96, v110\n"
        "v_mul_f32 v111, v96, v111\n"
        "v_mfma_f32_16x16x32_bf16 v[64:67], v[132:135], v[76:79], 0\n"
        "v_cvt_pk_bf16_f32 v108, v108, v109\n"
        "v_cvt_pk_bf16_f32 v109, v110, v111\n"
        "v_mfma_f32_16x16x32_bf16 v[64:67], v[128:131], v[72:75], v[64:67]\n"
        "ds_read_b128 v[156:159], v247 offset:0\n"
        "v_cvt_pk_bf16_f32 v218, v116, v117\n"
        "v_mfma_f32_16x16x32_bf16 v[68:71], v[136:139], v[72:75], 0\n"
        "ds_write_b64 v246, v[108:109] offset:0\n"
        "v_mfma_f32_16x16x32_bf16 v[68:71], v[140:143], v[76:79], v[68:71]\n"
        "ds_read_b128 v[160:163], v247 offset:4096\n"
        "v_cvt_pk_bf16_f32 v219, v118, v119\n"
        "v_mfma_f32_16x16x32_bf16 v[120:123], v[148:151], v[76:79], 0\n"
        "ds_read_b128 v[164:167], v247 offset:4352\n"
        "v_max_u32 v236, v96, v97\n"
        "v_mfma_f32_16x16x32_bf16 v[120:123], v[144:147], v[72:75], v[120:123]\n"
        "v_mov_b64 v[116:117], 0\n"
        "v_add_f32 v125, v124, v125\n"
        "v_mfma_f32_16x16x32_bf16 v[112:115], a[48:51], a[236:239], 0\n"
        "v_rcp_f32 v124, v125\n"
        "v_mfma_f32_16x16x32_bf16 v[112:115], a[52:55], a[240:243], v[112:115]\n"
        "v_mul_f32 v64, v124, v64\n"
        "v_mul_f32 v65, v124, v65\n"
        "v_mfma_f32_16x16x32_bf16 v[112:115], a[56:59], a[244:247], v[112:115]\n"
        "v_mul_f32 v66, v124, v66\n"
        "v_mul_f32 v67, v124, v67\n"
        "v_mfma_f32_16x16x32_bf16 v[112:115], a[60:63], a[248:251], v[112:115]\n"
        "v_cvt_pk_bf16_f32 v64, v64, v65\n"
        "v_cvt_pk_bf16_f32 v65, v66, v67\n"
        "v_mfma_f32_16x16x32_bf16 v[112:115], a[64:67], a[216:219], v[112:115]\n"
        "v_mul_f32 v68, v124, v68\n"
        "v_mul_f32 v69, v124, v69\n"
        "v_mfma_f32_16x16x32_bf16 v[112:115], a[68:71], a[220:223], v[112:115]\n"
        "ds_read_b128 a[216:219], v248 offset:0\n"
        "v_mul_f32 v70, v124, v70\n"
        "v_smfmac_f32_16x16x64_bf16 v[116:119], a[252:255], v[80:87], v237\n"
        "ds_read_b128 a[220:223], v248 offset:3072\n"
        "v_mul_f32 v71, v124, v71\n"
        "v_mfma_f32_16x16x32_bf16 v[72:75], v[132:135], v[84:87], 0\n"
        "v_cvt_pk_bf16_f32 v66, v68, v69\n"
        "v_cvt_pk_bf16_f32 v67, v70, v71\n"
        "v_mfma_f32_16x16x32_bf16 v[72:75], v[128:131], v[80:83], v[72:75]\n"
        "ds_write_b128 v245, v[64:67] offset:256\n"
        "v_mfma_f32_16x16x32_bf16 v[76:79], v[136:139], v[80:83], 0\n"
        "v_cvt_pk_bf16_f32 v220, v112, v113\n"
        "v_cvt_pk_bf16_f32 v221, v114, v115\n"
        "v_mfma_f32_16x16x32_bf16 v[76:79], v[140:143], v[84:87], v[76:79]\n"
        "ds_read_b128 a[224:227], v248 offset:32\n"
        "v_cvt_pk_bf16_f32 v226, v112, v113\n"
        "v_mfma_f32_16x16x32_bf16 v[96:99], v[148:151], v[84:87], 0\n"
        "ds_read_b128 a[228:231], v248 offset:3104\n"
        "v_cvt_pk_bf16_f32 v227, v114, v115\n"
        "v_mfma_f32_16x16x32_bf16 v[96:99], v[144:147], v[80:83], v[96:99]\n"
        "v_mul_f32 v120, v124, v120\n"
        "v_mul_f32 v121, v124, v121\n"
        "v_mfma_f32_16x16x32_bf16 v[100:103], v[152:155], v[216:219], v[48:51]\n"
        "s_waitcnt lgkmcnt(3)\n"
        "ds_read_b128 v[152:155], v247 offset:256\n"
        "v_mul_f32 v122, v124, v122\n"
        "v_mfma_f32_16x16x32_bf16 v[100:103], v[168:171], v[220:223], v[100:103]\n"
        "v_mul_f32 v123, v124, v123\n"
        "v_cvt_pk_bf16_f32 v120, v120, v121\n"
        "v_mfma_f32_16x16x32_bf16 v[104:107], v[172:175], v[216:219], v[52:55]\n"
        "v_cvt_pk_bf16_f32 v121, v122, v123\n"
        "v_max3_u32 v236, v236, v124, v125\n"
        "v_mfma_f32_16x16x32_bf16 v[104:107], v[188:191], v[224:227], v[104:107]\n"
        "ds_write_b64 v246, v[120:121] offset:256\n"
        "v_mfma_f32_16x16x32_bf16 v[176:179], v[156:159], a[216:219], 0\n"
        "ds_read_b128 v[222:225], v252 offset:4608\n"
        "v_add_f32 v117, v116, v117\n"
        "v_mfma_f32_16x16x32_bf16 v[176:179], v[160:163], a[220:223], v[176:179]\n"
        "ds_read_b128 a[232:235], v254 offset:0\n"
        "v_mfma_f32_16x16x32_bf16 v[196:199], v[164:167], a[220:223], 0\n"
        "s_waitcnt lgkmcnt(3)\n"
        "ds_read_b128 a[220:223], v254 offset:3072\n"
        "v_mfma_f32_16x16x32_bf16 v[196:199], v[152:155], a[216:219], v[196:199]\n"
        "v_rcp_f32 v116, v117\n"
        "v_mfma_f32_16x16x32_bf16 v[200:203], v[152:155], a[224:227], 0\n"
        "ds_read_b128 a[216:219], v254 offset:32\n"
        "v_mul_f32 v72, v116, v72\n"
        "v_mfma_f32_16x16x32_bf16 v[200:203], v[164:167], a[228:231], v[200:203]\n"
        "v_exp_f32 v100, v100\n"
        "v_mfma_f32_16x16x32_bf16 v[180:183], v[160:163], a[228:231], 0\n"
        "ds_read_b128 a[228:231], v254 offset:3104\n"
        "v_mul_f32 v73, v116, v73\n"
        "v_mfma_f32_16x16x32_bf16 v[180:183], v[156:159], a[224:227], v[180:183]\n"
        "ds_read_b128 a[236:239], v247 offset:8192\n"
        "v_mul_f32 v74, v116, v74\n"
        "v_mfma_f32_16x16x32_bf16 v[108:111], v[192:195], v[216:219], v[56:59]\n"
        "s_waitcnt lgkmcnt(4)\n"
        "ds_read_b128 a[224:227], v248 offset:16\n"
        "v_mul_f32 v75, v116, v75\n"
        "v_mfma_f32_16x16x32_bf16 v[108:111], v[208:211], v[220:223], v[108:111]\n"
        "ds_read_b128 a[240:243], v247 offset:12288\n"
        "v_cvt_pk_bf16_f32 v72, v72, v73\n"
        "v_mfma_f32_16x16x32_bf16 v[112:115], v[212:215], v[216:219], v[60:63]\n"
        "ds_read_b128 a[244:247], v247 offset:12544\n"
        "v_cvt_pk_bf16_f32 v73, v74, v75\n"
        "v_mfma_f32_16x16x32_bf16 v[112:115], v[228:231], v[224:227], v[112:115]\n"
        "ds_read_b128 a[248:251], v247 offset:8448\n"
        "v_mul_f32 v76, v116, v76\n"
        "v_mfma_f32_16x16x32_bf16 v[184:187], v[156:159], a[232:235], 0\n"
        "s_waitcnt lgkmcnt(6)\n"
        "v_exp_f32 v101, v101\n"
        "v_mfma_f32_16x16x32_bf16 v[184:187], v[160:163], a[220:223], v[184:187]\n"
        "v_mul_f32 v77, v116, v77\n"
        "v_mul_f32 v78, v116, v78\n"
        "v_mfma_f32_16x16x32_bf16 v[204:207], v[164:167], a[220:223], 0\n"
        "ds_read_b128 a[220:223], v248 offset:3088\n"
        "v_mul_f32 v79, v116, v79\n"
        "v_mfma_f32_16x16x32_bf16 v[204:207], v[152:155], a[232:235], v[204:207]\n"
        "ds_read_b128 a[232:235], v254 offset:16\n"
        "v_cvt_pk_bf16_f32 v88, v100, v101\n"
        "v_mfma_f32_16x16x32_bf16 v[192:195], v[152:155], a[216:219], 0\n"
        "s_waitcnt lgkmcnt(4)\n"
        "v_exp_f32 v102, v102\n"
        "v_mfma_f32_16x16x32_bf16 v[192:195], v[164:167], a[228:231], v[192:195]\n"
        "v_exp_f32 v104, v104\n"
        "v_mfma_f32_16x16x32_bf16 v[172:175], v[160:163], a[228:231], 0\n"
        "ds_read_b128 a[228:231], v254 offset:3088\n"
        "v_cvt_pk_bf16_f32 v74, v76, v77\n"
        "v_mfma_f32_16x16x32_bf16 v[172:175], v[156:159], a[216:219], v[172:175]\n"
        "ds_read_b128 a[216:219], v248 offset:6144\n"
        "v_cvt_pk_bf16_f32 v75, v78, v79\n"
        "v_mfma_f32_16x16x32_bf16 v[212:215], v[156:159], a[224:227], 0\n"
        "s_waitcnt lgkmcnt(3)\n"
        "ds_write_b128 v245, v[72:75] offset:512\n"
        "v_mfma_f32_16x16x32_bf16 v[212:215], v[160:163], a[220:223], v[212:215]\n"
        "v_exp_f32 v103, v103\n"
        "v_mfma_f32_16x16x32_bf16 v[216:219], v[164:167], a[220:223], 0\n"
        "ds_read_b128 a[220:223], v248 offset:9216\n"
        "v_cvt_pk_bf16_f32 v89, v102, v103\n"
        "v_mfma_f32_16x16x32_bf16 v[216:219], v[152:155], a[224:227], v[216:219]\n"
        "s_waitcnt lgkmcnt(4)\n"
        "ds_read_b128 a[224:227], v248 offset:6176\n"
        "v_mul_f32 v96, v116, v96\n"
        "v_mfma_f32_16x16x32_bf16 v[224:227], v[152:155], a[232:235], 0\n"
        "s_waitcnt lgkmcnt(4)\n"
        "v_exp_f32 v105, v105\n"
        "v_mfma_f32_16x16x32_bf16 v[224:227], v[164:167], a[228:231], v[224:227]\n"
        "v_exp_f32 v106, v106\n"
        "v_mfma_f32_16x16x32_bf16 v[220:223], v[160:163], a[228:231], 0\n"
        "ds_read_b128 a[228:231], v248 offset:9248\n"
        "v_cvt_pk_bf16_f32 v90, v104, v105\n"
        "v_mfma_f32_16x16x32_bf16 v[220:223], v[156:159], a[232:235], v[220:223]\n"
        "s_waitcnt lgkmcnt(4)\n"
        "ds_read_b128 a[232:235], v254 offset:6144\n"
        "v_mul_f32 v97, v116, v97\n"
        "v_mfma_f32_16x16x32_bf16 v[176:179], a[236:239], a[216:219], v[176:179]\n"
        "s_waitcnt lgkmcnt(3)\n"
        "v_mul_f32 v98, v116, v98\n"
        "v_mul_f32 v99, v116, v99\n"
        "v_mfma_f32_16x16x32_bf16 v[176:179], a[240:243], a[220:223], v[176:179]\n"
        "v_exp_f32 v107, v107\n"
        "v_mfma_f32_16x16x32_bf16 v[196:199], a[244:247], a[220:223], v[196:199]\n"
        "ds_read_b128 a[220:223], v254 offset:9216\n"
        "v_cvt_pk_bf16_f32 v91, v106, v107\n"
        "v_mfma_f32_16x16x32_bf16 v[196:199], a[248:251], a[216:219], v[196:199]\n"
        "s_waitcnt lgkmcnt(3)\n"
        "ds_read_b128 a[216:219], v254 offset:6176\n"
        "v_cvt_pk_bf16_f32 v96, v96, v97\n"
        "v_mfma_f32_16x16x32_bf16 v[200:203], a[248:251], a[224:227], v[200:203]\n"
        "s_waitcnt lgkmcnt(3)\n"
        "ds_read_b128 v[156:159], v247 offset:16384\n"
        "v_cvt_pk_bf16_f32 v97, v98, v99\n"
        "v_mfma_f32_16x16x32_bf16 v[200:203], a[244:247], a[228:231], v[200:203]\n"
        "ds_write_b64 v246, v[96:97] offset:512\n"
        "v_mfma_f32_16x16x32_bf16 v[180:183], a[240:243], a[228:231], v[180:183]\n"
        "ds_read_b128 a[228:231], v254 offset:9248\n"
        "v_max3_u32 v236, v236, v116, v117\n"
        "v_mfma_f32_16x16x32_bf16 v[180:183], a[236:239], a[224:227], v[180:183]\n"
        "s_waitcnt lgkmcnt(5)\n"
        "ds_read_b128 a[224:227], v248 offset:6160\n"
        "v_bfe_u32 v238, v250, 3, 4\n"
        "v_mfma_f32_16x16x32_bf16 v[184:187], a[236:239], a[232:235], v[184:187]\n"
        ".long 0xbec200ff\n"
        "44:\n"
        "s_branch 60b\n"
        "s_waitcnt lgkmcnt(5)\n"
        "ds_read_b128 v[160:163], v247 offset:20480\n"
        "v_mad_i32_i24 v238, v238, -4, v250\n"
        "v_mfma_f32_16x16x32_bf16 v[184:187], a[240:243], a[220:223], v[184:187]\n"
        "ds_read_b128 v[164:167], v247 offset:20736\n"
        "v_add_u32 v238, s49, v238\n"
        "v_mfma_f32_16x16x32_bf16 v[204:207], a[244:247], a[220:223], v[204:207]\n"
        "ds_read_b128 a[220:223], v248 offset:9232\n"
        "v_mfma_f32_16x16x32_bf16 v[204:207], a[248:251], a[232:235], v[204:207]\n"
        "s_waitcnt lgkmcnt(6)\n"
        "ds_read_b128 a[232:235], v254 offset:6160\n"
        "v_mfma_f32_16x16x32_bf16 v[192:195], a[248:251], a[216:219], v[192:195]\n"
        "s_waitcnt lgkmcnt(5)\n"
        "ds_read_b128 v[152:155], v247 offset:16640\n"
        "v_mfma_f32_16x16x32_bf16 v[192:195], a[244:247], a[228:231], v[192:195]\n"
        "v_exp_f32 v108, v108\n"
        "v_mfma_f32_16x16x32_bf16 v[172:175], a[240:243], a[228:231], v[172:175]\n"
        "ds_read_b128 a[228:231], v254 offset:9232\n"
        "v_mfma_f32_16x16x32_bf16 v[172:175], a[236:239], a[216:219], v[172:175]\n"
        "s_waitcnt lgkmcnt(5)\n"
        "ds_read_b128 a[216:219], v248 offset:12288\n"
        "v_mfma_f32_16x16x32_bf16 v[212:215], a[236:239], a[224:227], v[212:215]\n"
        "s_waitcnt lgkmcnt(4)\n"
        "v_exp_f32 v109, v109\n"
        "v_mfma_f32_16x16x32_bf16 v[212:215], a[240:243], a[220:223], v[212:215]\n"
        "v_exp_f32 v110, v110\n"
        "v_mfma_f32_16x16x32_bf16 v[216:219], a[244:247], a[220:223], v[216:219]\n"
        "ds_read_b128 a[220:223], v248 offset:15360\n"
        "v_cvt_pk_bf16_f32 v92, v108, v109\n"
        "v_mfma_f32_16x16x32_bf16 v[216:219], a[248:251], a[224:227], v[216:219]\n"
        "s_waitcnt lgkmcnt(3)\n"
        "ds_read_b128 a[224:227], v248 offset:12320\n"
        "v_mfma_f32_16x16x32_bf16 v[224:227], a[248:251], a[232:235], v[224:227]\n"
        "s_waitcnt lgkmcnt(3)\n"
        "v_exp_f32 v112, v112\n"
        "v_mfma_f32_16x16x32_bf16 v[224:227], a[244:247], a[228:231], v[224:227]\n"
        "v_exp_f32 v111, v111\n"
        "v_mfma_f32_16x16x32_bf16 v[220:223], a[240:243], a[228:231], v[220:223]\n"
        "ds_read_b128 a[228:231], v248 offset:15392\n"
        "v_cvt_pk_bf16_f32 v93, v110, v111\n"
        "v_mfma_f32_16x16x32_bf16 v[220:223], a[236:239], a[232:235], v[220:223]\n"
        "s_waitcnt lgkmcnt(3)\n"
        "ds_read_b128 a[232:235], v254 offset:12288\n"
        "v_mfma_f32_16x16x32_bf16 v[176:179], v[156:159], a[216:219], v[176:179]\n"
        "s_waitcnt lgkmcnt(3)\n"
        "v_exp_f32 v113, v113\n"
        "v_mfma_f32_16x16x32_bf16 v[176:179], v[160:163], a[220:223], v[176:179]\n"
        "v_exp_f32 v114, v114\n"
        "v_mfma_f32_16x16x32_bf16 v[196:199], v[164:167], a[220:223], v[196:199]\n"
        "ds_read_b128 a[220:223], v254 offset:15360\n"
        "v_cvt_pk_bf16_f32 v94, v112, v113\n"
        "v_mfma_f32_16x16x32_bf16 v[196:199], v[152:155], a[216:219], v[196:199]\n"
        "s_waitcnt lgkmcnt(3)\n"
        "ds_read_b128 a[216:219], v254 offset:12320\n"
        "v_mfma_f32_16x16x32_bf16 v[200:203], v[152:155], a[224:227], v[200:203]\n"
        "s_waitcnt lgkmcnt(3)\n"
        "v_exp_f32 v115, v115\n"
        "v_mfma_f32_16x16x32_bf16 v[200:203], v[164:167], a[228:231], v[200:203]\n"
        "v_cvt_pk_bf16_f32 v95, v114, v115\n"
        "v_mfma_f32_16x16x32_bf16 v[180:183], v[160:163], a[228:231], v[180:183]\n"
        "s_waitcnt vmcnt(0)\n"
        "s_barrier\n"
        "ds_read_b128 a[228:231], v254 offset:15392\n"
        "v_mfma_f32_16x16x32_bf16 v[180:183], v[156:159], a[224:227], v[180:183]\n"
        "s_waitcnt lgkmcnt(3)\n"
        "ds_read_b128 a[224:227], v248 offset:12304\n"
        "v_mfma_f32_16x16x32_bf16 v[184:187], v[156:159], a[232:235], v[184:187]\n"
        "s_waitcnt lgkmcnt(3)\n"
        "v_mfma_f32_16x16x32_bf16 v[184:187], v[160:163], a[220:223], v[184:187]\n"
        "v_mfma_f32_16x16x32_bf16 v[204:207], v[164:167], a[220:223], v[204:207]\n"
        "ds_read_b128 a[220:223], v248 offset:15376\n"
        "v_mfma_f32_16x16x32_bf16 v[204:207], v[152:155], a[232:235], v[204:207]\n"
        "s_waitcnt lgkmcnt(3)\n"
        "ds_read_b128 a[232:235], v254 offset:12304\n"
        "v_mfma_f32_16x16x32_bf16 v[192:195], v[152:155], a[216:219], v[192:195]\n"
        "s_waitcnt lgkmcnt(3)\n"
        "ds_read_b128 a[236:239], v240 offset:0\n"
        "v_mfma_f32_16x16x32_bf16 v[192:195], v[164:167], a[228:231], v[192:195]\n"
        "ds_read_b128 a[240:243], v241 offset:0\n"
        "v_mfma_f32_16x16x32_bf16 v[172:175], v[160:163], a[228:231], v[172:175]\n"
        "ds_read_b128 a[228:231], v254 offset:15376\n"
        "v_mfma_f32_16x16x32_bf16 v[172:175], v[156:159], a[216:219], v[172:175]\n"
        "s_waitcnt lgkmcnt(5)\n"
        "ds_read_b128 a[244:247], v240 offset:128\n"
        "v_mfma_f32_16x16x32_bf16 v[212:215], v[156:159], a[224:227], v[212:215]\n"
        "s_waitcnt lgkmcnt(5)\n"
        "ds_read_b128 a[248:251], v241 offset:128\n"
        "v_mfma_f32_16x16x32_bf16 v[212:215], v[160:163], a[220:223], v[212:215]\n"
        "ds_read_b128 a[216:219], v240 offset:256\n"
        "v_mfma_f32_16x16x32_bf16 v[216:219], v[164:167], a[220:223], v[216:219]\n"
        "ds_read_b128 a[220:223], v241 offset:256\n"
        "v_mfma_f32_16x16x32_bf16 v[216:219], v[152:155], a[224:227], v[216:219]\n"
        "s_waitcnt lgkmcnt(6)\n"
        "v_mfma_f32_16x16x32_bf16 v[224:227], v[152:155], a[232:235], v[224:227]\n"
        "s_waitcnt lgkmcnt(4)\n"
        "v_mfma_f32_16x16x32_bf16 v[224:227], v[164:167], a[228:231], v[224:227]\n"
        "v_mfma_f32_16x16x32_bf16 v[220:223], v[160:163], a[228:231], v[220:223]\n"
        "v_mfma_f32_16x16x32_bf16 v[220:223], v[156:159], a[232:235], v[220:223]\n"
        "s_waitcnt lgkmcnt(0)\n"
        "s_cmp_eq_u32 s42, 2\n"
        "s_cbranch_scc1 12f\n"
        "s_cmp_eq_u32 s42, 3\n"
        "s_cbranch_scc1 12f\n"
        ".p2align 6\n"
        "s_nop 0\n"
        "s_nop 0\n"
        "s_nop 0\n"
        "s_nop 0\n"
        "11:\n"
        "10:\n"
        "v_mfma_f32_16x16x32_bf16 v[112:115], a[72:75], a[236:239], 0\n"
        "v_mov_b64 v[124:125], 0\n"
        "v_cvt_pk_bf16_f32 v232, v176, v180\n"
        "v_mfma_f32_16x16x32_bf16 v[112:115], a[76:79], a[240:243], v[112:115]\n"
        "s_waitcnt lgkmcnt(3)\n"
        "s_add_u32 s41, s30, s40\n"
        "v_cvt_pk_bf16_f32 v233, v184, v172\n"
        "v_cvt_pk_bf16_f32 v234, v177, v181\n"
        "v_mfma_f32_16x16x32_bf16 v[112:115], a[80:83], a[244:247], v[112:115]\n"
        "s_waitcnt lgkmcnt(2)\n"
        "buffer_store_dwordx2 v[232:233], v250, s[16:19], s38 offen offset:0 sc1\n"
        "v_cvt_pk_bf16_f32 v235, v185, v173\n"
        "v_mfma_f32_16x16x32_bf16 v[112:115], a[84:87], a[248:251], v[112:115]\n"
        "s_waitcnt lgkmcnt(1)\n"
        "v_cvt_pk_bf16_f32 v232, v178, v182\n"
        "v_cvt_pk_bf16_f32 v233, v186, v174\n"
        "v_mfma_f32_16x16x32_bf16 v[112:115], a[88:91], a[216:219], v[112:115]\n"
        "s_waitcnt lgkmcnt(0)\n"
        "v_mfma_f32_16x16x32_bf16 v[112:115], a[92:95], a[220:223], v[112:115]\n"
        "s_nop 2\n"
        "v_smfmac_f32_16x16x64_bf16 v[124:127], a[252:255], v[88:95], v237\n"
        "s_nop 1\n"
        "v_mfma_f32_16x16x32_bf16 v[80:83], v[132:135], v[92:95], 0\n"
        "buffer_store_dwordx2 v[234:235], v250, s[16:19], s38 offen offset:384 sc1\n"
        "v_cvt_pk_bf16_f32 v152, v112, v113\n"
        "v_mfma_f32_16x16x32_bf16 v[80:83], v[128:131], v[88:91], v[80:83]\n"
        "v_cvt_pk_bf16_f32 v153, v114, v115\n"
        "v_cvt_pk_bf16_f32 v234, v179, v183\n"
        "v_mfma_f32_16x16x32_bf16 v[84:87], v[136:139], v[88:91], 0\n"
        "v_cvt_pk_bf16_f32 v235, v187, v175\n"
        "v_add_f32 v125, v124, v125\n"
        "v_mfma_f32_16x16x32_bf16 v[84:87], v[140:143], v[92:95], v[84:87]\n"
        "v_rcp_f32 v124, v125\n"
        "v_mfma_f32_16x16x32_bf16 v[96:99], v[148:151], v[92:95], 0\n"
        "buffer_store_dwordx2 v[232:233], v250, s[16:19], s38 offen offset:768 sc1\n"
        "v_mul_f32 v80, v124, v80\n"
        "v_mfma_f32_16x16x32_bf16 v[96:99], v[144:147], v[88:91], v[96:99]\n"
        "s_add_u32 s35, s47, 786432\n"
        "v_mul_f32 v81, v124, v81\n"
        "v_mul_f32 v82, v124, v82\n"
        "v_mfma_f32_16x16x32_bf16 v[116:119], a[116:119], a[220:223], 0\n"
        "s_sub_u32 s46, s35, 100663296\n"
        "v_mul_f32 v83, v124, v83\n"
        "v_cvt_pk_bf16_f32 v80, v80, v81\n"
        "v_mfma_f32_16x16x32_bf16 v[116:119], a[112:115], a[216:219], v[116:119]\n"
        "s_cmp_lt_u32 s46, 786432\n"
        "v_cvt_pk_bf16_f32 v81, v82, v83\n"
        "v_cvt_pk_bf16_f32 v232, v196, v200\n"
        "v_mfma_f32_16x16x32_bf16 v[116:119], a[108:111], a[248:251], v[116:119]\n"
        "buffer_store_dwordx2 v[234:235], v250, s[16:19], s38 offen offset:1152 sc1\n"
        "v_cvt_pk_bf16_f32 v233, v204, v192\n"
        "v_mfma_f32_16x16x32_bf16 v[116:119], a[104:107], a[244:247], v[116:119]\n"
        "s_cselect_b32 s35, s46, s35\n"
        "v_mul_f32 v84, v124, v84\n"
        "v_mul_f32 v85, v124, v85\n"
        "v_mfma_f32_16x16x32_bf16 v[116:119], a[100:103], a[240:243], v[116:119]\n"
        "v_mul_f32 v86, v124, v86\n"
        "v_mul_f32 v87, v124, v87\n"
        "v_mfma_f32_16x16x32_bf16 v[116:119], a[96:99], a[236:239], v[116:119]\n"
        "v_cvt_pk_bf16_f32 v82, v84, v85\n"
        "v_cvt_pk_bf16_f32 v83, v86, v87\n"
        "v_mfma_f32_16x16x32_bf16 v[120:123], a[120:123], a[236:239], 0\n"
        "ds_write_b128 v245, v[80:83] offset:768\n"
        "v_mfma_f32_16x16x32_bf16 v[120:123], a[124:127], a[240:243], v[120:123]\n"
        "buffer_store_dwordx2 v[232:233], v250, s[16:19], s39 offen offset:0 sc1\n"
        "v_mul_f32 v96, v124, v96\n"
        "v_mfma_f32_16x16x32_bf16 v[120:123], a[128:131], a[244:247], v[120:123]\n"
        "v_mul_f32 v97, v124, v97\n"
        "v_mul_f32 v98, v124, v98\n"
        "v_mfma_f32_16x16x32_bf16 v[120:123], a[132:135], a[248:251], v[120:123]\n"
        "v_mul_f32 v99, v124, v99\n"
        "v_cvt_pk_bf16_f32 v234, v197, v201\n"
        "v_mfma_f32_16x16x32_bf16 v[120:123], a[136:139], a[216:219], v[120:123]\n"
        "v_cvt_pk_bf16_f32 v235, v205, v193\n"
        "v_cvt_pk_bf16_f32 v154, v116, v117\n"
        "v_mfma_f32_16x16x32_bf16 v[120:123], a[140:143], a[220:223], v[120:123]\n"
        "buffer_store_dwordx2 v[234:235], v250, s[16:19], s39 offen offset:384 sc1\n"
        "v_cvt_pk_bf16_f32 v155, v118, v119\n"
        "v_mfma_f32_16x16x32_bf16 v[112:115], a[20:23], a[220:223], 0\n"
        "v_cvt_pk_bf16_f32 v96, v96, v97\n"
        "v_cvt_pk_bf16_f32 v97, v98, v99\n"
        "v_mfma_f32_16x16x32_bf16 v[112:115], a[16:19], a[216:219], v[112:115]\n"
        "ds_write_b64 v246, v[96:97] offset:768\n"
        "v_mfma_f32_16x16x32_bf16 v[112:115], a[12:15], a[248:251], v[112:115]\n"
        "v_max3_u32 v236, v236, v124, v125\n"
        "v_cvt_pk_bf16_f32 v232, v198, v202\n"
        "v_mfma_f32_16x16x32_bf16 v[112:115], a[8:11], a[244:247], v[112:115]\n"
        "v_cvt_pk_bf16_f32 v233, v206, v194\n"
        "v_cmp_lt_u32 vcc, s24, v236\n"
        "v_mfma_f32_16x16x32_bf16 v[112:115], a[4:7], a[240:243], v[112:115]\n"
        "buffer_store_dwordx2 v[232:233], v250, s[16:19], s39 offen offset:768 sc1\n"
        "v_cvt_pk_bf16_f32 v168, v120, v121\n"
        "v_mfma_f32_16x16x32_bf16 v[112:115], a[0:3], a[236:239], v[112:115]\n"
        "s_cmp_lg_u64 vcc, 0\n"
        "s_addc_u32 s25, s25, s25\n"
        "v_cvt_pk_bf16_f32 v169, v122, v123\n"
        "v_mfma_f32_16x16x32_bf16 v[116:119], a[24:27], a[236:239], 0\n"
        "v_cvt_pk_bf16_f32 v234, v199, v203\n"
        "v_cvt_pk_bf16_f32 v235, v207, v195\n"
        "v_mfma_f32_16x16x32_bf16 v[116:119], a[28:31], a[240:243], v[116:119]\n"
        "v_cvt_pk_bf16_f32 v232, v212, v220\n"
        "v_cvt_pk_bf16_f32 v156, v112, v113\n"
        "v_mfma_f32_16x16x32_bf16 v[116:119], a[32:35], a[244:247], v[116:119]\n"
        "buffer_store_dwordx2 v[234:235], v250, s[16:19], s39 offen offset:1152 sc1\n"
        "v_cvt_pk_bf16_f32 v157, v114, v115\n"
        "v_mfma_f32_16x16x32_bf16 v[116:119], a[36:39], a[248:251], v[116:119]\n"
        "v_cvt_pk_bf16_f32 v234, v213, v221\n"
        "v_xor_b32 v251, 0x2000, v251\n"
        "v_mfma_f32_16x16x32_bf16 v[116:119], a[40:43], a[216:219], v[116:119]\n"
        "v_xor_b32 v252, 0x2000, v252\n"
        "v_mfma_f32_16x16x32_bf16 v[116:119], a[44:47], a[220:223], v[116:119]\n"
        "s_nop 2\n"
        "v_mfma_f32_16x16x32_bf16 v[120:123], a[68:71], a[220:223], 0\n"
        "s_nop 2\n"
        "v_mfma_f32_16x16x32_bf16 v[120:123], a[64:67], a[216:219], v[120:123]\n"
        "buffer_store_dword v232, v238, s[16:19], s38 offen offset:0 sc1\n"
        "v_mfma_f32_16x16x32_bf16 v[120:123], a[60:63], a[248:251], v[120:123]\n"
        "ds_read_b128 a[224:227], v240 offset:1536\n"
        "v_cvt_pk_bf16_f32 v158, v116, v117\n"
        "v_mfma_f32_16x16x32_bf16 v[120:123], a[56:59], a[244:247], v[120:123]\n"
        "ds_read_b128 a[228:231], v241 offset:1536\n"
        "v_cvt_pk_bf16_f32 v159, v118, v119\n"
        "v_mfma_f32_16x16x32_bf16 v[120:123], a[52:55], a[240:243], v[120:123]\n"
        "ds_read_b128 a[232:235], v240 offset:1664\n"
        "v_cvt_pk_bf16_f32 v232, v214, v222\n"
        "v_mfma_f32_16x16x32_bf16 v[120:123], a[48:51], a[236:239], v[120:123]\n"
        "buffer_store_dword v234, v238, s[16:19], s38 offen offset:384 sc1\n"
        "v_mfma_f32_16x16x32_bf16 v[112:115], a[236:239], a[144:147], 0\n"
        "v_add_u32 v247, s45, v247\n"
        "v_cvt_pk_bf16_f32 v234, v215, v223\n"
        "v_mfma_f32_16x16x32_bf16 v[112:115], a[240:243], a[148:151], v[112:115]\n"
        "s_nop 2\n"
        "v_mfma_f32_16x16x32_bf16 v[112:115], a[244:247], a[152:155], v[112:115]\n"
        "v_cvt_pk_bf16_f32 v160, v120, v121\n"
        "v_cvt_pk_bf16_f32 v161, v122, v123\n"
        "v_mfma_f32_16x16x32_bf16 v[112:115], a[248:251], a[156:159], v[112:115]\n"
        "v_cvt_pk_bf16_f32 v166, v120, v121\n"
        "v_cvt_pk_bf16_f32 v167, v122, v123\n"
        "v_mfma_f32_16x16x32_bf16 v[112:115], a[216:219], a[160:163], v[112:115]\n"
        "buffer_store_dword v232, v238, s[16:19], s38 offen offset:768 sc1\n"
        "v_mfma_f32_16x16x32_bf16 v[112:115], a[220:223], a[164:167], v[112:115]\n"
        "s_nop 2\n"
        "v_mfma_f32_16x16x32_bf16 v[116:119], a[220:223], a[188:191], 0\n"
        "v_cvt_pk_bf16_f32 v232, v216, v224\n"
        "s_nop 0\n"
        "v_mfma_f32_16x16x32_bf16 v[116:119], a[216:219], a[184:187], v[116:119]\n"
        "s_nop 0\n"
        "v_cvt_pk_bf16_f32 v128, v112, v113\n"
        "v_cvt_pk_bf16_f32 v129, v114, v115\n"
        "v_mfma_f32_16x16x32_bf16 v[116:119], a[248:251], a[180:183], v[116:119]\n"
        "buffer_store_dword v234, v238, s[16:19], s38 offen offset:1152 sc1\n"
        "v_mfma_f32_16x16x32_bf16 v[116:119], a[244:247], a[176:179], v[116:119]\n"
        "s_nop 0\n"
        "v_cvt_pk_bf16_f32 v234, v217, v225\n"
        "v_mfma_f32_16x16x32_bf16 v[116:119], a[240:243], a[172:175], v[116:119]\n"
        "v_mfma_f32_16x16x32_bf16 v[116:119], a[236:239], a[168:171], v[116:119]\n"
        "s_nop 2\n"
        "v_mfma_f32_16x16x32_bf16 v[120:123], a[236:239], a[192:195], 0\n"
        "ds_read_b128 a[236:239], v241 offset:1664\n"
        "s_nop 0\n"
        "v_mfma_f32_16x16x32_bf16 v[120:123], a[240:243], a[196:199], v[120:123]\n"
        "buffer_store_dword v232, v238, s[16:19], s39 offen offset:0 sc1\n"
        "v_mfma_f32_16x16x32_bf16 v[120:123], a[244:247], a[200:203], v[120:123]\n"
        "ds_read_b128 a[240:243], v240 offset:1792\n"
        "ds_read_b128 a[244:247], v241 offset:1792\n"
        "v_mfma_f32_16x16x32_bf16 v[120:123], a[248:251], a[204:207], v[120:123]\n"
        "v_cvt_pk_bf16_f32 v136, v116, v117\n"
        "v_cvt_pk_bf16_f32 v137, v118, v119\n"
        "v_mfma_f32_16x16x32_bf16 v[120:123], a[216:219], a[208:211], v[120:123]\n"
        "v_cvt_pk_bf16_f32 v232, v218, v226\n"
        "v_mfma_f32_16x16x32_bf16 v[120:123], a[220:223], a[212:215], v[120:123]\n"
        "s_waitcnt lgkmcnt(5)\n"
        "buffer_store_dword v234, v238, s[16:19], s39 offen offset:384 sc1\n"
        "v_mfma_f32_16x16x32_bf16 v[112:115], a[72:75], a[224:227], 0\n"
        "s_waitcnt lgkmcnt(4)\n"
        "s_nop 1\n"
        "v_mfma_f32_16x16x32_bf16 v[112:115], a[76:79], a[228:231], v[112:115]\n"
        "s_waitcnt lgkmcnt(3)\n"
        "v_cvt_pk_bf16_f32 v234, v219, v227\n"
        "s_nop 0\n"
        "v_mfma_f32_16x16x32_bf16 v[112:115], a[80:83], a[232:235], v[112:115]\n"
        "s_waitcnt lgkmcnt(2)\n"
        "v_cvt_pk_bf16_f32 v144, v120, v121\n"
        "v_cvt_pk_bf16_f32 v145, v122, v123\n"
        "v_mfma_f32_16x16x32_bf16 v[112:115], a[84:87], a[236:239], v[112:115]\n"
        "s_waitcnt lgkmcnt(1)\n"
        "v_mfma_f32_16x16x32_bf16 v[112:115], a[88:91], a[240:243], v[112:115]\n"
        "s_waitcnt lgkmcnt(0)\n"
        "buffer_store_dword v232, v238, s[16:19], s39 offen offset:768 sc1\n"
        "v_mfma_f32_16x16x32_bf16 v[112:115], a[92:95], a[244:247], v[112:115]\n"
        "s_nop 2\n"
        "v_mfma_f32_16x16x32_bf16 v[116:119], a[116:119], a[244:247], 0\n"
        "s_nop 2\n"
        "v_mfma_f32_16x16x32_bf16 v[116:119], a[112:115], a[240:243], v[116:119]\n"
        "v_cvt_pk_bf16_f32 v172, v112, v113\n"
        "v_cvt_pk_bf16_f32 v173, v114, v115\n"
        "v_mfma_f32_16x16x32_bf16 v[116:119], a[108:111], a[236:239], v[116:119]\n"
        "buffer_store_dword v234, v238, s[16:19], s39 offen offset:1152 sc1\n"
        "v_mfma_f32_16x16x32_bf16 v[116:119], a[104:107], a[232:235], v[116:119]\n"
        "v_mfma_f32_16x16x32_bf16 v[116:119], a[100:103], a[228:231], v[116:119]\n"
        "v_mfma_f32_16x16x32_bf16 v[116:119], a[96:99], a[224:227], v[116:119]\n"
        "s_mov_b32 m0, s48\n"
        "s_nop 1\n"
        "v_mfma_f32_16x16x32_bf16 v[120:123], a[120:123], a[224:227], 0\n"
        "ds_read_b128 v[162:165], v251 offset:0\n"
        "s_nop 0\n"
        "v_mfma_f32_16x16x32_bf16 v[120:123], a[124:127], a[228:231], v[120:123]\n"
        "buffer_load_dwordx4 v249, s[12:15], s36 offen offset:0 lds\n"
        "v_mfma_f32_16x16x32_bf16 v[120:123], a[128:131], a[232:235], v[120:123]\n"
        "v_cvt_pk_bf16_f32 v174, v116, v117\n"
        "v_cvt_pk_bf16_f32 v175, v118, v119\n"
        "v_mfma_f32_16x16x32_bf16 v[120:123], a[132:135], a[236:239], v[120:123]\n"
        "v_mfma_f32_16x16x32_bf16 v[120:123], a[136:139], a[240:243], v[120:123]\n"
        "v_mfma_f32_16x16x32_bf16 v[120:123], a[140:143], a[244:247], v[120:123]\n"
        "s_nop 2\n"
        "v_mfma_f32_16x16x32_bf16 v[112:115], a[20:23], a[244:247], 0\n"
        "s_nop 2\n"
        "v_mfma_f32_16x16x32_bf16 v[112:115], a[16:19], a[240:243], v[112:115]\n"
        "v_cvt_pk_bf16_f32 v190, v120, v121\n"
        "v_cvt_pk_bf16_f32 v191, v122, v123\n"
        "v_mfma_f32_16x16x32_bf16 v[112:115], a[12:15], a[236:239], v[112:115]\n"
        "v_mfma_f32_16x16x32_bf16 v[112:115], a[8:11], a[232:235], v[112:115]\n"
        "v_mfma_f32_16x16x32_bf16 v[112:115], a[4:7], a[228:231], v[112:115]\n"
        "ds_read_b128 v[182:185], v251 offset:512\n"
        "v_mfma_f32_16x16x32_bf16 v[112:115], a[0:3], a[224:227], v[112:115]\n"
        "s_nop 2\n"
        "v_mfma_f32_16x16x32_bf16 v[116:119], a[24:27], a[224:227], 0\n"
        "s_nop 2\n"
        "v_mfma_f32_16x16x32_bf16 v[116:119], a[28:31], a[228:231], v[116:119]\n"
        "v_cvt_pk_bf16_f32 v176, v112, v113\n"
        "v_cvt_pk_bf16_f32 v177, v114, v115\n"
        "v_mfma_f32_16x16x32_bf16 v[116:119], a[32:35], a[232:235], v[116:119]\n"
        "v_mfma_f32_16x16x32_bf16 v[116:119], a[36:39], a[236:239], v[116:119]\n"
        "buffer_load_dwordx4 v253, s[12:15], s36 offen offset:1024 lds\n"
        "v_mfma_f32_16x16x32_bf16 v[116:119], a[40:43], a[240:243], v[116:119]\n"
        "v_mfma_f32_16x16x32_bf16 v[116:119], a[44:47], a[244:247], v[116:119]\n"
        "s_nop 2\n"
        "v_mfma_f32_16x16x32_bf16 v[120:123], a[68:71], a[244:247], 0\n"
        "s_nop 2\n"
        "v_mfma_f32_16x16x32_bf16 v[120:123], a[64:67], a[240:243], v[120:123]\n"
        "v_cvt_pk_bf16_f32 v178, v116, v117\n"
        "v_cvt_pk_bf16_f32 v179, v118, v119\n"
        "v_mfma_f32_16x16x32_bf16 v[120:123], a[60:63], a[236:239], v[120:123]\n"
        "v_mfma_f32_16x16x32_bf16 v[120:123], a[56:59], a[232:235], v[120:123]\n"
        "v_mfma_f32_16x16x32_bf16 v[120:123], a[52:55], a[228:231], v[120:123]\n"
        "v_mfma_f32_16x16x32_bf16 v[120:123], a[48:51], a[224:227], v[120:123]\n"
        "s_nop 2\n"
        "v_mfma_f32_16x16x32_bf16 v[112:115], a[224:227], a[144:147], 0\n"
        "s_nop 2\n"
        "v_mfma_f32_16x16x32_bf16 v[112:115], a[228:231], a[148:151], v[112:115]\n"
        "v_cvt_pk_bf16_f32 v180, v120, v121\n"
        "v_cvt_pk_bf16_f32 v181, v122, v123\n"
        "v_mfma_f32_16x16x32_bf16 v[112:115], a[232:235], a[152:155], v[112:115]\n"
        "v_cvt_pk_bf16_f32 v186, v120, v121\n"
        "v_cvt_pk_bf16_f32 v187, v122, v123\n"
        "v_mfma_f32_16x16x32_bf16 v[112:115], a[236:239], a[156:159], v[112:115]\n"
        "s_add_u32 m0, s41, 0\n"
        "v_mfma_f32_16x16x32_bf16 v[112:115], a[240:243], a[160:163], v[112:115]\n"
        "ds_read_b128 a[248:251], v240 offset:12288\n"
        "v_mfma_f32_16x16x32_bf16 v[112:115], a[244:247], a[164:167], v[112:115]\n"
        "buffer_load_dwordx4 v242, s[8:11], s47 offen lds\n"
        "v_mfma_f32_16x16x32_bf16 v[96:99], v[152:155], v[156:159], v[0:3]\n"
        "s_waitcnt lgkmcnt(2)\n"
        "ds_read_b128 a[216:219], v241 offset:12288\n"
        "s_nop 0\n"
        "v_mfma_f32_16x16x32_bf16 v[96:99], v[168:171], v[160:163], v[96:99]\n"
        "ds_read_b128 a[220:223], v240 offset:12416\n"
        "s_sub_u32 s31, s30, s28\n"
        "s_nop 0\n"
        "v_mfma_f32_16x16x32_bf16 v[100:103], v[172:175], v[156:159], v[4:7]\n"
        "s_sub_u32 s45, s28, s29\n"
        "v_cvt_pk_bf16_f32 v130, v112, v113\n"
        "v_cvt_pk_bf16_f32 v131, v114, v115\n"
        "v_mfma_f32_16x16x32_bf16 v[100:103], v[188:191], v[164:167], v[100:103]\n"
        "s_add_u32 s28, s28, s31\n"
        "ds_read_b128 v[162:165], v252 offset:0\n"
        "v_mfma_f32_16x16x32_bf16 v[116:119], a[244:247], a[188:191], 0\n"
        "s_mov_b32 s30, s29\n"
        "v_exp_f32 v96, v96\n"
        "v_mfma_f32_16x16x32_bf16 v[116:119], a[240:243], a[184:187], v[116:119]\n"
        "s_add_u32 s29, s29, s45\n"
        "v_exp_f32 v97, v97\n"
        "v_mfma_f32_16x16x32_bf16 v[116:119], a[236:239], a[180:183], v[116:119]\n"
        "v_exp_f32 v98, v98\n"
        "v_mfma_f32_16x16x32_bf16 v[116:119], a[232:235], a[176:179], v[116:119]\n"
        "v_exp_f32 v100, v100\n"
        "v_mfma_f32_16x16x32_bf16 v[116:119], a[228:231], a[172:175], v[116:119]\n"
        "v_exp_f32 v99, v99\n"
        "v_mfma_f32_16x16x32_bf16 v[116:119], a[224:227], a[168:171], v[116:119]\n"
        "v_exp_f32 v101, v101\n"
        "v_mfma_f32_16x16x32_bf16 v[104:107], v[152:155], v[176:179], v[16:19]\n"
        "s_waitcnt lgkmcnt(4)\n"
        "v_exp_f32 v102, v102\n"
        "v_mfma_f32_16x16x32_bf16 v[104:107], v[168:171], v[180:183], v[104:107]\n"
        "s_add_u32 m0, s41, 1024\n"
        "v_cvt_pk_bf16_f32 v64, v96, v97\n"
        "v_cvt_pk_bf16_f32 v65, v98, v99\n"
        "v_mfma_f32_16x16x32_bf16 v[108:111], v[172:175], v[176:179], v[20:23]\n"
        "v_cvt_pk_bf16_f32 v138, v116, v117\n"
        "v_cvt_pk_bf16_f32 v139, v118, v119\n"
        "v_mfma_f32_16x16x32_bf16 v[108:111], v[188:191], v[184:187], v[108:111]\n"
        "buffer_load_dwordx4 v243, s[8:11], s47 offen lds\n"
        "v_mfma_f32_16x16x32_bf16 v[120:123], a[224:227], a[192:195], 0\n"
        "ds_read_b128 a[224:227], v241 offset:12416\n"
        "v_cvt_pk_bf16_f32 v66, v100, v101\n"
        "v_mfma_f32_16x16x32_bf16 v[120:123], a[228:231], a[196:199], v[120:123]\n"
        "ds_read_b128 a[228:231], v240 offset:12544\n"
        "v_mfma_f32_16x16x32_bf16 v[120:123], a[232:235], a[200:203], v[120:123]\n"
        "ds_read_b128 a[232:235], v241 offset:12544\n"
        "v_mfma_f32_16x16x32_bf16 v[120:123], a[236:239], a[204:207], v[120:123]\n"
        "ds_read_b128 v[202:205], v251 offset:4096\n"
        "v_mfma_f32_16x16x32_bf16 v[120:123], a[240:243], a[208:211], v[120:123]\n"
        "ds_read_b128 v[182:185], v252 offset:512\n"
        "v_mfma_f32_16x16x32_bf16 v[120:123], a[244:247], a[212:215], v[120:123]\n"
        "s_waitcnt lgkmcnt(8)\n"
        "v_exp_f32 v103, v103\n"
        "v_mfma_f32_16x16x32_bf16 v[112:115], a[0:3], a[248:251], 0\n"
        "s_waitcnt lgkmcnt(7)\n"
        "v_cvt_pk_bf16_f32 v67, v102, v103\n"
        "s_nop 0\n"
        "v_mfma_f32_16x16x32_bf16 v[112:115], a[4:7], a[216:219], v[112:115]\n"
        "s_waitcnt lgkmcnt(6)\n"
        "v_exp_f32 v104, v104\n"
        "v_mfma_f32_16x16x32_bf16 v[112:115], a[8:11], a[220:223], v[112:115]\n"
        "s_waitcnt lgkmcnt(4)\n"
        "v_exp_f32 v105, v105\n"
        "v_mfma_f32_16x16x32_bf16 v[112:115], a[12:15], a[224:227], v[112:115]\n"
        "s_waitcnt lgkmcnt(3)\n"
        "v_exp_f32 v106, v106\n"
        "v_mfma_f32_16x16x32_bf16 v[112:115], a[16:19], a[228:231], v[112:115]\n"
        "s_waitcnt lgkmcnt(2)\n"
        "v_cvt_pk_bf16_f32 v72, v104, v105\n"
        "v_cvt_pk_bf16_f32 v146, v120, v121\n"
        "v_mfma_f32_16x16x32_bf16 v[112:115], a[20:23], a[232:235], v[112:115]\n"
        "s_add_u32 m0, s41, 2048\n"
        "v_exp_f32 v108, v108\n"
        "v_mfma_f32_16x16x32_bf16 v[116:119], a[44:47], a[232:235], 0\n"
        "v_cvt_pk_bf16_f32 v147, v122, v123\n"
        "v_add_u32 v245, s45, v245\n"
        "v_mfma_f32_16x16x32_bf16 v[116:119], a[40:43], a[228:231], v[116:119]\n"
        "buffer_load_dwordx4 v244, s[8:11], s47 offen lds\n"
        "v_mfma_f32_16x16x32_bf16 v[116:119], a[36:39], a[224:227], v[116:119]\n"
        "v_exp_f32 v107, v107\n"
        "v_mfma_f32_16x16x32_bf16 v[116:119], a[32:35], a[220:223], v[116:119]\n"
        "v_exp_f32 v109, v109\n"
        "v_mfma_f32_16x16x32_bf16 v[116:119], a[28:31], a[216:219], v[116:119]\n"
        "v_cvt_pk_bf16_f32 v73, v106, v107\n"
        "v_cvt_pk_bf16_f32 v74, v108, v109\n"
        "v_mfma_f32_16x16x32_bf16 v[116:119], a[24:27], a[248:251], v[116:119]\n"
        "v_exp_f32 v110, v110\n"
        "v_mfma_f32_16x16x32_bf16 v[120:123], a[48:51], a[248:251], 0\n"
        "v_exp_f32 v111, v111\n"
        "v_mfma_f32_16x16x32_bf16 v[120:123], a[52:55], a[216:219], v[120:123]\n"
        "v_cvt_pk_bf16_f32 v75, v110, v111\n"
        "v_cvt_pk_bf16_f32 v196, v112, v113\n"
        "v_mfma_f32_16x16x32_bf16 v[120:123], a[56:59], a[220:223], v[120:123]\n"
        "v_cvt_pk_bf16_f32 v197, v114, v115\n"
        "v_cvt_pk_bf16_f32 v198, v116, v117\n"
        "v_mfma_f32_16x16x32_bf16 v[120:123], a[60:63], a[224:227], v[120:123]\n"
        "v_cvt_pk_bf16_f32 v199, v118, v119\n"
        "v_add_u32 v246, s45, v246\n"
        "v_mfma_f32_16x16x32_bf16 v[120:123], a[64:67], a[228:231], v[120:123]\n"
        "v_mfma_f32_16x16x32_bf16 v[120:123], a[68:71], a[232:235], v[120:123]\n"
        "s_nop 2\n"
        "v_mfma_f32_16x16x32_bf16 v[112:115], a[92:95], a[232:235], 0\n"
        "s_nop 2\n"
        "v_mfma_f32_16x16x32_bf16 v[112:115], a[88:91], a[228:231], v[112:115]\n"
        "s_add_u32 m0, s41, 12288\n"
        "v_cvt_pk_bf16_f32 v200, v120, v121\n"
        "v_cvt_pk_bf16_f32 v201, v122, v123\n"
        "v_mfma_f32_16x16x32_bf16 v[112:115], a[84:87], a[224:227], v[112:115]\n"
        "v_cvt_pk_bf16_f32 v206, v120, v121\n"
        "v_cvt_pk_bf16_f32 v207, v122, v123\n"
        "v_mfma_f32_16x16x32_bf16 v[112:115], a[80:83], a[220:223], v[112:115]\n"
        "buffer_load_dwordx4 v242, s[8:11], s35 offen lds\n"
        "v_mfma_f32_16x16x32_bf16 v[112:115], a[76:79], a[216:219], v[112:115]\n"
        "v_mfma_f32_16x16x32_bf16 v[112:115], a[72:75], a[248:251], v[112:115]\n"
        "s_nop 2\n"
        "v_mfma_f32_16x16x32_bf16 v[96:99], v[152:155], v[196:199], v[32:35]\n"
        "s_waitcnt lgkmcnt(1)\n"
        "s_nop 1\n"
        "v_mfma_f32_16x16x32_bf16 v[96:99], v[168:171], v[200:203], v[96:99]\n"
        "s_nop 0\n"
        "v_cvt_pk_bf16_f32 v192, v112, v113\n"
        "v_cvt_pk_bf16_f32 v193, v114, v115\n"
        "v_mfma_f32_16x16x32_bf16 v[100:103], v[172:175], v[196:199], v[36:39]\n"
        "v_mfma_f32_16x16x32_bf16 v[100:103], v[188:191], v[204:207], v[100:103]\n"
        "ds_read_b128 v[202:205], v252 offset:4096\n"
        "s_cmp_eq_u32 s37, 100663296\n"
        "v_mfma_f32_16x16x32_bf16 v[116:119], a[96:99], a[248:251], 0\n"
        "s_cselect_b32 s46, 0, s37\n"
        "v_exp_f32 v96, v96\n"
        "v_mfma_f32_16x16x32_bf16 v[116:119], a[100:103], a[216:219], v[116:119]\n"
        "s_add_u32 s38, s46, s43\n"
        "v_exp_f32 v97, v97\n"
        "v_mfma_f32_16x16x32_bf16 v[116:119], a[104:107], a[220:223], v[116:119]\n"
        "s_add_u32 s39, s46, s44\n"
        "v_exp_f32 v98, v98\n"
        "v_mfma_f32_16x16x32_bf16 v[116:119], a[108:111], a[224:227], v[116:119]\n"
        "s_add_u32 s37, s37, 6291456\n"
        "v_exp_f32 v100, v100\n"
        "v_mfma_f32_16x16x32_bf16 v[116:119], a[112:115], a[228:231], v[116:119]\n"
        "v_exp_f32 v99, v99\n"
        "v_mfma_f32_16x16x32_bf16 v[116:119], a[116:119], a[232:235], v[116:119]\n"
        "s_add_u32 m0, s41, 13312\n"
        "v_exp_f32 v101, v101\n"
        "v_mfma_f32_16x16x32_bf16 v[120:123], a[140:143], a[232:235], 0\n"
        "v_exp_f32 v102, v102\n"
        "v_mfma_f32_16x16x32_bf16 v[120:123], a[136:139], a[228:231], v[120:123]\n"
        "buffer_load_dwordx4 v243, s[8:11], s35 offen lds\n"
        "v_mfma_f32_16x16x32_bf16 v[120:123], a[132:135], a[224:227], v[120:123]\n"
        "v_cvt_pk_bf16_f32 v80, v96, v97\n"
        "v_cvt_pk_bf16_f32 v194, v116, v117\n"
        "v_mfma_f32_16x16x32_bf16 v[120:123], a[128:131], a[220:223], v[120:123]\n"
        "v_cvt_pk_bf16_f32 v195, v118, v119\n"
        "v_cvt_pk_bf16_f32 v81, v98, v99\n"
        "v_mfma_f32_16x16x32_bf16 v[120:123], a[124:127], a[216:219], v[120:123]\n"
        "v_cvt_pk_bf16_f32 v82, v100, v101\n"
        "v_mfma_f32_16x16x32_bf16 v[120:123], a[120:123], a[248:251], v[120:123]\n"
        "v_exp_f32 v103, v103\n"
        "v_mfma_f32_16x16x32_bf16 v[112:115], a[248:251], a[144:147], 0\n"
        "v_cvt_pk_bf16_f32 v83, v102, v103\n"
        "s_nop 0\n"
        "v_mfma_f32_16x16x32_bf16 v[112:115], a[216:219], a[148:151], v[112:115]\n"
        "s_nop 2\n"
        "v_mfma_f32_16x16x32_bf16 v[112:115], a[220:223], a[152:155], v[112:115]\n"
        "ds_read_b128 a[236:239], v240 offset:13824\n"
        "v_cvt_pk_bf16_f32 v208, v120, v121\n"
        "v_mfma_f32_16x16x32_bf16 v[112:115], a[224:227], a[156:159], v[112:115]\n"
        "ds_read_b128 a[240:243], v241 offset:13824\n"
        "v_cvt_pk_bf16_f32 v209, v122, v123\n"
        "v_mfma_f32_16x16x32_bf16 v[112:115], a[228:231], a[160:163], v[112:115]\n"
        "ds_read_b128 a[244:247], v240 offset:13952\n"
        "v_mfma_f32_16x16x32_bf16 v[112:115], a[232:235], a[164:167], v[112:115]\n"
        "s_nop 2\n"
        "v_mfma_f32_16x16x32_bf16 v[104:107], v[192:195], v[156:159], v[8:11]\n"
        "s_nop 1\n"
        "v_mfma_f32_16x16x32_bf16 v[104:107], v[208:211], v[160:163], v[104:107]\n"
        "s_add_u32 m0, s41, 14336\n"
        "v_cvt_pk_bf16_f32 v132, v112, v113\n"
        "v_cvt_pk_bf16_f32 v133, v114, v115\n"
        "v_mfma_f32_16x16x32_bf16 v[116:119], a[232:235], a[188:191], 0\n"
        "v_mfma_f32_16x16x32_bf16 v[116:119], a[228:231], a[184:187], v[116:119]\n"
        "buffer_load_dwordx4 v244, s[8:11], s35 offen lds\n"
        "v_mfma_f32_16x16x32_bf16 v[116:119], a[224:227], a[180:183], v[116:119]\n"
        "v_mfma_f32_16x16x32_bf16 v[116:119], a[220:223], a[176:179], v[116:119]\n"
        "v_exp_f32 v104, v104\n"
        "v_mfma_f32_16x16x32_bf16 v[116:119], a[216:219], a[172:175], v[116:119]\n"
        "v_exp_f32 v105, v105\n"
        "v_mfma_f32_16x16x32_bf16 v[116:119], a[248:251], a[168:171], v[116:119]\n"
        "v_exp_f32 v106, v106\n"
        "v_mfma_f32_16x16x32_bf16 v[108:111], v[192:195], v[176:179], v[24:27]\n"
        "s_waitcnt lgkmcnt(4)\n"
        "v_exp_f32 v107, v107\n"
        "v_mfma_f32_16x16x32_bf16 v[108:111], v[208:211], v[180:183], v[108:111]\n"
        "v_cvt_pk_bf16_f32 v68, v104, v105\n"
        "v_cvt_pk_bf16_f32 v69, v106, v107\n"
        "v_mfma_f32_16x16x32_bf16 v[120:123], a[248:251], a[192:195], 0\n"
        "ds_read_b128 a[248:251], v241 offset:13952\n"
        "v_cvt_pk_bf16_f32 v140, v116, v117\n"
        "v_mfma_f32_16x16x32_bf16 v[120:123], a[216:219], a[196:199], v[120:123]\n"
        "ds_read_b128 a[216:219], v240 offset:14080\n"
        "v_cvt_pk_bf16_f32 v141, v118, v119\n"
        "v_mfma_f32_16x16x32_bf16 v[120:123], a[220:223], a[200:203], v[120:123]\n"
        "ds_read_b128 a[220:223], v241 offset:14080\n"
        "v_mfma_f32_16x16x32_bf16 v[120:123], a[224:227], a[204:207], v[120:123]\n"
        "v_exp_f32 v108, v108\n"
        "v_mfma_f32_16x16x32_bf16 v[120:123], a[228:231], a[208:211], v[120:123]\n"
        "v_exp_f32 v109, v109\n"
        "v_mfma_f32_16x16x32_bf16 v[120:123], a[232:235], a[212:215], v[120:123]\n"
        "v_exp_f32 v110, v110\n"
        "v_mfma_f32_16x16x32_bf16 v[96:99], v[192:195], v[196:199], v[40:43]\n"
        "s_waitcnt lgkmcnt(6)\n"
        "v_exp_f32 v111, v111\n"
        "v_mfma_f32_16x16x32_bf16 v[96:99], v[208:211], v[200:203], v[96:99]\n"
        "s_waitcnt lgkmcnt(5)\n"
        "v_cvt_pk_bf16_f32 v76, v108, v109\n"
        "v_cvt_pk_bf16_f32 v77, v110, v111\n"
        "v_mfma_f32_16x16x32_bf16 v[112:115], a[72:75], a[236:239], 0\n"
        "s_waitcnt lgkmcnt(4)\n"
        "s_nop 0\n"
        "v_cvt_pk_bf16_f32 v148, v120, v121\n"
        "v_mfma_f32_16x16x32_bf16 v[112:115], a[76:79], a[240:243], v[112:115]\n"
        ".long 0xbec200ff\n"
        "45:\n"
        "s_branch 61b\n"
        "s_waitcnt lgkmcnt(3)\n"
        "v_cvt_pk_bf16_f32 v149, v122, v123\n"
        "v_mfma_f32_16x16x32_bf16 v[112:115], a[80:83], a[244:247], v[112:115]\n"
        "s_waitcnt lgkmcnt(2)\n"
        "v_exp_f32 v96, v96\n"
        "v_mfma_f32_16x16x32_bf16 v[112:115], a[84:87], a[248:251], v[112:115]\n"
        "s_waitcnt lgkmcnt(1)\n"
        "v_exp_f32 v97, v97\n"
        "v_mfma_f32_16x16x32_bf16 v[112:115], a[88:91], a[216:219], v[112:115]\n"
        "s_waitcnt lgkmcnt(0)\n"
        "v_exp_f32 v98, v98\n"
        "v_mfma_f32_16x16x32_bf16 v[112:115], a[92:95], a[220:223], v[112:115]\n"
        "v_exp_f32 v99, v99\n"
        "v_mfma_f32_16x16x32_bf16 v[116:119], a[116:119], a[220:223], 0\n"
        "v_cvt_pk_bf16_f32 v84, v96, v97\n"
        "v_cvt_pk_bf16_f32 v85, v98, v99\n"
        "v_mfma_f32_16x16x32_bf16 v[116:119], a[112:115], a[216:219], v[116:119]\n"
        "v_add_u32 v240, s31, v240\n"
        "v_add_u32 v241, s31, v241\n"
        "v_mfma_f32_16x16x32_bf16 v[116:119], a[108:111], a[248:251], v[116:119]\n"
        "v_cvt_pk_bf16_f32 v212, v112, v113\n"
        "v_cvt_pk_bf16_f32 v213, v114, v115\n"
        "v_mfma_f32_16x16x32_bf16 v[116:119], a[104:107], a[244:247], v[116:119]\n"
        "v_mfma_f32_16x16x32_bf16 v[116:119], a[100:103], a[240:243], v[116:119]\n"
        "v_mfma_f32_16x16x32_bf16 v[116:119], a[96:99], a[236:239], v[116:119]\n"
        "s_nop 2\n"
        "v_mfma_f32_16x16x32_bf16 v[120:123], a[120:123], a[236:239], 0\n"
        "s_nop 2\n"
        "v_mfma_f32_16x16x32_bf16 v[120:123], a[124:127], a[240:243], v[120:123]\n"
        "s_add_u32 s47, s47, 6291456\n"
        "v_cvt_pk_bf16_f32 v214, v116, v117\n"
        "v_cvt_pk_bf16_f32 v215, v118, v119\n"
        "v_mfma_f32_16x16x32_bf16 v[120:123], a[128:131], a[244:247], v[120:123]\n"
        "s_add_u32 s36, s36, 2097152\n"
        "v_mfma_f32_16x16x32_bf16 v[120:123], a[132:135], a[248:251], v[120:123]\n"
        "s_xor_b32 s48, s48, 8192\n"
        "v_mfma_f32_16x16x32_bf16 v[120:123], a[136:139], a[216:219], v[120:123]\n"
        "v_mfma_f32_16x16x32_bf16 v[120:123], a[140:143], a[220:223], v[120:123]\n"
        "s_nop 2\n"
        "v_mfma_f32_16x16x32_bf16 v[112:115], a[220:223], a[164:167], 0\n"
        "s_nop 2\n"
        "v_mfma_f32_16x16x32_bf16 v[112:115], a[216:219], a[160:163], v[112:115]\n"
        "v_cvt_pk_bf16_f32 v230, v120, v121\n"
        "v_cvt_pk_bf16_f32 v231, v122, v123\n"
        "v_mfma_f32_16x16x32_bf16 v[112:115], a[248:251], a[156:159], v[112:115]\n"
        "s_barrier\n"
        "v_mfma_f32_16x16x32_bf16 v[112:115], a[244:247], a[152:155], v[112:115]\n"
        "v_mfma_f32_16x16x32_bf16 v[112:115], a[240:243], a[148:151], v[112:115]\n"
        "v_mfma_f32_16x16x32_bf16 v[112:115], a[236:239], a[144:147], v[112:115]\n"
        "s_nop 2\n"
        "v_mfma_f32_16x16x32_bf16 v[100:103], v[212:215], v[156:159], v[12:15]\n"
        "s_nop 2\n"
        "v_mfma_f32_16x16x32_bf16 v[100:103], v[228:231], v[164:167], v[100:103]\n"
        "v_cvt_pk_bf16_f32 v134, v112, v113\n"
        "v_cvt_pk_bf16_f32 v135, v114, v115\n"
        "v_mfma_f32_16x16x32_bf16 v[116:119], a[236:239], a[168:171], 0\n"
        "s_sub_u32 s27, s27, 1\n"
        "v_mfma_f32_16x16x32_bf16 v[116:119], a[240:243], a[172:175], v[116:119]\n"
        "s_cmp_lg_u32 s27, 0\n"
        "v_mfma_f32_16x16x32_bf16 v[116:119], a[244:247], a[176:179], v[116:119]\n"
        "s_cbranch_scc0 19f\n"
        "v_mfma_f32_16x16x32_bf16 v[116:119], a[248:251], a[180:183], v[116:119]\n"
        "v_mfma_f32_16x16x32_bf16 v[116:119], a[216:219], a[184:187], v[116:119]\n"
        "s_nop 0\n"
        "v_exp_f32 v100, v100\n"
        "v_mfma_f32_16x16x32_bf16 v[116:119], a[220:223], a[188:191], v[116:119]\n"
        "v_exp_f32 v101, v101\n"
        "v_mfma_f32_16x16x32_bf16 v[104:107], v[212:215], v[176:179], v[28:31]\n"
        "v_exp_f32 v102, v102\n"
        "v_mfma_f32_16x16x32_bf16 v[104:107], v[228:231], v[184:187], v[104:107]\n"
        "v_exp_f32 v103, v103\n"
        "v_mfma_f32_16x16x32_bf16 v[120:123], a[220:223], a[212:215], 0\n"
        "v_cvt_pk_bf16_f32 v70, v100, v101\n"
        "v_cvt_pk_bf16_f32 v71, v102, v103\n"
        "v_mfma_f32_16x16x32_bf16 v[120:123], a[216:219], a[208:211], v[120:123]\n"
        "v_cvt_pk_bf16_f32 v142, v116, v117\n"
        "v_cvt_pk_bf16_f32 v143, v118, v119\n"
        "v_mfma_f32_16x16x32_bf16 v[120:123], a[248:251], a[204:207], v[120:123]\n"
        "v_mov_b64 v[96:97], 0\n"
        "v_mfma_f32_16x16x32_bf16 v[120:123], a[244:247], a[200:203], v[120:123]\n"
        "v_exp_f32 v104, v104\n"
        "v_mfma_f32_16x16x32_bf16 v[120:123], a[240:243], a[196:199], v[120:123]\n"
        "v_exp_f32 v105, v105\n"
        "v_mfma_f32_16x16x32_bf16 v[120:123], a[236:239], a[192:195], v[120:123]\n"
        "v_exp_f32 v106, v106\n"
        "v_mfma_f32_16x16x32_bf16 v[108:111], v[212:215], v[196:199], v[44:47]\n"
        "v_exp_f32 v107, v107\n"
        "v_mfma_f32_16x16x32_bf16 v[108:111], v[228:231], v[204:207], v[108:111]\n"
        "v_cvt_pk_bf16_f32 v78, v104, v105\n"
        "v_cvt_pk_bf16_f32 v79, v106, v107\n"
        "v_mfma_f32_16x16x32_bf16 v[112:115], a[0:3], a[236:239], 0\n"
        "s_nop 0\n"
        "v_cvt_pk_bf16_f32 v150, v120, v121\n"
        "v_cvt_pk_bf16_f32 v151, v122, v123\n"
        "v_mfma_f32_16x16x32_bf16 v[112:115], a[4:7], a[240:243], v[112:115]\n"
        "v_mfma_f32_16x16x32_bf16 v[112:115], a[8:11], a[244:247], v[112:115]\n"
        "v_exp_f32 v108, v108\n"
        "v_mfma_f32_16x16x32_bf16 v[112:115], a[12:15], a[248:251], v[112:115]\n"
        "v_exp_f32 v109, v109\n"
        "v_mfma_f32_16x16x32_bf16 v[112:115], a[16:19], a[216:219], v[112:115]\n"
        "v_exp_f32 v110, v110\n"
        "v_mfma_f32_16x16x32_bf16 v[112:115], a[20:23], a[220:223], v[112:115]\n"
        "v_exp_f32 v111, v111\n"
        "v_smfmac_f32_16x16x64_bf16 v[96:99], a[252:255], v[64:71], v237\n"
        "v_cvt_pk_bf16_f32 v86, v108, v109\n"
        "v_mfma_f32_16x16x32_bf16 v[100:103], v[132:135], v[68:71], 0\n"
        "v_mov_b64 v[124:125], 0\n"
        "v_cvt_pk_bf16_f32 v87, v110, v111\n"
        "v_mfma_f32_16x16x32_bf16 v[100:103], v[128:131], v[64:67], v[100:103]\n"
        "s_nop 0\n"
        "v_cvt_pk_bf16_f32 v216, v112, v113\n"
        "v_cvt_pk_bf16_f32 v217, v114, v115\n"
        "v_mfma_f32_16x16x32_bf16 v[104:107], v[136:139], v[64:67], 0\n"
        "ds_read_b128 v[222:225], v251 offset:4608\n"
        "v_add_f32 v97, v96, v97\n"
        "v_mfma_f32_16x16x32_bf16 v[104:107], v[140:143], v[68:71], v[104:107]\n"
        "v_rcp_f32 v96, v97\n"
        "v_mfma_f32_16x16x32_bf16 v[108:111], v[148:151], v[68:71], 0\n"
        "v_mul_f32 v100, v96, v100\n"
        "v_mul_f32 v101, v96, v101\n"
        "v_mfma_f32_16x16x32_bf16 v[108:111], v[144:147], v[64:67], v[108:111]\n"
        "v_mul_f32 v102, v96, v102\n"
        "v_mul_f32 v103, v96, v103\n"
        "v_mfma_f32_16x16x32_bf16 v[116:119], a[44:47], a[220:223], 0\n"
        "v_cvt_pk_bf16_f32 v100, v100, v101\n"
        "v_cvt_pk_bf16_f32 v101, v102, v103\n"
        "v_mfma_f32_16x16x32_bf16 v[116:119], a[40:43], a[216:219], v[116:119]\n"
        "v_mul_f32 v104, v96, v104\n"
        "v_mul_f32 v105, v96, v105\n"
        "v_mfma_f32_16x16x32_bf16 v[116:119], a[36:39], a[248:251], v[116:119]\n"
        "v_mul_f32 v106, v96, v106\n"
        "v_mul_f32 v107, v96, v107\n"
        "v_mfma_f32_16x16x32_bf16 v[116:119], a[32:35], a[244:247], v[116:119]\n"
        "v_cvt_pk_bf16_f32 v102, v104, v105\n"
        "v_cvt_pk_bf16_f32 v103, v106, v107\n"
        "v_mfma_f32_16x16x32_bf16 v[116:119], a[28:31], a[240:243], v[116:119]\n"
        "ds_write_b128 v245, v[100:103] offset:0\n"
        "v_mfma_f32_16x16x32_bf16 v[116:119], a[24:27], a[236:239], v[116:119]\n"
        "v_mul_f32 v108, v96, v108\n"
        "v_mul_f32 v109, v96, v109\n"
        "v_smfmac_f32_16x16x64_bf16 v[124:127], a[252:255], v[72:79], v237\n"
        "v_mul_f32 v110, v96, v110\n"
        "v_mfma_f32_16x16x32_bf16 v[64:67], v[132:135], v[76:79], 0\n"
        "v_mul_f32 v111, v96, v111\n"
        "v_cvt_pk_bf16_f32 v108, v108, v109\n"
        "v_mfma_f32_16x16x32_bf16 v[64:67], v[128:131], v[72:75], v[64:67]\n"
        "ds_read_b128 v[156:159], v247 offset:0\n"
        "v_cvt_pk_bf16_f32 v218, v116, v117\n"
        "v_mfma_f32_16x16x32_bf16 v[68:71], v[136:139], v[72:75], 0\n"
        "ds_read_b128 v[160:163], v247 offset:4096\n"
        "v_cvt_pk_bf16_f32 v219, v118, v119\n"
        "v_mfma_f32_16x16x32_bf16 v[68:71], v[140:143], v[76:79], v[68:71]\n"
        "ds_read_b128 v[164:167], v247 offset:4352\n"
        "v_cvt_pk_bf16_f32 v109, v110, v111\n"
        "v_mfma_f32_16x16x32_bf16 v[120:123], v[148:151], v[76:79], 0\n"
        "ds_write_b64 v246, v[108:109] offset:0\n"
        "v_mfma_f32_16x16x32_bf16 v[120:123], v[144:147], v[72:75], v[120:123]\n"
        "v_max_u32 v236, v96, v97\n"
        "v_mov_b64 v[116:117], 0\n"
        "v_mfma_f32_16x16x32_bf16 v[112:115], a[48:51], a[236:239], 0\n"
        "v_add_f32 v125, v124, v125\n"
        "v_mfma_f32_16x16x32_bf16 v[112:115], a[52:55], a[240:243], v[112:115]\n"
        "v_rcp_f32 v124, v125\n"
        "v_mfma_f32_16x16x32_bf16 v[112:115], a[56:59], a[244:247], v[112:115]\n"
        "v_mul_f32 v64, v124, v64\n"
        "v_mul_f32 v65, v124, v65\n"
        "v_mfma_f32_16x16x32_bf16 v[112:115], a[60:63], a[248:251], v[112:115]\n"
        "v_mul_f32 v66, v124, v66\n"
        "v_mul_f32 v67, v124, v67\n"
        "v_mfma_f32_16x16x32_bf16 v[112:115], a[64:67], a[216:219], v[112:115]\n"
        "v_cvt_pk_bf16_f32 v64, v64, v65\n"
        "v_cvt_pk_bf16_f32 v65, v66, v67\n"
        "v_mfma_f32_16x16x32_bf16 v[112:115], a[68:71], a[220:223], v[112:115]\n"
        "ds_read_b128 a[216:219], v248 offset:0\n"
        "v_mul_f32 v68, v124, v68\n"
        "v_smfmac_f32_16x16x64_bf16 v[116:119], a[252:255], v[80:87], v237\n"
        "ds_read_b128 a[220:223], v248 offset:3072\n"
        "v_mfma_f32_16x16x32_bf16 v[72:75], v[132:135], v[84:87], 0\n"
        "v_mul_f32 v69, v124, v69\n"
        "v_mul_f32 v70, v124, v70\n"
        "v_mfma_f32_16x16x32_bf16 v[72:75], v[128:131], v[80:83], v[72:75]\n"
        "v_cvt_pk_bf16_f32 v220, v112, v113\n"
        "v_cvt_pk_bf16_f32 v221, v114, v115\n"
        "v_mfma_f32_16x16x32_bf16 v[76:79], v[136:139], v[80:83], 0\n"
        "ds_read_b128 a[224:227], v248 offset:32\n"
        "v_cvt_pk_bf16_f32 v226, v112, v113\n"
        "v_mfma_f32_16x16x32_bf16 v[76:79], v[140:143], v[84:87], v[76:79]\n"
        "ds_read_b128 a[228:231], v248 offset:3104\n"
        "v_cvt_pk_bf16_f32 v227, v114, v115\n"
        "v_mfma_f32_16x16x32_bf16 v[96:99], v[148:151], v[84:87], 0\n"
        "v_mul_f32 v71, v124, v71\n"
        "v_cvt_pk_bf16_f32 v66, v68, v69\n"
        "v_mfma_f32_16x16x32_bf16 v[96:99], v[144:147], v[80:83], v[96:99]\n"
        "v_cvt_pk_bf16_f32 v67, v70, v71\n"
        "v_mul_f32 v120, v124, v120\n"
        "v_mfma_f32_16x16x32_bf16 v[100:103], v[152:155], v[216:219], v[48:51]\n"
        "s_waitcnt lgkmcnt(9)\n"
        "ds_read_b128 v[152:155], v247 offset:256\n"
        "v_mul_f32 v121, v124, v121\n"
        "v_mfma_f32_16x16x32_bf16 v[100:103], v[168:171], v[220:223], v[100:103]\n"
        "ds_write_b128 v245, v[64:67] offset:256\n"
        "v_mfma_f32_16x16x32_bf16 v[104:107], v[172:175], v[216:219], v[52:55]\n"
        "v_mul_f32 v122, v124, v122\n"
        "v_mul_f32 v123, v124, v123\n"
        "v_mfma_f32_16x16x32_bf16 v[104:107], v[188:191], v[224:227], v[104:107]\n"
        "s_waitcnt lgkmcnt(5)\n"
        "v_cvt_pk_bf16_f32 v120, v120, v121\n"
        "v_cvt_pk_bf16_f32 v121, v122, v123\n"
        "v_mfma_f32_16x16x32_bf16 v[176:179], v[156:159], a[216:219], 0\n"
        "s_waitcnt lgkmcnt(4)\n"
        "ds_write_b64 v246, v[120:121] offset:256\n"
        "v_mfma_f32_16x16x32_bf16 v[176:179], v[160:163], a[220:223], v[176:179]\n"
        "ds_read_b128 v[222:225], v252 offset:4608\n"
        "v_max3_u32 v236, v236, v124, v125\n"
        "v_mfma_f32_16x16x32_bf16 v[196:199], v[164:167], a[220:223], 0\n"
        "s_waitcnt lgkmcnt(3)\n"
        "ds_read_b128 a[232:235], v254 offset:0\n"
        "v_add_f32 v117, v116, v117\n"
        "v_mfma_f32_16x16x32_bf16 v[196:199], v[152:155], a[216:219], v[196:199]\n"
        "ds_read_b128 a[220:223], v254 offset:3072\n"
        "v_mfma_f32_16x16x32_bf16 v[200:203], v[152:155], a[224:227], 0\n"
        "ds_read_b128 a[216:219], v254 offset:32\n"
        "v_mfma_f32_16x16x32_bf16 v[200:203], v[164:167], a[228:231], v[200:203]\n"
        "v_rcp_f32 v116, v117\n"
        "v_mfma_f32_16x16x32_bf16 v[180:183], v[160:163], a[228:231], 0\n"
        "ds_read_b128 a[228:231], v254 offset:3104\n"
        "v_mul_f32 v72, v116, v72\n"
        "v_mfma_f32_16x16x32_bf16 v[180:183], v[156:159], a[224:227], v[180:183]\n"
        "ds_read_b128 a[236:239], v247 offset:8192\n"
        "v_mul_f32 v73, v116, v73\n"
        "v_mfma_f32_16x16x32_bf16 v[108:111], v[192:195], v[216:219], v[56:59]\n"
        "s_waitcnt lgkmcnt(5)\n"
        "ds_read_b128 a[224:227], v248 offset:16\n"
        "v_mul_f32 v74, v116, v74\n"
        "v_mfma_f32_16x16x32_bf16 v[108:111], v[208:211], v[220:223], v[108:111]\n"
        "ds_read_b128 a[240:243], v247 offset:12288\n"
        "v_mul_f32 v75, v116, v75\n"
        "v_mfma_f32_16x16x32_bf16 v[112:115], v[212:215], v[216:219], v[60:63]\n"
        "ds_read_b128 a[244:247], v247 offset:12544\n"
        "v_cvt_pk_bf16_f32 v72, v72, v73\n"
        "v_mfma_f32_16x16x32_bf16 v[112:115], v[228:231], v[224:227], v[112:115]\n"
        "s_waitcnt lgkmcnt(7)\n"
        "ds_read_b128 a[248:251], v247 offset:8448\n"
        "v_cvt_pk_bf16_f32 v73, v74, v75\n"
        "v_mfma_f32_16x16x32_bf16 v[184:187], v[156:159], a[232:235], 0\n"
        "s_waitcnt lgkmcnt(7)\n"
        "v_exp_f32 v100, v100\n"
        "v_mfma_f32_16x16x32_bf16 v[184:187], v[160:163], a[220:223], v[184:187]\n"
        "v_exp_f32 v101, v101\n"
        "v_mfma_f32_16x16x32_bf16 v[204:207], v[164:167], a[220:223], 0\n"
        "ds_read_b128 a[220:223], v248 offset:3088\n"
        "v_mul_f32 v76, v116, v76\n"
        "v_mfma_f32_16x16x32_bf16 v[204:207], v[152:155], a[232:235], v[204:207]\n"
        "s_waitcnt lgkmcnt(7)\n"
        "ds_read_b128 a[232:235], v254 offset:16\n"
        "v_mul_f32 v77, v116, v77\n"
        "v_mfma_f32_16x16x32_bf16 v[192:195], v[152:155], a[216:219], 0\n"
        "s_waitcnt lgkmcnt(7)\n"
        "v_mul_f32 v78, v116, v78\n"
        "v_mul_f32 v79, v116, v79\n"
        "v_mfma_f32_16x16x32_bf16 v[192:195], v[164:167], a[228:231], v[192:195]\n"
        "v_exp_f32 v102, v102\n"
        "v_mfma_f32_16x16x32_bf16 v[172:175], v[160:163], a[228:231], 0\n"
        "ds_read_b128 a[228:231], v254 offset:3088\n"
        "v_cvt_pk_bf16_f32 v88, v100, v101\n"
        "v_mfma_f32_16x16x32_bf16 v[172:175], v[156:159], a[216:219], v[172:175]\n"
        "s_waitcnt lgkmcnt(6)\n"
        "ds_read_b128 a[216:219], v248 offset:6144\n"
        "v_cvt_pk_bf16_f32 v74, v76, v77\n"
        "v_mfma_f32_16x16x32_bf16 v[212:215], v[156:159], a[224:227], 0\n"
        "s_waitcnt lgkmcnt(3)\n"
        "v_exp_f32 v104, v104\n"
        "v_mfma_f32_16x16x32_bf16 v[212:215], v[160:163], a[220:223], v[212:215]\n"
        "v_exp_f32 v103, v103\n"
        "v_mfma_f32_16x16x32_bf16 v[216:219], v[164:167], a[220:223], 0\n"
        "ds_read_b128 a[220:223], v248 offset:9216\n"
        "v_cvt_pk_bf16_f32 v75, v78, v79\n"
        "v_mfma_f32_16x16x32_bf16 v[216:219], v[152:155], a[224:227], v[216:219]\n"
        "s_waitcnt lgkmcnt(3)\n"
        "ds_write_b128 v245, v[72:75] offset:512\n"
        "v_mfma_f32_16x16x32_bf16 v[224:227], v[152:155], a[232:235], 0\n"
        "s_waitcnt lgkmcnt(3)\n"
        "v_exp_f32 v105, v105\n"
        "v_mfma_f32_16x16x32_bf16 v[224:227], v[164:167], a[228:231], v[224:227]\n"
        "v_cvt_pk_bf16_f32 v89, v102, v103\n"
        "v_cvt_pk_bf16_f32 v90, v104, v105\n"
        "v_mfma_f32_16x16x32_bf16 v[220:223], v[160:163], a[228:231], 0\n"
        "ds_read_b128 a[224:227], v248 offset:6176\n"
        "ds_read_b128 a[228:231], v248 offset:9248\n"
        "v_mfma_f32_16x16x32_bf16 v[220:223], v[156:159], a[232:235], v[220:223]\n"
        "s_waitcnt lgkmcnt(4)\n"
        "ds_read_b128 a[232:235], v254 offset:6144\n"
        "v_mul_f32 v96, v116, v96\n"
        "v_mfma_f32_16x16x32_bf16 v[176:179], a[236:239], a[216:219], v[176:179]\n"
        "s_waitcnt lgkmcnt(4)\n"
        "v_exp_f32 v106, v106\n"
        "v_mfma_f32_16x16x32_bf16 v[176:179], a[240:243], a[220:223], v[176:179]\n"
        "v_mul_f32 v97, v116, v97\n"
        "v_mul_f32 v98, v116, v98\n"
        "v_mfma_f32_16x16x32_bf16 v[196:199], a[244:247], a[220:223], v[196:199]\n"
        "ds_read_b128 a[220:223], v254 offset:9216\n"
        "v_mul_f32 v99, v116, v99\n"
        "v_mfma_f32_16x16x32_bf16 v[196:199], a[248:251], a[216:219], v[196:199]\n"
        "s_waitcnt lgkmcnt(3)\n"
        "ds_read_b128 a[216:219], v254 offset:6176\n"
        "v_cvt_pk_bf16_f32 v96, v96, v97\n"
        "v_mfma_f32_16x16x32_bf16 v[200:203], a[248:251], a[224:227], v[200:203]\n"
        "s_waitcnt lgkmcnt(3)\n"
        "ds_read_b128 v[156:159], v247 offset:16384\n"
        "v_cvt_pk_bf16_f32 v97, v98, v99\n"
        "v_mfma_f32_16x16x32_bf16 v[200:203], a[244:247], a[228:231], v[200:203]\n"
        "ds_write_b64 v246, v[96:97] offset:512\n"
        "v_mfma_f32_16x16x32_bf16 v[180:183], a[240:243], a[228:231], v[180:183]\n"
        "ds_read_b128 a[228:231], v254 offset:9248\n"
        "v_max3_u32 v236, v236, v116, v117\n"
        "v_mfma_f32_16x16x32_bf16 v[180:183], a[236:239], a[224:227], v[180:183]\n"
        "s_waitcnt lgkmcnt(5)\n"
        "ds_read_b128 a[224:227], v248 offset:6160\n"
        "v_mfma_f32_16x16x32_bf16 v[184:187], a[236:239], a[232:235], v[184:187]\n"
        "s_waitcnt lgkmcnt(5)\n"
        "ds_read_b128 v[160:163], v247 offset:20480\n"
        "v_mfma_f32_16x16x32_bf16 v[184:187], a[240:243], a[220:223], v[184:187]\n"
        "ds_read_b128 v[164:167], v247 offset:20736\n"
        "v_mfma_f32_16x16x32_bf16 v[204:207], a[244:247], a[220:223], v[204:207]\n"
        "ds_read_b128 a[220:223], v248 offset:9232\n"
        "v_mfma_f32_16x16x32_bf16 v[204:207], a[248:251], a[232:235], v[204:207]\n"
        "s_waitcnt lgkmcnt(7)\n"
        "ds_read_b128 a[232:235], v254 offset:6160\n"
        "v_mfma_f32_16x16x32_bf16 v[192:195], a[248:251], a[216:219], v[192:195]\n"
        "s_waitcnt lgkmcnt(5)\n"
        "ds_read_b128 v[152:155], v247 offset:16640\n"
        "v_mfma_f32_16x16x32_bf16 v[192:195], a[244:247], a[228:231], v[192:195]\n"
        "v_exp_f32 v107, v107\n"
        "v_mfma_f32_16x16x32_bf16 v[172:175], a[240:243], a[228:231], v[172:175]\n"
        "ds_read_b128 a[228:231], v254 offset:9232\n"
        "v_cvt_pk_bf16_f32 v91, v106, v107\n"
        "v_mfma_f32_16x16x32_bf16 v[172:175], a[236:239], a[216:219], v[172:175]\n"
        "s_waitcnt lgkmcnt(6)\n"
        "ds_read_b128 a[216:219], v248 offset:12288\n"
        "v_mfma_f32_16x16x32_bf16 v[212:215], a[236:239], a[224:227], v[212:215]\n"
        "s_waitcnt lgkmcnt(4)\n"
        "v_exp_f32 v108, v108\n"
        "v_mfma_f32_16x16x32_bf16 v[212:215], a[240:243], a[220:223], v[212:215]\n"
        "v_exp_f32 v109, v109\n"
        "v_mfma_f32_16x16x32_bf16 v[216:219], a[244:247], a[220:223], v[216:219]\n"
        "ds_read_b128 a[220:223], v248 offset:15360\n"
        "v_cvt_pk_bf16_f32 v92, v108, v109\n"
        "v_mfma_f32_16x16x32_bf16 v[216:219], a[248:251], a[224:227], v[216:219]\n"
        "s_waitcnt lgkmcnt(4)\n"
        "ds_read_b128 a[224:227], v248 offset:12320\n"
        "v_mfma_f32_16x16x32_bf16 v[224:227], a[248:251], a[232:235], v[224:227]\n"
        "s_waitcnt lgkmcnt(3)\n"
        "v_exp_f32 v110, v110\n"
        "v_mfma_f32_16x16x32_bf16 v[224:227], a[244:247], a[228:231], v[224:227]\n"
        "v_exp_f32 v112, v112\n"
        "v_mfma_f32_16x16x32_bf16 v[220:223], a[240:243], a[228:231], v[220:223]\n"
        "ds_read_b128 a[228:231], v248 offset:15392\n"
        "v_mfma_f32_16x16x32_bf16 v[220:223], a[236:239], a[232:235], v[220:223]\n"
        "s_waitcnt lgkmcnt(3)\n"
        "ds_read_b128 a[232:235], v254 offset:12288\n"
        "v_mfma_f32_16x16x32_bf16 v[176:179], v[156:159], a[216:219], v[176:179]\n"
        "s_waitcnt lgkmcnt(3)\n"
        "v_exp_f32 v111, v111\n"
        "v_mfma_f32_16x16x32_bf16 v[176:179], v[160:163], a[220:223], v[176:179]\n"
        "v_exp_f32 v113, v113\n"
        "v_mfma_f32_16x16x32_bf16 v[196:199], v[164:167], a[220:223], v[196:199]\n"
        "ds_read_b128 a[220:223], v254 offset:15360\n"
        "v_cvt_pk_bf16_f32 v93, v110, v111\n"
        "v_mfma_f32_16x16x32_bf16 v[196:199], v[152:155], a[216:219], v[196:199]\n"
        "s_waitcnt lgkmcnt(3)\n"
        "ds_read_b128 a[216:219], v254 offset:12320\n"
        "v_cvt_pk_bf16_f32 v94, v112, v113\n"
        "v_mfma_f32_16x16x32_bf16 v[200:203], v[152:155], a[224:227], v[200:203]\n"
        "s_waitcnt lgkmcnt(3)\n"
        "v_exp_f32 v114, v114\n"
        "v_mfma_f32_16x16x32_bf16 v[200:203], v[164:167], a[228:231], v[200:203]\n"
        "v_exp_f32 v115, v115\n"
        "v_mfma_f32_16x16x32_bf16 v[180:183], v[160:163], a[228:231], v[180:183]\n"
        "s_waitcnt vmcnt(0)\n"
        "s_barrier\n"
        "ds_read_b128 a[228:231], v254 offset:15392\n"
        "v_cvt_pk_bf16_f32 v95, v114, v115\n"
        "v_mfma_f32_16x16x32_bf16 v[180:183], v[156:159], a[224:227], v[180:183]\n"
        "s_waitcnt lgkmcnt(3)\n"
        "ds_read_b128 a[224:227], v248 offset:12304\n"
        "v_mfma_f32_16x16x32_bf16 v[184:187], v[156:159], a[232:235], v[184:187]\n"
        "s_waitcnt lgkmcnt(3)\n"
        "v_mfma_f32_16x16x32_bf16 v[184:187], v[160:163], a[220:223], v[184:187]\n"
        "v_mfma_f32_16x16x32_bf16 v[204:207], v[164:167], a[220:223], v[204:207]\n"
        "ds_read_b128 a[220:223], v248 offset:15376\n"
        "v_mfma_f32_16x16x32_bf16 v[204:207], v[152:155], a[232:235], v[204:207]\n"
        "s_waitcnt lgkmcnt(3)\n"
        "ds_read_b128 a[232:235], v254 offset:12304\n"
        "v_mfma_f32_16x16x32_bf16 v[192:195], v[152:155], a[216:219], v[192:195]\n"
        "s_waitcnt lgkmcnt(3)\n"
        "ds_read_b128 a[236:239], v240 offset:0\n"
        "v_mfma_f32_16x16x32_bf16 v[192:195], v[164:167], a[228:231], v[192:195]\n"
        "ds_read_b128 a[240:243], v241 offset:0\n"
        "v_mfma_f32_16x16x32_bf16 v[172:175], v[160:163], a[228:231], v[172:175]\n"
        "ds_read_b128 a[228:231], v254 offset:15376\n"
        "v_mfma_f32_16x16x32_bf16 v[172:175], v[156:159], a[216:219], v[172:175]\n"
        "s_waitcnt lgkmcnt(5)\n"
        "ds_read_b128 a[244:247], v240 offset:128\n"
        "v_mfma_f32_16x16x32_bf16 v[212:215], v[156:159], a[224:227], v[212:215]\n"
        "s_waitcnt lgkmcnt(5)\n"
        "ds_read_b128 a[248:251], v241 offset:128\n"
        "v_mfma_f32_16x16x32_bf16 v[212:215], v[160:163], a[220:223], v[212:215]\n"
        "ds_read_b128 a[216:219], v240 offset:256\n"
        "v_mfma_f32_16x16x32_bf16 v[216:219], v[164:167], a[220:223], v[216:219]\n"
        "ds_read_b128 a[220:223], v241 offset:256\n"
        "v_mfma_f32_16x16x32_bf16 v[216:219], v[152:155], a[224:227], v[216:219]\n"
        "s_waitcnt lgkmcnt(7)\n"
        "v_mfma_f32_16x16x32_bf16 v[224:227], v[152:155], a[232:235], v[224:227]\n"
        "s_waitcnt lgkmcnt(4)\n"
        "v_mfma_f32_16x16x32_bf16 v[224:227], v[164:167], a[228:231], v[224:227]\n"
        "v_mfma_f32_16x16x32_bf16 v[220:223], v[160:163], a[228:231], v[220:223]\n"
        "v_mfma_f32_16x16x32_bf16 v[220:223], v[156:159], a[232:235], v[220:223]\n"
        "s_branch 10b\n"
        ".p2align 12\n"
        "51:\n"
        "s_branch 61b\n"
        "s_nop 0\n"
        "s_nop 0\n"
        "s_nop 0\n"
        "12:\n"
        "10:\n"
        "v_mfma_f32_16x16x32_bf16 v[112:115], a[72:75], a[236:239], 0\n"
        "v_mov_b64 v[124:125], 0\n"
        "v_cvt_pk_bf16_f32 v232, v176, v180\n"
        "v_mfma_f32_16x16x32_bf16 v[112:115], a[76:79], a[240:243], v[112:115]\n"
        "s_waitcnt lgkmcnt(3)\n"
        "s_add_u32 s41, s30, s40\n"
        "v_cvt_pk_bf16_f32 v233, v184, v172\n"
        "v_cvt_pk_bf16_f32 v234, v177, v181\n"
        "v_mfma_f32_16x16x32_bf16 v[112:115], a[80:83], a[244:247], v[112:115]\n"
        "s_waitcnt lgkmcnt(2)\n"
        "buffer_store_dwordx2 v[232:233], v250, s[16:19], s38 offen offset:0 sc1\n"
        "v_cvt_pk_bf16_f32 v235, v185, v173\n"
        "v_mfma_f32_16x16x32_bf16 v[112:115], a[84:87], a[248:251], v[112:115]\n"
        "s_waitcnt lgkmcnt(1)\n"
        "v_cvt_pk_bf16_f32 v232, v178, v182\n"
        "v_cvt_pk_bf16_f32 v233, v186, v174\n"
        "v_mfma_f32_16x16x32_bf16 v[112:115], a[88:91], a[216:219], v[112:115]\n"
        "s_waitcnt lgkmcnt(0)\n"
        "v_mfma_f32_16x16x32_bf16 v[112:115], a[92:95], a[220:223], v[112:115]\n"
        "s_nop 2\n"
        "v_smfmac_f32_16x16x64_bf16 v[124:127], a[252:255], v[88:95], v237\n"
        "s_nop 1\n"
        "v_mfma_f32_16x16x32_bf16 v[80:83], v[132:135], v[92:95], 0\n"
        "buffer_store_dwordx2 v[234:235], v250, s[16:19], s38 offen offset:384 sc1\n"
        "v_cvt_pk_bf16_f32 v152, v112, v113\n"
        "v_mfma_f32_16x16x32_bf16 v[80:83], v[128:131], v[88:91], v[80:83]\n"
        "v_cvt_pk_bf16_f32 v153, v114, v115\n"
        "v_cvt_pk_bf16_f32 v234, v179, v183\n"
        "v_mfma_f32_16x16x32_bf16 v[84:87], v[136:139], v[88:91], 0\n"
        "v_cvt_pk_bf16_f32 v235, v187, v175\n"
        "v_add_f32 v125, v124, v125\n"
        "v_mfma_f32_16x16x32_bf16 v[84:87], v[140:143], v[92:95], v[84:87]\n"
        "v_rcp_f32 v124, v125\n"
        "v_mfma_f32_16x16x32_bf16 v[96:99], v[148:151], v[92:95], 0\n"
        "buffer_store_dwordx2 v[232:233], v250, s[16:19], s38 offen offset:768 sc1\n"
        "v_mul_f32 v80, v124, v80\n"
        "v_mfma_f32_16x16x32_bf16 v[96:99], v[144:147], v[88:91], v[96:99]\n"
        "s_add_u32 s35, s47, 786432\n"
        "v_mul_f32 v81, v124, v81\n"
        "v_mul_f32 v82, v124, v82\n"
        "v_mfma_f32_16x16x32_bf16 v[116:119], a[116:119], a[220:223], 0\n"
        "s_sub_u32 s46, s35, 100663296\n"
        "v_mul_f32 v83, v124, v83\n"
        "v_cvt_pk_bf16_f32 v80, v80, v81\n"
        "v_mfma_f32_16x16x32_bf16 v[116:119], a[112:115], a[216:219], v[116:119]\n"
        "s_cmp_lt_u32 s46, 786432\n"
        "v_cvt_pk_bf16_f32 v81, v82, v83\n"
        "v_cvt_pk_bf16_f32 v232, v196, v200\n"
        "v_mfma_f32_16x16x32_bf16 v[116:119], a[108:111], a[248:251], v[116:119]\n"
        "buffer_store_dwordx2 v[234:235], v250, s[16:19], s38 offen offset:1152 sc1\n"
        "v_cvt_pk_bf16_f32 v233, v204, v192\n"
        "v_mfma_f32_16x16x32_bf16 v[116:119], a[104:107], a[244:247], v[116:119]\n"
        "s_cselect_b32 s35, s46, s35\n"
        "v_mul_f32 v84, v124, v84\n"
        "v_mul_f32 v85, v124, v85\n"
        "v_mfma_f32_16x16x32_bf16 v[116:119], a[100:103], a[240:243], v[116:119]\n"
        "v_mul_f32 v86, v124, v86\n"
        "v_mul_f32 v87, v124, v87\n"
        "v_mfma_f32_16x16x32_bf16 v[116:119], a[96:99], a[236:239], v[116:119]\n"
        "v_cvt_pk_bf16_f32 v82, v84, v85\n"
        "v_cvt_pk_bf16_f32 v83, v86, v87\n"
        "v_mfma_f32_16x16x32_bf16 v[120:123], a[120:123], a[236:239], 0\n"
        "ds_write_b128 v245, v[80:83] offset:768\n"
        "v_mfma_f32_16x16x32_bf16 v[120:123], a[124:127], a[240:243], v[120:123]\n"
        "buffer_store_dwordx2 v[232:233], v250, s[16:19], s39 offen offset:0 sc1\n"
        "v_mul_f32 v96, v124, v96\n"
        "v_mfma_f32_16x16x32_bf16 v[120:123], a[128:131], a[244:247], v[120:123]\n"
        "v_mul_f32 v97, v124, v97\n"
        "v_mul_f32 v98, v124, v98\n"
        "v_mfma_f32_16x16x32_bf16 v[120:123], a[132:135], a[248:251], v[120:123]\n"
        "v_mul_f32 v99, v124, v99\n"
        "v_cvt_pk_bf16_f32 v234, v197, v201\n"
        "v_mfma_f32_16x16x32_bf16 v[120:123], a[136:139], a[216:219], v[120:123]\n"
        "v_cvt_pk_bf16_f32 v235, v205, v193\n"
        "v_cvt_pk_bf16_f32 v154, v116, v117\n"
        "v_mfma_f32_16x16x32_bf16 v[120:123], a[140:143], a[220:223], v[120:123]\n"
        "buffer_store_dwordx2 v[234:235], v250, s[16:19], s39 offen offset:384 sc1\n"
        "v_cvt_pk_bf16_f32 v155, v118, v119\n"
        "v_mfma_f32_16x16x32_bf16 v[112:115], a[20:23], a[220:223], 0\n"
        "v_cvt_pk_bf16_f32 v96, v96, v97\n"
        "v_cvt_pk_bf16_f32 v97, v98, v99\n"
        "v_mfma_f32_16x16x32_bf16 v[112:115], a[16:19], a[216:219], v[112:115]\n"
        "ds_write_b64 v246, v[96:97] offset:768\n"
        "v_mfma_f32_16x16x32_bf16 v[112:115], a[12:15], a[248:251], v[112:115]\n"
        "v_max3_u32 v236, v236, v124, v125\n"
        "v_cvt_pk_bf16_f32 v232, v198, v202\n"
        "v_mfma_f32_16x16x32_bf16 v[112:115], a[8:11], a[244:247], v[112:115]\n"
        "v_cvt_pk_bf16_f32 v233, v206, v194\n"
        "v_cmp_lt_u32 vcc, s24, v236\n"
        "v_mfma_f32_16x16x32_bf16 v[112:115], a[4:7], a[240:243], v[112:115]\n"
        "buffer_store_dwordx2 v[232:233], v250, s[16:19], s39 offen offset:768 sc1\n"
        "v_cvt_pk_bf16_f32 v168, v120, v121\n"
        "v_mfma_f32_16x16x32_bf16 v[112:115], a[0:3], a[236:239], v[112:115]\n"
        "s_cmp_lg_u64 vcc, 0\n"
        "s_addc_u32 s25, s25, s25\n"
        "v_cvt_pk_bf16_f32 v169, v122, v123\n"
        "v_mfma_f32_16x16x32_bf16 v[116:119], a[24:27], a[236:239], 0\n"
        "v_cvt_pk_bf16_f32 v234, v199, v203\n"
        "v_cvt_pk_bf16_f32 v235, v207, v195\n"
        "v_mfma_f32_16x16x32_bf16 v[116:119], a[28:31], a[240:243], v[116:119]\n"
        "v_cvt_pk_bf16_f32 v232, v212, v220\n"
        "v_cvt_pk_bf16_f32 v156, v112, v113\n"
        "v_mfma_f32_16x16x32_bf16 v[116:119], a[32:35], a[244:247], v[116:119]\n"
        "buffer_store_dwordx2 v[234:235], v250, s[16:19], s39 offen offset:1152 sc1\n"
        "v_cvt_pk_bf16_f32 v157, v114, v115\n"
        "v_mfma_f32_16x16x32_bf16 v[116:119], a[36:39], a[248:251], v[116:119]\n"
        "v_cvt_pk_bf16_f32 v234, v213, v221\n"
        "v_xor_b32 v251, 0x2000, v251\n"
        "v_mfma_f32_16x16x32_bf16 v[116:119], a[40:43], a[216:219], v[116:119]\n"
        "v_xor_b32 v252, 0x2000, v252\n"
        "v_mfma_f32_16x16x32_bf16 v[116:119], a[44:47], a[220:223], v[116:119]\n"
        "s_nop 2\n"
        "v_mfma_f32_16x16x32_bf16 v[120:123], a[68:71], a[220:223], 0\n"
        "s_nop 2\n"
        "v_mfma_f32_16x16x32_bf16 v[120:123], a[64:67], a[216:219], v[120:123]\n"
        "buffer_store_dword v232, v238, s[16:19], s38 offen offset:0 sc1\n"
        "v_mfma_f32_16x16x32_bf16 v[120:123], a[60:63], a[248:251], v[120:123]\n"
        "ds_read_b128 a[224:227], v240 offset:1536\n"
        "v_cvt_pk_bf16_f32 v158, v116, v117\n"
        "v_mfma_f32_16x16x32_bf16 v[120:123], a[56:59], a[244:247], v[120:123]\n"
        "ds_read_b128 a[228:231], v241 offset:1536\n"
        "v_cvt_pk_bf16_f32 v159, v118, v119\n"
        "v_mfma_f32_16x16x32_bf16 v[120:123], a[52:55], a[240:243], v[120:123]\n"
        "ds_read_b128 a[232:235], v240 offset:1664\n"
        "v_cvt_pk_bf16_f32 v232, v214, v222\n"
        "v_mfma_f32_16x16x32_bf16 v[120:123], a[48:51], a[236:239], v[120:123]\n"
        "buffer_store_dword v234, v238, s[16:19], s38 offen offset:384 sc1\n"
        "v_mfma_f32_16x16x32_bf16 v[112:115], a[236:239], a[144:147], 0\n"
        "v_add_u32 v247, s45, v247\n"
        "v_cvt_pk_bf16_f32 v234, v215, v223\n"
        "v_mfma_f32_16x16x32_bf16 v[112:115], a[240:243], a[148:151], v[112:115]\n"
        "s_nop 2\n"
        "v_mfma_f32_16x16x32_bf16 v[112:115], a[244:247], a[152:155], v[112:115]\n"
        "v_cvt_pk_bf16_f32 v160, v120, v121\n"
        "v_cvt_pk_bf16_f32 v161, v122, v123\n"
        "v_mfma_f32_16x16x32_bf16 v[112:115], a[248:251], a[156:159], v[112:115]\n"
        "v_cvt_pk_bf16_f32 v166, v120, v121\n"
        "v_cvt_pk_bf16_f32 v167, v122, v123\n"
        "v_mfma_f32_16x16x32_bf16 v[112:115], a[216:219], a[160:163], v[112:115]\n"
        "buffer_store_dword v232, v238, s[16:19], s38 offen offset:768 sc1\n"
        "v_mfma_f32_16x16x32_bf16 v[112:115], a[220:223], a[164:167], v[112:115]\n"
        "s_nop 2\n"
        "v_mfma_f32_16x16x32_bf16 v[116:119], a[220:223], a[188:191], 0\n"
        "v_cvt_pk_bf16_f32 v232, v216, v224\n"
        "s_nop 0\n"
        "v_mfma_f32_16x16x32_bf16 v[116:119], a[216:219], a[184:187], v[116:119]\n"
        "s_nop 0\n"
        "v_cvt_pk_bf16_f32 v128, v112, v113\n"
        "v_cvt_pk_bf16_f32 v129, v114, v115\n"
        "v_mfma_f32_16x16x32_bf16 v[116:119], a[248:251], a[180:183], v[116:119]\n"
        "buffer_store_dword v234, v238, s[16:19], s38 offen offset:1152 sc1\n"
        "v_mfma_f32_16x16x32_bf16 v[116:119], a[244:247], a[176:179], v[116:119]\n"
        "s_nop 0\n"
        "v_cvt_pk_bf16_f32 v234, v217, v225\n"
        "v_mfma_f32_16x16x32_bf16 v[116:119], a[240:243], a[172:175], v[116:119]\n"
        "v_mfma_f32_16x16x32_bf16 v[116:119], a[236:239], a[168:171], v[116:119]\n"
        "s_nop 2\n"
        "v_mfma_f32_16x16x32_bf16 v[120:123], a[236:239], a[192:195], 0\n"
        "ds_read_b128 a[236:239], v241 offset:1664\n"
        "s_nop 0\n"
        "v_mfma_f32_16x16x32_bf16 v[120:123], a[240:243], a[196:199], v[120:123]\n"
        "buffer_store_dword v232, v238, s[16:19], s39 offen offset:0 sc1\n"
        "v_mfma_f32_16x16x32_bf16 v[120:123], a[244:247], a[200:203], v[120:123]\n"
        "ds_read_b128 a[240:243], v240 offset:1792\n"
        "ds_read_b128 a[244:247], v241 offset:1792\n"
        "v_mfma_f32_16x16x32_bf16 v[120:123], a[248:251], a[204:207], v[120:123]\n"
        "v_cvt_pk_bf16_f32 v136, v116, v117\n"
        "v_cvt_pk_bf16_f32 v137, v118, v119\n"
        "v_mfma_f32_16x16x32_bf16 v[120:123], a[216:219], a[208:211], v[120:123]\n"
        "v_cvt_pk_bf16_f32 v232, v218, v226\n"
        "v_mfma_f32_16x16x32_bf16 v[120:123], a[220:223], a[212:215], v[120:123]\n"
        "s_waitcnt lgkmcnt(5)\n"
        "buffer_store_dword v234, v238, s[16:19], s39 offen offset:384 sc1\n"
        "v_mfma_f32_16x16x32_bf16 v[112:115], a[72:75], a[224:227], 0\n"
        "s_waitcnt lgkmcnt(4)\n"
        "s_nop 1\n"
        "v_mfma_f32_16x16x32_bf16 v[112:115], a[76:79], a[228:231], v[112:115]\n"
        "s_waitcnt lgkmcnt(3)\n"
        "v_cvt_pk_bf16_f32 v234, v219, v227\n"
        "s_nop 0\n"
        "v_mfma_f32_16x16x32_bf16 v[112:115], a[80:83], a[232:235], v[112:115]\n"
        "s_waitcnt lgkmcnt(2)\n"
        "v_cvt_pk_bf16_f32 v144, v120, v121\n"
        "v_cvt_pk_bf16_f32 v145, v122, v123\n"
        "v_mfma_f32_16x16x32_bf16 v[112:115], a[84:87], a[236:239], v[112:115]\n"
        "s_waitcnt lgkmcnt(1)\n"
        "v_mfma_f32_16x16x32_bf16 v[112:115], a[88:91], a[240:243], v[112:115]\n"
        "s_waitcnt lgkmcnt(0)\n"
        "buffer_store_dword v232, v238, s[16:19], s39 offen offset:768 sc1\n"
        "v_mfma_f32_16x16x32_bf16 v[112:115], a[92:95], a[244:247], v[112:115]\n"
        "s_nop 2\n"
        "v_mfma_f32_16x16x32_bf16 v[116:119], a[116:119], a[244:247], 0\n"
        "s_nop 2\n"
        "v_mfma_f32_16x16x32_bf16 v[116:119], a[112:115], a[240:243], v[116:119]\n"
        "v_cvt_pk_bf16_f32 v172, v112, v113\n"
        "v_cvt_pk_bf16_f32 v173, v114, v115\n"
        "v_mfma_f32_16x16x32_bf16 v[116:119], a[108:111], a[236:239], v[116:119]\n"
        "buffer_store_dword v234, v238, s[16:19], s39 offen offset:1152 sc1\n"
        "v_mfma_f32_16x16x32_bf16 v[116:119], a[104:107], a[232:235], v[116:119]\n"
        "v_mfma_f32_16x16x32_bf16 v[116:119], a[100:103], a[228:231], v[116:119]\n"
        "v_mfma_f32_16x16x32_bf16 v[116:119], a[96:99], a[224:227], v[116:119]\n"
        "s_mov_b32 m0, s48\n"
        "s_nop 1\n"
        "v_mfma_f32_16x16x32_bf16 v[120:123], a[120:123], a[224:227], 0\n"
        "ds_read_b128 v[162:165], v251 offset:0\n"
        "s_nop 0\n"
        "v_mfma_f32_16x16x32_bf16 v[120:123], a[124:127], a[228:231], v[120:123]\n"
        "buffer_load_dwordx4 v249, s[12:15], s36 offen offset:0 lds\n"
        "v_mfma_f32_16x16x32_bf16 v[120:123], a[128:131], a[232:235], v[120:123]\n"
        "v_cvt_pk_bf16_f32 v174, v116, v117\n"
        "v_cvt_pk_bf16_f32 v175, v118, v119\n"
        "v_mfma_f32_16x16x32_bf16 v[120:123], a[132:135], a[236:239], v[120:123]\n"
        "v_mfma_f32_16x16x32_bf16 v[120:123], a[136:139], a[240:243], v[120:123]\n"
        "v_mfma_f32_16x16x32_bf16 v[120:123], a[140:143], a[244:247], v[120:123]\n"
        "s_nop 2\n"
        "v_mfma_f32_16x16x32_bf16 v[112:115], a[20:23], a[244:247], 0\n"
        "s_nop 2\n"
        "v_mfma_f32_16x16x32_bf16 v[112:115], a[16:19], a[240:243], v[112:115]\n"
        "v_cvt_pk_bf16_f32 v190, v120, v121\n"
        "v_cvt_pk_bf16_f32 v191, v122, v123\n"
        "v_mfma_f32_16x16x32_bf16 v[112:115], a[12:15], a[236:239], v[112:115]\n"
        "v_mfma_f32_16x16x32_bf16 v[112:115], a[8:11], a[232:235], v[112:115]\n"
        "v_mfma_f32_16x16x32_bf16 v[112:115], a[4:7], a[228:231], v[112:115]\n"
        "ds_read_b128 v[182:185], v251 offset:512\n"
        "v_mfma_f32_16x16x32_bf16 v[112:115], a[0:3], a[224:227], v[112:115]\n"
        "s_nop 2\n"
        "v_mfma_f32_16x16x32_bf16 v[116:119], a[24:27], a[224:227], 0\n"
        "s_nop 2\n"
        "v_mfma_f32_16x16x32_bf16 v[116:119], a[28:31], a[228:231], v[116:119]\n"
        "v_cvt_pk_bf16_f32 v176, v112, v113\n"
        "v_cvt_pk_bf16_f32 v177, v114, v115\n"
        "v_mfma_f32_16x16x32_bf16 v[116:119], a[32:35], a[232:235], v[116:119]\n"
        "v_mfma_f32_16x16x32_bf16 v[116:119], a[36:39], a[236:239], v[116:119]\n"
        "buffer_load_dwordx4 v253, s[12:15], s36 offen offset:1024 lds\n"
        "v_mfma_f32_16x16x32_bf16 v[116:119], a[40:43], a[240:243], v[116:119]\n"
        "v_mfma_f32_16x16x32_bf16 v[116:119], a[44:47], a[244:247], v[116:119]\n"
        "s_nop 2\n"
        "v_mfma_f32_16x16x32_bf16 v[120:123], a[68:71], a[244:247], 0\n"
        "s_nop 2\n"
        "v_mfma_f32_16x16x32_bf16 v[120:123], a[64:67], a[240:243], v[120:123]\n"
        "v_cvt_pk_bf16_f32 v178, v116, v117\n"
        "v_cvt_pk_bf16_f32 v179, v118, v119\n"
        "v_mfma_f32_16x16x32_bf16 v[120:123], a[60:63], a[236:239], v[120:123]\n"
        "v_mfma_f32_16x16x32_bf16 v[120:123], a[56:59], a[232:235], v[120:123]\n"
        "v_mfma_f32_16x16x32_bf16 v[120:123], a[52:55], a[228:231], v[120:123]\n"
        "v_mfma_f32_16x16x32_bf16 v[120:123], a[48:51], a[224:227], v[120:123]\n"
        "s_nop 2\n"
        "v_mfma_f32_16x16x32_bf16 v[112:115], a[224:227], a[144:147], 0\n"
        "s_nop 2\n"
        "v_mfma_f32_16x16x32_bf16 v[112:115], a[228:231], a[148:151], v[112:115]\n"
        "v_cvt_pk_bf16_f32 v180, v120, v121\n"
        "v_cvt_pk_bf16_f32 v181, v122, v123\n"
        "v_mfma_f32_16x16x32_bf16 v[112:115], a[232:235], a[152:155], v[112:115]\n"
        "v_cvt_pk_bf16_f32 v186, v120, v121\n"
        "v_cvt_pk_bf16_f32 v187, v122, v123\n"
        "v_mfma_f32_16x16x32_bf16 v[112:115], a[236:239], a[156:159], v[112:115]\n"
        "s_add_u32 m0, s41, 0\n"
        "v_mfma_f32_16x16x32_bf16 v[112:115], a[240:243], a[160:163], v[112:115]\n"
        "ds_read_b128 a[248:251], v240 offset:12288\n"
        "v_mfma_f32_16x16x32_bf16 v[112:115], a[244:247], a[164:167], v[112:115]\n"
        "buffer_load_dwordx4 v242, s[8:11], s47 offen lds\n"
        "v_mfma_f32_16x16x32_bf16 v[96:99], v[152:155], v[156:159], v[0:3]\n"
        "s_waitcnt lgkmcnt(2)\n"
        "ds_read_b128 a[216:219], v241 offset:12288\n"
        "s_nop 0\n"
        "v_mfma_f32_16x16x32_bf16 v[96:99], v[168:171], v[160:163], v[96:99]\n"
        "ds_read_b128 a[220:223], v240 offset:12416\n"
        "s_sub_u32 s31, s30, s28\n"
        "s_nop 0\n"
        "v_mfma_f32_16x16x32_bf16 v[100:103], v[172:175], v[156:159], v[4:7]\n"
        "s_sub_u32 s45, s28, s29\n"
        "v_cvt_pk_bf16_f32 v130, v112, v113\n"
        "v_cvt_pk_bf16_f32 v131, v114, v115\n"
        "v_mfma_f32_16x16x32_bf16 v[100:103], v[188:191], v[164:167], v[100:103]\n"
        "s_add_u32 s28, s28, s31\n"
        "ds_read_b128 v[162:165], v252 offset:0\n"
        "v_mfma_f32_16x16x32_bf16 v[116:119], a[244:247], a[188:191], 0\n"
        "s_mov_b32 s30, s29\n"
        "v_exp_f32 v96, v96\n"
        "v_mfma_f32_16x16x32_bf16 v[116:119], a[240:243], a[184:187], v[116:119]\n"
        "s_add_u32 s29, s29, s45\n"
        "v_exp_f32 v97, v97\n"
        "v_mfma_f32_16x16x32_bf16 v[116:119], a[236:239], a[180:183], v[116:119]\n"
        "v_exp_f32 v98, v98\n"
        "v_mfma_f32_16x16x32_bf16 v[116:119], a[232:235], a[176:179], v[116:119]\n"
        "v_exp_f32 v100, v100\n"
        "v_mfma_f32_16x16x32_bf16 v[116:119], a[228:231], a[172:175], v[116:119]\n"
        "v_exp_f32 v99, v99\n"
        "v_mfma_f32_16x16x32_bf16 v[116:119], a[224:227], a[168:171], v[116:119]\n"
        "v_exp_f32 v101, v101\n"
        "v_mfma_f32_16x16x32_bf16 v[104:107], v[152:155], v[176:179], v[16:19]\n"
        "s_waitcnt lgkmcnt(4)\n"
        "v_exp_f32 v102, v102\n"
        "v_mfma_f32_16x16x32_bf16 v[104:107], v[168:171], v[180:183], v[104:107]\n"
        "s_add_u32 m0, s41, 1024\n"
        "v_cvt_pk_bf16_f32 v64, v96, v97\n"
        "v_cvt_pk_bf16_f32 v65, v98, v99\n"
        "v_mfma_f32_16x16x32_bf16 v[108:111], v[172:175], v[176:179], v[20:23]\n"
        "v_cvt_pk_bf16_f32 v138, v116, v117\n"
        "v_cvt_pk_bf16_f32 v139, v118, v119\n"
        "v_mfma_f32_16x16x32_bf16 v[108:111], v[188:191], v[184:187], v[108:111]\n"
        "buffer_load_dwordx4 v243, s[8:11], s47 offen lds\n"
        "v_mfma_f32_16x16x32_bf16 v[120:123], a[224:227], a[192:195], 0\n"
        "ds_read_b128 a[224:227], v241 offset:12416\n"
        "v_cvt_pk_bf16_f32 v66, v100, v101\n"
        "v_mfma_f32_16x16x32_bf16 v[120:123], a[228:231], a[196:199], v[120:123]\n"
        "ds_read_b128 a[228:231], v240 offset:12544\n"
        "v_mfma_f32_16x16x32_bf16 v[120:123], a[232:235], a[200:203], v[120:123]\n"
        "ds_read_b128 a[232:235], v241 offset:12544\n"
        "v_mfma_f32_16x16x32_bf16 v[120:123], a[236:239], a[204:207], v[120:123]\n"
        "ds_read_b128 v[202:205], v251 offset:4096\n"
        "v_mfma_f32_16x16x32_bf16 v[120:123], a[240:243], a[208:211], v[120:123]\n"
        "ds_read_b128 v[182:185], v252 offset:512\n"
        "v_mfma_f32_16x16x32_bf16 v[120:123], a[244:247], a[212:215], v[120:123]\n"
        "s_waitcnt lgkmcnt(8)\n"
        "v_exp_f32 v103, v103\n"
        "v_mfma_f32_16x16x32_bf16 v[112:115], a[0:3], a[248:251], 0\n"
        "s_waitcnt lgkmcnt(7)\n"
        "v_cvt_pk_bf16_f32 v67, v102, v103\n"
        "s_nop 0\n"
        "v_mfma_f32_16x16x32_bf16 v[112:115], a[4:7], a[216:219], v[112:115]\n"
        "s_waitcnt lgkmcnt(6)\n"
        "v_exp_f32 v104, v104\n"
        "v_mfma_f32_16x16x32_bf16 v[112:115], a[8:11], a[220:223], v[112:115]\n"
        "s_waitcnt lgkmcnt(4)\n"
        "v_exp_f32 v105, v105\n"
        "v_mfma_f32_16x16x32_bf16 v[112:115], a[12:15], a[224:227], v[112:115]\n"
        "s_waitcnt lgkmcnt(3)\n"
        "v_exp_f32 v106, v106\n"
        "v_mfma_f32_16x16x32_bf16 v[112:115], a[16:19], a[228:231], v[112:115]\n"
        "s_waitcnt lgkmcnt(2)\n"
        "v_cvt_pk_bf16_f32 v72, v104, v105\n"
        "v_cvt_pk_bf16_f32 v146, v120, v121\n"
        "v_mfma_f32_16x16x32_bf16 v[112:115], a[20:23], a[232:235], v[112:115]\n"
        "s_add_u32 m0, s41, 2048\n"
        "v_exp_f32 v108, v108\n"
        "v_mfma_f32_16x16x32_bf16 v[116:119], a[44:47], a[232:235], 0\n"
        "v_cvt_pk_bf16_f32 v147, v122, v123\n"
        "v_add_u32 v245, s45, v245\n"
        "v_mfma_f32_16x16x32_bf16 v[116:119], a[40:43], a[228:231], v[116:119]\n"
        "buffer_load_dwordx4 v244, s[8:11], s47 offen lds\n"
        "v_mfma_f32_16x16x32_bf16 v[116:119], a[36:39], a[224:227], v[116:119]\n"
        "v_exp_f32 v107, v107\n"
        "v_mfma_f32_16x16x32_bf16 v[116:119], a[32:35], a[220:223], v[116:119]\n"
        "v_exp_f32 v109, v109\n"
        "v_mfma_f32_16x16x32_bf16 v[116:119], a[28:31], a[216:219], v[116:119]\n"
        "v_cvt_pk_bf16_f32 v73, v106, v107\n"
        "v_cvt_pk_bf16_f32 v74, v108, v109\n"
        "v_mfma_f32_16x16x32_bf16 v[116:119], a[24:27], a[248:251], v[116:119]\n"
        "v_exp_f32 v110, v110\n"
        "v_mfma_f32_16x16x32_bf16 v[120:123], a[48:51], a[248:251], 0\n"
        "v_exp_f32 v111, v111\n"
        "v_mfma_f32_16x16x32_bf16 v[120:123], a[52:55], a[216:219], v[120:123]\n"
        "v_cvt_pk_bf16_f32 v75, v110, v111\n"
        "v_cvt_pk_bf16_f32 v196, v112, v113\n"
        "v_mfma_f32_16x16x32_bf16 v[120:123], a[56:59], a[220:223], v[120:123]\n"
        "v_cvt_pk_bf16_f32 v197, v114, v115\n"
        "v_cvt_pk_bf16_f32 v198, v116, v117\n"
        "v_mfma_f32_16x16x32_bf16 v[120:123], a[60:63], a[224:227], v[120:123]\n"
        "v_cvt_pk_bf16_f32 v199, v118, v119\n"
        "v_add_u32 v246, s45, v246\n"
        "v_mfma_f32_16x16x32_bf16 v[120:123], a[64:67], a[228:231], v[120:123]\n"
        "v_mfma_f32_16x16x32_bf16 v[120:123], a[68:71], a[232:235], v[120:123]\n"
        "s_nop 2\n"
        "v_mfma_f32_16x16x32_bf16 v[112:115], a[92:95], a[232:235], 0\n"
        "s_nop 2\n"
        "v_mfma_f32_16x16x32_bf16 v[112:115], a[88:91], a[228:231], v[112:115]\n"
        "s_add_u32 m0, s41, 12288\n"
        "v_cvt_pk_bf16_f32 v200, v120, v121\n"
        "v_cvt_pk_bf16_f32 v201, v122, v123\n"
        "v_mfma_f32_16x16x32_bf16 v[112:115], a[84:87], a[224:227], v[112:115]\n"
        "v_cvt_pk_bf16_f32 v206, v120, v121\n"
        "v_cvt_pk_bf16_f32 v207, v122, v123\n"
        "v_mfma_f32_16x16x32_bf16 v[112:115], a[80:83], a[220:223], v[112:115]\n"
        "buffer_load_dwordx4 v242, s[8:11], s35 offen lds\n"
        "v_mfma_f32_16x16x32_bf16 v[112:115], a[76:79], a[216:219], v[112:115]\n"
        "v_mfma_f32_16x16x32_bf16 v[112:115], a[72:75], a[248:251], v[112:115]\n"
        "s_nop 2\n"
        "v_mfma_f32_16x16x32_bf16 v[96:99], v[152:155], v[196:199], v[32:35]\n"
        "s_waitcnt lgkmcnt(1)\n"
        "s_nop 1\n"
        "v_mfma_f32_16x16x32_bf16 v[96:99], v[168:171], v[200:203], v[96:99]\n"
        "s_nop 0\n"
        "v_cvt_pk_bf16_f32 v192, v112, v113\n"
        "v_cvt_pk_bf16_f32 v193, v114, v115\n"
        "v_mfma_f32_16x16x32_bf16 v[100:103], v[172:175], v[196:199], v[36:39]\n"
        "v_mfma_f32_16x16x32_bf16 v[100:103], v[188:191], v[204:207], v[100:103]\n"
        "ds_read_b128 v[202:205], v252 offset:4096\n"
        "s_cmp_eq_u32 s37, 100663296\n"
        "v_mfma_f32_16x16x32_bf16 v[116:119], a[96:99], a[248:251], 0\n"
        "s_cselect_b32 s46, 0, s37\n"
        "v_exp_f32 v96, v96\n"
        "v_mfma_f32_16x16x32_bf16 v[116:119], a[100:103], a[216:219], v[116:119]\n"
        "s_add_u32 s38, s46, s43\n"
        "v_exp_f32 v97, v97\n"
        "v_mfma_f32_16x16x32_bf16 v[116:119], a[104:107], a[220:223], v[116:119]\n"
        "s_add_u32 s39, s46, s44\n"
        "v_exp_f32 v98, v98\n"
        "v_mfma_f32_16x16x32_bf16 v[116:119], a[108:111], a[224:227], v[116:119]\n"
        "s_add_u32 s37, s37, 6291456\n"
        "v_exp_f32 v100, v100\n"
        "v_mfma_f32_16x16x32_bf16 v[116:119], a[112:115], a[228:231], v[116:119]\n"
        "v_exp_f32 v99, v99\n"
        "v_mfma_f32_16x16x32_bf16 v[116:119], a[116:119], a[232:235], v[116:119]\n"
        "s_add_u32 m0, s41, 13312\n"
        "v_exp_f32 v101, v101\n"
        "v_mfma_f32_16x16x32_bf16 v[120:123], a[140:143], a[232:235], 0\n"
        "v_exp_f32 v102, v102\n"
        "v_mfma_f32_16x16x32_bf16 v[120:123], a[136:139], a[228:231], v[120:123]\n"
        "buffer_load_dwordx4 v243, s[8:11], s35 offen lds\n"
        "v_mfma_f32_16x16x32_bf16 v[120:123], a[132:135], a[224:227], v[120:123]\n"
        "v_cvt_pk_bf16_f32 v80, v96, v97\n"
        "v_cvt_pk_bf16_f32 v194, v116, v117\n"
        "v_mfma_f32_16x16x32_bf16 v[120:123], a[128:131], a[220:223], v[120:123]\n"
        "v_cvt_pk_bf16_f32 v195, v118, v119\n"
        "v_cvt_pk_bf16_f32 v81, v98, v99\n"
        "v_mfma_f32_16x16x32_bf16 v[120:123], a[124:127], a[216:219], v[120:123]\n"
        "v_cvt_pk_bf16_f32 v82, v100, v101\n"
        "v_mfma_f32_16x16x32_bf16 v[120:123], a[120:123], a[248:251], v[120:123]\n"
        "v_exp_f32 v103, v103\n"
        "v_mfma_f32_16x16x32_bf16 v[112:115], a[248:251], a[144:147], 0\n"
        "v_cvt_pk_bf16_f32 v83, v102, v103\n"
        "s_nop 0\n"
        "v_mfma_f32_16x16x32_bf16 v[112:115], a[216:219], a[148:151], v[112:115]\n"
        "s_nop 2\n"
        "v_mfma_f32_16x16x32_bf16 v[112:115], a[220:223], a[152:155], v[112:115]\n"
        "ds_read_b128 a[236:239], v240 offset:13824\n"
        "v_cvt_pk_bf16_f32 v208, v120, v121\n"
        "v_mfma_f32_16x16x32_bf16 v[112:115], a[224:227], a[156:159], v[112:115]\n"
        "ds_read_b128 a[240:243], v241 offset:13824\n"
        "v_cvt_pk_bf16_f32 v209, v122, v123\n"
        "v_mfma_f32_16x16x32_bf16 v[112:115], a[228:231], a[160:163], v[112:115]\n"
        "ds_read_b128 a[244:247], v240 offset:13952\n"
        "v_mfma_f32_16x16x32_bf16 v[112:115], a[232:235], a[164:167], v[112:115]\n"
        "s_nop 2\n"
        "v_mfma_f32_16x16x32_bf16 v[104:107], v[192:195], v[156:159], v[8:11]\n"
        "s_nop 1\n"
        "v_mfma_f32_16x16x32_bf16 v[104:107], v[208:211], v[160:163], v[104:107]\n"
        "s_add_u32 m0, s41, 14336\n"
        "v_cvt_pk_bf16_f32 v132, v112, v113\n"
        "v_cvt_pk_bf16_f32 v133, v114, v115\n"
        "v_mfma_f32_16x16x32_bf16 v[116:119], a[232:235], a[188:191], 0\n"
        "v_mfma_f32_16x16x32_bf16 v[116:119], a[228:231], a[184:187], v[116:119]\n"
        "buffer_load_dwordx4 v244, s[8:11], s35 offen lds\n"
        "v_mfma_f32_16x16x32_bf16 v[116:119], a[224:227], a[180:183], v[116:119]\n"
        "v_mfma_f32_16x16x32_bf16 v[116:119], a[220:223], a[176:179], v[116:119]\n"
        "v_exp_f32 v104, v104\n"
        "v_mfma_f32_16x16x32_bf16 v[116:119], a[216:219], a[172:175], v[116:119]\n"
        "v_exp_f32 v105, v105\n"
        "v_mfma_f32_16x16x32_bf16 v[116:119], a[248:251], a[168:171], v[116:119]\n"
        "v_exp_f32 v106, v106\n"
        "v_mfma_f32_16x16x32_bf16 v[108:111], v[192:195], v[176:179], v[24:27]\n"
        "s_waitcnt lgkmcnt(4)\n"
        "v_exp_f32 v107, v107\n"
        "v_mfma_f32_16x16x32_bf16 v[108:111], v[208:211], v[180:183], v[108:111]\n"
        "v_cvt_pk_bf16_f32 v68, v104, v105\n"
        "v_cvt_pk_bf16_f32 v69, v106, v107\n"
        "v_mfma_f32_16x16x32_bf16 v[120:123], a[248:251], a[192:195], 0\n"
        "ds_read_b128 a[248:251], v241 offset:13952\n"
        "v_cvt_pk_bf16_f32 v140, v116, v117\n"
        "v_mfma_f32_16x16x32_bf16 v[120:123], a[216:219], a[196:199], v[120:123]\n"
        "ds_read_b128 a[216:219], v240 offset:14080\n"
        "v_cvt_pk_bf16_f32 v141, v118, v119\n"
        "v_mfma_f32_16x16x32_bf16 v[120:123], a[220:223], a[200:203], v[120:123]\n"
        "ds_read_b128 a[220:223], v241 offset:14080\n"
        "v_mfma_f32_16x16x32_bf16 v[120:123], a[224:227], a[204:207], v[120:123]\n"
        "v_exp_f32 v108, v108\n"
        "v_mfma_f32_16x16x32_bf16 v[120:123], a[228:231], a[208:211], v[120:123]\n"
        "v_exp_f32 v109, v109\n"
        "v_mfma_f32_16x16x32_bf16 v[120:123], a[232:235], a[212:215], v[120:123]\n"
        "v_exp_f32 v110, v110\n"
        "v_mfma_f32_16x16x32_bf16 v[96:99], v[192:195], v[196:199], v[40:43]\n"
        "s_waitcnt lgkmcnt(6)\n"
        "v_exp_f32 v111, v111\n"
        "v_mfma_f32_16x16x32_bf16 v[96:99], v[208:211], v[200:203], v[96:99]\n"
        "s_waitcnt lgkmcnt(5)\n"
        "v_cvt_pk_bf16_f32 v76, v108, v109\n"
        "v_cvt_pk_bf16_f32 v77, v110, v111\n"
        "v_mfma_f32_16x16x32_bf16 v[112:115], a[72:75], a[236:239], 0\n"
        "s_waitcnt lgkmcnt(4)\n"
        "s_nop 0\n"
        "v_cvt_pk_bf16_f32 v148, v120, v121\n"
        "v_mfma_f32_16x16x32_bf16 v[112:115], a[76:79], a[240:243], v[112:115]\n"
        "s_waitcnt lgkmcnt(3)\n"
        "v_cvt_pk_bf16_f32 v149, v122, v123\n"
        "v_mfma_f32_16x16x32_bf16 v[112:115], a[80:83], a[244:247], v[112:115]\n"
        "s_waitcnt lgkmcnt(2)\n"
        "v_exp_f32 v96, v96\n"
        "v_mfma_f32_16x16x32_bf16 v[112:115], a[84:87], a[248:251], v[112:115]\n"
        "s_waitcnt lgkmcnt(1)\n"
        "v_exp_f32 v97, v97\n"
        "v_mfma_f32_16x16x32_bf16 v[112:115], a[88:91], a[216:219], v[112:115]\n"
        "s_waitcnt lgkmcnt(0)\n"
        "v_exp_f32 v98, v98\n"
        "v_mfma_f32_16x16x32_bf16 v[112:115], a[92:95], a[220:223], v[112:115]\n"
        "v_exp_f32 v99, v99\n"
        "v_mfma_f32_16x16x32_bf16 v[116:119], a[116:119], a[220:223], 0\n"
        "v_cvt_pk_bf16_f32 v84, v96, v97\n"
        "v_cvt_pk_bf16_f32 v85, v98, v99\n"
        "v_mfma_f32_16x16x32_bf16 v[116:119], a[112:115], a[216:219], v[116:119]\n"
        "v_add_u32 v240, s31, v240\n"
        "v_add_u32 v241, s31, v241\n"
        "v_mfma_f32_16x16x32_bf16 v[116:119], a[108:111], a[248:251], v[116:119]\n"
        "v_cvt_pk_bf16_f32 v212, v112, v113\n"
        "v_cvt_pk_bf16_f32 v213, v114, v115\n"
        "v_mfma_f32_16x16x32_bf16 v[116:119], a[104:107], a[244:247], v[116:119]\n"
        "v_mfma_f32_16x16x32_bf16 v[116:119], a[100:103], a[240:243], v[116:119]\n"
        "v_mfma_f32_16x16x32_bf16 v[116:119], a[96:99], a[236:239], v[116:119]\n"
        "s_nop 2\n"
        "v_mfma_f32_16x16x32_bf16 v[120:123], a[120:123], a[236:239], 0\n"
        "s_nop 2\n"
        "v_mfma_f32_16x16x32_bf16 v[120:123], a[124:127], a[240:243], v[120:123]\n"
        "s_add_u32 s47, s47, 6291456\n"
        "v_cvt_pk_bf16_f32 v214, v116, v117\n"
        "v_cvt_pk_bf16_f32 v215, v118, v119\n"
        "v_mfma_f32_16x16x32_bf16 v[120:123], a[128:131], a[244:247], v[120:123]\n"
        "s_add_u32 s36, s36, 2097152\n"
        "v_mfma_f32_16x16x32_bf16 v[120:123], a[132:135], a[248:251], v[120:123]\n"
        "s_xor_b32 s48, s48, 8192\n"
        "v_mfma_f32_16x16x32_bf16 v[120:123], a[136:139], a[216:219], v[120:123]\n"
        "v_mfma_f32_16x16x32_bf16 v[120:123], a[140:143], a[220:223], v[120:123]\n"
        "s_nop 2\n"
        "v_mfma_f32_16x16x32_bf16 v[112:115], a[220:223], a[164:167], 0\n"
        "s_nop 2\n"
        "v_mfma_f32_16x16x32_bf16 v[112:115], a[216:219], a[160:163], v[112:115]\n"
        "v_cvt_pk_bf16_f32 v230, v120, v121\n"
        "v_cvt_pk_bf16_f32 v231, v122, v123\n"
        "v_mfma_f32_16x16x32_bf16 v[112:115], a[248:251], a[156:159], v[112:115]\n"
        "v_mfma_f32_16x16x32_bf16 v[112:115], a[244:247], a[152:155], v[112:115]\n"
        "v_mfma_f32_16x16x32_bf16 v[112:115], a[240:243], a[148:151], v[112:115]\n"
        "v_mfma_f32_16x16x32_bf16 v[112:115], a[236:239], a[144:147], v[112:115]\n"
        "s_barrier\n"
        "s_nop 2\n"
        "v_mfma_f32_16x16x32_bf16 v[100:103], v[212:215], v[156:159], v[12:15]\n"
        "s_nop 2\n"
        "v_mfma_f32_16x16x32_bf16 v[100:103], v[228:231], v[164:167], v[100:103]\n"
        "v_cvt_pk_bf16_f32 v134, v112, v113\n"
        "v_cvt_pk_bf16_f32 v135, v114, v115\n"
        "v_mfma_f32_16x16x32_bf16 v[116:119], a[236:239], a[168:171], 0\n"
        "s_sub_u32 s27, s27, 1\n"
        "v_mfma_f32_16x16x32_bf16 v[116:119], a[240:243], a[172:175], v[116:119]\n"
        "s_cmp_lg_u32 s27, 0\n"
        "v_mfma_f32_16x16x32_bf16 v[116:119], a[244:247], a[176:179], v[116:119]\n"
        "s_cbranch_scc0 19f\n"
        "v_mfma_f32_16x16x32_bf16 v[116:119], a[248:251], a[180:183], v[116:119]\n"
        "v_mfma_f32_16x16x32_bf16 v[116:119], a[216:219], a[184:187], v[116:119]\n"
        "s_nop 0\n"
        "v_exp_f32 v100, v100\n"
        "v_mfma_f32_16x16x32_bf16 v[116:119], a[220:223], a[188:191], v[116:119]\n"
        "v_exp_f32 v101, v101\n"
        "v_mfma_f32_16x16x32_bf16 v[104:107], v[212:215], v[176:179], v[28:31]\n"
        "v_exp_f32 v102, v102\n"
        "v_mfma_f32_16x16x32_bf16 v[104:107], v[228:231], v[184:187], v[104:107]\n"
        "v_exp_f32 v103, v103\n"
        "v_mfma_f32_16x16x32_bf16 v[120:123], a[220:223], a[212:215], 0\n"
        "v_cvt_pk_bf16_f32 v70, v100, v101\n"
        "v_cvt_pk_bf16_f32 v71, v102, v103\n"
        "v_mfma_f32_16x16x32_bf16 v[120:123], a[216:219], a[208:211], v[120:123]\n"
        "v_cvt_pk_bf16_f32 v142, v116, v117\n"
        "v_cvt_pk_bf16_f32 v143, v118, v119\n"
        "v_mfma_f32_16x16x32_bf16 v[120:123], a[248:251], a[204:207], v[120:123]\n"
        "v_mov_b64 v[96:97], 0\n"
        "v_mfma_f32_16x16x32_bf16 v[120:123], a[244:247], a[200:203], v[120:123]\n"
        "v_exp_f32 v104, v104\n"
        "v_mfma_f32_16x16x32_bf16 v[120:123], a[240:243], a[196:199], v[120:123]\n"
        "v_exp_f32 v105, v105\n"
        "v_mfma_f32_16x16x32_bf16 v[120:123], a[236:239], a[192:195], v[120:123]\n"
        "v_exp_f32 v106, v106\n"
        "v_mfma_f32_16x16x32_bf16 v[108:111], v[212:215], v[196:199], v[44:47]\n"
        "v_exp_f32 v107, v107\n"
        "v_mfma_f32_16x16x32_bf16 v[108:111], v[228:231], v[204:207], v[108:111]\n"
        "v_cvt_pk_bf16_f32 v78, v104, v105\n"
        "v_cvt_pk_bf16_f32 v79, v106, v107\n"
        "v_mfma_f32_16x16x32_bf16 v[112:115], a[0:3], a[236:239], 0\n"
        "s_nop 0\n"
        "v_cvt_pk_bf16_f32 v150, v120, v121\n"
        "v_cvt_pk_bf16_f32 v151, v122, v123\n"
        "v_mfma_f32_16x16x32_bf16 v[112:115], a[4:7], a[240:243], v[112:115]\n"
        "v_mfma_f32_16x16x32_bf16 v[112:115], a[8:11], a[244:247], v[112:115]\n"
        "v_exp_f32 v108, v108\n"
        "v_mfma_f32_16x16x32_bf16 v[112:115], a[12:15], a[248:251], v[112:115]\n"
        "v_exp_f32 v109, v109\n"
        "v_mfma_f32_16x16x32_bf16 v[112:115], a[16:19], a[216:219], v[112:115]\n"
        "v_exp_f32 v110, v110\n"
        "v_mfma_f32_16x16x32_bf16 v[112:115], a[20:23], a[220:223], v[112:115]\n"
        "v_exp_f32 v111, v111\n"
        "v_smfmac_f32_16x16x64_bf16 v[96:99], a[252:255], v[64:71], v237\n"
        "v_cvt_pk_bf16_f32 v86, v108, v109\n"
        "v_mfma_f32_16x16x32_bf16 v[100:103], v[132:135], v[68:71], 0\n"
        "v_mov_b64 v[124:125], 0\n"
        "v_cvt_pk_bf16_f32 v87, v110, v111\n"
        "v_mfma_f32_16x16x32_bf16 v[100:103], v[128:131], v[64:67], v[100:103]\n"
        "s_nop 0\n"
        "v_cvt_pk_bf16_f32 v216, v112, v113\n"
        "v_cvt_pk_bf16_f32 v217, v114, v115\n"
        "v_mfma_f32_16x16x32_bf16 v[104:107], v[136:139], v[64:67], 0\n"
        ".long 0xbec200ff\n"
        "47:\n"
        "s_branch 61b\n"
        "ds_read_b128 v[222:225], v251 offset:4608\n"
        "v_add_f32 v97, v96, v97\n"
        "v_mfma_f32_16x16x32_bf16 v[104:107], v[140:143], v[68:71], v[104:107]\n"
        "v_rcp_f32 v96, v97\n"
        "v_mfma_f32_16x16x32_bf16 v[108:111], v[148:151], v[68:71], 0\n"
        "v_mul_f32 v100, v96, v100\n"
        "v_mul_f32 v101, v96, v101\n"
        "v_mfma_f32_16x16x32_bf16 v[108:111], v[144:147], v[64:67], v[108:111]\n"
        "v_mul_f32 v102, v96, v102\n"
        "v_mul_f32 v103, v96, v103\n"
        "v_mfma_f32_16x16x32_bf16 v[116:119], a[44:47], a[220:223], 0\n"
        "v_cvt_pk_bf16_f32 v100, v100, v101\n"
        "v_cvt_pk_bf16_f32 v101, v102, v103\n"
        "v_mfma_f32_16x16x32_bf16 v[116:119], a[40:43], a[216:219], v[116:119]\n"
        "v_mul_f32 v104, v96, v104\n"
        "v_mul_f32 v105, v96, v105\n"
        "v_mfma_f32_16x16x32_bf16 v[116:119], a[36:39], a[248:251], v[116:119]\n"
        "v_mul_f32 v106, v96, v106\n"
        "v_mul_f32 v107, v96, v107\n"
        "v_mfma_f32_16x16x32_bf16 v[116:119], a[32:35], a[244:247], v[116:119]\n"
        "v_cvt_pk_bf16_f32 v102, v104, v105\n"
        "v_cvt_pk_bf16_f32 v103, v106, v107\n"
        "v_mfma_f32_16x16x32_bf16 v[116:119], a[28:31], a[240:243], v[116:119]\n"
        "ds_write_b128 v245, v[100:103] offset:0\n"
        "v_mfma_f32_16x16x32_bf16 v[116:119], a[24:27], a[236:239], v[116:119]\n"
        "v_mul_f32 v108, v96, v108\n"
        "v_mul_f32 v109, v96, v109\n"
        "v_smfmac_f32_16x16x64_bf16 v[124:127], a[252:255], v[72:79], v237\n"
        "v_mul_f32 v110, v96, v110\n"
        "v_mfma_f32_16x16x32_bf16 v[64:67], v[132:135], v[76:79], 0\n"
        "v_mul_f32 v111, v96, v111\n"
        "v_cvt_pk_bf16_f32 v108, v108, v109\n"
        "v_mfma_f32_16x16x32_bf16 v[64:67], v[128:131], v[72:75], v[64:67]\n"
        "ds_read_b128 v[156:159], v247 offset:0\n"
        "v_cvt_pk_bf16_f32 v218, v116, v117\n"
        "v_mfma_f32_16x16x32_bf16 v[68:71], v[136:139], v[72:75], 0\n"
        "ds_read_b128 v[160:163], v247 offset:4096\n"
        "v_cvt_pk_bf16_f32 v219, v118, v119\n"
        "v_mfma_f32_16x16x32_bf16 v[68:71], v[140:143], v[76:79], v[68:71]\n"
        "ds_read_b128 v[164:167], v247 offset:4352\n"
        "v_cvt_pk_bf16_f32 v109, v110, v111\n"
        "v_mfma_f32_16x16x32_bf16 v[120:123], v[148:151], v[76:79], 0\n"
        "ds_write_b64 v246, v[108:109] offset:0\n"
        "v_mfma_f32_16x16x32_bf16 v[120:123], v[144:147], v[72:75], v[120:123]\n"
        "v_max_u32 v236, v96, v97\n"
        "v_mov_b64 v[116:117], 0\n"
        "v_mfma_f32_16x16x32_bf16 v[112:115], a[48:51], a[236:239], 0\n"
        "v_add_f32 v125, v124, v125\n"
        "v_mfma_f32_16x16x32_bf16 v[112:115], a[52:55], a[240:243], v[112:115]\n"
        "v_rcp_f32 v124, v125\n"
        "v_mfma_f32_16x16x32_bf16 v[112:115], a[56:59], a[244:247], v[112:115]\n"
        "v_mul_f32 v64, v124, v64\n"
        "v_mul_f32 v65, v124, v65\n"
        "v_mfma_f32_16x16x32_bf16 v[112:115], a[60:63], a[248:251], v[112:115]\n"
        "v_mul_f32 v66, v124, v66\n"
        "v_mul_f32 v67, v124, v67\n"
        "v_mfma_f32_16x16x32_bf16 v[112:115], a[64:67], a[216:219], v[112:115]\n"
        "v_cvt_pk_bf16_f32 v64, v64, v65\n"
        "v_cvt_pk_bf16_f32 v65, v66, v67\n"
        "v_mfma_f32_16x16x32_bf16 v[112:115], a[68:71], a[220:223], v[112:115]\n"
        "ds_read_b128 a[216:219], v248 offset:0\n"
        "v_mul_f32 v68, v124, v68\n"
        "v_smfmac_f32_16x16x64_bf16 v[116:119], a[252:255], v[80:87], v237\n"
        "ds_read_b128 a[220:223], v248 offset:3072\n"
        "v_mfma_f32_16x16x32_bf16 v[72:75], v[132:135], v[84:87], 0\n"
        "v_mul_f32 v69, v124, v69\n"
        "v_mul_f32 v70, v124, v70\n"
        "v_mfma_f32_16x16x32_bf16 v[72:75], v[128:131], v[80:83], v[72:75]\n"
        "v_cvt_pk_bf16_f32 v220, v112, v113\n"
        "v_cvt_pk_bf16_f32 v221, v114, v115\n"
        "v_mfma_f32_16x16x32_bf16 v[76:79], v[136:139], v[80:83], 0\n"
        "ds_read_b128 a[224:227], v248 offset:32\n"
        "v_cvt_pk_bf16_f32 v226, v112, v113\n"
        "v_mfma_f32_16x16x32_bf16 v[76:79], v[140:143], v[84:87], v[76:79]\n"
        "ds_read_b128 a[228:231], v248 offset:3104\n"
        "v_cvt_pk_bf16_f32 v227, v114, v115\n"
        "v_mfma_f32_16x16x32_bf16 v[96:99], v[148:151], v[84:87], 0\n"
        "v_mul_f32 v71, v124, v71\n"
        "v_cvt_pk_bf16_f32 v66, v68, v69\n"
        "v_mfma_f32_16x16x32_bf16 v[96:99], v[144:147], v[80:83], v[96:99]\n"
        "v_cvt_pk_bf16_f32 v67, v70, v71\n"
        "v_mul_f32 v120, v124, v120\n"
        "v_mfma_f32_16x16x32_bf16 v[100:103], v[152:155], v[216:219], v[48:51]\n"
        "s_waitcnt lgkmcnt(9)\n"
        "ds_read_b128 v[152:155], v247 offset:256\n"
        "v_mul_f32 v121, v124, v121\n"
        "v_mfma_f32_16x16x32_bf16 v[100:103], v[168:171], v[220:223], v[100:103]\n"
        "ds_write_b128 v245, v[64:67] offset:256\n"
        "v_mfma_f32_16x16x32_bf16 v[104:107], v[172:175], v[216:219], v[52:55]\n"
        "v_mul_f32 v122, v124, v122\n"
        "v_mul_f32 v123, v124, v123\n"
        "v_mfma_f32_16x16x32_bf16 v[104:107], v[188:191], v[224:227], v[104:107]\n"
        "s_waitcnt lgkmcnt(5)\n"
        "v_cvt_pk_bf16_f32 v120, v120, v121\n"
        "v_cvt_pk_bf16_f32 v121, v122, v123\n"
        "v_mfma_f32_16x16x32_bf16 v[176:179], v[156:159], a[216:219], 0\n"
        "s_waitcnt lgkmcnt(4)\n"
        "ds_write_b64 v246, v[120:121] offset:256\n"
        "v_mfma_f32_16x16x32_bf16 v[176:179], v[160:163], a[220:223], v[176:179]\n"
        "ds_read_b128 v[222:225], v252 offset:4608\n"
        "v_max3_u32 v236, v236, v124, v125\n"
        "v_mfma_f32_16x16x32_bf16 v[196:199], v[164:167], a[220:223], 0\n"
        "s_waitcnt lgkmcnt(3)\n"
        "ds_read_b128 a[232:235], v254 offset:0\n"
        "v_add_f32 v117, v116, v117\n"
        "v_mfma_f32_16x16x32_bf16 v[196:199], v[152:155], a[216:219], v[196:199]\n"
        "ds_read_b128 a[220:223], v254 offset:3072\n"
        "v_mfma_f32_16x16x32_bf16 v[200:203], v[152:155], a[224:227], 0\n"
        "ds_read_b128 a[216:219], v254 offset:32\n"
        "v_mfma_f32_16x16x32_bf16 v[200:203], v[164:167], a[228:231], v[200:203]\n"
        "v_rcp_f32 v116, v117\n"
        "v_mfma_f32_16x16x32_bf16 v[180:183], v[160:163], a[228:231], 0\n"
        "ds_read_b128 a[228:231], v254 offset:3104\n"
        "v_mul_f32 v72, v116, v72\n"
        "v_mfma_f32_16x16x32_bf16 v[180:183], v[156:159], a[224:227], v[180:183]\n"
        "ds_read_b128 a[236:239], v247 offset:8192\n"
        "v_mul_f32 v73, v116, v73\n"
        "v_mfma_f32_16x16x32_bf16 v[108:111], v[192:195], v[216:219], v[56:59]\n"
        "s_waitcnt lgkmcnt(5)\n"
        "ds_read_b128 a[224:227], v248 offset:16\n"
        "v_mul_f32 v74, v116, v74\n"
        "v_mfma_f32_16x16x32_bf16 v[108:111], v[208:211], v[220:223], v[108:111]\n"
        "ds_read_b128 a[240:243], v247 offset:12288\n"
        "v_mul_f32 v75, v116, v75\n"
        "v_mfma_f32_16x16x32_bf16 v[112:115], v[212:215], v[216:219], v[60:63]\n"
        "ds_read_b128 a[244:247], v247 offset:12544\n"
        "v_cvt_pk_bf16_f32 v72, v72, v73\n"
        "v_mfma_f32_16x16x32_bf16 v[112:115], v[228:231], v[224:227], v[112:115]\n"
        "s_waitcnt lgkmcnt(7)\n"
        "ds_read_b128 a[248:251], v247 offset:8448\n"
        "v_cvt_pk_bf16_f32 v73, v74, v75\n"
        "v_mfma_f32_16x16x32_bf16 v[184:187], v[156:159], a[232:235], 0\n"
        "s_waitcnt lgkmcnt(7)\n"
        "v_exp_f32 v100, v100\n"
        "v_mfma_f32_16x16x32_bf16 v[184:187], v[160:163], a[220:223], v[184:187]\n"
        "v_exp_f32 v101, v101\n"
        "v_mfma_f32_16x16x32_bf16 v[204:207], v[164:167], a[220:223], 0\n"
        "ds_read_b128 a[220:223], v248 offset:3088\n"
        "v_mul_f32 v76, v116, v76\n"
        "v_mfma_f32_16x16x32_bf16 v[204:207], v[152:155], a[232:235], v[204:207]\n"
        "s_waitcnt lgkmcnt(7)\n"
        "ds_read_b128 a[232:235], v254 offset:16\n"
        "v_mul_f32 v77, v116, v77\n"
        "v_mfma_f32_16x16x32_bf16 v[192:195], v[152:155], a[216:219], 0\n"
        "s_waitcnt lgkmcnt(7)\n"
        "v_mul_f32 v78, v116, v78\n"
        "v_mul_f32 v79, v116, v79\n"
        "v_mfma_f32_16x16x32_bf16 v[192:195], v[164:167], a[228:231], v[192:195]\n"
        "v_exp_f32 v102, v102\n"
        "v_mfma_f32_16x16x32_bf16 v[172:175], v[160:163], a[228:231], 0\n"
        "ds_read_b128 a[228:231], v254 offset:3088\n"
        "v_cvt_pk_bf16_f32 v88, v100, v101\n"
        "v_mfma_f32_16x16x32_bf16 v[172:175], v[156:159], a[216:219], v[172:175]\n"
        "s_waitcnt lgkmcnt(6)\n"
        "ds_read_b128 a[216:219], v248 offset:6144\n"
        "v_cvt_pk_bf16_f32 v74, v76, v77\n"
        "v_mfma_f32_16x16x32_bf16 v[212:215], v[156:159], a[224:227], 0\n"
        "s_waitcnt lgkmcnt(3)\n"
        "v_exp_f32 v104, v104\n"
        "v_mfma_f32_16x16x32_bf16 v[212:215], v[160:163], a[220:223], v[212:215]\n"
        "v_exp_f32 v103, v103\n"
        "v_mfma_f32_16x16x32_bf16 v[216:219], v[164:167], a[220:223], 0\n"
        "ds_read_b128 a[220:223], v248 offset:9216\n"
        "v_cvt_pk_bf16_f32 v75, v78, v79\n"
        "v_mfma_f32_16x16x32_bf16 v[216:219], v[152:155], a[224:227], v[216:219]\n"
        "s_waitcnt lgkmcnt(3)\n"
        "ds_write_b128 v245, v[72:75] offset:512\n"
        "v_mfma_f32_16x16x32_bf16 v[224:227], v[152:155], a[232:235], 0\n"
        "s_waitcnt lgkmcnt(3)\n"
        "v_exp_f32 v105, v105\n"
        "v_mfma_f32_16x16x32_bf16 v[224:227], v[164:167], a[228:231], v[224:227]\n"
        "v_cvt_pk_bf16_f32 v89, v102, v103\n"
        "v_cvt_pk_bf16_f32 v90, v104, v105\n"
        "v_mfma_f32_16x16x32_bf16 v[220:223], v[160:163], a[228:231], 0\n"
        "ds_read_b128 a[224:227], v248 offset:6176\n"
        "ds_read_b128 a[228:231], v248 offset:9248\n"
        "v_mfma_f32_16x16x32_bf16 v[220:223], v[156:159], a[232:235], v[220:223]\n"
        "s_waitcnt lgkmcnt(4)\n"
        "ds_read_b128 a[232:235], v254 offset:6144\n"
        "v_mul_f32 v96, v116, v96\n"
        "v_mfma_f32_16x16x32_bf16 v[176:179], a[236:239], a[216:219], v[176:179]\n"
        "s_waitcnt lgkmcnt(4)\n"
        "v_exp_f32 v106, v106\n"
        "v_mfma_f32_16x16x32_bf16 v[176:179], a[240:243], a[220:223], v[176:179]\n"
        "v_mul_f32 v97, v116, v97\n"
        "v_mul_f32 v98, v116, v98\n"
        "v_mfma_f32_16x16x32_bf16 v[196:199], a[244:247], a[220:223], v[196:199]\n"
        "ds_read_b128 a[220:223], v254 offset:9216\n"
        "v_mul_f32 v99, v116, v99\n"
        "v_mfma_f32_16x16x32_bf16 v[196:199], a[248:251], a[216:219], v[196:199]\n"
        "s_waitcnt lgkmcnt(3)\n"
        "ds_read_b128 a[216:219], v254 offset:6176\n"
        "v_cvt_pk_bf16_f32 v96, v96, v97\n"
        "v_mfma_f32_16x16x32_bf16 v[200:203], a[248:251], a[224:227], v[200:203]\n"
        "s_waitcnt lgkmcnt(3)\n"
        "ds_read_b128 v[156:159], v247 offset:16384\n"
        "v_cvt_pk_bf16_f32 v97, v98, v99\n"
        "v_mfma_f32_16x16x32_bf16 v[200:203], a[244:247], a[228:231], v[200:203]\n"
        "ds_write_b64 v246, v[96:97] offset:512\n"
        "v_mfma_f32_16x16x32_bf16 v[180:183], a[240:243], a[228:231], v[180:183]\n"
        "ds_read_b128 a[228:231], v254 offset:9248\n"
        "v_max3_u32 v236, v236, v116, v117\n"
        "v_mfma_f32_16x16x32_bf16 v[180:183], a[236:239], a[224:227], v[180:183]\n"
        "s_waitcnt lgkmcnt(5)\n"
        "ds_read_b128 a[224:227], v248 offset:6160\n"
        "v_mfma_f32_16x16x32_bf16 v[184:187], a[236:239], a[232:235], v[184:187]\n"
        "s_waitcnt lgkmcnt(5)\n"
        "ds_read_b128 v[160:163], v247 offset:20480\n"
        "v_mfma_f32_16x16x32_bf16 v[184:187], a[240:243], a[220:223], v[184:187]\n"
        "ds_read_b128 v[164:167], v247 offset:20736\n"
        "v_mfma_f32_16x16x32_bf16 v[204:207], a[244:247], a[220:223], v[204:207]\n"
        "ds_read_b128 a[220:223], v248 offset:9232\n"
        "v_mfma_f32_16x16x32_bf16 v[204:207], a[248:251], a[232:235], v[204:207]\n"
        "s_waitcnt lgkmcnt(7)\n"
        "ds_read_b128 a[232:235], v254 offset:6160\n"
        "v_mfma_f32_16x16x32_bf16 v[192:195], a[248:251], a[216:219], v[192:195]\n"
        "s_waitcnt lgkmcnt(5)\n"
        "ds_read_b128 v[152:155], v247 offset:16640\n"
        "v_mfma_f32_16x16x32_bf16 v[192:195], a[244:247], a[228:231], v[192:195]\n"
        "v_exp_f32 v107, v107\n"
        "v_mfma_f32_16x16x32_bf16 v[172:175], a[240:243], a[228:231], v[172:175]\n"
        "ds_read_b128 a[228:231], v254 offset:9232\n"
        "v_cvt_pk_bf16_f32 v91, v106, v107\n"
        "v_mfma_f32_16x16x32_bf16 v[172:175], a[236:239], a[216:219], v[172:175]\n"
        "s_waitcnt lgkmcnt(6)\n"
        "ds_read_b128 a[216:219], v248 offset:12288\n"
        "v_mfma_f32_16x16x32_bf16 v[212:215], a[236:239], a[224:227], v[212:215]\n"
        "s_waitcnt lgkmcnt(4)\n"
        "v_exp_f32 v108, v108\n"
        "v_mfma_f32_16x16x32_bf16 v[212:215], a[240:243], a[220:223], v[212:215]\n"
        "v_exp_f32 v109, v109\n"
        "v_mfma_f32_16x16x32_bf16 v[216:219], a[244:247], a[220:223], v[216:219]\n"
        "ds_read_b128 a[220:223], v248 offset:15360\n"
        "v_cvt_pk_bf16_f32 v92, v108, v109\n"
        "v_mfma_f32_16x16x32_bf16 v[216:219], a[248:251], a[224:227], v[216:219]\n"
        "s_waitcnt lgkmcnt(4)\n"
        "ds_read_b128 a[224:227], v248 offset:12320\n"
        "v_mfma_f32_16x16x32_bf16 v[224:227], a[248:251], a[232:235], v[224:227]\n"
        "s_waitcnt lgkmcnt(3)\n"
        "v_exp_f32 v110, v110\n"
        "v_mfma_f32_16x16x32_bf16 v[224:227], a[244:247], a[228:231], v[224:227]\n"
        "v_exp_f32 v112, v112\n"
        "v_mfma_f32_16x16x32_bf16 v[220:223], a[240:243], a[228:231], v[220:223]\n"
        "ds_read_b128 a[228:231], v248 offset:15392\n"
        "v_mfma_f32_16x16x32_bf16 v[220:223], a[236:239], a[232:235], v[220:223]\n"
        "s_waitcnt lgkmcnt(3)\n"
        "ds_read_b128 a[232:235], v254 offset:12288\n"
        "v_mfma_f32_16x16x32_bf16 v[176:179], v[156:159], a[216:219], v[176:179]\n"
        "s_waitcnt lgkmcnt(3)\n"
        "v_exp_f32 v111, v111\n"
        "v_mfma_f32_16x16x32_bf16 v[176:179], v[160:163], a[220:223], v[176:179]\n"
        "v_exp_f32 v113, v113\n"
        "v_mfma_f32_16x16x32_bf16 v[196:199], v[164:167], a[220:223], v[196:199]\n"
        "ds_read_b128 a[220:223], v254 offset:15360\n"
        "v_cvt_pk_bf16_f32 v93, v110, v111\n"
        "v_mfma_f32_16x16x32_bf16 v[196:199], v[152:155], a[216:219], v[196:199]\n"
        "s_waitcnt lgkmcnt(3)\n"
        "ds_read_b128 a[216:219], v254 offset:12320\n"
        "v_cvt_pk_bf16_f32 v94, v112, v113\n"
        "v_mfma_f32_16x16x32_bf16 v[200:203], v[152:155], a[224:227], v[200:203]\n"
        "s_waitcnt lgkmcnt(3)\n"
        "v_exp_f32 v114, v114\n"
        "v_mfma_f32_16x16x32_bf16 v[200:203], v[164:167], a[228:231], v[200:203]\n"
        "v_exp_f32 v115, v115\n"
        "v_mfma_f32_16x16x32_bf16 v[180:183], v[160:163], a[228:231], v[180:183]\n"
        "s_waitcnt vmcnt(0)\n"
        "ds_read_b128 a[228:231], v254 offset:15392\n"
        "v_cvt_pk_bf16_f32 v95, v114, v115\n"
        "v_mfma_f32_16x16x32_bf16 v[180:183], v[156:159], a[224:227], v[180:183]\n"
        "s_waitcnt lgkmcnt(3)\n"
        "ds_read_b128 a[224:227], v248 offset:12304\n"
        "v_mfma_f32_16x16x32_bf16 v[184:187], v[156:159], a[232:235], v[184:187]\n"
        "s_waitcnt lgkmcnt(3)\n"
        "v_mfma_f32_16x16x32_bf16 v[184:187], v[160:163], a[220:223], v[184:187]\n"
        "s_barrier\n"
        "v_mfma_f32_16x16x32_bf16 v[204:207], v[164:167], a[220:223], v[204:207]\n"
        "ds_read_b128 a[220:223], v248 offset:15376\n"
        "v_mfma_f32_16x16x32_bf16 v[204:207], v[152:155], a[232:235], v[204:207]\n"
        "s_waitcnt lgkmcnt(3)\n"
        "ds_read_b128 a[232:235], v254 offset:12304\n"
        "v_mfma_f32_16x16x32_bf16 v[192:195], v[152:155], a[216:219], v[192:195]\n"
        "s_waitcnt lgkmcnt(3)\n"
        "ds_read_b128 a[236:239], v240 offset:0\n"
        "v_mfma_f32_16x16x32_bf16 v[192:195], v[164:167], a[228:231], v[192:195]\n"
        "ds_read_b128 a[240:243], v241 offset:0\n"
        "v_mfma_f32_16x16x32_bf16 v[172:175], v[160:163], a[228:231], v[172:175]\n"
        "ds_read_b128 a[228:231], v254 offset:15376\n"
        "v_mfma_f32_16x16x32_bf16 v[172:175], v[156:159], a[216:219], v[172:175]\n"
        "s_waitcnt lgkmcnt(5)\n"
        "ds_read_b128 a[244:247], v240 offset:128\n"
        "v_mfma_f32_16x16x32_bf16 v[212:215], v[156:159], a[224:227], v[212:215]\n"
        "s_waitcnt lgkmcnt(5)\n"
        "ds_read_b128 a[248:251], v241 offset:128\n"
        "v_mfma_f32_16x16x32_bf16 v[212:215], v[160:163], a[220:223], v[212:215]\n"
        "ds_read_b128 a[216:219], v240 offset:256\n"
        "v_mfma_f32_16x16x32_bf16 v[216:219], v[164:167], a[220:223], v[216:219]\n"
        "ds_read_b128 a[220:223], v241 offset:256\n"
        "v_mfma_f32_16x16x32_bf16 v[216:219], v[152:155], a[224:227], v[216:219]\n"
        "s_waitcnt lgkmcnt(7)\n"
        "v_mfma_f32_16x16x32_bf16 v[224:227], v[152:155], a[232:235], v[224:227]\n"
        "s_waitcnt lgkmcnt(4)\n"
        "v_mfma_f32_16x16x32_bf16 v[224:227], v[164:167], a[228:231], v[224:227]\n"
        "v_mfma_f32_16x16x32_bf16 v[220:223], v[160:163], a[228:231], v[220:223]\n"
        "v_mfma_f32_16x16x32_bf16 v[220:223], v[156:159], a[232:235], v[220:223]\n"
        "s_branch 10b\n"
        ".p2align 6\n"
        "s_nop 0\n"
        "19:\n"
        "v_mfma_f32_16x16x32_bf16 v[116:119], a[248:251], a[180:183], v[116:119]\n"
        "ds_read_b128 a[224:227], v247 offset:0\n"
        "ds_read_b128 a[120:123], v254 offset:0\n"
        "s_and_b32 s46, s42, 1\n"
        "v_mfma_f32_16x16x32_bf16 v[116:119], a[216:219], a[184:187], v[116:119]\n"
        "ds_read_b128 a[228:231], v247 offset:4096\n"
        "ds_read_b128 a[124:127], v254 offset:3072\n"
        "v_mfma_f32_16x16x32_bf16 v[116:119], a[220:223], a[188:191], v[116:119]\n"
        "ds_read_b128 a[232:235], v247 offset:8192\n"
        "s_lshl_b32 s31, s46, 8\n"
        "v_mov_b32 v241, 0\n"
        "v_mfma_f32_16x16x32_bf16 v[104:107], v[212:215], v[176:179], v[28:31]\n"
        "ds_read_b128 a[128:131], v254 offset:6144\n"
        "s_nop 0\n"
        "v_mfma_f32_16x16x32_bf16 v[104:107], v[228:231], v[184:187], v[104:107]\n"
        "ds_read_b128 v[156:159], v247 offset:12288\n"
        "s_sub_u32 s31, s45, s31\n"
        "v_cvt_pk_bf16_f32 v142, v116, v117\n"
        "v_mfma_f32_16x16x32_bf16 v[120:123], a[220:223], a[212:215], 0\n"
        "ds_read_b128 a[132:135], v254 offset:9216\n"
        "v_cvt_pk_bf16_f32 v143, v118, v119\n"
        "v_mfma_f32_16x16x32_bf16 v[120:123], a[216:219], a[208:211], v[120:123]\n"
        "ds_read_b128 v[160:163], v247 offset:16384\n"
        "ds_read_b128 a[136:139], v254 offset:12288\n"
        "s_mul_i32 s45, s46, 786432\n"
        "v_mfma_f32_16x16x32_bf16 v[120:123], a[248:251], a[204:207], v[120:123]\n"
        "ds_read_b128 v[164:167], v247 offset:20480\n"
        "ds_read_b128 a[140:143], v254 offset:15360\n"
        "v_mfma_f32_16x16x32_bf16 v[120:123], a[244:247], a[200:203], v[120:123]\n"
        "ds_read_b128 a[92:95], v248 offset:15360\n"
        "s_sub_u32 s37, s37, s45\n"
        "v_mfma_f32_16x16x32_bf16 v[120:123], a[240:243], a[196:199], v[120:123]\n"
        "ds_read_b128 a[88:91], v248 offset:12288\n"
        "v_mfma_f32_16x16x32_bf16 v[120:123], a[236:239], a[192:195], v[120:123]\n"
        "ds_read_b128 a[84:87], v248 offset:9216\n"
        "s_cmp_eq_u32 s46, 0\n"
        "s_cselect_b32 s43, s43, s44\n"
        "v_mfma_f32_16x16x32_bf16 v[108:111], v[212:215], v[196:199], v[44:47]\n"
        "ds_read_b128 a[80:83], v248 offset:6144\n"
        "s_nop 0\n"
        "v_mfma_f32_16x16x32_bf16 v[108:111], v[228:231], v[204:207], v[108:111]\n"
        "s_waitcnt lgkmcnt(14)\n"
        "ds_read_b128 a[76:79], v248 offset:3072\n"
        "v_cvt_pk_bf16_f32 v150, v120, v121\n"
        "v_mfma_f32_16x16x32_bf16 v[8:11], a[224:227], a[120:123], 0\n"
        "s_waitcnt lgkmcnt(13)\n"
        "ds_read_b128 a[72:75], v248 offset:0\n"
        "v_cvt_pk_bf16_f32 v151, v122, v123\n"
        "v_mfma_f32_16x16x32_bf16 v[8:11], a[228:231], a[124:127], v[8:11]\n"
        "s_waitcnt lgkmcnt(12)\n"
        "v_exp_f32 v100, v100\n"
        "v_mfma_f32_16x16x32_bf16 v[8:11], a[232:235], a[128:131], v[8:11]\n"
        "s_waitcnt lgkmcnt(10)\n"
        "v_exp_f32 v101, v101\n"
        "v_mfma_f32_16x16x32_bf16 v[8:11], v[156:159], a[132:135], v[8:11]\n"
        "s_waitcnt lgkmcnt(8)\n"
        "ds_read_b128 a[96:99], v248 offset:32\n"
        "v_cvt_pk_bf16_f32 v70, v100, v101\n"
        "v_mfma_f32_16x16x32_bf16 v[8:11], v[160:163], a[136:139], v[8:11]\n"
        "s_waitcnt lgkmcnt(7)\n"
        "ds_read_b128 a[100:103], v248 offset:3104\n"
        "v_mfma_f32_16x16x32_bf16 v[8:11], v[164:167], a[140:143], v[8:11]\n"
        "s_waitcnt lgkmcnt(7)\n"
        "ds_read_b128 a[104:107], v248 offset:6176\n"
        "v_mfma_f32_16x16x32_bf16 v[0:3], v[164:167], a[92:95], 0\n"
        "s_waitcnt lgkmcnt(7)\n"
        "ds_read_b128 a[108:111], v248 offset:9248\n"
        "v_mfma_f32_16x16x32_bf16 v[0:3], v[160:163], a[88:91], v[0:3]\n"
        "s_waitcnt lgkmcnt(7)\n"
        "ds_read_b128 a[112:115], v248 offset:12320\n"
        "v_mfma_f32_16x16x32_bf16 v[0:3], v[156:159], a[84:87], v[0:3]\n"
        "s_waitcnt lgkmcnt(7)\n"
        "ds_read_b128 a[116:119], v248 offset:15392\n"
        "v_mfma_f32_16x16x32_bf16 v[0:3], a[232:235], a[80:83], v[0:3]\n"
        "s_waitcnt lgkmcnt(7)\n"
        "v_exp_f32 v102, v102\n"
        "v_mfma_f32_16x16x32_bf16 v[0:3], a[228:231], a[76:79], v[0:3]\n"
        "s_waitcnt lgkmcnt(6)\n"
        "v_exp_f32 v103, v103\n"
        "v_mfma_f32_16x16x32_bf16 v[0:3], a[224:227], a[72:75], v[0:3]\n"
        "s_add_u32 s28, s37, 786432\n"
        "v_exp_f32 v104, v104\n"
        "v_mfma_f32_16x16x32_bf16 v[116:119], a[0:3], a[236:239], 0\n"
        "s_cmp_eq_u32 s28, 100663296\n"
        "s_cselect_b32 s28, 0, s28\n"
        "v_mov_b64 v[96:97], 0\n"
        "v_mfma_f32_16x16x32_bf16 v[116:119], a[4:7], a[240:243], v[116:119]\n"
        "s_lshl_b32 s29, s42, 2\n"
        "v_exp_f32 v105, v105\n"
        "v_mfma_f32_16x16x32_bf16 v[116:119], a[8:11], a[244:247], v[116:119]\n"
        "s_add_u32 s47, s37, s43\n"
        "v_exp_f32 v106, v106\n"
        "v_mfma_f32_16x16x32_bf16 v[116:119], a[12:15], a[248:251], v[116:119]\n"
        "s_add_u32 s35, s28, s43\n"
        "v_exp_f32 v107, v107\n"
        "v_mfma_f32_16x16x32_bf16 v[116:119], a[16:19], a[216:219], v[116:119]\n"
        "ds_read_b128 a[164:167], v254 offset:15392\n"
        "v_cvt_pk_bf16_f32 v71, v102, v103\n"
        "v_mfma_f32_16x16x32_bf16 v[116:119], a[20:23], a[220:223], v[116:119]\n"
        "s_waitcnt lgkmcnt(6)\n"
        "ds_read_b128 a[160:163], v254 offset:12320\n"
        "v_cvt_pk_bf16_f32 v78, v104, v105\n"
        "v_mfma_f32_16x16x32_bf16 v[4:7], a[224:227], a[96:99], 0\n"
        "s_waitcnt lgkmcnt(6)\n"
        "ds_read_b128 a[156:159], v254 offset:9248\n"
        "v_cvt_pk_bf16_f32 v79, v106, v107\n"
        "v_mfma_f32_16x16x32_bf16 v[4:7], a[228:231], a[100:103], v[4:7]\n"
        "s_waitcnt lgkmcnt(6)\n"
        "ds_read_b128 a[152:155], v254 offset:6176\n"
        "v_add_u32 v240, s31, v247\n"
        "v_mfma_f32_16x16x32_bf16 v[4:7], a[232:235], a[104:107], v[4:7]\n"
        "s_waitcnt lgkmcnt(6)\n"
        "ds_read_b128 a[148:151], v254 offset:3104\n"
        "v_cvt_pk_bf16_f32 v216, v116, v117\n"
        "v_mfma_f32_16x16x32_bf16 v[4:7], v[156:159], a[108:111], v[4:7]\n"
        "s_waitcnt lgkmcnt(6)\n"
        "ds_read_b128 a[144:147], v254 offset:32\n"
        "v_cvt_pk_bf16_f32 v217, v118, v119\n"
        "v_mfma_f32_16x16x32_bf16 v[4:7], v[160:163], a[112:115], v[4:7]\n"
        "s_waitcnt lgkmcnt(6)\n"
        "v_exp_f32 v108, v108\n"
        "v_mfma_f32_16x16x32_bf16 v[4:7], v[164:167], a[116:119], v[4:7]\n"
        "v_exp_f32 v109, v109\n"
        "v_smfmac_f32_16x16x64_bf16 v[96:99], a[252:255], v[64:71], v237\n"
        "v_cvt_pk_bf16_f32 v86, v108, v109\n"
        "v_mfma_f32_16x16x32_bf16 v[100:103], v[132:135], v[68:71], 0\n"
        "v_mov_b64 v[124:125], 0\n"
        "v_mov_b32 v249, s29\n"
        "v_mfma_f32_16x16x32_bf16 v[100:103], v[128:131], v[64:67], v[100:103]\n"
        "v_exp_f32 v110, v110\n"
        "v_mfma_f32_16x16x32_bf16 v[104:107], v[136:139], v[64:67], 0\n"
        "v_exp_f32 v111, v111\n"
        "v_mfma_f32_16x16x32_bf16 v[104:107], v[140:143], v[68:71], v[104:107]\n"
        "v_cvt_pk_bf16_f32 v87, v110, v111\n"
        "v_add_f32 v97, v96, v97\n"
        "v_mfma_f32_16x16x32_bf16 v[108:111], v[148:151], v[68:71], 0\n"
        "v_rcp_f32 v96, v97\n"
        "v_mfma_f32_16x16x32_bf16 v[108:111], v[144:147], v[64:67], v[108:111]\n"
        "s_waitcnt lgkmcnt(5)\n"
        "v_cvt_pk_bf16_f32 v232, v0, v4\n"
        "v_mul_f32 v100, v96, v100\n"
        "v_mfma_f32_16x16x32_bf16 v[12:15], v[164:167], a[164:167], 0\n"
        "s_waitcnt lgkmcnt(4)\n"
        "v_mul_f32 v101, v96, v101\n"
        "v_mul_f32 v102, v96, v102\n"
        "v_mfma_f32_16x16x32_bf16 v[12:15], v[160:163], a[160:163], v[12:15]\n"
        "s_waitcnt lgkmcnt(3)\n"
        "ds_read_b128 v[176:179], v247 offset:256\n"
        "v_cvt_pk_bf16_f32 v234, v1, v5\n"
        "v_mfma_f32_16x16x32_bf16 v[12:15], v[156:159], a[156:159], v[12:15]\n"
        "s_waitcnt lgkmcnt(3)\n"
        "ds_read_b128 v[180:183], v247 offset:4352\n"
        "v_mul_f32 v103, v96, v103\n"
        "v_mfma_f32_16x16x32_bf16 v[12:15], a[232:235], a[152:155], v[12:15]\n"
        "s_waitcnt lgkmcnt(3)\n"
        "ds_read_b128 v[184:187], v247 offset:8448\n"
        "v_cvt_pk_bf16_f32 v100, v100, v101\n"
        "v_mfma_f32_16x16x32_bf16 v[12:15], a[228:231], a[148:151], v[12:15]\n"
        "s_waitcnt lgkmcnt(3)\n"
        "ds_read_b128 a[168:171], v248 offset:16\n"
        "v_cvt_pk_bf16_f32 v242, v2, v6\n"
        "v_mfma_f32_16x16x32_bf16 v[12:15], a[224:227], a[144:147], v[12:15]\n"
        "ds_read_b128 a[172:175], v248 offset:3088\n"
        "v_cvt_pk_bf16_f32 v101, v102, v103\n"
        "v_smfmac_f32_16x16x64_bf16 v[124:127], a[252:255], v[72:79], v237\n"
        "ds_read_b128 a[176:179], v248 offset:6160\n"
        "v_mfma_f32_16x16x32_bf16 v[64:67], v[132:135], v[76:79], 0\n"
        "ds_read_b128 a[180:183], v248 offset:9232\n"
        "v_mul_f32 v104, v96, v104\n"
        "v_mfma_f32_16x16x32_bf16 v[64:67], v[128:131], v[72:75], v[64:67]\n"
        "ds_read_b128 a[184:187], v248 offset:12304\n"
        "v_cvt_pk_bf16_f32 v233, v8, v12\n"
        "v_mfma_f32_16x16x32_bf16 v[68:71], v[136:139], v[72:75], 0\n"
        "buffer_store_dwordx2 v[232:233], v250, s[16:19], s38 offen offset:0 sc1\n"
        "ds_read_b128 a[188:191], v248 offset:15376\n"
        "v_mfma_f32_16x16x32_bf16 v[68:71], v[140:143], v[76:79], v[68:71]\n"
        "ds_read_b128 v[196:199], v247 offset:12544\n"
        "v_cvt_pk_bf16_f32 v235, v9, v13\n"
        "v_mfma_f32_16x16x32_bf16 v[112:115], v[148:151], v[76:79], 0\n"
        "ds_read_b128 v[200:203], v247 offset:16640\n"
        "v_add_f32 v125, v124, v125\n"
        "v_mfma_f32_16x16x32_bf16 v[112:115], v[144:147], v[72:75], v[112:115]\n"
        "buffer_store_dwordx2 v[234:235], v250, s[16:19], s38 offen offset:384 sc1\n"
        "ds_read_b128 v[204:207], v247 offset:20736\n"
        "v_mfma_f32_16x16x32_bf16 v[120:123], a[44:47], a[220:223], 0\n"
        "v_rcp_f32 v124, v125\n"
        "v_mfma_f32_16x16x32_bf16 v[120:123], a[40:43], a[216:219], v[120:123]\n"
        "v_cvt_pk_bf16_f32 v243, v10, v14\n"
        "v_mul_f32 v64, v124, v64\n"
        "v_mfma_f32_16x16x32_bf16 v[120:123], a[36:39], a[248:251], v[120:123]\n"
        "buffer_store_dwordx2 v[242:243], v250, s[16:19], s38 offen offset:768 sc1\n"
        "v_mul_f32 v65, v124, v65\n"
        "v_mfma_f32_16x16x32_bf16 v[120:123], a[32:35], a[244:247], v[120:123]\n"
        "ds_read_b128 a[192:195], v254 offset:16\n"
        "v_mul_f32 v66, v124, v66\n"
        "v_mfma_f32_16x16x32_bf16 v[120:123], a[28:31], a[240:243], v[120:123]\n"
        "ds_read_b128 a[196:199], v254 offset:3088\n"
        "v_mul_f32 v67, v124, v67\n"
        "v_mfma_f32_16x16x32_bf16 v[120:123], a[24:27], a[236:239], v[120:123]\n"
        "s_waitcnt lgkmcnt(10)\n"
        "ds_read_b128 a[200:203], v254 offset:6160\n"
        "v_cvt_pk_bf16_f32 v64, v64, v65\n"
        "v_mfma_f32_16x16x32_bf16 v[16:19], a[224:227], a[168:171], 0\n"
        "s_waitcnt lgkmcnt(10)\n"
        "ds_read_b128 a[204:207], v254 offset:9232\n"
        "ds_read_b128 v[222:225], v251 offset:4608\n"
        "v_mfma_f32_16x16x32_bf16 v[16:19], a[228:231], a[172:175], v[16:19]\n"
        "s_waitcnt lgkmcnt(11)\n"
        "ds_read_b128 a[208:211], v254 offset:12304\n"
        "v_cvt_pk_bf16_f32 v65, v66, v67\n"
        "v_mfma_f32_16x16x32_bf16 v[16:19], a[232:235], a[176:179], v[16:19]\n"
        "s_waitcnt lgkmcnt(11)\n"
        "ds_read_b128 a[212:215], v254 offset:15376\n"
        "v_cvt_pk_bf16_f32 v232, v3, v7\n"
        "v_mfma_f32_16x16x32_bf16 v[16:19], v[156:159], a[180:183], v[16:19]\n"
        "s_waitcnt lgkmcnt(11)\n"
        "v_cvt_pk_bf16_f32 v233, v11, v15\n"
        "v_mul_f32 v68, v124, v68\n"
        "v_mfma_f32_16x16x32_bf16 v[16:19], v[160:163], a[184:187], v[16:19]\n"
        "s_waitcnt lgkmcnt(10)\n"
        "buffer_store_dwordx2 v[232:233], v250, s[16:19], s38 offen offset:1152 sc1\n"
        "v_mul_f32 v69, v124, v69\n"
        "v_mfma_f32_16x16x32_bf16 v[16:19], v[164:167], a[188:191], v[16:19]\n"
        "s_waitcnt lgkmcnt(7)\n"
        "v_mul_f32 v70, v124, v70\n"
        "v_mul_f32 v71, v124, v71\n"
        "v_mfma_f32_16x16x32_bf16 v[24:27], v[204:207], a[92:95], 0\n"
        "v_mul_f32 v105, v96, v105\n"
        "v_mul_f32 v106, v96, v106\n"
        "v_mfma_f32_16x16x32_bf16 v[24:27], v[200:203], a[88:91], v[24:27]\n"
        "v_mul_f32 v107, v96, v107\n"
        "v_cvt_pk_bf16_f32 v66, v68, v69\n"
        "v_mfma_f32_16x16x32_bf16 v[24:27], v[196:199], a[84:87], v[24:27]\n"
        "v_cvt_pk_bf16_f32 v67, v70, v71\n"
        "v_mul_f32 v112, v124, v112\n"
        "v_mfma_f32_16x16x32_bf16 v[24:27], v[184:187], a[80:83], v[24:27]\n"
        "ds_write_b128 v245, v[64:67] offset:256\n"
        "v_mfma_f32_16x16x32_bf16 v[24:27], v[180:183], a[76:79], v[24:27]\n"
        "v_mul_f32 v113, v124, v113\n"
        "v_mul_f32 v114, v124, v114\n"
        "v_mfma_f32_16x16x32_bf16 v[24:27], v[176:179], a[72:75], v[24:27]\n"
        "s_waitcnt lgkmcnt(7)\n"
        "v_mul_f32 v115, v124, v115\n"
        "v_cvt_pk_bf16_f32 v102, v104, v105\n"
        "v_mfma_f32_16x16x32_bf16 v[20:23], a[224:227], a[192:195], 0\n"
        "s_waitcnt lgkmcnt(6)\n"
        "v_cvt_pk_bf16_f32 v103, v106, v107\n"
        "v_cvt_pk_bf16_f32 v218, v120, v121\n"
        "v_mfma_f32_16x16x32_bf16 v[20:23], a[228:231], a[196:199], v[20:23]\n"
        "s_waitcnt lgkmcnt(5)\n"
        "ds_write_b128 v245, v[100:103] offset:0\n"
        "v_mfma_f32_16x16x32_bf16 v[20:23], a[232:235], a[200:203], v[20:23]\n"
        "s_waitcnt lgkmcnt(5)\n"
        "v_cvt_pk_bf16_f32 v219, v122, v123\n"
        "v_mul_f32 v108, v96, v108\n"
        "v_mfma_f32_16x16x32_bf16 v[20:23], v[156:159], a[204:207], v[20:23]\n"
        "s_waitcnt lgkmcnt(3)\n"
        "v_mul_f32 v109, v96, v109\n"
        "v_mul_f32 v110, v96, v110\n"
        "v_mfma_f32_16x16x32_bf16 v[20:23], v[160:163], a[208:211], v[20:23]\n"
        "s_waitcnt lgkmcnt(2)\n"
        "v_mul_f32 v111, v96, v111\n"
        "v_cvt_pk_bf16_f32 v112, v112, v113\n"
        "v_mfma_f32_16x16x32_bf16 v[20:23], v[164:167], a[212:215], v[20:23]\n"
        "v_cvt_pk_bf16_f32 v113, v114, v115\n"
        "v_cvt_pk_bf16_f32 v108, v108, v109\n"
        "v_mfma_f32_16x16x32_bf16 v[116:119], a[48:51], a[236:239], 0\n"
        "ds_write_b64 v246, v[112:113] offset:256\n"
        "v_mfma_f32_16x16x32_bf16 v[116:119], a[52:55], a[240:243], v[116:119]\n"
        "v_cvt_pk_bf16_f32 v109, v110, v111\n"
        "v_max_u32 v236, v96, v97\n"
        "v_mfma_f32_16x16x32_bf16 v[116:119], a[56:59], a[244:247], v[116:119]\n"
        "ds_write_b64 v246, v[108:109] offset:0\n"
        "v_mfma_f32_16x16x32_bf16 v[116:119], a[60:63], a[248:251], v[116:119]\n"
        "v_max3_u32 v236, v236, v124, v125\n"
        "v_cvt_pk_bf16_f32 v244, v16, v20\n"
        "v_mfma_f32_16x16x32_bf16 v[116:119], a[64:67], a[216:219], v[116:119]\n"
        "buffer_store_dword v244, v238, s[16:19], s38 offen offset:0 sc1\n"
        "v_mfma_f32_16x16x32_bf16 v[116:119], a[68:71], a[220:223], v[116:119]\n"
        "v_cvt_pk_bf16_f32 v239, v17, v21\n"
        "v_cvt_pk_bf16_f32 v255, v18, v22\n"
        "v_mfma_f32_16x16x32_bf16 v[32:35], v[204:207], a[140:143], 0\n"
        "v_cvt_pk_bf16_f32 v244, v19, v23\n"
        "s_nop 0\n"
        "v_mfma_f32_16x16x32_bf16 v[32:35], v[200:203], a[136:139], v[32:35]\n"
        "buffer_store_dword v239, v238, s[16:19], s38 offen offset:384 sc1\n"
        "v_mfma_f32_16x16x32_bf16 v[32:35], v[196:199], a[132:135], v[32:35]\n"
        "v_cvt_pk_bf16_f32 v220, v116, v117\n"
        "v_cvt_pk_bf16_f32 v221, v118, v119\n"
        "v_mfma_f32_16x16x32_bf16 v[32:35], v[184:187], a[128:131], v[32:35]\n"
        "v_cvt_pk_bf16_f32 v226, v116, v117\n"
        "v_cvt_pk_bf16_f32 v227, v118, v119\n"
        "v_mfma_f32_16x16x32_bf16 v[32:35], v[180:183], a[124:127], v[32:35]\n"
        "v_mfma_f32_16x16x32_bf16 v[32:35], v[176:179], a[120:123], v[32:35]\n"
        "buffer_store_dword v255, v238, s[16:19], s38 offen offset:768 sc1\n"
        "v_mfma_f32_16x16x32_bf16 v[0:3], v[152:155], v[216:219], v[48:51]\n"
        ".long 0xbec200ff\n"
        "48:\n"
        "s_branch 61b\n"
        "v_mfma_f32_16x16x32_bf16 v[0:3], v[168:171], v[220:223], v[0:3]\n"
        "s_nop 2\n"
        "v_mfma_f32_16x16x32_bf16 v[4:7], v[172:175], v[216:219], v[52:55]\n"
        "s_nop 2\n"
        "v_mfma_f32_16x16x32_bf16 v[4:7], v[188:191], v[224:227], v[4:7]\n"
        "buffer_store_dword v244, v238, s[16:19], s38 offen offset:1152 sc1\n"
        "v_mfma_f32_16x16x32_bf16 v[28:31], v[176:179], a[96:99], 0\n"
        "ds_read_b128 v[222:225], v252 offset:4608\n"
        "s_nop 0\n"
        "v_mfma_f32_16x16x32_bf16 v[28:31], v[180:183], a[100:103], v[28:31]\n"
        "v_exp_f32 v0, v0\n"
        "v_mfma_f32_16x16x32_bf16 v[28:31], v[184:187], a[104:107], v[28:31]\n"
        "v_exp_f32 v1, v1\n"
        "v_mfma_f32_16x16x32_bf16 v[28:31], v[196:199], a[108:111], v[28:31]\n"
        "v_exp_f32 v2, v2\n"
        "v_mfma_f32_16x16x32_bf16 v[28:31], v[200:203], a[112:115], v[28:31]\n"
        "v_cvt_pk_bf16_f32 v88, v0, v1\n"
        "v_mfma_f32_16x16x32_bf16 v[28:31], v[204:207], a[116:119], v[28:31]\n"
        "v_exp_f32 v4, v4\n"
        "v_mfma_f32_16x16x32_bf16 v[36:39], v[204:207], a[164:167], 0\n"
        "v_exp_f32 v3, v3\n"
        "v_mfma_f32_16x16x32_bf16 v[36:39], v[200:203], a[160:163], v[36:39]\n"
        "v_exp_f32 v5, v5\n"
        "v_mfma_f32_16x16x32_bf16 v[36:39], v[196:199], a[156:159], v[36:39]\n"
        "v_cvt_pk_bf16_f32 v89, v2, v3\n"
        "v_cvt_pk_bf16_f32 v90, v4, v5\n"
        "v_mfma_f32_16x16x32_bf16 v[36:39], v[184:187], a[152:155], v[36:39]\n"
        "v_exp_f32 v6, v6\n"
        "v_mfma_f32_16x16x32_bf16 v[36:39], v[180:183], a[148:151], v[36:39]\n"
        "v_exp_f32 v7, v7\n"
        "v_mfma_f32_16x16x32_bf16 v[36:39], v[176:179], a[144:147], v[36:39]\n"
        "v_cvt_pk_bf16_f32 v91, v6, v7\n"
        "v_mov_b64 v[96:97], 0\n"
        "v_mfma_f32_16x16x32_bf16 v[8:11], v[192:195], v[216:219], v[56:59]\n"
        "s_waitcnt lgkmcnt(0)\n"
        "v_cvt_pk_bf16_f32 v234, v24, v28\n"
        "v_cvt_pk_bf16_f32 v242, v25, v29\n"
        "v_mfma_f32_16x16x32_bf16 v[8:11], v[208:211], v[220:223], v[8:11]\n"
        "v_cvt_pk_bf16_f32 v232, v26, v30\n"
        "s_nop 0\n"
        "v_mfma_f32_16x16x32_bf16 v[44:47], v[176:179], a[192:195], 0\n"
        "v_cvt_pk_bf16_f32 v235, v32, v36\n"
        "v_cvt_pk_bf16_f32 v243, v33, v37\n"
        "v_mfma_f32_16x16x32_bf16 v[44:47], v[180:183], a[196:199], v[44:47]\n"
        "buffer_store_dwordx2 v[234:235], v250, s[16:19], s39 offen offset:0 sc1\n"
        "v_cvt_pk_bf16_f32 v233, v34, v38\n"
        "v_mfma_f32_16x16x32_bf16 v[44:47], v[184:187], a[200:203], v[44:47]\n"
        "v_exp_f32 v8, v8\n"
        "v_mfma_f32_16x16x32_bf16 v[44:47], v[196:199], a[204:207], v[44:47]\n"
        "v_exp_f32 v9, v9\n"
        "v_mfma_f32_16x16x32_bf16 v[44:47], v[200:203], a[208:211], v[44:47]\n"
        "buffer_store_dwordx2 v[242:243], v250, s[16:19], s39 offen offset:384 sc1\n"
        "v_cvt_pk_bf16_f32 v92, v8, v9\n"
        "v_mfma_f32_16x16x32_bf16 v[44:47], v[204:207], a[212:215], v[44:47]\n"
        "v_exp_f32 v10, v10\n"
        "v_mfma_f32_16x16x32_bf16 v[12:15], v[212:215], v[216:219], v[60:63]\n"
        "v_exp_f32 v11, v11\n"
        "v_mfma_f32_16x16x32_bf16 v[12:15], v[228:231], v[224:227], v[12:15]\n"
        "buffer_store_dwordx2 v[232:233], v250, s[16:19], s39 offen offset:768 sc1\n"
        "v_cvt_pk_bf16_f32 v234, v27, v31\n"
        "v_smfmac_f32_16x16x64_bf16 v[96:99], a[252:255], v[80:87], v237\n"
        "v_cvt_pk_bf16_f32 v235, v35, v39\n"
        "v_mfma_f32_16x16x32_bf16 v[72:75], v[132:135], v[84:87], 0\n"
        "s_barrier\n"
        "ds_read_b128 a[0:3], v240 offset:0\n"
        "ds_read_b128 a[4:7], v240 offset:4096\n"
        "v_mfma_f32_16x16x32_bf16 v[72:75], v[128:131], v[80:83], v[72:75]\n"
        "buffer_store_dwordx2 v[234:235], v250, s[16:19], s39 offen offset:1152 sc1\n"
        "ds_read_b128 a[8:11], v240 offset:8192\n"
        "v_mfma_f32_16x16x32_bf16 v[76:79], v[136:139], v[80:83], 0\n"
        "ds_read_b128 a[12:15], v240 offset:12288\n"
        "ds_read_b128 a[16:19], v240 offset:16384\n"
        "v_mfma_f32_16x16x32_bf16 v[76:79], v[140:143], v[84:87], v[76:79]\n"
        "ds_read_b128 a[20:23], v240 offset:20480\n"
        "v_cvt_pk_bf16_f32 v93, v10, v11\n"
        "v_mfma_f32_16x16x32_bf16 v[100:103], v[148:151], v[84:87], 0\n"
        "v_exp_f32 v12, v12\n"
        "v_mfma_f32_16x16x32_bf16 v[100:103], v[144:147], v[80:83], v[100:103]\n"
        "v_exp_f32 v13, v13\n"
        "v_mfma_f32_16x16x32_bf16 v[40:43], v[204:207], a[188:191], 0\n"
        "v_exp_f32 v14, v14\n"
        "v_mfma_f32_16x16x32_bf16 v[40:43], v[200:203], a[184:187], v[40:43]\n"
        "v_cvt_pk_bf16_f32 v94, v12, v13\n"
        "v_mov_b64 v[124:125], 0\n"
        "v_mfma_f32_16x16x32_bf16 v[40:43], v[196:199], a[180:183], v[40:43]\n"
        "v_exp_f32 v15, v15\n"
        "v_mfma_f32_16x16x32_bf16 v[40:43], v[184:187], a[176:179], v[40:43]\n"
        "v_cvt_pk_bf16_f32 v95, v14, v15\n"
        "v_add_f32 v97, v96, v97\n"
        "v_mfma_f32_16x16x32_bf16 v[40:43], v[180:183], a[172:175], v[40:43]\n"
        "v_rcp_f32 v96, v97\n"
        "v_mfma_f32_16x16x32_bf16 v[40:43], v[176:179], a[168:171], v[40:43]\n"
        "s_waitcnt lgkmcnt(5)\n"
        "v_mul_f32 v72, v96, v72\n"
        "v_mul_f32 v73, v96, v73\n"
        "v_mfma_f32_16x16x32_bf16 v[156:159], a[0:3], a[72:75], 0\n"
        "s_waitcnt lgkmcnt(4)\n"
        "v_mul_f32 v74, v96, v74\n"
        "v_mul_f32 v75, v96, v75\n"
        "v_mfma_f32_16x16x32_bf16 v[156:159], a[4:7], a[76:79], v[156:159]\n"
        "s_waitcnt lgkmcnt(3)\n"
        "v_cvt_pk_bf16_f32 v72, v72, v73\n"
        "v_cvt_pk_bf16_f32 v73, v74, v75\n"
        "v_mfma_f32_16x16x32_bf16 v[156:159], a[8:11], a[80:83], v[156:159]\n"
        "s_waitcnt lgkmcnt(2)\n"
        "v_cvt_pk_bf16_f32 v239, v40, v44\n"
        "v_cvt_pk_bf16_f32 v255, v41, v45\n"
        "v_mfma_f32_16x16x32_bf16 v[156:159], a[12:15], a[84:87], v[156:159]\n"
        "s_waitcnt lgkmcnt(1)\n"
        "buffer_store_dword v239, v238, s[16:19], s39 offen offset:0 sc1\n"
        "v_mfma_f32_16x16x32_bf16 v[156:159], a[16:19], a[88:91], v[156:159]\n"
        "s_waitcnt lgkmcnt(0)\n"
        "v_cvt_pk_bf16_f32 v244, v42, v46\n"
        "v_cvt_pk_bf16_f32 v239, v43, v47\n"
        "v_mfma_f32_16x16x32_bf16 v[156:159], a[20:23], a[92:95], v[156:159]\n"
        "buffer_store_dword v255, v238, s[16:19], s39 offen offset:384 sc1\n"
        "v_smfmac_f32_16x16x64_bf16 v[124:127], a[252:255], v[88:95], v237\n"
        "v_mul_f32 v76, v96, v76\n"
        "v_mfma_f32_16x16x32_bf16 v[104:107], v[132:135], v[92:95], 0\n"
        "buffer_store_dword v244, v238, s[16:19], s39 offen offset:768 sc1\n"
        "v_mfma_f32_16x16x32_bf16 v[104:107], v[128:131], v[88:91], v[104:107]\n"
        "v_mul_f32 v77, v96, v77\n"
        "v_mul_f32 v78, v96, v78\n"
        "v_mfma_f32_16x16x32_bf16 v[108:111], v[136:139], v[88:91], 0\n"
        "buffer_store_dword v239, v238, s[16:19], s39 offen offset:1152 sc1\n"
        "v_mfma_f32_16x16x32_bf16 v[108:111], v[140:143], v[92:95], v[108:111]\n"
        "v_mul_f32 v79, v96, v79\n"
        "v_add_f32 v125, v124, v125\n"
        "v_mfma_f32_16x16x32_bf16 v[112:115], v[148:151], v[92:95], 0\n"
        "v_cvt_pk_bf16_f32 v74, v76, v77\n"
        "v_cvt_pk_bf16_f32 v75, v78, v79\n"
        "v_mfma_f32_16x16x32_bf16 v[112:115], v[144:147], v[88:91], v[112:115]\n"
        "ds_write_b128 v245, v[72:75] offset:512\n"
        "v_mfma_f32_16x16x32_bf16 v[176:179], a[20:23], a[164:167], 0\n"
        "v_rcp_f32 v124, v125\n"
        "v_mfma_f32_16x16x32_bf16 v[176:179], a[16:19], a[160:163], v[176:179]\n"
        "v_mul_f32 v100, v96, v100\n"
        "v_mul_f32 v101, v96, v101\n"
        "v_mfma_f32_16x16x32_bf16 v[176:179], a[12:15], a[156:159], v[176:179]\n"
        "v_mul_f32 v102, v96, v102\n"
        "v_mul_f32 v103, v96, v103\n"
        "v_mfma_f32_16x16x32_bf16 v[176:179], a[8:11], a[152:155], v[176:179]\n"
        "v_mul_f32 v104, v124, v104\n"
        "v_mul_f32 v105, v124, v105\n"
        "v_mfma_f32_16x16x32_bf16 v[176:179], a[4:7], a[148:151], v[176:179]\n"
        "v_mul_f32 v106, v124, v106\n"
        "v_mul_f32 v107, v124, v107\n"
        "v_mfma_f32_16x16x32_bf16 v[176:179], a[0:3], a[144:147], v[176:179]\n"
        "v_cvt_pk_bf16_f32 v100, v100, v101\n"
        "v_cvt_pk_bf16_f32 v101, v102, v103\n"
        "v_mfma_f32_16x16x32_bf16 v[160:163], a[0:3], a[96:99], 0\n"
        "ds_write_b64 v246, v[100:101] offset:512\n"
        "v_mfma_f32_16x16x32_bf16 v[160:163], a[4:7], a[100:103], v[160:163]\n"
        "v_cvt_pk_bf16_f32 v104, v104, v105\n"
        "v_cvt_pk_bf16_f32 v105, v106, v107\n"
        "v_mfma_f32_16x16x32_bf16 v[160:163], a[8:11], a[104:107], v[160:163]\n"
        "v_mul_f32 v108, v124, v108\n"
        "v_mul_f32 v109, v124, v109\n"
        "v_mfma_f32_16x16x32_bf16 v[160:163], a[12:15], a[108:111], v[160:163]\n"
        "v_mul_f32 v110, v124, v110\n"
        "v_mul_f32 v111, v124, v111\n"
        "v_mfma_f32_16x16x32_bf16 v[160:163], a[16:19], a[112:115], v[160:163]\n"
        "v_max3_u32 v236, v236, v96, v97\n"
        "v_cvt_pk_bf16_f32 v106, v108, v109\n"
        "v_mfma_f32_16x16x32_bf16 v[160:163], a[20:23], a[116:119], v[160:163]\n"
        "v_cvt_pk_bf16_f32 v107, v110, v111\n"
        "v_mul_f32 v112, v124, v112\n"
        "v_mfma_f32_16x16x32_bf16 v[164:167], a[20:23], a[140:143], 0\n"
        "ds_write_b128 v245, v[104:107] offset:768\n"
        "v_mfma_f32_16x16x32_bf16 v[164:167], a[16:19], a[136:139], v[164:167]\n"
        "v_mul_f32 v113, v124, v113\n"
        "v_mul_f32 v114, v124, v114\n"
        "v_mfma_f32_16x16x32_bf16 v[164:167], a[12:15], a[132:135], v[164:167]\n"
        "v_mul_f32 v115, v124, v115\n"
        "v_cvt_pk_bf16_f32 v112, v112, v113\n"
        "v_mfma_f32_16x16x32_bf16 v[164:167], a[8:11], a[128:131], v[164:167]\n"
        "v_cvt_pk_bf16_f32 v113, v114, v115\n"
        "v_max3_u32 v236, v236, v124, v125\n"
        "v_mfma_f32_16x16x32_bf16 v[164:167], a[4:7], a[124:127], v[164:167]\n"
        "ds_write_b64 v246, v[112:113] offset:768\n"
        "v_mfma_f32_16x16x32_bf16 v[164:167], a[0:3], a[120:123], v[164:167]\n"
        "v_cmp_lt_u32 vcc, s24, v236\n"
        "v_cvt_pk_bf16_f32 v242, v156, v160\n"
        "v_mfma_f32_16x16x32_bf16 v[180:183], a[0:3], a[168:171], 0\n"
        "s_cmp_lg_u64 vcc, 0\n"
        "s_addc_u32 s25, s25, s25\n"
        "v_mov_b32 v253, s25\n"
        "v_mfma_f32_16x16x32_bf16 v[180:183], a[4:7], a[172:175], v[180:183]\n"
        "ds_write_b32 v249, v253\n"
        "v_mfma_f32_16x16x32_bf16 v[180:183], a[8:11], a[176:179], v[180:183]\n"
        "v_cvt_pk_bf16_f32 v243, v164, v176\n"
        "v_cvt_pk_bf16_f32 v232, v157, v161\n"
        "v_mfma_f32_16x16x32_bf16 v[180:183], a[12:15], a[180:183], v[180:183]\n"
        "buffer_store_dwordx2 v[242:243], v250, s[16:19], s47 offen offset:0 sc1\n"
        "v_cvt_pk_bf16_f32 v233, v165, v177\n"
        "v_mfma_f32_16x16x32_bf16 v[180:183], a[16:19], a[184:187], v[180:183]\n"
        "v_cvt_pk_bf16_f32 v234, v158, v162\n"
        "v_cvt_pk_bf16_f32 v235, v166, v178\n"
        "v_mfma_f32_16x16x32_bf16 v[180:183], a[20:23], a[188:191], v[180:183]\n"
        "s_waitcnt lgkmcnt(0)\n"
        "s_barrier\n"
        "ds_read_b128 a[24:27], v240 offset:512\n"
        "ds_read_b128 a[28:31], v240 offset:4608\n"
        "v_mfma_f32_16x16x32_bf16 v[184:187], a[20:23], a[212:215], 0\n"
        "buffer_store_dwordx2 v[232:233], v250, s[16:19], s47 offen offset:384 sc1\n"
        "ds_read_b128 a[32:35], v240 offset:8704\n"
        "v_mfma_f32_16x16x32_bf16 v[184:187], a[16:19], a[208:211], v[184:187]\n"
        "ds_read_b128 a[36:39], v240 offset:12800\n"
        "ds_read_b128 a[40:43], v240 offset:16896\n"
        "v_mfma_f32_16x16x32_bf16 v[184:187], a[12:15], a[204:207], v[184:187]\n"
        "ds_read_b128 a[44:47], v240 offset:20992\n"
        "v_cvt_pk_bf16_f32 v242, v159, v163\n"
        "v_mfma_f32_16x16x32_bf16 v[184:187], a[8:11], a[200:203], v[184:187]\n"
        "buffer_store_dwordx2 v[234:235], v250, s[16:19], s47 offen offset:768 sc1\n"
        "v_cvt_pk_bf16_f32 v243, v167, v179\n"
        "v_mfma_f32_16x16x32_bf16 v[184:187], a[4:7], a[196:199], v[184:187]\n"
        "v_mfma_f32_16x16x32_bf16 v[184:187], a[0:3], a[192:195], v[184:187]\n"
        "s_waitcnt lgkmcnt(5)\n"
        "s_nop 1\n"
        "v_mfma_f32_16x16x32_bf16 v[52:55], a[24:27], a[168:171], 0\n"
        "s_waitcnt lgkmcnt(4)\n"
        "buffer_store_dwordx2 v[242:243], v250, s[16:19], s47 offen offset:1152 sc1\n"
        "s_nop 0\n"
        "v_mfma_f32_16x16x32_bf16 v[52:55], a[28:31], a[172:175], v[52:55]\n"
        "s_waitcnt lgkmcnt(3)\n"
        "ds_read_b128 v[60:63], v241\n"
        "s_nop 0\n"
        "v_mfma_f32_16x16x32_bf16 v[52:55], a[32:35], a[176:179], v[52:55]\n"
        "s_waitcnt lgkmcnt(3)\n"
        "v_cvt_pk_bf16_f32 v255, v180, v184\n"
        "v_cvt_pk_bf16_f32 v244, v181, v185\n"
        "v_mfma_f32_16x16x32_bf16 v[52:55], a[36:39], a[180:183], v[52:55]\n"
        "s_waitcnt lgkmcnt(2)\n"
        "buffer_store_dword v255, v238, s[16:19], s47 offen offset:0 sc1\n"
        "v_mfma_f32_16x16x32_bf16 v[52:55], a[40:43], a[184:187], v[52:55]\n"
        "s_waitcnt lgkmcnt(1)\n"
        "v_cvt_pk_bf16_f32 v239, v182, v186\n"
        "v_cvt_pk_bf16_f32 v255, v183, v187\n"
        "v_mfma_f32_16x16x32_bf16 v[52:55], a[44:47], a[188:191], v[52:55]\n"
        "v_mfma_f32_16x16x32_bf16 v[196:199], a[44:47], a[92:95], 0\n"
        "buffer_store_dword v244, v238, s[16:19], s47 offen offset:384 sc1\n"
        "v_mfma_f32_16x16x32_bf16 v[196:199], a[40:43], a[88:91], v[196:199]\n"
        "v_mfma_f32_16x16x32_bf16 v[196:199], a[36:39], a[84:87], v[196:199]\n"
        "v_mfma_f32_16x16x32_bf16 v[196:199], a[32:35], a[80:83], v[196:199]\n"
        "buffer_store_dword v239, v238, s[16:19], s47 offen offset:768 sc1\n"
        "v_mfma_f32_16x16x32_bf16 v[196:199], a[28:31], a[76:79], v[196:199]\n"
        "v_mfma_f32_16x16x32_bf16 v[196:199], a[24:27], a[72:75], v[196:199]\n"
        "v_mfma_f32_16x16x32_bf16 v[56:59], a[24:27], a[192:195], 0\n"
        "buffer_store_dword v255, v238, s[16:19], s47 offen offset:1152 sc1\n"
        "v_mfma_f32_16x16x32_bf16 v[56:59], a[28:31], a[196:199], v[56:59]\n"
        "v_mfma_f32_16x16x32_bf16 v[56:59], a[32:35], a[200:203], v[56:59]\n"
        "v_mfma_f32_16x16x32_bf16 v[56:59], a[36:39], a[204:207], v[56:59]\n"
        "s_waitcnt lgkmcnt(0)\n"
        "v_or3_b32 v60, v60, v61, v62\n"
        "v_or_b32 v60, v60, v63\n"
        "v_mfma_f32_16x16x32_bf16 v[56:59], a[40:43], a[208:211], v[56:59]\n"
        "s_nop 0\n"
        "v_readfirstlane_b32 s25, v60\n"
        "v_mfma_f32_16x16x32_bf16 v[56:59], a[44:47], a[212:215], v[56:59]\n"
        "s_nop 2\n"
        "v_mfma_f32_16x16x32_bf16 v[204:207], a[44:47], a[140:143], 0\n"
        "s_nop 2\n"
        "v_mfma_f32_16x16x32_bf16 v[204:207], a[40:43], a[136:139], v[204:207]\n"
        "v_cvt_pk_bf16_f32 v88, v52, v56\n"
        "v_cvt_pk_bf16_f32 v90, v53, v57\n"
        "v_mfma_f32_16x16x32_bf16 v[204:207], a[36:39], a[132:135], v[204:207]\n"
        "v_cvt_pk_bf16_f32 v92, v54, v58\n"
        "v_cvt_pk_bf16_f32 v94, v55, v59\n"
        "v_mfma_f32_16x16x32_bf16 v[204:207], a[32:35], a[128:131], v[204:207]\n"
        "buffer_store_dword v88, v238, s[16:19], s35 offen offset:0 sc1\n"
        "v_mfma_f32_16x16x32_bf16 v[204:207], a[28:31], a[124:127], v[204:207]\n"
        "v_mfma_f32_16x16x32_bf16 v[204:207], a[24:27], a[120:123], v[204:207]\n"
        "buffer_store_dword v90, v238, s[16:19], s35 offen offset:384 sc1\n"
        "v_mfma_f32_16x16x32_bf16 v[200:203], a[24:27], a[96:99], 0\n"
        "v_mfma_f32_16x16x32_bf16 v[200:203], a[28:31], a[100:103], v[200:203]\n"
        "buffer_store_dword v92, v238, s[16:19], s35 offen offset:768 sc1\n"
        "v_mfma_f32_16x16x32_bf16 v[200:203], a[32:35], a[104:107], v[200:203]\n"
        "v_mfma_f32_16x16x32_bf16 v[200:203], a[36:39], a[108:111], v[200:203]\n"
        "buffer_store_dword v94, v238, s[16:19], s35 offen offset:1152 sc1\n"
        "v_mfma_f32_16x16x32_bf16 v[200:203], a[40:43], a[112:115], v[200:203]\n"
        "v_mfma_f32_16x16x32_bf16 v[200:203], a[44:47], a[116:119], v[200:203]\n"
        "v_mfma_f32_16x16x32_bf16 v[48:51], a[44:47], a[164:167], 0\n"
        "v_mfma_f32_16x16x32_bf16 v[48:51], a[40:43], a[160:163], v[48:51]\n"
        "s_nop 2\n"
        "v_mfma_f32_16x16x32_bf16 v[48:51], a[36:39], a[156:159], v[48:51]\n"
        "s_nop 1\n"
        "v_cvt_pk_bf16_f32 v80, v196, v200\n"
        "v_mfma_f32_16x16x32_bf16 v[48:51], a[32:35], a[152:155], v[48:51]\n"
        "v_cvt_pk_bf16_f32 v82, v197, v201\n"
        "v_mfma_f32_16x16x32_bf16 v[48:51], a[28:31], a[148:151], v[48:51]\n"
        "v_cvt_pk_bf16_f32 v84, v198, v202\n"
        "v_mfma_f32_16x16x32_bf16 v[48:51], a[24:27], a[144:147], v[48:51]\n"
        "s_nop 2\n"
        "v_cvt_pk_bf16_f32 v86, v199, v203\n"
        "s_nop 3\n"
        "v_cvt_pk_bf16_f32 v81, v204, v48\n"
        "buffer_store_dwordx2 v[80:81], v250, s[16:19], s35 offen offset:0 sc1\n"
        "v_cvt_pk_bf16_f32 v83, v205, v49\n"
        "buffer_store_dwordx2 v[82:83], v250, s[16:19], s35 offen offset:384 sc1\n"
        "v_cvt_pk_bf16_f32 v85, v206, v50\n"
        "buffer_store_dwordx2 v[84:85], v250, s[16:19], s35 offen offset:768 sc1\n"
        "v_cvt_pk_bf16_f32 v87, v207, v51\n"
        "buffer_store_dwordx2 v[86:87], v250, s[16:19], s35 offen offset:1152 sc1\n"
        "s_cmp_lg_u32 s25, 0\n"
        "s_cbranch_scc1 30f\n"
        "s_endpgm\n"
        "30:\n"
        : "={s25}"(flg), "={s42}"(w), "+{s[0:1]}"(ka), "+{s2}"(bid), "+{v0}"(tid)
        : "s"(ldsb)
        : "v1", "v2", "v3", "v4", "v5", "v6", "v7", "v8", "v9", "v10", "v11", "v12", "v13", "v14", "v15", "v16", "v17", "v18", "v19", "v20", "v21", "v22", "v23", "v24", "v25", "v26", "v27", "v28", "v29", "v30", "v31", "v32", "v33", "v34", "v35", "v36", "v37", "v38", "v39", "v40", "v41", "v42", "v43", "v44", "v45", "v46", "v47", "v48", "v49", "v50", "v51", "v52", "v53", "v54", "v55", "v56", "v57", "v58", "v59", "v60", "v61", "v62", "v63", "v64", "v65", "v66", "v67", "v68", "v69", "v70", "v71", "v72", "v73", "v74", "v75", "v76", "v77", "v78", "v79", "v80", "v81", "v82", "v83", "v84", "v85", "v86", "v87", "v88", "v89", "v90", "v91", "v92", "v93", "v94", "v95", "v96", "v97", "v98", "v99", "v100", "v101", "v102", "v103", "v104", "v105", "v106", "v107", "v108", "v109", "v110", "v111", "v112", "v113", "v114", "v115", "v116", "v117", "v118", "v119", "v120", "v121", "v122", "v123", "v124", "v125", "v126", "v127", "v128", "v129", "v130", "v131", "v132", "v133", "v134", "v135", "v136", "v137", "v138", "v139", "v140", "v141", "v142", "v143", "v144", "v145", "v146", "v147", "v148", "v149", "v150", "v151", "v152", "v153", "v154", "v155", "v156", "v157", "v158", "v159", "v160", "v161", "v162", "v163", "v164", "v165", "v166", "v167", "v168", "v169", "v170", "v171", "v172", "v173", "v174", "v175", "v176", "v177", "v178", "v179", "v180", "v181", "v182", "v183", "v184", "v185", "v186", "v187", "v188", "v189", "v190", "v191", "v192", "v193", "v194", "v195", "v196", "v197", "v198", "v199", "v200", "v201", "v202", "v203", "v204", "v205", "v206", "v207", "v208", "v209", "v210", "v211", "v212", "v213", "v214", "v215", "v216", "v217", "v218", "v219", "v220", "v221", "v222", "v223", "v224", "v225", "v226", "v227", "v228", "v229", "v230", "v231", "v232", "v233", "v234", "v235", "v236", "v237", "v238", "v239", "v240", "v241", "v242", "v243", "v244", "v245", "v246", "v247", "v248", "v249", "v250", "v251", "v252", "v253", "v254", "v255", "a0", "a1", "a2", "a3", "a4", "a5", "a6", "a7", "a8", "a9", "a10", "a11", "a12", "a13", "a14", "a15", "a16", "a17", "a18", "a19", "a20", "a21", "a22", "a23", "a24", "a25", "a26", "a27", "a28", "a29", "a30", "a31", "a32", "a33", "a34", "a35", "a36", "a37", "a38", "a39", "a40", "a41", "a42", "a43", "a44", "a45", "a46", "a47", "a48", "a49", "a50", "a51", "a52", "a53", "a54", "a55", "a56", "a57", "a58", "a59", "a60", "a61", "a62", "a63", "a64", "a65", "a66", "a67", "a68", "a69", "a70", "a71", "a72", "a73", "a74", "a75", "a76", "a77", "a78", "a79", "a80", "a81", "a82", "a83", "a84", "a85", "a86", "a87", "a88", "a89", "a90", "a91", "a92", "a93", "a94", "a95", "a96", "a97", "a98", "a99", "a100", "a101", "a102", "a103", "a104", "a105", "a106", "a107", "a108", "a109", "a110", "a111", "a112", "a113", "a114", "a115", "a116", "a117", "a118", "a119", "a120", "a121", "a122", "a123", "a124", "a125", "a126", "a127", "a128", "a129", "a130", "a131", "a132", "a133", "a134", "a135", "a136", "a137", "a138", "a139", "a140", "a141", "a142", "a143", "a144", "a145", "a146", "a147", "a148", "a149", "a150", "a151", "a152", "a153", "a154", "a155", "a156", "a157", "a158", "a159", "a160", "a161", "a162", "a163", "a164", "a165", "a166", "a167", "a168", "a169", "a170", "a171", "a172", "a173", "a174", "a175", "a176", "a177", "a178", "a179", "a180", "a181", "a182", "a183", "a184", "a185", "a186", "a187", "a188", "a189", "a190", "a191", "a192", "a193", "a194", "a195", "a196", "a197", "a198", "a199", "a200", "a201", "a202", "a203", "a204", "a205", "a206", "a207", "a208", "a209", "a210", "a211", "a212", "a213", "a214", "a215", "a216", "a217", "a218", "a219", "a220", "a221", "a222", "a223", "a224", "a225", "a226", "a227", "a228", "a229", "a230", "a231", "a232", "a233", "a234", "a235", "a236", "a237", "a238", "a239", "a240", "a241", "a242", "a243", "a244", "a245", "a246", "a247", "a248", "a249", "a250", "a251", "a252", "a253", "a254", "a255", "s4", "s5", "s6", "s7", "s8", "s9", "s10", "s11", "s12", "s13", "s14", "s15", "s16", "s17", "s18", "s19", "s20", "s21", "s22", "s23", "s24", "s26", "s27", "s28", "s29", "s30", "s31", "s34", "s35", "s36", "s37", "s38", "s39", "s40", "s41", "s43", "s44", "s45", "s46", "s47", "s48", "s49", "s50", "s51", "s52", "s53", "s54", "s55", "s56", "s57", "s58", "s59", "s60", "s61", "s62", "s63", "s64", "s65", "s66", "s67", "s68", "s69", "s70", "s71", "s72", "s73", "s74", "s75", "s76", "s77", "s78", "s79", "s80", "s81", "s82", "s83", "vcc", "scc", "memory");
    if (flg) {
        unsigned ln;
        asm volatile("s_waitcnt vmcnt(0)\n v_mbcnt_lo_u32_b32 %0, -1, 0\n v_mbcnt_hi_u32_b32 %0, -1, %0" : "=v"(ln) :: "memory");
        const unsigned t = w * 64u + ln, wx = bid & 63u, wy0 = bid >> 6;
        typedef const char* __attribute__((address_space(4))) const* kargp;
        const char *x = ((kargp)ka)[0], *wqkv = ((kargp)ka)[1], *wo = ((kargp)ka)[2], *rpb = ((kargp)ka)[3], *rpi = ((kargp)ka)[4],
                   *mask = ((kargp)ka)[5];
        char* out = (char*)((kargp)ka)[6];
        unsigned short* xs = (unsigned short*)smem;
        unsigned short* qs = xs + 64 * 192;
        const unsigned short* W1 = (const unsigned short*)wqkv;
        const unsigned short* W2 = (const unsigned short*)wo;
        for (unsigned i = 0; i < 16u; ++i) {
            if (!((flg >> (15u - i)) & 1u)) continue;
            const unsigned wy = wy0 + 4u * i;
            __syncthreads();
            for (unsigned e = t; e < 64u * 96u; e += 256u) {
                const unsigned tok = e / 96u, c2 = e % 96u;
                const unsigned pix = ((8u * wy + 4u + (tok >> 3)) & 511u) * 512u + ((8u * wx + 4u + (tok & 7u)) & 511u);
                ((unsigned*)xs)[e] = ((const unsigned*)x)[pix * 96u + c2];
            }
            __syncthreads();
            for (unsigned col = t; col < 576u; col += 256u)
                for (unsigned tb = 0; tb < 64u; tb += 8u) {
                    float a[8] = {0.f, 0.f, 0.f, 0.f, 0.f, 0.f, 0.f, 0.f};
                    for (unsigned c = 0; c < 192u; ++c) {
                        const float wt = bf2f(W1[c * 576u + col]);
#pragma unroll
                        for (int j = 0; j < 8; ++j) a[j] += bf2f(xs[(tb + j) * 192u + c]) * wt;
                    }
#pragma unroll
                    for (int j = 0; j < 8; ++j) qs[(tb + j) * 578u + col] = f2bf(a[j]);
                }
            __syncthreads();
            {
                float qv[48], o[48];
#pragma unroll
                for (int d = 0; d < 48; ++d) { qv[d] = bf2f(qs[ln * 578u + w * 48u + d]); o[d] = 0.f; }
                const unsigned short* mrow = (const unsigned short*)mask + (size_t)(wy * 64u + wx) * 4096u + ln * 64u;
                float mx = -__builtin_inff(), l = 0.f;
                for (int pass = 0; pass < 2; ++pass)
                    for (unsigned k = 0; k < 64u; ++k) {
                        float s = 0.f;
#pragma unroll
                        for (int d = 0; d < 48; ++d) s += qv[d] * bf2f(qs[k * 578u + 192u + w * 48u + d]);
                        s = s * 0.14433756729740643f + bf2f(((const unsigned short*)rpb)[((const unsigned*)rpi)[ln * 64u + k] * 4u + w]) + bf2f(mrow[k]);
                        if (pass == 0) mx = fmaxf(mx, s);
                        else {
                            const float p = __expf(s - mx);
                            l += p;
#pragma unroll
                            for (int d = 0; d < 48; ++d) o[d] += p * bf2f(qs[k * 578u + 384u + w * 48u + d]);
                        }
                    }
#pragma unroll
                for (int d = 0; d < 48; ++d) xs[ln * 192u + w * 48u + d] = f2bf(o[d] / l);
            }
            __syncthreads();
            if (t < 192u)
                for (unsigned tb = 0; tb < 64u; tb += 8u) {
                    float a[8] = {0.f, 0.f, 0.f, 0.f, 0.f, 0.f, 0.f, 0.f};
                    for (unsigned c = 0; c < 192u; ++c) {
                        const float wt = bf2f(W2[c * 192u + t]);
#pragma unroll
                        for (int j = 0; j < 8; ++j) a[j] += bf2f(xs[(tb + j) * 192u + c]) * wt;
                    }
                    const unsigned row = (8u * wy + 4u + (tb >> 3)) & 511u;
#pragma unroll
                    for (int j = 0; j < 8; ++j)
                        ((unsigned short*)out)[(row * 512u + ((8u * wx + 4u + j) & 511u)) * 192u + t] = f2bf(a[j]);
                }
        }
    }
}


extern "C" void kernel_launch(void* const* d_in, const int* in_sizes, int n_in, void* d_out, int out_size, void* d_ws,
                              size_t ws_size, hipStream_t stream) {
    win_attn_kernel<<<256, 256, 0, stream>>>((const char*)d_in[0], (const char*)d_in[1], (const char*)d_in[2],
                                             (const char*)d_in[3], (const char*)d_in[4], (const char*)d_in[5],
                                             (char*)d_out, (char*)d_ws);

}
```

```cpp
#include <hip/hip_runtime.h>
#include <hip/hip_bf16.h>

typedef unsigned int u32x4 __attribute__((ext_vector_type(4)));

static __device__ __forceinline__ float bf2f(unsigned short u) { return __uint_as_float((unsigned)u << 16); }
static __device__ __forceinline__ unsigned short f2bf(float f) { return __builtin_bit_cast(unsigned short, (__bf16)f); }

__global__ __launch_bounds__(256, 1) __attribute__((aligned(4096))) void win_attn_kernel(const char* x_, const char* wqkv_, const char* wo_, const char* rpb_,
                                                          const char* rpi_, const char* mask_, char* out_, char* ws) {
    __shared__ __attribute__((aligned(16))) char smem[163840];
    const void* ka = (const void*)__builtin_amdgcn_kernarg_segment_ptr();
    unsigned bid = blockIdx.x, tid = threadIdx.x, flg, w;
    unsigned ldsb = (unsigned)(size_t)smem;
    asm volatile(
        "s_load_dwordx16 s[68:83], s[0:1], 0\n"
        "v_readfirstlane_b32 s42, v0\n"
        "v_and_b32 v1, 63, v0\n"
        "s_lshr_b32 s31, s2, 1\n"
        "s_lshr_b32 s42, s42, 6\n"
        "s_and_b32 s31, s31, 0x7c\n"
        "v_lshlrev_b32 v2, 4, v1\n"
        "s_add_u32 s31, s31, s42\n"
        "s_mul_i32 s35, s31, 256\n"
        "v_min_u32 v10, 3, v1\n"
        "s_getpc_b64 s[66:67]\n"
        "v_lshl_add_u32 v10, v10, 6, s35\n"
        "global_load_dword v60, v10, s[66:67]\n"
        "s_lshl_b32 s50, s31, 6\n"
        "s_mul_i32 s50, s50, 27\n"
        "v_min_u32 v136, 26, v1\n"
        "v_lshl_add_u32 v136, v136, 6, s50\n"
        "v_mul_u32_u24 v11, 171, v1\n"
        "v_lshrrev_b32 v4, 10, v11\n"
        "v_bfe_u32 v7, v11, 13, 1\n"
        "v_add_u32 v11, 10944, v11\n"
        "v_lshrrev_b32 v5, 10, v11\n"
        "v_bfe_u32 v8, v11, 13, 1\n"
        "v_add_u32 v11, 10944, v11\n"
        "v_lshrrev_b32 v6, 10, v11\n"
        "v_bfe_u32 v9, v11, 13, 1\n"
        "s_mul_i32 s40, s42, 3072\n"
        "s_mov_b32 s10, 100663296\n"
        "s_mov_b32 s11, 0x20000\n"
        "s_mov_b32 s6, 16384\n"
        "s_mov_b32 s7, 0x20000\n"
        "s_mov_b32 s62, 1800\n"
        "s_mov_b32 s63, 0x20000\n"
        "s_mov_b32 s22, 221184\n"
        "s_mov_b32 s23, 0x20000\n"
        "s_and_b32 s51, s42, 1\n"
        "v_lshlrev_b32 v134, 3, v0\n"
        "s_lshl_b32 s35, s51, 7\n"
        "v_and_b32 v10, 48, v1\n"
        "v_xor_b32 v131, s35, v2\n"
        "s_lshl_b32 s46, s42, 12\n"
        "v_xor_b32 v131, v131, v10\n"
        "v_xor_b32 v132, 64, v131\n"
        "s_and_b32 s44, s2, 63\n"
        "s_lshl3_add_u32 s44, s44, 4\n"
        "s_lshl_b32 s35, s42, 5\n"
        "s_movk_i32 s41, 384\n"
        "s_movk_i32 s45, -384\n"
        "v_lshrrev_b32 v10, 2, v4\n"
        "v_mad_i32_i24 v11, v10, s45, v2\n"
        "v_add_u32 v10, s44, v10\n"
        "v_lshl_or_b32 v242, v7, 4, s35\n"
        "v_and_b32 v10, 511, v10\n"
        "v_xor_b32 v11, v11, v242\n"
        "v_mad_u32_u24 v242, v10, s41, v11\n"
        "v_lshrrev_b32 v10, 2, v5\n"
        "v_mad_i32_i24 v11, v10, s45, v2\n"
        "v_add_u32 v10, s44, v10\n"
        "v_add_u32 v11, 1024, v11\n"
        "v_lshl_or_b32 v243, v8, 4, s35\n"
        "v_and_b32 v10, 511, v10\n"
        "v_xor_b32 v11, v11, v243\n"
        "v_mad_u32_u24 v243, v10, s41, v11\n"
        "v_lshrrev_b32 v10, 2, v6\n"
        "v_mad_i32_i24 v11, v10, s45, v2\n"
        "v_add_u32 v10, s44, v10\n"
        "v_add_u32 v11, 2048, v11\n"
        "v_lshl_or_b32 v244, v9, 4, s35\n"
        "v_and_b32 v10, 511, v10\n"
        "v_xor_b32 v11, v11, v244\n"
        "v_mad_u32_u24 v244, v10, s41, v11\n"
        "s_mul_i32 s43, s44, 384\n"
        "s_add_u32 s44, s44, 4\n"
        "s_and_b32 s44, s44, 511\n"
        "s_mul_i32 s44, s44, 384\n"
        "s_lshr_b32 s37, s2, 6\n"
        "s_lshl3_add_u32 s47, s37, s42\n"
        "s_add_u32 s47, s47, 68\n"
        "s_mul_i32 s47, s47, 196608\n"
        "s_lshl1_add_u32 s37, s37, s51\n"
        "s_add_u32 s37, s37, 1\n"
        "s_mul_i32 s37, s37, 786432\n"
        "s_mul_i32 s65, s42, 18432\n"
        "s_mov_b32 s14, 33554432\n"
        "s_mov_b32 s15, 0x20000\n"
        "s_mul_i32 s35, s42, 96\n"
        "s_movk_i32 s31, 1056\n"
        "s_movk_i32 s45, 4608\n"
        "v_add_u32 v3, s35, v2\n"
        "s_waitcnt lgkmcnt(0)\n"
        "s_mov_b64 s[60:61], s[74:75]\n"
        "s_mov_b64 s[20:21], s[70:71]\n"
        "s_mov_b64 s[4:5], s[76:77]\n"
        "s_mov_b64 s[8:9], s[68:69]\n"
        "buffer_load_dwordx2 v[144:145], v134, s[60:63], 0 offen\n"
        "s_add_u32 m0, s46, 122880\n"
        "s_mov_b32 s64, s46\n"
        "buffer_load_dword v136, s[20:23], 0 offen lds\n"
        "s_mov_b64 s[52:53], s[72:73]\n"
        "s_mov_b64 s[12:13], s[78:79]\n"
        "buffer_load_dwordx4 v131, s[4:7], s64 offen lds\n"
        "s_mov_b64 s[16:17], s[80:81]\n"
        "v_bfe_u32 v12, v4, 2, 1\n"
        "v_mad_u32_u24 v128, v4, s31, v3\n"
        "v_sub_u32 v12, v12, v7\n"
        "v_mad_i32_i24 v128, v12, s45, v128\n"
        "v_bfe_u32 v13, v5, 2, 1\n"
        "buffer_load_dwordx4 v131, s[4:7], s64 offen offset:1024 lds\n"
        "v_mad_u32_u24 v129, v5, s31, v3\n"
        "v_sub_u32 v13, v13, v8\n"
        "v_mad_i32_i24 v129, v13, s45, v129\n"
        "v_bfe_u32 v14, v6, 2, 1\n"
        "v_mad_u32_u24 v130, v6, s31, v3\n"
        "v_sub_u32 v14, v14, v9\n"
        "v_mad_i32_i24 v130, v14, s45, v130\n"
        "s_lshl_b32 s48, s42, 11\n"
        "s_lshl_b32 s36, s2, 13\n"
        "v_and_b32 v19, 16, v1\n"
        "buffer_load_dwordx4 v132, s[4:7], s64 offen offset:2048 lds\n"
        "s_add_u32 s48, s48, 155648\n"
        "s_add_u32 s36, s36, s48\n"
        "s_add_u32 s36, s36, 4038656\n"
        "s_lshl_b32 s35, s51, 6\n"
        "v_xor_b32 v249, v2, v19\n"
        "v_xor_b32 v249, s35, v249\n"
        "v_xor_b32 v253, 32, v249\n"
        "v_and_b32 v15, 15, v1\n"
        "v_lshrrev_b32 v16, 4, v1\n"
        "v_and_b32 v17, 12, v1\n"
        "buffer_load_dwordx4 v132, s[4:7], s64 offen offset:3072 lds\n"
        "v_and_b32 v18, 0xf0, v2\n"
        "v_add_u32 v17, v17, v15\n"
        "v_bfe_u32 v10, v1, 2, 3\n"
        "v_lshrrev_b32 v11, 5, v1\n"
        "v_lshl_or_b32 v10, v11, 4, v10\n"
        "v_and_b32 v255, 3, v1\n"
        "v_mul_u32_u24 v10, 96, v10\n"
        "v_lshl_add_u32 v255, v255, 3, s65\n"
        "v_add_u32 v255, v255, v10\n"
        "v_lshrrev_b32 v20, 1, v15\n"
        "s_add_u32 m0, s40, 73728\n"
        "s_sub_u32 s64, s47, 12582912\n"
        "buffer_load_dwordx4 v242, s[8:11], s64 offen lds\n"
        "v_xor_b32 v20, v20, v16\n"
        "v_lshlrev_b32 v20, 4, v20\n"
        "v_mad_u32_u24 v240, v17, s41, v20\n"
        "v_lshl_or_b32 v251, v17, 7, v20\n"
        "v_add_u32 v240, 73728, v240\n"
        "v_add_u32 v251, 155648, v251\n"
        "v_xor_b32 v241, 64, v240\n"
        "v_xor_b32 v252, 64, v251\n"
        "s_lshl_b32 s67, s42, 10\n"
        "s_add_u32 s67, s67, 139264\n"
        "s_add_u32 m0, s40, 74752\n"
        "s_nop 0\n"
        "buffer_load_dwordx4 v243, s[8:11], s64 offen lds\n"
        "v_lshlrev_b32 v135, 2, v0\n"
        "v_add_u32 v135, 139264, v135\n"
        "v_lshlrev_b32 v11, 5, v16\n"
        "v_xor_b32 v11, v11, v18\n"
        "v_lshl_or_b32 v11, v17, 8, v11\n"
        "v_add_u32 v140, 122880, v11\n"
        "v_xor_b32 v141, 16, v140\n"
        "v_xor_b32 v142, 128, v140\n"
        "v_xor_b32 v143, 144, v140\n"
        "v_lshrrev_b32 v10, 2, v15\n"
        "s_add_u32 m0, s40, 75776\n"
        "s_nop 0\n"
        "buffer_load_dwordx4 v244, s[8:11], s64 offen lds\n"
        "v_cmp_eq_u32 vcc, v10, v16\n"
        "v_mov_b32 v11, 0x3fb9\n"
        "v_mov_b32 v189, 0\n"
        "v_and_b32 v10, 48, v2\n"
        "v_cndmask_b32 v188, 0, v11, vcc\n"
        "v_lshlrev_b64 v[188:189], v10, v[188:189]\n"
        "v_cmp_gt_u32 vcc, 32, v1\n"
        "v_mov_b32 v11, 15957\n"
        "v_lshrrev_b32 v149, 2, v15\n"
        "v_lshrrev_b32 v151, 3, v19\n"
        "v_mov_b32 v148, 14702\n"
        "v_cndmask_b32 v11, v148, v11, vcc\n"
        "v_xor_b32 v151, v149, v151\n"
        "v_cmp_eq_u32 vcc, 0, v151\n"
        "v_mov_b32 v149, 0\n"
        "v_cndmask_b32 v148, 0, v11, vcc\n"
        "v_lshlrev_b64 v[148:149], v10, v[148:149]\n"
        "v_cmp_eq_u32 vcc, 1, v151\n"
        "v_mov_b32 v151, 0\n"
        "v_cndmask_b32 v150, 0, v11, vcc\n"
        "v_lshlrev_b64 v[150:151], v10, v[150:151]\n"
        "v_and_b32 v10, 8, v1\n"
        "v_add_u32 v10, v10, v15\n"
        "v_mul_u32_u24 v10, 96, v10\n"
        "v_add3_u32 v137, v10, v19, s65\n"
        "v_and_b32 v237, 1, v1\n"
        "v_mul_u32_u24 v237, 0xaaaa, v237\n"
        "v_mov_b64 v[170:171], v[188:189]\n"
        "v_mov_b64 v[210:211], v[188:189]\n"
        "v_mov_b64 v[228:229], v[188:189]\n"
        "v_add_u32 v237, 0x4444, v237\n"
        "v_mov_b32 v11, 0x3f803f80\n"
        "v_accvgpr_write_b32 a252, v11\n"
        "v_accvgpr_write_b32 a253, v11\n"
        "v_accvgpr_write_b32 a254, v11\n"
        "v_accvgpr_write_b32 a255, v11\n"
        "v_lshl_or_b32 v10, v16, 10, v18\n"
        "s_mul_i32 s31, s42, 6144\n"
        "s_add_u32 s31, s31, 73728\n"
        "s_lshl_b32 s35, s51, 9\n"
        "s_add_u32 s35, s35, 73728\n"
        "v_lshl_or_b32 v11, v16, 2, v18\n"
        "v_add_u32 v247, s35, v10\n"
        "v_add_u32 v245, s31, v11\n"
        "v_mul_u32_u24 v10, 384, v16\n"
        "s_movk_i32 s45, 1728\n"
        "v_lshrrev_b32 v235, 2, v15\n"
        "v_lshrrev_b32 v254, 1, v15\n"
        "v_and_b32 v11, 1, v1\n"
        "v_mad_u32_u24 v235, v235, 48, v10\n"
        "v_and_b32 v248, 1, v254\n"
        "v_mad_u32_u24 v235, v248, s45, v235\n"
        "v_mul_u32_u24 v248, 36, v11\n"
        "v_lshlrev_b32 v11, 5, v11\n"
        "v_xor_b32 v254, v254, v248\n"
        "v_add3_u32 v235, v235, v11, s65\n"
        "v_mad_u32_u24 v254, v254, 48, v10\n"
        "v_add3_u32 v254, v254, 16, s65\n"
        "s_lshr_b32 s31, s42, 1\n"
        "s_mul_i32 s35, s31, 36864\n"
        "s_movk_i32 s45, 18048\n"
        "v_mad_u32_u24 v10, v15, 48, v10\n"
        "v_lshrrev_b32 v248, 3, v15\n"
        "v_mad_u32_u24 v10, v248, s45, v10\n"
        "v_add_u32 v248, s35, v10\n"
        "s_lshr_b32 s49, s42, 1\n"
        "s_mul_i32 s49, s49, -192\n"
        "s_add_u32 s49, s49, 128\n"
        "s_mul_i32 s31, s31, 256\n"
        "v_mul_u32_u24 v10, 196608, v16\n"
        "v_mul_u32_u24 v11, 8, v15\n"
        "v_add3_u32 v250, v10, v11, s31\n"
        "s_lshl_b32 s35, s51, 6\n"
        "s_add_u32 s66, s31, s35\n"
        "s_lshl_b32 s35, s42, 5\n"
        "s_sub_u32 s35, s35, s66\n"
        "s_add_u32 s27, s35, 64\n"
        "s_movk_i32 s31, 288\n"
        "s_movk_i32 s35, 768\n"
        "v_mad_u32_u24 v10, v4, s31, v2\n"
        "v_bfe_u32 v11, v10, 6, 1\n"
        "v_add_u32 v10, s66, v10\n"
        "v_mad_i32_i24 v10, v11, s27, v10\n"
        "v_bfe_u32 v11, v4, 1, 1\n"
        "v_bfe_u32 v232, v4, 2, 1\n"
        "v_sub_u32 v11, v11, v232\n"
        "v_mad_i32_i24 v232, v11, s35, v10\n"
        "v_mad_u32_u24 v10, v5, s31, v2\n"
        "v_bfe_u32 v11, v10, 6, 1\n"
        "v_add_u32 v10, s66, v10\n"
        "v_mad_i32_i24 v10, v11, s27, v10\n"
        "v_bfe_u32 v11, v5, 1, 1\n"
        "v_bfe_u32 v233, v5, 2, 1\n"
        "v_sub_u32 v11, v11, v233\n"
        "v_mad_i32_i24 v233, v11, s35, v10\n"
        "v_mad_u32_u24 v10, v6, s31, v2\n"
        "v_bfe_u32 v11, v10, 6, 1\n"
        "v_add_u32 v10, s66, v10\n"
        "v_mad_i32_i24 v10, v11, s27, v10\n"
        "v_bfe_u32 v11, v6, 1, 1\n"
        "v_bfe_u32 v234, v6, 2, 1\n"
        "v_sub_u32 v11, v11, v234\n"
        "v_mad_i32_i24 v234, v11, s35, v10\n"
        "s_mov_b32 s26, 0x3fb8aa3b\n"
        "s_mov_b32 s24, 0x5d800000\n"
        "s_mov_b32 s27, 14\n"
        "s_mov_b32 s28, 98304\n"
        "s_mov_b32 s29, 73728\n"
        "s_mov_b32 s30, 122880\n"
        "s_mov_b32 s45, 0\n"
        "s_mov_b32 s25, 0\n"
        "s_mov_b32 s54, 73728\n"
        "s_mov_b32 s55, 0x20000\n"
        "s_mov_b32 s18, 100663296\n"
        "s_mov_b32 s19, 0x20000\n"
        "s_mov_b32 s56, 36864\n"
        "s_mov_b32 s57, 73728\n"
        "s_mov_b32 s58, 110592\n"
        "s_mov_b32 s59, 147456\n"
        "s_mov_b32 s60, 184320\n"
        "s_mov_b32 s61, 24576\n"
        "s_mov_b32 s62, 61440\n"
        "s_mov_b32 s63, 12288\n"
        "s_mov_b32 s34, 49152\n"
        "s_add_u32 m0, s40, 86016\n"
        "s_sub_u32 s64, s47, 11796480\n"
        "buffer_load_dwordx4 v242, s[8:11], s64 offen lds\n"
        "s_waitcnt vmcnt(9)\n"
        "v_and_b32 v146, 0xffff0000, v144\n"
        "v_and_b32 v147, 0xffff0000, v145\n"
        "v_lshlrev_b32 v144, 16, v144\n"
        "v_lshlrev_b32 v145, 16, v145\n"
        "s_add_u32 m0, s40, 87040\n"
        "v_mul_f32 v146, s26, v146\n"
        "buffer_load_dwordx4 v243, s[8:11], s64 offen lds\n"
        "v_mul_f32 v147, s26, v147\n"
        "v_mul_f32 v144, s26, v144\n"
        "v_mul_f32 v145, s26, v145\n"
        "ds_write2st64_b32 v135, v144, v146 offset0:0 offset1:4\n"
        "ds_write2st64_b32 v135, v145, v147 offset0:8 offset1:12\n"
        "s_cmp_eq_u32 s42, 0\n"
        "s_cbranch_scc1 41f\n"
        "s_cmp_eq_u32 s42, 1\n"
        "s_cbranch_scc1 42f\n"
        "s_cmp_eq_u32 s42, 2\n"
        "s_cbranch_scc1 43f\n"
        "s_cmp_eq_u32 s42, 3\n"
        "s_cbranch_scc1 44f\n"
        "60:\n"
        "s_add_u32 m0, s40, 88064\n"
        "s_nop 0\n"
        "buffer_load_dwordx4 v244, s[8:11], s64 offen lds\n"
        "s_waitcnt lgkmcnt(0)\n"
        "s_waitcnt vmcnt(6)\n"
        "s_barrier\n"
        "s_add_u32 m0, s65, 0\n"
        "ds_read_b128 v[0:3], v140 offset:0\n"
        "buffer_load_dwordx4 v128, s[20:23], 0 offen lds\n"
        "buffer_load_dwordx4 v129, s[20:23], 0 offen offset:1024 lds\n"
        "ds_read_b128 v[4:7], v141 offset:0\n"
        "ds_read_b128 v[16:19], v140 offset:1024\n"
        "ds_read_b128 v[20:23], v141 offset:1024\n"
        "ds_read_b128 v[8:11], v142 offset:0\n"
        "ds_read_b128 v[24:27], v142 offset:1024\n"
        "ds_read_b128 v[12:15], v143 offset:0\n"
        "ds_read_b128 v[28:31], v143 offset:1024\n"
        "ds_read_b128 v[32:35], v140 offset:8192\n"
        "ds_read_b128 v[36:39], v141 offset:8192\n"
        "ds_read_b128 v[40:43], v142 offset:8192\n"
        "s_cmp_eq_u32 s42, 0\n"
        "s_cbranch_scc1 51f\n"
        "s_cmp_eq_u32 s42, 1\n"
        "s_cbranch_scc1 45f\n"
        "s_cmp_eq_u32 s42, 2\n"
        "s_cbranch_scc1 46f\n"
        "s_cmp_eq_u32 s42, 3\n"
        "s_cbranch_scc1 47f\n"
        "61:\n"
        "buffer_load_dwordx4 v130, s[20:23], 0 offen offset:2048 lds\n"
        "s_add_u32 m0, s65, 2688\n"
        "s_waitcnt lgkmcnt(10)\n"
        "v_lshl_add_u32 v0, v0, 2, s67\n"
        "buffer_load_dwordx4 v128, s[20:23], 0 offen offset:384 lds\n"
        "v_lshl_add_u32 v1, v1, 2, s67\n"
        "v_lshl_add_u32 v2, v2, 2, s67\n"
        "v_lshl_add_u32 v3, v3, 2, s67\n"
        "s_waitcnt lgkmcnt(9)\n"
        "v_lshl_add_u32 v4, v4, 2, s67\n"
        "v_lshl_add_u32 v5, v5, 2, s67\n"
        "v_lshl_add_u32 v6, v6, 2, s67\n"
        "v_lshl_add_u32 v7, v7, 2, s67\n"
        "s_waitcnt lgkmcnt(8)\n"
        "v_lshl_add_u32 v16, v16, 2, s67\n"
        "v_lshl_add_u32 v17, v17, 2, s67\n"
        "v_lshl_add_u32 v18, v18, 2, s67\n"
        "v_lshl_add_u32 v19, v19, 2, s67\n"
        "buffer_load_dwordx4 v129, s[20:23], 0 offen offset:1408 lds\n"
        "ds_read_b32 v0, v0\n"
        "ds_read_b32 v1, v1\n"
        "ds_read_b32 v2, v2\n"
        "ds_read_b32 v3, v3\n"
        "ds_read_b32 v4, v4\n"
        "ds_read_b32 v5, v5\n"
        "ds_read_b32 v6, v6\n"
        "ds_read_b32 v7, v7\n"
        "ds_read_b32 v16, v16\n"
        "ds_read_b32 v17, v17\n"
        "ds_read_b32 v18, v18\n"
        "ds_read_b32 v19, v19\n"
        "buffer_load_dwordx4 v130, s[20:23], 0 offen offset:2432 lds\n"
        "s_waitcnt lgkmcnt(15)\n"
        "v_lshl_add_u32 v20, v20, 2, s67\n"
        "v_lshl_add_u32 v21, v21, 2, s67\n"
        "v_lshl_add_u32 v22, v22, 2, s67\n"
        "v_lshl_add_u32 v23, v23, 2, s67\n"
        "v_lshl_add_u32 v8, v8, 2, s67\n"
        "v_lshl_add_u32 v9, v9, 2, s67\n"
        "v_lshl_add_u32 v10, v10, 2, s67\n"
        "v_lshl_add_u32 v11, v11, 2, s67\n"
        "v_lshl_add_u32 v24, v24, 2, s67\n"
        "v_lshl_add_u32 v25, v25, 2, s67\n"
        "v_lshl_add_u32 v26, v26, 2, s67\n"
        "v_lshl_add_u32 v27, v27, 2, s67\n"
        "ds_read_b32 v20, v20\n"
        "ds_read_b32 v21, v21\n"
        "ds_read_b32 v22, v22\n"
        "ds_read_b32 v23, v23\n"
        "ds_read_b32 v8, v8\n"
        "ds_read_b32 v9, v9\n"
        "ds_read_b32 v10, v10\n"
        "ds_read_b32 v11, v11\n"
        "ds_read_b32 v24, v24\n"
        "ds_read_b32 v25, v25\n"
        "ds_read_b32 v26, v26\n"
        "ds_read_b32 v27, v27\n"
        "s_add_u32 m0, s65, 6144\n"
        "v_lshl_add_u32 v12, v12, 2, s67\n"
        "buffer_load_dwordx4 v128, s[20:23], s56 offen lds\n"
        "v_lshl_add_u32 v13, v13, 2, s67\n"
        "v_lshl_add_u32 v14, v14, 2, s67\n"
        "v_lshl_add_u32 v15, v15, 2, s67\n"
        "v_lshl_add_u32 v28, v28, 2, s67\n"
        "v_lshl_add_u32 v29, v29, 2, s67\n"
        "v_lshl_add_u32 v30, v30, 2, s67\n"
        "v_lshl_add_u32 v31, v31, 2, s67\n"
        "s_waitcnt lgkmcnt(15)\n"
        "v_lshl_add_u32 v32, v32, 2, s67\n"
        "v_lshl_add_u32 v33, v33, 2, s67\n"
        "v_lshl_add_u32 v34, v34, 2, s67\n"
        "v_lshl_add_u32 v35, v35, 2, s67\n"
        "ds_read_b32 v12, v12\n"
        "ds_read_b32 v13, v13\n"
        "ds_read_b32 v14, v14\n"
        "ds_read_b32 v15, v15\n"
        "ds_read_b32 v28, v28\n"
        "ds_read_b32 v29, v29\n"
        "ds_read_b32 v30, v30\n"
        "ds_read_b32 v31, v31\n"
        "ds_read_b32 v32, v32\n"
        "ds_read_b32 v33, v33\n"
        "ds_read_b32 v34, v34\n"
        "ds_read_b32 v35, v35\n"
        "buffer_load_dwordx4 v129, s[20:23], s56 offen offset:1024 lds\n"
        "v_lshl_add_u32 v36, v36, 2, s67\n"
        "v_lshl_add_u32 v37, v37, 2, s67\n"
        "v_lshl_add_u32 v38, v38, 2, s67\n"
        "v_lshl_add_u32 v39, v39, 2, s67\n"
        "v_lshl_add_u32 v40, v40, 2, s67\n"
        "v_lshl_add_u32 v41, v41, 2, s67\n"
        "v_lshl_add_u32 v42, v42, 2, s67\n"
        "v_lshl_add_u32 v43, v43, 2, s67\n"
        "ds_read_b32 v36, v36\n"
        "ds_read_b32 v37, v37\n"
        "ds_read_b32 v38, v38\n"
        "ds_read_b32 v39, v39\n"
        "ds_read_b32 v40, v40\n"
        "ds_read_b32 v41, v41\n"
        "ds_read_b32 v42, v42\n"
        "ds_read_b32 v43, v43\n"
        "buffer_load_dwordx4 v130, s[20:23], s56 offen offset:2048 lds\n"
        "s_waitcnt vmcnt(9)\n"
        "s_barrier\n"
        "s_waitcnt vmcnt(6)\n"
        "ds_read_b128 a[212:215], v137 offset:0\n"
        "ds_read_b128 a[208:211], v137 offset:768\n"
        "ds_read_b128 a[204:207], v137 offset:32\n"
        "ds_read_b128 a[200:203], v137 offset:800\n"
        "ds_read_b128 a[196:199], v137 offset:64\n"
        "ds_read_b128 a[192:195], v137 offset:832\n"
        "s_add_u32 m0, s65, 8832\n"
        "s_nop 0\n"
        "buffer_load_dwordx4 v128, s[20:23], s56 offen offset:384 lds\n"
        "s_waitcnt lgkmcnt(5)\n"
        "v_mfma_f32_16x16x32_bf16 v[112:115], a[212:215], v[148:151], 0\n"
        "s_waitcnt lgkmcnt(4)\n"
        "ds_read_b128 a[216:219], v240 offset:0\n"
        "ds_read_b128 a[220:223], v240 offset:1536\n"
        "ds_read_b128 a[224:227], v240 offset:12288\n"
        "ds_read_b128 a[228:231], v240 offset:13824\n"
        "v_mfma_f32_16x16x32_bf16 v[116:119], a[208:211], v[148:151], 0\n"
        "s_waitcnt lgkmcnt(7)\n"
        "v_mfma_f32_16x16x32_bf16 v[120:123], a[204:207], v[148:151], 0\n"
        "s_waitcnt lgkmcnt(6)\n"
        "v_mfma_f32_16x16x32_bf16 v[124:127], a[200:203], v[148:151], 0\n"
        "s_waitcnt lgkmcnt(5)\n"
        "buffer_load_dwordx4 v129, s[20:23], s56 offen offset:1408 lds\n"
        "v_mfma_f32_16x16x32_bf16 v[96:99], a[196:199], v[148:151], 0\n"
        "s_waitcnt lgkmcnt(4)\n"
        "ds_read_b128 v[44:47], v143 offset:8192\n"
        "v_cvt_pk_bf16_f32 v112, v112, v113\n"
        "v_mfma_f32_16x16x32_bf16 v[100:103], a[192:195], v[148:151], 0\n"
        "s_waitcnt lgkmcnt(4)\n"
        "s_waitcnt vmcnt(5)\n"
        "ds_read_b64_tr_b16 a[72:73], v255 offset:3072\n"
        "ds_read_b64_tr_b16 a[74:75], v255 offset:3840\n"
        "v_accvgpr_write_b32 a0, v112\n"
        "v_cvt_pk_bf16_f32 v114, v114, v115\n"
        "v_accvgpr_write_b32 a1, v114\n"
        "v_cvt_pk_bf16_f32 v116, v116, v117\n"
        "v_accvgpr_write_b32 a2, v116\n"
        "v_cvt_pk_bf16_f32 v118, v118, v119\n"
        "v_accvgpr_write_b32 a3, v118\n"
        "s_nop 1\n"
        "v_mfma_f32_16x16x32_bf16 v[156:159], a[0:3], a[216:219], 0\n"
        "s_waitcnt lgkmcnt(5)\n"
        "ds_read_b64_tr_b16 a[96:97], v255 offset:3104\n"
        "ds_read_b64_tr_b16 a[98:99], v255 offset:3872\n"
        "v_mfma_f32_16x16x32_bf16 v[176:179], a[0:3], a[220:223], 0\n"
        "s_waitcnt lgkmcnt(5)\n"
        "buffer_load_dwordx4 v130, s[20:23], s56 offen offset:2432 lds\n"
        "ds_read_b64_tr_b16 a[120:121], v255 offset:3136\n"
        "ds_read_b64_tr_b16 a[122:123], v255 offset:3904\n"
        "v_mfma_f32_16x16x32_bf16 v[196:199], a[0:3], a[224:227], 0\n"
        "s_add_u32 m0, s65, 12288\n"
        "v_cvt_pk_bf16_f32 v120, v120, v121\n"
        "v_accvgpr_write_b32 a24, v120\n"
        "v_mfma_f32_16x16x32_bf16 v[216:219], a[0:3], a[228:231], 0\n"
        "s_waitcnt lgkmcnt(4)\n"
        "v_cvt_pk_bf16_f32 v122, v122, v123\n"
        "v_accvgpr_write_b32 a25, v122\n"
        "v_mfma_f32_16x16x32_bf16 v[152:155], a[72:75], a[216:219], 0\n"
        "buffer_load_dwordx4 v128, s[20:23], s57 offen lds\n"
        "s_waitcnt vmcnt(4)\n"
        "ds_read_b128 a[188:191], v137 offset:6144\n"
        "v_mfma_f32_16x16x32_bf16 v[172:175], a[72:75], a[220:223], 0\n"
        "ds_read_b128 a[184:187], v137 offset:6912\n"
        "ds_read_b128 a[180:183], v137 offset:6176\n"
        "v_cvt_pk_bf16_f32 v124, v124, v125\n"
        "v_accvgpr_write_b32 a26, v124\n"
        "v_cvt_pk_bf16_f32 v126, v126, v127\n"
        "v_accvgpr_write_b32 a27, v126\n"
        "s_nop 1\n"
        "v_mfma_f32_16x16x32_bf16 v[224:227], a[24:27], a[228:231], 0\n"
        "buffer_load_dwordx4 v129, s[20:23], s57 offen offset:1024 lds\n"
        "ds_read_b128 a[176:179], v137 offset:6944\n"
        "ds_read_b128 a[172:175], v137 offset:6208\n"
        "v_mfma_f32_16x16x32_bf16 v[204:207], a[24:27], a[224:227], 0\n"
        "ds_read_b128 a[168:171], v137 offset:6976\n"
        "ds_read_b128 a[232:235], v241 offset:0\n"
        "v_mfma_f32_16x16x32_bf16 v[184:187], a[24:27], a[220:223], 0\n"
        "ds_read_b128 a[236:239], v241 offset:1536\n"
        "ds_read_b128 a[240:243], v241 offset:12288\n"
        "v_mfma_f32_16x16x32_bf16 v[164:167], a[24:27], a[216:219], 0\n"
        "buffer_load_dwordx4 v130, s[20:23], s57 offen offset:2048 lds\n"
        "v_mfma_f32_16x16x32_bf16 v[192:195], a[72:75], a[224:227], 0\n"
        "s_add_u32 m0, s65, 14976\n"
        "ds_read_b128 a[244:247], v241 offset:13824\n"
        "v_cvt_pk_bf16_f32 v96, v96, v97\n"
        "v_mfma_f32_16x16x32_bf16 v[212:215], a[72:75], a[228:231], 0\n"
        "v_accvgpr_write_b32 a48, v96\n"
        "v_cvt_pk_bf16_f32 v98, v98, v99\n"
        "v_accvgpr_write_b32 a49, v98\n"
        "v_cvt_pk_bf16_f32 v100, v100, v101\n"
        "v_accvgpr_write_b32 a50, v100\n"
        "v_cvt_pk_bf16_f32 v102, v102, v103\n"
        "v_accvgpr_write_b32 a51, v102\n"
        "s_nop 1\n"
        "v_mfma_f32_16x16x32_bf16 v[160:163], a[48:51], a[216:219], 0\n"
        "v_lshl_add_u32 v44, v44, 2, s67\n"
        "v_lshl_add_u32 v45, v45, 2, s67\n"
        "v_mfma_f32_16x16x32_bf16 v[180:183], a[48:51], a[220:223], 0\n"
        "s_waitcnt lgkmcnt(5)\n"
        "ds_read_b32 v44, v44\n"
        "ds_read_b32 v45, v45\n"
        "v_mfma_f32_16x16x32_bf16 v[104:107], a[188:191], v[148:151], 0\n"
        "buffer_load_dwordx4 v128, s[20:23], s57 offen offset:384 lds\n"
        "v_mfma_f32_16x16x32_bf16 v[200:203], a[48:51], a[224:227], 0\n"
        "v_lshl_add_u32 v46, v46, 2, s67\n"
        "v_lshl_add_u32 v47, v47, 2, s67\n"
        "v_mfma_f32_16x16x32_bf16 v[108:111], a[184:187], v[148:151], 0\n"
        "ds_read_b32 v46, v46\n"
        "ds_read_b32 v47, v47\n"
        "v_mfma_f32_16x16x32_bf16 v[220:223], a[48:51], a[228:231], 0\n"
        "buffer_load_dwordx4 v129, s[20:23], s57 offen offset:1408 lds\n"
        "v_mfma_f32_16x16x32_bf16 v[88:91], a[96:99], a[228:231], 0\n"
        "ds_read_b128 v[48:51], v140 offset:9216\n"
        "v_cvt_pk_bf16_f32 v104, v104, v105\n"
        "v_mfma_f32_16x16x32_bf16 v[80:83], a[96:99], a[224:227], 0\n"
        "s_waitcnt vmcnt(5)\n"
        "ds_read_b64_tr_b16 a[76:77], v255 offset:9216\n"
        "ds_read_b64_tr_b16 a[78:79], v255 offset:9984\n"
        "v_mfma_f32_16x16x32_bf16 v[68:71], a[96:99], a[220:223], 0\n"
        "ds_read_b64_tr_b16 a[100:101], v255 offset:9248\n"
        "ds_read_b64_tr_b16 a[102:103], v255 offset:10016\n"
        "v_mfma_f32_16x16x32_bf16 v[64:67], a[96:99], a[216:219], 0\n"
        "buffer_load_dwordx4 v130, s[20:23], s57 offen offset:2432 lds\n"
        "ds_read_b64_tr_b16 a[124:125], v255 offset:9280\n"
        "ds_read_b64_tr_b16 a[126:127], v255 offset:10048\n"
        "v_mfma_f32_16x16x32_bf16 v[112:115], a[180:183], v[148:151], 0\n"
        "s_add_u32 m0, s65, 0\n"
        "v_accvgpr_write_b32 a4, v104\n"
        "v_cvt_pk_bf16_f32 v106, v106, v107\n"
        "v_mfma_f32_16x16x32_bf16 v[116:119], a[176:179], v[148:151], 0\n"
        "v_accvgpr_write_b32 a5, v106\n"
        "v_cvt_pk_bf16_f32 v108, v108, v109\n"
        "v_mfma_f32_16x16x32_bf16 v[72:75], a[120:123], a[216:219], 0\n"
        "buffer_load_dwordx4 v128, s[20:23], s58 offen lds\n"
        "v_mfma_f32_16x16x32_bf16 v[76:79], a[120:123], a[220:223], 0\n"
        "v_accvgpr_write_b32 a6, v108\n"
        "v_cvt_pk_bf16_f32 v110, v110, v111\n"
        "v_mfma_f32_16x16x32_bf16 v[84:87], a[120:123], a[224:227], 0\n"
        "v_accvgpr_write_b32 a7, v110\n"
        "v_cvt_pk_bf16_f32 v112, v112, v113\n"
        "v_mfma_f32_16x16x32_bf16 v[92:95], a[120:123], a[228:231], 0\n"
        "s_waitcnt lgkmcnt(2)\n"
        "v_accvgpr_write_b32 a28, v112\n"
        "v_cvt_pk_bf16_f32 v114, v114, v115\n"
        "v_mfma_f32_16x16x32_bf16 v[156:159], a[4:7], a[232:235], v[156:159]\n"
        "buffer_load_dwordx4 v129, s[20:23], s58 offen offset:1024 lds\n"
        "v_mfma_f32_16x16x32_bf16 v[176:179], a[4:7], a[236:239], v[176:179]\n"
        "v_lshl_add_u32 v48, v48, 2, s67\n"
        "v_lshl_add_u32 v49, v49, 2, s67\n"
        "v_mfma_f32_16x16x32_bf16 v[196:199], a[4:7], a[240:243], v[196:199]\n"
        "ds_read_b32 v48, v48\n"
        "ds_read_b32 v49, v49\n"
        "v_mfma_f32_16x16x32_bf16 v[216:219], a[4:7], a[244:247], v[216:219]\n"
        "v_lshl_add_u32 v50, v50, 2, s67\n"
        "v_lshl_add_u32 v51, v51, 2, s67\n"
        "v_mfma_f32_16x16x32_bf16 v[120:123], a[172:175], v[148:151], 0\n"
        "buffer_load_dwordx4 v130, s[20:23], s58 offen offset:2048 lds\n"
        "v_mfma_f32_16x16x32_bf16 v[124:127], a[168:171], v[148:151], 0\n"
        "s_add_u32 m0, s65, 2688\n"
        "ds_read_b32 v50, v50\n"
        "ds_read_b32 v51, v51\n"
        "v_mfma_f32_16x16x32_bf16 v[152:155], a[76:79], a[232:235], v[152:155]\n"
        ".long 0xbec200ff\n"
        "41:\n"
        "s_branch 60b\n"
        "ds_read_b128 a[248:251], v240 offset:128\n"
        "v_accvgpr_write_b32 a29, v114\n"
        "v_mfma_f32_16x16x32_bf16 v[172:175], a[76:79], a[236:239], v[172:175]\n"
        "s_waitcnt vmcnt(6)\n"
        "ds_read_b128 a[212:215], v137 offset:12288\n"
        "ds_read_b128 a[208:211], v137 offset:13056\n"
        "v_mfma_f32_16x16x32_bf16 v[192:195], a[76:79], a[240:243], v[192:195]\n"
        "buffer_load_dwordx4 v128, s[20:23], s58 offen offset:384 lds\n"
        "ds_read_b128 a[204:207], v137 offset:12320\n"
        "v_cvt_pk_bf16_f32 v116, v116, v117\n"
        "v_mfma_f32_16x16x32_bf16 v[212:215], a[76:79], a[244:247], v[212:215]\n"
        "ds_read_b128 a[200:203], v137 offset:13088\n"
        "ds_read_b128 a[196:199], v137 offset:12352\n"
        "v_accvgpr_write_b32 a30, v116\n"
        "v_cvt_pk_bf16_f32 v118, v118, v119\n"
        "v_accvgpr_write_b32 a31, v118\n"
        "s_nop 1\n"
        "v_mfma_f32_16x16x32_bf16 v[224:227], a[28:31], a[244:247], v[224:227]\n"
        "ds_read_b128 a[192:195], v137 offset:13120\n"
        "ds_read_b128 a[216:219], v240 offset:1664\n"
        "v_mfma_f32_16x16x32_bf16 v[204:207], a[28:31], a[240:243], v[204:207]\n"
        "buffer_load_dwordx4 v129, s[20:23], s58 offen offset:1408 lds\n"
        "v_mfma_f32_16x16x32_bf16 v[184:187], a[28:31], a[236:239], v[184:187]\n"
        "ds_read_b128 a[220:223], v240 offset:12416\n"
        "ds_read_b128 a[224:227], v240 offset:13952\n"
        "v_mfma_f32_16x16x32_bf16 v[164:167], a[28:31], a[232:235], v[164:167]\n"
        "s_waitcnt lgkmcnt(8)\n"
        "v_cvt_pk_bf16_f32 v120, v120, v121\n"
        "v_accvgpr_write_b32 a52, v120\n"
        "v_mfma_f32_16x16x32_bf16 v[96:99], a[212:215], v[148:151], 0\n"
        "s_waitcnt lgkmcnt(7)\n"
        "v_cvt_pk_bf16_f32 v122, v122, v123\n"
        "v_accvgpr_write_b32 a53, v122\n"
        "v_mfma_f32_16x16x32_bf16 v[100:103], a[208:211], v[148:151], 0\n"
        "buffer_load_dwordx4 v130, s[20:23], s58 offen offset:2432 lds\n"
        "v_mfma_f32_16x16x32_bf16 v[88:91], a[100:103], a[244:247], v[88:91]\n"
        "s_add_u32 m0, s65, 6144\n"
        "v_cvt_pk_bf16_f32 v124, v124, v125\n"
        "v_accvgpr_write_b32 a54, v124\n"
        "v_mfma_f32_16x16x32_bf16 v[80:83], a[100:103], a[240:243], v[80:83]\n"
        "v_cvt_pk_bf16_f32 v126, v126, v127\n"
        "v_accvgpr_write_b32 a55, v126\n"
        "v_mfma_f32_16x16x32_bf16 v[68:71], a[100:103], a[236:239], v[68:71]\n"
        "v_cvt_pk_bf16_f32 v96, v96, v97\n"
        "v_accvgpr_write_b32 a8, v96\n"
        "v_mfma_f32_16x16x32_bf16 v[64:67], a[100:103], a[232:235], v[64:67]\n"
        "buffer_load_dwordx4 v128, s[20:23], s59 offen lds\n"
        "v_mfma_f32_16x16x32_bf16 v[160:163], a[52:55], a[232:235], v[160:163]\n"
        "s_waitcnt vmcnt(7)\n"
        "ds_read_b64_tr_b16 a[80:81], v255 offset:15360\n"
        "ds_read_b64_tr_b16 a[82:83], v255 offset:16128\n"
        "v_mfma_f32_16x16x32_bf16 v[180:183], a[52:55], a[236:239], v[180:183]\n"
        "ds_read_b64_tr_b16 a[104:105], v255 offset:15392\n"
        "ds_read_b64_tr_b16 a[106:107], v255 offset:16160\n"
        "v_mfma_f32_16x16x32_bf16 v[200:203], a[52:55], a[240:243], v[200:203]\n"
        "ds_read_b64_tr_b16 a[128:129], v255 offset:15424\n"
        "ds_read_b64_tr_b16 a[130:131], v255 offset:16192\n"
        "v_mfma_f32_16x16x32_bf16 v[220:223], a[52:55], a[244:247], v[220:223]\n"
        "s_waitcnt lgkmcnt(6)\n"
        "v_cvt_pk_bf16_f32 v98, v98, v99\n"
        "v_accvgpr_write_b32 a9, v98\n"
        "v_mfma_f32_16x16x32_bf16 v[104:107], a[204:207], v[148:151], 0\n"
        "buffer_load_dwordx4 v129, s[20:23], s59 offen offset:1024 lds\n"
        "v_mfma_f32_16x16x32_bf16 v[108:111], a[200:203], v[148:151], 0\n"
        "v_cvt_pk_bf16_f32 v100, v100, v101\n"
        "v_accvgpr_write_b32 a10, v100\n"
        "v_mfma_f32_16x16x32_bf16 v[72:75], a[124:127], a[232:235], v[72:75]\n"
        "v_cvt_pk_bf16_f32 v102, v102, v103\n"
        "v_accvgpr_write_b32 a11, v102\n"
        "v_mfma_f32_16x16x32_bf16 v[76:79], a[124:127], a[236:239], v[76:79]\n"
        "buffer_load_dwordx4 v130, s[20:23], s59 offen offset:2048 lds\n"
        "v_mfma_f32_16x16x32_bf16 v[84:87], a[124:127], a[240:243], v[84:87]\n"
        "s_add_u32 m0, s65, 8832\n"
        "v_cvt_pk_bf16_f32 v104, v104, v105\n"
        "v_accvgpr_write_b32 a32, v104\n"
        "v_mfma_f32_16x16x32_bf16 v[92:95], a[124:127], a[244:247], v[92:95]\n"
        "s_waitcnt vmcnt(6)\n"
        "ds_read_b128 a[188:191], v137 offset:0\n"
        "ds_read_b128 a[184:187], v137 offset:768\n"
        "v_mfma_f32_16x16x32_bf16 v[156:159], a[8:11], a[248:251], v[156:159]\n"
        "buffer_load_dwordx4 v128, s[20:23], s59 offen offset:384 lds\n"
        "ds_read_b128 a[180:183], v137 offset:32\n"
        "v_mfma_f32_16x16x32_bf16 v[176:179], a[8:11], a[216:219], v[176:179]\n"
        "ds_read_b128 a[176:179], v137 offset:800\n"
        "ds_read_b128 a[172:175], v137 offset:64\n"
        "v_mfma_f32_16x16x32_bf16 v[196:199], a[8:11], a[220:223], v[196:199]\n"
        "ds_read_b128 a[168:171], v137 offset:832\n"
        "v_cvt_pk_bf16_f32 v106, v106, v107\n"
        "v_mfma_f32_16x16x32_bf16 v[216:219], a[8:11], a[224:227], v[216:219]\n"
        "v_accvgpr_write_b32 a33, v106\n"
        "v_cvt_pk_bf16_f32 v108, v108, v109\n"
        "v_mfma_f32_16x16x32_bf16 v[112:115], a[196:199], v[148:151], 0\n"
        "buffer_load_dwordx4 v129, s[20:23], s59 offen offset:1408 lds\n"
        "v_mfma_f32_16x16x32_bf16 v[116:119], a[192:195], v[148:151], 0\n"
        "s_waitcnt lgkmcnt(6)\n"
        "v_accvgpr_write_b32 a34, v108\n"
        "v_cvt_pk_bf16_f32 v110, v110, v111\n"
        "v_mfma_f32_16x16x32_bf16 v[152:155], a[80:83], a[248:251], v[152:155]\n"
        "ds_read_b128 a[228:231], v241 offset:128\n"
        "v_accvgpr_write_b32 a35, v110\n"
        "v_mfma_f32_16x16x32_bf16 v[172:175], a[80:83], a[216:219], v[172:175]\n"
        "ds_read_b128 a[232:235], v241 offset:1664\n"
        "v_cvt_pk_bf16_f32 v112, v112, v113\n"
        "v_mfma_f32_16x16x32_bf16 v[192:195], a[80:83], a[220:223], v[192:195]\n"
        "buffer_load_dwordx4 v130, s[20:23], s59 offen offset:2432 lds\n"
        "v_mfma_f32_16x16x32_bf16 v[212:215], a[80:83], a[224:227], v[212:215]\n"
        "s_add_u32 m0, s65, 12288\n"
        "ds_read_b128 a[236:239], v241 offset:12416\n"
        "ds_read_b128 a[240:243], v241 offset:13952\n"
        "v_mfma_f32_16x16x32_bf16 v[224:227], a[32:35], a[224:227], v[224:227]\n"
        "ds_read_b128 v[52:55], v141 offset:9216\n"
        "v_accvgpr_write_b32 a56, v112\n"
        "v_mfma_f32_16x16x32_bf16 v[204:207], a[32:35], a[220:223], v[204:207]\n"
        "v_cvt_pk_bf16_f32 v114, v114, v115\n"
        "v_accvgpr_write_b32 a57, v114\n"
        "v_mfma_f32_16x16x32_bf16 v[184:187], a[32:35], a[216:219], v[184:187]\n"
        "buffer_load_dwordx4 v128, s[20:23], s60 offen lds\n"
        "v_mfma_f32_16x16x32_bf16 v[164:167], a[32:35], a[248:251], v[164:167]\n"
        "s_waitcnt lgkmcnt(4)\n"
        "v_cvt_pk_bf16_f32 v116, v116, v117\n"
        "v_accvgpr_write_b32 a58, v116\n"
        "v_mfma_f32_16x16x32_bf16 v[120:123], a[188:191], v[148:151], 0\n"
        "s_waitcnt vmcnt(7)\n"
        "ds_read_b64_tr_b16 a[84:85], v255 offset:3072\n"
        "ds_read_b64_tr_b16 a[86:87], v255 offset:3840\n"
        "v_mfma_f32_16x16x32_bf16 v[124:127], a[184:187], v[148:151], 0\n"
        "ds_read_b64_tr_b16 a[108:109], v255 offset:3104\n"
        "ds_read_b64_tr_b16 a[110:111], v255 offset:3872\n"
        "v_mfma_f32_16x16x32_bf16 v[88:91], a[104:107], a[224:227], v[88:91]\n"
        "buffer_load_dwordx4 v129, s[20:23], s60 offen offset:1024 lds\n"
        "ds_read_b64_tr_b16 a[132:133], v255 offset:3136\n"
        "ds_read_b64_tr_b16 a[134:135], v255 offset:3904\n"
        "v_mfma_f32_16x16x32_bf16 v[80:83], a[104:107], a[220:223], v[80:83]\n"
        "v_cvt_pk_bf16_f32 v118, v118, v119\n"
        "v_accvgpr_write_b32 a59, v118\n"
        "v_mfma_f32_16x16x32_bf16 v[68:71], a[104:107], a[216:219], v[68:71]\n"
        "v_cvt_pk_bf16_f32 v120, v120, v121\n"
        "v_accvgpr_write_b32 a12, v120\n"
        "v_mfma_f32_16x16x32_bf16 v[64:67], a[104:107], a[248:251], v[64:67]\n"
        "buffer_load_dwordx4 v130, s[20:23], s60 offen offset:2048 lds\n"
        "v_mfma_f32_16x16x32_bf16 v[160:163], a[56:59], a[248:251], v[160:163]\n"
        "s_add_u32 m0, s65, 14976\n"
        "v_cvt_pk_bf16_f32 v122, v122, v123\n"
        "v_accvgpr_write_b32 a13, v122\n"
        "v_mfma_f32_16x16x32_bf16 v[180:183], a[56:59], a[216:219], v[180:183]\n"
        "s_waitcnt lgkmcnt(4)\n"
        "v_lshl_add_u32 v52, v52, 2, s67\n"
        "v_lshl_add_u32 v53, v53, 2, s67\n"
        "v_mfma_f32_16x16x32_bf16 v[200:203], a[56:59], a[220:223], v[200:203]\n"
        "ds_read_b32 v52, v52\n"
        "ds_read_b32 v53, v53\n"
        "v_mfma_f32_16x16x32_bf16 v[220:223], a[56:59], a[224:227], v[220:223]\n"
        "buffer_load_dwordx4 v128, s[20:23], s60 offen offset:384 lds\n"
        "v_mfma_f32_16x16x32_bf16 v[96:99], a[180:183], v[148:151], 0\n"
        "v_lshl_add_u32 v54, v54, 2, s67\n"
        "v_lshl_add_u32 v55, v55, 2, s67\n"
        "v_mfma_f32_16x16x32_bf16 v[100:103], a[176:179], v[148:151], 0\n"
        "ds_read_b32 v54, v54\n"
        "ds_read_b32 v55, v55\n"
        "v_mfma_f32_16x16x32_bf16 v[72:75], a[128:131], a[248:251], v[72:75]\n"
        "v_cvt_pk_bf16_f32 v124, v124, v125\n"
        "v_accvgpr_write_b32 a14, v124\n"
        "v_mfma_f32_16x16x32_bf16 v[76:79], a[128:131], a[216:219], v[76:79]\n"
        "buffer_load_dwordx4 v129, s[20:23], s60 offen offset:1408 lds\n"
        "v_mfma_f32_16x16x32_bf16 v[84:87], a[128:131], a[220:223], v[84:87]\n"
        "v_cvt_pk_bf16_f32 v126, v126, v127\n"
        "v_accvgpr_write_b32 a15, v126\n"
        "v_mfma_f32_16x16x32_bf16 v[92:95], a[128:131], a[224:227], v[92:95]\n"
        "v_cvt_pk_bf16_f32 v96, v96, v97\n"
        "v_accvgpr_write_b32 a36, v96\n"
        "v_mfma_f32_16x16x32_bf16 v[156:159], a[12:15], a[228:231], v[156:159]\n"
        "s_waitcnt vmcnt(8)\n"
        "ds_read_b128 a[212:215], v137 offset:6144\n"
        "ds_read_b128 a[208:211], v137 offset:6912\n"
        "v_mfma_f32_16x16x32_bf16 v[176:179], a[12:15], a[232:235], v[176:179]\n"
        "buffer_load_dwordx4 v130, s[20:23], s60 offen offset:2432 lds\n"
        "ds_read_b128 a[204:207], v137 offset:6176\n"
        "v_mfma_f32_16x16x32_bf16 v[196:199], a[12:15], a[236:239], v[196:199]\n"
        "ds_read_b128 a[200:203], v137 offset:6944\n"
        "ds_read_b128 a[196:199], v137 offset:6208\n"
        "v_mfma_f32_16x16x32_bf16 v[216:219], a[12:15], a[240:243], v[216:219]\n"
        "ds_read_b128 a[192:195], v137 offset:6976\n"
        "v_cvt_pk_bf16_f32 v98, v98, v99\n"
        "v_mfma_f32_16x16x32_bf16 v[104:107], a[172:175], v[148:151], 0\n"
        "v_accvgpr_write_b32 a37, v98\n"
        "v_cvt_pk_bf16_f32 v100, v100, v101\n"
        "v_mfma_f32_16x16x32_bf16 v[108:111], a[168:171], v[148:151], 0\n"
        "v_accvgpr_write_b32 a38, v100\n"
        "v_cvt_pk_bf16_f32 v102, v102, v103\n"
        "v_mfma_f32_16x16x32_bf16 v[152:155], a[84:87], a[228:231], v[152:155]\n"
        "ds_read_b128 a[244:247], v240 offset:256\n"
        "v_accvgpr_write_b32 a39, v102\n"
        "v_mfma_f32_16x16x32_bf16 v[172:175], a[84:87], a[232:235], v[172:175]\n"
        "s_mov_b32 m0, s48\n"
        "ds_read_b128 a[248:251], v240 offset:1792\n"
        "v_cvt_pk_bf16_f32 v104, v104, v105\n"
        "v_mfma_f32_16x16x32_bf16 v[192:195], a[84:87], a[236:239], v[192:195]\n"
        "s_sub_u32 s64, s36, 4194304\n"
        "ds_read_b128 a[216:219], v240 offset:12544\n"
        "v_accvgpr_write_b32 a60, v104\n"
        "v_mfma_f32_16x16x32_bf16 v[212:215], a[84:87], a[240:243], v[212:215]\n"
        "buffer_load_dwordx4 v249, s[12:15], s64 offen lds\n"
        "v_mfma_f32_16x16x32_bf16 v[224:227], a[36:39], a[240:243], v[224:227]\n"
        "ds_read_b128 a[220:223], v240 offset:14080\n"
        "v_cvt_pk_bf16_f32 v106, v106, v107\n"
        "v_mfma_f32_16x16x32_bf16 v[204:207], a[36:39], a[236:239], v[204:207]\n"
        "ds_read_b128 v[56:59], v142 offset:9216\n"
        "v_accvgpr_write_b32 a61, v106\n"
        "v_mfma_f32_16x16x32_bf16 v[184:187], a[36:39], a[232:235], v[184:187]\n"
        "s_waitcnt vmcnt(7)\n"
        "ds_read_b64_tr_b16 a[88:89], v255 offset:9216\n"
        "ds_read_b64_tr_b16 a[90:91], v255 offset:9984\n"
        "v_mfma_f32_16x16x32_bf16 v[164:167], a[36:39], a[228:231], v[164:167]\n"
        "s_waitcnt lgkmcnt(6)\n"
        "ds_read_b64_tr_b16 a[112:113], v255 offset:9248\n"
        "ds_read_b64_tr_b16 a[114:115], v255 offset:10016\n"
        "v_mfma_f32_16x16x32_bf16 v[112:115], a[212:215], v[148:151], 0\n"
        "buffer_load_dwordx4 v253, s[12:15], s64 offen offset:1024 lds\n"
        "ds_read_b64_tr_b16 a[136:137], v255 offset:9280\n"
        "v_mfma_f32_16x16x32_bf16 v[116:119], a[208:211], v[148:151], 0\n"
        "ds_read_b64_tr_b16 a[138:139], v255 offset:10048\n"
        "s_add_u32 m0, s65, 0\n"
        "v_cvt_pk_bf16_f32 v108, v108, v109\n"
        "v_mfma_f32_16x16x32_bf16 v[88:91], a[108:111], a[240:243], v[88:91]\n"
        "s_mov_b32 s64, 768\n"
        "v_accvgpr_write_b32 a62, v108\n"
        "v_cvt_pk_bf16_f32 v110, v110, v111\n"
        "v_mfma_f32_16x16x32_bf16 v[80:83], a[108:111], a[236:239], v[80:83]\n"
        "buffer_load_dwordx4 v128, s[20:23], s64 offen lds\n"
        "v_mfma_f32_16x16x32_bf16 v[68:71], a[108:111], a[232:235], v[68:71]\n"
        "v_accvgpr_write_b32 a63, v110\n"
        "v_cvt_pk_bf16_f32 v112, v112, v113\n"
        "v_mfma_f32_16x16x32_bf16 v[64:67], a[108:111], a[228:231], v[64:67]\n"
        "v_accvgpr_write_b32 a16, v112\n"
        "v_cvt_pk_bf16_f32 v114, v114, v115\n"
        "v_mfma_f32_16x16x32_bf16 v[160:163], a[60:63], a[228:231], v[160:163]\n"
        "v_accvgpr_write_b32 a17, v114\n"
        "v_cvt_pk_bf16_f32 v116, v116, v117\n"
        "v_mfma_f32_16x16x32_bf16 v[180:183], a[60:63], a[232:235], v[180:183]\n"
        "buffer_load_dwordx4 v129, s[20:23], s64 offen offset:1024 lds\n"
        "v_mfma_f32_16x16x32_bf16 v[200:203], a[60:63], a[236:239], v[200:203]\n"
        "ds_read_b128 v[60:63], v143 offset:9216\n"
        "v_accvgpr_write_b32 a18, v116\n"
        "v_mfma_f32_16x16x32_bf16 v[220:223], a[60:63], a[240:243], v[220:223]\n"
        "v_cvt_pk_bf16_f32 v118, v118, v119\n"
        "v_accvgpr_write_b32 a19, v118\n"
        "v_mfma_f32_16x16x32_bf16 v[120:123], a[204:207], v[148:151], 0\n"
        "s_waitcnt lgkmcnt(1)\n"
        "v_lshl_add_u32 v56, v56, 2, s67\n"
        "v_lshl_add_u32 v57, v57, 2, s67\n"
        "v_mfma_f32_16x16x32_bf16 v[124:127], a[200:203], v[148:151], 0\n"
        "buffer_load_dwordx4 v130, s[20:23], s64 offen offset:2048 lds\n"
        "v_mfma_f32_16x16x32_bf16 v[72:75], a[132:135], a[228:231], v[72:75]\n"
        "s_add_u32 m0, s65, 2304\n"
        "ds_read_b32 v56, v56\n"
        "ds_read_b32 v57, v57\n"
        "v_mfma_f32_16x16x32_bf16 v[76:79], a[132:135], a[232:235], v[76:79]\n"
        "v_lshl_add_u32 v58, v58, 2, s67\n"
        "v_lshl_add_u32 v59, v59, 2, s67\n"
        "v_mfma_f32_16x16x32_bf16 v[84:87], a[132:135], a[236:239], v[84:87]\n"
        "ds_read_b32 v58, v58\n"
        "ds_read_b32 v59, v59\n"
        "v_mfma_f32_16x16x32_bf16 v[92:95], a[132:135], a[240:243], v[92:95]\n"
        "buffer_load_dwordx4 v128, s[20:23], s56 offen offset:768 lds\n"
        "v_mfma_f32_16x16x32_bf16 v[156:159], a[16:19], a[244:247], v[156:159]\n"
        "v_cvt_pk_bf16_f32 v120, v120, v121\n"
        "v_accvgpr_write_b32 a40, v120\n"
        "v_mfma_f32_16x16x32_bf16 v[176:179], a[16:19], a[248:251], v[176:179]\n"
        "v_cvt_pk_bf16_f32 v122, v122, v123\n"
        "v_accvgpr_write_b32 a41, v122\n"
        "v_mfma_f32_16x16x32_bf16 v[196:199], a[16:19], a[216:219], v[196:199]\n"
        "v_cvt_pk_bf16_f32 v124, v124, v125\n"
        "v_accvgpr_write_b32 a42, v124\n"
        "v_mfma_f32_16x16x32_bf16 v[216:219], a[16:19], a[220:223], v[216:219]\n"
        "buffer_load_dwordx4 v129, s[20:23], s56 offen offset:1792 lds\n"
        "v_mfma_f32_16x16x32_bf16 v[96:99], a[196:199], v[148:151], 0\n"
        "ds_read_b128 a[224:227], v241 offset:256\n"
        "v_cvt_pk_bf16_f32 v126, v126, v127\n"
        "v_mfma_f32_16x16x32_bf16 v[100:103], a[192:195], v[148:151], 0\n"
        "s_waitcnt vmcnt(10)\n"
        "ds_read_b128 a[188:191], v137 offset:12288\n"
        "ds_read_b128 a[184:187], v137 offset:13056\n"
        "v_mfma_f32_16x16x32_bf16 v[152:155], a[88:91], a[244:247], v[152:155]\n"
        "ds_read_b128 a[180:183], v137 offset:12320\n"
        "ds_read_b128 a[176:179], v137 offset:13088\n"
        "v_mfma_f32_16x16x32_bf16 v[172:175], a[88:91], a[248:251], v[172:175]\n"
        "buffer_load_dwordx4 v130, s[20:23], s56 offen offset:2816 lds\n"
        "ds_read_b128 a[172:175], v137 offset:12352\n"
        "v_mfma_f32_16x16x32_bf16 v[192:195], a[88:91], a[216:219], v[192:195]\n"
        "ds_read_b128 a[168:171], v137 offset:13120\n"
        "s_add_u32 m0, s65, 5376\n"
        "ds_read_b128 a[228:231], v241 offset:1792\n"
        "v_accvgpr_write_b32 a43, v126\n"
        "v_mfma_f32_16x16x32_bf16 v[212:215], a[88:91], a[220:223], v[212:215]\n"
        "ds_read_b128 a[232:235], v241 offset:12544\n"
        "ds_read_b128 a[236:239], v241 offset:14080\n"
        "v_mfma_f32_16x16x32_bf16 v[224:227], a[40:43], a[220:223], v[224:227]\n"
        "s_waitcnt lgkmcnt(10)\n"
        "v_lshl_add_u32 v60, v60, 2, s67\n"
        "v_lshl_add_u32 v61, v61, 2, s67\n"
        "v_lshl_add_u32 v62, v62, 2, s67\n"
        "v_mfma_f32_16x16x32_bf16 v[204:207], a[40:43], a[216:219], v[204:207]\n"
        "buffer_load_dwordx4 v128, s[20:23], s57 offen offset:768 lds\n"
        "v_lshl_add_u32 v63, v63, 2, s67\n"
        "v_mfma_f32_16x16x32_bf16 v[184:187], a[40:43], a[248:251], v[184:187]\n"
        "ds_read_b32 v60, v60\n"
        "ds_read_b32 v61, v61\n"
        "v_mfma_f32_16x16x32_bf16 v[164:167], a[40:43], a[244:247], v[164:167]\n"
        "s_waitcnt lgkmcnt(7)\n"
        "ds_read_b32 v62, v62\n"
        "ds_read_b32 v63, v63\n"
        "v_mfma_f32_16x16x32_bf16 v[104:107], a[188:191], v[148:151], 0\n"
        "v_cvt_pk_bf16_f32 v96, v96, v97\n"
        "v_accvgpr_write_b32 a64, v96\n"
        "v_mfma_f32_16x16x32_bf16 v[108:111], a[184:187], v[148:151], 0\n"
        "buffer_load_dwordx4 v129, s[20:23], s57 offen offset:1792 lds\n"
        "v_mfma_f32_16x16x32_bf16 v[88:91], a[112:115], a[220:223], v[88:91]\n"
        "v_cvt_pk_bf16_f32 v98, v98, v99\n"
        "v_accvgpr_write_b32 a65, v98\n"
        "v_mfma_f32_16x16x32_bf16 v[80:83], a[112:115], a[216:219], v[80:83]\n"
        "v_cvt_pk_bf16_f32 v100, v100, v101\n"
        "v_accvgpr_write_b32 a66, v100\n"
        "v_mfma_f32_16x16x32_bf16 v[68:71], a[112:115], a[248:251], v[68:71]\n"
        "v_cvt_pk_bf16_f32 v102, v102, v103\n"
        "v_accvgpr_write_b32 a67, v102\n"
        "v_mfma_f32_16x16x32_bf16 v[64:67], a[112:115], a[244:247], v[64:67]\n"
        "s_waitcnt vmcnt(10)\n"
        "ds_read_b64_tr_b16 a[92:93], v255 offset:15360\n"
        "ds_read_b64_tr_b16 a[94:95], v255 offset:16128\n"
        "v_mfma_f32_16x16x32_bf16 v[160:163], a[64:67], a[244:247], v[160:163]\n"
        "ds_read_b64_tr_b16 a[116:117], v255 offset:15392\n"
        "ds_read_b64_tr_b16 a[118:119], v255 offset:16160\n"
        "v_mfma_f32_16x16x32_bf16 v[180:183], a[64:67], a[248:251], v[180:183]\n"
        "ds_read_b64_tr_b16 a[140:141], v255 offset:15424\n"
        "ds_read_b64_tr_b16 a[142:143], v255 offset:16192\n"
        "v_mfma_f32_16x16x32_bf16 v[200:203], a[64:67], a[216:219], v[200:203]\n"
        "v_cvt_pk_bf16_f32 v104, v104, v105\n"
        "v_accvgpr_write_b32 a20, v104\n"
        "v_mfma_f32_16x16x32_bf16 v[220:223], a[64:67], a[220:223], v[220:223]\n"
        "v_cvt_pk_bf16_f32 v106, v106, v107\n"
        "v_accvgpr_write_b32 a21, v106\n"
        "v_mfma_f32_16x16x32_bf16 v[112:115], a[180:183], v[148:151], 0\n"
        "v_cvt_pk_bf16_f32 v108, v108, v109\n"
        "v_accvgpr_write_b32 a22, v108\n"
        "v_mfma_f32_16x16x32_bf16 v[116:119], a[176:179], v[148:151], 0\n"
        "v_cvt_pk_bf16_f32 v110, v110, v111\n"
        "v_accvgpr_write_b32 a23, v110\n"
        "v_mfma_f32_16x16x32_bf16 v[72:75], a[136:139], a[244:247], v[72:75]\n"
        "v_mfma_f32_16x16x32_bf16 v[76:79], a[136:139], a[248:251], v[76:79]\n"
        "v_mfma_f32_16x16x32_bf16 v[156:159], a[20:23], a[224:227], v[156:159]\n"
        "s_waitcnt lgkmcnt(0)\n"
        "v_cvt_pk_bf16_f32 v112, v112, v113\n"
        "v_accvgpr_write_b32 a44, v112\n"
        "v_mfma_f32_16x16x32_bf16 v[176:179], a[20:23], a[228:231], v[176:179]\n"
        "v_cvt_pk_bf16_f32 v114, v114, v115\n"
        "v_accvgpr_write_b32 a45, v114\n"
        "v_mfma_f32_16x16x32_bf16 v[196:199], a[20:23], a[232:235], v[196:199]\n"
        "v_cvt_pk_bf16_f32 v116, v116, v117\n"
        "v_accvgpr_write_b32 a46, v116\n"
        "v_cvt_pk_bf16_f32 v156, v156, v157\n"
        "v_mfma_f32_16x16x32_bf16 v[216:219], a[20:23], a[236:239], v[216:219]\n"
        "v_cvt_pk_bf16_f32 v157, v158, v159\n"
        "v_cvt_pk_bf16_f32 v176, v176, v177\n"
        "v_cvt_pk_bf16_f32 v177, v178, v179\n"
        "v_mfma_f32_16x16x32_bf16 v[120:123], a[172:175], v[148:151], 0\n"
        "v_cvt_pk_bf16_f32 v196, v196, v197\n"
        "v_cvt_pk_bf16_f32 v197, v198, v199\n"
        "v_mfma_f32_16x16x32_bf16 v[124:127], a[168:171], v[148:151], 0\n"
        "v_cvt_pk_bf16_f32 v118, v118, v119\n"
        "v_cvt_pk_bf16_f32 v216, v216, v217\n"
        "v_cvt_pk_bf16_f32 v217, v218, v219\n"
        "v_mfma_f32_16x16x32_bf16 v[84:87], a[136:139], a[216:219], v[84:87]\n"
        "v_accvgpr_write_b32 a47, v118\n"
        "v_cvt_pk_bf16_f32 v120, v120, v121\n"
        "v_mfma_f32_16x16x32_bf16 v[92:95], a[136:139], a[220:223], v[92:95]\n"
        "v_accvgpr_write_b32 a68, v120\n"
        "v_cvt_pk_bf16_f32 v122, v122, v123\n"
        "v_mfma_f32_16x16x32_bf16 v[224:227], a[44:47], a[236:239], v[224:227]\n"
        "v_accvgpr_write_b32 a69, v122\n"
        "v_cvt_pk_bf16_f32 v124, v124, v125\n"
        "v_mfma_f32_16x16x32_bf16 v[204:207], a[44:47], a[232:235], v[204:207]\n"
        "v_accvgpr_write_b32 a70, v124\n"
        "v_cvt_pk_bf16_f32 v126, v126, v127\n"
        "v_mfma_f32_16x16x32_bf16 v[184:187], a[44:47], a[228:231], v[184:187]\n"
        "v_accvgpr_write_b32 a71, v126\n"
        "s_nop 0\n"
        "v_cvt_pk_bf16_f32 v218, v224, v225\n"
        "v_mfma_f32_16x16x32_bf16 v[164:167], a[44:47], a[224:227], v[164:167]\n"
        "v_cvt_pk_bf16_f32 v219, v226, v227\n"
        "v_cvt_pk_bf16_f32 v198, v204, v205\n"
        "v_cvt_pk_bf16_f32 v199, v206, v207\n"
        "v_mfma_f32_16x16x32_bf16 v[152:155], a[92:95], a[224:227], v[152:155]\n"
        "v_cvt_pk_bf16_f32 v178, v184, v185\n"
        "v_cvt_pk_bf16_f32 v179, v186, v187\n"
        "v_mfma_f32_16x16x32_bf16 v[64:67], a[116:119], a[224:227], v[64:67]\n"
        "buffer_load_dwordx4 v130, s[20:23], s57 offen offset:2816 lds\n"
        "v_cvt_pk_bf16_f32 v158, v164, v165\n"
        "v_mfma_f32_16x16x32_bf16 v[72:75], a[140:143], a[224:227], v[72:75]\n"
        "s_add_u32 m0, s65, 8448\n"
        "v_cvt_pk_bf16_f32 v159, v166, v167\n"
        "v_cvt_pk_bf16_f32 v152, v152, v153\n"
        "v_mfma_f32_16x16x32_bf16 v[172:175], a[92:95], a[228:231], v[172:175]\n"
        "v_cvt_pk_bf16_f32 v153, v154, v155\n"
        "v_cvt_pk_bf16_f32 v154, v64, v65\n"
        "v_mfma_f32_16x16x32_bf16 v[160:163], a[68:71], a[224:227], v[160:163]\n"
        "v_cvt_pk_bf16_f32 v155, v66, v67\n"
        "v_cvt_pk_bf16_f32 v168, v72, v73\n"
        "v_mfma_f32_16x16x32_bf16 v[180:183], a[68:71], a[228:231], v[180:183]\n"
        "buffer_load_dwordx4 v128, s[20:23], s58 offen offset:768 lds\n"
        "v_mfma_f32_16x16x32_bf16 v[200:203], a[68:71], a[232:235], v[200:203]\n"
        "v_cvt_pk_bf16_f32 v169, v74, v75\n"
        "v_cvt_pk_bf16_f32 v172, v172, v173\n"
        "v_mfma_f32_16x16x32_bf16 v[220:223], a[68:71], a[236:239], v[220:223]\n"
        "v_cvt_pk_bf16_f32 v166, v160, v161\n"
        "v_cvt_pk_bf16_f32 v160, v160, v161\n"
        "v_cvt_pk_bf16_f32 v161, v162, v163\n"
        "v_cvt_pk_bf16_f32 v167, v162, v163\n"
        "v_cvt_pk_bf16_f32 v186, v180, v181\n"
        "v_cvt_pk_bf16_f32 v180, v180, v181\n"
        "v_mfma_f32_16x16x32_bf16 v[68:71], a[116:119], a[228:231], v[68:71]\n"
        "buffer_load_dwordx4 v129, s[20:23], s58 offen offset:1792 lds\n"
        "v_cvt_pk_bf16_f32 v181, v182, v183\n"
        "v_cvt_pk_bf16_f32 v187, v182, v183\n"
        "v_cvt_pk_bf16_f32 v206, v200, v201\n"
        "v_cvt_pk_bf16_f32 v200, v200, v201\n"
        "v_mfma_f32_16x16x32_bf16 v[76:79], a[140:143], a[228:231], v[76:79]\n"
        "s_waitcnt vmcnt(11)\n"
        "s_barrier\n"
        "ds_read_b128 v[162:165], v251 offset:0\n"
        "ds_read_b128 v[182:185], v251 offset:512\n"
        "v_cvt_pk_bf16_f32 v201, v202, v203\n"
        "v_cvt_pk_bf16_f32 v207, v202, v203\n"
        "v_cvt_pk_bf16_f32 v226, v220, v221\n"
        "v_cvt_pk_bf16_f32 v220, v220, v221\n"
        "v_mfma_f32_16x16x32_bf16 v[192:195], a[92:95], a[232:235], v[192:195]\n"
        "ds_read_b128 v[202:205], v251 offset:4096\n"
        "ds_read_b128 a[240:243], v240 offset:0\n"
        "v_cvt_pk_bf16_f32 v221, v222, v223\n"
        "v_cvt_pk_bf16_f32 v227, v222, v223\n"
        "v_cvt_pk_bf16_f32 v173, v174, v175\n"
        "v_mfma_f32_16x16x32_bf16 v[80:83], a[116:119], a[232:235], v[80:83]\n"
        "buffer_load_dwordx4 v130, s[20:23], s58 offen offset:2816 lds\n"
        "v_mfma_f32_16x16x32_bf16 v[84:87], a[140:143], a[232:235], v[84:87]\n"
        "s_add_u32 m0, s65, 11520\n"
        "ds_read_b128 a[244:247], v241 offset:0\n"
        "ds_read_b128 a[248:251], v240 offset:128\n"
        "v_mfma_f32_16x16x32_bf16 v[212:215], a[92:95], a[236:239], v[212:215]\n"
        "v_cvt_pk_bf16_f32 v174, v68, v69\n"
        "v_cvt_pk_bf16_f32 v175, v70, v71\n"
        "v_mfma_f32_16x16x32_bf16 v[88:91], a[116:119], a[236:239], v[88:91]\n"
        "buffer_load_dwordx4 v128, s[20:23], s59 offen offset:768 lds\n"
        "v_mfma_f32_16x16x32_bf16 v[92:95], a[140:143], a[236:239], v[92:95]\n"
        "s_waitcnt vmcnt(10)\n"
        "ds_read_b64_tr_b16 a[144:145], v255 offset:0\n"
        "ds_read_b64_tr_b16 a[146:147], v255 offset:768\n"
        "v_mfma_f32_16x16x32_bf16 v[64:67], v[152:155], v[156:159], v[0:3]\n"
        "s_waitcnt lgkmcnt(6)\n"
        "ds_read_b64_tr_b16 a[168:169], v255 offset:32\n"
        "ds_read_b64_tr_b16 a[170:171], v255 offset:800\n"
        "v_cvt_pk_bf16_f32 v190, v76, v77\n"
        "v_cvt_pk_bf16_f32 v191, v78, v79\n"
        "v_mfma_f32_16x16x32_bf16 v[64:67], v[168:171], v[160:163], v[64:67]\n"
        "ds_read_b64_tr_b16 a[192:193], v255 offset:64\n"
        "ds_read_b64_tr_b16 a[194:195], v255 offset:832\n"
        "v_cvt_pk_bf16_f32 v192, v192, v193\n"
        "v_cvt_pk_bf16_f32 v193, v194, v195\n"
        "v_mfma_f32_16x16x32_bf16 v[96:99], v[172:175], v[156:159], v[4:7]\n"
        "buffer_load_dwordx4 v129, s[20:23], s59 offen offset:1792 lds\n"
        "v_cvt_pk_bf16_f32 v194, v80, v81\n"
        "v_cvt_pk_bf16_f32 v195, v82, v83\n"
        "v_exp_f32 v64, v64\n"
        "v_mfma_f32_16x16x32_bf16 v[96:99], v[188:191], v[164:167], v[96:99]\n"
        "ds_read_b128 v[162:165], v252 offset:0\n"
        "v_exp_f32 v65, v65\n"
        "v_exp_f32 v66, v66\n"
        "v_cvt_pk_bf16_f32 v64, v64, v65\n"
        "v_mfma_f32_16x16x32_bf16 v[72:75], v[152:155], v[176:179], v[16:19]\n"
        "buffer_load_dwordx4 v130, s[20:23], s59 offen offset:2816 lds\n"
        "v_cvt_pk_bf16_f32 v208, v84, v85\n"
        "v_cvt_pk_bf16_f32 v209, v86, v87\n"
        "v_exp_f32 v96, v96\n"
        "v_mfma_f32_16x16x32_bf16 v[72:75], v[168:171], v[180:183], v[72:75]\n"
        "s_add_u32 m0, s65, 14592\n"
        "s_waitcnt vmcnt(9)\n"
        "ds_read_b64_tr_b16 a[148:149], v255 offset:3072\n"
        "ds_read_b64_tr_b16 a[150:151], v255 offset:3840\n"
        "v_exp_f32 v67, v67\n"
        "v_exp_f32 v97, v97\n"
        "v_mfma_f32_16x16x32_bf16 v[100:103], v[172:175], v[176:179], v[20:23]\n"
        "buffer_load_dwordx4 v128, s[20:23], s60 offen offset:768 lds\n"
        "ds_read_b64_tr_b16 a[172:173], v255 offset:3104\n"
        "ds_read_b64_tr_b16 a[174:175], v255 offset:3872\n"
        "v_cvt_pk_bf16_f32 v212, v212, v213\n"
        "v_cvt_pk_bf16_f32 v213, v214, v215\n"
        "v_mfma_f32_16x16x32_bf16 v[100:103], v[188:191], v[184:187], v[100:103]\n"
        "ds_read_b128 v[182:185], v252 offset:512\n"
        "ds_read_b64_tr_b16 a[196:197], v255 offset:3136\n"
        "ds_read_b64_tr_b16 a[198:199], v255 offset:3904\n"
        "v_cvt_pk_bf16_f32 v65, v66, v67\n"
        "v_exp_f32 v98, v98\n"
        "v_mfma_f32_16x16x32_bf16 v[68:71], v[192:195], v[156:159], v[8:11]\n"
        "s_waitcnt lgkmcnt(7)\n"
        "ds_read_b128 a[216:219], v240 offset:1536\n"
        "ds_read_b128 a[220:223], v241 offset:1536\n"
        "v_cvt_pk_bf16_f32 v66, v96, v97\n"
        "v_exp_f32 v72, v72\n"
        "v_cvt_pk_bf16_f32 v214, v88, v89\n"
        "v_mfma_f32_16x16x32_bf16 v[68:71], v[208:211], v[160:163], v[68:71]\n"
        "buffer_load_dwordx4 v129, s[20:23], s60 offen offset:1792 lds\n"
        "s_waitcnt vmcnt(8)\n"
        "ds_read_b64_tr_b16 a[152:153], v255 offset:6144\n"
        "ds_read_b64_tr_b16 a[154:155], v255 offset:6912\n"
        "v_cvt_pk_bf16_f32 v215, v90, v91\n"
        "v_cvt_pk_bf16_f32 v230, v92, v93\n"
        "v_mfma_f32_16x16x32_bf16 v[76:79], v[192:195], v[176:179], v[24:27]\n"
        "s_waitcnt lgkmcnt(6)\n"
        "ds_read_b64_tr_b16 a[176:177], v255 offset:6176\n"
        "ds_read_b64_tr_b16 a[178:179], v255 offset:6944\n"
        "v_exp_f32 v99, v99\n"
        "v_exp_f32 v73, v73\n"
        "v_mfma_f32_16x16x32_bf16 v[76:79], v[208:211], v[180:183], v[76:79]\n"
        "buffer_load_dwordx4 v130, s[20:23], s60 offen offset:2816 lds\n"
        "ds_read_b64_tr_b16 a[200:201], v255 offset:6208\n"
        "ds_read_b64_tr_b16 a[202:203], v255 offset:6976\n"
        "v_cvt_pk_bf16_f32 v231, v94, v95\n"
        "v_cvt_pk_bf16_f32 v67, v98, v99\n"
        "v_mfma_f32_16x16x32_bf16 v[104:107], v[212:215], v[156:159], v[12:15]\n"
        "s_add_u32 m0, s40, 98304\n"
        "ds_read_b128 a[224:227], v240 offset:1664\n"
        "v_exp_f32 v74, v74\n"
        "v_mfma_f32_16x16x32_bf16 v[104:107], v[228:231], v[164:167], v[104:107]\n"
        "s_sub_u32 s64, s47, 6291456\n"
        "v_cvt_pk_bf16_f32 v72, v72, v73\n"
        "v_exp_f32 v100, v100\n"
        "v_mfma_f32_16x16x32_bf16 v[108:111], v[212:215], v[176:179], v[28:31]\n"
        ".long 0xbec200ff\n"
        "42:\n"
        "s_branch 60b\n"
        "buffer_load_dwordx4 v242, s[8:11], s64 offen lds\n"
        "v_mfma_f32_16x16x32_bf16 v[108:111], v[228:231], v[184:187], v[108:111]\n"
        "s_add_u32 m0, s40, 99328\n"
        "v_exp_f32 v75, v75\n"
        "v_mfma_f32_16x16x32_bf16 v[80:83], v[152:155], v[196:199], v[32:35]\n"
        "ds_read_b128 a[228:231], v240 offset:12288\n"
        "v_exp_f32 v101, v101\n"
        "v_mfma_f32_16x16x32_bf16 v[80:83], v[168:171], v[200:203], v[80:83]\n"
        "buffer_load_dwordx4 v243, s[8:11], s64 offen lds\n"
        "v_cvt_pk_bf16_f32 v73, v74, v75\n"
        "v_mfma_f32_16x16x32_bf16 v[112:115], a[240:243], a[144:147], 0\n"
        "s_add_u32 m0, s40, 100352\n"
        "ds_read_b128 a[232:235], v241 offset:12288\n"
        "ds_read_b128 a[236:239], v240 offset:12416\n"
        "v_cvt_pk_bf16_f32 v74, v100, v101\n"
        "v_mfma_f32_16x16x32_bf16 v[112:115], a[244:247], a[148:151], v[112:115]\n"
        "s_waitcnt lgkmcnt(3)\n"
        "v_exp_f32 v102, v102\n"
        "v_mfma_f32_16x16x32_bf16 v[112:115], a[248:251], a[152:155], v[112:115]\n"
        "buffer_load_dwordx4 v244, s[8:11], s64 offen lds\n"
        "v_mfma_f32_16x16x32_bf16 v[116:119], a[248:251], a[176:179], 0\n"
        "s_add_u32 m0, s40, 110592\n"
        "v_exp_f32 v68, v68\n"
        "v_mfma_f32_16x16x32_bf16 v[116:119], a[244:247], a[172:175], v[116:119]\n"
        "s_sub_u32 s64, s47, 5505024\n"
        "v_exp_f32 v103, v103\n"
        "v_mfma_f32_16x16x32_bf16 v[116:119], a[240:243], a[168:171], v[116:119]\n"
        "buffer_load_dwordx4 v242, s[8:11], s64 offen lds\n"
        "v_cvt_pk_bf16_f32 v75, v102, v103\n"
        "v_mfma_f32_16x16x32_bf16 v[96:99], v[172:175], v[196:199], v[36:39]\n"
        "s_add_u32 m0, s40, 111616\n"
        "s_waitcnt vmcnt(10)\n"
        "ds_read_b64_tr_b16 a[156:157], v255 offset:9216\n"
        "ds_read_b64_tr_b16 a[158:159], v255 offset:9984\n"
        "v_mfma_f32_16x16x32_bf16 v[96:99], v[188:191], v[204:207], v[96:99]\n"
        "ds_read_b64_tr_b16 a[180:181], v255 offset:9248\n"
        "ds_read_b64_tr_b16 a[182:183], v255 offset:10016\n"
        "ds_read_b128 v[202:205], v252 offset:4096\n"
        "v_mfma_f32_16x16x32_bf16 v[120:123], a[240:243], a[192:195], 0\n"
        "buffer_load_dwordx4 v243, s[8:11], s64 offen lds\n"
        "ds_read_b64_tr_b16 a[204:205], v255 offset:9280\n"
        "v_mfma_f32_16x16x32_bf16 v[120:123], a[244:247], a[196:199], v[120:123]\n"
        "ds_read_b64_tr_b16 a[206:207], v255 offset:10048\n"
        "s_add_u32 m0, s40, 112640\n"
        "ds_read_b128 a[240:243], v240 offset:13824\n"
        "v_mfma_f32_16x16x32_bf16 v[120:123], a[248:251], a[200:203], v[120:123]\n"
        "s_waitcnt vmcnt(8)\n"
        "ds_read_b64_tr_b16 a[160:161], v255 offset:12288\n"
        "ds_read_b64_tr_b16 a[162:163], v255 offset:13056\n"
        "v_mfma_f32_16x16x32_bf16 v[124:127], a[216:219], a[144:147], 0\n"
        "buffer_load_dwordx4 v244, s[8:11], s64 offen lds\n"
        "ds_read_b64_tr_b16 a[184:185], v255 offset:12320\n"
        "v_mfma_f32_16x16x32_bf16 v[124:127], a[220:223], a[148:151], v[124:127]\n"
        "ds_read_b64_tr_b16 a[186:187], v255 offset:13088\n"
        "s_xor_b32 m0, s48, 8192\n"
        "ds_read_b64_tr_b16 a[208:209], v255 offset:12352\n"
        "ds_read_b64_tr_b16 a[210:211], v255 offset:13120\n"
        "v_mfma_f32_16x16x32_bf16 v[124:127], a[224:227], a[152:155], v[124:127]\n"
        "s_sub_u32 s64, s36, 2097152\n"
        "ds_read_b128 a[244:247], v241 offset:13824\n"
        "ds_read_b128 a[248:251], v240 offset:13952\n"
        "v_mfma_f32_16x16x32_bf16 v[156:159], a[224:227], a[176:179], 0\n"
        "buffer_load_dwordx4 v249, s[12:15], s64 offen lds\n"
        "v_mfma_f32_16x16x32_bf16 v[156:159], a[220:223], a[172:175], v[156:159]\n"
        "ds_read_b128 v[222:225], v251 offset:4608\n"
        "v_exp_f32 v69, v69\n"
        "v_mfma_f32_16x16x32_bf16 v[156:159], a[216:219], a[168:171], v[156:159]\n"
        "v_exp_f32 v70, v70\n"
        "v_cvt_pk_bf16_f32 v68, v68, v69\n"
        "v_mfma_f32_16x16x32_bf16 v[84:87], v[192:195], v[196:199], v[40:43]\n"
        "s_waitcnt lgkmcnt(7)\n"
        "v_exp_f32 v76, v76\n"
        "v_mfma_f32_16x16x32_bf16 v[84:87], v[208:211], v[200:203], v[84:87]\n"
        "buffer_load_dwordx4 v253, s[12:15], s64 offen offset:1024 lds\n"
        "v_mfma_f32_16x16x32_bf16 v[160:163], a[216:219], a[192:195], 0\n"
        "s_add_u32 m0, s65, 12288\n"
        "v_exp_f32 v71, v71\n"
        "v_mfma_f32_16x16x32_bf16 v[160:163], a[220:223], a[196:199], v[160:163]\n"
        "ds_read_b128 a[216:219], v241 offset:128\n"
        "v_exp_f32 v77, v77\n"
        "v_mfma_f32_16x16x32_bf16 v[160:163], a[224:227], a[200:203], v[160:163]\n"
        "ds_read_b128 a[220:223], v240 offset:256\n"
        "v_cvt_pk_bf16_f32 v69, v70, v71\n"
        "v_cvt_pk_bf16_f32 v76, v76, v77\n"
        "v_mfma_f32_16x16x32_bf16 v[164:167], a[228:231], a[144:147], 0\n"
        "s_waitcnt lgkmcnt(2)\n"
        "buffer_load_dword v136, s[52:55], 0 offen lds\n"
        "v_mfma_f32_16x16x32_bf16 v[164:167], a[232:235], a[148:151], v[164:167]\n"
        "ds_read_b128 a[224:227], v241 offset:256\n"
        "v_exp_f32 v78, v78\n"
        "v_mfma_f32_16x16x32_bf16 v[164:167], a[236:239], a[152:155], v[164:167]\n"
        "v_exp_f32 v104, v104\n"
        "v_mfma_f32_16x16x32_bf16 v[100:103], v[212:215], v[196:199], v[44:47]\n"
        "v_exp_f32 v79, v79\n"
        "v_mfma_f32_16x16x32_bf16 v[100:103], v[228:231], v[204:207], v[100:103]\n"
        "v_exp_f32 v105, v105\n"
        "v_cvt_pk_bf16_f32 v77, v78, v79\n"
        "v_mfma_f32_16x16x32_bf16 v[176:179], a[236:239], a[176:179], 0\n"
        "v_exp_f32 v106, v106\n"
        "v_cvt_pk_bf16_f32 v70, v104, v105\n"
        "v_mfma_f32_16x16x32_bf16 v[176:179], a[232:235], a[172:175], v[176:179]\n"
        "v_exp_f32 v108, v108\n"
        "v_mfma_f32_16x16x32_bf16 v[176:179], a[228:231], a[168:171], v[176:179]\n"
        "s_waitcnt vmcnt(9)\n"
        "ds_read_b64_tr_b16 a[164:165], v255 offset:15360\n"
        "ds_read_b64_tr_b16 a[166:167], v255 offset:16128\n"
        "v_mfma_f32_16x16x32_bf16 v[180:183], a[228:231], a[192:195], 0\n"
        "ds_read_b64_tr_b16 a[188:189], v255 offset:15392\n"
        "ds_read_b64_tr_b16 a[190:191], v255 offset:16160\n"
        "v_mfma_f32_16x16x32_bf16 v[180:183], a[232:235], a[196:199], v[180:183]\n"
        "ds_read_b64_tr_b16 a[212:213], v255 offset:15424\n"
        "ds_read_b64_tr_b16 a[214:215], v255 offset:16192\n"
        "v_mfma_f32_16x16x32_bf16 v[180:183], a[236:239], a[200:203], v[180:183]\n"
        "ds_read_b128 a[228:231], v241 offset:1664\n"
        "ds_read_b128 a[232:235], v240 offset:1792\n"
        "v_mfma_f32_16x16x32_bf16 v[184:187], a[240:243], a[144:147], 0\n"
        "ds_read_b128 a[236:239], v241 offset:1792\n"
        "v_exp_f32 v107, v107\n"
        "v_mfma_f32_16x16x32_bf16 v[184:187], a[244:247], a[148:151], v[184:187]\n"
        "v_exp_f32 v109, v109\n"
        "v_cvt_pk_bf16_f32 v71, v106, v107\n"
        "v_mfma_f32_16x16x32_bf16 v[184:187], a[248:251], a[152:155], v[184:187]\n"
        "v_exp_f32 v110, v110\n"
        "v_cvt_pk_bf16_f32 v78, v108, v109\n"
        "v_mfma_f32_16x16x32_bf16 v[196:199], a[248:251], a[176:179], 0\n"
        "v_exp_f32 v111, v111\n"
        "v_mfma_f32_16x16x32_bf16 v[196:199], a[244:247], a[172:175], v[196:199]\n"
        "v_cvt_pk_bf16_f32 v79, v110, v111\n"
        "v_exp_f32 v80, v80\n"
        "v_mfma_f32_16x16x32_bf16 v[196:199], a[240:243], a[168:171], v[196:199]\n"
        "v_exp_f32 v81, v81\n"
        "v_mfma_f32_16x16x32_bf16 v[200:203], a[240:243], a[192:195], 0\n"
        "v_exp_f32 v82, v82\n"
        "v_cvt_pk_bf16_f32 v80, v80, v81\n"
        "v_mfma_f32_16x16x32_bf16 v[200:203], a[244:247], a[196:199], v[200:203]\n"
        "ds_read_b128 a[240:243], v241 offset:12416\n"
        "v_exp_f32 v83, v83\n"
        "v_mfma_f32_16x16x32_bf16 v[200:203], a[248:251], a[200:203], v[200:203]\n"
        "ds_read_b128 a[244:247], v240 offset:12544\n"
        "v_cvt_pk_bf16_f32 v81, v82, v83\n"
        "v_mfma_f32_16x16x32_bf16 v[88:91], v[152:155], v[216:219], v[48:51]\n"
        "ds_read_b128 a[248:251], v241 offset:12544\n"
        "v_exp_f32 v96, v96\n"
        "v_mfma_f32_16x16x32_bf16 v[88:91], v[168:171], v[220:223], v[88:91]\n"
        "s_waitcnt lgkmcnt(3)\n"
        "v_exp_f32 v97, v97\n"
        "v_mfma_f32_16x16x32_bf16 v[112:115], a[216:219], a[156:159], v[112:115]\n"
        "v_exp_f32 v98, v98\n"
        "v_cvt_pk_bf16_f32 v82, v96, v97\n"
        "v_mfma_f32_16x16x32_bf16 v[112:115], a[220:223], a[160:163], v[112:115]\n"
        "v_exp_f32 v99, v99\n"
        "v_mfma_f32_16x16x32_bf16 v[112:115], a[224:227], a[164:167], v[112:115]\n"
        "v_cvt_pk_bf16_f32 v83, v98, v99\n"
        "v_exp_f32 v84, v84\n"
        "v_mfma_f32_16x16x32_bf16 v[116:119], a[224:227], a[188:191], v[116:119]\n"
        "v_exp_f32 v85, v85\n"
        "v_mfma_f32_16x16x32_bf16 v[116:119], a[220:223], a[184:187], v[116:119]\n"
        "v_exp_f32 v86, v86\n"
        "v_cvt_pk_bf16_f32 v84, v84, v85\n"
        "v_mfma_f32_16x16x32_bf16 v[116:119], a[216:219], a[180:183], v[116:119]\n"
        "v_exp_f32 v87, v87\n"
        "v_mfma_f32_16x16x32_bf16 v[120:123], a[216:219], a[204:207], v[120:123]\n"
        "v_cvt_pk_bf16_f32 v85, v86, v87\n"
        "v_exp_f32 v100, v100\n"
        "v_mfma_f32_16x16x32_bf16 v[120:123], a[220:223], a[208:211], v[120:123]\n"
        "ds_read_b128 a[216:219], v241 offset:13952\n"
        "v_exp_f32 v101, v101\n"
        "v_mfma_f32_16x16x32_bf16 v[120:123], a[224:227], a[212:215], v[120:123]\n"
        "ds_read_b128 a[220:223], v240 offset:14080\n"
        "v_exp_f32 v102, v102\n"
        "v_mfma_f32_16x16x32_bf16 v[124:127], a[228:231], a[156:159], v[124:127]\n"
        "ds_read_b128 a[224:227], v241 offset:14080\n"
        "v_cvt_pk_bf16_f32 v86, v100, v101\n"
        "v_cvt_pk_bf16_f32 v128, v112, v113\n"
        "v_mfma_f32_16x16x32_bf16 v[124:127], a[232:235], a[160:163], v[124:127]\n"
        "v_exp_f32 v103, v103\n"
        "v_cvt_pk_bf16_f32 v129, v114, v115\n"
        "v_mfma_f32_16x16x32_bf16 v[124:127], a[236:239], a[164:167], v[124:127]\n"
        "v_cvt_pk_bf16_f32 v87, v102, v103\n"
        "v_cvt_pk_bf16_f32 v136, v116, v117\n"
        "v_mfma_f32_16x16x32_bf16 v[104:107], v[172:175], v[216:219], v[52:55]\n"
        "v_cvt_pk_bf16_f32 v137, v118, v119\n"
        "v_cvt_pk_bf16_f32 v144, v120, v121\n"
        "v_mfma_f32_16x16x32_bf16 v[104:107], v[188:191], v[224:227], v[104:107]\n"
        "ds_read_b128 v[222:225], v252 offset:4608\n"
        "v_cvt_pk_bf16_f32 v145, v122, v123\n"
        "v_mfma_f32_16x16x32_bf16 v[156:159], a[236:239], a[188:191], v[156:159]\n"
        "v_exp_f32 v88, v88\n"
        "v_mfma_f32_16x16x32_bf16 v[156:159], a[232:235], a[184:187], v[156:159]\n"
        "v_cvt_pk_bf16_f32 v130, v124, v125\n"
        "v_cvt_pk_bf16_f32 v131, v126, v127\n"
        "v_mfma_f32_16x16x32_bf16 v[156:159], a[228:231], a[180:183], v[156:159]\n"
        "v_exp_f32 v89, v89\n"
        "v_mfma_f32_16x16x32_bf16 v[160:163], a[228:231], a[204:207], v[160:163]\n"
        "v_exp_f32 v90, v90\n"
        "v_mfma_f32_16x16x32_bf16 v[160:163], a[232:235], a[208:211], v[160:163]\n"
        "v_exp_f32 v91, v91\n"
        "v_mfma_f32_16x16x32_bf16 v[160:163], a[236:239], a[212:215], v[160:163]\n"
        "s_waitcnt lgkmcnt(1)\n"
        "v_exp_f32 v104, v104\n"
        "v_mfma_f32_16x16x32_bf16 v[164:167], a[240:243], a[156:159], v[164:167]\n"
        "v_cvt_pk_bf16_f32 v138, v156, v157\n"
        "v_cvt_pk_bf16_f32 v139, v158, v159\n"
        "v_mfma_f32_16x16x32_bf16 v[164:167], a[244:247], a[160:163], v[164:167]\n"
        "v_cvt_pk_bf16_f32 v88, v88, v89\n"
        "v_cvt_pk_bf16_f32 v89, v90, v91\n"
        "v_mfma_f32_16x16x32_bf16 v[164:167], a[248:251], a[164:167], v[164:167]\n"
        "v_exp_f32 v105, v105\n"
        "v_mfma_f32_16x16x32_bf16 v[92:95], v[192:195], v[216:219], v[56:59]\n"
        "s_waitcnt lgkmcnt(0)\n"
        "v_cvt_pk_bf16_f32 v146, v160, v161\n"
        "v_cvt_pk_bf16_f32 v147, v162, v163\n"
        "v_mfma_f32_16x16x32_bf16 v[92:95], v[208:211], v[220:223], v[92:95]\n"
        "v_exp_f32 v106, v106\n"
        "v_mfma_f32_16x16x32_bf16 v[176:179], a[248:251], a[188:191], v[176:179]\n"
        "v_exp_f32 v107, v107\n"
        "v_mfma_f32_16x16x32_bf16 v[176:179], a[244:247], a[184:187], v[176:179]\n"
        "v_cvt_pk_bf16_f32 v132, v164, v165\n"
        "v_cvt_pk_bf16_f32 v133, v166, v167\n"
        "v_mfma_f32_16x16x32_bf16 v[176:179], a[240:243], a[180:183], v[176:179]\n"
        "v_cvt_pk_bf16_f32 v90, v104, v105\n"
        "v_cvt_pk_bf16_f32 v91, v106, v107\n"
        "v_mfma_f32_16x16x32_bf16 v[180:183], a[240:243], a[204:207], v[180:183]\n"
        "v_exp_f32 v92, v92\n"
        "v_mfma_f32_16x16x32_bf16 v[180:183], a[244:247], a[208:211], v[180:183]\n"
        "v_exp_f32 v93, v93\n"
        "v_mfma_f32_16x16x32_bf16 v[180:183], a[248:251], a[212:215], v[180:183]\n"
        "v_mov_b64 v[96:97], 0\n"
        "v_cvt_pk_bf16_f32 v140, v176, v177\n"
        "v_mfma_f32_16x16x32_bf16 v[184:187], a[216:219], a[156:159], v[184:187]\n"
        "v_cvt_pk_bf16_f32 v141, v178, v179\n"
        "v_cvt_pk_bf16_f32 v92, v92, v93\n"
        "v_mfma_f32_16x16x32_bf16 v[184:187], a[220:223], a[160:163], v[184:187]\n"
        "v_exp_f32 v94, v94\n"
        "v_mfma_f32_16x16x32_bf16 v[184:187], a[224:227], a[164:167], v[184:187]\n"
        "v_exp_f32 v95, v95\n"
        "v_mfma_f32_16x16x32_bf16 v[108:111], v[212:215], v[216:219], v[60:63]\n"
        "v_cvt_pk_bf16_f32 v148, v180, v181\n"
        "v_cvt_pk_bf16_f32 v149, v182, v183\n"
        "v_mfma_f32_16x16x32_bf16 v[108:111], v[228:231], v[224:227], v[108:111]\n"
        "v_add_u32 v240, 24576, v240\n"
        "v_add_u32 v241, 24576, v241\n"
        "v_mfma_f32_16x16x32_bf16 v[196:199], a[224:227], a[188:191], v[196:199]\n"
        "v_cvt_pk_bf16_f32 v134, v184, v185\n"
        "v_cvt_pk_bf16_f32 v135, v186, v187\n"
        "v_mfma_f32_16x16x32_bf16 v[196:199], a[220:223], a[184:187], v[196:199]\n"
        "v_mov_b64 v[172:173], 0\n"
        "v_cvt_pk_bf16_f32 v93, v94, v95\n"
        "v_mfma_f32_16x16x32_bf16 v[196:199], a[216:219], a[180:183], v[196:199]\n"
        "v_exp_f32 v108, v108\n"
        "v_mfma_f32_16x16x32_bf16 v[200:203], a[216:219], a[204:207], v[200:203]\n"
        "v_exp_f32 v109, v109\n"
        "v_mfma_f32_16x16x32_bf16 v[200:203], a[220:223], a[208:211], v[200:203]\n"
        "v_exp_f32 v110, v110\n"
        "v_mfma_f32_16x16x32_bf16 v[200:203], a[224:227], a[212:215], v[200:203]\n"
        "v_exp_f32 v111, v111\n"
        "v_smfmac_f32_16x16x64_bf16 v[96:99], a[252:255], v[64:71], v237\n"
        "v_cvt_pk_bf16_f32 v142, v196, v197\n"
        "v_mfma_f32_16x16x32_bf16 v[100:103], v[132:135], v[68:71], 0\n"
        "v_cvt_pk_bf16_f32 v143, v198, v199\n"
        "v_cvt_pk_bf16_f32 v94, v108, v109\n"
        "v_mfma_f32_16x16x32_bf16 v[100:103], v[128:131], v[64:67], v[100:103]\n"
        "v_mov_b64 v[192:193], 0\n"
        "v_cvt_pk_bf16_f32 v150, v200, v201\n"
        "v_mfma_f32_16x16x32_bf16 v[104:107], v[136:139], v[64:67], 0\n"
        "v_cvt_pk_bf16_f32 v151, v202, v203\n"
        "v_add_f32 v97, v96, v97\n"
        "v_mfma_f32_16x16x32_bf16 v[104:107], v[140:143], v[68:71], v[104:107]\n"
        "v_rcp_f32 v96, v97\n"
        "v_cvt_pk_bf16_f32 v95, v110, v111\n"
        "v_mfma_f32_16x16x32_bf16 v[108:111], v[148:151], v[68:71], 0\n"
        "v_mul_f32 v100, v96, v100\n"
        "v_mul_f32 v101, v96, v101\n"
        "v_mfma_f32_16x16x32_bf16 v[108:111], v[144:147], v[64:67], v[108:111]\n"
        "v_mul_f32 v102, v96, v102\n"
        "v_mul_f32 v103, v96, v103\n"
        "v_smfmac_f32_16x16x64_bf16 v[172:175], a[252:255], v[72:79], v237\n"
        "s_waitcnt vmcnt(6)\n"
        "s_barrier\n"
        "ds_read_b128 a[236:239], v240 offset:0\n"
        "v_mfma_f32_16x16x32_bf16 v[176:179], v[132:135], v[76:79], 0\n"
        "ds_read_b128 a[240:243], v241 offset:0\n"
        "ds_read_b128 a[244:247], v240 offset:128\n"
        "v_mfma_f32_16x16x32_bf16 v[176:179], v[128:131], v[72:75], v[176:179]\n"
        "ds_read_b128 a[248:251], v241 offset:128\n"
        "ds_read_b128 a[216:219], v240 offset:256\n"
        "v_mfma_f32_16x16x32_bf16 v[180:183], v[136:139], v[72:75], 0\n"
        "ds_read_b128 a[220:223], v241 offset:256\n"
        "v_cvt_pk_bf16_f32 v100, v100, v101\n"
        "v_mfma_f32_16x16x32_bf16 v[180:183], v[140:143], v[76:79], v[180:183]\n"
        "v_cvt_pk_bf16_f32 v101, v102, v103\n"
        "v_mul_f32 v104, v96, v104\n"
        "v_mfma_f32_16x16x32_bf16 v[184:187], v[148:151], v[76:79], 0\n"
        "ds_write2st64_b32 v245, v100, v101 offset0:0 offset1:4\n"
        "v_mfma_f32_16x16x32_bf16 v[184:187], v[144:147], v[72:75], v[184:187]\n"
        "v_mul_f32 v105, v96, v105\n"
        "v_mul_f32 v106, v96, v106\n"
        "v_smfmac_f32_16x16x64_bf16 v[192:195], a[252:255], v[80:87], v237\n"
        "v_mul_f32 v107, v96, v107\n"
        "v_mfma_f32_16x16x32_bf16 v[196:199], v[132:135], v[84:87], 0\n"
        "v_cvt_pk_bf16_f32 v102, v104, v105\n"
        "v_cvt_pk_bf16_f32 v103, v106, v107\n"
        "v_mfma_f32_16x16x32_bf16 v[196:199], v[128:131], v[80:83], v[196:199]\n"
        "buffer_load_dwordx4 v232, s[52:55], s34 offen lds\n"
        "v_mfma_f32_16x16x32_bf16 v[200:203], v[136:139], v[80:83], 0\n"
        "ds_write2st64_b32 v245, v102, v103 offset0:8 offset1:12\n"
        "v_mfma_f32_16x16x32_bf16 v[200:203], v[140:143], v[84:87], v[200:203]\n"
        "v_mov_b64 v[124:125], 0\n"
        "v_mul_f32 v108, v96, v108\n"
        "v_mul_f32 v109, v96, v109\n"
        "v_mfma_f32_16x16x32_bf16 v[204:207], v[148:151], v[84:87], 0\n"
        "v_mul_f32 v110, v96, v110\n"
        "v_mul_f32 v111, v96, v111\n"
        "v_mfma_f32_16x16x32_bf16 v[204:207], v[144:147], v[80:83], v[204:207]\n"
        "s_waitcnt lgkmcnt(2)\n"
        "v_cvt_pk_bf16_f32 v108, v108, v109\n"
        "v_cvt_pk_bf16_f32 v109, v110, v111\n"
        "v_mfma_f32_16x16x32_bf16 v[112:115], a[72:75], a[236:239], 0\n"
        "v_max_u32 v236, v96, v97\n"
        "v_add_f32 v173, v172, v173\n"
        "v_mfma_f32_16x16x32_bf16 v[112:115], a[76:79], a[240:243], v[112:115]\n"
        "ds_write2st64_b32 v245, v108, v109 offset0:16 offset1:20\n"
        "v_mfma_f32_16x16x32_bf16 v[112:115], a[80:83], a[244:247], v[112:115]\n"
        "v_rcp_f32 v172, v173\n"
        "v_mfma_f32_16x16x32_bf16 v[112:115], a[84:87], a[248:251], v[112:115]\n"
        "buffer_load_dwordx4 v233, s[52:55], s34 offen offset:1024 lds\n"
        "v_mfma_f32_16x16x32_bf16 v[112:115], a[88:91], a[216:219], v[112:115]\n"
        "v_mul_f32 v176, v172, v176\n"
        "v_mul_f32 v177, v172, v177\n"
        "v_mfma_f32_16x16x32_bf16 v[112:115], a[92:95], a[220:223], v[112:115]\n"
        "v_mul_f32 v178, v172, v178\n"
        "v_mul_f32 v179, v172, v179\n"
        "v_smfmac_f32_16x16x64_bf16 v[124:127], a[252:255], v[88:95], v237\n"
        "v_cvt_pk_bf16_f32 v176, v176, v177\n"
        "v_mfma_f32_16x16x32_bf16 v[80:83], v[132:135], v[92:95], 0\n"
        "v_cvt_pk_bf16_f32 v177, v178, v179\n"
        "v_mul_f32 v180, v172, v180\n"
        "v_mfma_f32_16x16x32_bf16 v[80:83], v[128:131], v[88:91], v[80:83]\n"
        "ds_write2st64_b32 v245, v176, v177 offset0:1 offset1:5\n"
        "v_mfma_f32_16x16x32_bf16 v[84:87], v[136:139], v[88:91], 0\n"
        "v_cvt_pk_bf16_f32 v152, v112, v113\n"
        "v_cvt_pk_bf16_f32 v153, v114, v115\n"
        "v_mfma_f32_16x16x32_bf16 v[84:87], v[140:143], v[92:95], v[84:87]\n"
        "v_mul_f32 v181, v172, v181\n"
        "v_mul_f32 v182, v172, v182\n"
        "v_mfma_f32_16x16x32_bf16 v[96:99], v[148:151], v[92:95], 0\n"
        "buffer_load_dwordx4 v234, s[52:55], s34 offen offset:2048 lds\n"
        "v_mfma_f32_16x16x32_bf16 v[96:99], v[144:147], v[88:91], v[96:99]\n"
        "v_mul_f32 v183, v172, v183\n"
        "v_cvt_pk_bf16_f32 v178, v180, v181\n"
        "v_mfma_f32_16x16x32_bf16 v[116:119], a[116:119], a[220:223], 0\n"
        "v_cvt_pk_bf16_f32 v179, v182, v183\n"
        "v_mul_f32 v184, v172, v184\n"
        "v_mfma_f32_16x16x32_bf16 v[116:119], a[112:115], a[216:219], v[116:119]\n"
        "ds_write2st64_b32 v245, v178, v179 offset0:9 offset1:13\n"
        "v_mfma_f32_16x16x32_bf16 v[116:119], a[108:111], a[248:251], v[116:119]\n"
        "v_mul_f32 v185, v172, v185\n"
        "v_mul_f32 v186, v172, v186\n"
        "v_mfma_f32_16x16x32_bf16 v[116:119], a[104:107], a[244:247], v[116:119]\n"
        "s_add_u32 m0, s65, 15360\n"
        "v_mul_f32 v187, v172, v187\n"
        "v_add_f32 v125, v124, v125\n"
        "v_mfma_f32_16x16x32_bf16 v[116:119], a[100:103], a[240:243], v[116:119]\n"
        "v_rcp_f32 v124, v125\n"
        "v_mfma_f32_16x16x32_bf16 v[116:119], a[96:99], a[236:239], v[116:119]\n"
        "v_cvt_pk_bf16_f32 v184, v184, v185\n"
        "v_cvt_pk_bf16_f32 v185, v186, v187\n"
        "v_mfma_f32_16x16x32_bf16 v[120:123], a[120:123], a[236:239], 0\n"
        "buffer_load_dwordx4 v232, s[52:55], s62 offen lds\n"
        "v_mfma_f32_16x16x32_bf16 v[120:123], a[124:127], a[240:243], v[120:123]\n"
        "ds_write2st64_b32 v245, v184, v185 offset0:17 offset1:21\n"
        "v_mfma_f32_16x16x32_bf16 v[120:123], a[128:131], a[244:247], v[120:123]\n"
        "v_mul_f32 v80, v124, v80\n"
        "v_mul_f32 v81, v124, v81\n"
        "v_mfma_f32_16x16x32_bf16 v[120:123], a[132:135], a[248:251], v[120:123]\n"
        "v_mul_f32 v82, v124, v82\n"
        "v_mul_f32 v83, v124, v83\n"
        "v_mfma_f32_16x16x32_bf16 v[120:123], a[136:139], a[216:219], v[120:123]\n"
        "v_max3_u32 v236, v236, v172, v173\n"
        "v_cvt_pk_bf16_f32 v80, v80, v81\n"
        "v_mfma_f32_16x16x32_bf16 v[120:123], a[140:143], a[220:223], v[120:123]\n"
        "v_cvt_pk_bf16_f32 v81, v82, v83\n"
        "v_mul_f32 v84, v124, v84\n"
        "v_mfma_f32_16x16x32_bf16 v[112:115], a[20:23], a[220:223], 0\n"
        "v_mul_f32 v85, v124, v85\n"
        "v_mul_f32 v86, v124, v86\n"
        "v_mfma_f32_16x16x32_bf16 v[112:115], a[16:19], a[216:219], v[112:115]\n"
        "v_mul_f32 v87, v124, v87\n"
        "v_cvt_pk_bf16_f32 v82, v84, v85\n"
        "v_mfma_f32_16x16x32_bf16 v[112:115], a[12:15], a[248:251], v[112:115]\n"
        "buffer_load_dwordx4 v233, s[52:55], s62 offen offset:1024 lds\n"
        "v_mfma_f32_16x16x32_bf16 v[112:115], a[8:11], a[244:247], v[112:115]\n"
        "v_cvt_pk_bf16_f32 v83, v86, v87\n"
        "v_mul_f32 v96, v124, v96\n"
        "v_mfma_f32_16x16x32_bf16 v[112:115], a[4:7], a[240:243], v[112:115]\n"
        "v_mul_f32 v97, v124, v97\n"
        "v_mul_f32 v98, v124, v98\n"
        "v_mfma_f32_16x16x32_bf16 v[112:115], a[0:3], a[236:239], v[112:115]\n"
        "v_mul_f32 v99, v124, v99\n"
        "v_cvt_pk_bf16_f32 v154, v116, v117\n"
        "v_cvt_pk_bf16_f32 v155, v118, v119\n"
        "v_mfma_f32_16x16x32_bf16 v[116:119], a[24:27], a[236:239], 0\n"
        "v_cvt_pk_bf16_f32 v96, v96, v97\n"
        "v_cvt_pk_bf16_f32 v97, v98, v99\n"
        "v_mfma_f32_16x16x32_bf16 v[116:119], a[28:31], a[240:243], v[116:119]\n"
        "ds_write_b32 v245, v96 offset:4864\n"
        "v_mfma_f32_16x16x32_bf16 v[116:119], a[32:35], a[244:247], v[116:119]\n"
        "v_cvt_pk_bf16_f32 v168, v120, v121\n"
        "v_cvt_pk_bf16_f32 v169, v122, v123\n"
        "v_mfma_f32_16x16x32_bf16 v[116:119], a[36:39], a[248:251], v[116:119]\n"
        "v_add_f32 v193, v192, v193\n"
        "v_cvt_pk_bf16_f32 v156, v112, v113\n"
        "v_mfma_f32_16x16x32_bf16 v[116:119], a[40:43], a[216:219], v[116:119]\n"
        "buffer_load_dwordx4 v234, s[52:55], s62 offen offset:2048 lds\n"
        "v_mfma_f32_16x16x32_bf16 v[116:119], a[44:47], a[220:223], v[116:119]\n"
        "v_rcp_f32 v192, v193\n"
        "v_mfma_f32_16x16x32_bf16 v[120:123], a[68:71], a[220:223], 0\n"
        "v_mul_f32 v196, v192, v196\n"
        "v_mul_f32 v197, v192, v197\n"
        "v_mfma_f32_16x16x32_bf16 v[120:123], a[64:67], a[216:219], v[120:123]\n"
        "v_mul_f32 v198, v192, v198\n"
        "v_mul_f32 v199, v192, v199\n"
        "v_mfma_f32_16x16x32_bf16 v[120:123], a[60:63], a[248:251], v[120:123]\n"
        "ds_read_b128 a[224:227], v240 offset:1536\n"
        "v_cvt_pk_bf16_f32 v157, v114, v115\n"
        "v_mfma_f32_16x16x32_bf16 v[120:123], a[56:59], a[244:247], v[120:123]\n"
        "s_add_u32 m0, s65, 0\n"
        "ds_read_b128 a[228:231], v241 offset:1536\n"
        "v_cvt_pk_bf16_f32 v196, v196, v197\n"
        "v_mfma_f32_16x16x32_bf16 v[120:123], a[52:55], a[240:243], v[120:123]\n"
        "ds_read_b128 a[232:235], v240 offset:1664\n"
        "v_cvt_pk_bf16_f32 v197, v198, v199\n"
        "v_mfma_f32_16x16x32_bf16 v[120:123], a[48:51], a[236:239], v[120:123]\n"
        "ds_write2st64_b32 v245, v196, v197 offset0:2 offset1:6\n"
        "v_mfma_f32_16x16x32_bf16 v[112:115], a[236:239], a[144:147], 0\n"
        "buffer_load_dwordx4 v232, s[52:55], 0 offen lds\n"
        "v_mfma_f32_16x16x32_bf16 v[112:115], a[240:243], a[148:151], v[112:115]\n"
        "v_mul_f32 v200, v192, v200\n"
        "v_mul_f32 v201, v192, v201\n"
        "v_mfma_f32_16x16x32_bf16 v[112:115], a[244:247], a[152:155], v[112:115]\n"
        "v_mul_f32 v202, v192, v202\n"
        "v_mul_f32 v203, v192, v203\n"
        "v_mfma_f32_16x16x32_bf16 v[112:115], a[248:251], a[156:159], v[112:115]\n"
        "v_cvt_pk_bf16_f32 v198, v200, v201\n"
        "v_cvt_pk_bf16_f32 v199, v202, v203\n"
        "v_mfma_f32_16x16x32_bf16 v[112:115], a[216:219], a[160:163], v[112:115]\n"
        "ds_write2st64_b32 v245, v198, v199 offset0:10 offset1:14\n"
        "v_mfma_f32_16x16x32_bf16 v[112:115], a[220:223], a[164:167], v[112:115]\n"
        "v_mul_f32 v204, v192, v204\n"
        "v_mul_f32 v205, v192, v205\n"
        "v_cvt_pk_bf16_f32 v158, v116, v117\n"
        "v_cvt_pk_bf16_f32 v159, v118, v119\n"
        "v_mfma_f32_16x16x32_bf16 v[116:119], a[220:223], a[188:191], 0\n"
        "v_mul_f32 v206, v192, v206\n"
        "v_mul_f32 v207, v192, v207\n"
        "v_mfma_f32_16x16x32_bf16 v[116:119], a[216:219], a[184:187], v[116:119]\n"
        "ds_write_b32 v245, v97 offset:5888\n"
        "v_mfma_f32_16x16x32_bf16 v[116:119], a[248:251], a[180:183], v[116:119]\n"
        "buffer_load_dwordx4 v233, s[52:55], 0 offen offset:1024 lds\n"
        "v_mfma_f32_16x16x32_bf16 v[116:119], a[244:247], a[176:179], v[116:119]\n"
        "s_waitcnt vmcnt(5)\n"
        "ds_read_b64_tr_b16 v[64:65], v255 offset:12288\n"
        "ds_read_b64_tr_b16 v[66:67], v255 offset:13056\n"
        "v_mfma_f32_16x16x32_bf16 v[116:119], a[240:243], a[172:175], v[116:119]\n"
        "v_cvt_pk_bf16_f32 v204, v204, v205\n"
        "v_cvt_pk_bf16_f32 v205, v206, v207\n"
        "v_mfma_f32_16x16x32_bf16 v[116:119], a[236:239], a[168:171], v[116:119]\n"
        "ds_write2st64_b32 v245, v204, v205 offset0:18 offset1:22\n"
        "v_cvt_pk_bf16_f32 v160, v120, v121\n"
        "v_cvt_pk_bf16_f32 v161, v122, v123\n"
        "v_cvt_pk_bf16_f32 v166, v120, v121\n"
        "v_cvt_pk_bf16_f32 v167, v122, v123\n"
        "v_mfma_f32_16x16x32_bf16 v[120:123], a[236:239], a[192:195], 0\n"
        "ds_read_b64_tr_b16 v[68:69], v255 offset:12320\n"
        "ds_read_b64_tr_b16 v[70:71], v255 offset:13088\n"
        "v_mfma_f32_16x16x32_bf16 v[120:123], a[240:243], a[196:199], v[120:123]\n"
        "ds_read_b128 a[236:239], v241 offset:1664\n"
        "ds_read_b128 a[240:243], v240 offset:1792\n"
        "v_mfma_f32_16x16x32_bf16 v[120:123], a[244:247], a[200:203], v[120:123]\n"
        "ds_read_b64_tr_b16 v[72:73], v255 offset:12352\n"
        "ds_read_b64_tr_b16 v[74:75], v255 offset:13120\n"
        "v_mfma_f32_16x16x32_bf16 v[120:123], a[248:251], a[204:207], v[120:123]\n"
        "ds_read_b128 a[244:247], v241 offset:1792\n"
        "v_max3_u32 v236, v236, v192, v193\n"
        "v_mfma_f32_16x16x32_bf16 v[120:123], a[216:219], a[208:211], v[120:123]\n"
        "s_waitcnt lgkmcnt(8)\n"
        "ds_write_b128 v235, v[64:67] offset:12288\n"
        "v_mfma_f32_16x16x32_bf16 v[120:123], a[220:223], a[212:215], v[120:123]\n"
        "v_max3_u32 v236, v236, v124, v125\n"
        "v_cmp_lt_u32 vcc, s24, v236\n"
        "v_cvt_pk_bf16_f32 v128, v112, v113\n"
        "v_cvt_pk_bf16_f32 v129, v114, v115\n"
        "v_mfma_f32_16x16x32_bf16 v[112:115], a[72:75], a[224:227], 0\n"
        "s_cmp_lg_u64 vcc, 0\n"
        "s_addc_u32 s25, s25, s25\n"
        "s_waitcnt vmcnt(2)\n"
        "ds_read_b64_tr_b16 v[76:77], v255 offset:15360\n"
        "v_mfma_f32_16x16x32_bf16 v[112:115], a[76:79], a[228:231], v[112:115]\n"
        "buffer_load_dwordx4 v234, s[52:55], 0 offen offset:2048 lds\n"
        "v_mfma_f32_16x16x32_bf16 v[112:115], a[80:83], a[232:235], v[112:115]\n"
        "s_waitcnt lgkmcnt(5)\n"
        "ds_read_b64_tr_b16 v[78:79], v255 offset:16128\n"
        "v_and_b32 v239, 32, v245\n"
        "v_mfma_f32_16x16x32_bf16 v[112:115], a[84:87], a[236:239], v[112:115]\n"
        "s_add_u32 m0, s65, 3072\n"
        "v_cvt_pk_bf16_f32 v136, v116, v117\n"
        "v_cvt_pk_bf16_f32 v137, v118, v119\n"
        "v_mfma_f32_16x16x32_bf16 v[112:115], a[88:91], a[240:243], v[112:115]\n"
        "s_waitcnt lgkmcnt(3)\n"
        "v_xor_b32 v251, 0x2000, v251\n"
        "v_xor_b32 v252, 0x2000, v252\n"
        "v_mfma_f32_16x16x32_bf16 v[112:115], a[92:95], a[244:247], v[112:115]\n"
        "s_barrier\n"
        "v_mad_i32_i24 v239, v239, -12, v235\n"
        "v_cvt_pk_bf16_f32 v144, v120, v121\n"
        "v_mfma_f32_16x16x32_bf16 v[116:119], a[116:119], a[244:247], 0\n"
        "ds_write_b128 v239, v[68:71] offset:12480\n"
        "v_mfma_f32_16x16x32_bf16 v[116:119], a[112:115], a[240:243], v[116:119]\n"
        "buffer_load_dwordx4 v232, s[52:55], s63 offen lds\n"
        "v_mfma_f32_16x16x32_bf16 v[116:119], a[108:111], a[236:239], v[116:119]\n"
        "ds_read_b64_tr_b16 v[88:89], v255 offset:15392\n"
        "ds_read_b64_tr_b16 v[90:91], v255 offset:16160\n"
        "v_mfma_f32_16x16x32_bf16 v[116:119], a[104:107], a[232:235], v[116:119]\n"
        "v_cvt_pk_bf16_f32 v145, v122, v123\n"
        "v_cvt_pk_bf16_f32 v172, v112, v113\n"
        "v_mfma_f32_16x16x32_bf16 v[116:119], a[100:103], a[228:231], v[116:119]\n"
        "v_cvt_pk_bf16_f32 v173, v114, v115\n"
        "v_add_u32 v247, s45, v247\n"
        "v_mfma_f32_16x16x32_bf16 v[116:119], a[96:99], a[224:227], v[116:119]\n"
        "ds_write_b128 v254, v[72:75] offset:12288\n"
        "v_mfma_f32_16x16x32_bf16 v[120:123], a[120:123], a[224:227], 0\n"
        "s_nop 2\n"
        "v_mfma_f32_16x16x32_bf16 v[120:123], a[124:127], a[228:231], v[120:123]\n"
        "ds_read_b128 v[162:165], v251 offset:0\n"
        "s_nop 0\n"
        "v_mfma_f32_16x16x32_bf16 v[120:123], a[128:131], a[232:235], v[120:123]\n"
        "buffer_load_dwordx4 v233, s[52:55], s63 offen offset:1024 lds\n"
        "v_mfma_f32_16x16x32_bf16 v[120:123], a[132:135], a[236:239], v[120:123]\n"
        "ds_read_b64_tr_b16 v[92:93], v255 offset:15424\n"
        "ds_read_b64_tr_b16 v[94:95], v255 offset:16192\n"
        "v_mfma_f32_16x16x32_bf16 v[120:123], a[136:139], a[240:243], v[120:123]\n"
        "v_cvt_pk_bf16_f32 v174, v116, v117\n"
        "v_cvt_pk_bf16_f32 v175, v118, v119\n"
        "v_mfma_f32_16x16x32_bf16 v[120:123], a[140:143], a[244:247], v[120:123]\n"
        "s_nop 2\n"
        "v_mfma_f32_16x16x32_bf16 v[112:115], a[20:23], a[244:247], 0\n"
        "s_nop 2\n"
        "v_mfma_f32_16x16x32_bf16 v[112:115], a[16:19], a[240:243], v[112:115]\n"
        "v_cvt_pk_bf16_f32 v190, v120, v121\n"
        "v_cvt_pk_bf16_f32 v191, v122, v123\n"
        "v_mfma_f32_16x16x32_bf16 v[112:115], a[12:15], a[236:239], v[112:115]\n"
        "v_mfma_f32_16x16x32_bf16 v[112:115], a[8:11], a[232:235], v[112:115]\n"
        "s_waitcnt vmcnt(2)\n"
        "ds_read_b64_tr_b16 v[212:213], v255 offset:0\n"
        "ds_read_b64_tr_b16 v[214:215], v255 offset:768\n"
        "v_mfma_f32_16x16x32_bf16 v[112:115], a[4:7], a[228:231], v[112:115]\n"
        "buffer_load_dwordx4 v234, s[52:55], s63 offen offset:2048 lds\n"
        "v_mfma_f32_16x16x32_bf16 v[112:115], a[0:3], a[224:227], v[112:115]\n"
        "ds_read_b128 v[182:185], v251 offset:512\n"
        "s_nop 0\n"
        "v_mfma_f32_16x16x32_bf16 v[116:119], a[24:27], a[224:227], 0\n"
        "s_nop 2\n"
        "v_mfma_f32_16x16x32_bf16 v[116:119], a[28:31], a[228:231], v[116:119]\n"
        "s_waitcnt lgkmcnt(3)\n"
        "ds_write_b128 v235, v[76:79] offset:15360\n"
        "v_mfma_f32_16x16x32_bf16 v[116:119], a[32:35], a[232:235], v[116:119]\n"
        "v_cvt_pk_bf16_f32 v176, v112, v113\n"
        "v_cvt_pk_bf16_f32 v177, v114, v115\n"
        "v_mfma_f32_16x16x32_bf16 v[116:119], a[36:39], a[236:239], v[116:119]\n"
        "s_add_u32 m0, s65, 6144\n"
        "v_mfma_f32_16x16x32_bf16 v[116:119], a[40:43], a[240:243], v[116:119]\n"
        "ds_read_b64_tr_b16 v[216:217], v255 offset:32\n"
        "ds_read_b64_tr_b16 v[218:219], v255 offset:800\n"
        "v_mfma_f32_16x16x32_bf16 v[116:119], a[44:47], a[244:247], v[116:119]\n"
        "ds_write_b32 v245, v80 offset:768\n"
        "v_mfma_f32_16x16x32_bf16 v[120:123], a[68:71], a[244:247], 0\n"
        "buffer_load_dwordx4 v232, s[52:55], s61 offen lds\n"
        "v_mfma_f32_16x16x32_bf16 v[120:123], a[64:67], a[240:243], v[120:123]\n"
        "s_nop 2\n"
        "v_mfma_f32_16x16x32_bf16 v[120:123], a[60:63], a[236:239], v[120:123]\n"
        ".long 0xbec200ff\n"
        "43:\n"
        "s_branch 60b\n"
        "ds_write_b128 v239, v[88:91] offset:15552\n"
        "v_mfma_f32_16x16x32_bf16 v[120:123], a[56:59], a[232:235], v[120:123]\n"
        "v_cvt_pk_bf16_f32 v178, v116, v117\n"
        "v_cvt_pk_bf16_f32 v179, v118, v119\n"
        "v_mfma_f32_16x16x32_bf16 v[120:123], a[52:55], a[228:231], v[120:123]\n"
        "v_mfma_f32_16x16x32_bf16 v[120:123], a[48:51], a[224:227], v[120:123]\n"
        "ds_write_b32 v245, v81 offset:1792\n"
        "v_mfma_f32_16x16x32_bf16 v[112:115], a[224:227], a[144:147], 0\n"
        "ds_read_b64_tr_b16 v[220:221], v255 offset:64\n"
        "ds_read_b64_tr_b16 v[222:223], v255 offset:832\n"
        "v_mfma_f32_16x16x32_bf16 v[112:115], a[228:231], a[148:151], v[112:115]\n"
        "ds_write_b32 v245, v82 offset:2816\n"
        "v_mfma_f32_16x16x32_bf16 v[112:115], a[232:235], a[152:155], v[112:115]\n"
        "ds_write_b32 v245, v83 offset:3840\n"
        "v_mfma_f32_16x16x32_bf16 v[112:115], a[236:239], a[156:159], v[112:115]\n"
        "buffer_load_dwordx4 v233, s[52:55], s61 offen offset:1024 lds\n"
        "v_mfma_f32_16x16x32_bf16 v[112:115], a[240:243], a[160:163], v[112:115]\n"
        "ds_read_b128 a[248:251], v240 offset:12288\n"
        "v_cvt_pk_bf16_f32 v180, v120, v121\n"
        "v_mfma_f32_16x16x32_bf16 v[112:115], a[244:247], a[164:167], v[112:115]\n"
        "ds_write_b128 v254, v[92:95] offset:15360\n"
        "v_mfma_f32_16x16x32_bf16 v[96:99], v[152:155], v[156:159], v[0:3]\n"
        "v_cvt_pk_bf16_f32 v181, v122, v123\n"
        "v_cvt_pk_bf16_f32 v186, v120, v121\n"
        "v_mfma_f32_16x16x32_bf16 v[96:99], v[168:171], v[160:163], v[96:99]\n"
        "s_waitcnt vmcnt(2)\n"
        "ds_read_b64_tr_b16 v[224:225], v255 offset:3072\n"
        "ds_read_b64_tr_b16 v[226:227], v255 offset:3840\n"
        "s_sub_u32 s31, s30, s28\n"
        "v_mfma_f32_16x16x32_bf16 v[100:103], v[172:175], v[156:159], v[4:7]\n"
        "ds_read_b128 a[216:219], v241 offset:12288\n"
        "s_sub_u32 s45, s28, s29\n"
        "v_cvt_pk_bf16_f32 v187, v122, v123\n"
        "v_mfma_f32_16x16x32_bf16 v[100:103], v[188:191], v[164:167], v[100:103]\n"
        "ds_read_b128 a[220:223], v240 offset:12416\n"
        "s_add_u32 s28, s28, s31\n"
        "v_cvt_pk_bf16_f32 v130, v112, v113\n"
        "v_mfma_f32_16x16x32_bf16 v[116:119], a[244:247], a[188:191], 0\n"
        "buffer_load_dwordx4 v234, s[52:55], s61 offen offset:2048 lds\n"
        "v_mfma_f32_16x16x32_bf16 v[116:119], a[240:243], a[184:187], v[116:119]\n"
        "s_waitcnt lgkmcnt(8)\n"
        "ds_write_b128 v235, v[212:215] offset:0\n"
        "v_mfma_f32_16x16x32_bf16 v[116:119], a[236:239], a[180:183], v[116:119]\n"
        "v_cvt_pk_bf16_f32 v131, v114, v115\n"
        "v_mfma_f32_16x16x32_bf16 v[116:119], a[232:235], a[176:179], v[116:119]\n"
        "ds_read_b64_tr_b16 v[88:89], v255 offset:3104\n"
        "ds_read_b64_tr_b16 v[90:91], v255 offset:3872\n"
        "v_mfma_f32_16x16x32_bf16 v[116:119], a[228:231], a[172:175], v[116:119]\n"
        "ds_read_b128 v[162:165], v252 offset:0\n"
        "v_mfma_f32_16x16x32_bf16 v[116:119], a[224:227], a[168:171], v[116:119]\n"
        "s_add_u32 m0, s65, 9216\n"
        "v_exp_f32 v96, v96\n"
        "v_mfma_f32_16x16x32_bf16 v[104:107], v[152:155], v[176:179], v[16:19]\n"
        "v_exp_f32 v97, v97\n"
        "v_mfma_f32_16x16x32_bf16 v[104:107], v[168:171], v[180:183], v[104:107]\n"
        "ds_write_b128 v239, v[216:219] offset:192\n"
        "v_mfma_f32_16x16x32_bf16 v[108:111], v[172:175], v[176:179], v[20:23]\n"
        "buffer_load_dwordx4 v232, s[52:55], s56 offen lds\n"
        "v_mfma_f32_16x16x32_bf16 v[108:111], v[188:191], v[184:187], v[108:111]\n"
        "ds_read_b128 v[182:185], v252 offset:512\n"
        "v_cvt_pk_bf16_f32 v64, v96, v97\n"
        "v_mfma_f32_16x16x32_bf16 v[120:123], a[224:227], a[192:195], 0\n"
        "ds_read_b128 a[224:227], v241 offset:12416\n"
        "ds_read_b64_tr_b16 v[92:93], v255 offset:3136\n"
        "v_mfma_f32_16x16x32_bf16 v[120:123], a[228:231], a[196:199], v[120:123]\n"
        "ds_read_b64_tr_b16 v[94:95], v255 offset:3904\n"
        "ds_read_b128 a[228:231], v240 offset:12544\n"
        "v_mfma_f32_16x16x32_bf16 v[120:123], a[232:235], a[200:203], v[120:123]\n"
        "ds_read_b128 a[232:235], v241 offset:12544\n"
        "v_cvt_pk_bf16_f32 v138, v116, v117\n"
        "v_mfma_f32_16x16x32_bf16 v[120:123], a[236:239], a[204:207], v[120:123]\n"
        "ds_read_b128 v[202:205], v251 offset:4096\n"
        "v_cvt_pk_bf16_f32 v139, v118, v119\n"
        "v_mfma_f32_16x16x32_bf16 v[120:123], a[240:243], a[208:211], v[120:123]\n"
        "ds_write_b128 v254, v[220:223] offset:0\n"
        "v_mfma_f32_16x16x32_bf16 v[120:123], a[244:247], a[212:215], v[120:123]\n"
        "s_waitcnt lgkmcnt(9)\n"
        "v_exp_f32 v98, v98\n"
        "v_mfma_f32_16x16x32_bf16 v[112:115], a[0:3], a[248:251], 0\n"
        "buffer_load_dwordx4 v233, s[52:55], s56 offen offset:1024 lds\n"
        "v_mfma_f32_16x16x32_bf16 v[112:115], a[4:7], a[216:219], v[112:115]\n"
        "s_waitcnt vmcnt(2)\n"
        "ds_read_b64_tr_b16 v[212:213], v255 offset:6144\n"
        "ds_read_b64_tr_b16 v[214:215], v255 offset:6912\n"
        "v_mfma_f32_16x16x32_bf16 v[112:115], a[8:11], a[220:223], v[112:115]\n"
        "s_waitcnt lgkmcnt(5)\n"
        "v_exp_f32 v100, v100\n"
        "v_mfma_f32_16x16x32_bf16 v[112:115], a[12:15], a[224:227], v[112:115]\n"
        "v_exp_f32 v99, v99\n"
        "v_mfma_f32_16x16x32_bf16 v[112:115], a[16:19], a[228:231], v[112:115]\n"
        "s_waitcnt lgkmcnt(3)\n"
        "v_exp_f32 v101, v101\n"
        "v_mfma_f32_16x16x32_bf16 v[112:115], a[20:23], a[232:235], v[112:115]\n"
        "ds_write_b128 v235, v[224:227] offset:3072\n"
        "v_mfma_f32_16x16x32_bf16 v[116:119], a[44:47], a[232:235], 0\n"
        "v_cvt_pk_bf16_f32 v65, v98, v99\n"
        "v_cvt_pk_bf16_f32 v66, v100, v101\n"
        "v_mfma_f32_16x16x32_bf16 v[116:119], a[40:43], a[228:231], v[116:119]\n"
        "v_exp_f32 v102, v102\n"
        "v_mfma_f32_16x16x32_bf16 v[116:119], a[36:39], a[224:227], v[116:119]\n"
        "buffer_load_dwordx4 v234, s[52:55], s56 offen offset:2048 lds\n"
        "s_add_u32 s35, s47, 786432\n"
        "s_sub_u32 s46, s35, 100663296\n"
        "s_cmp_lt_u32 s46, 786432\n"
        "v_mfma_f32_16x16x32_bf16 v[116:119], a[32:35], a[220:223], v[116:119]\n"
        "s_cselect_b32 s35, s46, s35\n"
        "s_add_u32 s41, s30, s40\n"
        "s_mov_b32 s30, s29\n"
        "s_add_u32 s29, s29, s45\n"
        "v_mfma_f32_16x16x32_bf16 v[116:119], a[28:31], a[216:219], v[116:119]\n"
        "ds_read_b64_tr_b16 v[216:217], v255 offset:6176\n"
        "ds_read_b64_tr_b16 v[218:219], v255 offset:6944\n"
        "v_mfma_f32_16x16x32_bf16 v[116:119], a[24:27], a[248:251], v[116:119]\n"
        "v_exp_f32 v103, v103\n"
        "v_cvt_pk_bf16_f32 v146, v120, v121\n"
        "v_cvt_pk_bf16_f32 v147, v122, v123\n"
        "v_mfma_f32_16x16x32_bf16 v[120:123], a[48:51], a[248:251], 0\n"
        "ds_write_b128 v239, v[88:91] offset:3264\n"
        "v_mfma_f32_16x16x32_bf16 v[120:123], a[52:55], a[216:219], v[120:123]\n"
        "v_cvt_pk_bf16_f32 v67, v102, v103\n"
        "v_cvt_pk_bf16_f32 v196, v112, v113\n"
        "v_mfma_f32_16x16x32_bf16 v[120:123], a[56:59], a[220:223], v[120:123]\n"
        "s_mov_b32 m0, s48\n"
        "v_exp_f32 v104, v104\n"
        "v_mfma_f32_16x16x32_bf16 v[120:123], a[60:63], a[224:227], v[120:123]\n"
        "ds_read_b64_tr_b16 v[220:221], v255 offset:6208\n"
        "ds_read_b64_tr_b16 v[222:223], v255 offset:6976\n"
        "v_mfma_f32_16x16x32_bf16 v[120:123], a[64:67], a[228:231], v[120:123]\n"
        "buffer_load_dwordx4 v249, s[12:15], s36 offen offset:0 lds\n"
        "v_mfma_f32_16x16x32_bf16 v[120:123], a[68:71], a[232:235], v[120:123]\n"
        "v_exp_f32 v105, v105\n"
        "v_cvt_pk_bf16_f32 v197, v114, v115\n"
        "v_mfma_f32_16x16x32_bf16 v[112:115], a[92:95], a[232:235], 0\n"
        "v_exp_f32 v106, v106\n"
        "v_mfma_f32_16x16x32_bf16 v[112:115], a[88:91], a[228:231], v[112:115]\n"
        "ds_write_b128 v254, v[92:95] offset:3072\n"
        "v_mfma_f32_16x16x32_bf16 v[112:115], a[84:87], a[224:227], v[112:115]\n"
        "v_cvt_pk_bf16_f32 v72, v104, v105\n"
        "v_cvt_pk_bf16_f32 v198, v116, v117\n"
        "v_mfma_f32_16x16x32_bf16 v[112:115], a[80:83], a[220:223], v[112:115]\n"
        "v_exp_f32 v108, v108\n"
        "v_mfma_f32_16x16x32_bf16 v[112:115], a[76:79], a[216:219], v[112:115]\n"
        "s_waitcnt vmcnt(1)\n"
        "ds_read_b64_tr_b16 v[224:225], v255 offset:9216\n"
        "ds_read_b64_tr_b16 v[226:227], v255 offset:9984\n"
        "v_mfma_f32_16x16x32_bf16 v[112:115], a[72:75], a[248:251], v[112:115]\n"
        "buffer_load_dwordx4 v253, s[12:15], s36 offen offset:1024 lds\n"
        "v_cvt_pk_bf16_f32 v199, v118, v119\n"
        "s_nop 1\n"
        "v_mfma_f32_16x16x32_bf16 v[96:99], v[152:155], v[196:199], v[32:35]\n"
        "v_exp_f32 v107, v107\n"
        "v_cvt_pk_bf16_f32 v200, v120, v121\n"
        "v_cvt_pk_bf16_f32 v201, v122, v123\n"
        "s_nop 1\n"
        "v_mfma_f32_16x16x32_bf16 v[96:99], v[168:171], v[200:203], v[96:99]\n"
        "v_exp_f32 v109, v109\n"
        "v_mfma_f32_16x16x32_bf16 v[100:103], v[172:175], v[196:199], v[36:39]\n"
        "s_waitcnt lgkmcnt(3)\n"
        "ds_write_b128 v235, v[212:215] offset:6144\n"
        "v_cvt_pk_bf16_f32 v206, v120, v121\n"
        "v_cvt_pk_bf16_f32 v207, v122, v123\n"
        "s_nop 1\n"
        "v_mfma_f32_16x16x32_bf16 v[100:103], v[188:191], v[204:207], v[100:103]\n"
        "s_cmp_eq_u32 s37, 100663296\n"
        "v_cvt_pk_bf16_f32 v73, v106, v107\n"
        "v_cvt_pk_bf16_f32 v74, v108, v109\n"
        "v_mfma_f32_16x16x32_bf16 v[116:119], a[96:99], a[248:251], 0\n"
        "s_cselect_b32 s46, 0, s37\n"
        "ds_read_b128 v[202:205], v252 offset:4096\n"
        "v_cvt_pk_bf16_f32 v192, v112, v113\n"
        "v_mfma_f32_16x16x32_bf16 v[116:119], a[100:103], a[216:219], v[116:119]\n"
        "ds_read_b64_tr_b16 v[88:89], v255 offset:9248\n"
        "ds_read_b64_tr_b16 v[90:91], v255 offset:10016\n"
        "s_add_u32 m0, s41, 0\n"
        "v_mfma_f32_16x16x32_bf16 v[116:119], a[104:107], a[220:223], v[116:119]\n"
        "buffer_load_dwordx4 v242, s[8:11], s47 offen lds\n"
        "v_mfma_f32_16x16x32_bf16 v[116:119], a[108:111], a[224:227], v[116:119]\n"
        "s_add_u32 s38, s46, s43\n"
        "v_exp_f32 v110, v110\n"
        "v_mfma_f32_16x16x32_bf16 v[116:119], a[112:115], a[228:231], v[116:119]\n"
        "s_add_u32 s39, s46, s44\n"
        "v_exp_f32 v111, v111\n"
        "v_mfma_f32_16x16x32_bf16 v[116:119], a[116:119], a[232:235], v[116:119]\n"
        "ds_write_b128 v239, v[216:219] offset:6336\n"
        "s_add_u32 s37, s37, 6291456\n"
        "v_mfma_f32_16x16x32_bf16 v[120:123], a[140:143], a[232:235], 0\n"
        "v_cvt_pk_bf16_f32 v75, v110, v111\n"
        "v_cvt_pk_bf16_f32 v193, v114, v115\n"
        "v_mfma_f32_16x16x32_bf16 v[120:123], a[136:139], a[228:231], v[120:123]\n"
        "s_add_u32 m0, s41, 1024\n"
        "v_exp_f32 v96, v96\n"
        "v_mfma_f32_16x16x32_bf16 v[120:123], a[132:135], a[224:227], v[120:123]\n"
        "ds_read_b64_tr_b16 v[92:93], v255 offset:9280\n"
        "ds_read_b64_tr_b16 v[94:95], v255 offset:10048\n"
        "v_mfma_f32_16x16x32_bf16 v[120:123], a[128:131], a[220:223], v[120:123]\n"
        "buffer_load_dwordx4 v243, s[8:11], s47 offen lds\n"
        "v_mfma_f32_16x16x32_bf16 v[120:123], a[124:127], a[216:219], v[120:123]\n"
        "v_exp_f32 v97, v97\n"
        "v_mfma_f32_16x16x32_bf16 v[120:123], a[120:123], a[248:251], v[120:123]\n"
        "v_exp_f32 v98, v98\n"
        "v_mfma_f32_16x16x32_bf16 v[112:115], a[248:251], a[144:147], 0\n"
        "ds_write_b128 v254, v[220:223] offset:6144\n"
        "v_mfma_f32_16x16x32_bf16 v[112:115], a[216:219], a[148:151], v[112:115]\n"
        "v_cvt_pk_bf16_f32 v80, v96, v97\n"
        "v_cvt_pk_bf16_f32 v194, v116, v117\n"
        "v_mfma_f32_16x16x32_bf16 v[112:115], a[220:223], a[152:155], v[112:115]\n"
        "s_add_u32 m0, s41, 2048\n"
        "ds_read_b128 a[236:239], v240 offset:13824\n"
        "v_cvt_pk_bf16_f32 v195, v118, v119\n"
        "v_mfma_f32_16x16x32_bf16 v[112:115], a[224:227], a[156:159], v[112:115]\n"
        "ds_read_b128 a[240:243], v241 offset:13824\n"
        "v_cvt_pk_bf16_f32 v208, v120, v121\n"
        "v_mfma_f32_16x16x32_bf16 v[112:115], a[228:231], a[160:163], v[112:115]\n"
        "buffer_load_dwordx4 v244, s[8:11], s47 offen lds\n"
        "v_mfma_f32_16x16x32_bf16 v[112:115], a[232:235], a[164:167], v[112:115]\n"
        "ds_read_b128 a[244:247], v240 offset:13952\n"
        "v_cvt_pk_bf16_f32 v209, v122, v123\n"
        "v_mfma_f32_16x16x32_bf16 v[104:107], v[192:195], v[156:159], v[8:11]\n"
        "v_exp_f32 v100, v100\n"
        "v_mfma_f32_16x16x32_bf16 v[104:107], v[208:211], v[160:163], v[104:107]\n"
        "s_waitcnt lgkmcnt(4)\n"
        "ds_write_b128 v235, v[224:227] offset:9216\n"
        "v_mfma_f32_16x16x32_bf16 v[116:119], a[232:235], a[188:191], 0\n"
        "v_exp_f32 v99, v99\n"
        "v_mfma_f32_16x16x32_bf16 v[116:119], a[228:231], a[184:187], v[116:119]\n"
        "s_add_u32 m0, s41, 12288\n"
        "v_exp_f32 v101, v101\n"
        "v_mfma_f32_16x16x32_bf16 v[116:119], a[224:227], a[180:183], v[116:119]\n"
        "v_cvt_pk_bf16_f32 v81, v98, v99\n"
        "v_cvt_pk_bf16_f32 v82, v100, v101\n"
        "v_mfma_f32_16x16x32_bf16 v[116:119], a[220:223], a[176:179], v[116:119]\n"
        "buffer_load_dwordx4 v242, s[8:11], s35 offen lds\n"
        "v_mfma_f32_16x16x32_bf16 v[116:119], a[216:219], a[172:175], v[116:119]\n"
        "v_exp_f32 v102, v102\n"
        "v_mfma_f32_16x16x32_bf16 v[116:119], a[248:251], a[168:171], v[116:119]\n"
        "v_exp_f32 v103, v103\n"
        "v_mfma_f32_16x16x32_bf16 v[108:111], v[192:195], v[176:179], v[24:27]\n"
        "ds_write_b128 v239, v[88:91] offset:9408\n"
        "v_mfma_f32_16x16x32_bf16 v[108:111], v[208:211], v[180:183], v[108:111]\n"
        "v_cvt_pk_bf16_f32 v83, v102, v103\n"
        "v_cvt_pk_bf16_f32 v132, v112, v113\n"
        "v_mfma_f32_16x16x32_bf16 v[120:123], a[248:251], a[192:195], 0\n"
        "ds_read_b128 a[248:251], v241 offset:13952\n"
        "s_add_u32 m0, s41, 13312\n"
        "v_cvt_pk_bf16_f32 v133, v114, v115\n"
        "v_mfma_f32_16x16x32_bf16 v[120:123], a[216:219], a[196:199], v[120:123]\n"
        "ds_read_b128 a[216:219], v240 offset:14080\n"
        "v_cvt_pk_bf16_f32 v140, v116, v117\n"
        "v_mfma_f32_16x16x32_bf16 v[120:123], a[220:223], a[200:203], v[120:123]\n"
        "buffer_load_dwordx4 v243, s[8:11], s35 offen lds\n"
        "v_mfma_f32_16x16x32_bf16 v[120:123], a[224:227], a[204:207], v[120:123]\n"
        "ds_read_b128 a[220:223], v241 offset:14080\n"
        "v_cvt_pk_bf16_f32 v141, v118, v119\n"
        "v_mfma_f32_16x16x32_bf16 v[120:123], a[228:231], a[208:211], v[120:123]\n"
        "v_exp_f32 v104, v104\n"
        "v_mfma_f32_16x16x32_bf16 v[120:123], a[232:235], a[212:215], v[120:123]\n"
        "ds_write_b128 v254, v[92:95] offset:9216\n"
        "v_mfma_f32_16x16x32_bf16 v[96:99], v[192:195], v[196:199], v[40:43]\n"
        "v_exp_f32 v105, v105\n"
        "v_mfma_f32_16x16x32_bf16 v[96:99], v[208:211], v[200:203], v[96:99]\n"
        "s_waitcnt lgkmcnt(5)\n"
        "s_add_u32 m0, s41, 14336\n"
        "v_exp_f32 v106, v106\n"
        "v_mfma_f32_16x16x32_bf16 v[112:115], a[72:75], a[236:239], 0\n"
        "v_cvt_pk_bf16_f32 v68, v104, v105\n"
        "v_cvt_pk_bf16_f32 v148, v120, v121\n"
        "v_mfma_f32_16x16x32_bf16 v[112:115], a[76:79], a[240:243], v[112:115]\n"
        "buffer_load_dwordx4 v244, s[8:11], s35 offen lds\n"
        "v_mfma_f32_16x16x32_bf16 v[112:115], a[80:83], a[244:247], v[112:115]\n"
        "s_waitcnt lgkmcnt(1)\n"
        "v_exp_f32 v107, v107\n"
        "v_mfma_f32_16x16x32_bf16 v[112:115], a[84:87], a[248:251], v[112:115]\n"
        "v_cvt_pk_bf16_f32 v69, v106, v107\n"
        "v_cvt_pk_bf16_f32 v149, v122, v123\n"
        "v_mfma_f32_16x16x32_bf16 v[112:115], a[88:91], a[216:219], v[112:115]\n"
        "v_exp_f32 v108, v108\n"
        "v_mfma_f32_16x16x32_bf16 v[112:115], a[92:95], a[220:223], v[112:115]\n"
        "v_exp_f32 v109, v109\n"
        "v_mfma_f32_16x16x32_bf16 v[116:119], a[116:119], a[220:223], 0\n"
        "v_bfe_u32 v238, v250, 3, 4\n"
        "v_cvt_pk_bf16_f32 v76, v108, v109\n"
        "v_mfma_f32_16x16x32_bf16 v[116:119], a[112:115], a[216:219], v[116:119]\n"
        "v_exp_f32 v110, v110\n"
        "v_mfma_f32_16x16x32_bf16 v[116:119], a[108:111], a[248:251], v[116:119]\n"
        "v_exp_f32 v111, v111\n"
        "v_mfma_f32_16x16x32_bf16 v[116:119], a[104:107], a[244:247], v[116:119]\n"
        "v_cvt_pk_bf16_f32 v212, v112, v113\n"
        "v_cvt_pk_bf16_f32 v213, v114, v115\n"
        "v_mfma_f32_16x16x32_bf16 v[116:119], a[100:103], a[240:243], v[116:119]\n"
        "v_cvt_pk_bf16_f32 v77, v110, v111\n"
        "v_add_u32 v245, s45, v245\n"
        "v_mfma_f32_16x16x32_bf16 v[116:119], a[96:99], a[236:239], v[116:119]\n"
        "v_exp_f32 v96, v96\n"
        "v_mfma_f32_16x16x32_bf16 v[120:123], a[120:123], a[236:239], 0\n"
        "v_exp_f32 v97, v97\n"
        "v_mfma_f32_16x16x32_bf16 v[120:123], a[124:127], a[240:243], v[120:123]\n"
        "s_add_u32 s47, s47, 6291456\n"
        "v_exp_f32 v98, v98\n"
        "v_mfma_f32_16x16x32_bf16 v[120:123], a[128:131], a[244:247], v[120:123]\n"
        "s_add_u32 s36, s36, 2097152\n"
        "v_cvt_pk_bf16_f32 v214, v116, v117\n"
        "v_cvt_pk_bf16_f32 v215, v118, v119\n"
        "v_mfma_f32_16x16x32_bf16 v[120:123], a[132:135], a[248:251], v[120:123]\n"
        "s_xor_b32 s48, s48, 8192\n"
        "v_cvt_pk_bf16_f32 v84, v96, v97\n"
        "v_and_b32 v254, 32, v250\n"
        "v_mfma_f32_16x16x32_bf16 v[120:123], a[136:139], a[216:219], v[120:123]\n"
        "s_waitcnt lgkmcnt(0)\n"
        "s_barrier\n"
        "v_exp_f32 v99, v99\n"
        "v_mfma_f32_16x16x32_bf16 v[120:123], a[140:143], a[220:223], v[120:123]\n"
        "v_mad_i32_i24 v238, v238, -4, v250\n"
        "v_cvt_pk_bf16_f32 v85, v98, v99\n"
        "v_mfma_f32_16x16x32_bf16 v[112:115], a[220:223], a[164:167], 0\n"
        "v_mad_i32_i24 v254, v254, -12, v248\n"
        "v_add_u32 v254, 1728, v254\n"
        "v_mfma_f32_16x16x32_bf16 v[112:115], a[216:219], a[160:163], v[112:115]\n"
        "v_add_u32 v238, s49, v238\n"
        "v_add_u32 v240, s31, v240\n"
        "v_mfma_f32_16x16x32_bf16 v[112:115], a[248:251], a[156:159], v[112:115]\n"
        "v_cvt_pk_bf16_f32 v230, v120, v121\n"
        "v_cvt_pk_bf16_f32 v231, v122, v123\n"
        "v_mfma_f32_16x16x32_bf16 v[112:115], a[244:247], a[152:155], v[112:115]\n"
        "v_add_u32 v241, s31, v241\n"
        "v_mfma_f32_16x16x32_bf16 v[112:115], a[240:243], a[148:151], v[112:115]\n"
        "v_mfma_f32_16x16x32_bf16 v[112:115], a[236:239], a[144:147], v[112:115]\n"
        "s_nop 2\n"
        "v_mfma_f32_16x16x32_bf16 v[100:103], v[212:215], v[156:159], v[12:15]\n"
        "s_nop 2\n"
        "v_mfma_f32_16x16x32_bf16 v[100:103], v[228:231], v[164:167], v[100:103]\n"
        "v_cvt_pk_bf16_f32 v134, v112, v113\n"
        "v_cvt_pk_bf16_f32 v135, v114, v115\n"
        "v_mfma_f32_16x16x32_bf16 v[116:119], a[236:239], a[168:171], 0\n"
        "v_mfma_f32_16x16x32_bf16 v[116:119], a[240:243], a[172:175], v[116:119]\n"
        "v_mfma_f32_16x16x32_bf16 v[116:119], a[244:247], a[176:179], v[116:119]\n"
        "s_waitcnt lgkmcnt(0)\n"
        "s_cmp_eq_u32 s42, 2\n"
        "s_cbranch_scc1 12f\n"
        "s_cmp_eq_u32 s42, 3\n"
        "s_cbranch_scc1 12f\n"
        ".p2align 6\n"
        "s_nop 0\n"
        "s_nop 0\n"
        "s_nop 0\n"
        "s_nop 0\n"
        "s_nop 0\n"
        "s_nop 0\n"
        "11:\n"
        "10:\n"
        "v_mfma_f32_16x16x32_bf16 v[116:119], a[248:251], a[180:183], v[116:119]\n"
        "v_mfma_f32_16x16x32_bf16 v[116:119], a[216:219], a[184:187], v[116:119]\n"
        "s_nop 0\n"
        "v_exp_f32 v100, v100\n"
        "v_mfma_f32_16x16x32_bf16 v[116:119], a[220:223], a[188:191], v[116:119]\n"
        "v_exp_f32 v101, v101\n"
        "v_mfma_f32_16x16x32_bf16 v[104:107], v[212:215], v[176:179], v[28:31]\n"
        "v_exp_f32 v102, v102\n"
        "v_mfma_f32_16x16x32_bf16 v[104:107], v[228:231], v[184:187], v[104:107]\n"
        "v_exp_f32 v103, v103\n"
        "v_mfma_f32_16x16x32_bf16 v[120:123], a[220:223], a[212:215], 0\n"
        "v_cvt_pk_bf16_f32 v70, v100, v101\n"
        "v_cvt_pk_bf16_f32 v71, v102, v103\n"
        "v_mfma_f32_16x16x32_bf16 v[120:123], a[216:219], a[208:211], v[120:123]\n"
        "v_cvt_pk_bf16_f32 v142, v116, v117\n"
        "v_cvt_pk_bf16_f32 v143, v118, v119\n"
        "v_mfma_f32_16x16x32_bf16 v[120:123], a[248:251], a[204:207], v[120:123]\n"
        "v_mov_b64 v[96:97], 0\n"
        "v_mfma_f32_16x16x32_bf16 v[120:123], a[244:247], a[200:203], v[120:123]\n"
        "v_exp_f32 v104, v104\n"
        "v_mfma_f32_16x16x32_bf16 v[120:123], a[240:243], a[196:199], v[120:123]\n"
        "v_exp_f32 v105, v105\n"
        "v_mfma_f32_16x16x32_bf16 v[120:123], a[236:239], a[192:195], v[120:123]\n"
        "v_exp_f32 v106, v106\n"
        "v_mfma_f32_16x16x32_bf16 v[108:111], v[212:215], v[196:199], v[44:47]\n"
        "v_exp_f32 v107, v107\n"
        "v_mfma_f32_16x16x32_bf16 v[108:111], v[228:231], v[204:207], v[108:111]\n"
        "v_cvt_pk_bf16_f32 v78, v104, v105\n"
        "v_cvt_pk_bf16_f32 v79, v106, v107\n"
        "v_mfma_f32_16x16x32_bf16 v[112:115], a[0:3], a[236:239], 0\n"
        "s_nop 0\n"
        "v_cvt_pk_bf16_f32 v150, v120, v121\n"
        "v_cvt_pk_bf16_f32 v151, v122, v123\n"
        "v_mfma_f32_16x16x32_bf16 v[112:115], a[4:7], a[240:243], v[112:115]\n"
        "v_mfma_f32_16x16x32_bf16 v[112:115], a[8:11], a[244:247], v[112:115]\n"
        "v_exp_f32 v108, v108\n"
        "v_mfma_f32_16x16x32_bf16 v[112:115], a[12:15], a[248:251], v[112:115]\n"
        "v_exp_f32 v109, v109\n"
        "v_mfma_f32_16x16x32_bf16 v[112:115], a[16:19], a[216:219], v[112:115]\n"
        "v_exp_f32 v110, v110\n"
        "v_mfma_f32_16x16x32_bf16 v[112:115], a[20:23], a[220:223], v[112:115]\n"
        "v_exp_f32 v111, v111\n"
        "v_smfmac_f32_16x16x64_bf16 v[96:99], a[252:255], v[64:71], v237\n"
        "v_cvt_pk_bf16_f32 v86, v108, v109\n"
        "v_mfma_f32_16x16x32_bf16 v[100:103], v[132:135], v[68:71], 0\n"
        "v_mov_b64 v[124:125], 0\n"
        "v_cvt_pk_bf16_f32 v87, v110, v111\n"
        "v_mfma_f32_16x16x32_bf16 v[100:103], v[128:131], v[64:67], v[100:103]\n"
        "s_nop 0\n"
        "v_cvt_pk_bf16_f32 v216, v112, v113\n"
        "v_cvt_pk_bf16_f32 v217, v114, v115\n"
        "v_mfma_f32_16x16x32_bf16 v[104:107], v[136:139], v[64:67], 0\n"
        "ds_read_b128 v[222:225], v251 offset:4608\n"
        "v_add_f32 v97, v96, v97\n"
        "v_mfma_f32_16x16x32_bf16 v[104:107], v[140:143], v[68:71], v[104:107]\n"
        "v_rcp_f32 v96, v97\n"
        "v_mfma_f32_16x16x32_bf16 v[108:111], v[148:151], v[68:71], 0\n"
        "v_mul_f32 v100, v96, v100\n"
        "v_mul_f32 v101, v96, v101\n"
        "v_mfma_f32_16x16x32_bf16 v[108:111], v[144:147], v[64:67], v[108:111]\n"
        "v_mul_f32 v102, v96, v102\n"
        "v_mul_f32 v103, v96, v103\n"
        "v_mfma_f32_16x16x32_bf16 v[116:119], a[44:47], a[220:223], 0\n"
        "v_cvt_pk_bf16_f32 v100, v100, v101\n"
        "v_cvt_pk_bf16_f32 v101, v102, v103\n"
        "v_mfma_f32_16x16x32_bf16 v[116:119], a[40:43], a[216:219], v[116:119]\n"
        "v_mul_f32 v104, v96, v104\n"
        "v_mul_f32 v105, v96, v105\n"
        "v_mfma_f32_16x16x32_bf16 v[116:119], a[36:39], a[248:251], v[116:119]\n"
        "v_mul_f32 v106, v96, v106\n"
        "v_mul_f32 v107, v96, v107\n"
        "v_mfma_f32_16x16x32_bf16 v[116:119], a[32:35], a[244:247], v[116:119]\n"
        "v_cvt_pk_bf16_f32 v102, v104, v105\n"
        "v_cvt_pk_bf16_f32 v103, v106, v107\n"
        "v_mfma_f32_16x16x32_bf16 v[116:119], a[28:31], a[240:243], v[116:119]\n"
        "v_mul_f32 v108, v96, v108\n"
        "v_mul_f32 v109, v96, v109\n"
        "v_mfma_f32_16x16x32_bf16 v[116:119], a[24:27], a[236:239], v[116:119]\n"
        "v_mul_f32 v110, v96, v110\n"
        "v_mul_f32 v111, v96, v111\n"
        "v_smfmac_f32_16x16x64_bf16 v[124:127], a[252:255], v[72:79], v237\n"
        "v_cvt_pk_bf16_f32 v108, v108, v109\n"
        "v_mfma_f32_16x16x32_bf16 v[64:67], v[132:135], v[76:79], 0\n"
        "ds_write_b32 v245, v108 offset:4096\n"
        "v_mfma_f32_16x16x32_bf16 v[64:67], v[128:131], v[72:75], v[64:67]\n"
        "ds_read_b128 v[156:159], v247 offset:0\n"
        "v_cvt_pk_bf16_f32 v218, v116, v117\n"
        "v_mfma_f32_16x16x32_bf16 v[68:71], v[136:139], v[72:75], 0\n"
        "ds_read_b128 v[160:163], v247 offset:4096\n"
        "v_cvt_pk_bf16_f32 v219, v118, v119\n"
        "v_mfma_f32_16x16x32_bf16 v[68:71], v[140:143], v[76:79], v[68:71]\n"
        "ds_read_b128 v[164:167], v247 offset:4352\n"
        "v_cvt_pk_bf16_f32 v109, v110, v111\n"
        "v_mfma_f32_16x16x32_bf16 v[120:123], v[148:151], v[76:79], 0\n"
        "ds_write_b32 v245, v109 offset:5120\n"
        "v_mfma_f32_16x16x32_bf16 v[120:123], v[144:147], v[72:75], v[120:123]\n"
        "v_max_u32 v236, v96, v97\n"
        "v_mov_b64 v[116:117], 0\n"
        "v_mfma_f32_16x16x32_bf16 v[112:115], a[48:51], a[236:239], 0\n"
        "v_add_f32 v125, v124, v125\n"
        "v_mfma_f32_16x16x32_bf16 v[112:115], a[52:55], a[240:243], v[112:115]\n"
        "v_rcp_f32 v124, v125\n"
        "v_mfma_f32_16x16x32_bf16 v[112:115], a[56:59], a[244:247], v[112:115]\n"
        "v_mul_f32 v64, v124, v64\n"
        "v_mul_f32 v65, v124, v65\n"
        "v_mfma_f32_16x16x32_bf16 v[112:115], a[60:63], a[248:251], v[112:115]\n"
        "v_mul_f32 v66, v124, v66\n"
        "v_mul_f32 v67, v124, v67\n"
        "v_mfma_f32_16x16x32_bf16 v[112:115], a[64:67], a[216:219], v[112:115]\n"
        "v_cvt_pk_bf16_f32 v64, v64, v65\n"
        "v_cvt_pk_bf16_f32 v65, v66, v67\n"
        "v_mfma_f32_16x16x32_bf16 v[112:115], a[68:71], a[220:223], v[112:115]\n"
        "ds_read_b128 a[216:219], v248 offset:0\n"
        "v_mul_f32 v68, v124, v68\n"
        "v_smfmac_f32_16x16x64_bf16 v[116:119], a[252:255], v[80:87], v237\n"
        "ds_read_b128 a[220:223], v248 offset:3072\n"
        "v_mfma_f32_16x16x32_bf16 v[72:75], v[132:135], v[84:87], 0\n"
        "v_mul_f32 v69, v124, v69\n"
        "v_mul_f32 v70, v124, v70\n"
        "v_mfma_f32_16x16x32_bf16 v[72:75], v[128:131], v[80:83], v[72:75]\n"
        "v_cvt_pk_bf16_f32 v220, v112, v113\n"
        "v_cvt_pk_bf16_f32 v221, v114, v115\n"
        "v_mfma_f32_16x16x32_bf16 v[76:79], v[136:139], v[80:83], 0\n"
        "ds_read_b128 a[224:227], v248 offset:32\n"
        "v_cvt_pk_bf16_f32 v226, v112, v113\n"
        "v_mfma_f32_16x16x32_bf16 v[76:79], v[140:143], v[84:87], v[76:79]\n"
        "ds_read_b128 a[228:231], v248 offset:3104\n"
        "v_cvt_pk_bf16_f32 v227, v114, v115\n"
        "v_mfma_f32_16x16x32_bf16 v[96:99], v[148:151], v[84:87], 0\n"
        "v_mul_f32 v71, v124, v71\n"
        "v_cvt_pk_bf16_f32 v66, v68, v69\n"
        "v_mfma_f32_16x16x32_bf16 v[96:99], v[144:147], v[80:83], v[96:99]\n"
        "v_cvt_pk_bf16_f32 v67, v70, v71\n"
        "v_mul_f32 v120, v124, v120\n"
        "v_mfma_f32_16x16x32_bf16 v[104:107], v[152:155], v[216:219], v[48:51]\n"
        "s_waitcnt lgkmcnt(9)\n"
        "ds_read_b128 v[152:155], v247 offset:256\n"
        "v_mul_f32 v121, v124, v121\n"
        "v_mfma_f32_16x16x32_bf16 v[104:107], v[168:171], v[220:223], v[104:107]\n"
        "v_mul_f32 v122, v124, v122\n"
        "v_mul_f32 v123, v124, v123\n"
        "v_mfma_f32_16x16x32_bf16 v[108:111], v[172:175], v[216:219], v[52:55]\n"
        "v_cvt_pk_bf16_f32 v120, v120, v121\n"
        "v_cvt_pk_bf16_f32 v121, v122, v123\n"
        "v_mfma_f32_16x16x32_bf16 v[108:111], v[188:191], v[224:227], v[108:111]\n"
        "s_waitcnt lgkmcnt(4)\n"
        "ds_read_b128 v[222:225], v252 offset:4608\n"
        "v_max3_u32 v236, v236, v124, v125\n"
        "v_mfma_f32_16x16x32_bf16 v[176:179], v[156:159], a[216:219], 0\n"
        "s_waitcnt lgkmcnt(4)\n"
        "ds_write_b32 v245, v120 offset:4352\n"
        "v_mfma_f32_16x16x32_bf16 v[176:179], v[160:163], a[220:223], v[176:179]\n"
        "ds_write_b32 v245, v121 offset:5376\n"
        "v_mfma_f32_16x16x32_bf16 v[196:199], v[164:167], a[220:223], 0\n"
        "s_waitcnt lgkmcnt(3)\n"
        "ds_read_b128 a[232:235], v254 offset:0\n"
        "v_add_f32 v117, v116, v117\n"
        "v_mfma_f32_16x16x32_bf16 v[196:199], v[152:155], a[216:219], v[196:199]\n"
        "ds_read_b128 a[220:223], v254 offset:3072\n"
        "v_mfma_f32_16x16x32_bf16 v[200:203], v[152:155], a[224:227], 0\n"
        "ds_read_b128 a[216:219], v254 offset:32\n"
        "v_mfma_f32_16x16x32_bf16 v[200:203], v[164:167], a[228:231], v[200:203]\n"
        "v_rcp_f32 v116, v117\n"
        "v_mfma_f32_16x16x32_bf16 v[180:183], v[160:163], a[228:231], 0\n"
        "ds_read_b128 a[228:231], v254 offset:3104\n"
        "v_mul_f32 v72, v116, v72\n"
        "v_mfma_f32_16x16x32_bf16 v[180:183], v[156:159], a[224:227], v[180:183]\n"
        "ds_read_b128 a[236:239], v247 offset:8192\n"
        "v_mul_f32 v73, v116, v73\n"
        "v_mfma_f32_16x16x32_bf16 v[112:115], v[192:195], v[216:219], v[56:59]\n"
        "s_waitcnt lgkmcnt(7)\n"
        "ds_read_b128 a[224:227], v248 offset:16\n"
        "v_mul_f32 v74, v116, v74\n"
        "v_mfma_f32_16x16x32_bf16 v[112:115], v[208:211], v[220:223], v[112:115]\n"
        "ds_read_b128 a[240:243], v247 offset:12288\n"
        "v_mul_f32 v75, v116, v75\n"
        "v_mfma_f32_16x16x32_bf16 v[68:71], v[212:215], v[216:219], v[60:63]\n"
        "ds_read_b128 a[244:247], v247 offset:12544\n"
        "v_cvt_pk_bf16_f32 v72, v72, v73\n"
        "v_mfma_f32_16x16x32_bf16 v[68:71], v[228:231], v[224:227], v[68:71]\n"
        "s_waitcnt lgkmcnt(7)\n"
        "ds_read_b128 a[248:251], v247 offset:8448\n"
        "v_cvt_pk_bf16_f32 v73, v74, v75\n"
        "v_mfma_f32_16x16x32_bf16 v[184:187], v[156:159], a[232:235], 0\n"
        "s_waitcnt lgkmcnt(7)\n"
        "v_exp_f32 v104, v104\n"
        "v_mfma_f32_16x16x32_bf16 v[184:187], v[160:163], a[220:223], v[184:187]\n"
        "v_exp_f32 v105, v105\n"
        "v_mfma_f32_16x16x32_bf16 v[204:207], v[164:167], a[220:223], 0\n"
        "ds_read_b128 a[220:223], v248 offset:3088\n"
        "v_mul_f32 v76, v116, v76\n"
        "v_mfma_f32_16x16x32_bf16 v[204:207], v[152:155], a[232:235], v[204:207]\n"
        "s_waitcnt lgkmcnt(7)\n"
        "ds_read_b128 a[232:235], v254 offset:16\n"
        "v_mul_f32 v77, v116, v77\n"
        "v_mfma_f32_16x16x32_bf16 v[192:195], v[152:155], a[216:219], 0\n"
        "s_waitcnt lgkmcnt(7)\n"
        "v_mul_f32 v78, v116, v78\n"
        "v_mul_f32 v79, v116, v79\n"
        "v_mfma_f32_16x16x32_bf16 v[192:195], v[164:167], a[228:231], v[192:195]\n"
        "v_exp_f32 v106, v106\n"
        "v_mfma_f32_16x16x32_bf16 v[172:175], v[160:163], a[228:231], 0\n"
        "ds_read_b128 a[228:231], v254 offset:3088\n"
        "v_cvt_pk_bf16_f32 v88, v104, v105\n"
        "v_mfma_f32_16x16x32_bf16 v[172:175], v[156:159], a[216:219], v[172:175]\n"
        "s_waitcnt lgkmcnt(6)\n"
        "ds_read_b128 a[216:219], v248 offset:6144\n"
        "v_cvt_pk_bf16_f32 v74, v76, v77\n"
        "v_mfma_f32_16x16x32_bf16 v[212:215], v[156:159], a[224:227], 0\n"
        "s_waitcnt lgkmcnt(3)\n"
        "v_exp_f32 v108, v108\n"
        "v_mfma_f32_16x16x32_bf16 v[212:215], v[160:163], a[220:223], v[212:215]\n"
        "v_exp_f32 v107, v107\n"
        "v_mfma_f32_16x16x32_bf16 v[216:219], v[164:167], a[220:223], 0\n"
        "ds_read_b128 a[220:223], v248 offset:9216\n"
        "v_cvt_pk_bf16_f32 v75, v78, v79\n"
        "v_mfma_f32_16x16x32_bf16 v[216:219], v[152:155], a[224:227], v[216:219]\n"
        "s_waitcnt lgkmcnt(3)\n"
        "ds_read_b128 a[224:227], v248 offset:6176\n"
        "v_cvt_pk_bf16_f32 v89, v106, v107\n"
        "v_mfma_f32_16x16x32_bf16 v[224:227], v[152:155], a[232:235], 0\n"
        "s_waitcnt lgkmcnt(3)\n"
        "v_exp_f32 v109, v109\n"
        "v_mfma_f32_16x16x32_bf16 v[224:227], v[164:167], a[228:231], v[224:227]\n"
        "v_exp_f32 v110, v110\n"
        "v_mfma_f32_16x16x32_bf16 v[220:223], v[160:163], a[228:231], 0\n"
        "ds_read_b128 a[228:231], v248 offset:9248\n"
        "v_cvt_pk_bf16_f32 v90, v108, v109\n"
        "v_mfma_f32_16x16x32_bf16 v[220:223], v[156:159], a[232:235], v[220:223]\n"
        "s_waitcnt lgkmcnt(3)\n"
        "ds_read_b128 a[232:235], v254 offset:6144\n"
        "v_mul_f32 v96, v116, v96\n"
        "v_mfma_f32_16x16x32_bf16 v[176:179], a[236:239], a[216:219], v[176:179]\n"
        "s_waitcnt lgkmcnt(3)\n"
        "v_mul_f32 v97, v116, v97\n"
        "v_mul_f32 v98, v116, v98\n"
        "v_mfma_f32_16x16x32_bf16 v[176:179], a[240:243], a[220:223], v[176:179]\n"
        "v_mul_f32 v99, v116, v99\n"
        "v_cvt_pk_bf16_f32 v96, v96, v97\n"
        "v_mfma_f32_16x16x32_bf16 v[196:199], a[244:247], a[220:223], v[196:199]\n"
        ".long 0xbec200ff\n"
        "44:\n"
        "s_branch 60b\n"
        "ds_read_b128 a[220:223], v254 offset:9216\n"
        "v_cvt_pk_bf16_f32 v97, v98, v99\n"
        "v_mfma_f32_16x16x32_bf16 v[196:199], a[248:251], a[216:219], v[196:199]\n"
        "s_waitcnt lgkmcnt(3)\n"
        "ds_read_b128 a[216:219], v254 offset:6176\n"
        "v_max3_u32 v236, v236, v116, v117\n"
        "v_mfma_f32_16x16x32_bf16 v[200:203], a[248:251], a[224:227], v[200:203]\n"
        "s_waitcnt lgkmcnt(3)\n"
        "ds_read_b128 v[156:159], v247 offset:16384\n"
        "v_mfma_f32_16x16x32_bf16 v[200:203], a[244:247], a[228:231], v[200:203]\n"
        "ds_read_b128 v[160:163], v247 offset:20480\n"
        "v_mfma_f32_16x16x32_bf16 v[180:183], a[240:243], a[228:231], v[180:183]\n"
        "ds_read_b128 a[228:231], v254 offset:9248\n"
        "v_mfma_f32_16x16x32_bf16 v[180:183], a[236:239], a[224:227], v[180:183]\n"
        "s_waitcnt lgkmcnt(5)\n"
        "ds_read_b128 a[224:227], v248 offset:6160\n"
        "v_mfma_f32_16x16x32_bf16 v[184:187], a[236:239], a[232:235], v[184:187]\n"
        "s_waitcnt lgkmcnt(5)\n"
        "ds_read_b128 v[164:167], v247 offset:20736\n"
        "v_mfma_f32_16x16x32_bf16 v[184:187], a[240:243], a[220:223], v[184:187]\n"
        "ds_read_b128 v[152:155], v247 offset:16640\n"
        "v_mfma_f32_16x16x32_bf16 v[204:207], a[244:247], a[220:223], v[204:207]\n"
        "ds_read_b128 a[220:223], v248 offset:9232\n"
        "v_mfma_f32_16x16x32_bf16 v[204:207], a[248:251], a[232:235], v[204:207]\n"
        "s_waitcnt lgkmcnt(7)\n"
        "ds_read_b128 a[232:235], v254 offset:6160\n"
        "v_mfma_f32_16x16x32_bf16 v[192:195], a[248:251], a[216:219], v[192:195]\n"
        "s_waitcnt lgkmcnt(5)\n"
        "v_exp_f32 v111, v111\n"
        "v_mfma_f32_16x16x32_bf16 v[192:195], a[244:247], a[228:231], v[192:195]\n"
        "v_cvt_pk_bf16_f32 v91, v110, v111\n"
        "v_mfma_f32_16x16x32_bf16 v[172:175], a[240:243], a[228:231], v[172:175]\n"
        "ds_read_b128 a[228:231], v254 offset:9232\n"
        "v_mfma_f32_16x16x32_bf16 v[172:175], a[236:239], a[216:219], v[172:175]\n"
        "s_waitcnt lgkmcnt(5)\n"
        "ds_read_b128 a[216:219], v248 offset:12288\n"
        "v_mfma_f32_16x16x32_bf16 v[212:215], a[236:239], a[224:227], v[212:215]\n"
        "s_waitcnt lgkmcnt(3)\n"
        "v_exp_f32 v112, v112\n"
        "v_mfma_f32_16x16x32_bf16 v[212:215], a[240:243], a[220:223], v[212:215]\n"
        "v_exp_f32 v113, v113\n"
        "v_mfma_f32_16x16x32_bf16 v[216:219], a[244:247], a[220:223], v[216:219]\n"
        "ds_read_b128 a[220:223], v248 offset:15360\n"
        "v_cvt_pk_bf16_f32 v92, v112, v113\n"
        "v_mfma_f32_16x16x32_bf16 v[216:219], a[248:251], a[224:227], v[216:219]\n"
        "s_waitcnt lgkmcnt(3)\n"
        "ds_read_b128 a[224:227], v248 offset:12320\n"
        "v_mfma_f32_16x16x32_bf16 v[224:227], a[248:251], a[232:235], v[224:227]\n"
        "s_waitcnt lgkmcnt(3)\n"
        "v_exp_f32 v114, v114\n"
        "v_mfma_f32_16x16x32_bf16 v[224:227], a[244:247], a[228:231], v[224:227]\n"
        "v_exp_f32 v68, v68\n"
        "v_mfma_f32_16x16x32_bf16 v[220:223], a[240:243], a[228:231], v[220:223]\n"
        "ds_read_b128 a[228:231], v248 offset:15392\n"
        "v_mfma_f32_16x16x32_bf16 v[220:223], a[236:239], a[232:235], v[220:223]\n"
        "s_waitcnt lgkmcnt(3)\n"
        "ds_read_b128 a[232:235], v254 offset:12288\n"
        "v_mfma_f32_16x16x32_bf16 v[176:179], v[156:159], a[216:219], v[176:179]\n"
        "s_waitcnt lgkmcnt(3)\n"
        "v_exp_f32 v115, v115\n"
        "v_mfma_f32_16x16x32_bf16 v[176:179], v[160:163], a[220:223], v[176:179]\n"
        "v_exp_f32 v69, v69\n"
        "v_mfma_f32_16x16x32_bf16 v[196:199], v[164:167], a[220:223], v[196:199]\n"
        "ds_read_b128 a[220:223], v254 offset:15360\n"
        "v_cvt_pk_bf16_f32 v93, v114, v115\n"
        "v_mfma_f32_16x16x32_bf16 v[196:199], v[152:155], a[216:219], v[196:199]\n"
        "s_waitcnt lgkmcnt(3)\n"
        "ds_read_b128 a[216:219], v254 offset:12320\n"
        "v_cvt_pk_bf16_f32 v94, v68, v69\n"
        "v_mfma_f32_16x16x32_bf16 v[200:203], v[152:155], a[224:227], v[200:203]\n"
        "s_waitcnt lgkmcnt(3)\n"
        "v_exp_f32 v70, v70\n"
        "v_mfma_f32_16x16x32_bf16 v[200:203], v[164:167], a[228:231], v[200:203]\n"
        "s_waitcnt vmcnt(0)\n"
        "v_exp_f32 v71, v71\n"
        "v_mfma_f32_16x16x32_bf16 v[180:183], v[160:163], a[228:231], v[180:183]\n"
        "s_barrier\n"
        "ds_read_b128 a[228:231], v254 offset:15392\n"
        "v_cvt_pk_bf16_f32 v95, v70, v71\n"
        "v_mfma_f32_16x16x32_bf16 v[180:183], v[156:159], a[224:227], v[180:183]\n"
        "s_waitcnt lgkmcnt(3)\n"
        "ds_read_b128 a[224:227], v248 offset:12304\n"
        "v_mfma_f32_16x16x32_bf16 v[184:187], v[156:159], a[232:235], v[184:187]\n"
        "s_waitcnt lgkmcnt(3)\n"
        "v_mfma_f32_16x16x32_bf16 v[184:187], v[160:163], a[220:223], v[184:187]\n"
        "v_mfma_f32_16x16x32_bf16 v[204:207], v[164:167], a[220:223], v[204:207]\n"
        "ds_read_b128 a[220:223], v248 offset:15376\n"
        "v_mfma_f32_16x16x32_bf16 v[204:207], v[152:155], a[232:235], v[204:207]\n"
        "s_waitcnt lgkmcnt(3)\n"
        "ds_read_b128 a[232:235], v254 offset:12304\n"
        "v_mfma_f32_16x16x32_bf16 v[192:195], v[152:155], a[216:219], v[192:195]\n"
        "s_waitcnt lgkmcnt(3)\n"
        "ds_read_b128 a[236:239], v240 offset:0\n"
        "v_mfma_f32_16x16x32_bf16 v[192:195], v[164:167], a[228:231], v[192:195]\n"
        "ds_read_b128 a[240:243], v241 offset:0\n"
        "v_mfma_f32_16x16x32_bf16 v[172:175], v[160:163], a[228:231], v[172:175]\n"
        "ds_read_b128 a[228:231], v254 offset:15376\n"
        "v_mfma_f32_16x16x32_bf16 v[172:175], v[156:159], a[216:219], v[172:175]\n"
        "s_waitcnt lgkmcnt(5)\n"
        "ds_read_b128 a[244:247], v240 offset:128\n"
        "v_mfma_f32_16x16x32_bf16 v[212:215], v[156:159], a[224:227], v[212:215]\n"
        "s_waitcnt lgkmcnt(5)\n"
        "ds_read_b128 a[248:251], v241 offset:128\n"
        "v_mfma_f32_16x16x32_bf16 v[212:215], v[160:163], a[220:223], v[212:215]\n"
        "ds_read_b128 a[216:219], v240 offset:256\n"
        "v_mfma_f32_16x16x32_bf16 v[216:219], v[164:167], a[220:223], v[216:219]\n"
        "ds_read_b128 a[220:223], v241 offset:256\n"
        "v_mfma_f32_16x16x32_bf16 v[216:219], v[152:155], a[224:227], v[216:219]\n"
        "s_waitcnt lgkmcnt(7)\n"
        "v_mfma_f32_16x16x32_bf16 v[224:227], v[152:155], a[232:235], v[224:227]\n"
        "s_waitcnt lgkmcnt(4)\n"
        "v_mfma_f32_16x16x32_bf16 v[224:227], v[164:167], a[228:231], v[224:227]\n"
        "ds_write_b32 v245, v96 offset:4608\n"
        "v_mfma_f32_16x16x32_bf16 v[220:223], v[160:163], a[228:231], v[220:223]\n"
        "v_mfma_f32_16x16x32_bf16 v[220:223], v[156:159], a[232:235], v[220:223]\n"
        "ds_write_b32 v245, v97 offset:5632\n"
        "v_mfma_f32_16x16x32_bf16 v[112:115], a[72:75], a[236:239], 0\n"
        "v_mov_b64 v[124:125], 0\n"
        "v_cvt_pk_bf16_f32 v232, v176, v180\n"
        "v_mfma_f32_16x16x32_bf16 v[112:115], a[76:79], a[240:243], v[112:115]\n"
        "s_waitcnt lgkmcnt(5)\n"
        "v_cvt_pk_bf16_f32 v233, v184, v172\n"
        "v_cvt_pk_bf16_f32 v234, v177, v181\n"
        "v_mfma_f32_16x16x32_bf16 v[112:115], a[80:83], a[244:247], v[112:115]\n"
        "s_waitcnt lgkmcnt(4)\n"
        "buffer_store_dwordx2 v[232:233], v250, s[16:19], s38 offen offset:0 sc1\n"
        "v_cvt_pk_bf16_f32 v235, v185, v173\n"
        "v_mfma_f32_16x16x32_bf16 v[112:115], a[84:87], a[248:251], v[112:115]\n"
        "s_waitcnt lgkmcnt(3)\n"
        "v_cvt_pk_bf16_f32 v232, v178, v182\n"
        "v_cvt_pk_bf16_f32 v233, v186, v174\n"
        "v_mfma_f32_16x16x32_bf16 v[112:115], a[88:91], a[216:219], v[112:115]\n"
        "s_waitcnt lgkmcnt(2)\n"
        "v_mfma_f32_16x16x32_bf16 v[112:115], a[92:95], a[220:223], v[112:115]\n"
        "ds_write_b32 v245, v100 offset:0\n"
        "v_smfmac_f32_16x16x64_bf16 v[124:127], a[252:255], v[88:95], v237\n"
        "s_nop 1\n"
        "v_mfma_f32_16x16x32_bf16 v[80:83], v[132:135], v[92:95], 0\n"
        "buffer_store_dwordx2 v[234:235], v250, s[16:19], s38 offen offset:384 sc1\n"
        "s_nop 0\n"
        "v_mfma_f32_16x16x32_bf16 v[80:83], v[128:131], v[88:91], v[80:83]\n"
        "s_add_u32 s41, s30, s40\n"
        "v_cvt_pk_bf16_f32 v152, v112, v113\n"
        "v_cvt_pk_bf16_f32 v153, v114, v115\n"
        "v_mfma_f32_16x16x32_bf16 v[84:87], v[136:139], v[88:91], 0\n"
        "v_cvt_pk_bf16_f32 v234, v179, v183\n"
        "v_cvt_pk_bf16_f32 v235, v187, v175\n"
        "v_mfma_f32_16x16x32_bf16 v[84:87], v[140:143], v[92:95], v[84:87]\n"
        "v_add_f32 v125, v124, v125\n"
        "v_mfma_f32_16x16x32_bf16 v[96:99], v[148:151], v[92:95], 0\n"
        "buffer_store_dwordx2 v[232:233], v250, s[16:19], s38 offen offset:768 sc1\n"
        "v_mfma_f32_16x16x32_bf16 v[96:99], v[144:147], v[88:91], v[96:99]\n"
        "s_add_u32 s35, s47, 786432\n"
        "v_rcp_f32 v124, v125\n"
        "v_mfma_f32_16x16x32_bf16 v[116:119], a[116:119], a[220:223], 0\n"
        "s_sub_u32 s46, s35, 100663296\n"
        "v_mul_f32 v80, v124, v80\n"
        "v_mul_f32 v81, v124, v81\n"
        "v_mfma_f32_16x16x32_bf16 v[116:119], a[112:115], a[216:219], v[116:119]\n"
        "s_cmp_lt_u32 s46, 786432\n"
        "v_mul_f32 v82, v124, v82\n"
        "v_mul_f32 v83, v124, v83\n"
        "v_mfma_f32_16x16x32_bf16 v[116:119], a[108:111], a[248:251], v[116:119]\n"
        "buffer_store_dwordx2 v[234:235], v250, s[16:19], s38 offen offset:1152 sc1\n"
        "v_cvt_pk_bf16_f32 v80, v80, v81\n"
        "v_mfma_f32_16x16x32_bf16 v[116:119], a[104:107], a[244:247], v[116:119]\n"
        "s_cselect_b32 s35, s46, s35\n"
        "v_cvt_pk_bf16_f32 v81, v82, v83\n"
        "v_cvt_pk_bf16_f32 v232, v196, v200\n"
        "v_mfma_f32_16x16x32_bf16 v[116:119], a[100:103], a[240:243], v[116:119]\n"
        "v_cvt_pk_bf16_f32 v233, v204, v192\n"
        "v_mul_f32 v84, v124, v84\n"
        "v_mfma_f32_16x16x32_bf16 v[116:119], a[96:99], a[236:239], v[116:119]\n"
        "v_mul_f32 v85, v124, v85\n"
        "v_mul_f32 v86, v124, v86\n"
        "v_mfma_f32_16x16x32_bf16 v[120:123], a[120:123], a[236:239], 0\n"
        "ds_write_b32 v245, v101 offset:1024\n"
        "v_mfma_f32_16x16x32_bf16 v[120:123], a[124:127], a[240:243], v[120:123]\n"
        "buffer_store_dwordx2 v[232:233], v250, s[16:19], s39 offen offset:0 sc1\n"
        "v_mul_f32 v87, v124, v87\n"
        "v_mfma_f32_16x16x32_bf16 v[120:123], a[128:131], a[244:247], v[120:123]\n"
        "v_cvt_pk_bf16_f32 v82, v84, v85\n"
        "v_cvt_pk_bf16_f32 v83, v86, v87\n"
        "v_mfma_f32_16x16x32_bf16 v[120:123], a[132:135], a[248:251], v[120:123]\n"
        "v_mul_f32 v96, v124, v96\n"
        "v_mul_f32 v97, v124, v97\n"
        "v_mfma_f32_16x16x32_bf16 v[120:123], a[136:139], a[216:219], v[120:123]\n"
        "v_mul_f32 v98, v124, v98\n"
        "v_mul_f32 v99, v124, v99\n"
        "v_mfma_f32_16x16x32_bf16 v[120:123], a[140:143], a[220:223], v[120:123]\n"
        "v_cvt_pk_bf16_f32 v234, v197, v201\n"
        "v_cvt_pk_bf16_f32 v235, v205, v193\n"
        "v_mfma_f32_16x16x32_bf16 v[112:115], a[20:23], a[220:223], 0\n"
        "buffer_store_dwordx2 v[234:235], v250, s[16:19], s39 offen offset:384 sc1\n"
        "v_cvt_pk_bf16_f32 v154, v116, v117\n"
        "v_mfma_f32_16x16x32_bf16 v[112:115], a[16:19], a[216:219], v[112:115]\n"
        "v_cvt_pk_bf16_f32 v155, v118, v119\n"
        "v_cvt_pk_bf16_f32 v96, v96, v97\n"
        "v_mfma_f32_16x16x32_bf16 v[112:115], a[12:15], a[248:251], v[112:115]\n"
        "ds_write_b32 v245, v96 offset:4864\n"
        "v_mfma_f32_16x16x32_bf16 v[112:115], a[8:11], a[244:247], v[112:115]\n"
        "v_cvt_pk_bf16_f32 v97, v98, v99\n"
        "v_max3_u32 v236, v236, v124, v125\n"
        "v_mfma_f32_16x16x32_bf16 v[112:115], a[4:7], a[240:243], v[112:115]\n"
        "v_cvt_pk_bf16_f32 v232, v198, v202\n"
        "v_cvt_pk_bf16_f32 v233, v206, v194\n"
        "v_mfma_f32_16x16x32_bf16 v[112:115], a[0:3], a[236:239], v[112:115]\n"
        "buffer_store_dwordx2 v[232:233], v250, s[16:19], s39 offen offset:768 sc1\n"
        "v_cmp_lt_u32 vcc, s24, v236\n"
        "v_mfma_f32_16x16x32_bf16 v[116:119], a[24:27], a[236:239], 0\n"
        "v_cvt_pk_bf16_f32 v168, v120, v121\n"
        "v_cvt_pk_bf16_f32 v169, v122, v123\n"
        "v_mfma_f32_16x16x32_bf16 v[116:119], a[28:31], a[240:243], v[116:119]\n"
        "s_cmp_lg_u64 vcc, 0\n"
        "s_addc_u32 s25, s25, s25\n"
        "v_cvt_pk_bf16_f32 v234, v199, v203\n"
        "v_mfma_f32_16x16x32_bf16 v[116:119], a[32:35], a[244:247], v[116:119]\n"
        "v_cvt_pk_bf16_f32 v235, v207, v195\n"
        "v_cvt_pk_bf16_f32 v156, v112, v113\n"
        "v_mfma_f32_16x16x32_bf16 v[116:119], a[36:39], a[248:251], v[116:119]\n"
        "buffer_store_dwordx2 v[234:235], v250, s[16:19], s39 offen offset:1152 sc1\n"
        "v_cvt_pk_bf16_f32 v157, v114, v115\n"
        "v_mfma_f32_16x16x32_bf16 v[116:119], a[40:43], a[216:219], v[116:119]\n"
        "v_cvt_pk_bf16_f32 v232, v212, v220\n"
        "v_cvt_pk_bf16_f32 v234, v213, v221\n"
        "v_mfma_f32_16x16x32_bf16 v[116:119], a[44:47], a[220:223], v[116:119]\n"
        "ds_write_b32 v245, v102 offset:2048\n"
        "v_mfma_f32_16x16x32_bf16 v[120:123], a[68:71], a[220:223], 0\n"
        "v_xor_b32 v251, 0x2000, v251\n"
        "v_xor_b32 v252, 0x2000, v252\n"
        "v_mfma_f32_16x16x32_bf16 v[120:123], a[64:67], a[216:219], v[120:123]\n"
        "buffer_store_dword v232, v238, s[16:19], s38 offen offset:0 sc1\n"
        "v_mfma_f32_16x16x32_bf16 v[120:123], a[60:63], a[248:251], v[120:123]\n"
        "ds_read_b128 a[224:227], v240 offset:1536\n"
        "v_cvt_pk_bf16_f32 v158, v116, v117\n"
        "v_mfma_f32_16x16x32_bf16 v[120:123], a[56:59], a[244:247], v[120:123]\n"
        "ds_read_b128 a[228:231], v241 offset:1536\n"
        "v_cvt_pk_bf16_f32 v159, v118, v119\n"
        "v_mfma_f32_16x16x32_bf16 v[120:123], a[52:55], a[240:243], v[120:123]\n"
        "ds_read_b128 a[232:235], v240 offset:1664\n"
        "v_cvt_pk_bf16_f32 v232, v214, v222\n"
        "v_mfma_f32_16x16x32_bf16 v[120:123], a[48:51], a[236:239], v[120:123]\n"
        "buffer_store_dword v234, v238, s[16:19], s38 offen offset:384 sc1\n"
        "v_mfma_f32_16x16x32_bf16 v[112:115], a[236:239], a[144:147], 0\n"
        "v_add_u32 v247, s45, v247\n"
        "v_cvt_pk_bf16_f32 v234, v215, v223\n"
        "v_mfma_f32_16x16x32_bf16 v[112:115], a[240:243], a[148:151], v[112:115]\n"
        "ds_write_b32 v245, v103 offset:3072\n"
        "v_mfma_f32_16x16x32_bf16 v[112:115], a[244:247], a[152:155], v[112:115]\n"
        "s_nop 0\n"
        "v_cvt_pk_bf16_f32 v160, v120, v121\n"
        "v_cvt_pk_bf16_f32 v161, v122, v123\n"
        "v_mfma_f32_16x16x32_bf16 v[112:115], a[248:251], a[156:159], v[112:115]\n"
        "v_cvt_pk_bf16_f32 v166, v120, v121\n"
        "v_cvt_pk_bf16_f32 v167, v122, v123\n"
        "v_mfma_f32_16x16x32_bf16 v[112:115], a[216:219], a[160:163], v[112:115]\n"
        "buffer_store_dword v232, v238, s[16:19], s38 offen offset:768 sc1\n"
        "v_mfma_f32_16x16x32_bf16 v[112:115], a[220:223], a[164:167], v[112:115]\n"
        "ds_write_b32 v245, v97 offset:5888\n"
        "v_mfma_f32_16x16x32_bf16 v[116:119], a[220:223], a[188:191], 0\n"
        "v_cvt_pk_bf16_f32 v232, v216, v224\n"
        "s_nop 0\n"
        "v_mfma_f32_16x16x32_bf16 v[116:119], a[216:219], a[184:187], v[116:119]\n"
        "s_nop 2\n"
        "v_mfma_f32_16x16x32_bf16 v[116:119], a[248:251], a[180:183], v[116:119]\n"
        "buffer_store_dword v234, v238, s[16:19], s38 offen offset:1152 sc1\n"
        "v_mfma_f32_16x16x32_bf16 v[116:119], a[244:247], a[176:179], v[116:119]\n"
        "v_cvt_pk_bf16_f32 v128, v112, v113\n"
        "v_cvt_pk_bf16_f32 v129, v114, v115\n"
        "v_mfma_f32_16x16x32_bf16 v[116:119], a[240:243], a[172:175], v[116:119]\n"
        "ds_write_b32 v245, v64 offset:256\n"
        "v_mfma_f32_16x16x32_bf16 v[116:119], a[236:239], a[168:171], v[116:119]\n"
        "v_cvt_pk_bf16_f32 v234, v217, v225\n"
        "s_nop 0\n"
        "v_mfma_f32_16x16x32_bf16 v[120:123], a[236:239], a[192:195], 0\n"
        "ds_read_b128 a[236:239], v241 offset:1664\n"
        "s_nop 0\n"
        "v_mfma_f32_16x16x32_bf16 v[120:123], a[240:243], a[196:199], v[120:123]\n"
        "buffer_store_dword v232, v238, s[16:19], s39 offen offset:0 sc1\n"
        "v_mfma_f32_16x16x32_bf16 v[120:123], a[244:247], a[200:203], v[120:123]\n"
        "ds_read_b128 a[240:243], v240 offset:1792\n"
        "ds_read_b128 a[244:247], v241 offset:1792\n"
        "v_mfma_f32_16x16x32_bf16 v[120:123], a[248:251], a[204:207], v[120:123]\n"
        "v_cvt_pk_bf16_f32 v136, v116, v117\n"
        "v_cvt_pk_bf16_f32 v137, v118, v119\n"
        "v_mfma_f32_16x16x32_bf16 v[120:123], a[216:219], a[208:211], v[120:123]\n"
        "v_cvt_pk_bf16_f32 v232, v218, v226\n"
        "v_mfma_f32_16x16x32_bf16 v[120:123], a[220:223], a[212:215], v[120:123]\n"
        "s_waitcnt lgkmcnt(8)\n"
        "buffer_store_dword v234, v238, s[16:19], s39 offen offset:384 sc1\n"
        "v_mfma_f32_16x16x32_bf16 v[112:115], a[72:75], a[224:227], 0\n"
        "s_waitcnt lgkmcnt(7)\n"
        "s_nop 1\n"
        "v_mfma_f32_16x16x32_bf16 v[112:115], a[76:79], a[228:231], v[112:115]\n"
        "s_waitcnt lgkmcnt(6)\n"
        "v_cvt_pk_bf16_f32 v234, v219, v227\n"
        "s_nop 0\n"
        "v_mfma_f32_16x16x32_bf16 v[112:115], a[80:83], a[232:235], v[112:115]\n"
        "s_waitcnt lgkmcnt(2)\n"
        "v_cvt_pk_bf16_f32 v144, v120, v121\n"
        "v_cvt_pk_bf16_f32 v145, v122, v123\n"
        "v_mfma_f32_16x16x32_bf16 v[112:115], a[84:87], a[236:239], v[112:115]\n"
        "s_waitcnt lgkmcnt(1)\n"
        "ds_write_b32 v245, v65 offset:1280\n"
        "v_mfma_f32_16x16x32_bf16 v[112:115], a[88:91], a[240:243], v[112:115]\n"
        "s_waitcnt lgkmcnt(1)\n"
        "buffer_store_dword v232, v238, s[16:19], s39 offen offset:768 sc1\n"
        "v_mfma_f32_16x16x32_bf16 v[112:115], a[92:95], a[244:247], v[112:115]\n"
        "s_nop 2\n"
        "v_mfma_f32_16x16x32_bf16 v[116:119], a[116:119], a[244:247], 0\n"
        "s_nop 2\n"
        "v_mfma_f32_16x16x32_bf16 v[116:119], a[112:115], a[240:243], v[116:119]\n"
        "v_cvt_pk_bf16_f32 v172, v112, v113\n"
        "v_cvt_pk_bf16_f32 v173, v114, v115\n"
        "v_mfma_f32_16x16x32_bf16 v[116:119], a[108:111], a[236:239], v[116:119]\n"
        "buffer_store_dword v234, v238, s[16:19], s39 offen offset:1152 sc1\n"
        "v_mfma_f32_16x16x32_bf16 v[116:119], a[104:107], a[232:235], v[116:119]\n"
        "ds_write_b32 v245, v66 offset:2304\n"
        "v_mfma_f32_16x16x32_bf16 v[116:119], a[100:103], a[228:231], v[116:119]\n"
        "v_mfma_f32_16x16x32_bf16 v[116:119], a[96:99], a[224:227], v[116:119]\n"
        "s_mov_b32 m0, s48\n"
        "s_nop 1\n"
        "v_mfma_f32_16x16x32_bf16 v[120:123], a[120:123], a[224:227], 0\n"
        "ds_read_b128 v[162:165], v251 offset:0\n"
        "s_nop 0\n"
        "v_mfma_f32_16x16x32_bf16 v[120:123], a[124:127], a[228:231], v[120:123]\n"
        "buffer_load_dwordx4 v249, s[12:15], s36 offen offset:0 lds\n"
        "v_mfma_f32_16x16x32_bf16 v[120:123], a[128:131], a[232:235], v[120:123]\n"
        "v_cvt_pk_bf16_f32 v174, v116, v117\n"
        "v_cvt_pk_bf16_f32 v175, v118, v119\n"
        "v_mfma_f32_16x16x32_bf16 v[120:123], a[132:135], a[236:239], v[120:123]\n"
        "ds_write_b32 v245, v67 offset:3328\n"
        "v_mfma_f32_16x16x32_bf16 v[120:123], a[136:139], a[240:243], v[120:123]\n"
        "ds_write_b32 v245, v72 offset:512\n"
        "v_mfma_f32_16x16x32_bf16 v[120:123], a[140:143], a[244:247], v[120:123]\n"
        "s_nop 2\n"
        "v_mfma_f32_16x16x32_bf16 v[112:115], a[20:23], a[244:247], 0\n"
        "s_nop 2\n"
        "v_mfma_f32_16x16x32_bf16 v[112:115], a[16:19], a[240:243], v[112:115]\n"
        "v_cvt_pk_bf16_f32 v190, v120, v121\n"
        "v_cvt_pk_bf16_f32 v191, v122, v123\n"
        "v_mfma_f32_16x16x32_bf16 v[112:115], a[12:15], a[236:239], v[112:115]\n"
        "ds_write_b32 v245, v73 offset:1536\n"
        "v_mfma_f32_16x16x32_bf16 v[112:115], a[8:11], a[232:235], v[112:115]\n"
        "ds_write_b32 v245, v74 offset:2560\n"
        "v_mfma_f32_16x16x32_bf16 v[112:115], a[4:7], a[228:231], v[112:115]\n"
        "ds_read_b128 v[182:185], v251 offset:512\n"
        "v_mfma_f32_16x16x32_bf16 v[112:115], a[0:3], a[224:227], v[112:115]\n"
        "s_nop 2\n"
        "v_mfma_f32_16x16x32_bf16 v[116:119], a[24:27], a[224:227], 0\n"
        "s_nop 2\n"
        "v_mfma_f32_16x16x32_bf16 v[116:119], a[28:31], a[228:231], v[116:119]\n"
        "v_cvt_pk_bf16_f32 v176, v112, v113\n"
        "v_cvt_pk_bf16_f32 v177, v114, v115\n"
        "v_mfma_f32_16x16x32_bf16 v[116:119], a[32:35], a[232:235], v[116:119]\n"
        "ds_write_b32 v245, v75 offset:3584\n"
        "v_mfma_f32_16x16x32_bf16 v[116:119], a[36:39], a[236:239], v[116:119]\n"
        "buffer_load_dwordx4 v253, s[12:15], s36 offen offset:1024 lds\n"
        "v_mfma_f32_16x16x32_bf16 v[116:119], a[40:43], a[240:243], v[116:119]\n"
        "ds_write_b32 v245, v80 offset:768\n"
        "v_mfma_f32_16x16x32_bf16 v[116:119], a[44:47], a[244:247], v[116:119]\n"
        "s_nop 2\n"
        "v_mfma_f32_16x16x32_bf16 v[120:123], a[68:71], a[244:247], 0\n"
        "s_nop 2\n"
        "v_mfma_f32_16x16x32_bf16 v[120:123], a[64:67], a[240:243], v[120:123]\n"
        "v_cvt_pk_bf16_f32 v178, v116, v117\n"
        "v_cvt_pk_bf16_f32 v179, v118, v119\n"
        "v_mfma_f32_16x16x32_bf16 v[120:123], a[60:63], a[236:239], v[120:123]\n"
        "ds_write_b32 v245, v81 offset:1792\n"
        "v_mfma_f32_16x16x32_bf16 v[120:123], a[56:59], a[232:235], v[120:123]\n"
        "ds_write_b32 v245, v82 offset:2816\n"
        "v_mfma_f32_16x16x32_bf16 v[120:123], a[52:55], a[228:231], v[120:123]\n"
        "ds_write_b32 v245, v83 offset:3840\n"
        "v_mfma_f32_16x16x32_bf16 v[120:123], a[48:51], a[224:227], v[120:123]\n"
        "s_nop 2\n"
        "v_mfma_f32_16x16x32_bf16 v[112:115], a[224:227], a[144:147], 0\n"
        "s_nop 2\n"
        "v_mfma_f32_16x16x32_bf16 v[112:115], a[228:231], a[148:151], v[112:115]\n"
        "v_cvt_pk_bf16_f32 v180, v120, v121\n"
        "v_cvt_pk_bf16_f32 v181, v122, v123\n"
        "v_mfma_f32_16x16x32_bf16 v[112:115], a[232:235], a[152:155], v[112:115]\n"
        "v_cvt_pk_bf16_f32 v186, v120, v121\n"
        "v_cvt_pk_bf16_f32 v187, v122, v123\n"
        "v_mfma_f32_16x16x32_bf16 v[112:115], a[236:239], a[156:159], v[112:115]\n"
        "s_add_u32 m0, s41, 0\n"
        "v_mfma_f32_16x16x32_bf16 v[112:115], a[240:243], a[160:163], v[112:115]\n"
        "ds_read_b128 a[248:251], v240 offset:12288\n"
        "v_mfma_f32_16x16x32_bf16 v[112:115], a[244:247], a[164:167], v[112:115]\n"
        "buffer_load_dwordx4 v242, s[8:11], s47 offen lds\n"
        "v_mfma_f32_16x16x32_bf16 v[96:99], v[152:155], v[156:159], v[0:3]\n"
        "s_waitcnt lgkmcnt(11)\n"
        "ds_read_b128 a[216:219], v241 offset:12288\n"
        "s_nop 0\n"
        "v_mfma_f32_16x16x32_bf16 v[96:99], v[168:171], v[160:163], v[96:99]\n"
        "ds_read_b128 a[220:223], v240 offset:12416\n"
        "s_nop 0\n"
        "v_mfma_f32_16x16x32_bf16 v[100:103], v[172:175], v[156:159], v[4:7]\n"
        "s_sub_u32 s31, s30, s28\n"
        "v_cvt_pk_bf16_f32 v130, v112, v113\n"
        "v_cvt_pk_bf16_f32 v131, v114, v115\n"
        "v_mfma_f32_16x16x32_bf16 v[100:103], v[188:191], v[164:167], v[100:103]\n"
        "s_sub_u32 s45, s28, s29\n"
        "ds_read_b128 v[162:165], v252 offset:0\n"
        "v_mfma_f32_16x16x32_bf16 v[116:119], a[244:247], a[188:191], 0\n"
        "s_add_u32 s28, s28, s31\n"
        "v_exp_f32 v96, v96\n"
        "v_mfma_f32_16x16x32_bf16 v[116:119], a[240:243], a[184:187], v[116:119]\n"
        "s_mov_b32 s30, s29\n"
        "v_exp_f32 v97, v97\n"
        "v_mfma_f32_16x16x32_bf16 v[116:119], a[236:239], a[180:183], v[116:119]\n"
        "s_add_u32 s29, s29, s45\n"
        "v_exp_f32 v98, v98\n"
        "v_mfma_f32_16x16x32_bf16 v[116:119], a[232:235], a[176:179], v[116:119]\n"
        "v_exp_f32 v100, v100\n"
        "v_mfma_f32_16x16x32_bf16 v[116:119], a[228:231], a[172:175], v[116:119]\n"
        "v_exp_f32 v99, v99\n"
        "v_mfma_f32_16x16x32_bf16 v[116:119], a[224:227], a[168:171], v[116:119]\n"
        "v_exp_f32 v101, v101\n"
        "v_mfma_f32_16x16x32_bf16 v[104:107], v[152:155], v[176:179], v[16:19]\n"
        "s_waitcnt lgkmcnt(9)\n"
        "v_exp_f32 v102, v102\n"
        "v_mfma_f32_16x16x32_bf16 v[104:107], v[168:171], v[180:183], v[104:107]\n"
        "s_add_u32 m0, s41, 1024\n"
        "v_cvt_pk_bf16_f32 v64, v96, v97\n"
        "v_cvt_pk_bf16_f32 v65, v98, v99\n"
        "v_mfma_f32_16x16x32_bf16 v[108:111], v[172:175], v[176:179], v[20:23]\n"
        "v_cvt_pk_bf16_f32 v138, v116, v117\n"
        "v_cvt_pk_bf16_f32 v139, v118, v119\n"
        "v_mfma_f32_16x16x32_bf16 v[108:111], v[188:191], v[184:187], v[108:111]\n"
        "buffer_load_dwordx4 v243, s[8:11], s47 offen lds\n"
        "v_mfma_f32_16x16x32_bf16 v[120:123], a[224:227], a[192:195], 0\n"
        "ds_read_b128 a[224:227], v241 offset:12416\n"
        "v_cvt_pk_bf16_f32 v66, v100, v101\n"
        "v_mfma_f32_16x16x32_bf16 v[120:123], a[228:231], a[196:199], v[120:123]\n"
        "ds_read_b128 a[228:231], v240 offset:12544\n"
        "v_mfma_f32_16x16x32_bf16 v[120:123], a[232:235], a[200:203], v[120:123]\n"
        "ds_read_b128 a[232:235], v241 offset:12544\n"
        "v_mfma_f32_16x16x32_bf16 v[120:123], a[236:239], a[204:207], v[120:123]\n"
        "ds_read_b128 v[202:205], v251 offset:4096\n"
        "v_mfma_f32_16x16x32_bf16 v[120:123], a[240:243], a[208:211], v[120:123]\n"
        "ds_read_b128 v[182:185], v252 offset:512\n"
        "v_mfma_f32_16x16x32_bf16 v[120:123], a[244:247], a[212:215], v[120:123]\n"
        "s_waitcnt lgkmcnt(8)\n"
        "v_exp_f32 v103, v103\n"
        "v_mfma_f32_16x16x32_bf16 v[112:115], a[0:3], a[248:251], 0\n"
        "s_waitcnt lgkmcnt(7)\n"
        "v_cvt_pk_bf16_f32 v67, v102, v103\n"
        "s_nop 0\n"
        "v_mfma_f32_16x16x32_bf16 v[112:115], a[4:7], a[216:219], v[112:115]\n"
        "s_waitcnt lgkmcnt(6)\n"
        "v_exp_f32 v104, v104\n"
        "v_mfma_f32_16x16x32_bf16 v[112:115], a[8:11], a[220:223], v[112:115]\n"
        "s_waitcnt lgkmcnt(4)\n"
        "v_exp_f32 v105, v105\n"
        "v_mfma_f32_16x16x32_bf16 v[112:115], a[12:15], a[224:227], v[112:115]\n"
        "s_waitcnt lgkmcnt(3)\n"
        "v_exp_f32 v106, v106\n"
        "v_mfma_f32_16x16x32_bf16 v[112:115], a[16:19], a[228:231], v[112:115]\n"
        "s_waitcnt lgkmcnt(2)\n"
        "v_cvt_pk_bf16_f32 v72, v104, v105\n"
        "v_cvt_pk_bf16_f32 v146, v120, v121\n"
        "v_mfma_f32_16x16x32_bf16 v[112:115], a[20:23], a[232:235], v[112:115]\n"
        "s_add_u32 m0, s41, 2048\n"
        "v_exp_f32 v108, v108\n"
        "v_mfma_f32_16x16x32_bf16 v[116:119], a[44:47], a[232:235], 0\n"
        "v_cvt_pk_bf16_f32 v147, v122, v123\n"
        "v_add_u32 v245, s45, v245\n"
        "v_mfma_f32_16x16x32_bf16 v[116:119], a[40:43], a[228:231], v[116:119]\n"
        "buffer_load_dwordx4 v244, s[8:11], s47 offen lds\n"
        "v_mfma_f32_16x16x32_bf16 v[116:119], a[36:39], a[224:227], v[116:119]\n"
        "v_exp_f32 v107, v107\n"
        "v_mfma_f32_16x16x32_bf16 v[116:119], a[32:35], a[220:223], v[116:119]\n"
        "v_exp_f32 v109, v109\n"
        "v_mfma_f32_16x16x32_bf16 v[116:119], a[28:31], a[216:219], v[116:119]\n"
        "v_cvt_pk_bf16_f32 v73, v106, v107\n"
        "v_cvt_pk_bf16_f32 v74, v108, v109\n"
        "v_mfma_f32_16x16x32_bf16 v[116:119], a[24:27], a[248:251], v[116:119]\n"
        "v_exp_f32 v110, v110\n"
        "v_mfma_f32_16x16x32_bf16 v[120:123], a[48:51], a[248:251], 0\n"
        "v_exp_f32 v111, v111\n"
        "v_mfma_f32_16x16x32_bf16 v[120:123], a[52:55], a[216:219], v[120:123]\n"
        "v_cvt_pk_bf16_f32 v75, v110, v111\n"
        "v_cvt_pk_bf16_f32 v196, v112, v113\n"
        "v_mfma_f32_16x16x32_bf16 v[120:123], a[56:59], a[220:223], v[120:123]\n"
        "v_cvt_pk_bf16_f32 v197, v114, v115\n"
        "v_cvt_pk_bf16_f32 v198, v116, v117\n"
        "v_mfma_f32_16x16x32_bf16 v[120:123], a[60:63], a[224:227], v[120:123]\n"
        "v_cvt_pk_bf16_f32 v199, v118, v119\n"
        "v_mfma_f32_16x16x32_bf16 v[120:123], a[64:67], a[228:231], v[120:123]\n"
        "v_mfma_f32_16x16x32_bf16 v[120:123], a[68:71], a[232:235], v[120:123]\n"
        "s_nop 2\n"
        "v_mfma_f32_16x16x32_bf16 v[112:115], a[92:95], a[232:235], 0\n"
        "s_nop 2\n"
        "v_mfma_f32_16x16x32_bf16 v[112:115], a[88:91], a[228:231], v[112:115]\n"
        "s_add_u32 m0, s41, 12288\n"
        "v_cvt_pk_bf16_f32 v200, v120, v121\n"
        "v_cvt_pk_bf16_f32 v201, v122, v123\n"
        "v_mfma_f32_16x16x32_bf16 v[112:115], a[84:87], a[224:227], v[112:115]\n"
        "v_cvt_pk_bf16_f32 v206, v120, v121\n"
        "v_cvt_pk_bf16_f32 v207, v122, v123\n"
        "v_mfma_f32_16x16x32_bf16 v[112:115], a[80:83], a[220:223], v[112:115]\n"
        "buffer_load_dwordx4 v242, s[8:11], s35 offen lds\n"
        "v_mfma_f32_16x16x32_bf16 v[112:115], a[76:79], a[216:219], v[112:115]\n"
        "v_mfma_f32_16x16x32_bf16 v[112:115], a[72:75], a[248:251], v[112:115]\n"
        "s_nop 2\n"
        "v_mfma_f32_16x16x32_bf16 v[96:99], v[152:155], v[196:199], v[32:35]\n"
        "s_waitcnt lgkmcnt(1)\n"
        "s_nop 1\n"
        "v_mfma_f32_16x16x32_bf16 v[96:99], v[168:171], v[200:203], v[96:99]\n"
        "s_nop 0\n"
        "v_cvt_pk_bf16_f32 v192, v112, v113\n"
        "v_cvt_pk_bf16_f32 v193, v114, v115\n"
        "v_mfma_f32_16x16x32_bf16 v[100:103], v[172:175], v[196:199], v[36:39]\n"
        "v_mfma_f32_16x16x32_bf16 v[100:103], v[188:191], v[204:207], v[100:103]\n"
        "ds_read_b128 v[202:205], v252 offset:4096\n"
        "v_mfma_f32_16x16x32_bf16 v[116:119], a[96:99], a[248:251], 0\n"
        "s_cmp_eq_u32 s37, 100663296\n"
        "v_exp_f32 v96, v96\n"
        "v_mfma_f32_16x16x32_bf16 v[116:119], a[100:103], a[216:219], v[116:119]\n"
        "s_cselect_b32 s46, 0, s37\n"
        "v_exp_f32 v97, v97\n"
        "v_mfma_f32_16x16x32_bf16 v[116:119], a[104:107], a[220:223], v[116:119]\n"
        "s_add_u32 s38, s46, s43\n"
        "v_exp_f32 v98, v98\n"
        "v_mfma_f32_16x16x32_bf16 v[116:119], a[108:111], a[224:227], v[116:119]\n"
        "s_add_u32 s39, s46, s44\n"
        "v_exp_f32 v100, v100\n"
        "v_mfma_f32_16x16x32_bf16 v[116:119], a[112:115], a[228:231], v[116:119]\n"
        "s_add_u32 s37, s37, 6291456\n"
        "v_exp_f32 v99, v99\n"
        "v_mfma_f32_16x16x32_bf16 v[116:119], a[116:119], a[232:235], v[116:119]\n"
        "s_add_u32 m0, s41, 13312\n"
        "v_exp_f32 v101, v101\n"
        "v_mfma_f32_16x16x32_bf16 v[120:123], a[140:143], a[232:235], 0\n"
        "v_exp_f32 v102, v102\n"
        "v_mfma_f32_16x16x32_bf16 v[120:123], a[136:139], a[228:231], v[120:123]\n"
        "buffer_load_dwordx4 v243, s[8:11], s35 offen lds\n"
        "v_mfma_f32_16x16x32_bf16 v[120:123], a[132:135], a[224:227], v[120:123]\n"
        "v_cvt_pk_bf16_f32 v80, v96, v97\n"
        "v_cvt_pk_bf16_f32 v194, v116, v117\n"
        "v_mfma_f32_16x16x32_bf16 v[120:123], a[128:131], a[220:223], v[120:123]\n"
        "v_cvt_pk_bf16_f32 v195, v118, v119\n"
        "v_cvt_pk_bf16_f32 v81, v98, v99\n"
        "v_mfma_f32_16x16x32_bf16 v[120:123], a[124:127], a[216:219], v[120:123]\n"
        "v_cvt_pk_bf16_f32 v82, v100, v101\n"
        "v_mfma_f32_16x16x32_bf16 v[120:123], a[120:123], a[248:251], v[120:123]\n"
        "v_exp_f32 v103, v103\n"
        "v_mfma_f32_16x16x32_bf16 v[112:115], a[248:251], a[144:147], 0\n"
        "v_cvt_pk_bf16_f32 v83, v102, v103\n"
        "s_nop 0\n"
        "v_mfma_f32_16x16x32_bf16 v[112:115], a[216:219], a[148:151], v[112:115]\n"
        "s_nop 2\n"
        "v_mfma_f32_16x16x32_bf16 v[112:115], a[220:223], a[152:155], v[112:115]\n"
        "ds_read_b128 a[236:239], v240 offset:13824\n"
        "v_cvt_pk_bf16_f32 v208, v120, v121\n"
        "v_mfma_f32_16x16x32_bf16 v[112:115], a[224:227], a[156:159], v[112:115]\n"
        "ds_read_b128 a[240:243], v241 offset:13824\n"
        "v_cvt_pk_bf16_f32 v209, v122, v123\n"
        "v_mfma_f32_16x16x32_bf16 v[112:115], a[228:231], a[160:163], v[112:115]\n"
        "ds_read_b128 a[244:247], v240 offset:13952\n"
        "v_mfma_f32_16x16x32_bf16 v[112:115], a[232:235], a[164:167], v[112:115]\n"
        "s_nop 2\n"
        "v_mfma_f32_16x16x32_bf16 v[104:107], v[192:195], v[156:159], v[8:11]\n"
        "s_nop 1\n"
        "v_mfma_f32_16x16x32_bf16 v[104:107], v[208:211], v[160:163], v[104:107]\n"
        "s_add_u32 m0, s41, 14336\n"
        "v_cvt_pk_bf16_f32 v132, v112, v113\n"
        "v_cvt_pk_bf16_f32 v133, v114, v115\n"
        "v_mfma_f32_16x16x32_bf16 v[116:119], a[232:235], a[188:191], 0\n"
        "v_mfma_f32_16x16x32_bf16 v[116:119], a[228:231], a[184:187], v[116:119]\n"
        "buffer_load_dwordx4 v244, s[8:11], s35 offen lds\n"
        "v_mfma_f32_16x16x32_bf16 v[116:119], a[224:227], a[180:183], v[116:119]\n"
        "v_mfma_f32_16x16x32_bf16 v[116:119], a[220:223], a[176:179], v[116:119]\n"
        "v_exp_f32 v104, v104\n"
        "v_mfma_f32_16x16x32_bf16 v[116:119], a[216:219], a[172:175], v[116:119]\n"
        "v_exp_f32 v105, v105\n"
        "v_mfma_f32_16x16x32_bf16 v[116:119], a[248:251], a[168:171], v[116:119]\n"
        "v_exp_f32 v106, v106\n"
        "v_mfma_f32_16x16x32_bf16 v[108:111], v[192:195], v[176:179], v[24:27]\n"
        "s_waitcnt lgkmcnt(4)\n"
        "v_exp_f32 v107, v107\n"
        "v_mfma_f32_16x16x32_bf16 v[108:111], v[208:211], v[180:183], v[108:111]\n"
        ".long 0xbec200ff\n"
        "45:\n"
        "s_branch 61b\n"
        "v_cvt_pk_bf16_f32 v68, v104, v105\n"
        "v_cvt_pk_bf16_f32 v69, v106, v107\n"
        "v_mfma_f32_16x16x32_bf16 v[120:123], a[248:251], a[192:195], 0\n"
        "ds_read_b128 a[248:251], v241 offset:13952\n"
        "v_cvt_pk_bf16_f32 v140, v116, v117\n"
        "v_mfma_f32_16x16x32_bf16 v[120:123], a[216:219], a[196:199], v[120:123]\n"
        "ds_read_b128 a[216:219], v240 offset:14080\n"
        "v_cvt_pk_bf16_f32 v141, v118, v119\n"
        "v_mfma_f32_16x16x32_bf16 v[120:123], a[220:223], a[200:203], v[120:123]\n"
        "ds_read_b128 a[220:223], v241 offset:14080\n"
        "v_mfma_f32_16x16x32_bf16 v[120:123], a[224:227], a[204:207], v[120:123]\n"
        "v_exp_f32 v108, v108\n"
        "v_mfma_f32_16x16x32_bf16 v[120:123], a[228:231], a[208:211], v[120:123]\n"
        "v_exp_f32 v109, v109\n"
        "v_mfma_f32_16x16x32_bf16 v[120:123], a[232:235], a[212:215], v[120:123]\n"
        "v_exp_f32 v110, v110\n"
        "v_mfma_f32_16x16x32_bf16 v[96:99], v[192:195], v[196:199], v[40:43]\n"
        "s_waitcnt lgkmcnt(6)\n"
        "v_exp_f32 v111, v111\n"
        "v_mfma_f32_16x16x32_bf16 v[96:99], v[208:211], v[200:203], v[96:99]\n"
        "s_waitcnt lgkmcnt(5)\n"
        "v_cvt_pk_bf16_f32 v76, v108, v109\n"
        "v_cvt_pk_bf16_f32 v77, v110, v111\n"
        "v_mfma_f32_16x16x32_bf16 v[112:115], a[72:75], a[236:239], 0\n"
        "s_waitcnt lgkmcnt(4)\n"
        "s_nop 0\n"
        "v_cvt_pk_bf16_f32 v148, v120, v121\n"
        "v_mfma_f32_16x16x32_bf16 v[112:115], a[76:79], a[240:243], v[112:115]\n"
        "s_waitcnt lgkmcnt(3)\n"
        "v_cvt_pk_bf16_f32 v149, v122, v123\n"
        "v_mfma_f32_16x16x32_bf16 v[112:115], a[80:83], a[244:247], v[112:115]\n"
        "s_waitcnt lgkmcnt(2)\n"
        "v_exp_f32 v96, v96\n"
        "v_mfma_f32_16x16x32_bf16 v[112:115], a[84:87], a[248:251], v[112:115]\n"
        "s_waitcnt lgkmcnt(1)\n"
        "v_exp_f32 v97, v97\n"
        "v_mfma_f32_16x16x32_bf16 v[112:115], a[88:91], a[216:219], v[112:115]\n"
        "s_waitcnt lgkmcnt(0)\n"
        "v_exp_f32 v98, v98\n"
        "v_mfma_f32_16x16x32_bf16 v[112:115], a[92:95], a[220:223], v[112:115]\n"
        "v_exp_f32 v99, v99\n"
        "v_mfma_f32_16x16x32_bf16 v[116:119], a[116:119], a[220:223], 0\n"
        "v_cvt_pk_bf16_f32 v84, v96, v97\n"
        "v_cvt_pk_bf16_f32 v85, v98, v99\n"
        "v_mfma_f32_16x16x32_bf16 v[116:119], a[112:115], a[216:219], v[116:119]\n"
        "v_add_u32 v240, s31, v240\n"
        "v_add_u32 v241, s31, v241\n"
        "v_mfma_f32_16x16x32_bf16 v[116:119], a[108:111], a[248:251], v[116:119]\n"
        "v_cvt_pk_bf16_f32 v212, v112, v113\n"
        "v_cvt_pk_bf16_f32 v213, v114, v115\n"
        "v_mfma_f32_16x16x32_bf16 v[116:119], a[104:107], a[244:247], v[116:119]\n"
        "v_mfma_f32_16x16x32_bf16 v[116:119], a[100:103], a[240:243], v[116:119]\n"
        "v_mfma_f32_16x16x32_bf16 v[116:119], a[96:99], a[236:239], v[116:119]\n"
        "s_nop 2\n"
        "v_mfma_f32_16x16x32_bf16 v[120:123], a[120:123], a[236:239], 0\n"
        "s_nop 2\n"
        "v_mfma_f32_16x16x32_bf16 v[120:123], a[124:127], a[240:243], v[120:123]\n"
        "s_add_u32 s47, s47, 6291456\n"
        "v_cvt_pk_bf16_f32 v214, v116, v117\n"
        "v_cvt_pk_bf16_f32 v215, v118, v119\n"
        "v_mfma_f32_16x16x32_bf16 v[120:123], a[128:131], a[244:247], v[120:123]\n"
        "s_add_u32 s36, s36, 2097152\n"
        "v_mfma_f32_16x16x32_bf16 v[120:123], a[132:135], a[248:251], v[120:123]\n"
        "s_xor_b32 s48, s48, 8192\n"
        "v_mfma_f32_16x16x32_bf16 v[120:123], a[136:139], a[216:219], v[120:123]\n"
        "s_barrier\n"
        "v_mfma_f32_16x16x32_bf16 v[120:123], a[140:143], a[220:223], v[120:123]\n"
        "s_nop 2\n"
        "v_mfma_f32_16x16x32_bf16 v[112:115], a[220:223], a[164:167], 0\n"
        "s_nop 2\n"
        "v_mfma_f32_16x16x32_bf16 v[112:115], a[216:219], a[160:163], v[112:115]\n"
        "v_cvt_pk_bf16_f32 v230, v120, v121\n"
        "v_cvt_pk_bf16_f32 v231, v122, v123\n"
        "v_mfma_f32_16x16x32_bf16 v[112:115], a[248:251], a[156:159], v[112:115]\n"
        "v_mfma_f32_16x16x32_bf16 v[112:115], a[244:247], a[152:155], v[112:115]\n"
        "v_mfma_f32_16x16x32_bf16 v[112:115], a[240:243], a[148:151], v[112:115]\n"
        "v_mfma_f32_16x16x32_bf16 v[112:115], a[236:239], a[144:147], v[112:115]\n"
        "s_nop 2\n"
        "v_mfma_f32_16x16x32_bf16 v[100:103], v[212:215], v[156:159], v[12:15]\n"
        "s_nop 2\n"
        "v_mfma_f32_16x16x32_bf16 v[100:103], v[228:231], v[164:167], v[100:103]\n"
        "v_cvt_pk_bf16_f32 v134, v112, v113\n"
        "v_cvt_pk_bf16_f32 v135, v114, v115\n"
        "v_mfma_f32_16x16x32_bf16 v[116:119], a[236:239], a[168:171], 0\n"
        "s_sub_u32 s27, s27, 1\n"
        "v_mfma_f32_16x16x32_bf16 v[116:119], a[240:243], a[172:175], v[116:119]\n"
        "s_cmp_lg_u32 s27, 0\n"
        "v_mfma_f32_16x16x32_bf16 v[116:119], a[244:247], a[176:179], v[116:119]\n"
        "s_cbranch_scc1 10b\n"
        "s_branch 19f\n"
        ".p2align 6\n"
        "51:\n"
        "s_branch 61b\n"
        "s_nop 0\n"
        "s_nop 0\n"
        "s_nop 0\n"
        "s_nop 0\n"
        "s_nop 0\n"
        "12:\n"
        "10:\n"
        "v_mfma_f32_16x16x32_bf16 v[116:119], a[248:251], a[180:183], v[116:119]\n"
        "v_mfma_f32_16x16x32_bf16 v[116:119], a[216:219], a[184:187], v[116:119]\n"
        "s_nop 0\n"
        "v_exp_f32 v100, v100\n"
        "v_mfma_f32_16x16x32_bf16 v[116:119], a[220:223], a[188:191], v[116:119]\n"
        "v_exp_f32 v101, v101\n"
        "v_mfma_f32_16x16x32_bf16 v[104:107], v[212:215], v[176:179], v[28:31]\n"
        "v_exp_f32 v102, v102\n"
        "v_mfma_f32_16x16x32_bf16 v[104:107], v[228:231], v[184:187], v[104:107]\n"
        "v_exp_f32 v103, v103\n"
        "v_mfma_f32_16x16x32_bf16 v[120:123], a[220:223], a[212:215], 0\n"
        "v_cvt_pk_bf16_f32 v70, v100, v101\n"
        "v_cvt_pk_bf16_f32 v71, v102, v103\n"
        "v_mfma_f32_16x16x32_bf16 v[120:123], a[216:219], a[208:211], v[120:123]\n"
        "v_cvt_pk_bf16_f32 v142, v116, v117\n"
        "v_cvt_pk_bf16_f32 v143, v118, v119\n"
        "v_mfma_f32_16x16x32_bf16 v[120:123], a[248:251], a[204:207], v[120:123]\n"
        "v_mov_b64 v[96:97], 0\n"
        "v_mfma_f32_16x16x32_bf16 v[120:123], a[244:247], a[200:203], v[120:123]\n"
        "v_exp_f32 v104, v104\n"
        "v_mfma_f32_16x16x32_bf16 v[120:123], a[240:243], a[196:199], v[120:123]\n"
        "v_exp_f32 v105, v105\n"
        "v_mfma_f32_16x16x32_bf16 v[120:123], a[236:239], a[192:195], v[120:123]\n"
        "v_exp_f32 v106, v106\n"
        "v_mfma_f32_16x16x32_bf16 v[108:111], v[212:215], v[196:199], v[44:47]\n"
        "v_exp_f32 v107, v107\n"
        "v_mfma_f32_16x16x32_bf16 v[108:111], v[228:231], v[204:207], v[108:111]\n"
        "v_cvt_pk_bf16_f32 v78, v104, v105\n"
        "v_cvt_pk_bf16_f32 v79, v106, v107\n"
        "v_mfma_f32_16x16x32_bf16 v[112:115], a[0:3], a[236:239], 0\n"
        "s_nop 0\n"
        "v_cvt_pk_bf16_f32 v150, v120, v121\n"
        "v_cvt_pk_bf16_f32 v151, v122, v123\n"
        "v_mfma_f32_16x16x32_bf16 v[112:115], a[4:7], a[240:243], v[112:115]\n"
        "v_mfma_f32_16x16x32_bf16 v[112:115], a[8:11], a[244:247], v[112:115]\n"
        "v_exp_f32 v108, v108\n"
        "v_mfma_f32_16x16x32_bf16 v[112:115], a[12:15], a[248:251], v[112:115]\n"
        "v_exp_f32 v109, v109\n"
        "v_mfma_f32_16x16x32_bf16 v[112:115], a[16:19], a[216:219], v[112:115]\n"
        "v_exp_f32 v110, v110\n"
        "v_mfma_f32_16x16x32_bf16 v[112:115], a[20:23], a[220:223], v[112:115]\n"
        "v_exp_f32 v111, v111\n"
        "v_smfmac_f32_16x16x64_bf16 v[96:99], a[252:255], v[64:71], v237\n"
        "v_cvt_pk_bf16_f32 v86, v108, v109\n"
        "v_mfma_f32_16x16x32_bf16 v[100:103], v[132:135], v[68:71], 0\n"
        "v_mov_b64 v[124:125], 0\n"
        "v_cvt_pk_bf16_f32 v87, v110, v111\n"
        "v_mfma_f32_16x16x32_bf16 v[100:103], v[128:131], v[64:67], v[100:103]\n"
        "s_nop 0\n"
        "v_cvt_pk_bf16_f32 v216, v112, v113\n"
        "v_cvt_pk_bf16_f32 v217, v114, v115\n"
        "v_mfma_f32_16x16x32_bf16 v[104:107], v[136:139], v[64:67], 0\n"
        "ds_read_b128 v[222:225], v251 offset:4608\n"
        "v_add_f32 v97, v96, v97\n"
        "v_mfma_f32_16x16x32_bf16 v[104:107], v[140:143], v[68:71], v[104:107]\n"
        "v_rcp_f32 v96, v97\n"
        "v_mfma_f32_16x16x32_bf16 v[108:111], v[148:151], v[68:71], 0\n"
        "v_mul_f32 v100, v96, v100\n"
        "v_mul_f32 v101, v96, v101\n"
        "v_mfma_f32_16x16x32_bf16 v[108:111], v[144:147], v[64:67], v[108:111]\n"
        "v_mul_f32 v102, v96, v102\n"
        "v_mul_f32 v103, v96, v103\n"
        "v_mfma_f32_16x16x32_bf16 v[116:119], a[44:47], a[220:223], 0\n"
        "v_cvt_pk_bf16_f32 v100, v100, v101\n"
        "v_cvt_pk_bf16_f32 v101, v102, v103\n"
        "v_mfma_f32_16x16x32_bf16 v[116:119], a[40:43], a[216:219], v[116:119]\n"
        "v_mul_f32 v104, v96, v104\n"
        "v_mul_f32 v105, v96, v105\n"
        "v_mfma_f32_16x16x32_bf16 v[116:119], a[36:39], a[248:251], v[116:119]\n"
        "v_mul_f32 v106, v96, v106\n"
        "v_mul_f32 v107, v96, v107\n"
        "v_mfma_f32_16x16x32_bf16 v[116:119], a[32:35], a[244:247], v[116:119]\n"
        "v_cvt_pk_bf16_f32 v102, v104, v105\n"
        "v_cvt_pk_bf16_f32 v103, v106, v107\n"
        "v_mfma_f32_16x16x32_bf16 v[116:119], a[28:31], a[240:243], v[116:119]\n"
        "v_mul_f32 v108, v96, v108\n"
        "v_mul_f32 v109, v96, v109\n"
        "v_mfma_f32_16x16x32_bf16 v[116:119], a[24:27], a[236:239], v[116:119]\n"
        "v_mul_f32 v110, v96, v110\n"
        "v_mul_f32 v111, v96, v111\n"
        "v_smfmac_f32_16x16x64_bf16 v[124:127], a[252:255], v[72:79], v237\n"
        "v_cvt_pk_bf16_f32 v108, v108, v109\n"
        "v_mfma_f32_16x16x32_bf16 v[64:67], v[132:135], v[76:79], 0\n"
        "ds_write_b32 v245, v108 offset:4096\n"
        "v_mfma_f32_16x16x32_bf16 v[64:67], v[128:131], v[72:75], v[64:67]\n"
        "ds_read_b128 v[156:159], v247 offset:0\n"
        "v_cvt_pk_bf16_f32 v218, v116, v117\n"
        "v_mfma_f32_16x16x32_bf16 v[68:71], v[136:139], v[72:75], 0\n"
        "ds_read_b128 v[160:163], v247 offset:4096\n"
        "v_cvt_pk_bf16_f32 v219, v118, v119\n"
        "v_mfma_f32_16x16x32_bf16 v[68:71], v[140:143], v[76:79], v[68:71]\n"
        "ds_read_b128 v[164:167], v247 offset:4352\n"
        "v_cvt_pk_bf16_f32 v109, v110, v111\n"
        "v_mfma_f32_16x16x32_bf16 v[120:123], v[148:151], v[76:79], 0\n"
        "ds_write_b32 v245, v109 offset:5120\n"
        "v_mfma_f32_16x16x32_bf16 v[120:123], v[144:147], v[72:75], v[120:123]\n"
        "v_max_u32 v236, v96, v97\n"
        "v_mov_b64 v[116:117], 0\n"
        "v_mfma_f32_16x16x32_bf16 v[112:115], a[48:51], a[236:239], 0\n"
        "v_add_f32 v125, v124, v125\n"
        "v_mfma_f32_16x16x32_bf16 v[112:115], a[52:55], a[240:243], v[112:115]\n"
        "v_rcp_f32 v124, v125\n"
        "v_mfma_f32_16x16x32_bf16 v[112:115], a[56:59], a[244:247], v[112:115]\n"
        "v_mul_f32 v64, v124, v64\n"
        "v_mul_f32 v65, v124, v65\n"
        "v_mfma_f32_16x16x32_bf16 v[112:115], a[60:63], a[248:251], v[112:115]\n"
        "v_mul_f32 v66, v124, v66\n"
        "v_mul_f32 v67, v124, v67\n"
        "v_mfma_f32_16x16x32_bf16 v[112:115], a[64:67], a[216:219], v[112:115]\n"
        "v_cvt_pk_bf16_f32 v64, v64, v65\n"
        "v_cvt_pk_bf16_f32 v65, v66, v67\n"
        "v_mfma_f32_16x16x32_bf16 v[112:115], a[68:71], a[220:223], v[112:115]\n"
        "ds_read_b128 a[216:219], v248 offset:0\n"
        "v_mul_f32 v68, v124, v68\n"
        "v_smfmac_f32_16x16x64_bf16 v[116:119], a[252:255], v[80:87], v237\n"
        "ds_read_b128 a[220:223], v248 offset:3072\n"
        "v_mfma_f32_16x16x32_bf16 v[72:75], v[132:135], v[84:87], 0\n"
        "v_mul_f32 v69, v124, v69\n"
        "v_mul_f32 v70, v124, v70\n"
        "v_mfma_f32_16x16x32_bf16 v[72:75], v[128:131], v[80:83], v[72:75]\n"
        "v_cvt_pk_bf16_f32 v220, v112, v113\n"
        "v_cvt_pk_bf16_f32 v221, v114, v115\n"
        "v_mfma_f32_16x16x32_bf16 v[76:79], v[136:139], v[80:83], 0\n"
        "ds_read_b128 a[224:227], v248 offset:32\n"
        "v_cvt_pk_bf16_f32 v226, v112, v113\n"
        "v_mfma_f32_16x16x32_bf16 v[76:79], v[140:143], v[84:87], v[76:79]\n"
        "ds_read_b128 a[228:231], v248 offset:3104\n"
        "v_cvt_pk_bf16_f32 v227, v114, v115\n"
        "v_mfma_f32_16x16x32_bf16 v[96:99], v[148:151], v[84:87], 0\n"
        "v_mul_f32 v71, v124, v71\n"
        "v_cvt_pk_bf16_f32 v66, v68, v69\n"
        "v_mfma_f32_16x16x32_bf16 v[96:99], v[144:147], v[80:83], v[96:99]\n"
        "v_cvt_pk_bf16_f32 v67, v70, v71\n"
        "v_mul_f32 v120, v124, v120\n"
        "v_mfma_f32_16x16x32_bf16 v[104:107], v[152:155], v[216:219], v[48:51]\n"
        "s_waitcnt lgkmcnt(9)\n"
        "ds_read_b128 v[152:155], v247 offset:256\n"
        "v_mul_f32 v121, v124, v121\n"
        "v_mfma_f32_16x16x32_bf16 v[104:107], v[168:171], v[220:223], v[104:107]\n"
        "v_mul_f32 v122, v124, v122\n"
        "v_mul_f32 v123, v124, v123\n"
        "v_mfma_f32_16x16x32_bf16 v[108:111], v[172:175], v[216:219], v[52:55]\n"
        "v_cvt_pk_bf16_f32 v120, v120, v121\n"
        "v_cvt_pk_bf16_f32 v121, v122, v123\n"
        "v_mfma_f32_16x16x32_bf16 v[108:111], v[188:191], v[224:227], v[108:111]\n"
        "s_waitcnt lgkmcnt(4)\n"
        "ds_read_b128 v[222:225], v252 offset:4608\n"
        "v_max3_u32 v236, v236, v124, v125\n"
        "v_mfma_f32_16x16x32_bf16 v[176:179], v[156:159], a[216:219], 0\n"
        "s_waitcnt lgkmcnt(4)\n"
        "ds_write_b32 v245, v120 offset:4352\n"
        "v_mfma_f32_16x16x32_bf16 v[176:179], v[160:163], a[220:223], v[176:179]\n"
        "ds_write_b32 v245, v121 offset:5376\n"
        "v_mfma_f32_16x16x32_bf16 v[196:199], v[164:167], a[220:223], 0\n"
        "s_waitcnt lgkmcnt(3)\n"
        "ds_read_b128 a[232:235], v254 offset:0\n"
        "v_add_f32 v117, v116, v117\n"
        "v_mfma_f32_16x16x32_bf16 v[196:199], v[152:155], a[216:219], v[196:199]\n"
        "ds_read_b128 a[220:223], v254 offset:3072\n"
        "v_mfma_f32_16x16x32_bf16 v[200:203], v[152:155], a[224:227], 0\n"
        "ds_read_b128 a[216:219], v254 offset:32\n"
        "v_mfma_f32_16x16x32_bf16 v[200:203], v[164:167], a[228:231], v[200:203]\n"
        "v_rcp_f32 v116, v117\n"
        "v_mfma_f32_16x16x32_bf16 v[180:183], v[160:163], a[228:231], 0\n"
        "ds_read_b128 a[228:231], v254 offset:3104\n"
        "v_mul_f32 v72, v116, v72\n"
        "v_mfma_f32_16x16x32_bf16 v[180:183], v[156:159], a[224:227], v[180:183]\n"
        "ds_read_b128 a[236:239], v247 offset:8192\n"
        "v_mul_f32 v73, v116, v73\n"
        "v_mfma_f32_16x16x32_bf16 v[112:115], v[192:195], v[216:219], v[56:59]\n"
        "s_waitcnt lgkmcnt(7)\n"
        "ds_read_b128 a[224:227], v248 offset:16\n"
        "v_mul_f32 v74, v116, v74\n"
        "v_mfma_f32_16x16x32_bf16 v[112:115], v[208:211], v[220:223], v[112:115]\n"
        "ds_read_b128 a[240:243], v247 offset:12288\n"
        "v_mul_f32 v75, v116, v75\n"
        "v_mfma_f32_16x16x32_bf16 v[68:71], v[212:215], v[216:219], v[60:63]\n"
        "ds_read_b128 a[244:247], v247 offset:12544\n"
        "v_cvt_pk_bf16_f32 v72, v72, v73\n"
        "v_mfma_f32_16x16x32_bf16 v[68:71], v[228:231], v[224:227], v[68:71]\n"
        "s_waitcnt lgkmcnt(7)\n"
        "ds_read_b128 a[248:251], v247 offset:8448\n"
        "v_cvt_pk_bf16_f32 v73, v74, v75\n"
        "v_mfma_f32_16x16x32_bf16 v[184:187], v[156:159], a[232:235], 0\n"
        "s_waitcnt lgkmcnt(7)\n"
        "v_exp_f32 v104, v104\n"
        "v_mfma_f32_16x16x32_bf16 v[184:187], v[160:163], a[220:223], v[184:187]\n"
        "v_exp_f32 v105, v105\n"
        "v_mfma_f32_16x16x32_bf16 v[204:207], v[164:167], a[220:223], 0\n"
        "ds_read_b128 a[220:223], v248 offset:3088\n"
        "v_mul_f32 v76, v116, v76\n"
        "v_mfma_f32_16x16x32_bf16 v[204:207], v[152:155], a[232:235], v[204:207]\n"
        "s_waitcnt lgkmcnt(7)\n"
        "ds_read_b128 a[232:235], v254 offset:16\n"
        "v_mul_f32 v77, v116, v77\n"
        "v_mfma_f32_16x16x32_bf16 v[192:195], v[152:155], a[216:219], 0\n"
        "s_waitcnt lgkmcnt(7)\n"
        "v_mul_f32 v78, v116, v78\n"
        "v_mul_f32 v79, v116, v79\n"
        "v_mfma_f32_16x16x32_bf16 v[192:195], v[164:167], a[228:231], v[192:195]\n"
        "v_exp_f32 v106, v106\n"
        "v_mfma_f32_16x16x32_bf16 v[172:175], v[160:163], a[228:231], 0\n"
        "ds_read_b128 a[228:231], v254 offset:3088\n"
        "v_cvt_pk_bf16_f32 v88, v104, v105\n"
        "v_mfma_f32_16x16x32_bf16 v[172:175], v[156:159], a[216:219], v[172:175]\n"
        "s_waitcnt lgkmcnt(6)\n"
        "ds_read_b128 a[216:219], v248 offset:6144\n"
        "v_cvt_pk_bf16_f32 v74, v76, v77\n"
        "v_mfma_f32_16x16x32_bf16 v[212:215], v[156:159], a[224:227], 0\n"
        "s_waitcnt lgkmcnt(3)\n"
        "v_exp_f32 v108, v108\n"
        "v_mfma_f32_16x16x32_bf16 v[212:215], v[160:163], a[220:223], v[212:215]\n"
        "v_exp_f32 v107, v107\n"
        "v_mfma_f32_16x16x32_bf16 v[216:219], v[164:167], a[220:223], 0\n"
        "ds_read_b128 a[220:223], v248 offset:9216\n"
        "v_cvt_pk_bf16_f32 v75, v78, v79\n"
        "v_mfma_f32_16x16x32_bf16 v[216:219], v[152:155], a[224:227], v[216:219]\n"
        "s_waitcnt lgkmcnt(3)\n"
        "ds_read_b128 a[224:227], v248 offset:6176\n"
        "v_cvt_pk_bf16_f32 v89, v106, v107\n"
        "v_mfma_f32_16x16x32_bf16 v[224:227], v[152:155], a[232:235], 0\n"
        "s_waitcnt lgkmcnt(3)\n"
        "v_exp_f32 v109, v109\n"
        "v_mfma_f32_16x16x32_bf16 v[224:227], v[164:167], a[228:231], v[224:227]\n"
        "v_exp_f32 v110, v110\n"
        "v_mfma_f32_16x16x32_bf16 v[220:223], v[160:163], a[228:231], 0\n"
        "ds_read_b128 a[228:231], v248 offset:9248\n"
        "v_cvt_pk_bf16_f32 v90, v108, v109\n"
        "v_mfma_f32_16x16x32_bf16 v[220:223], v[156:159], a[232:235], v[220:223]\n"
        "s_waitcnt lgkmcnt(3)\n"
        "ds_read_b128 a[232:235], v254 offset:6144\n"
        "v_mul_f32 v96, v116, v96\n"
        "v_mfma_f32_16x16x32_bf16 v[176:179], a[236:239], a[216:219], v[176:179]\n"
        "s_waitcnt lgkmcnt(3)\n"
        "v_mul_f32 v97, v116, v97\n"
        "v_mul_f32 v98, v116, v98\n"
        "v_mfma_f32_16x16x32_bf16 v[176:179], a[240:243], a[220:223], v[176:179]\n"
        "v_mul_f32 v99, v116, v99\n"
        "v_cvt_pk_bf16_f32 v96, v96, v97\n"
        "v_mfma_f32_16x16x32_bf16 v[196:199], a[244:247], a[220:223], v[196:199]\n"
        "ds_read_b128 a[220:223], v254 offset:9216\n"
        "v_cvt_pk_bf16_f32 v97, v98, v99\n"
        "v_mfma_f32_16x16x32_bf16 v[196:199], a[248:251], a[216:219], v[196:199]\n"
        "s_waitcnt lgkmcnt(3)\n"
        "ds_read_b128 a[216:219], v254 offset:6176\n"
        "v_max3_u32 v236, v236, v116, v117\n"
        "v_mfma_f32_16x16x32_bf16 v[200:203], a[248:251], a[224:227], v[200:203]\n"
        "s_waitcnt lgkmcnt(3)\n"
        "ds_read_b128 v[156:159], v247 offset:16384\n"
        "v_mfma_f32_16x16x32_bf16 v[200:203], a[244:247], a[228:231], v[200:203]\n"
        "ds_read_b128 v[160:163], v247 offset:20480\n"
        "v_mfma_f32_16x16x32_bf16 v[180:183], a[240:243], a[228:231], v[180:183]\n"
        "ds_read_b128 a[228:231], v254 offset:9248\n"
        "v_mfma_f32_16x16x32_bf16 v[180:183], a[236:239], a[224:227], v[180:183]\n"
        "s_waitcnt lgkmcnt(5)\n"
        "ds_read_b128 a[224:227], v248 offset:6160\n"
        "v_mfma_f32_16x16x32_bf16 v[184:187], a[236:239], a[232:235], v[184:187]\n"
        "s_waitcnt lgkmcnt(5)\n"
        "ds_read_b128 v[164:167], v247 offset:20736\n"
        "v_mfma_f32_16x16x32_bf16 v[184:187], a[240:243], a[220:223], v[184:187]\n"
        "ds_read_b128 v[152:155], v247 offset:16640\n"
        "v_mfma_f32_16x16x32_bf16 v[204:207], a[244:247], a[220:223], v[204:207]\n"
        "ds_read_b128 a[220:223], v248 offset:9232\n"
        "v_mfma_f32_16x16x32_bf16 v[204:207], a[248:251], a[232:235], v[204:207]\n"
        "s_waitcnt lgkmcnt(7)\n"
        "ds_read_b128 a[232:235], v254 offset:6160\n"
        "v_mfma_f32_16x16x32_bf16 v[192:195], a[248:251], a[216:219], v[192:195]\n"
        "s_waitcnt lgkmcnt(5)\n"
        "v_exp_f32 v111, v111\n"
        "v_mfma_f32_16x16x32_bf16 v[192:195], a[244:247], a[228:231], v[192:195]\n"
        "v_cvt_pk_bf16_f32 v91, v110, v111\n"
        "v_mfma_f32_16x16x32_bf16 v[172:175], a[240:243], a[228:231], v[172:175]\n"
        "ds_read_b128 a[228:231], v254 offset:9232\n"
        "v_mfma_f32_16x16x32_bf16 v[172:175], a[236:239], a[216:219], v[172:175]\n"
        "s_waitcnt lgkmcnt(5)\n"
        "ds_read_b128 a[216:219], v248 offset:12288\n"
        "v_mfma_f32_16x16x32_bf16 v[212:215], a[236:239], a[224:227], v[212:215]\n"
        "s_waitcnt lgkmcnt(3)\n"
        "v_exp_f32 v112, v112\n"
        "v_mfma_f32_16x16x32_bf16 v[212:215], a[240:243], a[220:223], v[212:215]\n"
        "v_exp_f32 v113, v113\n"
        "v_mfma_f32_16x16x32_bf16 v[216:219], a[244:247], a[220:223], v[216:219]\n"
        "ds_read_b128 a[220:223], v248 offset:15360\n"
        "v_cvt_pk_bf16_f32 v92, v112, v113\n"
        "v_mfma_f32_16x16x32_bf16 v[216:219], a[248:251], a[224:227], v[216:219]\n"
        "s_waitcnt lgkmcnt(3)\n"
        "ds_read_b128 a[224:227], v248 offset:12320\n"
        "v_mfma_f32_16x16x32_bf16 v[224:227], a[248:251], a[232:235], v[224:227]\n"
        "s_waitcnt lgkmcnt(3)\n"
        "v_exp_f32 v114, v114\n"
        "v_mfma_f32_16x16x32_bf16 v[224:227], a[244:247], a[228:231], v[224:227]\n"
        "v_exp_f32 v68, v68\n"
        "v_mfma_f32_16x16x32_bf16 v[220:223], a[240:243], a[228:231], v[220:223]\n"
        "ds_read_b128 a[228:231], v248 offset:15392\n"
        "v_mfma_f32_16x16x32_bf16 v[220:223], a[236:239], a[232:235], v[220:223]\n"
        "s_waitcnt lgkmcnt(3)\n"
        "ds_read_b128 a[232:235], v254 offset:12288\n"
        "v_mfma_f32_16x16x32_bf16 v[176:179], v[156:159], a[216:219], v[176:179]\n"
        "s_waitcnt lgkmcnt(3)\n"
        "v_exp_f32 v115, v115\n"
        "v_mfma_f32_16x16x32_bf16 v[176:179], v[160:163], a[220:223], v[176:179]\n"
        "v_exp_f32 v69, v69\n"
        "v_mfma_f32_16x16x32_bf16 v[196:199], v[164:167], a[220:223], v[196:199]\n"
        "ds_read_b128 a[220:223], v254 offset:15360\n"
        "v_cvt_pk_bf16_f32 v93, v114, v115\n"
        "v_mfma_f32_16x16x32_bf16 v[196:199], v[152:155], a[216:219], v[196:199]\n"
        "s_waitcnt lgkmcnt(3)\n"
        "ds_read_b128 a[216:219], v254 offset:12320\n"
        "v_cvt_pk_bf16_f32 v94, v68, v69\n"
        "v_mfma_f32_16x16x32_bf16 v[200:203], v[152:155], a[224:227], v[200:203]\n"
        "s_waitcnt lgkmcnt(3)\n"
        "v_exp_f32 v70, v70\n"
        "v_mfma_f32_16x16x32_bf16 v[200:203], v[164:167], a[228:231], v[200:203]\n"
        "s_waitcnt vmcnt(0)\n"
        "v_exp_f32 v71, v71\n"
        "v_mfma_f32_16x16x32_bf16 v[180:183], v[160:163], a[228:231], v[180:183]\n"
        "ds_read_b128 a[228:231], v254 offset:15392\n"
        "v_cvt_pk_bf16_f32 v95, v70, v71\n"
        "v_mfma_f32_16x16x32_bf16 v[180:183], v[156:159], a[224:227], v[180:183]\n"
        "s_waitcnt lgkmcnt(3)\n"
        "ds_read_b128 a[224:227], v248 offset:12304\n"
        "v_mfma_f32_16x16x32_bf16 v[184:187], v[156:159], a[232:235], v[184:187]\n"
        "s_waitcnt lgkmcnt(3)\n"
        "v_mfma_f32_16x16x32_bf16 v[184:187], v[160:163], a[220:223], v[184:187]\n"
        "s_barrier\n"
        "v_mfma_f32_16x16x32_bf16 v[204:207], v[164:167], a[220:223], v[204:207]\n"
        "ds_read_b128 a[220:223], v248 offset:15376\n"
        "v_mfma_f32_16x16x32_bf16 v[204:207], v[152:155], a[232:235], v[204:207]\n"
        "s_waitcnt lgkmcnt(3)\n"
        "ds_read_b128 a[232:235], v254 offset:12304\n"
        "v_mfma_f32_16x16x32_bf16 v[192:195], v[152:155], a[216:219], v[192:195]\n"
        "s_waitcnt lgkmcnt(3)\n"
        "ds_read_b128 a[236:239], v240 offset:0\n"
        "v_mfma_f32_16x16x32_bf16 v[192:195], v[164:167], a[228:231], v[192:195]\n"
        "ds_read_b128 a[240:243], v241 offset:0\n"
        "v_mfma_f32_16x16x32_bf16 v[172:175], v[160:163], a[228:231], v[172:175]\n"
        "ds_read_b128 a[228:231], v254 offset:15376\n"
        "v_mfma_f32_16x16x32_bf16 v[172:175], v[156:159], a[216:219], v[172:175]\n"
        "s_waitcnt lgkmcnt(5)\n"
        "ds_read_b128 a[244:247], v240 offset:128\n"
        "v_mfma_f32_16x16x32_bf16 v[212:215], v[156:159], a[224:227], v[212:215]\n"
        "s_waitcnt lgkmcnt(5)\n"
        "ds_read_b128 a[248:251], v241 offset:128\n"
        "v_mfma_f32_16x16x32_bf16 v[212:215], v[160:163], a[220:223], v[212:215]\n"
        "ds_read_b128 a[216:219], v240 offset:256\n"
        "v_mfma_f32_16x16x32_bf16 v[216:219], v[164:167], a[220:223], v[216:219]\n"
        "ds_read_b128 a[220:223], v241 offset:256\n"
        "v_mfma_f32_16x16x32_bf16 v[216:219], v[152:155], a[224:227], v[216:219]\n"
        "s_waitcnt lgkmcnt(7)\n"
        "v_mfma_f32_16x16x32_bf16 v[224:227], v[152:155], a[232:235], v[224:227]\n"
        "s_waitcnt lgkmcnt(4)\n"
        "v_mfma_f32_16x16x32_bf16 v[224:227], v[164:167], a[228:231], v[224:227]\n"
        "ds_write_b32 v245, v96 offset:4608\n"
        "v_mfma_f32_16x16x32_bf16 v[220:223], v[160:163], a[228:231], v[220:223]\n"
        "v_mfma_f32_16x16x32_bf16 v[220:223], v[156:159], a[232:235], v[220:223]\n"
        "ds_write_b32 v245, v97 offset:5632\n"
        "v_mfma_f32_16x16x32_bf16 v[112:115], a[72:75], a[236:239], 0\n"
        "v_mov_b64 v[124:125], 0\n"
        "v_cvt_pk_bf16_f32 v232, v176, v180\n"
        "v_mfma_f32_16x16x32_bf16 v[112:115], a[76:79], a[240:243], v[112:115]\n"
        "s_waitcnt lgkmcnt(5)\n"
        "v_cvt_pk_bf16_f32 v233, v184, v172\n"
        "v_cvt_pk_bf16_f32 v234, v177, v181\n"
        "v_mfma_f32_16x16x32_bf16 v[112:115], a[80:83], a[244:247], v[112:115]\n"
        "s_waitcnt lgkmcnt(4)\n"
        "buffer_store_dwordx2 v[232:233], v250, s[16:19], s38 offen offset:0 sc1\n"
        "v_cvt_pk_bf16_f32 v235, v185, v173\n"
        "v_mfma_f32_16x16x32_bf16 v[112:115], a[84:87], a[248:251], v[112:115]\n"
        "s_waitcnt lgkmcnt(3)\n"
        "v_cvt_pk_bf16_f32 v232, v178, v182\n"
        "v_cvt_pk_bf16_f32 v233, v186, v174\n"
        "v_mfma_f32_16x16x32_bf16 v[112:115], a[88:91], a[216:219], v[112:115]\n"
        "s_waitcnt lgkmcnt(2)\n"
        "v_mfma_f32_16x16x32_bf16 v[112:115], a[92:95], a[220:223], v[112:115]\n"
        "ds_write_b32 v245, v100 offset:0\n"
        "v_smfmac_f32_16x16x64_bf16 v[124:127], a[252:255], v[88:95], v237\n"
        "s_nop 1\n"
        "v_mfma_f32_16x16x32_bf16 v[80:83], v[132:135], v[92:95], 0\n"
        "buffer_store_dwordx2 v[234:235], v250, s[16:19], s38 offen offset:384 sc1\n"
        "s_nop 0\n"
        "v_mfma_f32_16x16x32_bf16 v[80:83], v[128:131], v[88:91], v[80:83]\n"
        "s_add_u32 s41, s30, s40\n"
        "v_cvt_pk_bf16_f32 v152, v112, v113\n"
        "v_cvt_pk_bf16_f32 v153, v114, v115\n"
        "v_mfma_f32_16x16x32_bf16 v[84:87], v[136:139], v[88:91], 0\n"
        "v_cvt_pk_bf16_f32 v234, v179, v183\n"
        "v_cvt_pk_bf16_f32 v235, v187, v175\n"
        "v_mfma_f32_16x16x32_bf16 v[84:87], v[140:143], v[92:95], v[84:87]\n"
        "v_add_f32 v125, v124, v125\n"
        "v_mfma_f32_16x16x32_bf16 v[96:99], v[148:151], v[92:95], 0\n"
        "buffer_store_dwordx2 v[232:233], v250, s[16:19], s38 offen offset:768 sc1\n"
        "v_mfma_f32_16x16x32_bf16 v[96:99], v[144:147], v[88:91], v[96:99]\n"
        "s_add_u32 s35, s47, 786432\n"
        "v_rcp_f32 v124, v125\n"
        "v_mfma_f32_16x16x32_bf16 v[116:119], a[116:119], a[220:223], 0\n"
        "s_sub_u32 s46, s35, 100663296\n"
        "v_mul_f32 v80, v124, v80\n"
        "v_mul_f32 v81, v124, v81\n"
        "v_mfma_f32_16x16x32_bf16 v[116:119], a[112:115], a[216:219], v[116:119]\n"
        "s_cmp_lt_u32 s46, 786432\n"
        "v_mul_f32 v82, v124, v82\n"
        "v_mul_f32 v83, v124, v83\n"
        "v_mfma_f32_16x16x32_bf16 v[116:119], a[108:111], a[248:251], v[116:119]\n"
        "buffer_store_dwordx2 v[234:235], v250, s[16:19], s38 offen offset:1152 sc1\n"
        "v_cvt_pk_bf16_f32 v80, v80, v81\n"
        "v_mfma_f32_16x16x32_bf16 v[116:119], a[104:107], a[244:247], v[116:119]\n"
        "s_cselect_b32 s35, s46, s35\n"
        "v_cvt_pk_bf16_f32 v81, v82, v83\n"
        "v_cvt_pk_bf16_f32 v232, v196, v200\n"
        "v_mfma_f32_16x16x32_bf16 v[116:119], a[100:103], a[240:243], v[116:119]\n"
        "v_cvt_pk_bf16_f32 v233, v204, v192\n"
        "v_mul_f32 v84, v124, v84\n"
        "v_mfma_f32_16x16x32_bf16 v[116:119], a[96:99], a[236:239], v[116:119]\n"
        "v_mul_f32 v85, v124, v85\n"
        "v_mul_f32 v86, v124, v86\n"
        "v_mfma_f32_16x16x32_bf16 v[120:123], a[120:123], a[236:239], 0\n"
        "ds_write_b32 v245, v101 offset:1024\n"
        "v_mfma_f32_16x16x32_bf16 v[120:123], a[124:127], a[240:243], v[120:123]\n"
        "buffer_store_dwordx2 v[232:233], v250, s[16:19], s39 offen offset:0 sc1\n"
        "v_mul_f32 v87, v124, v87\n"
        "v_mfma_f32_16x16x32_bf16 v[120:123], a[128:131], a[244:247], v[120:123]\n"
        "v_cvt_pk_bf16_f32 v82, v84, v85\n"
        "v_cvt_pk_bf16_f32 v83, v86, v87\n"
        "v_mfma_f32_16x16x32_bf16 v[120:123], a[132:135], a[248:251], v[120:123]\n"
        "v_mul_f32 v96, v124, v96\n"
        "v_mul_f32 v97, v124, v97\n"
        "v_mfma_f32_16x16x32_bf16 v[120:123], a[136:139], a[216:219], v[120:123]\n"
        "v_mul_f32 v98, v124, v98\n"
        "v_mul_f32 v99, v124, v99\n"
        "v_mfma_f32_16x16x32_bf16 v[120:123], a[140:143], a[220:223], v[120:123]\n"
        "v_cvt_pk_bf16_f32 v234, v197, v201\n"
        "v_cvt_pk_bf16_f32 v235, v205, v193\n"
        "v_mfma_f32_16x16x32_bf16 v[112:115], a[20:23], a[220:223], 0\n"
        "buffer_store_dwordx2 v[234:235], v250, s[16:19], s39 offen offset:384 sc1\n"
        "v_cvt_pk_bf16_f32 v154, v116, v117\n"
        "v_mfma_f32_16x16x32_bf16 v[112:115], a[16:19], a[216:219], v[112:115]\n"
        "v_cvt_pk_bf16_f32 v155, v118, v119\n"
        "v_cvt_pk_bf16_f32 v96, v96, v97\n"
        "v_mfma_f32_16x16x32_bf16 v[112:115], a[12:15], a[248:251], v[112:115]\n"
        "ds_write_b32 v245, v96 offset:4864\n"
        "v_mfma_f32_16x16x32_bf16 v[112:115], a[8:11], a[244:247], v[112:115]\n"
        "v_cvt_pk_bf16_f32 v97, v98, v99\n"
        "v_max3_u32 v236, v236, v124, v125\n"
        "v_mfma_f32_16x16x32_bf16 v[112:115], a[4:7], a[240:243], v[112:115]\n"
        "v_cvt_pk_bf16_f32 v232, v198, v202\n"
        "v_cvt_pk_bf16_f32 v233, v206, v194\n"
        "v_mfma_f32_16x16x32_bf16 v[112:115], a[0:3], a[236:239], v[112:115]\n"
        "buffer_store_dwordx2 v[232:233], v250, s[16:19], s39 offen offset:768 sc1\n"
        "v_cmp_lt_u32 vcc, s24, v236\n"
        "v_mfma_f32_16x16x32_bf16 v[116:119], a[24:27], a[236:239], 0\n"
        "v_cvt_pk_bf16_f32 v168, v120, v121\n"
        "v_cvt_pk_bf16_f32 v169, v122, v123\n"
        "v_mfma_f32_16x16x32_bf16 v[116:119], a[28:31], a[240:243], v[116:119]\n"
        "s_cmp_lg_u64 vcc, 0\n"
        "s_addc_u32 s25, s25, s25\n"
        "v_cvt_pk_bf16_f32 v234, v199, v203\n"
        "v_mfma_f32_16x16x32_bf16 v[116:119], a[32:35], a[244:247], v[116:119]\n"
        "v_cvt_pk_bf16_f32 v235, v207, v195\n"
        "v_cvt_pk_bf16_f32 v156, v112, v113\n"
        "v_mfma_f32_16x16x32_bf16 v[116:119], a[36:39], a[248:251], v[116:119]\n"
        "buffer_store_dwordx2 v[234:235], v250, s[16:19], s39 offen offset:1152 sc1\n"
        "v_cvt_pk_bf16_f32 v157, v114, v115\n"
        "v_mfma_f32_16x16x32_bf16 v[116:119], a[40:43], a[216:219], v[116:119]\n"
        "v_cvt_pk_bf16_f32 v232, v212, v220\n"
        "v_cvt_pk_bf16_f32 v234, v213, v221\n"
        "v_mfma_f32_16x16x32_bf16 v[116:119], a[44:47], a[220:223], v[116:119]\n"
        "ds_write_b32 v245, v102 offset:2048\n"
        "v_mfma_f32_16x16x32_bf16 v[120:123], a[68:71], a[220:223], 0\n"
        "v_xor_b32 v251, 0x2000, v251\n"
        "v_xor_b32 v252, 0x2000, v252\n"
        "v_mfma_f32_16x16x32_bf16 v[120:123], a[64:67], a[216:219], v[120:123]\n"
        "buffer_store_dword v232, v238, s[16:19], s38 offen offset:0 sc1\n"
        "v_mfma_f32_16x16x32_bf16 v[120:123], a[60:63], a[248:251], v[120:123]\n"
        "ds_read_b128 a[224:227], v240 offset:1536\n"
        "v_cvt_pk_bf16_f32 v158, v116, v117\n"
        "v_mfma_f32_16x16x32_bf16 v[120:123], a[56:59], a[244:247], v[120:123]\n"
        "ds_read_b128 a[228:231], v241 offset:1536\n"
        "v_cvt_pk_bf16_f32 v159, v118, v119\n"
        "v_mfma_f32_16x16x32_bf16 v[120:123], a[52:55], a[240:243], v[120:123]\n"
        "ds_read_b128 a[232:235], v240 offset:1664\n"
        "v_cvt_pk_bf16_f32 v232, v214, v222\n"
        "v_mfma_f32_16x16x32_bf16 v[120:123], a[48:51], a[236:239], v[120:123]\n"
        "buffer_store_dword v234, v238, s[16:19], s38 offen offset:384 sc1\n"
        "v_mfma_f32_16x16x32_bf16 v[112:115], a[236:239], a[144:147], 0\n"
        "v_add_u32 v247, s45, v247\n"
        "v_cvt_pk_bf16_f32 v234, v215, v223\n"
        "v_mfma_f32_16x16x32_bf16 v[112:115], a[240:243], a[148:151], v[112:115]\n"
        "ds_write_b32 v245, v103 offset:3072\n"
        "v_mfma_f32_16x16x32_bf16 v[112:115], a[244:247], a[152:155], v[112:115]\n"
        "s_nop 0\n"
        "v_cvt_pk_bf16_f32 v160, v120, v121\n"
        "v_cvt_pk_bf16_f32 v161, v122, v123\n"
        "v_mfma_f32_16x16x32_bf16 v[112:115], a[248:251], a[156:159], v[112:115]\n"
        "v_cvt_pk_bf16_f32 v166, v120, v121\n"
        "v_cvt_pk_bf16_f32 v167, v122, v123\n"
        "v_mfma_f32_16x16x32_bf16 v[112:115], a[216:219], a[160:163], v[112:115]\n"
        "buffer_store_dword v232, v238, s[16:19], s38 offen offset:768 sc1\n"
        "v_mfma_f32_16x16x32_bf16 v[112:115], a[220:223], a[164:167], v[112:115]\n"
        "ds_write_b32 v245, v97 offset:5888\n"
        "v_mfma_f32_16x16x32_bf16 v[116:119], a[220:223], a[188:191], 0\n"
        "v_cvt_pk_bf16_f32 v232, v216, v224\n"
        "s_nop 0\n"
        "v_mfma_f32_16x16x32_bf16 v[116:119], a[216:219], a[184:187], v[116:119]\n"
        "s_nop 2\n"
        "v_mfma_f32_16x16x32_bf16 v[116:119], a[248:251], a[180:183], v[116:119]\n"
        ".long 0xbec200ff\n"
        "46:\n"
        "s_branch 61b\n"
        "buffer_store_dword v234, v238, s[16:19], s38 offen offset:1152 sc1\n"
        "v_mfma_f32_16x16x32_bf16 v[116:119], a[244:247], a[176:179], v[116:119]\n"
        "v_cvt_pk_bf16_f32 v128, v112, v113\n"
        "v_cvt_pk_bf16_f32 v129, v114, v115\n"
        "v_mfma_f32_16x16x32_bf16 v[116:119], a[240:243], a[172:175], v[116:119]\n"
        "ds_write_b32 v245, v64 offset:256\n"
        "v_mfma_f32_16x16x32_bf16 v[116:119], a[236:239], a[168:171], v[116:119]\n"
        "v_cvt_pk_bf16_f32 v234, v217, v225\n"
        "s_nop 0\n"
        "v_mfma_f32_16x16x32_bf16 v[120:123], a[236:239], a[192:195], 0\n"
        "ds_read_b128 a[236:239], v241 offset:1664\n"
        "s_nop 0\n"
        "v_mfma_f32_16x16x32_bf16 v[120:123], a[240:243], a[196:199], v[120:123]\n"
        "buffer_store_dword v232, v238, s[16:19], s39 offen offset:0 sc1\n"
        "v_mfma_f32_16x16x32_bf16 v[120:123], a[244:247], a[200:203], v[120:123]\n"
        "ds_read_b128 a[240:243], v240 offset:1792\n"
        "ds_read_b128 a[244:247], v241 offset:1792\n"
        "v_mfma_f32_16x16x32_bf16 v[120:123], a[248:251], a[204:207], v[120:123]\n"
        "v_cvt_pk_bf16_f32 v136, v116, v117\n"
        "v_cvt_pk_bf16_f32 v137, v118, v119\n"
        "v_mfma_f32_16x16x32_bf16 v[120:123], a[216:219], a[208:211], v[120:123]\n"
        "v_cvt_pk_bf16_f32 v232, v218, v226\n"
        "v_mfma_f32_16x16x32_bf16 v[120:123], a[220:223], a[212:215], v[120:123]\n"
        "s_waitcnt lgkmcnt(8)\n"
        "buffer_store_dword v234, v238, s[16:19], s39 offen offset:384 sc1\n"
        "v_mfma_f32_16x16x32_bf16 v[112:115], a[72:75], a[224:227], 0\n"
        "s_waitcnt lgkmcnt(7)\n"
        "s_nop 1\n"
        "v_mfma_f32_16x16x32_bf16 v[112:115], a[76:79], a[228:231], v[112:115]\n"
        "s_waitcnt lgkmcnt(6)\n"
        "v_cvt_pk_bf16_f32 v234, v219, v227\n"
        "s_nop 0\n"
        "v_mfma_f32_16x16x32_bf16 v[112:115], a[80:83], a[232:235], v[112:115]\n"
        "s_waitcnt lgkmcnt(2)\n"
        "v_cvt_pk_bf16_f32 v144, v120, v121\n"
        "v_cvt_pk_bf16_f32 v145, v122, v123\n"
        "v_mfma_f32_16x16x32_bf16 v[112:115], a[84:87], a[236:239], v[112:115]\n"
        "s_waitcnt lgkmcnt(1)\n"
        "ds_write_b32 v245, v65 offset:1280\n"
        "v_mfma_f32_16x16x32_bf16 v[112:115], a[88:91], a[240:243], v[112:115]\n"
        "s_waitcnt lgkmcnt(1)\n"
        "buffer_store_dword v232, v238, s[16:19], s39 offen offset:768 sc1\n"
        "v_mfma_f32_16x16x32_bf16 v[112:115], a[92:95], a[244:247], v[112:115]\n"
        "s_nop 2\n"
        "v_mfma_f32_16x16x32_bf16 v[116:119], a[116:119], a[244:247], 0\n"
        "s_nop 2\n"
        "v_mfma_f32_16x16x32_bf16 v[116:119], a[112:115], a[240:243], v[116:119]\n"
        "v_cvt_pk_bf16_f32 v172, v112, v113\n"
        "v_cvt_pk_bf16_f32 v173, v114, v115\n"
        "v_mfma_f32_16x16x32_bf16 v[116:119], a[108:111], a[236:239], v[116:119]\n"
        "buffer_store_dword v234, v238, s[16:19], s39 offen offset:1152 sc1\n"
        "v_mfma_f32_16x16x32_bf16 v[116:119], a[104:107], a[232:235], v[116:119]\n"
        "ds_write_b32 v245, v66 offset:2304\n"
        "v_mfma_f32_16x16x32_bf16 v[116:119], a[100:103], a[228:231], v[116:119]\n"
        "v_mfma_f32_16x16x32_bf16 v[116:119], a[96:99], a[224:227], v[116:119]\n"
        "s_mov_b32 m0, s48\n"
        "s_nop 1\n"
        "v_mfma_f32_16x16x32_bf16 v[120:123], a[120:123], a[224:227], 0\n"
        "ds_read_b128 v[162:165], v251 offset:0\n"
        "s_nop 0\n"
        "v_mfma_f32_16x16x32_bf16 v[120:123], a[124:127], a[228:231], v[120:123]\n"
        "buffer_load_dwordx4 v249, s[12:15], s36 offen offset:0 lds\n"
        "v_mfma_f32_16x16x32_bf16 v[120:123], a[128:131], a[232:235], v[120:123]\n"
        "v_cvt_pk_bf16_f32 v174, v116, v117\n"
        "v_cvt_pk_bf16_f32 v175, v118, v119\n"
        "v_mfma_f32_16x16x32_bf16 v[120:123], a[132:135], a[236:239], v[120:123]\n"
        "ds_write_b32 v245, v67 offset:3328\n"
        "v_mfma_f32_16x16x32_bf16 v[120:123], a[136:139], a[240:243], v[120:123]\n"
        "ds_write_b32 v245, v72 offset:512\n"
        "v_mfma_f32_16x16x32_bf16 v[120:123], a[140:143], a[244:247], v[120:123]\n"
        "s_nop 2\n"
        "v_mfma_f32_16x16x32_bf16 v[112:115], a[20:23], a[244:247], 0\n"
        "s_nop 2\n"
        "v_mfma_f32_16x16x32_bf16 v[112:115], a[16:19], a[240:243], v[112:115]\n"
        "v_cvt_pk_bf16_f32 v190, v120, v121\n"
        "v_cvt_pk_bf16_f32 v191, v122, v123\n"
        "v_mfma_f32_16x16x32_bf16 v[112:115], a[12:15], a[236:239], v[112:115]\n"
        "ds_write_b32 v245, v73 offset:1536\n"
        "v_mfma_f32_16x16x32_bf16 v[112:115], a[8:11], a[232:235], v[112:115]\n"
        "ds_write_b32 v245, v74 offset:2560\n"
        "v_mfma_f32_16x16x32_bf16 v[112:115], a[4:7], a[228:231], v[112:115]\n"
        "ds_read_b128 v[182:185], v251 offset:512\n"
        "v_mfma_f32_16x16x32_bf16 v[112:115], a[0:3], a[224:227], v[112:115]\n"
        "s_nop 2\n"
        "v_mfma_f32_16x16x32_bf16 v[116:119], a[24:27], a[224:227], 0\n"
        "s_nop 2\n"
        "v_mfma_f32_16x16x32_bf16 v[116:119], a[28:31], a[228:231], v[116:119]\n"
        "v_cvt_pk_bf16_f32 v176, v112, v113\n"
        "v_cvt_pk_bf16_f32 v177, v114, v115\n"
        "v_mfma_f32_16x16x32_bf16 v[116:119], a[32:35], a[232:235], v[116:119]\n"
        "ds_write_b32 v245, v75 offset:3584\n"
        "v_mfma_f32_16x16x32_bf16 v[116:119], a[36:39], a[236:239], v[116:119]\n"
        "buffer_load_dwordx4 v253, s[12:15], s36 offen offset:1024 lds\n"
        "v_mfma_f32_16x16x32_bf16 v[116:119], a[40:43], a[240:243], v[116:119]\n"
        "ds_write_b32 v245, v80 offset:768\n"
        "v_mfma_f32_16x16x32_bf16 v[116:119], a[44:47], a[244:247], v[116:119]\n"
        "s_nop 2\n"
        "v_mfma_f32_16x16x32_bf16 v[120:123], a[68:71], a[244:247], 0\n"
        "s_nop 2\n"
        "v_mfma_f32_16x16x32_bf16 v[120:123], a[64:67], a[240:243], v[120:123]\n"
        "v_cvt_pk_bf16_f32 v178, v116, v117\n"
        "v_cvt_pk_bf16_f32 v179, v118, v119\n"
        "v_mfma_f32_16x16x32_bf16 v[120:123], a[60:63], a[236:239], v[120:123]\n"
        "ds_write_b32 v245, v81 offset:1792\n"
        "v_mfma_f32_16x16x32_bf16 v[120:123], a[56:59], a[232:235], v[120:123]\n"
        "ds_write_b32 v245, v82 offset:2816\n"
        "v_mfma_f32_16x16x32_bf16 v[120:123], a[52:55], a[228:231], v[120:123]\n"
        "ds_write_b32 v245, v83 offset:3840\n"
        "v_mfma_f32_16x16x32_bf16 v[120:123], a[48:51], a[224:227], v[120:123]\n"
        "s_nop 2\n"
        "v_mfma_f32_16x16x32_bf16 v[112:115], a[224:227], a[144:147], 0\n"
        "s_nop 2\n"
        "v_mfma_f32_16x16x32_bf16 v[112:115], a[228:231], a[148:151], v[112:115]\n"
        "v_cvt_pk_bf16_f32 v180, v120, v121\n"
        "v_cvt_pk_bf16_f32 v181, v122, v123\n"
        "v_mfma_f32_16x16x32_bf16 v[112:115], a[232:235], a[152:155], v[112:115]\n"
        "v_cvt_pk_bf16_f32 v186, v120, v121\n"
        "v_cvt_pk_bf16_f32 v187, v122, v123\n"
        "v_mfma_f32_16x16x32_bf16 v[112:115], a[236:239], a[156:159], v[112:115]\n"
        "s_add_u32 m0, s41, 0\n"
        "v_mfma_f32_16x16x32_bf16 v[112:115], a[240:243], a[160:163], v[112:115]\n"
        "ds_read_b128 a[248:251], v240 offset:12288\n"
        "v_mfma_f32_16x16x32_bf16 v[112:115], a[244:247], a[164:167], v[112:115]\n"
        "buffer_load_dwordx4 v242, s[8:11], s47 offen lds\n"
        "v_mfma_f32_16x16x32_bf16 v[96:99], v[152:155], v[156:159], v[0:3]\n"
        "s_waitcnt lgkmcnt(11)\n"
        "ds_read_b128 a[216:219], v241 offset:12288\n"
        "s_nop 0\n"
        "v_mfma_f32_16x16x32_bf16 v[96:99], v[168:171], v[160:163], v[96:99]\n"
        "ds_read_b128 a[220:223], v240 offset:12416\n"
        "s_nop 0\n"
        "v_mfma_f32_16x16x32_bf16 v[100:103], v[172:175], v[156:159], v[4:7]\n"
        "s_sub_u32 s31, s30, s28\n"
        "v_cvt_pk_bf16_f32 v130, v112, v113\n"
        "v_cvt_pk_bf16_f32 v131, v114, v115\n"
        "v_mfma_f32_16x16x32_bf16 v[100:103], v[188:191], v[164:167], v[100:103]\n"
        "s_sub_u32 s45, s28, s29\n"
        "ds_read_b128 v[162:165], v252 offset:0\n"
        "v_mfma_f32_16x16x32_bf16 v[116:119], a[244:247], a[188:191], 0\n"
        "s_add_u32 s28, s28, s31\n"
        "v_exp_f32 v96, v96\n"
        "v_mfma_f32_16x16x32_bf16 v[116:119], a[240:243], a[184:187], v[116:119]\n"
        "s_mov_b32 s30, s29\n"
        "v_exp_f32 v97, v97\n"
        "v_mfma_f32_16x16x32_bf16 v[116:119], a[236:239], a[180:183], v[116:119]\n"
        "s_add_u32 s29, s29, s45\n"
        "v_exp_f32 v98, v98\n"
        "v_mfma_f32_16x16x32_bf16 v[116:119], a[232:235], a[176:179], v[116:119]\n"
        "v_exp_f32 v100, v100\n"
        "v_mfma_f32_16x16x32_bf16 v[116:119], a[228:231], a[172:175], v[116:119]\n"
        "v_exp_f32 v99, v99\n"
        "v_mfma_f32_16x16x32_bf16 v[116:119], a[224:227], a[168:171], v[116:119]\n"
        "v_exp_f32 v101, v101\n"
        "v_mfma_f32_16x16x32_bf16 v[104:107], v[152:155], v[176:179], v[16:19]\n"
        "s_waitcnt lgkmcnt(9)\n"
        "v_exp_f32 v102, v102\n"
        "v_mfma_f32_16x16x32_bf16 v[104:107], v[168:171], v[180:183], v[104:107]\n"
        "s_add_u32 m0, s41, 1024\n"
        "v_cvt_pk_bf16_f32 v64, v96, v97\n"
        "v_cvt_pk_bf16_f32 v65, v98, v99\n"
        "v_mfma_f32_16x16x32_bf16 v[108:111], v[172:175], v[176:179], v[20:23]\n"
        "v_cvt_pk_bf16_f32 v138, v116, v117\n"
        "v_cvt_pk_bf16_f32 v139, v118, v119\n"
        "v_mfma_f32_16x16x32_bf16 v[108:111], v[188:191], v[184:187], v[108:111]\n"
        "buffer_load_dwordx4 v243, s[8:11], s47 offen lds\n"
        "v_mfma_f32_16x16x32_bf16 v[120:123], a[224:227], a[192:195], 0\n"
        "ds_read_b128 a[224:227], v241 offset:12416\n"
        "v_cvt_pk_bf16_f32 v66, v100, v101\n"
        "v_mfma_f32_16x16x32_bf16 v[120:123], a[228:231], a[196:199], v[120:123]\n"
        "ds_read_b128 a[228:231], v240 offset:12544\n"
        "v_mfma_f32_16x16x32_bf16 v[120:123], a[232:235], a[200:203], v[120:123]\n"
        "ds_read_b128 a[232:235], v241 offset:12544\n"
        "v_mfma_f32_16x16x32_bf16 v[120:123], a[236:239], a[204:207], v[120:123]\n"
        "ds_read_b128 v[202:205], v251 offset:4096\n"
        "v_mfma_f32_16x16x32_bf16 v[120:123], a[240:243], a[208:211], v[120:123]\n"
        "ds_read_b128 v[182:185], v252 offset:512\n"
        "v_mfma_f32_16x16x32_bf16 v[120:123], a[244:247], a[212:215], v[120:123]\n"
        "s_waitcnt lgkmcnt(8)\n"
        "v_exp_f32 v103, v103\n"
        "v_mfma_f32_16x16x32_bf16 v[112:115], a[0:3], a[248:251], 0\n"
        "s_waitcnt lgkmcnt(7)\n"
        "v_cvt_pk_bf16_f32 v67, v102, v103\n"
        "s_nop 0\n"
        "v_mfma_f32_16x16x32_bf16 v[112:115], a[4:7], a[216:219], v[112:115]\n"
        "s_waitcnt lgkmcnt(6)\n"
        "v_exp_f32 v104, v104\n"
        "v_mfma_f32_16x16x32_bf16 v[112:115], a[8:11], a[220:223], v[112:115]\n"
        "s_waitcnt lgkmcnt(4)\n"
        "v_exp_f32 v105, v105\n"
        "v_mfma_f32_16x16x32_bf16 v[112:115], a[12:15], a[224:227], v[112:115]\n"
        "s_waitcnt lgkmcnt(3)\n"
        "v_exp_f32 v106, v106\n"
        "v_mfma_f32_16x16x32_bf16 v[112:115], a[16:19], a[228:231], v[112:115]\n"
        "s_waitcnt lgkmcnt(2)\n"
        "v_cvt_pk_bf16_f32 v72, v104, v105\n"
        "v_cvt_pk_bf16_f32 v146, v120, v121\n"
        "v_mfma_f32_16x16x32_bf16 v[112:115], a[20:23], a[232:235], v[112:115]\n"
        "s_add_u32 m0, s41, 2048\n"
        "v_exp_f32 v108, v108\n"
        "v_mfma_f32_16x16x32_bf16 v[116:119], a[44:47], a[232:235], 0\n"
        "v_cvt_pk_bf16_f32 v147, v122, v123\n"
        "v_add_u32 v245, s45, v245\n"
        "v_mfma_f32_16x16x32_bf16 v[116:119], a[40:43], a[228:231], v[116:119]\n"
        "buffer_load_dwordx4 v244, s[8:11], s47 offen lds\n"
        "v_mfma_f32_16x16x32_bf16 v[116:119], a[36:39], a[224:227], v[116:119]\n"
        "v_exp_f32 v107, v107\n"
        "v_mfma_f32_16x16x32_bf16 v[116:119], a[32:35], a[220:223], v[116:119]\n"
        "v_exp_f32 v109, v109\n"
        "v_mfma_f32_16x16x32_bf16 v[116:119], a[28:31], a[216:219], v[116:119]\n"
        "v_cvt_pk_bf16_f32 v73, v106, v107\n"
        "v_cvt_pk_bf16_f32 v74, v108, v109\n"
        "v_mfma_f32_16x16x32_bf16 v[116:119], a[24:27], a[248:251], v[116:119]\n"
        "v_exp_f32 v110, v110\n"
        "v_mfma_f32_16x16x32_bf16 v[120:123], a[48:51], a[248:251], 0\n"
        "v_exp_f32 v111, v111\n"
        "v_mfma_f32_16x16x32_bf16 v[120:123], a[52:55], a[216:219], v[120:123]\n"
        "v_cvt_pk_bf16_f32 v75, v110, v111\n"
        "v_cvt_pk_bf16_f32 v196, v112, v113\n"
        "v_mfma_f32_16x16x32_bf16 v[120:123], a[56:59], a[220:223], v[120:123]\n"
        "v_cvt_pk_bf16_f32 v197, v114, v115\n"
        "v_cvt_pk_bf16_f32 v198, v116, v117\n"
        "v_mfma_f32_16x16x32_bf16 v[120:123], a[60:63], a[224:227], v[120:123]\n"
        "v_cvt_pk_bf16_f32 v199, v118, v119\n"
        "v_mfma_f32_16x16x32_bf16 v[120:123], a[64:67], a[228:231], v[120:123]\n"
        "v_mfma_f32_16x16x32_bf16 v[120:123], a[68:71], a[232:235], v[120:123]\n"
        "s_nop 2\n"
        "v_mfma_f32_16x16x32_bf16 v[112:115], a[92:95], a[232:235], 0\n"
        "s_nop 2\n"
        "v_mfma_f32_16x16x32_bf16 v[112:115], a[88:91], a[228:231], v[112:115]\n"
        "s_add_u32 m0, s41, 12288\n"
        "v_cvt_pk_bf16_f32 v200, v120, v121\n"
        "v_cvt_pk_bf16_f32 v201, v122, v123\n"
        "v_mfma_f32_16x16x32_bf16 v[112:115], a[84:87], a[224:227], v[112:115]\n"
        "v_cvt_pk_bf16_f32 v206, v120, v121\n"
        "v_cvt_pk_bf16_f32 v207, v122, v123\n"
        "v_mfma_f32_16x16x32_bf16 v[112:115], a[80:83], a[220:223], v[112:115]\n"
        "buffer_load_dwordx4 v242, s[8:11], s35 offen lds\n"
        "v_mfma_f32_16x16x32_bf16 v[112:115], a[76:79], a[216:219], v[112:115]\n"
        "v_mfma_f32_16x16x32_bf16 v[112:115], a[72:75], a[248:251], v[112:115]\n"
        "s_nop 2\n"
        "v_mfma_f32_16x16x32_bf16 v[96:99], v[152:155], v[196:199], v[32:35]\n"
        "s_waitcnt lgkmcnt(1)\n"
        "s_nop 1\n"
        "v_mfma_f32_16x16x32_bf16 v[96:99], v[168:171], v[200:203], v[96:99]\n"
        "s_nop 0\n"
        "v_cvt_pk_bf16_f32 v192, v112, v113\n"
        "v_cvt_pk_bf16_f32 v193, v114, v115\n"
        "v_mfma_f32_16x16x32_bf16 v[100:103], v[172:175], v[196:199], v[36:39]\n"
        "v_mfma_f32_16x16x32_bf16 v[100:103], v[188:191], v[204:207], v[100:103]\n"
        "ds_read_b128 v[202:205], v252 offset:4096\n"
        "v_mfma_f32_16x16x32_bf16 v[116:119], a[96:99], a[248:251], 0\n"
        "s_cmp_eq_u32 s37, 100663296\n"
        "v_exp_f32 v96, v96\n"
        "v_mfma_f32_16x16x32_bf16 v[116:119], a[100:103], a[216:219], v[116:119]\n"
        "s_cselect_b32 s46, 0, s37\n"
        "v_exp_f32 v97, v97\n"
        "v_mfma_f32_16x16x32_bf16 v[116:119], a[104:107], a[220:223], v[116:119]\n"
        "s_add_u32 s38, s46, s43\n"
        "v_exp_f32 v98, v98\n"
        "v_mfma_f32_16x16x32_bf16 v[116:119], a[108:111], a[224:227], v[116:119]\n"
        "s_add_u32 s39, s46, s44\n"
        "v_exp_f32 v100, v100\n"
        "v_mfma_f32_16x16x32_bf16 v[116:119], a[112:115], a[228:231], v[116:119]\n"
        "s_add_u32 s37, s37, 6291456\n"
        "v_exp_f32 v99, v99\n"
        "v_mfma_f32_16x16x32_bf16 v[116:119], a[116:119], a[232:235], v[116:119]\n"
        "s_add_u32 m0, s41, 13312\n"
        "v_exp_f32 v101, v101\n"
        "v_mfma_f32_16x16x32_bf16 v[120:123], a[140:143], a[232:235], 0\n"
        "v_exp_f32 v102, v102\n"
        "v_mfma_f32_16x16x32_bf16 v[120:123], a[136:139], a[228:231], v[120:123]\n"
        "buffer_load_dwordx4 v243, s[8:11], s35 offen lds\n"
        "v_mfma_f32_16x16x32_bf16 v[120:123], a[132:135], a[224:227], v[120:123]\n"
        "v_cvt_pk_bf16_f32 v80, v96, v97\n"
        "v_cvt_pk_bf16_f32 v194, v116, v117\n"
        "v_mfma_f32_16x16x32_bf16 v[120:123], a[128:131], a[220:223], v[120:123]\n"
        "v_cvt_pk_bf16_f32 v195, v118, v119\n"
        "v_cvt_pk_bf16_f32 v81, v98, v99\n"
        "v_mfma_f32_16x16x32_bf16 v[120:123], a[124:127], a[216:219], v[120:123]\n"
        "v_cvt_pk_bf16_f32 v82, v100, v101\n"
        "v_mfma_f32_16x16x32_bf16 v[120:123], a[120:123], a[248:251], v[120:123]\n"
        "v_exp_f32 v103, v103\n"
        "v_mfma_f32_16x16x32_bf16 v[112:115], a[248:251], a[144:147], 0\n"
        "v_cvt_pk_bf16_f32 v83, v102, v103\n"
        "s_nop 0\n"
        "v_mfma_f32_16x16x32_bf16 v[112:115], a[216:219], a[148:151], v[112:115]\n"
        "s_nop 2\n"
        "v_mfma_f32_16x16x32_bf16 v[112:115], a[220:223], a[152:155], v[112:115]\n"
        "ds_read_b128 a[236:239], v240 offset:13824\n"
        "v_cvt_pk_bf16_f32 v208, v120, v121\n"
        "v_mfma_f32_16x16x32_bf16 v[112:115], a[224:227], a[156:159], v[112:115]\n"
        "ds_read_b128 a[240:243], v241 offset:13824\n"
        "v_cvt_pk_bf16_f32 v209, v122, v123\n"
        "v_mfma_f32_16x16x32_bf16 v[112:115], a[228:231], a[160:163], v[112:115]\n"
        "ds_read_b128 a[244:247], v240 offset:13952\n"
        "v_mfma_f32_16x16x32_bf16 v[112:115], a[232:235], a[164:167], v[112:115]\n"
        "s_nop 2\n"
        "v_mfma_f32_16x16x32_bf16 v[104:107], v[192:195], v[156:159], v[8:11]\n"
        "s_nop 1\n"
        "v_mfma_f32_16x16x32_bf16 v[104:107], v[208:211], v[160:163], v[104:107]\n"
        "s_add_u32 m0, s41, 14336\n"
        "v_cvt_pk_bf16_f32 v132, v112, v113\n"
        "v_cvt_pk_bf16_f32 v133, v114, v115\n"
        "v_mfma_f32_16x16x32_bf16 v[116:119], a[232:235], a[188:191], 0\n"
        "v_mfma_f32_16x16x32_bf16 v[116:119], a[228:231], a[184:187], v[116:119]\n"
        "buffer_load_dwordx4 v244, s[8:11], s35 offen lds\n"
        "v_mfma_f32_16x16x32_bf16 v[116:119], a[224:227], a[180:183], v[116:119]\n"
        "v_mfma_f32_16x16x32_bf16 v[116:119], a[220:223], a[176:179], v[116:119]\n"
        "v_exp_f32 v104, v104\n"
        "v_mfma_f32_16x16x32_bf16 v[116:119], a[216:219], a[172:175], v[116:119]\n"
        "v_exp_f32 v105, v105\n"
        "v_mfma_f32_16x16x32_bf16 v[116:119], a[248:251], a[168:171], v[116:119]\n"
        "v_exp_f32 v106, v106\n"
        "v_mfma_f32_16x16x32_bf16 v[108:111], v[192:195], v[176:179], v[24:27]\n"
        "s_waitcnt lgkmcnt(4)\n"
        "v_exp_f32 v107, v107\n"
        "v_mfma_f32_16x16x32_bf16 v[108:111], v[208:211], v[180:183], v[108:111]\n"
        "v_cvt_pk_bf16_f32 v68, v104, v105\n"
        "v_cvt_pk_bf16_f32 v69, v106, v107\n"
        "v_mfma_f32_16x16x32_bf16 v[120:123], a[248:251], a[192:195], 0\n"
        "ds_read_b128 a[248:251], v241 offset:13952\n"
        "v_cvt_pk_bf16_f32 v140, v116, v117\n"
        "v_mfma_f32_16x16x32_bf16 v[120:123], a[216:219], a[196:199], v[120:123]\n"
        "ds_read_b128 a[216:219], v240 offset:14080\n"
        "v_cvt_pk_bf16_f32 v141, v118, v119\n"
        "v_mfma_f32_16x16x32_bf16 v[120:123], a[220:223], a[200:203], v[120:123]\n"
        "ds_read_b128 a[220:223], v241 offset:14080\n"
        "v_mfma_f32_16x16x32_bf16 v[120:123], a[224:227], a[204:207], v[120:123]\n"
        "v_exp_f32 v108, v108\n"
        "v_mfma_f32_16x16x32_bf16 v[120:123], a[228:231], a[208:211], v[120:123]\n"
        "v_exp_f32 v109, v109\n"
        "v_mfma_f32_16x16x32_bf16 v[120:123], a[232:235], a[212:215], v[120:123]\n"
        "v_exp_f32 v110, v110\n"
        "v_mfma_f32_16x16x32_bf16 v[96:99], v[192:195], v[196:199], v[40:43]\n"
        "s_waitcnt lgkmcnt(6)\n"
        "v_exp_f32 v111, v111\n"
        "v_mfma_f32_16x16x32_bf16 v[96:99], v[208:211], v[200:203], v[96:99]\n"
        "s_waitcnt lgkmcnt(5)\n"
        "v_cvt_pk_bf16_f32 v76, v108, v109\n"
        "v_cvt_pk_bf16_f32 v77, v110, v111\n"
        "v_mfma_f32_16x16x32_bf16 v[112:115], a[72:75], a[236:239], 0\n"
        "s_waitcnt lgkmcnt(4)\n"
        "s_nop 0\n"
        "v_cvt_pk_bf16_f32 v148, v120, v121\n"
        "v_mfma_f32_16x16x32_bf16 v[112:115], a[76:79], a[240:243], v[112:115]\n"
        "s_waitcnt lgkmcnt(3)\n"
        "v_cvt_pk_bf16_f32 v149, v122, v123\n"
        "v_mfma_f32_16x16x32_bf16 v[112:115], a[80:83], a[244:247], v[112:115]\n"
        "s_waitcnt lgkmcnt(2)\n"
        "v_exp_f32 v96, v96\n"
        "v_mfma_f32_16x16x32_bf16 v[112:115], a[84:87], a[248:251], v[112:115]\n"
        "s_waitcnt lgkmcnt(1)\n"
        "v_exp_f32 v97, v97\n"
        "v_mfma_f32_16x16x32_bf16 v[112:115], a[88:91], a[216:219], v[112:115]\n"
        "s_waitcnt lgkmcnt(0)\n"
        "v_exp_f32 v98, v98\n"
        "v_mfma_f32_16x16x32_bf16 v[112:115], a[92:95], a[220:223], v[112:115]\n"
        "v_exp_f32 v99, v99\n"
        "v_mfma_f32_16x16x32_bf16 v[116:119], a[116:119], a[220:223], 0\n"
        "v_cvt_pk_bf16_f32 v84, v96, v97\n"
        "v_cvt_pk_bf16_f32 v85, v98, v99\n"
        "v_mfma_f32_16x16x32_bf16 v[116:119], a[112:115], a[216:219], v[116:119]\n"
        "v_add_u32 v240, s31, v240\n"
        "v_add_u32 v241, s31, v241\n"
        "v_mfma_f32_16x16x32_bf16 v[116:119], a[108:111], a[248:251], v[116:119]\n"
        "v_cvt_pk_bf16_f32 v212, v112, v113\n"
        "v_cvt_pk_bf16_f32 v213, v114, v115\n"
        "v_mfma_f32_16x16x32_bf16 v[116:119], a[104:107], a[244:247], v[116:119]\n"
        "v_mfma_f32_16x16x32_bf16 v[116:119], a[100:103], a[240:243], v[116:119]\n"
        "v_mfma_f32_16x16x32_bf16 v[116:119], a[96:99], a[236:239], v[116:119]\n"
        "s_nop 2\n"
        "v_mfma_f32_16x16x32_bf16 v[120:123], a[120:123], a[236:239], 0\n"
        "s_nop 2\n"
        "v_mfma_f32_16x16x32_bf16 v[120:123], a[124:127], a[240:243], v[120:123]\n"
        "s_add_u32 s47, s47, 6291456\n"
        "v_cvt_pk_bf16_f32 v214, v116, v117\n"
        "v_cvt_pk_bf16_f32 v215, v118, v119\n"
        "v_mfma_f32_16x16x32_bf16 v[120:123], a[128:131], a[244:247], v[120:123]\n"
        "s_add_u32 s36, s36, 2097152\n"
        "v_mfma_f32_16x16x32_bf16 v[120:123], a[132:135], a[248:251], v[120:123]\n"
        "s_xor_b32 s48, s48, 8192\n"
        "v_mfma_f32_16x16x32_bf16 v[120:123], a[136:139], a[216:219], v[120:123]\n"
        "v_mfma_f32_16x16x32_bf16 v[120:123], a[140:143], a[220:223], v[120:123]\n"
        "s_nop 2\n"
        "v_mfma_f32_16x16x32_bf16 v[112:115], a[220:223], a[164:167], 0\n"
        "s_nop 2\n"
        "v_mfma_f32_16x16x32_bf16 v[112:115], a[216:219], a[160:163], v[112:115]\n"
        "s_barrier\n"
        "v_cvt_pk_bf16_f32 v230, v120, v121\n"
        "v_cvt_pk_bf16_f32 v231, v122, v123\n"
        "v_mfma_f32_16x16x32_bf16 v[112:115], a[248:251], a[156:159], v[112:115]\n"
        "v_mfma_f32_16x16x32_bf16 v[112:115], a[244:247], a[152:155], v[112:115]\n"
        "v_mfma_f32_16x16x32_bf16 v[112:115], a[240:243], a[148:151], v[112:115]\n"
        "v_mfma_f32_16x16x32_bf16 v[112:115], a[236:239], a[144:147], v[112:115]\n"
        "s_nop 2\n"
        "v_mfma_f32_16x16x32_bf16 v[100:103], v[212:215], v[156:159], v[12:15]\n"
        "s_nop 2\n"
        "v_mfma_f32_16x16x32_bf16 v[100:103], v[228:231], v[164:167], v[100:103]\n"
        "v_cvt_pk_bf16_f32 v134, v112, v113\n"
        "v_cvt_pk_bf16_f32 v135, v114, v115\n"
        "v_mfma_f32_16x16x32_bf16 v[116:119], a[236:239], a[168:171], 0\n"
        "s_sub_u32 s27, s27, 1\n"
        "v_mfma_f32_16x16x32_bf16 v[116:119], a[240:243], a[172:175], v[116:119]\n"
        "s_cmp_lg_u32 s27, 0\n"
        "v_mfma_f32_16x16x32_bf16 v[116:119], a[244:247], a[176:179], v[116:119]\n"
        "s_cbranch_scc1 10b\n"
        "19:\n"
        "v_mfma_f32_16x16x32_bf16 v[116:119], a[248:251], a[180:183], v[116:119]\n"
        "ds_read_b128 a[224:227], v247 offset:0\n"
        "ds_read_b128 a[228:231], v247 offset:4096\n"
        "v_mfma_f32_16x16x32_bf16 v[116:119], a[216:219], a[184:187], v[116:119]\n"
        "ds_read_b128 a[232:235], v247 offset:8192\n"
        "ds_read_b128 v[156:159], v247 offset:12288\n"
        "v_mfma_f32_16x16x32_bf16 v[116:119], a[220:223], a[188:191], v[116:119]\n"
        "ds_read_b128 v[160:163], v247 offset:16384\n"
        "ds_read_b128 v[164:167], v247 offset:20480\n"
        "v_mfma_f32_16x16x32_bf16 v[104:107], v[212:215], v[176:179], v[28:31]\n"
        "s_and_b32 s46, s42, 1\n"
        "v_exp_f32 v100, v100\n"
        "v_mfma_f32_16x16x32_bf16 v[104:107], v[228:231], v[184:187], v[104:107]\n"
        "s_lshl_b32 s31, s46, 8\n"
        "v_exp_f32 v101, v101\n"
        "v_mfma_f32_16x16x32_bf16 v[120:123], a[220:223], a[212:215], 0\n"
        "s_sub_u32 s31, s45, s31\n"
        "v_cvt_pk_bf16_f32 v142, v116, v117\n"
        "v_cvt_pk_bf16_f32 v143, v118, v119\n"
        "v_mfma_f32_16x16x32_bf16 v[120:123], a[216:219], a[208:211], v[120:123]\n"
        "ds_read_b128 a[96:99], v248 offset:32\n"
        "v_cvt_pk_bf16_f32 v70, v100, v101\n"
        "v_mfma_f32_16x16x32_bf16 v[120:123], a[248:251], a[204:207], v[120:123]\n"
        "ds_read_b128 a[100:103], v248 offset:3104\n"
        "v_mov_b32 v241, 0\n"
        "v_mfma_f32_16x16x32_bf16 v[120:123], a[244:247], a[200:203], v[120:123]\n"
        "ds_read_b128 a[104:107], v248 offset:6176\n"
        "v_mfma_f32_16x16x32_bf16 v[120:123], a[240:243], a[196:199], v[120:123]\n"
        "ds_read_b128 a[108:111], v248 offset:9248\n"
        "v_mfma_f32_16x16x32_bf16 v[120:123], a[236:239], a[192:195], v[120:123]\n"
        "ds_read_b128 a[112:115], v248 offset:12320\n"
        "s_nop 0\n"
        "v_mfma_f32_16x16x32_bf16 v[108:111], v[212:215], v[196:199], v[44:47]\n"
        "ds_read_b128 a[116:119], v248 offset:15392\n"
        "s_nop 0\n"
        "v_mfma_f32_16x16x32_bf16 v[108:111], v[228:231], v[204:207], v[108:111]\n"
        "ds_read_b128 a[92:95], v248 offset:15360\n"
        "s_nop 0\n"
        "v_mfma_f32_16x16x32_bf16 v[116:119], a[0:3], a[236:239], 0\n"
        "ds_read_b128 a[88:91], v248 offset:12288\n"
        "v_cvt_pk_bf16_f32 v150, v120, v121\n"
        "v_mfma_f32_16x16x32_bf16 v[116:119], a[4:7], a[240:243], v[116:119]\n"
        "ds_read_b128 a[84:87], v248 offset:9216\n"
        "v_cvt_pk_bf16_f32 v151, v122, v123\n"
        "v_mfma_f32_16x16x32_bf16 v[116:119], a[8:11], a[244:247], v[116:119]\n"
        "ds_read_b128 a[80:83], v248 offset:6144\n"
        "v_mfma_f32_16x16x32_bf16 v[116:119], a[12:15], a[248:251], v[116:119]\n"
        "ds_read_b128 a[76:79], v248 offset:3072\n"
        "v_mfma_f32_16x16x32_bf16 v[116:119], a[16:19], a[216:219], v[116:119]\n"
        "ds_read_b128 a[72:75], v248 offset:0\n"
        "v_mfma_f32_16x16x32_bf16 v[116:119], a[20:23], a[220:223], v[116:119]\n"
        "s_waitcnt lgkmcnt(6)\n"
        "ds_read_b128 a[120:123], v254 offset:0\n"
        "v_mfma_f32_16x16x32_bf16 v[4:7], a[224:227], a[96:99], 0\n"
        "ds_read_b128 a[124:127], v254 offset:3072\n"
        "v_mfma_f32_16x16x32_bf16 v[4:7], a[228:231], a[100:103], v[4:7]\n"
        "ds_read_b128 a[128:131], v254 offset:6144\n"
        "v_mfma_f32_16x16x32_bf16 v[4:7], a[232:235], a[104:107], v[4:7]\n"
        "ds_read_b128 a[132:135], v254 offset:9216\n"
        "s_nop 0\n"
        "v_mfma_f32_16x16x32_bf16 v[4:7], v[156:159], a[108:111], v[4:7]\n"
        "ds_read_b128 a[136:139], v254 offset:12288\n"
        "v_cvt_pk_bf16_f32 v216, v116, v117\n"
        "v_mfma_f32_16x16x32_bf16 v[4:7], v[160:163], a[112:115], v[4:7]\n"
        "ds_read_b128 a[140:143], v254 offset:15360\n"
        "v_cvt_pk_bf16_f32 v217, v118, v119\n"
        "v_mfma_f32_16x16x32_bf16 v[4:7], v[164:167], a[116:119], v[4:7]\n"
        "s_waitcnt lgkmcnt(6)\n"
        "v_exp_f32 v102, v102\n"
        "v_mfma_f32_16x16x32_bf16 v[0:3], v[164:167], a[92:95], 0\n"
        "s_mul_i32 s45, s46, 786432\n"
        "v_mov_b64 v[96:97], 0\n"
        "v_mfma_f32_16x16x32_bf16 v[0:3], v[160:163], a[88:91], v[0:3]\n"
        "s_sub_u32 s37, s37, s45\n"
        "v_exp_f32 v103, v103\n"
        "v_mfma_f32_16x16x32_bf16 v[0:3], v[156:159], a[84:87], v[0:3]\n"
        "s_cmp_eq_u32 s46, 0\n"
        "s_cselect_b32 s43, s43, s44\n"
        "v_cvt_pk_bf16_f32 v71, v102, v103\n"
        "v_mfma_f32_16x16x32_bf16 v[0:3], a[232:235], a[80:83], v[0:3]\n"
        "s_add_u32 s28, s37, 786432\n"
        "v_exp_f32 v104, v104\n"
        "v_mfma_f32_16x16x32_bf16 v[0:3], a[228:231], a[76:79], v[0:3]\n"
        "s_cmp_eq_u32 s28, 100663296\n"
        "s_cselect_b32 s28, 0, s28\n"
        "v_mfma_f32_16x16x32_bf16 v[0:3], a[224:227], a[72:75], v[0:3]\n"
        "s_waitcnt lgkmcnt(0)\n"
        "v_exp_f32 v105, v105\n"
        "v_mfma_f32_16x16x32_bf16 v[8:11], a[224:227], a[120:123], 0\n"
        "s_lshl_b32 s29, s42, 2\n"
        "v_exp_f32 v106, v106\n"
        "v_mfma_f32_16x16x32_bf16 v[8:11], a[228:231], a[124:127], v[8:11]\n"
        "s_add_u32 s47, s37, s43\n"
        "v_exp_f32 v107, v107\n"
        "v_mfma_f32_16x16x32_bf16 v[8:11], a[232:235], a[128:131], v[8:11]\n"
        "s_add_u32 s35, s28, s43\n"
        "v_exp_f32 v108, v108\n"
        "v_mfma_f32_16x16x32_bf16 v[8:11], v[156:159], a[132:135], v[8:11]\n"
        "v_exp_f32 v109, v109\n"
        "v_mfma_f32_16x16x32_bf16 v[8:11], v[160:163], a[136:139], v[8:11]\n"
        "v_exp_f32 v110, v110\n"
        "v_mfma_f32_16x16x32_bf16 v[8:11], v[164:167], a[140:143], v[8:11]\n"
        "v_exp_f32 v111, v111\n"
        "v_smfmac_f32_16x16x64_bf16 v[96:99], a[252:255], v[64:71], v237\n"
        "ds_read_b128 a[164:167], v254 offset:15392\n"
        "v_mfma_f32_16x16x32_bf16 v[100:103], v[132:135], v[68:71], 0\n"
        "ds_read_b128 a[160:163], v254 offset:12320\n"
        "v_cvt_pk_bf16_f32 v78, v104, v105\n"
        "v_mfma_f32_16x16x32_bf16 v[100:103], v[128:131], v[64:67], v[100:103]\n"
        "ds_read_b128 a[156:159], v254 offset:9248\n"
        "v_cvt_pk_bf16_f32 v79, v106, v107\n"
        "v_mfma_f32_16x16x32_bf16 v[104:107], v[136:139], v[64:67], 0\n"
        "ds_read_b128 a[152:155], v254 offset:6176\n"
        "v_cvt_pk_bf16_f32 v86, v108, v109\n"
        "v_mfma_f32_16x16x32_bf16 v[104:107], v[140:143], v[68:71], v[104:107]\n"
        "ds_read_b128 a[148:151], v254 offset:3104\n"
        "v_cvt_pk_bf16_f32 v87, v110, v111\n"
        "v_mfma_f32_16x16x32_bf16 v[108:111], v[148:151], v[68:71], 0\n"
        "ds_read_b128 a[144:147], v254 offset:32\n"
        "v_add_f32 v97, v96, v97\n"
        "v_mfma_f32_16x16x32_bf16 v[108:111], v[144:147], v[64:67], v[108:111]\n"
        "v_rcp_f32 v96, v97\n"
        "v_mfma_f32_16x16x32_bf16 v[120:123], a[44:47], a[220:223], 0\n"
        "v_mul_f32 v100, v96, v100\n"
        "v_mul_f32 v101, v96, v101\n"
        "v_mfma_f32_16x16x32_bf16 v[120:123], a[40:43], a[216:219], v[120:123]\n"
        "ds_read_b128 v[176:179], v247 offset:256\n"
        "v_mul_f32 v102, v96, v102\n"
        "v_mfma_f32_16x16x32_bf16 v[120:123], a[36:39], a[248:251], v[120:123]\n"
        "ds_read_b128 v[180:183], v247 offset:4352\n"
        "v_mul_f32 v103, v96, v103\n"
        "v_mfma_f32_16x16x32_bf16 v[120:123], a[32:35], a[244:247], v[120:123]\n"
        "ds_read_b128 v[184:187], v247 offset:8448\n"
        "v_cvt_pk_bf16_f32 v100, v100, v101\n"
        "v_mfma_f32_16x16x32_bf16 v[120:123], a[28:31], a[240:243], v[120:123]\n"
        "ds_read_b128 v[196:199], v247 offset:12544\n"
        "v_cvt_pk_bf16_f32 v232, v0, v4\n"
        "v_mfma_f32_16x16x32_bf16 v[120:123], a[24:27], a[236:239], v[120:123]\n"
        "s_waitcnt lgkmcnt(4)\n"
        "ds_read_b128 v[200:203], v247 offset:16640\n"
        "v_cvt_pk_bf16_f32 v101, v102, v103\n"
        "v_mfma_f32_16x16x32_bf16 v[12:15], v[164:167], a[164:167], 0\n"
        "ds_read_b128 v[204:207], v247 offset:20736\n"
        "v_mov_b64 v[124:125], 0\n"
        "v_mfma_f32_16x16x32_bf16 v[12:15], v[160:163], a[160:163], v[12:15]\n"
        "ds_write_b32 v245, v100 offset:0\n"
        "v_mfma_f32_16x16x32_bf16 v[12:15], v[156:159], a[156:159], v[12:15]\n"
        "ds_write_b32 v245, v101 offset:1024\n"
        "v_mfma_f32_16x16x32_bf16 v[12:15], a[232:235], a[152:155], v[12:15]\n"
        "v_cvt_pk_bf16_f32 v218, v120, v121\n"
        "v_cvt_pk_bf16_f32 v219, v122, v123\n"
        "v_mfma_f32_16x16x32_bf16 v[12:15], a[228:231], a[148:151], v[12:15]\n"
        "v_cvt_pk_bf16_f32 v234, v1, v5\n"
        "v_mul_f32 v104, v96, v104\n"
        "v_mfma_f32_16x16x32_bf16 v[12:15], a[224:227], a[144:147], v[12:15]\n"
        "v_mul_f32 v105, v96, v105\n"
        "v_mul_f32 v106, v96, v106\n"
        "v_mfma_f32_16x16x32_bf16 v[116:119], a[48:51], a[236:239], 0\n"
        "v_mul_f32 v107, v96, v107\n"
        "v_cvt_pk_bf16_f32 v242, v2, v6\n"
        "v_mfma_f32_16x16x32_bf16 v[116:119], a[52:55], a[240:243], v[116:119]\n"
        "ds_read_b128 v[222:225], v251 offset:4608\n"
        "v_cvt_pk_bf16_f32 v102, v104, v105\n"
        "v_mfma_f32_16x16x32_bf16 v[116:119], a[56:59], a[244:247], v[116:119]\n"
        "v_cvt_pk_bf16_f32 v233, v8, v12\n"
        "v_cvt_pk_bf16_f32 v235, v9, v13\n"
        "v_mfma_f32_16x16x32_bf16 v[116:119], a[60:63], a[248:251], v[116:119]\n"
        "buffer_store_dwordx2 v[232:233], v250, s[16:19], s38 offen offset:0 sc1\n"
        "v_cvt_pk_bf16_f32 v243, v10, v14\n"
        "v_mfma_f32_16x16x32_bf16 v[116:119], a[64:67], a[216:219], v[116:119]\n"
        "v_cvt_pk_bf16_f32 v103, v106, v107\n"
        "v_mul_f32 v108, v96, v108\n"
        "v_mfma_f32_16x16x32_bf16 v[116:119], a[68:71], a[220:223], v[116:119]\n"
        "ds_write2st64_b32 v245, v102, v103 offset0:8 offset1:12\n"
        "v_smfmac_f32_16x16x64_bf16 v[124:127], a[252:255], v[72:79], v237\n"
        "ds_read_b128 a[212:215], v254 offset:15376\n"
        "v_mfma_f32_16x16x32_bf16 v[64:67], v[132:135], v[76:79], 0\n"
        "buffer_store_dwordx2 v[234:235], v250, s[16:19], s38 offen offset:384 sc1\n"
        "ds_read_b128 a[208:211], v254 offset:12304\n"
        "v_mfma_f32_16x16x32_bf16 v[64:67], v[128:131], v[72:75], v[64:67]\n"
        ".long 0xbec200ff\n"
        "47:\n"
        "s_branch 61b\n"
        "ds_read_b128 a[204:207], v254 offset:9232\n"
        "v_mul_f32 v109, v96, v109\n"
        "v_mfma_f32_16x16x32_bf16 v[68:71], v[136:139], v[72:75], 0\n"
        "buffer_store_dwordx2 v[242:243], v250, s[16:19], s38 offen offset:768 sc1\n"
        "ds_read_b128 a[200:203], v254 offset:6160\n"
        "v_mfma_f32_16x16x32_bf16 v[68:71], v[140:143], v[76:79], v[68:71]\n"
        "ds_read_b128 a[196:199], v254 offset:3088\n"
        "v_mul_f32 v110, v96, v110\n"
        "v_mfma_f32_16x16x32_bf16 v[112:115], v[148:151], v[76:79], 0\n"
        "ds_read_b128 a[192:195], v254 offset:16\n"
        "v_mul_f32 v111, v96, v111\n"
        "v_mfma_f32_16x16x32_bf16 v[112:115], v[144:147], v[72:75], v[112:115]\n"
        "s_waitcnt lgkmcnt(7)\n"
        "ds_read_b128 a[168:171], v248 offset:16\n"
        "v_cvt_pk_bf16_f32 v232, v3, v7\n"
        "v_mfma_f32_16x16x32_bf16 v[24:27], v[176:179], a[72:75], 0\n"
        "ds_read_b128 a[172:175], v248 offset:3088\n"
        "v_cvt_pk_bf16_f32 v233, v11, v15\n"
        "v_mfma_f32_16x16x32_bf16 v[24:27], v[180:183], a[76:79], v[24:27]\n"
        "buffer_store_dwordx2 v[232:233], v250, s[16:19], s38 offen offset:1152 sc1\n"
        "ds_read_b128 a[176:179], v248 offset:6160\n"
        "v_mfma_f32_16x16x32_bf16 v[24:27], v[184:187], a[80:83], v[24:27]\n"
        "ds_read_b128 a[180:183], v248 offset:9232\n"
        "v_cvt_pk_bf16_f32 v108, v108, v109\n"
        "v_mfma_f32_16x16x32_bf16 v[24:27], v[196:199], a[84:87], v[24:27]\n"
        "ds_read_b128 a[184:187], v248 offset:12304\n"
        "v_cvt_pk_bf16_f32 v109, v110, v111\n"
        "v_mfma_f32_16x16x32_bf16 v[24:27], v[200:203], a[88:91], v[24:27]\n"
        "ds_write2st64_b32 v245, v108, v109 offset0:16 offset1:20\n"
        "v_mfma_f32_16x16x32_bf16 v[24:27], v[204:207], a[92:95], v[24:27]\n"
        "s_waitcnt lgkmcnt(6)\n"
        "ds_read_b128 a[188:191], v248 offset:15376\n"
        "v_add_f32 v125, v124, v125\n"
        "v_mfma_f32_16x16x32_bf16 v[20:23], v[164:167], a[212:215], 0\n"
        "v_rcp_f32 v124, v125\n"
        "v_mfma_f32_16x16x32_bf16 v[20:23], v[160:163], a[208:211], v[20:23]\n"
        "v_add_u32 v240, s31, v247\n"
        "v_max_u32 v236, v96, v97\n"
        "v_mfma_f32_16x16x32_bf16 v[20:23], v[156:159], a[204:207], v[20:23]\n"
        "v_cvt_pk_bf16_f32 v220, v116, v117\n"
        "v_cvt_pk_bf16_f32 v221, v118, v119\n"
        "v_mfma_f32_16x16x32_bf16 v[20:23], a[232:235], a[200:203], v[20:23]\n"
        "v_cvt_pk_bf16_f32 v226, v116, v117\n"
        "v_cvt_pk_bf16_f32 v227, v118, v119\n"
        "v_mfma_f32_16x16x32_bf16 v[20:23], a[228:231], a[196:199], v[20:23]\n"
        "v_mul_f32 v64, v124, v64\n"
        "v_mul_f32 v65, v124, v65\n"
        "v_mfma_f32_16x16x32_bf16 v[20:23], a[224:227], a[192:195], v[20:23]\n"
        "s_waitcnt lgkmcnt(2)\n"
        "v_mul_f32 v66, v124, v66\n"
        "v_mul_f32 v67, v124, v67\n"
        "v_mfma_f32_16x16x32_bf16 v[16:19], a[224:227], a[168:171], 0\n"
        "v_cvt_pk_bf16_f32 v64, v64, v65\n"
        "v_cvt_pk_bf16_f32 v65, v66, v67\n"
        "v_mfma_f32_16x16x32_bf16 v[16:19], a[228:231], a[172:175], v[16:19]\n"
        "ds_write2st64_b32 v245, v64, v65 offset0:1 offset1:5\n"
        "v_mfma_f32_16x16x32_bf16 v[16:19], a[232:235], a[176:179], v[16:19]\n"
        "v_mul_f32 v68, v124, v68\n"
        "v_mul_f32 v69, v124, v69\n"
        "v_mfma_f32_16x16x32_bf16 v[16:19], v[156:159], a[180:183], v[16:19]\n"
        "v_mul_f32 v70, v124, v70\n"
        "v_mul_f32 v71, v124, v71\n"
        "v_mfma_f32_16x16x32_bf16 v[16:19], v[160:163], a[184:187], v[16:19]\n"
        "s_waitcnt lgkmcnt(1)\n"
        "v_cvt_pk_bf16_f32 v66, v68, v69\n"
        "v_cvt_pk_bf16_f32 v67, v70, v71\n"
        "v_mfma_f32_16x16x32_bf16 v[16:19], v[164:167], a[188:191], v[16:19]\n"
        "ds_write2st64_b32 v245, v66, v67 offset0:9 offset1:13\n"
        "v_mfma_f32_16x16x32_bf16 v[0:3], v[152:155], v[216:219], v[48:51]\n"
        "v_mul_f32 v112, v124, v112\n"
        "v_mul_f32 v113, v124, v113\n"
        "v_mfma_f32_16x16x32_bf16 v[0:3], v[168:171], v[220:223], v[0:3]\n"
        "v_mul_f32 v114, v124, v114\n"
        "v_mul_f32 v115, v124, v115\n"
        "v_mfma_f32_16x16x32_bf16 v[4:7], v[172:175], v[216:219], v[52:55]\n"
        "v_cvt_pk_bf16_f32 v112, v112, v113\n"
        "v_cvt_pk_bf16_f32 v113, v114, v115\n"
        "v_mfma_f32_16x16x32_bf16 v[4:7], v[188:191], v[224:227], v[4:7]\n"
        "ds_write_b32 v245, v112 offset:4352\n"
        "v_mfma_f32_16x16x32_bf16 v[36:39], v[204:207], a[164:167], 0\n"
        "ds_write_b32 v245, v113 offset:5376\n"
        "v_mfma_f32_16x16x32_bf16 v[36:39], v[200:203], a[160:163], v[36:39]\n"
        "ds_read_b128 v[222:225], v252 offset:4608\n"
        "v_max3_u32 v236, v236, v124, v125\n"
        "v_mfma_f32_16x16x32_bf16 v[36:39], v[196:199], a[156:159], v[36:39]\n"
        "v_cvt_pk_bf16_f32 v244, v16, v20\n"
        "v_cvt_pk_bf16_f32 v239, v17, v21\n"
        "v_mfma_f32_16x16x32_bf16 v[36:39], v[184:187], a[152:155], v[36:39]\n"
        "buffer_store_dword v244, v238, s[16:19], s38 offen offset:0 sc1\n"
        "v_mfma_f32_16x16x32_bf16 v[36:39], v[180:183], a[148:151], v[36:39]\n"
        "buffer_store_dword v239, v238, s[16:19], s38 offen offset:384 sc1\n"
        "v_mfma_f32_16x16x32_bf16 v[36:39], v[176:179], a[144:147], v[36:39]\n"
        "v_exp_f32 v0, v0\n"
        "v_mfma_f32_16x16x32_bf16 v[32:35], v[176:179], a[120:123], 0\n"
        "v_exp_f32 v1, v1\n"
        "v_mfma_f32_16x16x32_bf16 v[32:35], v[180:183], a[124:127], v[32:35]\n"
        "v_exp_f32 v2, v2\n"
        "v_mfma_f32_16x16x32_bf16 v[32:35], v[184:187], a[128:131], v[32:35]\n"
        "v_cvt_pk_bf16_f32 v88, v0, v1\n"
        "v_cvt_pk_bf16_f32 v255, v18, v22\n"
        "v_mfma_f32_16x16x32_bf16 v[32:35], v[196:199], a[132:135], v[32:35]\n"
        "buffer_store_dword v255, v238, s[16:19], s38 offen offset:768 sc1\n"
        "v_mfma_f32_16x16x32_bf16 v[32:35], v[200:203], a[136:139], v[32:35]\n"
        "v_exp_f32 v4, v4\n"
        "v_mfma_f32_16x16x32_bf16 v[32:35], v[204:207], a[140:143], v[32:35]\n"
        "v_exp_f32 v3, v3\n"
        "v_mfma_f32_16x16x32_bf16 v[28:31], v[204:207], a[116:119], 0\n"
        "v_exp_f32 v5, v5\n"
        "v_mfma_f32_16x16x32_bf16 v[28:31], v[200:203], a[112:115], v[28:31]\n"
        "v_cvt_pk_bf16_f32 v89, v2, v3\n"
        "v_cvt_pk_bf16_f32 v90, v4, v5\n"
        "v_mfma_f32_16x16x32_bf16 v[28:31], v[196:199], a[108:111], v[28:31]\n"
        "v_exp_f32 v6, v6\n"
        "v_mfma_f32_16x16x32_bf16 v[28:31], v[184:187], a[104:107], v[28:31]\n"
        "v_exp_f32 v7, v7\n"
        "v_mfma_f32_16x16x32_bf16 v[28:31], v[180:183], a[100:103], v[28:31]\n"
        "v_cvt_pk_bf16_f32 v91, v6, v7\n"
        "v_cvt_pk_bf16_f32 v244, v19, v23\n"
        "v_mfma_f32_16x16x32_bf16 v[28:31], v[176:179], a[96:99], v[28:31]\n"
        "buffer_store_dword v244, v238, s[16:19], s38 offen offset:1152 sc1\n"
        "v_mfma_f32_16x16x32_bf16 v[8:11], v[192:195], v[216:219], v[56:59]\n"
        "s_waitcnt lgkmcnt(0)\n"
        "v_mov_b64 v[96:97], 0\n"
        "v_cvt_pk_bf16_f32 v235, v32, v36\n"
        "v_mfma_f32_16x16x32_bf16 v[8:11], v[208:211], v[220:223], v[8:11]\n"
        "v_cvt_pk_bf16_f32 v243, v33, v37\n"
        "v_cvt_pk_bf16_f32 v233, v34, v38\n"
        "v_mfma_f32_16x16x32_bf16 v[40:43], v[176:179], a[168:171], 0\n"
        "v_cvt_pk_bf16_f32 v234, v24, v28\n"
        "v_cvt_pk_bf16_f32 v242, v25, v29\n"
        "v_mfma_f32_16x16x32_bf16 v[40:43], v[180:183], a[172:175], v[40:43]\n"
        "buffer_store_dwordx2 v[234:235], v250, s[16:19], s39 offen offset:0 sc1\n"
        "v_cvt_pk_bf16_f32 v232, v26, v30\n"
        "v_mfma_f32_16x16x32_bf16 v[40:43], v[184:187], a[176:179], v[40:43]\n"
        "v_exp_f32 v8, v8\n"
        "v_mfma_f32_16x16x32_bf16 v[40:43], v[196:199], a[180:183], v[40:43]\n"
        "s_barrier\n"
        "ds_read_b128 a[0:3], v240 offset:0\n"
        "ds_read_b128 a[4:7], v240 offset:4096\n"
        "v_mfma_f32_16x16x32_bf16 v[40:43], v[200:203], a[184:187], v[40:43]\n"
        "buffer_store_dwordx2 v[242:243], v250, s[16:19], s39 offen offset:384 sc1\n"
        "ds_read_b128 a[8:11], v240 offset:8192\n"
        "v_mfma_f32_16x16x32_bf16 v[40:43], v[204:207], a[188:191], v[40:43]\n"
        "ds_read_b128 a[12:15], v240 offset:12288\n"
        "ds_read_b128 a[16:19], v240 offset:16384\n"
        "v_smfmac_f32_16x16x64_bf16 v[96:99], a[252:255], v[80:87], v237\n"
        "ds_read_b128 a[20:23], v240 offset:20480\n"
        "v_mfma_f32_16x16x32_bf16 v[72:75], v[132:135], v[84:87], 0\n"
        "buffer_store_dwordx2 v[232:233], v250, s[16:19], s39 offen offset:768 sc1\n"
        "v_cvt_pk_bf16_f32 v234, v27, v31\n"
        "v_mfma_f32_16x16x32_bf16 v[72:75], v[128:131], v[80:83], v[72:75]\n"
        "v_exp_f32 v9, v9\n"
        "v_mfma_f32_16x16x32_bf16 v[76:79], v[136:139], v[80:83], 0\n"
        "v_exp_f32 v10, v10\n"
        "v_mfma_f32_16x16x32_bf16 v[76:79], v[140:143], v[84:87], v[76:79]\n"
        "v_cvt_pk_bf16_f32 v92, v8, v9\n"
        "v_cvt_pk_bf16_f32 v235, v35, v39\n"
        "v_mfma_f32_16x16x32_bf16 v[100:103], v[148:151], v[84:87], 0\n"
        "buffer_store_dwordx2 v[234:235], v250, s[16:19], s39 offen offset:1152 sc1\n"
        "v_add_f32 v97, v96, v97\n"
        "v_mfma_f32_16x16x32_bf16 v[100:103], v[144:147], v[80:83], v[100:103]\n"
        "v_exp_f32 v11, v11\n"
        "v_mfma_f32_16x16x32_bf16 v[12:15], v[212:215], v[216:219], v[60:63]\n"
        "v_cvt_pk_bf16_f32 v93, v10, v11\n"
        "v_mov_b32 v249, s29\n"
        "v_mfma_f32_16x16x32_bf16 v[12:15], v[228:231], v[224:227], v[12:15]\n"
        "s_waitcnt lgkmcnt(0)\n"
        "v_rcp_f32 v96, v97\n"
        "v_mfma_f32_16x16x32_bf16 v[156:159], a[20:23], a[92:95], 0\n"
        "v_mul_f32 v72, v96, v72\n"
        "v_mul_f32 v73, v96, v73\n"
        "v_mfma_f32_16x16x32_bf16 v[156:159], a[16:19], a[88:91], v[156:159]\n"
        "v_mul_f32 v74, v96, v74\n"
        "v_mul_f32 v75, v96, v75\n"
        "v_mfma_f32_16x16x32_bf16 v[156:159], a[12:15], a[84:87], v[156:159]\n"
        "v_exp_f32 v12, v12\n"
        "v_mfma_f32_16x16x32_bf16 v[156:159], a[8:11], a[80:83], v[156:159]\n"
        "v_exp_f32 v13, v13\n"
        "v_mfma_f32_16x16x32_bf16 v[156:159], a[4:7], a[76:79], v[156:159]\n"
        "v_exp_f32 v14, v14\n"
        "v_mfma_f32_16x16x32_bf16 v[156:159], a[0:3], a[72:75], v[156:159]\n"
        "v_cvt_pk_bf16_f32 v94, v12, v13\n"
        "v_cvt_pk_bf16_f32 v72, v72, v73\n"
        "v_mfma_f32_16x16x32_bf16 v[44:47], v[176:179], a[192:195], 0\n"
        "v_exp_f32 v15, v15\n"
        "v_mfma_f32_16x16x32_bf16 v[44:47], v[180:183], a[196:199], v[44:47]\n"
        "v_cvt_pk_bf16_f32 v95, v14, v15\n"
        "v_mov_b64 v[124:125], 0\n"
        "v_mfma_f32_16x16x32_bf16 v[44:47], v[184:187], a[200:203], v[44:47]\n"
        "v_cvt_pk_bf16_f32 v73, v74, v75\n"
        "v_mul_f32 v76, v96, v76\n"
        "v_mfma_f32_16x16x32_bf16 v[44:47], v[196:199], a[204:207], v[44:47]\n"
        "ds_write2st64_b32 v245, v72, v73 offset0:2 offset1:6\n"
        "v_mfma_f32_16x16x32_bf16 v[44:47], v[200:203], a[208:211], v[44:47]\n"
        "v_mul_f32 v77, v96, v77\n"
        "v_mul_f32 v78, v96, v78\n"
        "v_mfma_f32_16x16x32_bf16 v[44:47], v[204:207], a[212:215], v[44:47]\n"
        "v_mul_f32 v79, v96, v79\n"
        "v_cvt_pk_bf16_f32 v74, v76, v77\n"
        "v_mfma_f32_16x16x32_bf16 v[160:163], a[20:23], a[116:119], 0\n"
        "v_cvt_pk_bf16_f32 v75, v78, v79\n"
        "v_mul_f32 v100, v96, v100\n"
        "v_mfma_f32_16x16x32_bf16 v[160:163], a[16:19], a[112:115], v[160:163]\n"
        "v_mul_f32 v101, v96, v101\n"
        "v_mul_f32 v102, v96, v102\n"
        "v_mfma_f32_16x16x32_bf16 v[160:163], a[12:15], a[108:111], v[160:163]\n"
        "ds_write2st64_b32 v245, v74, v75 offset0:10 offset1:14\n"
        "v_mfma_f32_16x16x32_bf16 v[160:163], a[8:11], a[104:107], v[160:163]\n"
        "v_cvt_pk_bf16_f32 v239, v40, v44\n"
        "v_cvt_pk_bf16_f32 v255, v41, v45\n"
        "v_mfma_f32_16x16x32_bf16 v[160:163], a[4:7], a[100:103], v[160:163]\n"
        "buffer_store_dword v239, v238, s[16:19], s39 offen offset:0 sc1\n"
        "v_mfma_f32_16x16x32_bf16 v[160:163], a[0:3], a[96:99], v[160:163]\n"
        "v_mul_f32 v103, v96, v103\n"
        "v_cvt_pk_bf16_f32 v244, v42, v46\n"
        "v_smfmac_f32_16x16x64_bf16 v[124:127], a[252:255], v[88:95], v237\n"
        "v_cvt_pk_bf16_f32 v100, v100, v101\n"
        "v_mfma_f32_16x16x32_bf16 v[104:107], v[132:135], v[92:95], 0\n"
        "buffer_store_dword v255, v238, s[16:19], s39 offen offset:384 sc1\n"
        "v_mfma_f32_16x16x32_bf16 v[104:107], v[128:131], v[88:91], v[104:107]\n"
        "v_cvt_pk_bf16_f32 v101, v102, v103\n"
        "v_cvt_pk_bf16_f32 v239, v43, v47\n"
        "v_mfma_f32_16x16x32_bf16 v[108:111], v[136:139], v[88:91], 0\n"
        "buffer_store_dword v244, v238, s[16:19], s39 offen offset:768 sc1\n"
        "v_mfma_f32_16x16x32_bf16 v[108:111], v[140:143], v[92:95], v[108:111]\n"
        "ds_write2st64_b32 v245, v100, v101 offset0:18 offset1:22\n"
        "v_mfma_f32_16x16x32_bf16 v[112:115], v[148:151], v[92:95], 0\n"
        "v_max3_u32 v236, v236, v96, v97\n"
        "v_add_f32 v125, v124, v125\n"
        "v_mfma_f32_16x16x32_bf16 v[112:115], v[144:147], v[88:91], v[112:115]\n"
        "buffer_store_dword v239, v238, s[16:19], s39 offen offset:1152 sc1\n"
        "v_mfma_f32_16x16x32_bf16 v[176:179], a[0:3], a[144:147], 0\n"
        "v_rcp_f32 v124, v125\n"
        "v_mfma_f32_16x16x32_bf16 v[176:179], a[4:7], a[148:151], v[176:179]\n"
        "v_mul_f32 v104, v124, v104\n"
        "v_mul_f32 v105, v124, v105\n"
        "v_mfma_f32_16x16x32_bf16 v[176:179], a[8:11], a[152:155], v[176:179]\n"
        "v_mul_f32 v106, v124, v106\n"
        "v_mul_f32 v107, v124, v107\n"
        "v_mfma_f32_16x16x32_bf16 v[176:179], a[12:15], a[156:159], v[176:179]\n"
        "v_cvt_pk_bf16_f32 v104, v104, v105\n"
        "v_cvt_pk_bf16_f32 v105, v106, v107\n"
        "v_mfma_f32_16x16x32_bf16 v[176:179], a[16:19], a[160:163], v[176:179]\n"
        "ds_write2st64_b32 v245, v104, v105 offset0:3 offset1:7\n"
        "v_mfma_f32_16x16x32_bf16 v[176:179], a[20:23], a[164:167], v[176:179]\n"
        "v_mul_f32 v108, v124, v108\n"
        "v_mul_f32 v109, v124, v109\n"
        "v_mfma_f32_16x16x32_bf16 v[164:167], a[20:23], a[140:143], 0\n"
        "v_mul_f32 v110, v124, v110\n"
        "v_mul_f32 v111, v124, v111\n"
        "v_mfma_f32_16x16x32_bf16 v[164:167], a[16:19], a[136:139], v[164:167]\n"
        "v_cvt_pk_bf16_f32 v106, v108, v109\n"
        "v_cvt_pk_bf16_f32 v107, v110, v111\n"
        "v_mfma_f32_16x16x32_bf16 v[164:167], a[12:15], a[132:135], v[164:167]\n"
        "v_mul_f32 v112, v124, v112\n"
        "v_mul_f32 v113, v124, v113\n"
        "v_mfma_f32_16x16x32_bf16 v[164:167], a[8:11], a[128:131], v[164:167]\n"
        "ds_write2st64_b32 v245, v106, v107 offset0:11 offset1:15\n"
        "v_mfma_f32_16x16x32_bf16 v[164:167], a[4:7], a[124:127], v[164:167]\n"
        "v_mul_f32 v114, v124, v114\n"
        "v_mul_f32 v115, v124, v115\n"
        "v_mfma_f32_16x16x32_bf16 v[164:167], a[0:3], a[120:123], v[164:167]\n"
        "v_cvt_pk_bf16_f32 v112, v112, v113\n"
        "v_cvt_pk_bf16_f32 v113, v114, v115\n"
        "v_mfma_f32_16x16x32_bf16 v[180:183], a[0:3], a[168:171], 0\n"
        "v_max3_u32 v236, v236, v124, v125\n"
        "v_cvt_pk_bf16_f32 v242, v156, v160\n"
        "v_mfma_f32_16x16x32_bf16 v[180:183], a[4:7], a[172:175], v[180:183]\n"
        "ds_write2st64_b32 v245, v112, v113 offset0:19 offset1:23\n"
        "v_mfma_f32_16x16x32_bf16 v[180:183], a[8:11], a[176:179], v[180:183]\n"
        "v_cvt_pk_bf16_f32 v243, v164, v176\n"
        "v_cmp_lt_u32 vcc, s24, v236\n"
        "v_mfma_f32_16x16x32_bf16 v[180:183], a[12:15], a[180:183], v[180:183]\n"
        "buffer_store_dwordx2 v[242:243], v250, s[16:19], s47 offen offset:0 sc1\n"
        "v_cvt_pk_bf16_f32 v232, v157, v161\n"
        "v_mfma_f32_16x16x32_bf16 v[180:183], a[16:19], a[184:187], v[180:183]\n"
        "s_cmp_lg_u64 vcc, 0\n"
        "s_addc_u32 s25, s25, s25\n"
        "v_mov_b32 v253, s25\n"
        "v_mfma_f32_16x16x32_bf16 v[180:183], a[20:23], a[188:191], v[180:183]\n"
        "ds_write_b32 v249, v253\n"
        "v_mfma_f32_16x16x32_bf16 v[184:187], a[20:23], a[212:215], 0\n"
        "v_cvt_pk_bf16_f32 v233, v165, v177\n"
        "v_cvt_pk_bf16_f32 v234, v158, v162\n"
        "v_mfma_f32_16x16x32_bf16 v[184:187], a[16:19], a[208:211], v[184:187]\n"
        "buffer_store_dwordx2 v[232:233], v250, s[16:19], s47 offen offset:384 sc1\n"
        "v_cvt_pk_bf16_f32 v235, v166, v178\n"
        "v_mfma_f32_16x16x32_bf16 v[184:187], a[12:15], a[204:207], v[184:187]\n"
        "s_waitcnt lgkmcnt(0)\n"
        "s_barrier\n"
        "ds_read_b128 a[24:27], v240 offset:512\n"
        "ds_read_b128 a[28:31], v240 offset:4608\n"
        "v_mfma_f32_16x16x32_bf16 v[184:187], a[8:11], a[200:203], v[184:187]\n"
        "buffer_store_dwordx2 v[234:235], v250, s[16:19], s47 offen offset:768 sc1\n"
        "ds_read_b128 a[32:35], v240 offset:8704\n"
        "v_mfma_f32_16x16x32_bf16 v[184:187], a[4:7], a[196:199], v[184:187]\n"
        "ds_read_b128 a[36:39], v240 offset:12800\n"
        "ds_read_b128 a[40:43], v240 offset:16896\n"
        "v_mfma_f32_16x16x32_bf16 v[184:187], a[0:3], a[192:195], v[184:187]\n"
        "s_waitcnt lgkmcnt(4)\n"
        "ds_read_b128 a[44:47], v240 offset:20992\n"
        "v_cvt_pk_bf16_f32 v242, v159, v163\n"
        "v_mfma_f32_16x16x32_bf16 v[52:55], a[24:27], a[168:171], 0\n"
        "s_waitcnt lgkmcnt(4)\n"
        "v_cvt_pk_bf16_f32 v243, v167, v179\n"
        "s_nop 0\n"
        "v_mfma_f32_16x16x32_bf16 v[52:55], a[28:31], a[172:175], v[52:55]\n"
        "s_waitcnt lgkmcnt(3)\n"
        "buffer_store_dwordx2 v[242:243], v250, s[16:19], s47 offen offset:1152 sc1\n"
        "s_nop 0\n"
        "v_mfma_f32_16x16x32_bf16 v[52:55], a[32:35], a[176:179], v[52:55]\n"
        "s_waitcnt lgkmcnt(2)\n"
        "v_cvt_pk_bf16_f32 v255, v180, v184\n"
        "v_cvt_pk_bf16_f32 v244, v181, v185\n"
        "v_mfma_f32_16x16x32_bf16 v[52:55], a[36:39], a[180:183], v[52:55]\n"
        "s_waitcnt lgkmcnt(1)\n"
        "buffer_store_dword v255, v238, s[16:19], s47 offen offset:0 sc1\n"
        "v_mfma_f32_16x16x32_bf16 v[52:55], a[40:43], a[184:187], v[52:55]\n"
        "s_waitcnt lgkmcnt(0)\n"
        "ds_read_b128 v[60:63], v241\n"
        "v_cvt_pk_bf16_f32 v239, v182, v186\n"
        "v_mfma_f32_16x16x32_bf16 v[52:55], a[44:47], a[188:191], v[52:55]\n"
        "v_cvt_pk_bf16_f32 v255, v183, v187\n"
        "v_mfma_f32_16x16x32_bf16 v[56:59], a[44:47], a[212:215], 0\n"
        "buffer_store_dword v244, v238, s[16:19], s47 offen offset:384 sc1\n"
        "v_mfma_f32_16x16x32_bf16 v[56:59], a[40:43], a[208:211], v[56:59]\n"
        "v_mfma_f32_16x16x32_bf16 v[56:59], a[36:39], a[204:207], v[56:59]\n"
        "v_mfma_f32_16x16x32_bf16 v[56:59], a[32:35], a[200:203], v[56:59]\n"
        "buffer_store_dword v239, v238, s[16:19], s47 offen offset:768 sc1\n"
        "v_mfma_f32_16x16x32_bf16 v[56:59], a[28:31], a[196:199], v[56:59]\n"
        "v_mfma_f32_16x16x32_bf16 v[56:59], a[24:27], a[192:195], v[56:59]\n"
        "s_nop 2\n"
        "v_mfma_f32_16x16x32_bf16 v[196:199], a[24:27], a[72:75], 0\n"
        "buffer_store_dword v255, v238, s[16:19], s47 offen offset:1152 sc1\n"
        "v_mfma_f32_16x16x32_bf16 v[196:199], a[28:31], a[76:79], v[196:199]\n"
        "s_nop 1\n"
        "v_cvt_pk_bf16_f32 v88, v52, v56\n"
        "v_mfma_f32_16x16x32_bf16 v[196:199], a[32:35], a[80:83], v[196:199]\n"
        "v_cvt_pk_bf16_f32 v90, v53, v57\n"
        "v_cvt_pk_bf16_f32 v92, v54, v58\n"
        "v_mfma_f32_16x16x32_bf16 v[196:199], a[36:39], a[84:87], v[196:199]\n"
        "buffer_store_dword v88, v238, s[16:19], s35 offen offset:0 sc1\n"
        "v_mfma_f32_16x16x32_bf16 v[196:199], a[40:43], a[88:91], v[196:199]\n"
        "v_cvt_pk_bf16_f32 v94, v55, v59\n"
        "v_mfma_f32_16x16x32_bf16 v[196:199], a[44:47], a[92:95], v[196:199]\n"
        "v_mfma_f32_16x16x32_bf16 v[48:51], a[44:47], a[164:167], 0\n"
        "buffer_store_dword v90, v238, s[16:19], s35 offen offset:384 sc1\n"
        "v_mfma_f32_16x16x32_bf16 v[48:51], a[40:43], a[160:163], v[48:51]\n"
        "s_waitcnt lgkmcnt(0)\n"
        "v_or3_b32 v60, v60, v61, v62\n"
        "v_or_b32 v60, v60, v63\n"
        "v_mfma_f32_16x16x32_bf16 v[48:51], a[36:39], a[156:159], v[48:51]\n"
        "s_nop 0\n"
        "v_readfirstlane_b32 s25, v60\n"
        "v_mfma_f32_16x16x32_bf16 v[48:51], a[32:35], a[152:155], v[48:51]\n"
        "buffer_store_dword v92, v238, s[16:19], s35 offen offset:768 sc1\n"
        "v_mfma_f32_16x16x32_bf16 v[48:51], a[28:31], a[148:151], v[48:51]\n"
        "v_mfma_f32_16x16x32_bf16 v[48:51], a[24:27], a[144:147], v[48:51]\n"
        "v_mfma_f32_16x16x32_bf16 v[204:207], a[24:27], a[120:123], 0\n"
        "buffer_store_dword v94, v238, s[16:19], s35 offen offset:1152 sc1\n"
        "v_mfma_f32_16x16x32_bf16 v[204:207], a[28:31], a[124:127], v[204:207]\n"
        "v_mfma_f32_16x16x32_bf16 v[204:207], a[32:35], a[128:131], v[204:207]\n"
        "v_mfma_f32_16x16x32_bf16 v[204:207], a[36:39], a[132:135], v[204:207]\n"
        "v_mfma_f32_16x16x32_bf16 v[204:207], a[40:43], a[136:139], v[204:207]\n"
        "v_mfma_f32_16x16x32_bf16 v[204:207], a[44:47], a[140:143], v[204:207]\n"
        "v_mfma_f32_16x16x32_bf16 v[200:203], a[44:47], a[116:119], 0\n"
        "v_mfma_f32_16x16x32_bf16 v[200:203], a[40:43], a[112:115], v[200:203]\n"
        "s_nop 2\n"
        "v_mfma_f32_16x16x32_bf16 v[200:203], a[36:39], a[108:111], v[200:203]\n"
        "s_nop 1\n"
        "v_cvt_pk_bf16_f32 v81, v204, v48\n"
        "v_mfma_f32_16x16x32_bf16 v[200:203], a[32:35], a[104:107], v[200:203]\n"
        "v_cvt_pk_bf16_f32 v83, v205, v49\n"
        "v_mfma_f32_16x16x32_bf16 v[200:203], a[28:31], a[100:103], v[200:203]\n"
        "v_cvt_pk_bf16_f32 v85, v206, v50\n"
        "v_mfma_f32_16x16x32_bf16 v[200:203], a[24:27], a[96:99], v[200:203]\n"
        "s_nop 2\n"
        "v_cvt_pk_bf16_f32 v87, v207, v51\n"
        "s_nop 3\n"
        "v_cvt_pk_bf16_f32 v80, v196, v200\n"
        "buffer_store_dwordx2 v[80:81], v250, s[16:19], s35 offen offset:0 sc1\n"
        "v_cvt_pk_bf16_f32 v82, v197, v201\n"
        "buffer_store_dwordx2 v[82:83], v250, s[16:19], s35 offen offset:384 sc1\n"
        "v_cvt_pk_bf16_f32 v84, v198, v202\n"
        "buffer_store_dwordx2 v[84:85], v250, s[16:19], s35 offen offset:768 sc1\n"
        "v_cvt_pk_bf16_f32 v86, v199, v203\n"
        "buffer_store_dwordx2 v[86:87], v250, s[16:19], s35 offen offset:1152 sc1\n"
        "s_cmp_lg_u32 s25, 0\n"
        "s_cbranch_scc1 30f\n"
        "s_endpgm\n"
        "30:\n"
        : "={s25}"(flg), "={s42}"(w), "+{s[0:1]}"(ka), "+{s2}"(bid), "+{v0}"(tid)
        : "s"(ldsb)
        : "v1", "v2", "v3", "v4", "v5", "v6", "v7", "v8", "v9", "v10", "v11", "v12", "v13", "v14", "v15", "v16", "v17", "v18", "v19", "v20", "v21", "v22", "v23", "v24", "v25", "v26", "v27", "v28", "v29", "v30", "v31", "v32", "v33", "v34", "v35", "v36", "v37", "v38", "v39", "v40", "v41", "v42", "v43", "v44", "v45", "v46", "v47", "v48", "v49", "v50", "v51", "v52", "v53", "v54", "v55", "v56", "v57", "v58", "v59", "v60", "v61", "v62", "v63", "v64", "v65", "v66", "v67", "v68", "v69", "v70", "v71", "v72", "v73", "v74", "v75", "v76", "v77", "v78", "v79", "v80", "v81", "v82", "v83", "v84", "v85", "v86", "v87", "v88", "v89", "v90", "v91", "v92", "v93", "v94", "v95", "v96", "v97", "v98", "v99", "v100", "v101", "v102", "v103", "v104", "v105", "v106", "v107", "v108", "v109", "v110", "v111", "v112", "v113", "v114", "v115", "v116", "v117", "v118", "v119", "v120", "v121", "v122", "v123", "v124", "v125", "v126", "v127", "v128", "v129", "v130", "v131", "v132", "v133", "v134", "v135", "v136", "v137", "v138", "v139", "v140", "v141", "v142", "v143", "v144", "v145", "v146", "v147", "v148", "v149", "v150", "v151", "v152", "v153", "v154", "v155", "v156", "v157", "v158", "v159", "v160", "v161", "v162", "v163", "v164", "v165", "v166", "v167", "v168", "v169", "v170", "v171", "v172", "v173", "v174", "v175", "v176", "v177", "v178", "v179", "v180", "v181", "v182", "v183", "v184", "v185", "v186", "v187", "v188", "v189", "v190", "v191", "v192", "v193", "v194", "v195", "v196", "v197", "v198", "v199", "v200", "v201", "v202", "v203", "v204", "v205", "v206", "v207", "v208", "v209", "v210", "v211", "v212", "v213", "v214", "v215", "v216", "v217", "v218", "v219", "v220", "v221", "v222", "v223", "v224", "v225", "v226", "v227", "v228", "v229", "v230", "v231", "v232", "v233", "v234", "v235", "v236", "v237", "v238", "v239", "v240", "v241", "v242", "v243", "v244", "v245", "v246", "v247", "v248", "v249", "v250", "v251", "v252", "v253", "v254", "v255", "a0", "a1", "a2", "a3", "a4", "a5", "a6", "a7", "a8", "a9", "a10", "a11", "a12", "a13", "a14", "a15", "a16", "a17", "a18", "a19", "a20", "a21", "a22", "a23", "a24", "a25", "a26", "a27", "a28", "a29", "a30", "a31", "a32", "a33", "a34", "a35", "a36", "a37", "a38", "a39", "a40", "a41", "a42", "a43", "a44", "a45", "a46", "a47", "a48", "a49", "a50", "a51", "a52", "a53", "a54", "a55", "a56", "a57", "a58", "a59", "a60", "a61", "a62", "a63", "a64", "a65", "a66", "a67", "a68", "a69", "a70", "a71", "a72", "a73", "a74", "a75", "a76", "a77", "a78", "a79", "a80", "a81", "a82", "a83", "a84", "a85", "a86", "a87", "a88", "a89", "a90", "a91", "a92", "a93", "a94", "a95", "a96", "a97", "a98", "a99", "a100", "a101", "a102", "a103", "a104", "a105", "a106", "a107", "a108", "a109", "a110", "a111", "a112", "a113", "a114", "a115", "a116", "a117", "a118", "a119", "a120", "a121", "a122", "a123", "a124", "a125", "a126", "a127", "a128", "a129", "a130", "a131", "a132", "a133", "a134", "a135", "a136", "a137", "a138", "a139", "a140", "a141", "a142", "a143", "a144", "a145", "a146", "a147", "a148", "a149", "a150", "a151", "a152", "a153", "a154", "a155", "a156", "a157", "a158", "a159", "a160", "a161", "a162", "a163", "a164", "a165", "a166", "a167", "a168", "a169", "a170", "a171", "a172", "a173", "a174", "a175", "a176", "a177", "a178", "a179", "a180", "a181", "a182", "a183", "a184", "a185", "a186", "a187", "a188", "a189", "a190", "a191", "a192", "a193", "a194", "a195", "a196", "a197", "a198", "a199", "a200", "a201", "a202", "a203", "a204", "a205", "a206", "a207", "a208", "a209", "a210", "a211", "a212", "a213", "a214", "a215", "a216", "a217", "a218", "a219", "a220", "a221", "a222", "a223", "a224", "a225", "a226", "a227", "a228", "a229", "a230", "a231", "a232", "a233", "a234", "a235", "a236", "a237", "a238", "a239", "a240", "a241", "a242", "a243", "a244", "a245", "a246", "a247", "a248", "a249", "a250", "a251", "a252", "a253", "a254", "a255", "s4", "s5", "s6", "s7", "s8", "s9", "s10", "s11", "s12", "s13", "s14", "s15", "s16", "s17", "s18", "s19", "s20", "s21", "s22", "s23", "s24", "s26", "s27", "s28", "s29", "s30", "s31", "s34", "s35", "s36", "s37", "s38", "s39", "s40", "s41", "s43", "s44", "s45", "s46", "s47", "s48", "s49", "s50", "s51", "s52", "s53", "s54", "s55", "s56", "s57", "s58", "s59", "s60", "s61", "s62", "s63", "s64", "s65", "s66", "s67", "s68", "s69", "s70", "s71", "s72", "s73", "s74", "s75", "s76", "s77", "s78", "s79", "s80", "s81", "s82", "s83", "vcc", "scc", "memory");
    if (flg) {
        unsigned ln;
        asm volatile("s_waitcnt vmcnt(0)\n v_mbcnt_lo_u32_b32 %0, -1, 0\n v_mbcnt_hi_u32_b32 %0, -1, %0" : "=v"(ln) :: "memory");
        const unsigned t = w * 64u + ln, wx = bid & 63u, wy0 = bid >> 6;
        typedef const char* __attribute__((address_space(4))) const* kargp;
        const char *x = ((kargp)ka)[0], *wqkv = ((kargp)ka)[1], *wo = ((kargp)ka)[2], *rpb = ((kargp)ka)[3], *rpi = ((kargp)ka)[4],
                   *mask = ((kargp)ka)[5];
        char* out = (char*)((kargp)ka)[6];
        unsigned short* xs = (unsigned short*)smem;
        unsigned short* qs = xs + 64 * 192;
        const unsigned short* W1 = (const unsigned short*)wqkv;
        const unsigned short* W2 = (const unsigned short*)wo;
        for (unsigned i = 0; i < 16u; ++i) {
            if (!((flg >> (15u - i)) & 1u)) continue;
            const unsigned wy = wy0 + 4u * i;
            __syncthreads();
            for (unsigned e = t; e < 64u * 96u; e += 256u) {
                const unsigned tok = e / 96u, c2 = e % 96u;
                const unsigned pix = ((8u * wy + 4u + (tok >> 3)) & 511u) * 512u + ((8u * wx + 4u + (tok & 7u)) & 511u);
                ((unsigned*)xs)[e] = ((const unsigned*)x)[pix * 96u + c2];
            }
            __syncthreads();
            for (unsigned col = t; col < 576u; col += 256u)
                for (unsigned tb = 0; tb < 64u; tb += 8u) {
                    float a[8] = {0.f, 0.f, 0.f, 0.f, 0.f, 0.f, 0.f, 0.f};
                    for (unsigned c = 0; c < 192u; ++c) {
                        const float wt = bf2f(W1[c * 576u + col]);
#pragma unroll
                        for (int j = 0; j < 8; ++j) a[j] += bf2f(xs[(tb + j) * 192u + c]) * wt;
                    }
#pragma unroll
                    for (int j = 0; j < 8; ++j) qs[(tb + j) * 578u + col] = f2bf(a[j]);
                }
            __syncthreads();
            {
                float qv[48], o[48];
#pragma unroll
                for (int d = 0; d < 48; ++d) { qv[d] = bf2f(qs[ln * 578u + w * 48u + d]); o[d] = 0.f; }
                const unsigned short* mrow = (const unsigned short*)mask + (size_t)(wy * 64u + wx) * 4096u + ln * 64u;
                float mx = -__builtin_inff(), l = 0.f;
                for (int pass = 0; pass < 2; ++pass)
                    for (unsigned k = 0; k < 64u; ++k) {
                        float s = 0.f;
#pragma unroll
                        for (int d = 0; d < 48; ++d) s += qv[d] * bf2f(qs[k * 578u + 192u + w * 48u + d]);
                        s = s * 0.14433756729740643f + bf2f(((const unsigned short*)rpb)[((const unsigned*)rpi)[ln * 64u + k] * 4u + w]) + bf2f(mrow[k]);
                        if (pass == 0) mx = fmaxf(mx, s);
                        else {
                            const float p = __expf(s - mx);
                            l += p;
#pragma unroll
                            for (int d = 0; d < 48; ++d) o[d] += p * bf2f(qs[k * 578u + 384u + w * 48u + d]);
                        }
                    }
#pragma unroll
                for (int d = 0; d < 48; ++d) xs[ln * 192u + w * 48u + d] = f2bf(o[d] / l);
            }
            __syncthreads();
            if (t < 192u)
                for (unsigned tb = 0; tb < 64u; tb += 8u) {
                    float a[8] = {0.f, 0.f, 0.f, 0.f, 0.f, 0.f, 0.f, 0.f};
                    for (unsigned c = 0; c < 192u; ++c) {
                        const float wt = bf2f(W2[c * 192u + t]);
#pragma unroll
                        for (int j = 0; j < 8; ++j) a[j] += bf2f(xs[(tb + j) * 192u + c]) * wt;
                    }
                    const unsigned row = (8u * wy + 4u + (tb >> 3)) & 511u;
#pragma unroll
                    for (int j = 0; j < 8; ++j)
                        ((unsigned short*)out)[(row * 512u + ((8u * wx + 4u + j) & 511u)) * 192u + t] = f2bf(a[j]);
                }
        }
    }
}


extern "C" void kernel_launch(void* const* d_in, const int* in_sizes, int n_in, void* d_out, int out_size, void* d_ws,
                              size_t ws_size, hipStream_t stream) {
    win_attn_kernel<<<256, 256, 0, stream>>>((const char*)d_in[0], (const char*)d_in[1], (const char*)d_in[2],
                                             (const char*)d_in[3], (const char*)d_in[4], (const char*)d_in[5],
                                             (char*)d_out, (char*)d_ws);

}
```

```cpp
#include <hip/hip_runtime.h>
#include <hip/hip_bf16.h>

typedef unsigned int u32x4 __attribute__((ext_vector_type(4)));

static __device__ __forceinline__ float bf2f(unsigned short u) { return __uint_as_float((unsigned)u << 16); }
static __device__ __forceinline__ unsigned short f2bf(float f) { return __builtin_bit_cast(unsigned short, (__bf16)f); }

__global__ __launch_bounds__(256, 1) __attribute__((aligned(4096))) void win_attn_kernel(const char* x_, const char* wqkv_, const char* wo_, const char* rpb_,
                                                          const char* rpi_, const char* mask_, char* out_, char* ws) {
    __shared__ __attribute__((aligned(16))) char smem[163840];
    const void* ka = (const void*)__builtin_amdgcn_kernarg_segment_ptr();
    unsigned bid = blockIdx.x, tid = threadIdx.x, flg, w;
    unsigned ldsb = (unsigned)(size_t)smem;
    asm volatile(
        "s_load_dwordx16 s[68:83], s[0:1], 0\n"
        "v_readfirstlane_b32 s42, v0\n"
        "v_and_b32 v1, 63, v0\n"
        "s_lshr_b32 s31, s2, 1\n"
        "s_lshr_b32 s42, s42, 6\n"
        "s_and_b32 s31, s31, 0x7c\n"
        "v_lshlrev_b32 v2, 4, v1\n"
        "s_add_u32 s31, s31, s42\n"
        "s_mul_i32 s35, s31, 256\n"
        "v_min_u32 v10, 3, v1\n"
        "s_getpc_b64 s[66:67]\n"
        "v_lshl_add_u32 v10, v10, 6, s35\n"
        "global_load_dword v60, v10, s[66:67]\n"
        "s_lshl_b32 s50, s31, 6\n"
        "s_mul_i32 s50, s50, 27\n"
        "v_min_u32 v136, 26, v1\n"
        "v_lshl_add_u32 v136, v136, 6, s50\n"
        "v_mul_u32_u24 v11, 171, v1\n"
        "v_lshrrev_b32 v4, 10, v11\n"
        "v_bfe_u32 v7, v11, 13, 1\n"
        "v_add_u32 v11, 10944, v11\n"
        "v_lshrrev_b32 v5, 10, v11\n"
        "v_bfe_u32 v8, v11, 13, 1\n"
        "v_add_u32 v11, 10944, v11\n"
        "v_lshrrev_b32 v6, 10, v11\n"
        "v_bfe_u32 v9, v11, 13, 1\n"
        "s_mul_i32 s40, s42, 3072\n"
        "s_mov_b32 s10, 100663296\n"
        "s_mov_b32 s11, 0x20000\n"
        "s_mov_b32 s6, 16384\n"
        "s_mov_b32 s7, 0x20000\n"
        "s_mov_b32 s62, 1800\n"
        "s_mov_b32 s63, 0x20000\n"
        "s_mov_b32 s22, 221184\n"
        "s_mov_b32 s23, 0x20000\n"
        "s_and_b32 s51, s42, 1\n"
        "v_lshlrev_b32 v134, 3, v0\n"
        "s_lshl_b32 s35, s51, 7\n"
        "v_and_b32 v10, 48, v1\n"
        "v_xor_b32 v131, s35, v2\n"
        "s_lshl_b32 s46, s42, 12\n"
        "v_xor_b32 v131, v131, v10\n"
        "v_xor_b32 v132, 64, v131\n"
        "s_and_b32 s44, s2, 63\n"
        "s_lshl3_add_u32 s44, s44, 4\n"
        "s_lshl_b32 s35, s42, 5\n"
        "s_movk_i32 s41, 384\n"
        "s_movk_i32 s45, -384\n"
        "v_lshrrev_b32 v10, 2, v4\n"
        "v_mad_i32_i24 v11, v10, s45, v2\n"
        "v_add_u32 v10, s44, v10\n"
        "v_lshl_or_b32 v242, v7, 4, s35\n"
        "v_and_b32 v10, 511, v10\n"
        "v_xor_b32 v11, v11, v242\n"
        "v_mad_u32_u24 v242, v10, s41, v11\n"
        "v_lshrrev_b32 v10, 2, v5\n"
        "v_mad_i32_i24 v11, v10, s45, v2\n"
        "v_add_u32 v10, s44, v10\n"
        "v_add_u32 v11, 1024, v11\n"
        "v_lshl_or_b32 v243, v8, 4, s35\n"
        "v_and_b32 v10, 511, v10\n"
        "v_xor_b32 v11, v11, v243\n"
        "v_mad_u32_u24 v243, v10, s41, v11\n"
        "v_lshrrev_b32 v10, 2, v6\n"
        "v_mad_i32_i24 v11, v10, s45, v2\n"
        "v_add_u32 v10, s44, v10\n"
        "v_add_u32 v11, 2048, v11\n"
        "v_lshl_or_b32 v244, v9, 4, s35\n"
        "v_and_b32 v10, 511, v10\n"
        "v_xor_b32 v11, v11, v244\n"
        "v_mad_u32_u24 v244, v10, s41, v11\n"
        "s_mul_i32 s43, s44, 384\n"
        "s_add_u32 s44, s44, 4\n"
        "s_and_b32 s44, s44, 511\n"
        "s_mul_i32 s44, s44, 384\n"
        "s_lshr_b32 s37, s2, 6\n"
        "s_lshl3_add_u32 s47, s37, s42\n"
        "s_add_u32 s47, s47, 68\n"
        "s_mul_i32 s47, s47, 196608\n"
        "s_lshl1_add_u32 s37, s37, s51\n"
        "s_add_u32 s37, s37, 1\n"
        "s_mul_i32 s37, s37, 786432\n"
        "s_mul_i32 s65, s42, 18432\n"
        "s_mov_b32 s14, 33554432\n"
        "s_mov_b32 s15, 0x20000\n"
        "s_mul_i32 s35, s42, 96\n"
        "s_movk_i32 s31, 1056\n"
        "s_movk_i32 s45, 4608\n"
        "v_add_u32 v3, s35, v2\n"
        "s_waitcnt lgkmcnt(0)\n"
        "s_mov_b64 s[60:61], s[74:75]\n"
        "s_mov_b64 s[20:21], s[70:71]\n"
        "s_mov_b64 s[4:5], s[76:77]\n"
        "s_mov_b64 s[8:9], s[68:69]\n"
        "buffer_load_dwordx2 v[144:145], v134, s[60:63], 0 offen\n"
        "s_add_u32 m0, s46, 122880\n"
        "s_mov_b32 s64, s46\n"
        "buffer_load_dword v136, s[20:23], 0 offen lds\n"
        "s_mov_b64 s[52:53], s[72:73]\n"
        "s_mov_b64 s[12:13], s[78:79]\n"
        "buffer_load_dwordx4 v131, s[4:7], s64 offen lds\n"
        "s_mov_b64 s[16:17], s[80:81]\n"
        "v_bfe_u32 v12, v4, 2, 1\n"
        "v_mad_u32_u24 v128, v4, s31, v3\n"
        "v_sub_u32 v12, v12, v7\n"
        "v_mad_i32_i24 v128, v12, s45, v128\n"
        "v_bfe_u32 v13, v5, 2, 1\n"
        "buffer_load_dwordx4 v131, s[4:7], s64 offen offset:1024 lds\n"
        "v_mad_u32_u24 v129, v5, s31, v3\n"
        "v_sub_u32 v13, v13, v8\n"
        "v_mad_i32_i24 v129, v13, s45, v129\n"
        "v_bfe_u32 v14, v6, 2, 1\n"
        "v_mad_u32_u24 v130, v6, s31, v3\n"
        "v_sub_u32 v14, v14, v9\n"
        "v_mad_i32_i24 v130, v14, s45, v130\n"
        "s_lshl_b32 s48, s42, 11\n"
        "s_lshl_b32 s36, s2, 13\n"
        "v_and_b32 v19, 16, v1\n"
        "buffer_load_dwordx4 v132, s[4:7], s64 offen offset:2048 lds\n"
        "s_add_u32 s48, s48, 155648\n"
        "s_add_u32 s36, s36, s48\n"
        "s_add_u32 s36, s36, 4038656\n"
        "s_lshl_b32 s35, s51, 6\n"
        "v_xor_b32 v249, v2, v19\n"
        "v_xor_b32 v249, s35, v249\n"
        "v_xor_b32 v253, 32, v249\n"
        "v_and_b32 v15, 15, v1\n"
        "v_lshrrev_b32 v16, 4, v1\n"
        "v_and_b32 v17, 12, v1\n"
        "buffer_load_dwordx4 v132, s[4:7], s64 offen offset:3072 lds\n"
        "v_and_b32 v18, 0xf0, v2\n"
        "v_add_u32 v17, v17, v15\n"
        "v_bfe_u32 v10, v1, 2, 3\n"
        "v_lshrrev_b32 v11, 5, v1\n"
        "v_lshl_or_b32 v10, v11, 4, v10\n"
        "v_and_b32 v255, 3, v1\n"
        "v_mul_u32_u24 v10, 96, v10\n"
        "v_lshl_add_u32 v255, v255, 3, s65\n"
        "v_add_u32 v255, v255, v10\n"
        "v_lshrrev_b32 v20, 1, v15\n"
        "s_add_u32 m0, s40, 73728\n"
        "s_sub_u32 s64, s47, 12582912\n"
        "buffer_load_dwordx4 v242, s[8:11], s64 offen lds\n"
        "v_xor_b32 v20, v20, v16\n"
        "v_lshlrev_b32 v20, 4, v20\n"
        "v_mad_u32_u24 v240, v17, s41, v20\n"
        "v_lshl_or_b32 v251, v17, 7, v20\n"
        "v_add_u32 v240, 73728, v240\n"
        "v_add_u32 v251, 155648, v251\n"
        "v_xor_b32 v241, 64, v240\n"
        "v_xor_b32 v252, 64, v251\n"
        "s_lshl_b32 s67, s42, 10\n"
        "s_add_u32 s67, s67, 139264\n"
        "s_add_u32 m0, s40, 74752\n"
        "s_nop 0\n"
        "buffer_load_dwordx4 v243, s[8:11], s64 offen lds\n"
        "v_lshlrev_b32 v135, 2, v0\n"
        "v_add_u32 v135, 139264, v135\n"
        "v_lshlrev_b32 v11, 5, v16\n"
        "v_xor_b32 v11, v11, v18\n"
        "v_lshl_or_b32 v11, v17, 8, v11\n"
        "v_add_u32 v140, 122880, v11\n"
        "v_xor_b32 v141, 16, v140\n"
        "v_xor_b32 v142, 128, v140\n"
        "v_xor_b32 v143, 144, v140\n"
        "v_lshrrev_b32 v10, 2, v15\n"
        "s_add_u32 m0, s40, 75776\n"
        "s_nop 0\n"
        "buffer_load_dwordx4 v244, s[8:11], s64 offen lds\n"
        "v_cmp_eq_u32 vcc, v10, v16\n"
        "v_mov_b32 v11, 0x3fb9\n"
        "v_mov_b32 v189, 0\n"
        "v_and_b32 v10, 48, v2\n"
        "v_cndmask_b32 v188, 0, v11, vcc\n"
        "v_lshlrev_b64 v[188:189], v10, v[188:189]\n"
        "v_cmp_gt_u32 vcc, 32, v1\n"
        "v_mov_b32 v11, 15957\n"
        "v_lshrrev_b32 v149, 2, v15\n"
        "v_lshrrev_b32 v151, 3, v19\n"
        "v_mov_b32 v148, 14702\n"
        "v_cndmask_b32 v11, v148, v11, vcc\n"
        "v_xor_b32 v151, v149, v151\n"
        "v_cmp_eq_u32 vcc, 0, v151\n"
        "v_mov_b32 v149, 0\n"
        "v_cndmask_b32 v148, 0, v11, vcc\n"
        "v_lshlrev_b64 v[148:149], v10, v[148:149]\n"
        "v_cmp_eq_u32 vcc, 1, v151\n"
        "v_mov_b32 v151, 0\n"
        "v_cndmask_b32 v150, 0, v11, vcc\n"
        "v_lshlrev_b64 v[150:151], v10, v[150:151]\n"
        "v_and_b32 v10, 8, v1\n"
        "v_add_u32 v10, v10, v15\n"
        "v_mul_u32_u24 v10, 96, v10\n"
        "v_add3_u32 v137, v10, v19, s65\n"
        "v_and_b32 v237, 1, v1\n"
        "v_mul_u32_u24 v237, 0xaaaa, v237\n"
        "v_mov_b64 v[170:171], v[188:189]\n"
        "v_mov_b64 v[210:211], v[188:189]\n"
        "v_mov_b64 v[228:229], v[188:189]\n"
        "v_add_u32 v237, 0x4444, v237\n"
        "v_mov_b32 v11, 0x3f803f80\n"
        "v_accvgpr_write_b32 a252, v11\n"
        "v_accvgpr_write_b32 a253, v11\n"
        "v_accvgpr_write_b32 a254, v11\n"
        "v_accvgpr_write_b32 a255, v11\n"
        "v_lshl_or_b32 v10, v16, 10, v18\n"
        "s_mul_i32 s31, s42, 6144\n"
        "s_add_u32 s31, s31, 73728\n"
        "s_lshl_b32 s35, s51, 9\n"
        "s_add_u32 s35, s35, 73728\n"
        "v_lshl_or_b32 v11, v16, 2, v18\n"
        "v_add_u32 v247, s35, v10\n"
        "v_add_u32 v245, s31, v11\n"
        "v_mul_u32_u24 v10, 384, v16\n"
        "s_movk_i32 s45, 1728\n"
        "v_lshrrev_b32 v235, 2, v15\n"
        "v_lshrrev_b32 v254, 1, v15\n"
        "v_and_b32 v11, 1, v1\n"
        "v_mad_u32_u24 v235, v235, 48, v10\n"
        "v_and_b32 v248, 1, v254\n"
        "v_mad_u32_u24 v235, v248, s45, v235\n"
        "v_mul_u32_u24 v248, 36, v11\n"
        "v_lshlrev_b32 v11, 5, v11\n"
        "v_xor_b32 v254, v254, v248\n"
        "v_add3_u32 v235, v235, v11, s65\n"
        "v_mad_u32_u24 v254, v254, 48, v10\n"
        "v_add3_u32 v254, v254, 16, s65\n"
        "s_lshr_b32 s31, s42, 1\n"
        "s_mul_i32 s35, s31, 36864\n"
        "s_movk_i32 s45, 18048\n"
        "v_mad_u32_u24 v10, v15, 48, v10\n"
        "v_lshrrev_b32 v248, 3, v15\n"
        "v_mad_u32_u24 v10, v248, s45, v10\n"
        "v_add_u32 v248, s35, v10\n"
        "s_lshr_b32 s49, s42, 1\n"
        "s_mul_i32 s49, s49, -192\n"
        "s_add_u32 s49, s49, 128\n"
        "s_mul_i32 s31, s31, 256\n"
        "v_mul_u32_u24 v10, 196608, v16\n"
        "v_mul_u32_u24 v11, 8, v15\n"
        "v_add3_u32 v250, v10, v11, s31\n"
        "s_lshl_b32 s35, s51, 6\n"
        "s_add_u32 s66, s31, s35\n"
        "s_lshl_b32 s35, s42, 5\n"
        "s_sub_u32 s35, s35, s66\n"
        "s_add_u32 s27, s35, 64\n"
        "s_movk_i32 s31, 288\n"
        "s_movk_i32 s35, 768\n"
        "v_mad_u32_u24 v10, v4, s31, v2\n"
        "v_bfe_u32 v11, v10, 6, 1\n"
        "v_add_u32 v10, s66, v10\n"
        "v_mad_i32_i24 v10, v11, s27, v10\n"
        "v_bfe_u32 v11, v4, 1, 1\n"
        "v_bfe_u32 v232, v4, 2, 1\n"
        "v_sub_u32 v11, v11, v232\n"
        "v_mad_i32_i24 v232, v11, s35, v10\n"
        "v_mad_u32_u24 v10, v5, s31, v2\n"
        "v_bfe_u32 v11, v10, 6, 1\n"
        "v_add_u32 v10, s66, v10\n"
        "v_mad_i32_i24 v10, v11, s27, v10\n"
        "v_bfe_u32 v11, v5, 1, 1\n"
        "v_bfe_u32 v233, v5, 2, 1\n"
        "v_sub_u32 v11, v11, v233\n"
        "v_mad_i32_i24 v233, v11, s35, v10\n"
        "v_mad_u32_u24 v10, v6, s31, v2\n"
        "v_bfe_u32 v11, v10, 6, 1\n"
        "v_add_u32 v10, s66, v10\n"
        "v_mad_i32_i24 v10, v11, s27, v10\n"
        "v_bfe_u32 v11, v6, 1, 1\n"
        "v_bfe_u32 v234, v6, 2, 1\n"
        "v_sub_u32 v11, v11, v234\n"
        "v_mad_i32_i24 v234, v11, s35, v10\n"
        "s_mov_b32 s26, 0x3fb8aa3b\n"
        "s_mov_b32 s24, 0x5d800000\n"
        "s_mov_b32 s27, 14\n"
        "s_mov_b32 s28, 98304\n"
        "s_mov_b32 s29, 73728\n"
        "s_mov_b32 s30, 122880\n"
        "s_mov_b32 s45, 0\n"
        "s_mov_b32 s25, 0\n"
        "s_mov_b32 s54, 73728\n"
        "s_mov_b32 s55, 0x20000\n"
        "s_mov_b32 s18, 100663296\n"
        "s_mov_b32 s19, 0x20000\n"
        "s_mov_b32 s56, 36864\n"
        "s_mov_b32 s57, 73728\n"
        "s_mov_b32 s58, 110592\n"
        "s_mov_b32 s59, 147456\n"
        "s_mov_b32 s60, 184320\n"
        "s_mov_b32 s61, 24576\n"
        "s_mov_b32 s62, 61440\n"
        "s_mov_b32 s63, 12288\n"
        "s_mov_b32 s34, 49152\n"
        "s_add_u32 m0, s40, 86016\n"
        "s_sub_u32 s64, s47, 11796480\n"
        "buffer_load_dwordx4 v242, s[8:11], s64 offen lds\n"
        "s_waitcnt vmcnt(9)\n"
        "v_and_b32 v146, 0xffff0000, v144\n"
        "v_and_b32 v147, 0xffff0000, v145\n"
        "v_lshlrev_b32 v144, 16, v144\n"
        "v_lshlrev_b32 v145, 16, v145\n"
        "s_add_u32 m0, s40, 87040\n"
        "v_mul_f32 v146, s26, v146\n"
        "buffer_load_dwordx4 v243, s[8:11], s64 offen lds\n"
        "v_mul_f32 v147, s26, v147\n"
        "v_mul_f32 v144, s26, v144\n"
        "v_mul_f32 v145, s26, v145\n"
        "ds_write2st64_b32 v135, v144, v146 offset0:0 offset1:4\n"
        "ds_write2st64_b32 v135, v145, v147 offset0:8 offset1:12\n"
        "s_cmp_eq_u32 s42, 0\n"
        "s_cbranch_scc1 41f\n"
        "s_cmp_eq_u32 s42, 1\n"
        "s_cbranch_scc1 42f\n"
        "s_cmp_eq_u32 s42, 2\n"
        "s_cbranch_scc1 43f\n"
        "s_cmp_eq_u32 s42, 3\n"
        "s_cbranch_scc1 44f\n"
        "60:\n"
        "s_add_u32 m0, s40, 88064\n"
        "s_nop 0\n"
        "buffer_load_dwordx4 v244, s[8:11], s64 offen lds\n"
        "s_waitcnt lgkmcnt(0)\n"
        "s_waitcnt vmcnt(6)\n"
        "s_barrier\n"
        "s_add_u32 m0, s65, 0\n"
        "ds_read_b128 v[0:3], v140 offset:0\n"
        "buffer_load_dwordx4 v128, s[20:23], 0 offen lds\n"
        "buffer_load_dwordx4 v129, s[20:23], 0 offen offset:1024 lds\n"
        "ds_read_b128 v[4:7], v141 offset:0\n"
        "ds_read_b128 v[16:19], v140 offset:1024\n"
        "ds_read_b128 v[20:23], v141 offset:1024\n"
        "ds_read_b128 v[8:11], v142 offset:0\n"
        "ds_read_b128 v[24:27], v142 offset:1024\n"
        "ds_read_b128 v[12:15], v143 offset:0\n"
        "ds_read_b128 v[28:31], v143 offset:1024\n"
        "ds_read_b128 v[32:35], v140 offset:8192\n"
        "ds_read_b128 v[36:39], v141 offset:8192\n"
        "ds_read_b128 v[40:43], v142 offset:8192\n"
        "s_cmp_eq_u32 s42, 0\n"
        "s_cbranch_scc1 51f\n"
        "s_cmp_eq_u32 s42, 1\n"
        "s_cbranch_scc1 45f\n"
        "s_cmp_eq_u32 s42, 2\n"
        "s_cbranch_scc1 46f\n"
        "s_cmp_eq_u32 s42, 3\n"
        "s_cbranch_scc1 47f\n"
        "s_nop 0\n"
        "s_nop 0\n"
        "s_nop 0\n"
        "s_nop 0\n"
        "s_nop 0\n"
        "s_nop 0\n"
        "s_nop 0\n"
        "s_nop 0\n"
        "s_nop 0\n"
        "s_nop 0\n"
        "s_nop 0\n"
        "s_nop 0\n"
        "s_nop 0\n"
        "s_nop 0\n"
        "s_nop 0\n"
        "s_nop 0\n"
        "s_nop 0\n"
        "61:\n"
        "buffer_load_dwordx4 v130, s[20:23], 0 offen offset:2048 lds\n"
        "s_add_u32 m0, s65, 2688\n"
        "s_waitcnt lgkmcnt(10)\n"
        "v_lshl_add_u32 v0, v0, 2, s67\n"
        "buffer_load_dwordx4 v128, s[20:23], 0 offen offset:384 lds\n"
        "v_lshl_add_u32 v1, v1, 2, s67\n"
        "v_lshl_add_u32 v2, v2, 2, s67\n"
        "v_lshl_add_u32 v3, v3, 2, s67\n"
        "s_waitcnt lgkmcnt(9)\n"
        "v_lshl_add_u32 v4, v4, 2, s67\n"
        "v_lshl_add_u32 v5, v5, 2, s67\n"
        "v_lshl_add_u32 v6, v6, 2, s67\n"
        "v_lshl_add_u32 v7, v7, 2, s67\n"
        "s_waitcnt lgkmcnt(8)\n"
        "v_lshl_add_u32 v16, v16, 2, s67\n"
        "v_lshl_add_u32 v17, v17, 2, s67\n"
        "v_lshl_add_u32 v18, v18, 2, s67\n"
        "v_lshl_add_u32 v19, v19, 2, s67\n"
        "buffer_load_dwordx4 v129, s[20:23], 0 offen offset:1408 lds\n"
        "ds_read_b32 v0, v0\n"
        "ds_read_b32 v1, v1\n"
        "ds_read_b32 v2, v2\n"
        "ds_read_b32 v3, v3\n"
        "ds_read_b32 v4, v4\n"
        "ds_read_b32 v5, v5\n"
        "ds_read_b32 v6, v6\n"
        "ds_read_b32 v7, v7\n"
        "ds_read_b32 v16, v16\n"
        "ds_read_b32 v17, v17\n"
        "ds_read_b32 v18, v18\n"
        "ds_read_b32 v19, v19\n"
        "buffer_load_dwordx4 v130, s[20:23], 0 offen offset:2432 lds\n"
        "s_waitcnt lgkmcnt(15)\n"
        "v_lshl_add_u32 v20, v20, 2, s67\n"
        "v_lshl_add_u32 v21, v21, 2, s67\n"
        "v_lshl_add_u32 v22, v22, 2, s67\n"
        "v_lshl_add_u32 v23, v23, 2, s67\n"
        "v_lshl_add_u32 v8, v8, 2, s67\n"
        "v_lshl_add_u32 v9, v9, 2, s67\n"
        "v_lshl_add_u32 v10, v10, 2, s67\n"
        "v_lshl_add_u32 v11, v11, 2, s67\n"
        "v_lshl_add_u32 v24, v24, 2, s67\n"
        "v_lshl_add_u32 v25, v25, 2, s67\n"
        "v_lshl_add_u32 v26, v26, 2, s67\n"
        "v_lshl_add_u32 v27, v27, 2, s67\n"
        "ds_read_b32 v20, v20\n"
        "ds_read_b32 v21, v21\n"
        "ds_read_b32 v22, v22\n"
        "ds_read_b32 v23, v23\n"
        "ds_read_b32 v8, v8\n"
        "ds_read_b32 v9, v9\n"
        "ds_read_b32 v10, v10\n"
        "ds_read_b32 v11, v11\n"
        "ds_read_b32 v24, v24\n"
        "ds_read_b32 v25, v25\n"
        "ds_read_b32 v26, v26\n"
        "ds_read_b32 v27, v27\n"
        "s_add_u32 m0, s65, 6144\n"
        "v_lshl_add_u32 v12, v12, 2, s67\n"
        "buffer_load_dwordx4 v128, s[20:23], s56 offen lds\n"
        "v_lshl_add_u32 v13, v13, 2, s67\n"
        "v_lshl_add_u32 v14, v14, 2, s67\n"
        "v_lshl_add_u32 v15, v15, 2, s67\n"
        "v_lshl_add_u32 v28, v28, 2, s67\n"
        "v_lshl_add_u32 v29, v29, 2, s67\n"
        "v_lshl_add_u32 v30, v30, 2, s67\n"
        "v_lshl_add_u32 v31, v31, 2, s67\n"
        "s_waitcnt lgkmcnt(15)\n"
        "v_lshl_add_u32 v32, v32, 2, s67\n"
        "v_lshl_add_u32 v33, v33, 2, s67\n"
        "v_lshl_add_u32 v34, v34, 2, s67\n"
        "v_lshl_add_u32 v35, v35, 2, s67\n"
        "ds_read_b32 v12, v12\n"
        "ds_read_b32 v13, v13\n"
        "ds_read_b32 v14, v14\n"
        "ds_read_b32 v15, v15\n"
        "ds_read_b32 v28, v28\n"
        "ds_read_b32 v29, v29\n"
        "ds_read_b32 v30, v30\n"
        "ds_read_b32 v31, v31\n"
        "ds_read_b32 v32, v32\n"
        "ds_read_b32 v33, v33\n"
        "ds_read_b32 v34, v34\n"
        "ds_read_b32 v35, v35\n"
        "buffer_load_dwordx4 v129, s[20:23], s56 offen offset:1024 lds\n"
        "v_lshl_add_u32 v36, v36, 2, s67\n"
        "v_lshl_add_u32 v37, v37, 2, s67\n"
        "v_lshl_add_u32 v38, v38, 2, s67\n"
        "v_lshl_add_u32 v39, v39, 2, s67\n"
        "v_lshl_add_u32 v40, v40, 2, s67\n"
        "v_lshl_add_u32 v41, v41, 2, s67\n"
        "v_lshl_add_u32 v42, v42, 2, s67\n"
        "v_lshl_add_u32 v43, v43, 2, s67\n"
        "ds_read_b32 v36, v36\n"
        "ds_read_b32 v37, v37\n"
        "ds_read_b32 v38, v38\n"
        "ds_read_b32 v39, v39\n"
        "ds_read_b32 v40, v40\n"
        "ds_read_b32 v41, v41\n"
        "ds_read_b32 v42, v42\n"
        "ds_read_b32 v43, v43\n"
        "buffer_load_dwordx4 v130, s[20:23], s56 offen offset:2048 lds\n"
        "s_waitcnt vmcnt(9)\n"
        "s_barrier\n"
        "s_waitcnt vmcnt(6)\n"
        "ds_read_b128 a[212:215], v137 offset:0\n"
        "ds_read_b128 a[208:211], v137 offset:768\n"
        "ds_read_b128 a[204:207], v137 offset:32\n"
        "ds_read_b128 a[200:203], v137 offset:800\n"
        "ds_read_b128 a[196:199], v137 offset:64\n"
        "ds_read_b128 a[192:195], v137 offset:832\n"
        "s_add_u32 m0, s65, 8832\n"
        "s_nop 0\n"
        "buffer_load_dwordx4 v128, s[20:23], s56 offen offset:384 lds\n"
        "s_waitcnt lgkmcnt(5)\n"
        "v_mfma_f32_16x16x32_bf16 v[112:115], a[212:215], v[148:151], 0\n"
        "s_waitcnt lgkmcnt(4)\n"
        "ds_read_b128 a[216:219], v240 offset:0\n"
        "ds_read_b128 a[220:223], v240 offset:1536\n"
        "ds_read_b128 a[224:227], v240 offset:12288\n"
        "ds_read_b128 a[228:231], v240 offset:13824\n"
        "v_mfma_f32_16x16x32_bf16 v[116:119], a[208:211], v[148:151], 0\n"
        "s_waitcnt lgkmcnt(7)\n"
        "v_mfma_f32_16x16x32_bf16 v[120:123], a[204:207], v[148:151], 0\n"
        "s_waitcnt lgkmcnt(6)\n"
        "v_mfma_f32_16x16x32_bf16 v[124:127], a[200:203], v[148:151], 0\n"
        "s_waitcnt lgkmcnt(5)\n"
        "buffer_load_dwordx4 v129, s[20:23], s56 offen offset:1408 lds\n"
        "v_mfma_f32_16x16x32_bf16 v[96:99], a[196:199], v[148:151], 0\n"
        "s_waitcnt lgkmcnt(4)\n"
        "ds_read_b128 v[44:47], v143 offset:8192\n"
        "v_cvt_pk_bf16_f32 v112, v112, v113\n"
        "v_mfma_f32_16x16x32_bf16 v[100:103], a[192:195], v[148:151], 0\n"
        "s_waitcnt lgkmcnt(4)\n"
        "s_waitcnt vmcnt(5)\n"
        "ds_read_b64_tr_b16 a[72:73], v255 offset:3072\n"
        "ds_read_b64_tr_b16 a[74:75], v255 offset:3840\n"
        "v_accvgpr_write_b32 a0, v112\n"
        "v_cvt_pk_bf16_f32 v114, v114, v115\n"
        "v_accvgpr_write_b32 a1, v114\n"
        "v_cvt_pk_bf16_f32 v116, v116, v117\n"
        "v_accvgpr_write_b32 a2, v116\n"
        "v_cvt_pk_bf16_f32 v118, v118, v119\n"
        "v_accvgpr_write_b32 a3, v118\n"
        "s_nop 1\n"
        "v_mfma_f32_16x16x32_bf16 v[156:159], a[0:3], a[216:219], 0\n"
        "s_waitcnt lgkmcnt(3)\n"
        "ds_read_b64_tr_b16 a[96:97], v255 offset:3104\n"
        "ds_read_b64_tr_b16 a[98:99], v255 offset:3872\n"
        "v_mfma_f32_16x16x32_bf16 v[176:179], a[0:3], a[220:223], 0\n"
        "buffer_load_dwordx4 v130, s[20:23], s56 offen offset:2432 lds\n"
        "ds_read_b64_tr_b16 a[120:121], v255 offset:3136\n"
        "ds_read_b64_tr_b16 a[122:123], v255 offset:3904\n"
        "v_mfma_f32_16x16x32_bf16 v[196:199], a[0:3], a[224:227], 0\n"
        "s_add_u32 m0, s65, 12288\n"
        "v_cvt_pk_bf16_f32 v120, v120, v121\n"
        "v_accvgpr_write_b32 a24, v120\n"
        "v_mfma_f32_16x16x32_bf16 v[216:219], a[0:3], a[228:231], 0\n"
        "s_waitcnt lgkmcnt(4)\n"
        "v_cvt_pk_bf16_f32 v122, v122, v123\n"
        "v_accvgpr_write_b32 a25, v122\n"
        "v_mfma_f32_16x16x32_bf16 v[152:155], a[72:75], a[216:219], 0\n"
        "buffer_load_dwordx4 v128, s[20:23], s57 offen lds\n"
        "s_waitcnt vmcnt(4)\n"
        "ds_read_b128 a[188:191], v137 offset:6144\n"
        "v_mfma_f32_16x16x32_bf16 v[172:175], a[72:75], a[220:223], 0\n"
        "ds_read_b128 a[184:187], v137 offset:6912\n"
        "ds_read_b128 a[180:183], v137 offset:6176\n"
        "v_cvt_pk_bf16_f32 v124, v124, v125\n"
        "v_accvgpr_write_b32 a26, v124\n"
        "v_cvt_pk_bf16_f32 v126, v126, v127\n"
        "v_accvgpr_write_b32 a27, v126\n"
        "s_nop 1\n"
        "v_mfma_f32_16x16x32_bf16 v[224:227], a[24:27], a[228:231], 0\n"
        "buffer_load_dwordx4 v129, s[20:23], s57 offen offset:1024 lds\n"
        "ds_read_b128 a[176:179], v137 offset:6944\n"
        "ds_read_b128 a[172:175], v137 offset:6208\n"
        "v_mfma_f32_16x16x32_bf16 v[204:207], a[24:27], a[224:227], 0\n"
        "ds_read_b128 a[168:171], v137 offset:6976\n"
        "ds_read_b128 a[232:235], v241 offset:0\n"
        "v_mfma_f32_16x16x32_bf16 v[184:187], a[24:27], a[220:223], 0\n"
        "ds_read_b128 a[236:239], v241 offset:1536\n"
        "ds_read_b128 a[240:243], v241 offset:12288\n"
        "v_mfma_f32_16x16x32_bf16 v[164:167], a[24:27], a[216:219], 0\n"
        "buffer_load_dwordx4 v130, s[20:23], s57 offen offset:2048 lds\n"
        "v_mfma_f32_16x16x32_bf16 v[192:195], a[72:75], a[224:227], 0\n"
        "s_add_u32 m0, s65, 14976\n"
        "ds_read_b128 a[244:247], v241 offset:13824\n"
        "v_cvt_pk_bf16_f32 v96, v96, v97\n"
        "v_mfma_f32_16x16x32_bf16 v[212:215], a[72:75], a[228:231], 0\n"
        "v_accvgpr_write_b32 a48, v96\n"
        "v_cvt_pk_bf16_f32 v98, v98, v99\n"
        "v_accvgpr_write_b32 a49, v98\n"
        "v_cvt_pk_bf16_f32 v100, v100, v101\n"
        "v_accvgpr_write_b32 a50, v100\n"
        "v_cvt_pk_bf16_f32 v102, v102, v103\n"
        "v_accvgpr_write_b32 a51, v102\n"
        "s_nop 1\n"
        "v_mfma_f32_16x16x32_bf16 v[160:163], a[48:51], a[216:219], 0\n"
        "v_lshl_add_u32 v44, v44, 2, s67\n"
        "v_lshl_add_u32 v45, v45, 2, s67\n"
        "v_mfma_f32_16x16x32_bf16 v[180:183], a[48:51], a[220:223], 0\n"
        "s_waitcnt lgkmcnt(3)\n"
        "ds_read_b32 v44, v44\n"
        "ds_read_b32 v45, v45\n"
        "v_mfma_f32_16x16x32_bf16 v[104:107], a[188:191], v[148:151], 0\n"
        "buffer_load_dwordx4 v128, s[20:23], s57 offen offset:384 lds\n"
        "v_mfma_f32_16x16x32_bf16 v[200:203], a[48:51], a[224:227], 0\n"
        "v_lshl_add_u32 v46, v46, 2, s67\n"
        "v_lshl_add_u32 v47, v47, 2, s67\n"
        "v_mfma_f32_16x16x32_bf16 v[108:111], a[184:187], v[148:151], 0\n"
        "ds_read_b32 v46, v46\n"
        "ds_read_b32 v47, v47\n"
        "v_mfma_f32_16x16x32_bf16 v[220:223], a[48:51], a[228:231], 0\n"
        "buffer_load_dwordx4 v129, s[20:23], s57 offen offset:1408 lds\n"
        "v_mfma_f32_16x16x32_bf16 v[88:91], a[96:99], a[228:231], 0\n"
        "ds_read_b128 v[48:51], v140 offset:9216\n"
        "v_cvt_pk_bf16_f32 v104, v104, v105\n"
        "v_mfma_f32_16x16x32_bf16 v[80:83], a[96:99], a[224:227], 0\n"
        "s_waitcnt vmcnt(5)\n"
        "ds_read_b64_tr_b16 a[76:77], v255 offset:9216\n"
        "ds_read_b64_tr_b16 a[78:79], v255 offset:9984\n"
        "v_mfma_f32_16x16x32_bf16 v[68:71], a[96:99], a[220:223], 0\n"
        "ds_read_b64_tr_b16 a[100:101], v255 offset:9248\n"
        "ds_read_b64_tr_b16 a[102:103], v255 offset:10016\n"
        "v_mfma_f32_16x16x32_bf16 v[64:67], a[96:99], a[216:219], 0\n"
        "buffer_load_dwordx4 v130, s[20:23], s57 offen offset:2432 lds\n"
        "ds_read_b64_tr_b16 a[124:125], v255 offset:9280\n"
        "ds_read_b64_tr_b16 a[126:127], v255 offset:10048\n"
        "v_mfma_f32_16x16x32_bf16 v[112:115], a[180:183], v[148:151], 0\n"
        "s_add_u32 m0, s65, 0\n"
        "v_accvgpr_write_b32 a4, v104\n"
        "v_cvt_pk_bf16_f32 v106, v106, v107\n"
        "v_mfma_f32_16x16x32_bf16 v[116:119], a[176:179], v[148:151], 0\n"
        "v_accvgpr_write_b32 a5, v106\n"
        "v_cvt_pk_bf16_f32 v108, v108, v109\n"
        "v_mfma_f32_16x16x32_bf16 v[72:75], a[120:123], a[216:219], 0\n"
        "buffer_load_dwordx4 v128, s[20:23], s58 offen lds\n"
        "v_mfma_f32_16x16x32_bf16 v[76:79], a[120:123], a[220:223], 0\n"
        "v_accvgpr_write_b32 a6, v108\n"
        "v_cvt_pk_bf16_f32 v110, v110, v111\n"
        "v_mfma_f32_16x16x32_bf16 v[84:87], a[120:123], a[224:227], 0\n"
        "v_accvgpr_write_b32 a7, v110\n"
        "v_cvt_pk_bf16_f32 v112, v112, v113\n"
        "v_mfma_f32_16x16x32_bf16 v[92:95], a[120:123], a[228:231], 0\n"
        "v_accvgpr_write_b32 a28, v112\n"
        "v_cvt_pk_bf16_f32 v114, v114, v115\n"
        "v_mfma_f32_16x16x32_bf16 v[156:159], a[4:7], a[232:235], v[156:159]\n"
        "s_waitcnt lgkmcnt(0)\n"
        "v_lshl_add_u32 v48, v48, 2, s67\n"
        "v_lshl_add_u32 v49, v49, 2, s67\n"
        "v_mfma_f32_16x16x32_bf16 v[176:179], a[4:7], a[236:239], v[176:179]\n"
        "buffer_load_dwordx4 v129, s[20:23], s58 offen offset:1024 lds\n"
        "v_mfma_f32_16x16x32_bf16 v[196:199], a[4:7], a[240:243], v[196:199]\n"
        "ds_read_b32 v48, v48\n"
        "ds_read_b32 v49, v49\n"
        "v_mfma_f32_16x16x32_bf16 v[216:219], a[4:7], a[244:247], v[216:219]\n"
        ".long 0xbec200ff\n"
        "41:\n"
        "s_branch 60b\n"
        "v_lshl_add_u32 v50, v50, 2, s67\n"
        "v_lshl_add_u32 v51, v51, 2, s67\n"
        "v_mfma_f32_16x16x32_bf16 v[120:123], a[172:175], v[148:151], 0\n"
        "buffer_load_dwordx4 v130, s[20:23], s58 offen offset:2048 lds\n"
        "v_mfma_f32_16x16x32_bf16 v[124:127], a[168:171], v[148:151], 0\n"
        "s_add_u32 m0, s65, 2688\n"
        "ds_read_b32 v50, v50\n"
        "ds_read_b32 v51, v51\n"
        "v_mfma_f32_16x16x32_bf16 v[152:155], a[76:79], a[232:235], v[152:155]\n"
        "ds_read_b128 a[248:251], v240 offset:128\n"
        "v_accvgpr_write_b32 a29, v114\n"
        "v_mfma_f32_16x16x32_bf16 v[172:175], a[76:79], a[236:239], v[172:175]\n"
        "s_waitcnt vmcnt(6)\n"
        "ds_read_b128 a[212:215], v137 offset:12288\n"
        "ds_read_b128 a[208:211], v137 offset:13056\n"
        "v_mfma_f32_16x16x32_bf16 v[192:195], a[76:79], a[240:243], v[192:195]\n"
        "buffer_load_dwordx4 v128, s[20:23], s58 offen offset:384 lds\n"
        "ds_read_b128 a[204:207], v137 offset:12320\n"
        "v_cvt_pk_bf16_f32 v116, v116, v117\n"
        "v_mfma_f32_16x16x32_bf16 v[212:215], a[76:79], a[244:247], v[212:215]\n"
        "ds_read_b128 a[200:203], v137 offset:13088\n"
        "ds_read_b128 a[196:199], v137 offset:12352\n"
        "v_accvgpr_write_b32 a30, v116\n"
        "v_cvt_pk_bf16_f32 v118, v118, v119\n"
        "v_accvgpr_write_b32 a31, v118\n"
        "s_nop 1\n"
        "v_mfma_f32_16x16x32_bf16 v[224:227], a[28:31], a[244:247], v[224:227]\n"
        "ds_read_b128 a[192:195], v137 offset:13120\n"
        "ds_read_b128 a[216:219], v240 offset:1664\n"
        "v_mfma_f32_16x16x32_bf16 v[204:207], a[28:31], a[240:243], v[204:207]\n"
        "buffer_load_dwordx4 v129, s[20:23], s58 offen offset:1408 lds\n"
        "v_mfma_f32_16x16x32_bf16 v[184:187], a[28:31], a[236:239], v[184:187]\n"
        "ds_read_b128 a[220:223], v240 offset:12416\n"
        "ds_read_b128 a[224:227], v240 offset:13952\n"
        "v_mfma_f32_16x16x32_bf16 v[164:167], a[28:31], a[232:235], v[164:167]\n"
        "s_waitcnt lgkmcnt(7)\n"
        "v_cvt_pk_bf16_f32 v120, v120, v121\n"
        "v_accvgpr_write_b32 a52, v120\n"
        "v_mfma_f32_16x16x32_bf16 v[96:99], a[212:215], v[148:151], 0\n"
        "v_cvt_pk_bf16_f32 v122, v122, v123\n"
        "v_accvgpr_write_b32 a53, v122\n"
        "v_mfma_f32_16x16x32_bf16 v[100:103], a[208:211], v[148:151], 0\n"
        "buffer_load_dwordx4 v130, s[20:23], s58 offen offset:2432 lds\n"
        "v_mfma_f32_16x16x32_bf16 v[88:91], a[100:103], a[244:247], v[88:91]\n"
        "s_add_u32 m0, s65, 6144\n"
        "v_cvt_pk_bf16_f32 v124, v124, v125\n"
        "v_accvgpr_write_b32 a54, v124\n"
        "v_mfma_f32_16x16x32_bf16 v[80:83], a[100:103], a[240:243], v[80:83]\n"
        "v_cvt_pk_bf16_f32 v126, v126, v127\n"
        "v_accvgpr_write_b32 a55, v126\n"
        "v_mfma_f32_16x16x32_bf16 v[68:71], a[100:103], a[236:239], v[68:71]\n"
        "v_cvt_pk_bf16_f32 v96, v96, v97\n"
        "v_accvgpr_write_b32 a8, v96\n"
        "v_mfma_f32_16x16x32_bf16 v[64:67], a[100:103], a[232:235], v[64:67]\n"
        "buffer_load_dwordx4 v128, s[20:23], s59 offen lds\n"
        "v_mfma_f32_16x16x32_bf16 v[160:163], a[52:55], a[232:235], v[160:163]\n"
        "s_waitcnt vmcnt(7)\n"
        "ds_read_b64_tr_b16 a[80:81], v255 offset:15360\n"
        "ds_read_b64_tr_b16 a[82:83], v255 offset:16128\n"
        "v_mfma_f32_16x16x32_bf16 v[180:183], a[52:55], a[236:239], v[180:183]\n"
        "ds_read_b64_tr_b16 a[104:105], v255 offset:15392\n"
        "ds_read_b64_tr_b16 a[106:107], v255 offset:16160\n"
        "v_mfma_f32_16x16x32_bf16 v[200:203], a[52:55], a[240:243], v[200:203]\n"
        "ds_read_b64_tr_b16 a[128:129], v255 offset:15424\n"
        "ds_read_b64_tr_b16 a[130:131], v255 offset:16192\n"
        "v_mfma_f32_16x16x32_bf16 v[220:223], a[52:55], a[244:247], v[220:223]\n"
        "s_waitcnt lgkmcnt(6)\n"
        "v_cvt_pk_bf16_f32 v98, v98, v99\n"
        "v_accvgpr_write_b32 a9, v98\n"
        "v_mfma_f32_16x16x32_bf16 v[104:107], a[204:207], v[148:151], 0\n"
        "buffer_load_dwordx4 v129, s[20:23], s59 offen offset:1024 lds\n"
        "v_mfma_f32_16x16x32_bf16 v[108:111], a[200:203], v[148:151], 0\n"
        "v_cvt_pk_bf16_f32 v100, v100, v101\n"
        "v_accvgpr_write_b32 a10, v100\n"
        "v_mfma_f32_16x16x32_bf16 v[72:75], a[124:127], a[232:235], v[72:75]\n"
        "v_cvt_pk_bf16_f32 v102, v102, v103\n"
        "v_accvgpr_write_b32 a11, v102\n"
        "v_mfma_f32_16x16x32_bf16 v[76:79], a[124:127], a[236:239], v[76:79]\n"
        "buffer_load_dwordx4 v130, s[20:23], s59 offen offset:2048 lds\n"
        "v_mfma_f32_16x16x32_bf16 v[84:87], a[124:127], a[240:243], v[84:87]\n"
        "s_add_u32 m0, s65, 8832\n"
        "v_cvt_pk_bf16_f32 v104, v104, v105\n"
        "v_accvgpr_write_b32 a32, v104\n"
        "v_mfma_f32_16x16x32_bf16 v[92:95], a[124:127], a[244:247], v[92:95]\n"
        "s_waitcnt vmcnt(6)\n"
        "ds_read_b128 a[188:191], v137 offset:0\n"
        "ds_read_b128 a[184:187], v137 offset:768\n"
        "v_mfma_f32_16x16x32_bf16 v[156:159], a[8:11], a[248:251], v[156:159]\n"
        "buffer_load_dwordx4 v128, s[20:23], s59 offen offset:384 lds\n"
        "ds_read_b128 a[180:183], v137 offset:32\n"
        "v_mfma_f32_16x16x32_bf16 v[176:179], a[8:11], a[216:219], v[176:179]\n"
        "ds_read_b128 a[176:179], v137 offset:800\n"
        "ds_read_b128 a[172:175], v137 offset:64\n"
        "v_mfma_f32_16x16x32_bf16 v[196:199], a[8:11], a[220:223], v[196:199]\n"
        "ds_read_b128 a[168:171], v137 offset:832\n"
        "v_cvt_pk_bf16_f32 v106, v106, v107\n"
        "v_mfma_f32_16x16x32_bf16 v[216:219], a[8:11], a[224:227], v[216:219]\n"
        "v_accvgpr_write_b32 a33, v106\n"
        "v_cvt_pk_bf16_f32 v108, v108, v109\n"
        "v_mfma_f32_16x16x32_bf16 v[112:115], a[196:199], v[148:151], 0\n"
        "buffer_load_dwordx4 v129, s[20:23], s59 offen offset:1408 lds\n"
        "v_mfma_f32_16x16x32_bf16 v[116:119], a[192:195], v[148:151], 0\n"
        "s_waitcnt lgkmcnt(4)\n"
        "v_accvgpr_write_b32 a34, v108\n"
        "v_cvt_pk_bf16_f32 v110, v110, v111\n"
        "v_mfma_f32_16x16x32_bf16 v[152:155], a[80:83], a[248:251], v[152:155]\n"
        "ds_read_b128 a[228:231], v241 offset:128\n"
        "v_accvgpr_write_b32 a35, v110\n"
        "v_mfma_f32_16x16x32_bf16 v[172:175], a[80:83], a[216:219], v[172:175]\n"
        "ds_read_b128 a[232:235], v241 offset:1664\n"
        "v_cvt_pk_bf16_f32 v112, v112, v113\n"
        "v_mfma_f32_16x16x32_bf16 v[192:195], a[80:83], a[220:223], v[192:195]\n"
        "buffer_load_dwordx4 v130, s[20:23], s59 offen offset:2432 lds\n"
        "v_mfma_f32_16x16x32_bf16 v[212:215], a[80:83], a[224:227], v[212:215]\n"
        "s_add_u32 m0, s65, 12288\n"
        "ds_read_b128 a[236:239], v241 offset:12416\n"
        "ds_read_b128 a[240:243], v241 offset:13952\n"
        "v_mfma_f32_16x16x32_bf16 v[224:227], a[32:35], a[224:227], v[224:227]\n"
        "ds_read_b128 v[52:55], v141 offset:9216\n"
        "v_accvgpr_write_b32 a56, v112\n"
        "v_mfma_f32_16x16x32_bf16 v[204:207], a[32:35], a[220:223], v[204:207]\n"
        "v_cvt_pk_bf16_f32 v114, v114, v115\n"
        "v_accvgpr_write_b32 a57, v114\n"
        "v_mfma_f32_16x16x32_bf16 v[184:187], a[32:35], a[216:219], v[184:187]\n"
        "buffer_load_dwordx4 v128, s[20:23], s60 offen lds\n"
        "v_mfma_f32_16x16x32_bf16 v[164:167], a[32:35], a[248:251], v[164:167]\n"
        "v_cvt_pk_bf16_f32 v116, v116, v117\n"
        "v_accvgpr_write_b32 a58, v116\n"
        "v_mfma_f32_16x16x32_bf16 v[120:123], a[188:191], v[148:151], 0\n"
        "s_waitcnt vmcnt(7)\n"
        "ds_read_b64_tr_b16 a[84:85], v255 offset:3072\n"
        "ds_read_b64_tr_b16 a[86:87], v255 offset:3840\n"
        "v_mfma_f32_16x16x32_bf16 v[124:127], a[184:187], v[148:151], 0\n"
        "ds_read_b64_tr_b16 a[108:109], v255 offset:3104\n"
        "ds_read_b64_tr_b16 a[110:111], v255 offset:3872\n"
        "v_mfma_f32_16x16x32_bf16 v[88:91], a[104:107], a[224:227], v[88:91]\n"
        "buffer_load_dwordx4 v129, s[20:23], s60 offen offset:1024 lds\n"
        "ds_read_b64_tr_b16 a[132:133], v255 offset:3136\n"
        "ds_read_b64_tr_b16 a[134:135], v255 offset:3904\n"
        "v_mfma_f32_16x16x32_bf16 v[80:83], a[104:107], a[220:223], v[80:83]\n"
        "v_cvt_pk_bf16_f32 v118, v118, v119\n"
        "v_accvgpr_write_b32 a59, v118\n"
        "v_mfma_f32_16x16x32_bf16 v[68:71], a[104:107], a[216:219], v[68:71]\n"
        "v_cvt_pk_bf16_f32 v120, v120, v121\n"
        "v_accvgpr_write_b32 a12, v120\n"
        "v_mfma_f32_16x16x32_bf16 v[64:67], a[104:107], a[248:251], v[64:67]\n"
        "buffer_load_dwordx4 v130, s[20:23], s60 offen offset:2048 lds\n"
        "v_mfma_f32_16x16x32_bf16 v[160:163], a[56:59], a[248:251], v[160:163]\n"
        "s_add_u32 m0, s65, 14976\n"
        "v_cvt_pk_bf16_f32 v122, v122, v123\n"
        "v_accvgpr_write_b32 a13, v122\n"
        "v_mfma_f32_16x16x32_bf16 v[180:183], a[56:59], a[216:219], v[180:183]\n"
        "s_waitcnt lgkmcnt(2)\n"
        "v_lshl_add_u32 v52, v52, 2, s67\n"
        "v_lshl_add_u32 v53, v53, 2, s67\n"
        "v_mfma_f32_16x16x32_bf16 v[200:203], a[56:59], a[220:223], v[200:203]\n"
        "ds_read_b32 v52, v52\n"
        "ds_read_b32 v53, v53\n"
        "v_mfma_f32_16x16x32_bf16 v[220:223], a[56:59], a[224:227], v[220:223]\n"
        "v_lshl_add_u32 v54, v54, 2, s67\n"
        "v_lshl_add_u32 v55, v55, 2, s67\n"
        "v_mfma_f32_16x16x32_bf16 v[96:99], a[180:183], v[148:151], 0\n"
        "buffer_load_dwordx4 v128, s[20:23], s60 offen offset:384 lds\n"
        "v_mfma_f32_16x16x32_bf16 v[100:103], a[176:179], v[148:151], 0\n"
        "ds_read_b32 v54, v54\n"
        "ds_read_b32 v55, v55\n"
        "v_mfma_f32_16x16x32_bf16 v[72:75], a[128:131], a[248:251], v[72:75]\n"
        "v_cvt_pk_bf16_f32 v124, v124, v125\n"
        "v_accvgpr_write_b32 a14, v124\n"
        "v_mfma_f32_16x16x32_bf16 v[76:79], a[128:131], a[216:219], v[76:79]\n"
        "buffer_load_dwordx4 v129, s[20:23], s60 offen offset:1408 lds\n"
        "v_mfma_f32_16x16x32_bf16 v[84:87], a[128:131], a[220:223], v[84:87]\n"
        "v_cvt_pk_bf16_f32 v126, v126, v127\n"
        "v_accvgpr_write_b32 a15, v126\n"
        "v_mfma_f32_16x16x32_bf16 v[92:95], a[128:131], a[224:227], v[92:95]\n"
        "v_cvt_pk_bf16_f32 v96, v96, v97\n"
        "v_accvgpr_write_b32 a36, v96\n"
        "v_mfma_f32_16x16x32_bf16 v[156:159], a[12:15], a[228:231], v[156:159]\n"
        "s_waitcnt vmcnt(8)\n"
        "ds_read_b128 a[212:215], v137 offset:6144\n"
        "ds_read_b128 a[208:211], v137 offset:6912\n"
        "v_mfma_f32_16x16x32_bf16 v[176:179], a[12:15], a[232:235], v[176:179]\n"
        "buffer_load_dwordx4 v130, s[20:23], s60 offen offset:2432 lds\n"
        "ds_read_b128 a[204:207], v137 offset:6176\n"
        "v_mfma_f32_16x16x32_bf16 v[196:199], a[12:15], a[236:239], v[196:199]\n"
        "ds_read_b128 a[200:203], v137 offset:6944\n"
        "ds_read_b128 a[196:199], v137 offset:6208\n"
        "v_mfma_f32_16x16x32_bf16 v[216:219], a[12:15], a[240:243], v[216:219]\n"
        "ds_read_b128 a[192:195], v137 offset:6976\n"
        "v_cvt_pk_bf16_f32 v98, v98, v99\n"
        "v_mfma_f32_16x16x32_bf16 v[104:107], a[172:175], v[148:151], 0\n"
        "v_accvgpr_write_b32 a37, v98\n"
        "v_cvt_pk_bf16_f32 v100, v100, v101\n"
        "v_mfma_f32_16x16x32_bf16 v[108:111], a[168:171], v[148:151], 0\n"
        "v_accvgpr_write_b32 a38, v100\n"
        "v_cvt_pk_bf16_f32 v102, v102, v103\n"
        "v_mfma_f32_16x16x32_bf16 v[152:155], a[84:87], a[228:231], v[152:155]\n"
        "ds_read_b128 a[244:247], v240 offset:256\n"
        "v_accvgpr_write_b32 a39, v102\n"
        "v_mfma_f32_16x16x32_bf16 v[172:175], a[84:87], a[232:235], v[172:175]\n"
        "s_mov_b32 m0, s48\n"
        "ds_read_b128 a[248:251], v240 offset:1792\n"
        "v_cvt_pk_bf16_f32 v104, v104, v105\n"
        "v_mfma_f32_16x16x32_bf16 v[192:195], a[84:87], a[236:239], v[192:195]\n"
        "s_sub_u32 s64, s36, 4194304\n"
        "ds_read_b128 a[216:219], v240 offset:12544\n"
        "v_accvgpr_write_b32 a60, v104\n"
        "v_mfma_f32_16x16x32_bf16 v[212:215], a[84:87], a[240:243], v[212:215]\n"
        "buffer_load_dwordx4 v249, s[12:15], s64 offen lds\n"
        "v_mfma_f32_16x16x32_bf16 v[224:227], a[36:39], a[240:243], v[224:227]\n"
        "ds_read_b128 a[220:223], v240 offset:14080\n"
        "v_cvt_pk_bf16_f32 v106, v106, v107\n"
        "v_mfma_f32_16x16x32_bf16 v[204:207], a[36:39], a[236:239], v[204:207]\n"
        "ds_read_b128 v[56:59], v142 offset:9216\n"
        "v_accvgpr_write_b32 a61, v106\n"
        "v_mfma_f32_16x16x32_bf16 v[184:187], a[36:39], a[232:235], v[184:187]\n"
        "s_waitcnt vmcnt(7)\n"
        "ds_read_b64_tr_b16 a[88:89], v255 offset:9216\n"
        "ds_read_b64_tr_b16 a[90:91], v255 offset:9984\n"
        "v_mfma_f32_16x16x32_bf16 v[164:167], a[36:39], a[228:231], v[164:167]\n"
        "s_waitcnt lgkmcnt(5)\n"
        "ds_read_b64_tr_b16 a[112:113], v255 offset:9248\n"
        "ds_read_b64_tr_b16 a[114:115], v255 offset:10016\n"
        "v_mfma_f32_16x16x32_bf16 v[112:115], a[212:215], v[148:151], 0\n"
        "buffer_load_dwordx4 v253, s[12:15], s64 offen offset:1024 lds\n"
        "ds_read_b64_tr_b16 a[136:137], v255 offset:9280\n"
        "v_mfma_f32_16x16x32_bf16 v[116:119], a[208:211], v[148:151], 0\n"
        "ds_read_b64_tr_b16 a[138:139], v255 offset:10048\n"
        "s_add_u32 m0, s65, 0\n"
        "v_cvt_pk_bf16_f32 v108, v108, v109\n"
        "v_mfma_f32_16x16x32_bf16 v[88:91], a[108:111], a[240:243], v[88:91]\n"
        "s_mov_b32 s64, 768\n"
        "v_accvgpr_write_b32 a62, v108\n"
        "v_cvt_pk_bf16_f32 v110, v110, v111\n"
        "v_mfma_f32_16x16x32_bf16 v[80:83], a[108:111], a[236:239], v[80:83]\n"
        "buffer_load_dwordx4 v128, s[20:23], s64 offen lds\n"
        "v_mfma_f32_16x16x32_bf16 v[68:71], a[108:111], a[232:235], v[68:71]\n"
        "v_accvgpr_write_b32 a63, v110\n"
        "v_cvt_pk_bf16_f32 v112, v112, v113\n"
        "v_mfma_f32_16x16x32_bf16 v[64:67], a[108:111], a[228:231], v[64:67]\n"
        "v_accvgpr_write_b32 a16, v112\n"
        "v_cvt_pk_bf16_f32 v114, v114, v115\n"
        "v_mfma_f32_16x16x32_bf16 v[160:163], a[60:63], a[228:231], v[160:163]\n"
        "v_accvgpr_write_b32 a17, v114\n"
        "v_cvt_pk_bf16_f32 v116, v116, v117\n"
        "v_mfma_f32_16x16x32_bf16 v[180:183], a[60:63], a[232:235], v[180:183]\n"
        "buffer_load_dwordx4 v129, s[20:23], s64 offen offset:1024 lds\n"
        "v_mfma_f32_16x16x32_bf16 v[200:203], a[60:63], a[236:239], v[200:203]\n"
        "ds_read_b128 v[60:63], v143 offset:9216\n"
        "v_accvgpr_write_b32 a18, v116\n"
        "v_mfma_f32_16x16x32_bf16 v[220:223], a[60:63], a[240:243], v[220:223]\n"
        "v_cvt_pk_bf16_f32 v118, v118, v119\n"
        "v_accvgpr_write_b32 a19, v118\n"
        "v_mfma_f32_16x16x32_bf16 v[120:123], a[204:207], v[148:151], 0\n"
        "s_waitcnt lgkmcnt(1)\n"
        "v_lshl_add_u32 v56, v56, 2, s67\n"
        "v_lshl_add_u32 v57, v57, 2, s67\n"
        "v_mfma_f32_16x16x32_bf16 v[124:127], a[200:203], v[148:151], 0\n"
        "buffer_load_dwordx4 v130, s[20:23], s64 offen offset:2048 lds\n"
        "v_mfma_f32_16x16x32_bf16 v[72:75], a[132:135], a[228:231], v[72:75]\n"
        "s_add_u32 m0, s65, 2304\n"
        "ds_read_b32 v56, v56\n"
        "ds_read_b32 v57, v57\n"
        "v_mfma_f32_16x16x32_bf16 v[76:79], a[132:135], a[232:235], v[76:79]\n"
        "v_lshl_add_u32 v58, v58, 2, s67\n"
        "v_lshl_add_u32 v59, v59, 2, s67\n"
        "v_mfma_f32_16x16x32_bf16 v[84:87], a[132:135], a[236:239], v[84:87]\n"
        "ds_read_b32 v58, v58\n"
        "ds_read_b32 v59, v59\n"
        "v_mfma_f32_16x16x32_bf16 v[92:95], a[132:135], a[240:243], v[92:95]\n"
        "buffer_load_dwordx4 v128, s[20:23], s56 offen offset:768 lds\n"
        "v_mfma_f32_16x16x32_bf16 v[156:159], a[16:19], a[244:247], v[156:159]\n"
        "v_cvt_pk_bf16_f32 v120, v120, v121\n"
        "v_accvgpr_write_b32 a40, v120\n"
        "v_mfma_f32_16x16x32_bf16 v[176:179], a[16:19], a[248:251], v[176:179]\n"
        "v_cvt_pk_bf16_f32 v122, v122, v123\n"
        "v_accvgpr_write_b32 a41, v122\n"
        "v_mfma_f32_16x16x32_bf16 v[196:199], a[16:19], a[216:219], v[196:199]\n"
        "v_cvt_pk_bf16_f32 v124, v124, v125\n"
        "v_accvgpr_write_b32 a42, v124\n"
        "v_mfma_f32_16x16x32_bf16 v[216:219], a[16:19], a[220:223], v[216:219]\n"
        "buffer_load_dwordx4 v129, s[20:23], s56 offen offset:1792 lds\n"
        "v_mfma_f32_16x16x32_bf16 v[96:99], a[196:199], v[148:151], 0\n"
        "ds_read_b128 a[224:227], v241 offset:256\n"
        "v_cvt_pk_bf16_f32 v126, v126, v127\n"
        "v_mfma_f32_16x16x32_bf16 v[100:103], a[192:195], v[148:151], 0\n"
        "s_waitcnt vmcnt(10)\n"
        "ds_read_b128 a[188:191], v137 offset:12288\n"
        "ds_read_b128 a[184:187], v137 offset:13056\n"
        "v_mfma_f32_16x16x32_bf16 v[152:155], a[88:91], a[244:247], v[152:155]\n"
        "ds_read_b128 a[180:183], v137 offset:12320\n"
        "ds_read_b128 a[176:179], v137 offset:13088\n"
        "v_mfma_f32_16x16x32_bf16 v[172:175], a[88:91], a[248:251], v[172:175]\n"
        "buffer_load_dwordx4 v130, s[20:23], s56 offen offset:2816 lds\n"
        "ds_read_b128 a[172:175], v137 offset:12352\n"
        "v_mfma_f32_16x16x32_bf16 v[192:195], a[88:91], a[216:219], v[192:195]\n"
        "ds_read_b128 a[168:171], v137 offset:13120\n"
        "s_add_u32 m0, s65, 5376\n"
        "ds_read_b128 a[228:231], v241 offset:1792\n"
        "v_accvgpr_write_b32 a43, v126\n"
        "v_mfma_f32_16x16x32_bf16 v[212:215], a[88:91], a[220:223], v[212:215]\n"
        "ds_read_b128 a[232:235], v241 offset:12544\n"
        "ds_read_b128 a[236:239], v241 offset:14080\n"
        "v_mfma_f32_16x16x32_bf16 v[224:227], a[40:43], a[220:223], v[224:227]\n"
        "s_waitcnt lgkmcnt(9)\n"
        "v_lshl_add_u32 v60, v60, 2, s67\n"
        "v_lshl_add_u32 v61, v61, 2, s67\n"
        "v_lshl_add_u32 v62, v62, 2, s67\n"
        "v_mfma_f32_16x16x32_bf16 v[204:207], a[40:43], a[216:219], v[204:207]\n"
        "buffer_load_dwordx4 v128, s[20:23], s57 offen offset:768 lds\n"
        "v_lshl_add_u32 v63, v63, 2, s67\n"
        "v_mfma_f32_16x16x32_bf16 v[184:187], a[40:43], a[248:251], v[184:187]\n"
        "ds_read_b32 v60, v60\n"
        "ds_read_b32 v61, v61\n"
        "v_mfma_f32_16x16x32_bf16 v[164:167], a[40:43], a[244:247], v[164:167]\n"
        "s_waitcnt lgkmcnt(6)\n"
        "ds_read_b32 v62, v62\n"
        "ds_read_b32 v63, v63\n"
        "v_mfma_f32_16x16x32_bf16 v[104:107], a[188:191], v[148:151], 0\n"
        "v_cvt_pk_bf16_f32 v96, v96, v97\n"
        "v_accvgpr_write_b32 a64, v96\n"
        "v_mfma_f32_16x16x32_bf16 v[108:111], a[184:187], v[148:151], 0\n"
        "buffer_load_dwordx4 v129, s[20:23], s57 offen offset:1792 lds\n"
        "v_mfma_f32_16x16x32_bf16 v[88:91], a[112:115], a[220:223], v[88:91]\n"
        "v_cvt_pk_bf16_f32 v98, v98, v99\n"
        "v_accvgpr_write_b32 a65, v98\n"
        "v_mfma_f32_16x16x32_bf16 v[80:83], a[112:115], a[216:219], v[80:83]\n"
        "v_cvt_pk_bf16_f32 v100, v100, v101\n"
        "v_accvgpr_write_b32 a66, v100\n"
        "v_mfma_f32_16x16x32_bf16 v[68:71], a[112:115], a[248:251], v[68:71]\n"
        "v_cvt_pk_bf16_f32 v102, v102, v103\n"
        "v_accvgpr_write_b32 a67, v102\n"
        "v_mfma_f32_16x16x32_bf16 v[64:67], a[112:115], a[244:247], v[64:67]\n"
        "s_waitcnt vmcnt(10)\n"
        "ds_read_b64_tr_b16 a[92:93], v255 offset:15360\n"
        "ds_read_b64_tr_b16 a[94:95], v255 offset:16128\n"
        "v_mfma_f32_16x16x32_bf16 v[160:163], a[64:67], a[244:247], v[160:163]\n"
        "ds_read_b64_tr_b16 a[116:117], v255 offset:15392\n"
        "ds_read_b64_tr_b16 a[118:119], v255 offset:16160\n"
        "v_mfma_f32_16x16x32_bf16 v[180:183], a[64:67], a[248:251], v[180:183]\n"
        "ds_read_b64_tr_b16 a[140:141], v255 offset:15424\n"
        "ds_read_b64_tr_b16 a[142:143], v255 offset:16192\n"
        "v_mfma_f32_16x16x32_bf16 v[200:203], a[64:67], a[216:219], v[200:203]\n"
        "v_cvt_pk_bf16_f32 v104, v104, v105\n"
        "v_accvgpr_write_b32 a20, v104\n"
        "v_mfma_f32_16x16x32_bf16 v[220:223], a[64:67], a[220:223], v[220:223]\n"
        "v_cvt_pk_bf16_f32 v106, v106, v107\n"
        "v_accvgpr_write_b32 a21, v106\n"
        "v_mfma_f32_16x16x32_bf16 v[112:115], a[180:183], v[148:151], 0\n"
        "v_cvt_pk_bf16_f32 v108, v108, v109\n"
        "v_accvgpr_write_b32 a22, v108\n"
        "v_mfma_f32_16x16x32_bf16 v[116:119], a[176:179], v[148:151], 0\n"
        "v_cvt_pk_bf16_f32 v110, v110, v111\n"
        "v_accvgpr_write_b32 a23, v110\n"
        "v_mfma_f32_16x16x32_bf16 v[72:75], a[136:139], a[244:247], v[72:75]\n"
        "v_mfma_f32_16x16x32_bf16 v[76:79], a[136:139], a[248:251], v[76:79]\n"
        "v_mfma_f32_16x16x32_bf16 v[156:159], a[20:23], a[224:227], v[156:159]\n"
        "s_waitcnt lgkmcnt(0)\n"
        "v_cvt_pk_bf16_f32 v112, v112, v113\n"
        "v_accvgpr_write_b32 a44, v112\n"
        "v_mfma_f32_16x16x32_bf16 v[176:179], a[20:23], a[228:231], v[176:179]\n"
        "v_cvt_pk_bf16_f32 v114, v114, v115\n"
        "v_accvgpr_write_b32 a45, v114\n"
        "v_mfma_f32_16x16x32_bf16 v[196:199], a[20:23], a[232:235], v[196:199]\n"
        "v_cvt_pk_bf16_f32 v116, v116, v117\n"
        "v_accvgpr_write_b32 a46, v116\n"
        "v_cvt_pk_bf16_f32 v156, v156, v157\n"
        "v_mfma_f32_16x16x32_bf16 v[216:219], a[20:23], a[236:239], v[216:219]\n"
        "v_cvt_pk_bf16_f32 v157, v158, v159\n"
        "v_cvt_pk_bf16_f32 v176, v176, v177\n"
        "v_cvt_pk_bf16_f32 v177, v178, v179\n"
        "v_mfma_f32_16x16x32_bf16 v[120:123], a[172:175], v[148:151], 0\n"
        "v_cvt_pk_bf16_f32 v196, v196, v197\n"
        "v_cvt_pk_bf16_f32 v197, v198, v199\n"
        "v_mfma_f32_16x16x32_bf16 v[124:127], a[168:171], v[148:151], 0\n"
        "v_cvt_pk_bf16_f32 v118, v118, v119\n"
        "v_cvt_pk_bf16_f32 v216, v216, v217\n"
        "v_cvt_pk_bf16_f32 v217, v218, v219\n"
        "v_mfma_f32_16x16x32_bf16 v[84:87], a[136:139], a[216:219], v[84:87]\n"
        "v_accvgpr_write_b32 a47, v118\n"
        "v_cvt_pk_bf16_f32 v120, v120, v121\n"
        "v_mfma_f32_16x16x32_bf16 v[92:95], a[136:139], a[220:223], v[92:95]\n"
        "v_accvgpr_write_b32 a68, v120\n"
        "v_cvt_pk_bf16_f32 v122, v122, v123\n"
        "v_mfma_f32_16x16x32_bf16 v[224:227], a[44:47], a[236:239], v[224:227]\n"
        "v_accvgpr_write_b32 a69, v122\n"
        "v_cvt_pk_bf16_f32 v124, v124, v125\n"
        "v_mfma_f32_16x16x32_bf16 v[204:207], a[44:47], a[232:235], v[204:207]\n"
        "v_accvgpr_write_b32 a70, v124\n"
        "v_cvt_pk_bf16_f32 v126, v126, v127\n"
        "v_mfma_f32_16x16x32_bf16 v[184:187], a[44:47], a[228:231], v[184:187]\n"
        "v_accvgpr_write_b32 a71, v126\n"
        "s_nop 0\n"
        "v_cvt_pk_bf16_f32 v218, v224, v225\n"
        "v_mfma_f32_16x16x32_bf16 v[164:167], a[44:47], a[224:227], v[164:167]\n"
        "v_cvt_pk_bf16_f32 v219, v226, v227\n"
        "v_cvt_pk_bf16_f32 v198, v204, v205\n"
        "v_cvt_pk_bf16_f32 v199, v206, v207\n"
        "v_mfma_f32_16x16x32_bf16 v[152:155], a[92:95], a[224:227], v[152:155]\n"
        "v_cvt_pk_bf16_f32 v178, v184, v185\n"
        "v_cvt_pk_bf16_f32 v179, v186, v187\n"
        "v_mfma_f32_16x16x32_bf16 v[64:67], a[116:119], a[224:227], v[64:67]\n"
        "buffer_load_dwordx4 v130, s[20:23], s57 offen offset:2816 lds\n"
        "v_cvt_pk_bf16_f32 v158, v164, v165\n"
        "v_mfma_f32_16x16x32_bf16 v[72:75], a[140:143], a[224:227], v[72:75]\n"
        "s_add_u32 m0, s65, 8448\n"
        "v_cvt_pk_bf16_f32 v159, v166, v167\n"
        "v_cvt_pk_bf16_f32 v152, v152, v153\n"
        "v_mfma_f32_16x16x32_bf16 v[172:175], a[92:95], a[228:231], v[172:175]\n"
        "v_cvt_pk_bf16_f32 v153, v154, v155\n"
        "v_cvt_pk_bf16_f32 v154, v64, v65\n"
        "v_mfma_f32_16x16x32_bf16 v[160:163], a[68:71], a[224:227], v[160:163]\n"
        "v_cvt_pk_bf16_f32 v155, v66, v67\n"
        "v_cvt_pk_bf16_f32 v168, v72, v73\n"
        "v_mfma_f32_16x16x32_bf16 v[180:183], a[68:71], a[228:231], v[180:183]\n"
        "buffer_load_dwordx4 v128, s[20:23], s58 offen offset:768 lds\n"
        "v_mfma_f32_16x16x32_bf16 v[200:203], a[68:71], a[232:235], v[200:203]\n"
        "v_cvt_pk_bf16_f32 v169, v74, v75\n"
        "v_cvt_pk_bf16_f32 v172, v172, v173\n"
        "v_mfma_f32_16x16x32_bf16 v[220:223], a[68:71], a[236:239], v[220:223]\n"
        "v_cvt_pk_bf16_f32 v166, v160, v161\n"
        "v_cvt_pk_bf16_f32 v160, v160, v161\n"
        "v_cvt_pk_bf16_f32 v161, v162, v163\n"
        "v_cvt_pk_bf16_f32 v167, v162, v163\n"
        "v_cvt_pk_bf16_f32 v186, v180, v181\n"
        "v_cvt_pk_bf16_f32 v180, v180, v181\n"
        "v_mfma_f32_16x16x32_bf16 v[68:71], a[116:119], a[228:231], v[68:71]\n"
        "buffer_load_dwordx4 v129, s[20:23], s58 offen offset:1792 lds\n"
        "v_cvt_pk_bf16_f32 v181, v182, v183\n"
        "v_cvt_pk_bf16_f32 v187, v182, v183\n"
        "v_cvt_pk_bf16_f32 v206, v200, v201\n"
        "v_cvt_pk_bf16_f32 v200, v200, v201\n"
        "v_mfma_f32_16x16x32_bf16 v[76:79], a[140:143], a[228:231], v[76:79]\n"
        "s_waitcnt vmcnt(11)\n"
        "s_barrier\n"
        "ds_read_b128 v[162:165], v251 offset:0\n"
        "ds_read_b128 v[182:185], v251 offset:512\n"
        "v_cvt_pk_bf16_f32 v201, v202, v203\n"
        "v_cvt_pk_bf16_f32 v207, v202, v203\n"
        "v_cvt_pk_bf16_f32 v226, v220, v221\n"
        "v_cvt_pk_bf16_f32 v220, v220, v221\n"
        "v_mfma_f32_16x16x32_bf16 v[192:195], a[92:95], a[232:235], v[192:195]\n"
        "ds_read_b128 v[202:205], v251 offset:4096\n"
        "ds_read_b128 a[240:243], v240 offset:0\n"
        "v_cvt_pk_bf16_f32 v221, v222, v223\n"
        "v_cvt_pk_bf16_f32 v227, v222, v223\n"
        "v_cvt_pk_bf16_f32 v173, v174, v175\n"
        "v_mfma_f32_16x16x32_bf16 v[80:83], a[116:119], a[232:235], v[80:83]\n"
        "buffer_load_dwordx4 v130, s[20:23], s58 offen offset:2816 lds\n"
        "v_mfma_f32_16x16x32_bf16 v[84:87], a[140:143], a[232:235], v[84:87]\n"
        "s_add_u32 m0, s65, 11520\n"
        "ds_read_b128 a[244:247], v241 offset:0\n"
        "ds_read_b128 a[248:251], v240 offset:128\n"
        "v_mfma_f32_16x16x32_bf16 v[212:215], a[92:95], a[236:239], v[212:215]\n"
        "v_cvt_pk_bf16_f32 v174, v68, v69\n"
        "v_cvt_pk_bf16_f32 v175, v70, v71\n"
        "v_mfma_f32_16x16x32_bf16 v[88:91], a[116:119], a[236:239], v[88:91]\n"
        "buffer_load_dwordx4 v128, s[20:23], s59 offen offset:768 lds\n"
        "v_mfma_f32_16x16x32_bf16 v[92:95], a[140:143], a[236:239], v[92:95]\n"
        "s_waitcnt vmcnt(10)\n"
        "ds_read_b64_tr_b16 a[144:145], v255 offset:0\n"
        "ds_read_b64_tr_b16 a[146:147], v255 offset:768\n"
        "v_mfma_f32_16x16x32_bf16 v[64:67], v[152:155], v[156:159], v[0:3]\n"
        "s_waitcnt lgkmcnt(4)\n"
        "ds_read_b64_tr_b16 a[168:169], v255 offset:32\n"
        "ds_read_b64_tr_b16 a[170:171], v255 offset:800\n"
        "v_cvt_pk_bf16_f32 v190, v76, v77\n"
        "v_cvt_pk_bf16_f32 v191, v78, v79\n"
        "v_mfma_f32_16x16x32_bf16 v[64:67], v[168:171], v[160:163], v[64:67]\n"
        "ds_read_b64_tr_b16 a[192:193], v255 offset:64\n"
        "ds_read_b64_tr_b16 a[194:195], v255 offset:832\n"
        "v_cvt_pk_bf16_f32 v192, v192, v193\n"
        "v_cvt_pk_bf16_f32 v193, v194, v195\n"
        "v_mfma_f32_16x16x32_bf16 v[96:99], v[172:175], v[156:159], v[4:7]\n"
        "buffer_load_dwordx4 v129, s[20:23], s59 offen offset:1792 lds\n"
        "v_cvt_pk_bf16_f32 v194, v80, v81\n"
        "v_cvt_pk_bf16_f32 v195, v82, v83\n"
        "v_exp_f32 v64, v64\n"
        "v_mfma_f32_16x16x32_bf16 v[96:99], v[188:191], v[164:167], v[96:99]\n"
        "ds_read_b128 v[162:165], v252 offset:0\n"
        "v_exp_f32 v65, v65\n"
        "v_exp_f32 v66, v66\n"
        "v_cvt_pk_bf16_f32 v64, v64, v65\n"
        "v_mfma_f32_16x16x32_bf16 v[72:75], v[152:155], v[176:179], v[16:19]\n"
        "buffer_load_dwordx4 v130, s[20:23], s59 offen offset:2816 lds\n"
        "v_cvt_pk_bf16_f32 v208, v84, v85\n"
        "v_cvt_pk_bf16_f32 v209, v86, v87\n"
        "v_exp_f32 v96, v96\n"
        "v_mfma_f32_16x16x32_bf16 v[72:75], v[168:171], v[180:183], v[72:75]\n"
        "s_add_u32 m0, s65, 14592\n"
        "s_waitcnt vmcnt(9)\n"
        "ds_read_b64_tr_b16 a[148:149], v255 offset:3072\n"
        "ds_read_b64_tr_b16 a[150:151], v255 offset:3840\n"
        "v_exp_f32 v67, v67\n"
        "v_exp_f32 v97, v97\n"
        "v_mfma_f32_16x16x32_bf16 v[100:103], v[172:175], v[176:179], v[20:23]\n"
        "buffer_load_dwordx4 v128, s[20:23], s60 offen offset:768 lds\n"
        "ds_read_b64_tr_b16 a[172:173], v255 offset:3104\n"
        "ds_read_b64_tr_b16 a[174:175], v255 offset:3872\n"
        "v_cvt_pk_bf16_f32 v212, v212, v213\n"
        "v_cvt_pk_bf16_f32 v213, v214, v215\n"
        "v_mfma_f32_16x16x32_bf16 v[100:103], v[188:191], v[184:187], v[100:103]\n"
        "ds_read_b128 v[182:185], v252 offset:512\n"
        "ds_read_b64_tr_b16 a[196:197], v255 offset:3136\n"
        "ds_read_b64_tr_b16 a[198:199], v255 offset:3904\n"
        "v_cvt_pk_bf16_f32 v65, v66, v67\n"
        "v_exp_f32 v98, v98\n"
        "v_mfma_f32_16x16x32_bf16 v[68:71], v[192:195], v[156:159], v[8:11]\n"
        "s_waitcnt lgkmcnt(7)\n"
        "ds_read_b128 a[216:219], v240 offset:1536\n"
        "ds_read_b128 a[220:223], v241 offset:1536\n"
        "v_cvt_pk_bf16_f32 v66, v96, v97\n"
        "v_exp_f32 v72, v72\n"
        "v_cvt_pk_bf16_f32 v214, v88, v89\n"
        "v_mfma_f32_16x16x32_bf16 v[68:71], v[208:211], v[160:163], v[68:71]\n"
        "buffer_load_dwordx4 v129, s[20:23], s60 offen offset:1792 lds\n"
        "s_waitcnt vmcnt(8)\n"
        "ds_read_b64_tr_b16 a[152:153], v255 offset:6144\n"
        "ds_read_b64_tr_b16 a[154:155], v255 offset:6912\n"
        "v_cvt_pk_bf16_f32 v215, v90, v91\n"
        "v_cvt_pk_bf16_f32 v230, v92, v93\n"
        "v_mfma_f32_16x16x32_bf16 v[76:79], v[192:195], v[176:179], v[24:27]\n"
        "s_waitcnt lgkmcnt(6)\n"
        "ds_read_b64_tr_b16 a[176:177], v255 offset:6176\n"
        "ds_read_b64_tr_b16 a[178:179], v255 offset:6944\n"
        "v_exp_f32 v99, v99\n"
        "v_exp_f32 v73, v73\n"
        "v_mfma_f32_16x16x32_bf16 v[76:79], v[208:211], v[180:183], v[76:79]\n"
        "buffer_load_dwordx4 v130, s[20:23], s60 offen offset:2816 lds\n"
        "ds_read_b64_tr_b16 a[200:201], v255 offset:6208\n"
        "ds_read_b64_tr_b16 a[202:203], v255 offset:6976\n"
        "v_cvt_pk_bf16_f32 v231, v94, v95\n"
        "v_cvt_pk_bf16_f32 v67, v98, v99\n"
        "v_mfma_f32_16x16x32_bf16 v[104:107], v[212:215], v[156:159], v[12:15]\n"
        ".long 0xbec200ff\n"
        "42:\n"
        "s_branch 60b\n"
        "s_add_u32 m0, s40, 98304\n"
        "ds_read_b128 a[224:227], v240 offset:1664\n"
        "v_exp_f32 v74, v74\n"
        "v_mfma_f32_16x16x32_bf16 v[104:107], v[228:231], v[164:167], v[104:107]\n"
        "s_sub_u32 s64, s47, 6291456\n"
        "v_cvt_pk_bf16_f32 v72, v72, v73\n"
        "v_exp_f32 v100, v100\n"
        "v_mfma_f32_16x16x32_bf16 v[108:111], v[212:215], v[176:179], v[28:31]\n"
        "buffer_load_dwordx4 v242, s[8:11], s64 offen lds\n"
        "v_mfma_f32_16x16x32_bf16 v[108:111], v[228:231], v[184:187], v[108:111]\n"
        "s_add_u32 m0, s40, 99328\n"
        "v_exp_f32 v75, v75\n"
        "v_mfma_f32_16x16x32_bf16 v[80:83], v[152:155], v[196:199], v[32:35]\n"
        "ds_read_b128 a[228:231], v240 offset:12288\n"
        "v_exp_f32 v101, v101\n"
        "v_mfma_f32_16x16x32_bf16 v[80:83], v[168:171], v[200:203], v[80:83]\n"
        "buffer_load_dwordx4 v243, s[8:11], s64 offen lds\n"
        "v_cvt_pk_bf16_f32 v73, v74, v75\n"
        "v_mfma_f32_16x16x32_bf16 v[112:115], a[240:243], a[144:147], 0\n"
        "s_add_u32 m0, s40, 100352\n"
        "ds_read_b128 a[232:235], v241 offset:12288\n"
        "ds_read_b128 a[236:239], v240 offset:12416\n"
        "v_cvt_pk_bf16_f32 v74, v100, v101\n"
        "v_mfma_f32_16x16x32_bf16 v[112:115], a[244:247], a[148:151], v[112:115]\n"
        "s_waitcnt lgkmcnt(3)\n"
        "v_exp_f32 v102, v102\n"
        "v_mfma_f32_16x16x32_bf16 v[112:115], a[248:251], a[152:155], v[112:115]\n"
        "buffer_load_dwordx4 v244, s[8:11], s64 offen lds\n"
        "v_mfma_f32_16x16x32_bf16 v[116:119], a[248:251], a[176:179], 0\n"
        "s_add_u32 m0, s40, 110592\n"
        "v_exp_f32 v68, v68\n"
        "v_mfma_f32_16x16x32_bf16 v[116:119], a[244:247], a[172:175], v[116:119]\n"
        "s_sub_u32 s64, s47, 5505024\n"
        "v_exp_f32 v103, v103\n"
        "v_mfma_f32_16x16x32_bf16 v[116:119], a[240:243], a[168:171], v[116:119]\n"
        "buffer_load_dwordx4 v242, s[8:11], s64 offen lds\n"
        "v_cvt_pk_bf16_f32 v75, v102, v103\n"
        "v_mfma_f32_16x16x32_bf16 v[96:99], v[172:175], v[196:199], v[36:39]\n"
        "s_add_u32 m0, s40, 111616\n"
        "s_waitcnt vmcnt(10)\n"
        "ds_read_b64_tr_b16 a[156:157], v255 offset:9216\n"
        "ds_read_b64_tr_b16 a[158:159], v255 offset:9984\n"
        "v_mfma_f32_16x16x32_bf16 v[96:99], v[188:191], v[204:207], v[96:99]\n"
        "ds_read_b64_tr_b16 a[180:181], v255 offset:9248\n"
        "ds_read_b64_tr_b16 a[182:183], v255 offset:10016\n"
        "ds_read_b128 v[202:205], v252 offset:4096\n"
        "v_mfma_f32_16x16x32_bf16 v[120:123], a[240:243], a[192:195], 0\n"
        "buffer_load_dwordx4 v243, s[8:11], s64 offen lds\n"
        "ds_read_b64_tr_b16 a[204:205], v255 offset:9280\n"
        "v_mfma_f32_16x16x32_bf16 v[120:123], a[244:247], a[196:199], v[120:123]\n"
        "ds_read_b64_tr_b16 a[206:207], v255 offset:10048\n"
        "s_add_u32 m0, s40, 112640\n"
        "ds_read_b128 a[240:243], v240 offset:13824\n"
        "v_mfma_f32_16x16x32_bf16 v[120:123], a[248:251], a[200:203], v[120:123]\n"
        "s_waitcnt vmcnt(8)\n"
        "ds_read_b64_tr_b16 a[160:161], v255 offset:12288\n"
        "ds_read_b64_tr_b16 a[162:163], v255 offset:13056\n"
        "v_mfma_f32_16x16x32_bf16 v[124:127], a[216:219], a[144:147], 0\n"
        "buffer_load_dwordx4 v244, s[8:11], s64 offen lds\n"
        "ds_read_b64_tr_b16 a[184:185], v255 offset:12320\n"
        "v_mfma_f32_16x16x32_bf16 v[124:127], a[220:223], a[148:151], v[124:127]\n"
        "ds_read_b64_tr_b16 a[186:187], v255 offset:13088\n"
        "s_xor_b32 m0, s48, 8192\n"
        "ds_read_b64_tr_b16 a[208:209], v255 offset:12352\n"
        "ds_read_b64_tr_b16 a[210:211], v255 offset:13120\n"
        "v_mfma_f32_16x16x32_bf16 v[124:127], a[224:227], a[152:155], v[124:127]\n"
        "s_sub_u32 s64, s36, 2097152\n"
        "ds_read_b128 a[244:247], v241 offset:13824\n"
        "ds_read_b128 a[248:251], v240 offset:13952\n"
        "v_mfma_f32_16x16x32_bf16 v[156:159], a[224:227], a[176:179], 0\n"
        "buffer_load_dwordx4 v249, s[12:15], s64 offen lds\n"
        "v_mfma_f32_16x16x32_bf16 v[156:159], a[220:223], a[172:175], v[156:159]\n"
        "ds_read_b128 v[222:225], v251 offset:4608\n"
        "v_exp_f32 v69, v69\n"
        "v_mfma_f32_16x16x32_bf16 v[156:159], a[216:219], a[168:171], v[156:159]\n"
        "v_exp_f32 v70, v70\n"
        "v_cvt_pk_bf16_f32 v68, v68, v69\n"
        "v_mfma_f32_16x16x32_bf16 v[84:87], v[192:195], v[196:199], v[40:43]\n"
        "s_waitcnt lgkmcnt(6)\n"
        "v_exp_f32 v76, v76\n"
        "v_mfma_f32_16x16x32_bf16 v[84:87], v[208:211], v[200:203], v[84:87]\n"
        "buffer_load_dwordx4 v253, s[12:15], s64 offen offset:1024 lds\n"
        "v_mfma_f32_16x16x32_bf16 v[160:163], a[216:219], a[192:195], 0\n"
        "s_add_u32 m0, s65, 12288\n"
        "v_exp_f32 v71, v71\n"
        "v_mfma_f32_16x16x32_bf16 v[160:163], a[220:223], a[196:199], v[160:163]\n"
        "ds_read_b128 a[216:219], v241 offset:128\n"
        "v_exp_f32 v77, v77\n"
        "v_mfma_f32_16x16x32_bf16 v[160:163], a[224:227], a[200:203], v[160:163]\n"
        "ds_read_b128 a[220:223], v240 offset:256\n"
        "v_cvt_pk_bf16_f32 v69, v70, v71\n"
        "v_cvt_pk_bf16_f32 v76, v76, v77\n"
        "v_mfma_f32_16x16x32_bf16 v[164:167], a[228:231], a[144:147], 0\n"
        "s_waitcnt lgkmcnt(2)\n"
        "buffer_load_dword v136, s[52:55], 0 offen lds\n"
        "v_mfma_f32_16x16x32_bf16 v[164:167], a[232:235], a[148:151], v[164:167]\n"
        "ds_read_b128 a[224:227], v241 offset:256\n"
        "v_exp_f32 v78, v78\n"
        "v_mfma_f32_16x16x32_bf16 v[164:167], a[236:239], a[152:155], v[164:167]\n"
        "v_exp_f32 v104, v104\n"
        "v_mfma_f32_16x16x32_bf16 v[100:103], v[212:215], v[196:199], v[44:47]\n"
        "v_exp_f32 v79, v79\n"
        "v_mfma_f32_16x16x32_bf16 v[100:103], v[228:231], v[204:207], v[100:103]\n"
        "v_exp_f32 v105, v105\n"
        "v_cvt_pk_bf16_f32 v77, v78, v79\n"
        "v_mfma_f32_16x16x32_bf16 v[176:179], a[236:239], a[176:179], 0\n"
        "v_exp_f32 v106, v106\n"
        "v_cvt_pk_bf16_f32 v70, v104, v105\n"
        "v_mfma_f32_16x16x32_bf16 v[176:179], a[232:235], a[172:175], v[176:179]\n"
        "v_exp_f32 v108, v108\n"
        "v_mfma_f32_16x16x32_bf16 v[176:179], a[228:231], a[168:171], v[176:179]\n"
        "s_waitcnt vmcnt(9)\n"
        "ds_read_b64_tr_b16 a[164:165], v255 offset:15360\n"
        "ds_read_b64_tr_b16 a[166:167], v255 offset:16128\n"
        "v_mfma_f32_16x16x32_bf16 v[180:183], a[228:231], a[192:195], 0\n"
        "ds_read_b64_tr_b16 a[188:189], v255 offset:15392\n"
        "ds_read_b64_tr_b16 a[190:191], v255 offset:16160\n"
        "v_mfma_f32_16x16x32_bf16 v[180:183], a[232:235], a[196:199], v[180:183]\n"
        "ds_read_b64_tr_b16 a[212:213], v255 offset:15424\n"
        "ds_read_b64_tr_b16 a[214:215], v255 offset:16192\n"
        "v_mfma_f32_16x16x32_bf16 v[180:183], a[236:239], a[200:203], v[180:183]\n"
        "ds_read_b128 a[228:231], v241 offset:1664\n"
        "ds_read_b128 a[232:235], v240 offset:1792\n"
        "v_mfma_f32_16x16x32_bf16 v[184:187], a[240:243], a[144:147], 0\n"
        "ds_read_b128 a[236:239], v241 offset:1792\n"
        "v_exp_f32 v107, v107\n"
        "v_mfma_f32_16x16x32_bf16 v[184:187], a[244:247], a[148:151], v[184:187]\n"
        "v_exp_f32 v109, v109\n"
        "v_cvt_pk_bf16_f32 v71, v106, v107\n"
        "v_mfma_f32_16x16x32_bf16 v[184:187], a[248:251], a[152:155], v[184:187]\n"
        "v_exp_f32 v110, v110\n"
        "v_cvt_pk_bf16_f32 v78, v108, v109\n"
        "v_mfma_f32_16x16x32_bf16 v[196:199], a[248:251], a[176:179], 0\n"
        "v_exp_f32 v111, v111\n"
        "v_mfma_f32_16x16x32_bf16 v[196:199], a[244:247], a[172:175], v[196:199]\n"
        "v_cvt_pk_bf16_f32 v79, v110, v111\n"
        "v_exp_f32 v80, v80\n"
        "v_mfma_f32_16x16x32_bf16 v[196:199], a[240:243], a[168:171], v[196:199]\n"
        "v_exp_f32 v81, v81\n"
        "v_mfma_f32_16x16x32_bf16 v[200:203], a[240:243], a[192:195], 0\n"
        "v_exp_f32 v82, v82\n"
        "v_cvt_pk_bf16_f32 v80, v80, v81\n"
        "v_mfma_f32_16x16x32_bf16 v[200:203], a[244:247], a[196:199], v[200:203]\n"
        "ds_read_b128 a[240:243], v241 offset:12416\n"
        "v_exp_f32 v83, v83\n"
        "v_mfma_f32_16x16x32_bf16 v[200:203], a[248:251], a[200:203], v[200:203]\n"
        "ds_read_b128 a[244:247], v240 offset:12544\n"
        "v_cvt_pk_bf16_f32 v81, v82, v83\n"
        "v_mfma_f32_16x16x32_bf16 v[88:91], v[152:155], v[216:219], v[48:51]\n"
        "ds_read_b128 a[248:251], v241 offset:12544\n"
        "v_exp_f32 v96, v96\n"
        "v_mfma_f32_16x16x32_bf16 v[88:91], v[168:171], v[220:223], v[88:91]\n"
        "s_waitcnt lgkmcnt(3)\n"
        "v_exp_f32 v97, v97\n"
        "v_mfma_f32_16x16x32_bf16 v[112:115], a[216:219], a[156:159], v[112:115]\n"
        "v_exp_f32 v98, v98\n"
        "v_cvt_pk_bf16_f32 v82, v96, v97\n"
        "v_mfma_f32_16x16x32_bf16 v[112:115], a[220:223], a[160:163], v[112:115]\n"
        "v_exp_f32 v99, v99\n"
        "v_mfma_f32_16x16x32_bf16 v[112:115], a[224:227], a[164:167], v[112:115]\n"
        "v_cvt_pk_bf16_f32 v83, v98, v99\n"
        "v_exp_f32 v84, v84\n"
        "v_mfma_f32_16x16x32_bf16 v[116:119], a[224:227], a[188:191], v[116:119]\n"
        "v_exp_f32 v85, v85\n"
        "v_mfma_f32_16x16x32_bf16 v[116:119], a[220:223], a[184:187], v[116:119]\n"
        "v_exp_f32 v86, v86\n"
        "v_cvt_pk_bf16_f32 v84, v84, v85\n"
        "v_mfma_f32_16x16x32_bf16 v[116:119], a[216:219], a[180:183], v[116:119]\n"
        "v_exp_f32 v87, v87\n"
        "v_mfma_f32_16x16x32_bf16 v[120:123], a[216:219], a[204:207], v[120:123]\n"
        "v_cvt_pk_bf16_f32 v85, v86, v87\n"
        "v_exp_f32 v100, v100\n"
        "v_mfma_f32_16x16x32_bf16 v[120:123], a[220:223], a[208:211], v[120:123]\n"
        "ds_read_b128 a[216:219], v241 offset:13952\n"
        "v_exp_f32 v101, v101\n"
        "v_mfma_f32_16x16x32_bf16 v[120:123], a[224:227], a[212:215], v[120:123]\n"
        "ds_read_b128 a[220:223], v240 offset:14080\n"
        "v_exp_f32 v102, v102\n"
        "v_mfma_f32_16x16x32_bf16 v[124:127], a[228:231], a[156:159], v[124:127]\n"
        "ds_read_b128 a[224:227], v241 offset:14080\n"
        "v_cvt_pk_bf16_f32 v86, v100, v101\n"
        "v_cvt_pk_bf16_f32 v128, v112, v113\n"
        "v_mfma_f32_16x16x32_bf16 v[124:127], a[232:235], a[160:163], v[124:127]\n"
        "v_exp_f32 v103, v103\n"
        "v_cvt_pk_bf16_f32 v129, v114, v115\n"
        "v_mfma_f32_16x16x32_bf16 v[124:127], a[236:239], a[164:167], v[124:127]\n"
        "v_cvt_pk_bf16_f32 v87, v102, v103\n"
        "v_cvt_pk_bf16_f32 v136, v116, v117\n"
        "v_mfma_f32_16x16x32_bf16 v[104:107], v[172:175], v[216:219], v[52:55]\n"
        "v_cvt_pk_bf16_f32 v137, v118, v119\n"
        "v_cvt_pk_bf16_f32 v144, v120, v121\n"
        "v_mfma_f32_16x16x32_bf16 v[104:107], v[188:191], v[224:227], v[104:107]\n"
        "ds_read_b128 v[222:225], v252 offset:4608\n"
        "v_cvt_pk_bf16_f32 v145, v122, v123\n"
        "v_mfma_f32_16x16x32_bf16 v[156:159], a[236:239], a[188:191], v[156:159]\n"
        "v_exp_f32 v88, v88\n"
        "v_mfma_f32_16x16x32_bf16 v[156:159], a[232:235], a[184:187], v[156:159]\n"
        "v_cvt_pk_bf16_f32 v130, v124, v125\n"
        "v_cvt_pk_bf16_f32 v131, v126, v127\n"
        "v_mfma_f32_16x16x32_bf16 v[156:159], a[228:231], a[180:183], v[156:159]\n"
        "v_exp_f32 v89, v89\n"
        "v_mfma_f32_16x16x32_bf16 v[160:163], a[228:231], a[204:207], v[160:163]\n"
        "v_exp_f32 v90, v90\n"
        "v_mfma_f32_16x16x32_bf16 v[160:163], a[232:235], a[208:211], v[160:163]\n"
        "v_exp_f32 v91, v91\n"
        "v_mfma_f32_16x16x32_bf16 v[160:163], a[236:239], a[212:215], v[160:163]\n"
        "s_waitcnt lgkmcnt(0)\n"
        "v_exp_f32 v104, v104\n"
        "v_mfma_f32_16x16x32_bf16 v[164:167], a[240:243], a[156:159], v[164:167]\n"
        "v_cvt_pk_bf16_f32 v138, v156, v157\n"
        "v_cvt_pk_bf16_f32 v139, v158, v159\n"
        "v_mfma_f32_16x16x32_bf16 v[164:167], a[244:247], a[160:163], v[164:167]\n"
        "v_cvt_pk_bf16_f32 v88, v88, v89\n"
        "v_cvt_pk_bf16_f32 v89, v90, v91\n"
        "v_mfma_f32_16x16x32_bf16 v[164:167], a[248:251], a[164:167], v[164:167]\n"
        "v_exp_f32 v105, v105\n"
        "v_mfma_f32_16x16x32_bf16 v[92:95], v[192:195], v[216:219], v[56:59]\n"
        "v_cvt_pk_bf16_f32 v146, v160, v161\n"
        "v_cvt_pk_bf16_f32 v147, v162, v163\n"
        "v_mfma_f32_16x16x32_bf16 v[92:95], v[208:211], v[220:223], v[92:95]\n"
        "v_exp_f32 v106, v106\n"
        "v_mfma_f32_16x16x32_bf16 v[176:179], a[248:251], a[188:191], v[176:179]\n"
        "v_exp_f32 v107, v107\n"
        "v_mfma_f32_16x16x32_bf16 v[176:179], a[244:247], a[184:187], v[176:179]\n"
        "v_cvt_pk_bf16_f32 v132, v164, v165\n"
        "v_cvt_pk_bf16_f32 v133, v166, v167\n"
        "v_mfma_f32_16x16x32_bf16 v[176:179], a[240:243], a[180:183], v[176:179]\n"
        "v_cvt_pk_bf16_f32 v90, v104, v105\n"
        "v_cvt_pk_bf16_f32 v91, v106, v107\n"
        "v_mfma_f32_16x16x32_bf16 v[180:183], a[240:243], a[204:207], v[180:183]\n"
        "v_exp_f32 v92, v92\n"
        "v_mfma_f32_16x16x32_bf16 v[180:183], a[244:247], a[208:211], v[180:183]\n"
        "v_exp_f32 v93, v93\n"
        "v_mfma_f32_16x16x32_bf16 v[180:183], a[248:251], a[212:215], v[180:183]\n"
        "v_mov_b64 v[96:97], 0\n"
        "v_cvt_pk_bf16_f32 v140, v176, v177\n"
        "v_mfma_f32_16x16x32_bf16 v[184:187], a[216:219], a[156:159], v[184:187]\n"
        "v_cvt_pk_bf16_f32 v141, v178, v179\n"
        "v_cvt_pk_bf16_f32 v92, v92, v93\n"
        "v_mfma_f32_16x16x32_bf16 v[184:187], a[220:223], a[160:163], v[184:187]\n"
        "v_exp_f32 v94, v94\n"
        "v_mfma_f32_16x16x32_bf16 v[184:187], a[224:227], a[164:167], v[184:187]\n"
        "v_exp_f32 v95, v95\n"
        "v_mfma_f32_16x16x32_bf16 v[108:111], v[212:215], v[216:219], v[60:63]\n"
        "v_cvt_pk_bf16_f32 v148, v180, v181\n"
        "v_cvt_pk_bf16_f32 v149, v182, v183\n"
        "v_mfma_f32_16x16x32_bf16 v[108:111], v[228:231], v[224:227], v[108:111]\n"
        "v_add_u32 v240, 24576, v240\n"
        "v_add_u32 v241, 24576, v241\n"
        "v_mfma_f32_16x16x32_bf16 v[196:199], a[224:227], a[188:191], v[196:199]\n"
        "v_cvt_pk_bf16_f32 v134, v184, v185\n"
        "v_cvt_pk_bf16_f32 v135, v186, v187\n"
        "v_mfma_f32_16x16x32_bf16 v[196:199], a[220:223], a[184:187], v[196:199]\n"
        "v_mov_b64 v[172:173], 0\n"
        "v_cvt_pk_bf16_f32 v93, v94, v95\n"
        "v_mfma_f32_16x16x32_bf16 v[196:199], a[216:219], a[180:183], v[196:199]\n"
        "v_exp_f32 v108, v108\n"
        "v_mfma_f32_16x16x32_bf16 v[200:203], a[216:219], a[204:207], v[200:203]\n"
        "v_exp_f32 v109, v109\n"
        "v_mfma_f32_16x16x32_bf16 v[200:203], a[220:223], a[208:211], v[200:203]\n"
        "v_exp_f32 v110, v110\n"
        "v_mfma_f32_16x16x32_bf16 v[200:203], a[224:227], a[212:215], v[200:203]\n"
        "v_exp_f32 v111, v111\n"
        "v_smfmac_f32_16x16x64_bf16 v[96:99], a[252:255], v[64:71], v237\n"
        "v_cvt_pk_bf16_f32 v142, v196, v197\n"
        "v_mfma_f32_16x16x32_bf16 v[100:103], v[132:135], v[68:71], 0\n"
        "v_cvt_pk_bf16_f32 v143, v198, v199\n"
        "v_cvt_pk_bf16_f32 v94, v108, v109\n"
        "v_mfma_f32_16x16x32_bf16 v[100:103], v[128:131], v[64:67], v[100:103]\n"
        "v_mov_b64 v[192:193], 0\n"
        "v_cvt_pk_bf16_f32 v150, v200, v201\n"
        "v_mfma_f32_16x16x32_bf16 v[104:107], v[136:139], v[64:67], 0\n"
        "v_cvt_pk_bf16_f32 v151, v202, v203\n"
        "v_add_f32 v97, v96, v97\n"
        "v_mfma_f32_16x16x32_bf16 v[104:107], v[140:143], v[68:71], v[104:107]\n"
        "v_rcp_f32 v96, v97\n"
        "v_cvt_pk_bf16_f32 v95, v110, v111\n"
        "v_mfma_f32_16x16x32_bf16 v[108:111], v[148:151], v[68:71], 0\n"
        "v_mul_f32 v100, v96, v100\n"
        "v_mul_f32 v101, v96, v101\n"
        "v_mfma_f32_16x16x32_bf16 v[108:111], v[144:147], v[64:67], v[108:111]\n"
        "v_mul_f32 v102, v96, v102\n"
        "v_mul_f32 v103, v96, v103\n"
        "v_smfmac_f32_16x16x64_bf16 v[172:175], a[252:255], v[72:79], v237\n"
        "s_waitcnt vmcnt(6)\n"
        "s_barrier\n"
        "ds_read_b128 a[236:239], v240 offset:0\n"
        "v_mfma_f32_16x16x32_bf16 v[176:179], v[132:135], v[76:79], 0\n"
        "ds_read_b128 a[240:243], v241 offset:0\n"
        "ds_read_b128 a[244:247], v240 offset:128\n"
        "v_mfma_f32_16x16x32_bf16 v[176:179], v[128:131], v[72:75], v[176:179]\n"
        "ds_read_b128 a[248:251], v241 offset:128\n"
        "ds_read_b128 a[216:219], v240 offset:256\n"
        "v_mfma_f32_16x16x32_bf16 v[180:183], v[136:139], v[72:75], 0\n"
        "ds_read_b128 a[220:223], v241 offset:256\n"
        "v_cvt_pk_bf16_f32 v100, v100, v101\n"
        "v_mfma_f32_16x16x32_bf16 v[180:183], v[140:143], v[76:79], v[180:183]\n"
        "v_cvt_pk_bf16_f32 v101, v102, v103\n"
        "v_mul_f32 v104, v96, v104\n"
        "v_mfma_f32_16x16x32_bf16 v[184:187], v[148:151], v[76:79], 0\n"
        "ds_write2st64_b32 v245, v100, v101 offset0:0 offset1:4\n"
        "v_mfma_f32_16x16x32_bf16 v[184:187], v[144:147], v[72:75], v[184:187]\n"
        "v_mul_f32 v105, v96, v105\n"
        "v_mul_f32 v106, v96, v106\n"
        "v_smfmac_f32_16x16x64_bf16 v[192:195], a[252:255], v[80:87], v237\n"
        "v_mul_f32 v107, v96, v107\n"
        "v_mfma_f32_16x16x32_bf16 v[196:199], v[132:135], v[84:87], 0\n"
        "v_cvt_pk_bf16_f32 v102, v104, v105\n"
        "v_cvt_pk_bf16_f32 v103, v106, v107\n"
        "v_mfma_f32_16x16x32_bf16 v[196:199], v[128:131], v[80:83], v[196:199]\n"
        "buffer_load_dwordx4 v232, s[52:55], s34 offen lds\n"
        "v_mfma_f32_16x16x32_bf16 v[200:203], v[136:139], v[80:83], 0\n"
        "ds_write2st64_b32 v245, v102, v103 offset0:8 offset1:12\n"
        "v_mfma_f32_16x16x32_bf16 v[200:203], v[140:143], v[84:87], v[200:203]\n"
        "v_mov_b64 v[124:125], 0\n"
        "v_mul_f32 v108, v96, v108\n"
        "v_mul_f32 v109, v96, v109\n"
        "v_mfma_f32_16x16x32_bf16 v[204:207], v[148:151], v[84:87], 0\n"
        "v_mul_f32 v110, v96, v110\n"
        "v_mul_f32 v111, v96, v111\n"
        "v_mfma_f32_16x16x32_bf16 v[204:207], v[144:147], v[80:83], v[204:207]\n"
        "s_waitcnt lgkmcnt(2)\n"
        "v_cvt_pk_bf16_f32 v108, v108, v109\n"
        "v_cvt_pk_bf16_f32 v109, v110, v111\n"
        "v_mfma_f32_16x16x32_bf16 v[112:115], a[72:75], a[236:239], 0\n"
        "v_max_u32 v236, v96, v97\n"
        "v_add_f32 v173, v172, v173\n"
        "v_mfma_f32_16x16x32_bf16 v[112:115], a[76:79], a[240:243], v[112:115]\n"
        "ds_write2st64_b32 v245, v108, v109 offset0:16 offset1:20\n"
        "v_mfma_f32_16x16x32_bf16 v[112:115], a[80:83], a[244:247], v[112:115]\n"
        "v_rcp_f32 v172, v173\n"
        "v_mfma_f32_16x16x32_bf16 v[112:115], a[84:87], a[248:251], v[112:115]\n"
        "buffer_load_dwordx4 v233, s[52:55], s34 offen offset:1024 lds\n"
        "v_mfma_f32_16x16x32_bf16 v[112:115], a[88:91], a[216:219], v[112:115]\n"
        "v_mul_f32 v176, v172, v176\n"
        "v_mul_f32 v177, v172, v177\n"
        "v_mfma_f32_16x16x32_bf16 v[112:115], a[92:95], a[220:223], v[112:115]\n"
        "v_mul_f32 v178, v172, v178\n"
        "v_mul_f32 v179, v172, v179\n"
        "v_smfmac_f32_16x16x64_bf16 v[124:127], a[252:255], v[88:95], v237\n"
        "v_cvt_pk_bf16_f32 v176, v176, v177\n"
        "v_mfma_f32_16x16x32_bf16 v[80:83], v[132:135], v[92:95], 0\n"
        "v_cvt_pk_bf16_f32 v177, v178, v179\n"
        "v_mul_f32 v180, v172, v180\n"
        "v_mfma_f32_16x16x32_bf16 v[80:83], v[128:131], v[88:91], v[80:83]\n"
        "ds_write2st64_b32 v245, v176, v177 offset0:1 offset1:5\n"
        "v_mfma_f32_16x16x32_bf16 v[84:87], v[136:139], v[88:91], 0\n"
        "v_cvt_pk_bf16_f32 v152, v112, v113\n"
        "v_cvt_pk_bf16_f32 v153, v114, v115\n"
        "v_mfma_f32_16x16x32_bf16 v[84:87], v[140:143], v[92:95], v[84:87]\n"
        "v_mul_f32 v181, v172, v181\n"
        "v_mul_f32 v182, v172, v182\n"
        "v_mfma_f32_16x16x32_bf16 v[96:99], v[148:151], v[92:95], 0\n"
        "buffer_load_dwordx4 v234, s[52:55], s34 offen offset:2048 lds\n"
        "v_mfma_f32_16x16x32_bf16 v[96:99], v[144:147], v[88:91], v[96:99]\n"
        "v_mul_f32 v183, v172, v183\n"
        "v_cvt_pk_bf16_f32 v178, v180, v181\n"
        "v_mfma_f32_16x16x32_bf16 v[116:119], a[116:119], a[220:223], 0\n"
        "v_cvt_pk_bf16_f32 v179, v182, v183\n"
        "v_mul_f32 v184, v172, v184\n"
        "v_mfma_f32_16x16x32_bf16 v[116:119], a[112:115], a[216:219], v[116:119]\n"
        "ds_write2st64_b32 v245, v178, v179 offset0:9 offset1:13\n"
        "v_mfma_f32_16x16x32_bf16 v[116:119], a[108:111], a[248:251], v[116:119]\n"
        "v_mul_f32 v185, v172, v185\n"
        "v_mul_f32 v186, v172, v186\n"
        "v_mfma_f32_16x16x32_bf16 v[116:119], a[104:107], a[244:247], v[116:119]\n"
        "s_add_u32 m0, s65, 15360\n"
        "v_mul_f32 v187, v172, v187\n"
        "v_add_f32 v125, v124, v125\n"
        "v_mfma_f32_16x16x32_bf16 v[116:119], a[100:103], a[240:243], v[116:119]\n"
        "v_rcp_f32 v124, v125\n"
        "v_mfma_f32_16x16x32_bf16 v[116:119], a[96:99], a[236:239], v[116:119]\n"
        "v_cvt_pk_bf16_f32 v184, v184, v185\n"
        "v_cvt_pk_bf16_f32 v185, v186, v187\n"
        "v_mfma_f32_16x16x32_bf16 v[120:123], a[120:123], a[236:239], 0\n"
        "buffer_load_dwordx4 v232, s[52:55], s62 offen lds\n"
        "v_mfma_f32_16x16x32_bf16 v[120:123], a[124:127], a[240:243], v[120:123]\n"
        "ds_write2st64_b32 v245, v184, v185 offset0:17 offset1:21\n"
        "v_mfma_f32_16x16x32_bf16 v[120:123], a[128:131], a[244:247], v[120:123]\n"
        "v_mul_f32 v80, v124, v80\n"
        "v_mul_f32 v81, v124, v81\n"
        "v_mfma_f32_16x16x32_bf16 v[120:123], a[132:135], a[248:251], v[120:123]\n"
        "v_mul_f32 v82, v124, v82\n"
        "v_mul_f32 v83, v124, v83\n"
        "v_mfma_f32_16x16x32_bf16 v[120:123], a[136:139], a[216:219], v[120:123]\n"
        "v_max3_u32 v236, v236, v172, v173\n"
        "v_cvt_pk_bf16_f32 v80, v80, v81\n"
        "v_mfma_f32_16x16x32_bf16 v[120:123], a[140:143], a[220:223], v[120:123]\n"
        "v_cvt_pk_bf16_f32 v81, v82, v83\n"
        "v_mul_f32 v84, v124, v84\n"
        "v_mfma_f32_16x16x32_bf16 v[112:115], a[20:23], a[220:223], 0\n"
        "v_mul_f32 v85, v124, v85\n"
        "v_mul_f32 v86, v124, v86\n"
        "v_mfma_f32_16x16x32_bf16 v[112:115], a[16:19], a[216:219], v[112:115]\n"
        "v_mul_f32 v87, v124, v87\n"
        "v_cvt_pk_bf16_f32 v82, v84, v85\n"
        "v_mfma_f32_16x16x32_bf16 v[112:115], a[12:15], a[248:251], v[112:115]\n"
        "buffer_load_dwordx4 v233, s[52:55], s62 offen offset:1024 lds\n"
        "v_mfma_f32_16x16x32_bf16 v[112:115], a[8:11], a[244:247], v[112:115]\n"
        "v_cvt_pk_bf16_f32 v83, v86, v87\n"
        "v_mul_f32 v96, v124, v96\n"
        "v_mfma_f32_16x16x32_bf16 v[112:115], a[4:7], a[240:243], v[112:115]\n"
        "v_mul_f32 v97, v124, v97\n"
        "v_mul_f32 v98, v124, v98\n"
        "v_mfma_f32_16x16x32_bf16 v[112:115], a[0:3], a[236:239], v[112:115]\n"
        "v_mul_f32 v99, v124, v99\n"
        "v_cvt_pk_bf16_f32 v154, v116, v117\n"
        "v_cvt_pk_bf16_f32 v155, v118, v119\n"
        "v_mfma_f32_16x16x32_bf16 v[116:119], a[24:27], a[236:239], 0\n"
        "v_cvt_pk_bf16_f32 v96, v96, v97\n"
        "v_cvt_pk_bf16_f32 v97, v98, v99\n"
        "v_mfma_f32_16x16x32_bf16 v[116:119], a[28:31], a[240:243], v[116:119]\n"
        "ds_write_b32 v245, v96 offset:4864\n"
        "v_mfma_f32_16x16x32_bf16 v[116:119], a[32:35], a[244:247], v[116:119]\n"
        "v_cvt_pk_bf16_f32 v168, v120, v121\n"
        "v_cvt_pk_bf16_f32 v169, v122, v123\n"
        "v_mfma_f32_16x16x32_bf16 v[116:119], a[36:39], a[248:251], v[116:119]\n"
        "v_add_f32 v193, v192, v193\n"
        "v_cvt_pk_bf16_f32 v156, v112, v113\n"
        "v_mfma_f32_16x16x32_bf16 v[116:119], a[40:43], a[216:219], v[116:119]\n"
        "buffer_load_dwordx4 v234, s[52:55], s62 offen offset:2048 lds\n"
        "v_mfma_f32_16x16x32_bf16 v[116:119], a[44:47], a[220:223], v[116:119]\n"
        "v_rcp_f32 v192, v193\n"
        "v_mfma_f32_16x16x32_bf16 v[120:123], a[68:71], a[220:223], 0\n"
        "v_mul_f32 v196, v192, v196\n"
        "v_mul_f32 v197, v192, v197\n"
        "v_mfma_f32_16x16x32_bf16 v[120:123], a[64:67], a[216:219], v[120:123]\n"
        "v_mul_f32 v198, v192, v198\n"
        "v_mul_f32 v199, v192, v199\n"
        "v_mfma_f32_16x16x32_bf16 v[120:123], a[60:63], a[248:251], v[120:123]\n"
        "ds_read_b128 a[224:227], v240 offset:1536\n"
        "v_cvt_pk_bf16_f32 v157, v114, v115\n"
        "v_mfma_f32_16x16x32_bf16 v[120:123], a[56:59], a[244:247], v[120:123]\n"
        "s_add_u32 m0, s65, 0\n"
        "ds_read_b128 a[228:231], v241 offset:1536\n"
        "v_cvt_pk_bf16_f32 v196, v196, v197\n"
        "v_mfma_f32_16x16x32_bf16 v[120:123], a[52:55], a[240:243], v[120:123]\n"
        "ds_read_b128 a[232:235], v240 offset:1664\n"
        "v_cvt_pk_bf16_f32 v197, v198, v199\n"
        "v_mfma_f32_16x16x32_bf16 v[120:123], a[48:51], a[236:239], v[120:123]\n"
        "ds_write2st64_b32 v245, v196, v197 offset0:2 offset1:6\n"
        "v_mfma_f32_16x16x32_bf16 v[112:115], a[236:239], a[144:147], 0\n"
        "buffer_load_dwordx4 v232, s[52:55], 0 offen lds\n"
        "v_mfma_f32_16x16x32_bf16 v[112:115], a[240:243], a[148:151], v[112:115]\n"
        "v_mul_f32 v200, v192, v200\n"
        "v_mul_f32 v201, v192, v201\n"
        "v_mfma_f32_16x16x32_bf16 v[112:115], a[244:247], a[152:155], v[112:115]\n"
        "v_mul_f32 v202, v192, v202\n"
        "v_mul_f32 v203, v192, v203\n"
        "v_mfma_f32_16x16x32_bf16 v[112:115], a[248:251], a[156:159], v[112:115]\n"
        "v_cvt_pk_bf16_f32 v198, v200, v201\n"
        "v_cvt_pk_bf16_f32 v199, v202, v203\n"
        "v_mfma_f32_16x16x32_bf16 v[112:115], a[216:219], a[160:163], v[112:115]\n"
        "ds_write2st64_b32 v245, v198, v199 offset0:10 offset1:14\n"
        "v_mfma_f32_16x16x32_bf16 v[112:115], a[220:223], a[164:167], v[112:115]\n"
        "v_mul_f32 v204, v192, v204\n"
        "v_mul_f32 v205, v192, v205\n"
        "v_cvt_pk_bf16_f32 v158, v116, v117\n"
        "v_cvt_pk_bf16_f32 v159, v118, v119\n"
        "v_mfma_f32_16x16x32_bf16 v[116:119], a[220:223], a[188:191], 0\n"
        "s_waitcnt vmcnt(4)\n"
        "ds_read_b64_tr_b16 v[64:65], v255 offset:12288\n"
        "ds_read_b64_tr_b16 v[66:67], v255 offset:13056\n"
        "v_mfma_f32_16x16x32_bf16 v[116:119], a[216:219], a[184:187], v[116:119]\n"
        "ds_write_b32 v245, v97 offset:5888\n"
        "v_mfma_f32_16x16x32_bf16 v[116:119], a[248:251], a[180:183], v[116:119]\n"
        "buffer_load_dwordx4 v233, s[52:55], 0 offen offset:1024 lds\n"
        "v_mfma_f32_16x16x32_bf16 v[116:119], a[244:247], a[176:179], v[116:119]\n"
        "ds_read_b64_tr_b16 v[68:69], v255 offset:12320\n"
        "ds_read_b64_tr_b16 v[70:71], v255 offset:13088\n"
        "v_mfma_f32_16x16x32_bf16 v[116:119], a[240:243], a[172:175], v[116:119]\n"
        "ds_read_b64_tr_b16 v[72:73], v255 offset:12352\n"
        "ds_read_b64_tr_b16 v[74:75], v255 offset:13120\n"
        "v_mfma_f32_16x16x32_bf16 v[116:119], a[236:239], a[168:171], v[116:119]\n"
        "v_mul_f32 v206, v192, v206\n"
        "v_mul_f32 v207, v192, v207\n"
        "v_cvt_pk_bf16_f32 v160, v120, v121\n"
        "v_cvt_pk_bf16_f32 v161, v122, v123\n"
        "v_cvt_pk_bf16_f32 v166, v120, v121\n"
        "v_cvt_pk_bf16_f32 v167, v122, v123\n"
        "v_mfma_f32_16x16x32_bf16 v[120:123], a[236:239], a[192:195], 0\n"
        "ds_read_b128 a[236:239], v241 offset:1664\n"
        "v_cvt_pk_bf16_f32 v204, v204, v205\n"
        "v_mfma_f32_16x16x32_bf16 v[120:123], a[240:243], a[196:199], v[120:123]\n"
        "ds_read_b128 a[240:243], v240 offset:1792\n"
        "v_cvt_pk_bf16_f32 v205, v206, v207\n"
        "v_mfma_f32_16x16x32_bf16 v[120:123], a[244:247], a[200:203], v[120:123]\n"
        "ds_write2st64_b32 v245, v204, v205 offset0:18 offset1:22\n"
        "v_mfma_f32_16x16x32_bf16 v[120:123], a[248:251], a[204:207], v[120:123]\n"
        "ds_read_b128 a[244:247], v241 offset:1792\n"
        "v_max3_u32 v236, v236, v192, v193\n"
        "v_mfma_f32_16x16x32_bf16 v[120:123], a[216:219], a[208:211], v[120:123]\n"
        "buffer_load_dwordx4 v234, s[52:55], 0 offen offset:2048 lds\n"
        "v_mfma_f32_16x16x32_bf16 v[120:123], a[220:223], a[212:215], v[120:123]\n"
        "s_waitcnt lgkmcnt(4)\n"
        "ds_write_b64 v235, v[64:65] offset:12288\n"
        "v_cvt_pk_bf16_f32 v128, v112, v113\n"
        "v_cvt_pk_bf16_f32 v129, v114, v115\n"
        "v_mfma_f32_16x16x32_bf16 v[112:115], a[72:75], a[224:227], 0\n"
        "s_barrier\n"
        "s_waitcnt vmcnt(3)\n"
        "ds_read_b64_tr_b16 v[76:77], v255 offset:15360\n"
        "ds_read_b64_tr_b16 v[78:79], v255 offset:16128\n"
        "v_mfma_f32_16x16x32_bf16 v[112:115], a[76:79], a[228:231], v[112:115]\n"
        "v_max3_u32 v236, v236, v124, v125\n"
        "v_cmp_lt_u32 vcc, s24, v236\n"
        "v_mfma_f32_16x16x32_bf16 v[112:115], a[80:83], a[232:235], v[112:115]\n"
        "s_waitcnt lgkmcnt(5)\n"
        "ds_write_b64 v235, v[66:67] offset:12296\n"
        "s_cmp_lg_u64 vcc, 0\n"
        "s_addc_u32 s25, s25, s25\n"
        "v_mfma_f32_16x16x32_bf16 v[112:115], a[84:87], a[236:239], v[112:115]\n"
        "s_add_u32 m0, s65, 3072\n"
        "v_and_b32 v239, 32, v245\n"
        "v_cvt_pk_bf16_f32 v136, v116, v117\n"
        "v_mfma_f32_16x16x32_bf16 v[112:115], a[88:91], a[240:243], v[112:115]\n"
        "s_waitcnt lgkmcnt(4)\n"
        "v_cvt_pk_bf16_f32 v137, v118, v119\n"
        "v_xor_b32 v251, 0x2000, v251\n"
        "v_mfma_f32_16x16x32_bf16 v[112:115], a[92:95], a[244:247], v[112:115]\n"
        "v_xor_b32 v252, 0x2000, v252\n"
        "v_mad_i32_i24 v239, v239, -12, v235\n"
        "v_mfma_f32_16x16x32_bf16 v[116:119], a[116:119], a[244:247], 0\n"
        "ds_write_b64 v239, v[68:69] offset:12480\n"
        "v_mfma_f32_16x16x32_bf16 v[116:119], a[112:115], a[240:243], v[116:119]\n"
        "buffer_load_dwordx4 v232, s[52:55], s63 offen lds\n"
        "v_mfma_f32_16x16x32_bf16 v[116:119], a[108:111], a[236:239], v[116:119]\n"
        "ds_read_b64_tr_b16 v[88:89], v255 offset:15392\n"
        "ds_read_b64_tr_b16 v[90:91], v255 offset:16160\n"
        "v_mfma_f32_16x16x32_bf16 v[116:119], a[104:107], a[232:235], v[116:119]\n"
        "ds_write_b64 v239, v[70:71] offset:12488\n"
        "v_mfma_f32_16x16x32_bf16 v[116:119], a[100:103], a[228:231], v[116:119]\n"
        "v_cvt_pk_bf16_f32 v144, v120, v121\n"
        "v_cvt_pk_bf16_f32 v145, v122, v123\n"
        "v_mfma_f32_16x16x32_bf16 v[116:119], a[96:99], a[224:227], v[116:119]\n"
        "v_cvt_pk_bf16_f32 v172, v112, v113\n"
        "v_cvt_pk_bf16_f32 v173, v114, v115\n"
        "v_mfma_f32_16x16x32_bf16 v[120:123], a[120:123], a[224:227], 0\n"
        "ds_write_b64 v254, v[72:73] offset:12288\n"
        "v_mfma_f32_16x16x32_bf16 v[120:123], a[124:127], a[228:231], v[120:123]\n"
        "ds_read_b128 v[162:165], v251 offset:0\n"
        "v_add_u32 v247, s45, v247\n"
        "v_mfma_f32_16x16x32_bf16 v[120:123], a[128:131], a[232:235], v[120:123]\n"
        "buffer_load_dwordx4 v233, s[52:55], s63 offen offset:1024 lds\n"
        "v_mfma_f32_16x16x32_bf16 v[120:123], a[132:135], a[236:239], v[120:123]\n"
        "ds_write_b64 v254, v[74:75] offset:12296\n"
        "v_mfma_f32_16x16x32_bf16 v[120:123], a[136:139], a[240:243], v[120:123]\n"
        "ds_read_b64_tr_b16 v[92:93], v255 offset:15424\n"
        "ds_read_b64_tr_b16 v[94:95], v255 offset:16192\n"
        "v_mfma_f32_16x16x32_bf16 v[120:123], a[140:143], a[244:247], v[120:123]\n"
        "v_cvt_pk_bf16_f32 v174, v116, v117\n"
        "v_cvt_pk_bf16_f32 v175, v118, v119\n"
        "v_mfma_f32_16x16x32_bf16 v[112:115], a[20:23], a[244:247], 0\n"
        "s_nop 2\n"
        "v_mfma_f32_16x16x32_bf16 v[112:115], a[16:19], a[240:243], v[112:115]\n"
        "s_nop 0\n"
        "v_cvt_pk_bf16_f32 v190, v120, v121\n"
        "v_cvt_pk_bf16_f32 v191, v122, v123\n"
        "v_mfma_f32_16x16x32_bf16 v[112:115], a[12:15], a[236:239], v[112:115]\n"
        "v_mfma_f32_16x16x32_bf16 v[112:115], a[8:11], a[232:235], v[112:115]\n"
        "s_waitcnt vmcnt(2)\n"
        "ds_read_b64_tr_b16 v[212:213], v255 offset:0\n"
        "ds_read_b64_tr_b16 v[214:215], v255 offset:768\n"
        "v_mfma_f32_16x16x32_bf16 v[112:115], a[4:7], a[228:231], v[112:115]\n"
        "buffer_load_dwordx4 v234, s[52:55], s63 offen offset:2048 lds\n"
        "v_mfma_f32_16x16x32_bf16 v[112:115], a[0:3], a[224:227], v[112:115]\n"
        "ds_read_b128 v[182:185], v251 offset:512\n"
        "s_nop 0\n"
        "v_mfma_f32_16x16x32_bf16 v[116:119], a[24:27], a[224:227], 0\n"
        "s_nop 2\n"
        "v_mfma_f32_16x16x32_bf16 v[116:119], a[28:31], a[228:231], v[116:119]\n"
        "s_waitcnt lgkmcnt(3)\n"
        "ds_write_b128 v235, v[76:79] offset:15360\n"
        "v_mfma_f32_16x16x32_bf16 v[116:119], a[32:35], a[232:235], v[116:119]\n"
        "v_cvt_pk_bf16_f32 v176, v112, v113\n"
        "v_cvt_pk_bf16_f32 v177, v114, v115\n"
        "v_mfma_f32_16x16x32_bf16 v[116:119], a[36:39], a[236:239], v[116:119]\n"
        "s_add_u32 m0, s65, 6144\n"
        "v_mfma_f32_16x16x32_bf16 v[116:119], a[40:43], a[240:243], v[116:119]\n"
        ".long 0xbec200ff\n"
        "43:\n"
        "s_branch 60b\n"
        "ds_read_b64_tr_b16 v[216:217], v255 offset:32\n"
        "ds_read_b64_tr_b16 v[218:219], v255 offset:800\n"
        "v_mfma_f32_16x16x32_bf16 v[116:119], a[44:47], a[244:247], v[116:119]\n"
        "ds_write_b32 v245, v80 offset:768\n"
        "v_mfma_f32_16x16x32_bf16 v[120:123], a[68:71], a[244:247], 0\n"
        "buffer_load_dwordx4 v232, s[52:55], s61 offen lds\n"
        "v_mfma_f32_16x16x32_bf16 v[120:123], a[64:67], a[240:243], v[120:123]\n"
        "s_nop 2\n"
        "v_mfma_f32_16x16x32_bf16 v[120:123], a[60:63], a[236:239], v[120:123]\n"
        "ds_write_b128 v239, v[88:91] offset:15552\n"
        "v_mfma_f32_16x16x32_bf16 v[120:123], a[56:59], a[232:235], v[120:123]\n"
        "v_cvt_pk_bf16_f32 v178, v116, v117\n"
        "v_cvt_pk_bf16_f32 v179, v118, v119\n"
        "v_mfma_f32_16x16x32_bf16 v[120:123], a[52:55], a[228:231], v[120:123]\n"
        "v_mfma_f32_16x16x32_bf16 v[120:123], a[48:51], a[224:227], v[120:123]\n"
        "ds_write_b32 v245, v81 offset:1792\n"
        "v_mfma_f32_16x16x32_bf16 v[112:115], a[224:227], a[144:147], 0\n"
        "ds_read_b64_tr_b16 v[220:221], v255 offset:64\n"
        "ds_read_b64_tr_b16 v[222:223], v255 offset:832\n"
        "v_mfma_f32_16x16x32_bf16 v[112:115], a[228:231], a[148:151], v[112:115]\n"
        "ds_write_b32 v245, v82 offset:2816\n"
        "v_mfma_f32_16x16x32_bf16 v[112:115], a[232:235], a[152:155], v[112:115]\n"
        "ds_write_b32 v245, v83 offset:3840\n"
        "v_mfma_f32_16x16x32_bf16 v[112:115], a[236:239], a[156:159], v[112:115]\n"
        "buffer_load_dwordx4 v233, s[52:55], s61 offen offset:1024 lds\n"
        "v_mfma_f32_16x16x32_bf16 v[112:115], a[240:243], a[160:163], v[112:115]\n"
        "ds_read_b128 a[248:251], v240 offset:12288\n"
        "v_cvt_pk_bf16_f32 v180, v120, v121\n"
        "v_mfma_f32_16x16x32_bf16 v[112:115], a[244:247], a[164:167], v[112:115]\n"
        "ds_write_b128 v254, v[92:95] offset:15360\n"
        "v_mfma_f32_16x16x32_bf16 v[96:99], v[152:155], v[156:159], v[0:3]\n"
        "v_cvt_pk_bf16_f32 v181, v122, v123\n"
        "v_cvt_pk_bf16_f32 v186, v120, v121\n"
        "v_mfma_f32_16x16x32_bf16 v[96:99], v[168:171], v[160:163], v[96:99]\n"
        "s_waitcnt vmcnt(2)\n"
        "ds_read_b64_tr_b16 v[224:225], v255 offset:3072\n"
        "ds_read_b64_tr_b16 v[226:227], v255 offset:3840\n"
        "s_sub_u32 s31, s30, s28\n"
        "v_mfma_f32_16x16x32_bf16 v[100:103], v[172:175], v[156:159], v[4:7]\n"
        "ds_read_b128 a[216:219], v241 offset:12288\n"
        "s_sub_u32 s45, s28, s29\n"
        "v_cvt_pk_bf16_f32 v187, v122, v123\n"
        "v_mfma_f32_16x16x32_bf16 v[100:103], v[188:191], v[164:167], v[100:103]\n"
        "ds_read_b128 a[220:223], v240 offset:12416\n"
        "s_add_u32 s28, s28, s31\n"
        "v_cvt_pk_bf16_f32 v130, v112, v113\n"
        "v_mfma_f32_16x16x32_bf16 v[116:119], a[244:247], a[188:191], 0\n"
        "buffer_load_dwordx4 v234, s[52:55], s61 offen offset:2048 lds\n"
        "v_mfma_f32_16x16x32_bf16 v[116:119], a[240:243], a[184:187], v[116:119]\n"
        "s_waitcnt lgkmcnt(7)\n"
        "ds_write_b128 v235, v[212:215] offset:0\n"
        "v_mfma_f32_16x16x32_bf16 v[116:119], a[236:239], a[180:183], v[116:119]\n"
        "v_cvt_pk_bf16_f32 v131, v114, v115\n"
        "v_mfma_f32_16x16x32_bf16 v[116:119], a[232:235], a[176:179], v[116:119]\n"
        "ds_read_b64_tr_b16 v[88:89], v255 offset:3104\n"
        "ds_read_b64_tr_b16 v[90:91], v255 offset:3872\n"
        "v_mfma_f32_16x16x32_bf16 v[116:119], a[228:231], a[172:175], v[116:119]\n"
        "ds_read_b128 v[162:165], v252 offset:0\n"
        "v_mfma_f32_16x16x32_bf16 v[116:119], a[224:227], a[168:171], v[116:119]\n"
        "s_add_u32 m0, s65, 9216\n"
        "v_exp_f32 v96, v96\n"
        "v_mfma_f32_16x16x32_bf16 v[104:107], v[152:155], v[176:179], v[16:19]\n"
        "v_exp_f32 v97, v97\n"
        "v_mfma_f32_16x16x32_bf16 v[104:107], v[168:171], v[180:183], v[104:107]\n"
        "ds_write_b128 v239, v[216:219] offset:192\n"
        "v_mfma_f32_16x16x32_bf16 v[108:111], v[172:175], v[176:179], v[20:23]\n"
        "buffer_load_dwordx4 v232, s[52:55], s56 offen lds\n"
        "v_mfma_f32_16x16x32_bf16 v[108:111], v[188:191], v[184:187], v[108:111]\n"
        "ds_read_b128 v[182:185], v252 offset:512\n"
        "v_cvt_pk_bf16_f32 v64, v96, v97\n"
        "v_mfma_f32_16x16x32_bf16 v[120:123], a[224:227], a[192:195], 0\n"
        "ds_read_b128 a[224:227], v241 offset:12416\n"
        "ds_read_b64_tr_b16 v[92:93], v255 offset:3136\n"
        "v_mfma_f32_16x16x32_bf16 v[120:123], a[228:231], a[196:199], v[120:123]\n"
        "ds_read_b64_tr_b16 v[94:95], v255 offset:3904\n"
        "ds_read_b128 a[228:231], v240 offset:12544\n"
        "v_mfma_f32_16x16x32_bf16 v[120:123], a[232:235], a[200:203], v[120:123]\n"
        "ds_read_b128 a[232:235], v241 offset:12544\n"
        "v_cvt_pk_bf16_f32 v138, v116, v117\n"
        "v_mfma_f32_16x16x32_bf16 v[120:123], a[236:239], a[204:207], v[120:123]\n"
        "ds_read_b128 v[202:205], v251 offset:4096\n"
        "v_cvt_pk_bf16_f32 v139, v118, v119\n"
        "v_mfma_f32_16x16x32_bf16 v[120:123], a[240:243], a[208:211], v[120:123]\n"
        "ds_write_b128 v254, v[220:223] offset:0\n"
        "v_mfma_f32_16x16x32_bf16 v[120:123], a[244:247], a[212:215], v[120:123]\n"
        "s_waitcnt lgkmcnt(9)\n"
        "v_exp_f32 v98, v98\n"
        "v_mfma_f32_16x16x32_bf16 v[112:115], a[0:3], a[248:251], 0\n"
        "buffer_load_dwordx4 v233, s[52:55], s56 offen offset:1024 lds\n"
        "v_mfma_f32_16x16x32_bf16 v[112:115], a[4:7], a[216:219], v[112:115]\n"
        "v_exp_f32 v100, v100\n"
        "v_mfma_f32_16x16x32_bf16 v[112:115], a[8:11], a[220:223], v[112:115]\n"
        "s_waitcnt lgkmcnt(2)\n"
        "v_exp_f32 v99, v99\n"
        "v_mfma_f32_16x16x32_bf16 v[112:115], a[12:15], a[224:227], v[112:115]\n"
        "v_exp_f32 v101, v101\n"
        "v_mfma_f32_16x16x32_bf16 v[112:115], a[16:19], a[228:231], v[112:115]\n"
        "s_waitcnt vmcnt(2)\n"
        "ds_read_b64_tr_b16 v[212:213], v255 offset:6144\n"
        "ds_read_b64_tr_b16 v[214:215], v255 offset:6912\n"
        "v_mfma_f32_16x16x32_bf16 v[112:115], a[20:23], a[232:235], v[112:115]\n"
        "ds_write_b128 v235, v[224:227] offset:3072\n"
        "v_mfma_f32_16x16x32_bf16 v[116:119], a[44:47], a[232:235], 0\n"
        "v_cvt_pk_bf16_f32 v65, v98, v99\n"
        "v_cvt_pk_bf16_f32 v66, v100, v101\n"
        "v_mfma_f32_16x16x32_bf16 v[116:119], a[40:43], a[228:231], v[116:119]\n"
        "v_exp_f32 v102, v102\n"
        "v_mfma_f32_16x16x32_bf16 v[116:119], a[36:39], a[224:227], v[116:119]\n"
        "buffer_load_dwordx4 v234, s[52:55], s56 offen offset:2048 lds\n"
        "s_add_u32 s35, s47, 786432\n"
        "s_sub_u32 s46, s35, 100663296\n"
        "s_cmp_lt_u32 s46, 786432\n"
        "v_mfma_f32_16x16x32_bf16 v[116:119], a[32:35], a[220:223], v[116:119]\n"
        "s_cselect_b32 s35, s46, s35\n"
        "s_add_u32 s41, s30, s40\n"
        "s_mov_b32 s30, s29\n"
        "s_add_u32 s29, s29, s45\n"
        "v_mfma_f32_16x16x32_bf16 v[116:119], a[28:31], a[216:219], v[116:119]\n"
        "ds_read_b64_tr_b16 v[216:217], v255 offset:6176\n"
        "ds_read_b64_tr_b16 v[218:219], v255 offset:6944\n"
        "v_mfma_f32_16x16x32_bf16 v[116:119], a[24:27], a[248:251], v[116:119]\n"
        "v_exp_f32 v103, v103\n"
        "v_cvt_pk_bf16_f32 v146, v120, v121\n"
        "v_cvt_pk_bf16_f32 v147, v122, v123\n"
        "v_mfma_f32_16x16x32_bf16 v[120:123], a[48:51], a[248:251], 0\n"
        "ds_write_b128 v239, v[88:91] offset:3264\n"
        "v_mfma_f32_16x16x32_bf16 v[120:123], a[52:55], a[216:219], v[120:123]\n"
        "v_cvt_pk_bf16_f32 v67, v102, v103\n"
        "v_cvt_pk_bf16_f32 v196, v112, v113\n"
        "v_mfma_f32_16x16x32_bf16 v[120:123], a[56:59], a[220:223], v[120:123]\n"
        "s_mov_b32 m0, s48\n"
        "v_exp_f32 v104, v104\n"
        "v_mfma_f32_16x16x32_bf16 v[120:123], a[60:63], a[224:227], v[120:123]\n"
        "ds_read_b64_tr_b16 v[220:221], v255 offset:6208\n"
        "ds_read_b64_tr_b16 v[222:223], v255 offset:6976\n"
        "v_mfma_f32_16x16x32_bf16 v[120:123], a[64:67], a[228:231], v[120:123]\n"
        "buffer_load_dwordx4 v249, s[12:15], s36 offen offset:0 lds\n"
        "v_mfma_f32_16x16x32_bf16 v[120:123], a[68:71], a[232:235], v[120:123]\n"
        "v_exp_f32 v105, v105\n"
        "v_cvt_pk_bf16_f32 v197, v114, v115\n"
        "v_mfma_f32_16x16x32_bf16 v[112:115], a[92:95], a[232:235], 0\n"
        "v_exp_f32 v106, v106\n"
        "v_mfma_f32_16x16x32_bf16 v[112:115], a[88:91], a[228:231], v[112:115]\n"
        "ds_write_b128 v254, v[92:95] offset:3072\n"
        "v_mfma_f32_16x16x32_bf16 v[112:115], a[84:87], a[224:227], v[112:115]\n"
        "v_cvt_pk_bf16_f32 v72, v104, v105\n"
        "v_cvt_pk_bf16_f32 v198, v116, v117\n"
        "v_mfma_f32_16x16x32_bf16 v[112:115], a[80:83], a[220:223], v[112:115]\n"
        "v_exp_f32 v108, v108\n"
        "v_mfma_f32_16x16x32_bf16 v[112:115], a[76:79], a[216:219], v[112:115]\n"
        "s_waitcnt vmcnt(1)\n"
        "ds_read_b64_tr_b16 v[224:225], v255 offset:9216\n"
        "ds_read_b64_tr_b16 v[226:227], v255 offset:9984\n"
        "v_mfma_f32_16x16x32_bf16 v[112:115], a[72:75], a[248:251], v[112:115]\n"
        "buffer_load_dwordx4 v253, s[12:15], s36 offen offset:1024 lds\n"
        "v_cvt_pk_bf16_f32 v199, v118, v119\n"
        "s_nop 1\n"
        "v_mfma_f32_16x16x32_bf16 v[96:99], v[152:155], v[196:199], v[32:35]\n"
        "s_waitcnt lgkmcnt(3)\n"
        "v_exp_f32 v107, v107\n"
        "v_cvt_pk_bf16_f32 v200, v120, v121\n"
        "v_cvt_pk_bf16_f32 v201, v122, v123\n"
        "s_nop 1\n"
        "v_mfma_f32_16x16x32_bf16 v[96:99], v[168:171], v[200:203], v[96:99]\n"
        "v_exp_f32 v109, v109\n"
        "v_mfma_f32_16x16x32_bf16 v[100:103], v[172:175], v[196:199], v[36:39]\n"
        "ds_write_b128 v235, v[212:215] offset:6144\n"
        "v_cvt_pk_bf16_f32 v206, v120, v121\n"
        "v_cvt_pk_bf16_f32 v207, v122, v123\n"
        "s_nop 1\n"
        "v_mfma_f32_16x16x32_bf16 v[100:103], v[188:191], v[204:207], v[100:103]\n"
        "s_cmp_eq_u32 s37, 100663296\n"
        "v_cvt_pk_bf16_f32 v73, v106, v107\n"
        "v_cvt_pk_bf16_f32 v74, v108, v109\n"
        "v_mfma_f32_16x16x32_bf16 v[116:119], a[96:99], a[248:251], 0\n"
        "s_cselect_b32 s46, 0, s37\n"
        "ds_read_b128 v[202:205], v252 offset:4096\n"
        "v_cvt_pk_bf16_f32 v192, v112, v113\n"
        "v_mfma_f32_16x16x32_bf16 v[116:119], a[100:103], a[216:219], v[116:119]\n"
        "ds_read_b64_tr_b16 v[88:89], v255 offset:9248\n"
        "ds_read_b64_tr_b16 v[90:91], v255 offset:10016\n"
        "s_add_u32 m0, s41, 0\n"
        "v_mfma_f32_16x16x32_bf16 v[116:119], a[104:107], a[220:223], v[116:119]\n"
        "buffer_load_dwordx4 v242, s[8:11], s47 offen lds\n"
        "v_mfma_f32_16x16x32_bf16 v[116:119], a[108:111], a[224:227], v[116:119]\n"
        "s_add_u32 s38, s46, s43\n"
        "v_exp_f32 v110, v110\n"
        "v_mfma_f32_16x16x32_bf16 v[116:119], a[112:115], a[228:231], v[116:119]\n"
        "s_add_u32 s39, s46, s44\n"
        "v_exp_f32 v111, v111\n"
        "v_mfma_f32_16x16x32_bf16 v[116:119], a[116:119], a[232:235], v[116:119]\n"
        "ds_write_b128 v239, v[216:219] offset:6336\n"
        "s_add_u32 s37, s37, 6291456\n"
        "v_mfma_f32_16x16x32_bf16 v[120:123], a[140:143], a[232:235], 0\n"
        "v_cvt_pk_bf16_f32 v75, v110, v111\n"
        "v_cvt_pk_bf16_f32 v193, v114, v115\n"
        "v_mfma_f32_16x16x32_bf16 v[120:123], a[136:139], a[228:231], v[120:123]\n"
        "s_add_u32 m0, s41, 1024\n"
        "v_exp_f32 v96, v96\n"
        "v_mfma_f32_16x16x32_bf16 v[120:123], a[132:135], a[224:227], v[120:123]\n"
        "ds_read_b64_tr_b16 v[92:93], v255 offset:9280\n"
        "ds_read_b64_tr_b16 v[94:95], v255 offset:10048\n"
        "v_mfma_f32_16x16x32_bf16 v[120:123], a[128:131], a[220:223], v[120:123]\n"
        "buffer_load_dwordx4 v243, s[8:11], s47 offen lds\n"
        "v_mfma_f32_16x16x32_bf16 v[120:123], a[124:127], a[216:219], v[120:123]\n"
        "s_mov_b64 s[58:59], s[18:19]\n"
        "v_exp_f32 v97, v97\n"
        "v_mfma_f32_16x16x32_bf16 v[120:123], a[120:123], a[248:251], v[120:123]\n"
        "s_mov_b64 s[62:63], s[18:19]\n"
        "v_exp_f32 v98, v98\n"
        "v_mfma_f32_16x16x32_bf16 v[112:115], a[248:251], a[144:147], 0\n"
        "ds_write_b128 v254, v[220:223] offset:6144\n"
        "v_mfma_f32_16x16x32_bf16 v[112:115], a[216:219], a[148:151], v[112:115]\n"
        "v_cvt_pk_bf16_f32 v80, v96, v97\n"
        "v_cvt_pk_bf16_f32 v194, v116, v117\n"
        "v_mfma_f32_16x16x32_bf16 v[112:115], a[220:223], a[152:155], v[112:115]\n"
        "s_add_u32 m0, s41, 2048\n"
        "ds_read_b128 a[236:239], v240 offset:13824\n"
        "v_cvt_pk_bf16_f32 v195, v118, v119\n"
        "v_mfma_f32_16x16x32_bf16 v[112:115], a[224:227], a[156:159], v[112:115]\n"
        "ds_read_b128 a[240:243], v241 offset:13824\n"
        "v_cvt_pk_bf16_f32 v208, v120, v121\n"
        "v_mfma_f32_16x16x32_bf16 v[112:115], a[228:231], a[160:163], v[112:115]\n"
        "buffer_load_dwordx4 v244, s[8:11], s47 offen lds\n"
        "v_mfma_f32_16x16x32_bf16 v[112:115], a[232:235], a[164:167], v[112:115]\n"
        "ds_read_b128 a[244:247], v240 offset:13952\n"
        "v_cvt_pk_bf16_f32 v209, v122, v123\n"
        "v_mfma_f32_16x16x32_bf16 v[104:107], v[192:195], v[156:159], v[8:11]\n"
        "v_exp_f32 v100, v100\n"
        "v_mfma_f32_16x16x32_bf16 v[104:107], v[208:211], v[160:163], v[104:107]\n"
        "s_waitcnt lgkmcnt(4)\n"
        "ds_write_b128 v235, v[224:227] offset:9216\n"
        "v_mfma_f32_16x16x32_bf16 v[116:119], a[232:235], a[188:191], 0\n"
        "v_exp_f32 v99, v99\n"
        "v_mfma_f32_16x16x32_bf16 v[116:119], a[228:231], a[184:187], v[116:119]\n"
        "s_add_u32 m0, s41, 12288\n"
        "v_exp_f32 v101, v101\n"
        "v_mfma_f32_16x16x32_bf16 v[116:119], a[224:227], a[180:183], v[116:119]\n"
        "v_cvt_pk_bf16_f32 v81, v98, v99\n"
        "v_cvt_pk_bf16_f32 v82, v100, v101\n"
        "v_mfma_f32_16x16x32_bf16 v[116:119], a[220:223], a[176:179], v[116:119]\n"
        "buffer_load_dwordx4 v242, s[8:11], s35 offen lds\n"
        "v_mfma_f32_16x16x32_bf16 v[116:119], a[216:219], a[172:175], v[116:119]\n"
        "v_exp_f32 v102, v102\n"
        "v_mfma_f32_16x16x32_bf16 v[116:119], a[248:251], a[168:171], v[116:119]\n"
        "v_exp_f32 v103, v103\n"
        "v_mfma_f32_16x16x32_bf16 v[108:111], v[192:195], v[176:179], v[24:27]\n"
        "ds_write_b128 v239, v[88:91] offset:9408\n"
        "v_mfma_f32_16x16x32_bf16 v[108:111], v[208:211], v[180:183], v[108:111]\n"
        "v_cvt_pk_bf16_f32 v83, v102, v103\n"
        "v_cvt_pk_bf16_f32 v132, v112, v113\n"
        "v_mfma_f32_16x16x32_bf16 v[120:123], a[248:251], a[192:195], 0\n"
        "ds_read_b128 a[248:251], v241 offset:13952\n"
        "s_add_u32 m0, s41, 13312\n"
        "v_cvt_pk_bf16_f32 v133, v114, v115\n"
        "v_mfma_f32_16x16x32_bf16 v[120:123], a[216:219], a[196:199], v[120:123]\n"
        "ds_read_b128 a[216:219], v240 offset:14080\n"
        "v_cvt_pk_bf16_f32 v140, v116, v117\n"
        "v_mfma_f32_16x16x32_bf16 v[120:123], a[220:223], a[200:203], v[120:123]\n"
        "buffer_load_dwordx4 v243, s[8:11], s35 offen lds\n"
        "v_mfma_f32_16x16x32_bf16 v[120:123], a[224:227], a[204:207], v[120:123]\n"
        "ds_read_b128 a[220:223], v241 offset:14080\n"
        "v_cvt_pk_bf16_f32 v141, v118, v119\n"
        "v_mfma_f32_16x16x32_bf16 v[120:123], a[228:231], a[208:211], v[120:123]\n"
        "v_exp_f32 v104, v104\n"
        "v_mfma_f32_16x16x32_bf16 v[120:123], a[232:235], a[212:215], v[120:123]\n"
        "ds_write_b128 v254, v[92:95] offset:9216\n"
        "v_mfma_f32_16x16x32_bf16 v[96:99], v[192:195], v[196:199], v[40:43]\n"
        "v_exp_f32 v105, v105\n"
        "v_mfma_f32_16x16x32_bf16 v[96:99], v[208:211], v[200:203], v[96:99]\n"
        "s_waitcnt lgkmcnt(5)\n"
        "s_add_u32 m0, s41, 14336\n"
        "v_exp_f32 v106, v106\n"
        "v_mfma_f32_16x16x32_bf16 v[112:115], a[72:75], a[236:239], 0\n"
        "v_cvt_pk_bf16_f32 v68, v104, v105\n"
        "v_cvt_pk_bf16_f32 v148, v120, v121\n"
        "v_mfma_f32_16x16x32_bf16 v[112:115], a[76:79], a[240:243], v[112:115]\n"
        "buffer_load_dwordx4 v244, s[8:11], s35 offen lds\n"
        "v_mfma_f32_16x16x32_bf16 v[112:115], a[80:83], a[244:247], v[112:115]\n"
        "s_waitcnt lgkmcnt(1)\n"
        "v_exp_f32 v107, v107\n"
        "v_mfma_f32_16x16x32_bf16 v[112:115], a[84:87], a[248:251], v[112:115]\n"
        "v_cvt_pk_bf16_f32 v69, v106, v107\n"
        "v_cvt_pk_bf16_f32 v149, v122, v123\n"
        "v_mfma_f32_16x16x32_bf16 v[112:115], a[88:91], a[216:219], v[112:115]\n"
        "v_exp_f32 v108, v108\n"
        "v_mfma_f32_16x16x32_bf16 v[112:115], a[92:95], a[220:223], v[112:115]\n"
        "v_exp_f32 v109, v109\n"
        "v_mfma_f32_16x16x32_bf16 v[116:119], a[116:119], a[220:223], 0\n"
        "v_bfe_u32 v238, v250, 3, 4\n"
        "v_cvt_pk_bf16_f32 v76, v108, v109\n"
        "v_mfma_f32_16x16x32_bf16 v[116:119], a[112:115], a[216:219], v[116:119]\n"
        "v_exp_f32 v110, v110\n"
        "v_mfma_f32_16x16x32_bf16 v[116:119], a[108:111], a[248:251], v[116:119]\n"
        "v_exp_f32 v111, v111\n"
        "v_mfma_f32_16x16x32_bf16 v[116:119], a[104:107], a[244:247], v[116:119]\n"
        "s_add_u32 s56, s16, s38\n"
        "s_addc_u32 s57, s17, 0\n"
        "v_cvt_pk_bf16_f32 v212, v112, v113\n"
        "v_mfma_f32_16x16x32_bf16 v[116:119], a[100:103], a[240:243], v[116:119]\n"
        "s_add_u32 s60, s16, s39\n"
        "s_addc_u32 s61, s17, 0\n"
        "v_cvt_pk_bf16_f32 v213, v114, v115\n"
        "v_mfma_f32_16x16x32_bf16 v[116:119], a[96:99], a[236:239], v[116:119]\n"
        "v_cvt_pk_bf16_f32 v77, v110, v111\n"
        "v_add_u32 v245, s45, v245\n"
        "v_mfma_f32_16x16x32_bf16 v[120:123], a[120:123], a[236:239], 0\n"
        "v_exp_f32 v96, v96\n"
        "v_mfma_f32_16x16x32_bf16 v[120:123], a[124:127], a[240:243], v[120:123]\n"
        "s_add_u32 s47, s47, 6291456\n"
        "v_exp_f32 v97, v97\n"
        "v_mfma_f32_16x16x32_bf16 v[120:123], a[128:131], a[244:247], v[120:123]\n"
        "s_add_u32 s36, s36, 2097152\n"
        "v_cvt_pk_bf16_f32 v214, v116, v117\n"
        "v_cvt_pk_bf16_f32 v215, v118, v119\n"
        "v_mfma_f32_16x16x32_bf16 v[120:123], a[132:135], a[248:251], v[120:123]\n"
        "s_xor_b32 s48, s48, 8192\n"
        "v_exp_f32 v98, v98\n"
        "v_mfma_f32_16x16x32_bf16 v[120:123], a[136:139], a[216:219], v[120:123]\n"
        "s_waitcnt lgkmcnt(0)\n"
        "s_barrier\n"
        "v_cvt_pk_bf16_f32 v84, v96, v97\n"
        "v_and_b32 v254, 32, v250\n"
        "v_mfma_f32_16x16x32_bf16 v[120:123], a[140:143], a[220:223], v[120:123]\n"
        "v_exp_f32 v99, v99\n"
        "v_mfma_f32_16x16x32_bf16 v[112:115], a[220:223], a[164:167], 0\n"
        "v_mad_i32_i24 v238, v238, -4, v250\n"
        "v_cvt_pk_bf16_f32 v85, v98, v99\n"
        "v_mfma_f32_16x16x32_bf16 v[112:115], a[216:219], a[160:163], v[112:115]\n"
        "v_mad_i32_i24 v254, v254, -12, v248\n"
        "v_add_u32 v254, 1728, v254\n"
        "v_mfma_f32_16x16x32_bf16 v[112:115], a[248:251], a[156:159], v[112:115]\n"
        "v_cvt_pk_bf16_f32 v230, v120, v121\n"
        "v_cvt_pk_bf16_f32 v231, v122, v123\n"
        "v_mfma_f32_16x16x32_bf16 v[112:115], a[244:247], a[152:155], v[112:115]\n"
        "v_add_u32 v238, s49, v238\n"
        "v_add_u32 v240, s31, v240\n"
        "v_mfma_f32_16x16x32_bf16 v[112:115], a[240:243], a[148:151], v[112:115]\n"
        "v_add_u32 v241, s31, v241\n"
        "v_mfma_f32_16x16x32_bf16 v[112:115], a[236:239], a[144:147], v[112:115]\n"
        "s_nop 2\n"
        "v_mfma_f32_16x16x32_bf16 v[100:103], v[212:215], v[156:159], v[12:15]\n"
        "s_nop 2\n"
        "v_mfma_f32_16x16x32_bf16 v[100:103], v[228:231], v[164:167], v[100:103]\n"
        "v_cvt_pk_bf16_f32 v134, v112, v113\n"
        "v_cvt_pk_bf16_f32 v135, v114, v115\n"
        "v_mfma_f32_16x16x32_bf16 v[116:119], a[236:239], a[168:171], 0\n"
        "s_waitcnt lgkmcnt(0)\n"
        "v_mfma_f32_16x16x32_bf16 v[116:119], a[240:243], a[172:175], v[116:119]\n"
        "s_bitcmp1_b32 5, s42\n"
        "v_mfma_f32_16x16x32_bf16 v[116:119], a[244:247], a[176:179], v[116:119]\n"
        "s_cbranch_scc1 12f\n"
        "11:\n"
        "10:\n"
        "v_mfma_f32_16x16x32_bf16 v[116:119], a[248:251], a[180:183], v[116:119]\n"
        "v_mfma_f32_16x16x32_bf16 v[116:119], a[216:219], a[184:187], v[116:119]\n"
        "s_nop 0\n"
        "v_exp_f32 v100, v100\n"
        "v_mfma_f32_16x16x32_bf16 v[116:119], a[220:223], a[188:191], v[116:119]\n"
        "v_exp_f32 v101, v101\n"
        "v_mfma_f32_16x16x32_bf16 v[104:107], v[212:215], v[176:179], v[28:31]\n"
        "v_exp_f32 v102, v102\n"
        "v_mfma_f32_16x16x32_bf16 v[104:107], v[228:231], v[184:187], v[104:107]\n"
        "v_exp_f32 v103, v103\n"
        "v_mfma_f32_16x16x32_bf16 v[120:123], a[220:223], a[212:215], 0\n"
        "v_cvt_pk_bf16_f32 v70, v100, v101\n"
        "v_cvt_pk_bf16_f32 v71, v102, v103\n"
        "v_mfma_f32_16x16x32_bf16 v[120:123], a[216:219], a[208:211], v[120:123]\n"
        "v_cvt_pk_bf16_f32 v142, v116, v117\n"
        "v_cvt_pk_bf16_f32 v143, v118, v119\n"
        "v_mfma_f32_16x16x32_bf16 v[120:123], a[248:251], a[204:207], v[120:123]\n"
        "v_mov_b64 v[96:97], 0\n"
        "v_mfma_f32_16x16x32_bf16 v[120:123], a[244:247], a[200:203], v[120:123]\n"
        "v_exp_f32 v104, v104\n"
        "v_mfma_f32_16x16x32_bf16 v[120:123], a[240:243], a[196:199], v[120:123]\n"
        "v_exp_f32 v105, v105\n"
        "v_mfma_f32_16x16x32_bf16 v[120:123], a[236:239], a[192:195], v[120:123]\n"
        "v_exp_f32 v106, v106\n"
        "v_mfma_f32_16x16x32_bf16 v[108:111], v[212:215], v[196:199], v[44:47]\n"
        "v_exp_f32 v107, v107\n"
        "v_mfma_f32_16x16x32_bf16 v[108:111], v[228:231], v[204:207], v[108:111]\n"
        "v_cvt_pk_bf16_f32 v78, v104, v105\n"
        "v_cvt_pk_bf16_f32 v79, v106, v107\n"
        "v_mfma_f32_16x16x32_bf16 v[112:115], a[0:3], a[236:239], 0\n"
        "s_nop 0\n"
        "v_cvt_pk_bf16_f32 v150, v120, v121\n"
        "v_cvt_pk_bf16_f32 v151, v122, v123\n"
        "v_mfma_f32_16x16x32_bf16 v[112:115], a[4:7], a[240:243], v[112:115]\n"
        "v_mfma_f32_16x16x32_bf16 v[112:115], a[8:11], a[244:247], v[112:115]\n"
        "v_exp_f32 v108, v108\n"
        "v_mfma_f32_16x16x32_bf16 v[112:115], a[12:15], a[248:251], v[112:115]\n"
        "v_exp_f32 v109, v109\n"
        "v_mfma_f32_16x16x32_bf16 v[112:115], a[16:19], a[216:219], v[112:115]\n"
        "v_exp_f32 v110, v110\n"
        "v_mfma_f32_16x16x32_bf16 v[112:115], a[20:23], a[220:223], v[112:115]\n"
        "v_exp_f32 v111, v111\n"
        "v_smfmac_f32_16x16x64_bf16 v[96:99], a[252:255], v[64:71], v237\n"
        "v_cvt_pk_bf16_f32 v86, v108, v109\n"
        "v_mfma_f32_16x16x32_bf16 v[100:103], v[132:135], v[68:71], 0\n"
        "v_mov_b64 v[124:125], 0\n"
        "v_cvt_pk_bf16_f32 v87, v110, v111\n"
        "v_mfma_f32_16x16x32_bf16 v[100:103], v[128:131], v[64:67], v[100:103]\n"
        "s_nop 0\n"
        "v_cvt_pk_bf16_f32 v216, v112, v113\n"
        "v_cvt_pk_bf16_f32 v217, v114, v115\n"
        "v_mfma_f32_16x16x32_bf16 v[104:107], v[136:139], v[64:67], 0\n"
        "ds_read_b128 v[222:225], v251 offset:4608\n"
        "v_add_f32 v97, v96, v97\n"
        "v_mfma_f32_16x16x32_bf16 v[104:107], v[140:143], v[68:71], v[104:107]\n"
        "v_rcp_f32 v96, v97\n"
        "v_mfma_f32_16x16x32_bf16 v[108:111], v[148:151], v[68:71], 0\n"
        "v_mul_f32 v100, v96, v100\n"
        "v_mul_f32 v101, v96, v101\n"
        "v_mfma_f32_16x16x32_bf16 v[108:111], v[144:147], v[64:67], v[108:111]\n"
        "v_mul_f32 v102, v96, v102\n"
        "v_mul_f32 v103, v96, v103\n"
        "v_mfma_f32_16x16x32_bf16 v[116:119], a[44:47], a[220:223], 0\n"
        "v_cvt_pk_bf16_f32 v100, v100, v101\n"
        "v_cvt_pk_bf16_f32 v101, v102, v103\n"
        "v_mfma_f32_16x16x32_bf16 v[116:119], a[40:43], a[216:219], v[116:119]\n"
        "v_mul_f32 v104, v96, v104\n"
        "v_mul_f32 v105, v96, v105\n"
        "v_mfma_f32_16x16x32_bf16 v[116:119], a[36:39], a[248:251], v[116:119]\n"
        "v_mul_f32 v106, v96, v106\n"
        "v_mul_f32 v107, v96, v107\n"
        "v_mfma_f32_16x16x32_bf16 v[116:119], a[32:35], a[244:247], v[116:119]\n"
        "v_cvt_pk_bf16_f32 v102, v104, v105\n"
        "v_cvt_pk_bf16_f32 v103, v106, v107\n"
        "v_mfma_f32_16x16x32_bf16 v[116:119], a[28:31], a[240:243], v[116:119]\n"
        "v_mul_f32 v108, v96, v108\n"
        "v_mul_f32 v109, v96, v109\n"
        "v_mfma_f32_16x16x32_bf16 v[116:119], a[24:27], a[236:239], v[116:119]\n"
        "v_mul_f32 v110, v96, v110\n"
        "v_mul_f32 v111, v96, v111\n"
        "v_smfmac_f32_16x16x64_bf16 v[124:127], a[252:255], v[72:79], v237\n"
        "v_cvt_pk_bf16_f32 v108, v108, v109\n"
        "v_mfma_f32_16x16x32_bf16 v[64:67], v[132:135], v[76:79], 0\n"
        "ds_write_b32 v245, v108 offset:4096\n"
        "v_mfma_f32_16x16x32_bf16 v[64:67], v[128:131], v[72:75], v[64:67]\n"
        "ds_read_b128 v[156:159], v247 offset:0\n"
        "v_cvt_pk_bf16_f32 v218, v116, v117\n"
        "v_mfma_f32_16x16x32_bf16 v[68:71], v[136:139], v[72:75], 0\n"
        "ds_read_b128 v[160:163], v247 offset:4096\n"
        "v_cvt_pk_bf16_f32 v219, v118, v119\n"
        "v_mfma_f32_16x16x32_bf16 v[68:71], v[140:143], v[76:79], v[68:71]\n"
        "ds_read_b128 v[164:167], v247 offset:4352\n"
        "v_cvt_pk_bf16_f32 v109, v110, v111\n"
        "v_mfma_f32_16x16x32_bf16 v[120:123], v[148:151], v[76:79], 0\n"
        "ds_write_b32 v245, v109 offset:5120\n"
        "v_mfma_f32_16x16x32_bf16 v[120:123], v[144:147], v[72:75], v[120:123]\n"
        "v_max_u32 v236, v96, v97\n"
        "v_mov_b64 v[116:117], 0\n"
        "v_mfma_f32_16x16x32_bf16 v[112:115], a[48:51], a[236:239], 0\n"
        "v_add_f32 v125, v124, v125\n"
        "v_mfma_f32_16x16x32_bf16 v[112:115], a[52:55], a[240:243], v[112:115]\n"
        "v_rcp_f32 v124, v125\n"
        "v_mfma_f32_16x16x32_bf16 v[112:115], a[56:59], a[244:247], v[112:115]\n"
        "v_mul_f32 v64, v124, v64\n"
        "v_mul_f32 v65, v124, v65\n"
        "v_mfma_f32_16x16x32_bf16 v[112:115], a[60:63], a[248:251], v[112:115]\n"
        "v_mul_f32 v66, v124, v66\n"
        "v_mul_f32 v67, v124, v67\n"
        "v_mfma_f32_16x16x32_bf16 v[112:115], a[64:67], a[216:219], v[112:115]\n"
        "v_cvt_pk_bf16_f32 v64, v64, v65\n"
        "v_cvt_pk_bf16_f32 v65, v66, v67\n"
        "v_mfma_f32_16x16x32_bf16 v[112:115], a[68:71], a[220:223], v[112:115]\n"
        "ds_read_b128 a[216:219], v248 offset:0\n"
        "v_mul_f32 v68, v124, v68\n"
        "v_smfmac_f32_16x16x64_bf16 v[116:119], a[252:255], v[80:87], v237\n"
        "ds_read_b128 a[220:223], v248 offset:3072\n"
        "v_mfma_f32_16x16x32_bf16 v[72:75], v[132:135], v[84:87], 0\n"
        "v_mul_f32 v69, v124, v69\n"
        "v_mul_f32 v70, v124, v70\n"
        "v_mfma_f32_16x16x32_bf16 v[72:75], v[128:131], v[80:83], v[72:75]\n"
        "v_cvt_pk_bf16_f32 v220, v112, v113\n"
        "v_cvt_pk_bf16_f32 v221, v114, v115\n"
        "v_mfma_f32_16x16x32_bf16 v[76:79], v[136:139], v[80:83], 0\n"
        "ds_read_b128 a[224:227], v248 offset:32\n"
        "v_cvt_pk_bf16_f32 v226, v112, v113\n"
        "v_mfma_f32_16x16x32_bf16 v[76:79], v[140:143], v[84:87], v[76:79]\n"
        "ds_read_b128 a[228:231], v248 offset:3104\n"
        "v_cvt_pk_bf16_f32 v227, v114, v115\n"
        "v_mfma_f32_16x16x32_bf16 v[96:99], v[148:151], v[84:87], 0\n"
        "v_mul_f32 v71, v124, v71\n"
        "v_cvt_pk_bf16_f32 v66, v68, v69\n"
        "v_mfma_f32_16x16x32_bf16 v[96:99], v[144:147], v[80:83], v[96:99]\n"
        "v_cvt_pk_bf16_f32 v67, v70, v71\n"
        "v_mul_f32 v120, v124, v120\n"
        "v_mfma_f32_16x16x32_bf16 v[104:107], v[152:155], v[216:219], v[48:51]\n"
        "s_waitcnt lgkmcnt(2)\n"
        "ds_read_b128 v[152:155], v247 offset:256\n"
        "v_mul_f32 v121, v124, v121\n"
        "v_mfma_f32_16x16x32_bf16 v[104:107], v[168:171], v[220:223], v[104:107]\n"
        "v_mul_f32 v122, v124, v122\n"
        "v_mul_f32 v123, v124, v123\n"
        "v_mfma_f32_16x16x32_bf16 v[108:111], v[172:175], v[216:219], v[52:55]\n"
        "v_cvt_pk_bf16_f32 v120, v120, v121\n"
        "v_cvt_pk_bf16_f32 v121, v122, v123\n"
        "v_mfma_f32_16x16x32_bf16 v[108:111], v[188:191], v[224:227], v[108:111]\n"
        "ds_read_b128 v[222:225], v252 offset:4608\n"
        "v_max3_u32 v236, v236, v124, v125\n"
        "v_mfma_f32_16x16x32_bf16 v[176:179], v[156:159], a[216:219], 0\n"
        "ds_write_b32 v245, v120 offset:4352\n"
        "v_mfma_f32_16x16x32_bf16 v[176:179], v[160:163], a[220:223], v[176:179]\n"
        "ds_write_b32 v245, v121 offset:5376\n"
        "v_mfma_f32_16x16x32_bf16 v[196:199], v[164:167], a[220:223], 0\n"
        "s_waitcnt lgkmcnt(3)\n"
        "ds_read_b128 a[232:235], v254 offset:0\n"
        "v_add_f32 v117, v116, v117\n"
        "v_mfma_f32_16x16x32_bf16 v[196:199], v[152:155], a[216:219], v[196:199]\n"
        "ds_read_b128 a[220:223], v254 offset:3072\n"
        "v_mfma_f32_16x16x32_bf16 v[200:203], v[152:155], a[224:227], 0\n"
        "ds_read_b128 a[216:219], v254 offset:32\n"
        "v_mfma_f32_16x16x32_bf16 v[200:203], v[164:167], a[228:231], v[200:203]\n"
        "v_rcp_f32 v116, v117\n"
        "v_mfma_f32_16x16x32_bf16 v[180:183], v[160:163], a[228:231], 0\n"
        "ds_read_b128 a[228:231], v254 offset:3104\n"
        "v_mul_f32 v72, v116, v72\n"
        "v_mfma_f32_16x16x32_bf16 v[180:183], v[156:159], a[224:227], v[180:183]\n"
        "ds_read_b128 a[236:239], v247 offset:8192\n"
        "v_mul_f32 v73, v116, v73\n"
        "v_mfma_f32_16x16x32_bf16 v[112:115], v[192:195], v[216:219], v[56:59]\n"
        "s_waitcnt lgkmcnt(7)\n"
        "ds_read_b128 a[224:227], v248 offset:16\n"
        "v_mul_f32 v74, v116, v74\n"
        "v_mfma_f32_16x16x32_bf16 v[112:115], v[208:211], v[220:223], v[112:115]\n"
        "ds_read_b128 a[240:243], v247 offset:12288\n"
        "v_mul_f32 v75, v116, v75\n"
        "v_mfma_f32_16x16x32_bf16 v[68:71], v[212:215], v[216:219], v[60:63]\n"
        "ds_read_b128 a[244:247], v247 offset:12544\n"
        "v_cvt_pk_bf16_f32 v72, v72, v73\n"
        "v_mfma_f32_16x16x32_bf16 v[68:71], v[228:231], v[224:227], v[68:71]\n"
        "s_waitcnt lgkmcnt(4)\n"
        "ds_read_b128 a[248:251], v247 offset:8448\n"
        "v_cvt_pk_bf16_f32 v73, v74, v75\n"
        "v_mfma_f32_16x16x32_bf16 v[184:187], v[156:159], a[232:235], 0\n"
        "v_mul_f32 v76, v116, v76\n"
        "v_mul_f32 v77, v116, v77\n"
        "v_mfma_f32_16x16x32_bf16 v[184:187], v[160:163], a[220:223], v[184:187]\n"
        "v_mul_f32 v78, v116, v78\n"
        "v_mul_f32 v79, v116, v79\n"
        "v_mfma_f32_16x16x32_bf16 v[204:207], v[164:167], a[220:223], 0\n"
        "ds_read_b128 a[220:223], v248 offset:3088\n"
        "v_cvt_pk_bf16_f32 v74, v76, v77\n"
        "v_mfma_f32_16x16x32_bf16 v[204:207], v[152:155], a[232:235], v[204:207]\n"
        "ds_read_b128 a[232:235], v254 offset:16\n"
        "v_cvt_pk_bf16_f32 v75, v78, v79\n"
        "v_mfma_f32_16x16x32_bf16 v[192:195], v[152:155], a[216:219], 0\n"
        "v_exp_f32 v104, v104\n"
        "v_mfma_f32_16x16x32_bf16 v[192:195], v[164:167], a[228:231], v[192:195]\n"
        "v_mul_f32 v96, v116, v96\n"
        "v_mul_f32 v97, v116, v97\n"
        "v_mfma_f32_16x16x32_bf16 v[172:175], v[160:163], a[228:231], 0\n"
        "ds_read_b128 a[228:231], v254 offset:3088\n"
        "v_mul_f32 v98, v116, v98\n"
        "v_mfma_f32_16x16x32_bf16 v[172:175], v[156:159], a[216:219], v[172:175]\n"
        "s_waitcnt lgkmcnt(2)\n"
        "ds_read_b128 a[216:219], v248 offset:6144\n"
        "v_mul_f32 v99, v116, v99\n"
        "v_mfma_f32_16x16x32_bf16 v[212:215], v[156:159], a[224:227], 0\n"
        "v_exp_f32 v105, v105\n"
        "v_mfma_f32_16x16x32_bf16 v[212:215], v[160:163], a[220:223], v[212:215]\n"
        "v_exp_f32 v106, v106\n"
        "v_mfma_f32_16x16x32_bf16 v[216:219], v[164:167], a[220:223], 0\n"
        "ds_read_b128 a[220:223], v248 offset:9216\n"
        "v_cvt_pk_bf16_f32 v88, v104, v105\n"
        "v_mfma_f32_16x16x32_bf16 v[216:219], v[152:155], a[224:227], v[216:219]\n"
        "s_waitcnt lgkmcnt(2)\n"
        "ds_read_b128 a[224:227], v248 offset:6176\n"
        "v_cvt_pk_bf16_f32 v96, v96, v97\n"
        "v_mfma_f32_16x16x32_bf16 v[224:227], v[152:155], a[232:235], 0\n"
        "v_exp_f32 v108, v108\n"
        "v_mfma_f32_16x16x32_bf16 v[224:227], v[164:167], a[228:231], v[224:227]\n"
        "v_cvt_pk_bf16_f32 v97, v98, v99\n"
        "v_max3_u32 v236, v236, v116, v117\n"
        "v_mfma_f32_16x16x32_bf16 v[220:223], v[160:163], a[228:231], 0\n"
        "ds_read_b128 a[228:231], v248 offset:9248\n"
        "v_mfma_f32_16x16x32_bf16 v[220:223], v[156:159], a[232:235], v[220:223]\n"
        "s_waitcnt lgkmcnt(2)\n"
        "ds_read_b128 a[232:235], v254 offset:6144\n"
        "v_mfma_f32_16x16x32_bf16 v[176:179], a[236:239], a[216:219], v[176:179]\n"
        "v_exp_f32 v107, v107\n"
        "v_mfma_f32_16x16x32_bf16 v[176:179], a[240:243], a[220:223], v[176:179]\n"
        "v_exp_f32 v109, v109\n"
        "v_mfma_f32_16x16x32_bf16 v[196:199], a[244:247], a[220:223], v[196:199]\n"
        "ds_read_b128 a[220:223], v254 offset:9216\n"
        "v_cvt_pk_bf16_f32 v89, v106, v107\n"
        "v_mfma_f32_16x16x32_bf16 v[196:199], a[248:251], a[216:219], v[196:199]\n"
        "s_waitcnt lgkmcnt(2)\n"
        "ds_read_b128 a[216:219], v254 offset:6176\n"
        "v_cvt_pk_bf16_f32 v90, v108, v109\n"
        "v_mfma_f32_16x16x32_bf16 v[200:203], a[248:251], a[224:227], v[200:203]\n"
        ".long 0xbec200ff\n"
        "44:\n"
        "s_branch 60b\n"
        "ds_read_b128 v[156:159], v247 offset:16384\n"
        "v_mfma_f32_16x16x32_bf16 v[200:203], a[244:247], a[228:231], v[200:203]\n"
        "ds_read_b128 v[160:163], v247 offset:20480\n"
        "v_mfma_f32_16x16x32_bf16 v[180:183], a[240:243], a[228:231], v[180:183]\n"
        "ds_read_b128 a[228:231], v254 offset:9248\n"
        "v_mfma_f32_16x16x32_bf16 v[180:183], a[236:239], a[224:227], v[180:183]\n"
        "s_waitcnt lgkmcnt(4)\n"
        "ds_read_b128 a[224:227], v248 offset:6160\n"
        "v_mfma_f32_16x16x32_bf16 v[184:187], a[236:239], a[232:235], v[184:187]\n"
        "ds_read_b128 v[164:167], v247 offset:20736\n"
        "v_mfma_f32_16x16x32_bf16 v[184:187], a[240:243], a[220:223], v[184:187]\n"
        "ds_read_b128 v[152:155], v247 offset:16640\n"
        "v_mfma_f32_16x16x32_bf16 v[204:207], a[244:247], a[220:223], v[204:207]\n"
        "ds_read_b128 a[220:223], v248 offset:9232\n"
        "v_mfma_f32_16x16x32_bf16 v[204:207], a[248:251], a[232:235], v[204:207]\n"
        "s_waitcnt lgkmcnt(4)\n"
        "ds_read_b128 a[232:235], v254 offset:6160\n"
        "v_mfma_f32_16x16x32_bf16 v[192:195], a[248:251], a[216:219], v[192:195]\n"
        "v_exp_f32 v110, v110\n"
        "v_mfma_f32_16x16x32_bf16 v[192:195], a[244:247], a[228:231], v[192:195]\n"
        "v_exp_f32 v111, v111\n"
        "v_mfma_f32_16x16x32_bf16 v[172:175], a[240:243], a[228:231], v[172:175]\n"
        "ds_read_b128 a[228:231], v254 offset:9232\n"
        "v_cvt_pk_bf16_f32 v91, v110, v111\n"
        "v_mfma_f32_16x16x32_bf16 v[172:175], a[236:239], a[216:219], v[172:175]\n"
        "s_waitcnt lgkmcnt(2)\n"
        "ds_read_b128 a[216:219], v248 offset:12288\n"
        "v_mfma_f32_16x16x32_bf16 v[212:215], a[236:239], a[224:227], v[212:215]\n"
        "v_exp_f32 v112, v112\n"
        "v_mfma_f32_16x16x32_bf16 v[212:215], a[240:243], a[220:223], v[212:215]\n"
        "v_exp_f32 v113, v113\n"
        "v_mfma_f32_16x16x32_bf16 v[216:219], a[244:247], a[220:223], v[216:219]\n"
        "ds_read_b128 a[220:223], v248 offset:15360\n"
        "v_cvt_pk_bf16_f32 v92, v112, v113\n"
        "v_mfma_f32_16x16x32_bf16 v[216:219], a[248:251], a[224:227], v[216:219]\n"
        "s_waitcnt lgkmcnt(2)\n"
        "ds_read_b128 a[224:227], v248 offset:12320\n"
        "v_mfma_f32_16x16x32_bf16 v[224:227], a[248:251], a[232:235], v[224:227]\n"
        "v_exp_f32 v114, v114\n"
        "v_mfma_f32_16x16x32_bf16 v[224:227], a[244:247], a[228:231], v[224:227]\n"
        "v_exp_f32 v68, v68\n"
        "v_mfma_f32_16x16x32_bf16 v[220:223], a[240:243], a[228:231], v[220:223]\n"
        "ds_read_b128 a[228:231], v248 offset:15392\n"
        "v_mfma_f32_16x16x32_bf16 v[220:223], a[236:239], a[232:235], v[220:223]\n"
        "s_waitcnt lgkmcnt(2)\n"
        "ds_read_b128 a[232:235], v254 offset:12288\n"
        "v_mfma_f32_16x16x32_bf16 v[176:179], v[156:159], a[216:219], v[176:179]\n"
        "v_exp_f32 v115, v115\n"
        "v_mfma_f32_16x16x32_bf16 v[176:179], v[160:163], a[220:223], v[176:179]\n"
        "v_exp_f32 v69, v69\n"
        "v_mfma_f32_16x16x32_bf16 v[196:199], v[164:167], a[220:223], v[196:199]\n"
        "ds_read_b128 a[220:223], v254 offset:15360\n"
        "v_cvt_pk_bf16_f32 v93, v114, v115\n"
        "v_mfma_f32_16x16x32_bf16 v[196:199], v[152:155], a[216:219], v[196:199]\n"
        "s_waitcnt lgkmcnt(2)\n"
        "ds_read_b128 a[216:219], v254 offset:12320\n"
        "v_cvt_pk_bf16_f32 v94, v68, v69\n"
        "v_mfma_f32_16x16x32_bf16 v[200:203], v[152:155], a[224:227], v[200:203]\n"
        "v_exp_f32 v70, v70\n"
        "v_mfma_f32_16x16x32_bf16 v[200:203], v[164:167], a[228:231], v[200:203]\n"
        "s_waitcnt vmcnt(0)\n"
        "v_exp_f32 v71, v71\n"
        "v_mfma_f32_16x16x32_bf16 v[180:183], v[160:163], a[228:231], v[180:183]\n"
        "ds_read_b128 a[228:231], v254 offset:15392\n"
        "v_cvt_pk_bf16_f32 v95, v70, v71\n"
        "v_mfma_f32_16x16x32_bf16 v[180:183], v[156:159], a[224:227], v[180:183]\n"
        "s_waitcnt lgkmcnt(2)\n"
        "ds_read_b128 a[224:227], v248 offset:12304\n"
        "v_mfma_f32_16x16x32_bf16 v[184:187], v[156:159], a[232:235], v[184:187]\n"
        "v_mfma_f32_16x16x32_bf16 v[184:187], v[160:163], a[220:223], v[184:187]\n"
        "s_barrier\n"
        "v_mfma_f32_16x16x32_bf16 v[204:207], v[164:167], a[220:223], v[204:207]\n"
        "ds_read_b128 a[220:223], v248 offset:15376\n"
        "v_mfma_f32_16x16x32_bf16 v[204:207], v[152:155], a[232:235], v[204:207]\n"
        "s_waitcnt lgkmcnt(2)\n"
        "ds_read_b128 a[232:235], v254 offset:12304\n"
        "v_mfma_f32_16x16x32_bf16 v[192:195], v[152:155], a[216:219], v[192:195]\n"
        "ds_read_b128 a[236:239], v240 offset:0\n"
        "v_mfma_f32_16x16x32_bf16 v[192:195], v[164:167], a[228:231], v[192:195]\n"
        "ds_read_b128 a[240:243], v241 offset:0\n"
        "v_mfma_f32_16x16x32_bf16 v[172:175], v[160:163], a[228:231], v[172:175]\n"
        "ds_read_b128 a[228:231], v254 offset:15376\n"
        "v_mfma_f32_16x16x32_bf16 v[172:175], v[156:159], a[216:219], v[172:175]\n"
        "s_waitcnt lgkmcnt(4)\n"
        "ds_read_b128 a[244:247], v240 offset:128\n"
        "v_mfma_f32_16x16x32_bf16 v[212:215], v[156:159], a[224:227], v[212:215]\n"
        "ds_read_b128 a[248:251], v241 offset:128\n"
        "v_mfma_f32_16x16x32_bf16 v[212:215], v[160:163], a[220:223], v[212:215]\n"
        "ds_read_b128 a[216:219], v240 offset:256\n"
        "v_mfma_f32_16x16x32_bf16 v[216:219], v[164:167], a[220:223], v[216:219]\n"
        "ds_read_b128 a[220:223], v241 offset:256\n"
        "v_mfma_f32_16x16x32_bf16 v[216:219], v[152:155], a[224:227], v[216:219]\n"
        "s_waitcnt lgkmcnt(4)\n"
        "v_mfma_f32_16x16x32_bf16 v[224:227], v[152:155], a[232:235], v[224:227]\n"
        "v_mfma_f32_16x16x32_bf16 v[224:227], v[164:167], a[228:231], v[224:227]\n"
        "ds_write_b32 v245, v96 offset:4608\n"
        "v_mfma_f32_16x16x32_bf16 v[220:223], v[160:163], a[228:231], v[220:223]\n"
        "v_mfma_f32_16x16x32_bf16 v[220:223], v[156:159], a[232:235], v[220:223]\n"
        "ds_write_b32 v245, v97 offset:5632\n"
        "v_mfma_f32_16x16x32_bf16 v[112:115], a[72:75], a[236:239], 0\n"
        "v_mov_b64 v[124:125], 0\n"
        "v_cvt_pk_bf16_f32 v232, v176, v180\n"
        "v_mfma_f32_16x16x32_bf16 v[112:115], a[76:79], a[240:243], v[112:115]\n"
        "s_waitcnt lgkmcnt(2)\n"
        "v_cvt_pk_bf16_f32 v233, v184, v172\n"
        "v_cvt_pk_bf16_f32 v234, v177, v181\n"
        "v_mfma_f32_16x16x32_bf16 v[112:115], a[80:83], a[244:247], v[112:115]\n"
        "buffer_store_dwordx2 v[232:233], v250, s[56:59], 0 offen offset:0 sc1\n"
        "v_cvt_pk_bf16_f32 v235, v185, v173\n"
        "v_mfma_f32_16x16x32_bf16 v[112:115], a[84:87], a[248:251], v[112:115]\n"
        "v_cvt_pk_bf16_f32 v232, v178, v182\n"
        "v_cvt_pk_bf16_f32 v233, v186, v174\n"
        "v_mfma_f32_16x16x32_bf16 v[112:115], a[88:91], a[216:219], v[112:115]\n"
        "v_mfma_f32_16x16x32_bf16 v[112:115], a[92:95], a[220:223], v[112:115]\n"
        "ds_write_b32 v245, v100 offset:0\n"
        "v_smfmac_f32_16x16x64_bf16 v[124:127], a[252:255], v[88:95], v237\n"
        "s_nop 1\n"
        "v_mfma_f32_16x16x32_bf16 v[80:83], v[132:135], v[92:95], 0\n"
        "buffer_store_dwordx2 v[234:235], v250, s[56:59], 0 offen offset:384 sc1\n"
        "s_nop 0\n"
        "v_mfma_f32_16x16x32_bf16 v[80:83], v[128:131], v[88:91], v[80:83]\n"
        "s_add_u32 s41, s30, s40\n"
        "v_cvt_pk_bf16_f32 v152, v112, v113\n"
        "v_cvt_pk_bf16_f32 v153, v114, v115\n"
        "v_mfma_f32_16x16x32_bf16 v[84:87], v[136:139], v[88:91], 0\n"
        "v_cvt_pk_bf16_f32 v234, v179, v183\n"
        "v_cvt_pk_bf16_f32 v235, v187, v175\n"
        "v_mfma_f32_16x16x32_bf16 v[84:87], v[140:143], v[92:95], v[84:87]\n"
        "v_add_f32 v125, v124, v125\n"
        "v_mfma_f32_16x16x32_bf16 v[96:99], v[148:151], v[92:95], 0\n"
        "buffer_store_dwordx2 v[232:233], v250, s[56:59], 0 offen offset:768 sc1\n"
        "v_mfma_f32_16x16x32_bf16 v[96:99], v[144:147], v[88:91], v[96:99]\n"
        "s_add_u32 s35, s47, 786432\n"
        "v_rcp_f32 v124, v125\n"
        "v_mfma_f32_16x16x32_bf16 v[116:119], a[116:119], a[220:223], 0\n"
        "s_sub_u32 s46, s35, 100663296\n"
        "v_mul_f32 v80, v124, v80\n"
        "v_mul_f32 v81, v124, v81\n"
        "v_mfma_f32_16x16x32_bf16 v[116:119], a[112:115], a[216:219], v[116:119]\n"
        "s_cmp_lt_u32 s46, 786432\n"
        "v_mul_f32 v82, v124, v82\n"
        "v_mul_f32 v83, v124, v83\n"
        "v_mfma_f32_16x16x32_bf16 v[116:119], a[108:111], a[248:251], v[116:119]\n"
        "buffer_store_dwordx2 v[234:235], v250, s[56:59], 0 offen offset:1152 sc1\n"
        "v_cvt_pk_bf16_f32 v80, v80, v81\n"
        "v_mfma_f32_16x16x32_bf16 v[116:119], a[104:107], a[244:247], v[116:119]\n"
        "s_cselect_b32 s35, s46, s35\n"
        "v_cvt_pk_bf16_f32 v81, v82, v83\n"
        "v_cvt_pk_bf16_f32 v232, v196, v200\n"
        "v_mfma_f32_16x16x32_bf16 v[116:119], a[100:103], a[240:243], v[116:119]\n"
        "v_cvt_pk_bf16_f32 v233, v204, v192\n"
        "v_mul_f32 v84, v124, v84\n"
        "v_mfma_f32_16x16x32_bf16 v[116:119], a[96:99], a[236:239], v[116:119]\n"
        "v_mul_f32 v85, v124, v85\n"
        "v_mul_f32 v86, v124, v86\n"
        "v_mfma_f32_16x16x32_bf16 v[120:123], a[120:123], a[236:239], 0\n"
        "ds_write_b32 v245, v101 offset:1024\n"
        "v_mfma_f32_16x16x32_bf16 v[120:123], a[124:127], a[240:243], v[120:123]\n"
        "buffer_store_dwordx2 v[232:233], v250, s[60:63], 0 offen offset:0 sc1\n"
        "v_mul_f32 v87, v124, v87\n"
        "v_mfma_f32_16x16x32_bf16 v[120:123], a[128:131], a[244:247], v[120:123]\n"
        "v_cvt_pk_bf16_f32 v82, v84, v85\n"
        "v_cvt_pk_bf16_f32 v83, v86, v87\n"
        "v_mfma_f32_16x16x32_bf16 v[120:123], a[132:135], a[248:251], v[120:123]\n"
        "v_mul_f32 v96, v124, v96\n"
        "v_mul_f32 v97, v124, v97\n"
        "v_mfma_f32_16x16x32_bf16 v[120:123], a[136:139], a[216:219], v[120:123]\n"
        "v_mul_f32 v98, v124, v98\n"
        "v_mul_f32 v99, v124, v99\n"
        "v_mfma_f32_16x16x32_bf16 v[120:123], a[140:143], a[220:223], v[120:123]\n"
        "v_cvt_pk_bf16_f32 v234, v197, v201\n"
        "v_cvt_pk_bf16_f32 v235, v205, v193\n"
        "v_mfma_f32_16x16x32_bf16 v[112:115], a[20:23], a[220:223], 0\n"
        "buffer_store_dwordx2 v[234:235], v250, s[60:63], 0 offen offset:384 sc1\n"
        "v_cvt_pk_bf16_f32 v154, v116, v117\n"
        "v_mfma_f32_16x16x32_bf16 v[112:115], a[16:19], a[216:219], v[112:115]\n"
        "v_cvt_pk_bf16_f32 v155, v118, v119\n"
        "v_cvt_pk_bf16_f32 v96, v96, v97\n"
        "v_mfma_f32_16x16x32_bf16 v[112:115], a[12:15], a[248:251], v[112:115]\n"
        "ds_write_b32 v245, v96 offset:4864\n"
        "v_mfma_f32_16x16x32_bf16 v[112:115], a[8:11], a[244:247], v[112:115]\n"
        "v_cvt_pk_bf16_f32 v97, v98, v99\n"
        "v_max3_u32 v236, v236, v124, v125\n"
        "v_mfma_f32_16x16x32_bf16 v[112:115], a[4:7], a[240:243], v[112:115]\n"
        "v_cvt_pk_bf16_f32 v232, v198, v202\n"
        "v_cvt_pk_bf16_f32 v233, v206, v194\n"
        "v_mfma_f32_16x16x32_bf16 v[112:115], a[0:3], a[236:239], v[112:115]\n"
        "buffer_store_dwordx2 v[232:233], v250, s[60:63], 0 offen offset:768 sc1\n"
        "v_cmp_lt_u32 vcc, s24, v236\n"
        "v_mfma_f32_16x16x32_bf16 v[116:119], a[24:27], a[236:239], 0\n"
        "v_cvt_pk_bf16_f32 v168, v120, v121\n"
        "v_cvt_pk_bf16_f32 v169, v122, v123\n"
        "v_mfma_f32_16x16x32_bf16 v[116:119], a[28:31], a[240:243], v[116:119]\n"
        "s_cmp_lg_u64 vcc, 0\n"
        "s_addc_u32 s25, s25, s25\n"
        "v_cvt_pk_bf16_f32 v234, v199, v203\n"
        "v_mfma_f32_16x16x32_bf16 v[116:119], a[32:35], a[244:247], v[116:119]\n"
        "v_cvt_pk_bf16_f32 v235, v207, v195\n"
        "v_cvt_pk_bf16_f32 v156, v112, v113\n"
        "v_mfma_f32_16x16x32_bf16 v[116:119], a[36:39], a[248:251], v[116:119]\n"
        "buffer_store_dwordx2 v[234:235], v250, s[60:63], 0 offen offset:1152 sc1\n"
        "v_cvt_pk_bf16_f32 v157, v114, v115\n"
        "v_mfma_f32_16x16x32_bf16 v[116:119], a[40:43], a[216:219], v[116:119]\n"
        "v_cvt_pk_bf16_f32 v232, v212, v220\n"
        "v_cvt_pk_bf16_f32 v234, v213, v221\n"
        "v_mfma_f32_16x16x32_bf16 v[116:119], a[44:47], a[220:223], v[116:119]\n"
        "ds_write_b32 v245, v102 offset:2048\n"
        "v_mfma_f32_16x16x32_bf16 v[120:123], a[68:71], a[220:223], 0\n"
        "v_xor_b32 v251, 0x2000, v251\n"
        "v_xor_b32 v252, 0x2000, v252\n"
        "v_mfma_f32_16x16x32_bf16 v[120:123], a[64:67], a[216:219], v[120:123]\n"
        "buffer_store_dword v232, v238, s[56:59], 0 offen offset:0 sc1\n"
        "v_mfma_f32_16x16x32_bf16 v[120:123], a[60:63], a[248:251], v[120:123]\n"
        "ds_read_b128 a[224:227], v240 offset:1536\n"
        "v_cvt_pk_bf16_f32 v158, v116, v117\n"
        "v_mfma_f32_16x16x32_bf16 v[120:123], a[56:59], a[244:247], v[120:123]\n"
        "ds_read_b128 a[228:231], v241 offset:1536\n"
        "v_cvt_pk_bf16_f32 v159, v118, v119\n"
        "v_mfma_f32_16x16x32_bf16 v[120:123], a[52:55], a[240:243], v[120:123]\n"
        "ds_read_b128 a[232:235], v240 offset:1664\n"
        "v_cvt_pk_bf16_f32 v232, v214, v222\n"
        "v_mfma_f32_16x16x32_bf16 v[120:123], a[48:51], a[236:239], v[120:123]\n"
        "buffer_store_dword v234, v238, s[56:59], 0 offen offset:384 sc1\n"
        "v_mfma_f32_16x16x32_bf16 v[112:115], a[236:239], a[144:147], 0\n"
        "v_add_u32 v247, s45, v247\n"
        "v_cvt_pk_bf16_f32 v234, v215, v223\n"
        "v_mfma_f32_16x16x32_bf16 v[112:115], a[240:243], a[148:151], v[112:115]\n"
        "ds_write_b32 v245, v103 offset:3072\n"
        "v_mfma_f32_16x16x32_bf16 v[112:115], a[244:247], a[152:155], v[112:115]\n"
        "s_nop 0\n"
        "v_cvt_pk_bf16_f32 v160, v120, v121\n"
        "v_cvt_pk_bf16_f32 v161, v122, v123\n"
        "v_mfma_f32_16x16x32_bf16 v[112:115], a[248:251], a[156:159], v[112:115]\n"
        "v_cvt_pk_bf16_f32 v166, v120, v121\n"
        "v_cvt_pk_bf16_f32 v167, v122, v123\n"
        "v_mfma_f32_16x16x32_bf16 v[112:115], a[216:219], a[160:163], v[112:115]\n"
        "buffer_store_dword v232, v238, s[56:59], 0 offen offset:768 sc1\n"
        "v_mfma_f32_16x16x32_bf16 v[112:115], a[220:223], a[164:167], v[112:115]\n"
        "ds_write_b32 v245, v97 offset:5888\n"
        "v_mfma_f32_16x16x32_bf16 v[116:119], a[220:223], a[188:191], 0\n"
        "v_cvt_pk_bf16_f32 v232, v216, v224\n"
        "s_nop 0\n"
        "v_mfma_f32_16x16x32_bf16 v[116:119], a[216:219], a[184:187], v[116:119]\n"
        "s_nop 2\n"
        "v_mfma_f32_16x16x32_bf16 v[116:119], a[248:251], a[180:183], v[116:119]\n"
        "buffer_store_dword v234, v238, s[56:59], 0 offen offset:1152 sc1\n"
        "v_mfma_f32_16x16x32_bf16 v[116:119], a[244:247], a[176:179], v[116:119]\n"
        "v_cvt_pk_bf16_f32 v128, v112, v113\n"
        "v_cvt_pk_bf16_f32 v129, v114, v115\n"
        "v_mfma_f32_16x16x32_bf16 v[116:119], a[240:243], a[172:175], v[116:119]\n"
        "ds_write_b32 v245, v64 offset:256\n"
        "v_mfma_f32_16x16x32_bf16 v[116:119], a[236:239], a[168:171], v[116:119]\n"
        "v_cvt_pk_bf16_f32 v234, v217, v225\n"
        "s_nop 0\n"
        "v_mfma_f32_16x16x32_bf16 v[120:123], a[236:239], a[192:195], 0\n"
        "ds_read_b128 a[236:239], v241 offset:1664\n"
        "s_nop 0\n"
        "v_mfma_f32_16x16x32_bf16 v[120:123], a[240:243], a[196:199], v[120:123]\n"
        "buffer_store_dword v232, v238, s[60:63], 0 offen offset:0 sc1\n"
        "v_mfma_f32_16x16x32_bf16 v[120:123], a[244:247], a[200:203], v[120:123]\n"
        "ds_read_b128 a[240:243], v240 offset:1792\n"
        "ds_read_b128 a[244:247], v241 offset:1792\n"
        "v_mfma_f32_16x16x32_bf16 v[120:123], a[248:251], a[204:207], v[120:123]\n"
        "v_cvt_pk_bf16_f32 v136, v116, v117\n"
        "v_cvt_pk_bf16_f32 v137, v118, v119\n"
        "v_mfma_f32_16x16x32_bf16 v[120:123], a[216:219], a[208:211], v[120:123]\n"
        "v_cvt_pk_bf16_f32 v232, v218, v226\n"
        "v_mfma_f32_16x16x32_bf16 v[120:123], a[220:223], a[212:215], v[120:123]\n"
        "s_waitcnt lgkmcnt(4)\n"
        "buffer_store_dword v234, v238, s[60:63], 0 offen offset:384 sc1\n"
        "v_mfma_f32_16x16x32_bf16 v[112:115], a[72:75], a[224:227], 0\n"
        "s_nop 2\n"
        "v_mfma_f32_16x16x32_bf16 v[112:115], a[76:79], a[228:231], v[112:115]\n"
        "v_cvt_pk_bf16_f32 v234, v219, v227\n"
        "s_nop 0\n"
        "v_mfma_f32_16x16x32_bf16 v[112:115], a[80:83], a[232:235], v[112:115]\n"
        "s_waitcnt lgkmcnt(0)\n"
        "v_cvt_pk_bf16_f32 v144, v120, v121\n"
        "v_cvt_pk_bf16_f32 v145, v122, v123\n"
        "v_mfma_f32_16x16x32_bf16 v[112:115], a[84:87], a[236:239], v[112:115]\n"
        "ds_write_b32 v245, v65 offset:1280\n"
        "v_mfma_f32_16x16x32_bf16 v[112:115], a[88:91], a[240:243], v[112:115]\n"
        "buffer_store_dword v232, v238, s[60:63], 0 offen offset:768 sc1\n"
        "v_mfma_f32_16x16x32_bf16 v[112:115], a[92:95], a[244:247], v[112:115]\n"
        "s_nop 2\n"
        "v_mfma_f32_16x16x32_bf16 v[116:119], a[116:119], a[244:247], 0\n"
        "s_nop 2\n"
        "v_mfma_f32_16x16x32_bf16 v[116:119], a[112:115], a[240:243], v[116:119]\n"
        "v_cvt_pk_bf16_f32 v172, v112, v113\n"
        "v_cvt_pk_bf16_f32 v173, v114, v115\n"
        "v_mfma_f32_16x16x32_bf16 v[116:119], a[108:111], a[236:239], v[116:119]\n"
        "buffer_store_dword v234, v238, s[60:63], 0 offen offset:1152 sc1\n"
        "v_mfma_f32_16x16x32_bf16 v[116:119], a[104:107], a[232:235], v[116:119]\n"
        "ds_write_b32 v245, v66 offset:2304\n"
        "v_mfma_f32_16x16x32_bf16 v[116:119], a[100:103], a[228:231], v[116:119]\n"
        "v_mfma_f32_16x16x32_bf16 v[116:119], a[96:99], a[224:227], v[116:119]\n"
        "s_mov_b32 m0, s48\n"
        "s_nop 1\n"
        "v_mfma_f32_16x16x32_bf16 v[120:123], a[120:123], a[224:227], 0\n"
        "ds_read_b128 v[162:165], v251 offset:0\n"
        "s_nop 0\n"
        "v_mfma_f32_16x16x32_bf16 v[120:123], a[124:127], a[228:231], v[120:123]\n"
        "buffer_load_dwordx4 v249, s[12:15], s36 offen offset:0 lds\n"
        "v_mfma_f32_16x16x32_bf16 v[120:123], a[128:131], a[232:235], v[120:123]\n"
        "v_cvt_pk_bf16_f32 v174, v116, v117\n"
        "v_cvt_pk_bf16_f32 v175, v118, v119\n"
        "v_mfma_f32_16x16x32_bf16 v[120:123], a[132:135], a[236:239], v[120:123]\n"
        "ds_write_b32 v245, v67 offset:3328\n"
        "v_mfma_f32_16x16x32_bf16 v[120:123], a[136:139], a[240:243], v[120:123]\n"
        "ds_write_b32 v245, v72 offset:512\n"
        "v_mfma_f32_16x16x32_bf16 v[120:123], a[140:143], a[244:247], v[120:123]\n"
        "s_nop 2\n"
        "v_mfma_f32_16x16x32_bf16 v[112:115], a[20:23], a[244:247], 0\n"
        "s_nop 2\n"
        "v_mfma_f32_16x16x32_bf16 v[112:115], a[16:19], a[240:243], v[112:115]\n"
        "v_cvt_pk_bf16_f32 v190, v120, v121\n"
        "v_cvt_pk_bf16_f32 v191, v122, v123\n"
        "v_mfma_f32_16x16x32_bf16 v[112:115], a[12:15], a[236:239], v[112:115]\n"
        "ds_write_b32 v245, v73 offset:1536\n"
        "v_mfma_f32_16x16x32_bf16 v[112:115], a[8:11], a[232:235], v[112:115]\n"
        "ds_write_b32 v245, v74 offset:2560\n"
        "v_mfma_f32_16x16x32_bf16 v[112:115], a[4:7], a[228:231], v[112:115]\n"
        "ds_read_b128 v[182:185], v251 offset:512\n"
        "v_mfma_f32_16x16x32_bf16 v[112:115], a[0:3], a[224:227], v[112:115]\n"
        "s_nop 2\n"
        "v_mfma_f32_16x16x32_bf16 v[116:119], a[24:27], a[224:227], 0\n"
        "s_nop 2\n"
        "v_mfma_f32_16x16x32_bf16 v[116:119], a[28:31], a[228:231], v[116:119]\n"
        "v_cvt_pk_bf16_f32 v176, v112, v113\n"
        "v_cvt_pk_bf16_f32 v177, v114, v115\n"
        "v_mfma_f32_16x16x32_bf16 v[116:119], a[32:35], a[232:235], v[116:119]\n"
        "ds_write_b32 v245, v75 offset:3584\n"
        "v_mfma_f32_16x16x32_bf16 v[116:119], a[36:39], a[236:239], v[116:119]\n"
        "buffer_load_dwordx4 v253, s[12:15], s36 offen offset:1024 lds\n"
        "v_mfma_f32_16x16x32_bf16 v[116:119], a[40:43], a[240:243], v[116:119]\n"
        "ds_write_b32 v245, v80 offset:768\n"
        "v_mfma_f32_16x16x32_bf16 v[116:119], a[44:47], a[244:247], v[116:119]\n"
        "s_nop 2\n"
        "v_mfma_f32_16x16x32_bf16 v[120:123], a[68:71], a[244:247], 0\n"
        "s_nop 2\n"
        "v_mfma_f32_16x16x32_bf16 v[120:123], a[64:67], a[240:243], v[120:123]\n"
        "v_cvt_pk_bf16_f32 v178, v116, v117\n"
        "v_cvt_pk_bf16_f32 v179, v118, v119\n"
        "v_mfma_f32_16x16x32_bf16 v[120:123], a[60:63], a[236:239], v[120:123]\n"
        "ds_write_b32 v245, v81 offset:1792\n"
        "v_mfma_f32_16x16x32_bf16 v[120:123], a[56:59], a[232:235], v[120:123]\n"
        "ds_write_b32 v245, v82 offset:2816\n"
        "v_mfma_f32_16x16x32_bf16 v[120:123], a[52:55], a[228:231], v[120:123]\n"
        "ds_write_b32 v245, v83 offset:3840\n"
        "v_mfma_f32_16x16x32_bf16 v[120:123], a[48:51], a[224:227], v[120:123]\n"
        "s_nop 2\n"
        "v_mfma_f32_16x16x32_bf16 v[112:115], a[224:227], a[144:147], 0\n"
        "s_nop 2\n"
        "v_mfma_f32_16x16x32_bf16 v[112:115], a[228:231], a[148:151], v[112:115]\n"
        "v_cvt_pk_bf16_f32 v180, v120, v121\n"
        "v_cvt_pk_bf16_f32 v181, v122, v123\n"
        "v_mfma_f32_16x16x32_bf16 v[112:115], a[232:235], a[152:155], v[112:115]\n"
        "v_cvt_pk_bf16_f32 v186, v120, v121\n"
        "v_cvt_pk_bf16_f32 v187, v122, v123\n"
        "v_mfma_f32_16x16x32_bf16 v[112:115], a[236:239], a[156:159], v[112:115]\n"
        "s_add_u32 m0, s41, 0\n"
        "v_mfma_f32_16x16x32_bf16 v[112:115], a[240:243], a[160:163], v[112:115]\n"
        "ds_read_b128 a[248:251], v240 offset:12288\n"
        "v_mfma_f32_16x16x32_bf16 v[112:115], a[244:247], a[164:167], v[112:115]\n"
        "buffer_load_dwordx4 v242, s[8:11], s47 offen lds\n"
        "v_mfma_f32_16x16x32_bf16 v[96:99], v[152:155], v[156:159], v[0:3]\n"
        "s_waitcnt lgkmcnt(2)\n"
        "ds_read_b128 a[216:219], v241 offset:12288\n"
        "s_nop 0\n"
        "v_mfma_f32_16x16x32_bf16 v[96:99], v[168:171], v[160:163], v[96:99]\n"
        "ds_read_b128 a[220:223], v240 offset:12416\n"
        "s_nop 0\n"
        "v_mfma_f32_16x16x32_bf16 v[100:103], v[172:175], v[156:159], v[4:7]\n"
        "s_sub_u32 s31, s30, s28\n"
        "v_cvt_pk_bf16_f32 v130, v112, v113\n"
        "v_cvt_pk_bf16_f32 v131, v114, v115\n"
        "v_mfma_f32_16x16x32_bf16 v[100:103], v[188:191], v[164:167], v[100:103]\n"
        "s_sub_u32 s45, s28, s29\n"
        "ds_read_b128 v[162:165], v252 offset:0\n"
        "v_mfma_f32_16x16x32_bf16 v[116:119], a[244:247], a[188:191], 0\n"
        "s_add_u32 s28, s28, s31\n"
        "v_exp_f32 v96, v96\n"
        "v_mfma_f32_16x16x32_bf16 v[116:119], a[240:243], a[184:187], v[116:119]\n"
        "s_mov_b32 s30, s29\n"
        "v_exp_f32 v97, v97\n"
        "v_mfma_f32_16x16x32_bf16 v[116:119], a[236:239], a[180:183], v[116:119]\n"
        "s_add_u32 s29, s29, s45\n"
        "v_exp_f32 v98, v98\n"
        "v_mfma_f32_16x16x32_bf16 v[116:119], a[232:235], a[176:179], v[116:119]\n"
        "v_exp_f32 v100, v100\n"
        "v_mfma_f32_16x16x32_bf16 v[116:119], a[228:231], a[172:175], v[116:119]\n"
        "v_exp_f32 v99, v99\n"
        "v_mfma_f32_16x16x32_bf16 v[116:119], a[224:227], a[168:171], v[116:119]\n"
        "v_exp_f32 v101, v101\n"
        "v_mfma_f32_16x16x32_bf16 v[104:107], v[152:155], v[176:179], v[16:19]\n"
        "v_exp_f32 v102, v102\n"
        "v_mfma_f32_16x16x32_bf16 v[104:107], v[168:171], v[180:183], v[104:107]\n"
        "s_add_u32 m0, s41, 1024\n"
        "v_cvt_pk_bf16_f32 v64, v96, v97\n"
        "v_cvt_pk_bf16_f32 v65, v98, v99\n"
        "v_mfma_f32_16x16x32_bf16 v[108:111], v[172:175], v[176:179], v[20:23]\n"
        "v_cvt_pk_bf16_f32 v138, v116, v117\n"
        "v_cvt_pk_bf16_f32 v139, v118, v119\n"
        "v_mfma_f32_16x16x32_bf16 v[108:111], v[188:191], v[184:187], v[108:111]\n"
        "buffer_load_dwordx4 v243, s[8:11], s47 offen lds\n"
        "v_mfma_f32_16x16x32_bf16 v[120:123], a[224:227], a[192:195], 0\n"
        "ds_read_b128 a[224:227], v241 offset:12416\n"
        "v_cvt_pk_bf16_f32 v66, v100, v101\n"
        "v_mfma_f32_16x16x32_bf16 v[120:123], a[228:231], a[196:199], v[120:123]\n"
        "ds_read_b128 a[228:231], v240 offset:12544\n"
        "v_mfma_f32_16x16x32_bf16 v[120:123], a[232:235], a[200:203], v[120:123]\n"
        "ds_read_b128 a[232:235], v241 offset:12544\n"
        "v_mfma_f32_16x16x32_bf16 v[120:123], a[236:239], a[204:207], v[120:123]\n"
        "ds_read_b128 v[202:205], v251 offset:4096\n"
        "v_mfma_f32_16x16x32_bf16 v[120:123], a[240:243], a[208:211], v[120:123]\n"
        "ds_read_b128 v[182:185], v252 offset:512\n"
        "v_mfma_f32_16x16x32_bf16 v[120:123], a[244:247], a[212:215], v[120:123]\n"
        "s_waitcnt lgkmcnt(4)\n"
        "v_exp_f32 v103, v103\n"
        "v_mfma_f32_16x16x32_bf16 v[112:115], a[0:3], a[248:251], 0\n"
        "v_cvt_pk_bf16_f32 v67, v102, v103\n"
        "s_nop 0\n"
        "v_mfma_f32_16x16x32_bf16 v[112:115], a[4:7], a[216:219], v[112:115]\n"
        "v_exp_f32 v104, v104\n"
        "v_mfma_f32_16x16x32_bf16 v[112:115], a[8:11], a[220:223], v[112:115]\n"
        "v_exp_f32 v105, v105\n"
        "v_mfma_f32_16x16x32_bf16 v[112:115], a[12:15], a[224:227], v[112:115]\n"
        "s_waitcnt lgkmcnt(0)\n"
        "v_exp_f32 v106, v106\n"
        "v_mfma_f32_16x16x32_bf16 v[112:115], a[16:19], a[228:231], v[112:115]\n"
        "v_cvt_pk_bf16_f32 v72, v104, v105\n"
        "v_cvt_pk_bf16_f32 v146, v120, v121\n"
        "v_mfma_f32_16x16x32_bf16 v[112:115], a[20:23], a[232:235], v[112:115]\n"
        "s_add_u32 m0, s41, 2048\n"
        "v_exp_f32 v108, v108\n"
        "v_mfma_f32_16x16x32_bf16 v[116:119], a[44:47], a[232:235], 0\n"
        "v_cvt_pk_bf16_f32 v147, v122, v123\n"
        "v_add_u32 v245, s45, v245\n"
        "v_mfma_f32_16x16x32_bf16 v[116:119], a[40:43], a[228:231], v[116:119]\n"
        "buffer_load_dwordx4 v244, s[8:11], s47 offen lds\n"
        "v_mfma_f32_16x16x32_bf16 v[116:119], a[36:39], a[224:227], v[116:119]\n"
        "v_exp_f32 v107, v107\n"
        "v_mfma_f32_16x16x32_bf16 v[116:119], a[32:35], a[220:223], v[116:119]\n"
        "v_exp_f32 v109, v109\n"
        "v_mfma_f32_16x16x32_bf16 v[116:119], a[28:31], a[216:219], v[116:119]\n"
        "v_cvt_pk_bf16_f32 v73, v106, v107\n"
        "v_cvt_pk_bf16_f32 v74, v108, v109\n"
        "v_mfma_f32_16x16x32_bf16 v[116:119], a[24:27], a[248:251], v[116:119]\n"
        "v_exp_f32 v110, v110\n"
        "v_mfma_f32_16x16x32_bf16 v[120:123], a[48:51], a[248:251], 0\n"
        "v_exp_f32 v111, v111\n"
        "v_mfma_f32_16x16x32_bf16 v[120:123], a[52:55], a[216:219], v[120:123]\n"
        "v_cvt_pk_bf16_f32 v75, v110, v111\n"
        "v_cvt_pk_bf16_f32 v196, v112, v113\n"
        "v_mfma_f32_16x16x32_bf16 v[120:123], a[56:59], a[220:223], v[120:123]\n"
        "v_cvt_pk_bf16_f32 v197, v114, v115\n"
        "v_cvt_pk_bf16_f32 v198, v116, v117\n"
        "v_mfma_f32_16x16x32_bf16 v[120:123], a[60:63], a[224:227], v[120:123]\n"
        "v_cvt_pk_bf16_f32 v199, v118, v119\n"
        "v_mfma_f32_16x16x32_bf16 v[120:123], a[64:67], a[228:231], v[120:123]\n"
        "v_mfma_f32_16x16x32_bf16 v[120:123], a[68:71], a[232:235], v[120:123]\n"
        "s_nop 2\n"
        "v_mfma_f32_16x16x32_bf16 v[112:115], a[92:95], a[232:235], 0\n"
        "s_nop 2\n"
        "v_mfma_f32_16x16x32_bf16 v[112:115], a[88:91], a[228:231], v[112:115]\n"
        "s_add_u32 m0, s41, 12288\n"
        "v_cvt_pk_bf16_f32 v200, v120, v121\n"
        "v_cvt_pk_bf16_f32 v201, v122, v123\n"
        "v_mfma_f32_16x16x32_bf16 v[112:115], a[84:87], a[224:227], v[112:115]\n"
        "v_cvt_pk_bf16_f32 v206, v120, v121\n"
        "v_cvt_pk_bf16_f32 v207, v122, v123\n"
        "v_mfma_f32_16x16x32_bf16 v[112:115], a[80:83], a[220:223], v[112:115]\n"
        "buffer_load_dwordx4 v242, s[8:11], s35 offen lds\n"
        "v_mfma_f32_16x16x32_bf16 v[112:115], a[76:79], a[216:219], v[112:115]\n"
        "v_mfma_f32_16x16x32_bf16 v[112:115], a[72:75], a[248:251], v[112:115]\n"
        "s_nop 2\n"
        "v_mfma_f32_16x16x32_bf16 v[96:99], v[152:155], v[196:199], v[32:35]\n"
        "s_nop 2\n"
        "v_mfma_f32_16x16x32_bf16 v[96:99], v[168:171], v[200:203], v[96:99]\n"
        "v_cvt_pk_bf16_f32 v192, v112, v113\n"
        "v_cvt_pk_bf16_f32 v193, v114, v115\n"
        "v_mfma_f32_16x16x32_bf16 v[100:103], v[172:175], v[196:199], v[36:39]\n"
        "v_mfma_f32_16x16x32_bf16 v[100:103], v[188:191], v[204:207], v[100:103]\n"
        "ds_read_b128 v[202:205], v252 offset:4096\n"
        "v_mfma_f32_16x16x32_bf16 v[116:119], a[96:99], a[248:251], 0\n"
        "s_cmp_eq_u32 s37, 100663296\n"
        "v_mfma_f32_16x16x32_bf16 v[116:119], a[100:103], a[216:219], v[116:119]\n"
        "s_cselect_b32 s46, 0, s37\n"
        "v_exp_f32 v96, v96\n"
        "v_mfma_f32_16x16x32_bf16 v[116:119], a[104:107], a[220:223], v[116:119]\n"
        "s_add_u32 s38, s46, s43\n"
        "v_exp_f32 v97, v97\n"
        "v_mfma_f32_16x16x32_bf16 v[116:119], a[108:111], a[224:227], v[116:119]\n"
        "s_add_u32 s39, s46, s44\n"
        "v_exp_f32 v98, v98\n"
        "v_mfma_f32_16x16x32_bf16 v[116:119], a[112:115], a[228:231], v[116:119]\n"
        "s_add_u32 s37, s37, 6291456\n"
        "v_exp_f32 v100, v100\n"
        "v_mfma_f32_16x16x32_bf16 v[116:119], a[116:119], a[232:235], v[116:119]\n"
        "s_add_u32 m0, s41, 13312\n"
        "v_exp_f32 v99, v99\n"
        "v_mfma_f32_16x16x32_bf16 v[120:123], a[140:143], a[232:235], 0\n"
        "v_exp_f32 v101, v101\n"
        "v_mfma_f32_16x16x32_bf16 v[120:123], a[136:139], a[228:231], v[120:123]\n"
        "buffer_load_dwordx4 v243, s[8:11], s35 offen lds\n"
        "v_mfma_f32_16x16x32_bf16 v[120:123], a[132:135], a[224:227], v[120:123]\n"
        "v_cvt_pk_bf16_f32 v80, v96, v97\n"
        "v_cvt_pk_bf16_f32 v194, v116, v117\n"
        "v_mfma_f32_16x16x32_bf16 v[120:123], a[128:131], a[220:223], v[120:123]\n"
        "s_mov_b64 s[58:59], s[18:19]\n"
        "v_cvt_pk_bf16_f32 v195, v118, v119\n"
        "v_cvt_pk_bf16_f32 v81, v98, v99\n"
        "v_mfma_f32_16x16x32_bf16 v[120:123], a[124:127], a[216:219], v[120:123]\n"
        "s_mov_b64 s[62:63], s[18:19]\n"
        "v_exp_f32 v102, v102\n"
        "v_mfma_f32_16x16x32_bf16 v[120:123], a[120:123], a[248:251], v[120:123]\n"
        "v_cvt_pk_bf16_f32 v82, v100, v101\n"
        "s_nop 0\n"
        "v_mfma_f32_16x16x32_bf16 v[112:115], a[248:251], a[144:147], 0\n"
        "v_exp_f32 v103, v103\n"
        "v_mfma_f32_16x16x32_bf16 v[112:115], a[216:219], a[148:151], v[112:115]\n"
        "v_cvt_pk_bf16_f32 v83, v102, v103\n"
        "s_nop 0\n"
        "v_mfma_f32_16x16x32_bf16 v[112:115], a[220:223], a[152:155], v[112:115]\n"
        "ds_read_b128 a[236:239], v240 offset:13824\n"
        "v_cvt_pk_bf16_f32 v208, v120, v121\n"
        "v_mfma_f32_16x16x32_bf16 v[112:115], a[224:227], a[156:159], v[112:115]\n"
        "ds_read_b128 a[240:243], v241 offset:13824\n"
        "v_cvt_pk_bf16_f32 v209, v122, v123\n"
        "v_mfma_f32_16x16x32_bf16 v[112:115], a[228:231], a[160:163], v[112:115]\n"
        "ds_read_b128 a[244:247], v240 offset:13952\n"
        "v_mfma_f32_16x16x32_bf16 v[112:115], a[232:235], a[164:167], v[112:115]\n"
        "s_nop 2\n"
        "v_mfma_f32_16x16x32_bf16 v[104:107], v[192:195], v[156:159], v[8:11]\n"
        "s_nop 2\n"
        "v_mfma_f32_16x16x32_bf16 v[104:107], v[208:211], v[160:163], v[104:107]\n"
        "s_add_u32 m0, s41, 14336\n"
        "v_cvt_pk_bf16_f32 v132, v112, v113\n"
        "v_cvt_pk_bf16_f32 v133, v114, v115\n"
        "v_mfma_f32_16x16x32_bf16 v[116:119], a[232:235], a[188:191], 0\n"
        "v_mfma_f32_16x16x32_bf16 v[116:119], a[228:231], a[184:187], v[116:119]\n"
        "buffer_load_dwordx4 v244, s[8:11], s35 offen lds\n"
        "v_mfma_f32_16x16x32_bf16 v[116:119], a[224:227], a[180:183], v[116:119]\n"
        "v_mfma_f32_16x16x32_bf16 v[116:119], a[220:223], a[176:179], v[116:119]\n"
        "v_exp_f32 v104, v104\n"
        "v_mfma_f32_16x16x32_bf16 v[116:119], a[216:219], a[172:175], v[116:119]\n"
        "v_exp_f32 v105, v105\n"
        "v_mfma_f32_16x16x32_bf16 v[116:119], a[248:251], a[168:171], v[116:119]\n"
        "v_exp_f32 v106, v106\n"
        "v_mfma_f32_16x16x32_bf16 v[108:111], v[192:195], v[176:179], v[24:27]\n"
        "v_exp_f32 v107, v107\n"
        "v_mfma_f32_16x16x32_bf16 v[108:111], v[208:211], v[180:183], v[108:111]\n"
        "v_cvt_pk_bf16_f32 v68, v104, v105\n"
        "v_cvt_pk_bf16_f32 v69, v106, v107\n"
        "v_mfma_f32_16x16x32_bf16 v[120:123], a[248:251], a[192:195], 0\n"
        "ds_read_b128 a[248:251], v241 offset:13952\n"
        "v_cvt_pk_bf16_f32 v140, v116, v117\n"
        "v_mfma_f32_16x16x32_bf16 v[120:123], a[216:219], a[196:199], v[120:123]\n"
        "ds_read_b128 a[216:219], v240 offset:14080\n"
        "v_cvt_pk_bf16_f32 v141, v118, v119\n"
        "v_mfma_f32_16x16x32_bf16 v[120:123], a[220:223], a[200:203], v[120:123]\n"
        "ds_read_b128 a[220:223], v241 offset:14080\n"
        "v_mfma_f32_16x16x32_bf16 v[120:123], a[224:227], a[204:207], v[120:123]\n"
        "v_exp_f32 v108, v108\n"
        "v_mfma_f32_16x16x32_bf16 v[120:123], a[228:231], a[208:211], v[120:123]\n"
        "v_exp_f32 v109, v109\n"
        "v_mfma_f32_16x16x32_bf16 v[120:123], a[232:235], a[212:215], v[120:123]\n"
        "v_exp_f32 v110, v110\n"
        "v_mfma_f32_16x16x32_bf16 v[96:99], v[192:195], v[196:199], v[40:43]\n"
        "s_waitcnt lgkmcnt(1)\n"
        "v_exp_f32 v111, v111\n"
        "v_mfma_f32_16x16x32_bf16 v[96:99], v[208:211], v[200:203], v[96:99]\n"
        ".long 0xbec200ff\n"
        "45:\n"
        "s_branch 61b\n"
        "v_cvt_pk_bf16_f32 v76, v108, v109\n"
        "v_cvt_pk_bf16_f32 v77, v110, v111\n"
        "v_mfma_f32_16x16x32_bf16 v[112:115], a[72:75], a[236:239], 0\n"
        "s_nop 0\n"
        "v_cvt_pk_bf16_f32 v148, v120, v121\n"
        "v_cvt_pk_bf16_f32 v149, v122, v123\n"
        "v_mfma_f32_16x16x32_bf16 v[112:115], a[76:79], a[240:243], v[112:115]\n"
        "v_mfma_f32_16x16x32_bf16 v[112:115], a[80:83], a[244:247], v[112:115]\n"
        "v_exp_f32 v96, v96\n"
        "v_mfma_f32_16x16x32_bf16 v[112:115], a[84:87], a[248:251], v[112:115]\n"
        "v_exp_f32 v97, v97\n"
        "v_mfma_f32_16x16x32_bf16 v[112:115], a[88:91], a[216:219], v[112:115]\n"
        "s_waitcnt lgkmcnt(0)\n"
        "v_exp_f32 v98, v98\n"
        "v_mfma_f32_16x16x32_bf16 v[112:115], a[92:95], a[220:223], v[112:115]\n"
        "v_exp_f32 v99, v99\n"
        "v_mfma_f32_16x16x32_bf16 v[116:119], a[116:119], a[220:223], 0\n"
        "v_cvt_pk_bf16_f32 v84, v96, v97\n"
        "v_cvt_pk_bf16_f32 v85, v98, v99\n"
        "v_mfma_f32_16x16x32_bf16 v[116:119], a[112:115], a[216:219], v[116:119]\n"
        "v_add_u32 v240, s31, v240\n"
        "v_add_u32 v241, s31, v241\n"
        "v_mfma_f32_16x16x32_bf16 v[116:119], a[108:111], a[248:251], v[116:119]\n"
        "v_cvt_pk_bf16_f32 v212, v112, v113\n"
        "v_cvt_pk_bf16_f32 v213, v114, v115\n"
        "v_mfma_f32_16x16x32_bf16 v[116:119], a[104:107], a[244:247], v[116:119]\n"
        "s_add_u32 s56, s16, s38\n"
        "s_addc_u32 s57, s17, 0\n"
        "v_mfma_f32_16x16x32_bf16 v[116:119], a[100:103], a[240:243], v[116:119]\n"
        "s_add_u32 s60, s16, s39\n"
        "s_addc_u32 s61, s17, 0\n"
        "v_mfma_f32_16x16x32_bf16 v[116:119], a[96:99], a[236:239], v[116:119]\n"
        "s_nop 2\n"
        "v_mfma_f32_16x16x32_bf16 v[120:123], a[120:123], a[236:239], 0\n"
        "s_nop 2\n"
        "v_mfma_f32_16x16x32_bf16 v[120:123], a[124:127], a[240:243], v[120:123]\n"
        "s_add_u32 s47, s47, 6291456\n"
        "v_cvt_pk_bf16_f32 v214, v116, v117\n"
        "v_cvt_pk_bf16_f32 v215, v118, v119\n"
        "v_mfma_f32_16x16x32_bf16 v[120:123], a[128:131], a[244:247], v[120:123]\n"
        "s_add_u32 s36, s36, 2097152\n"
        "v_mfma_f32_16x16x32_bf16 v[120:123], a[132:135], a[248:251], v[120:123]\n"
        "s_xor_b32 s48, s48, 8192\n"
        "v_mfma_f32_16x16x32_bf16 v[120:123], a[136:139], a[216:219], v[120:123]\n"
        "v_mfma_f32_16x16x32_bf16 v[120:123], a[140:143], a[220:223], v[120:123]\n"
        "s_nop 2\n"
        "v_mfma_f32_16x16x32_bf16 v[112:115], a[220:223], a[164:167], 0\n"
        "s_nop 2\n"
        "v_mfma_f32_16x16x32_bf16 v[112:115], a[216:219], a[160:163], v[112:115]\n"
        "s_barrier\n"
        "v_cvt_pk_bf16_f32 v230, v120, v121\n"
        "v_cvt_pk_bf16_f32 v231, v122, v123\n"
        "v_mfma_f32_16x16x32_bf16 v[112:115], a[248:251], a[156:159], v[112:115]\n"
        "v_mfma_f32_16x16x32_bf16 v[112:115], a[244:247], a[152:155], v[112:115]\n"
        "v_mfma_f32_16x16x32_bf16 v[112:115], a[240:243], a[148:151], v[112:115]\n"
        "v_mfma_f32_16x16x32_bf16 v[112:115], a[236:239], a[144:147], v[112:115]\n"
        "s_nop 2\n"
        "v_mfma_f32_16x16x32_bf16 v[100:103], v[212:215], v[156:159], v[12:15]\n"
        "s_nop 2\n"
        "v_mfma_f32_16x16x32_bf16 v[100:103], v[228:231], v[164:167], v[100:103]\n"
        "v_cvt_pk_bf16_f32 v134, v112, v113\n"
        "v_cvt_pk_bf16_f32 v135, v114, v115\n"
        "v_mfma_f32_16x16x32_bf16 v[116:119], a[236:239], a[168:171], 0\n"
        "s_sub_u32 s27, s27, 1\n"
        "v_mfma_f32_16x16x32_bf16 v[116:119], a[240:243], a[172:175], v[116:119]\n"
        "s_cmp_lg_u32 s27, 0\n"
        "v_mfma_f32_16x16x32_bf16 v[116:119], a[244:247], a[176:179], v[116:119]\n"
        "s_cbranch_scc1 10b\n"
        "s_branch 19f\n"
        ".p2align 6\n"
        "51:\n"
        "s_branch 61b\n"
        "s_nop 0\n"
        "s_nop 0\n"
        "s_nop 0\n"
        "s_nop 0\n"
        "s_nop 0\n"
        "12:\n"
        "10:\n"
        "v_mfma_f32_16x16x32_bf16 v[116:119], a[248:251], a[180:183], v[116:119]\n"
        "v_mfma_f32_16x16x32_bf16 v[116:119], a[216:219], a[184:187], v[116:119]\n"
        "s_nop 0\n"
        "v_exp_f32 v100, v100\n"
        "v_mfma_f32_16x16x32_bf16 v[116:119], a[220:223], a[188:191], v[116:119]\n"
        "v_exp_f32 v101, v101\n"
        "v_mfma_f32_16x16x32_bf16 v[104:107], v[212:215], v[176:179], v[28:31]\n"
        "v_exp_f32 v102, v102\n"
        "v_mfma_f32_16x16x32_bf16 v[104:107], v[228:231], v[184:187], v[104:107]\n"
        "v_exp_f32 v103, v103\n"
        "v_mfma_f32_16x16x32_bf16 v[120:123], a[220:223], a[212:215], 0\n"
        "v_cvt_pk_bf16_f32 v70, v100, v101\n"
        "v_cvt_pk_bf16_f32 v71, v102, v103\n"
        "v_mfma_f32_16x16x32_bf16 v[120:123], a[216:219], a[208:211], v[120:123]\n"
        "v_cvt_pk_bf16_f32 v142, v116, v117\n"
        "v_cvt_pk_bf16_f32 v143, v118, v119\n"
        "v_mfma_f32_16x16x32_bf16 v[120:123], a[248:251], a[204:207], v[120:123]\n"
        "v_mov_b64 v[96:97], 0\n"
        "v_mfma_f32_16x16x32_bf16 v[120:123], a[244:247], a[200:203], v[120:123]\n"
        "v_exp_f32 v104, v104\n"
        "v_mfma_f32_16x16x32_bf16 v[120:123], a[240:243], a[196:199], v[120:123]\n"
        "v_exp_f32 v105, v105\n"
        "v_mfma_f32_16x16x32_bf16 v[120:123], a[236:239], a[192:195], v[120:123]\n"
        "v_exp_f32 v106, v106\n"
        "v_mfma_f32_16x16x32_bf16 v[108:111], v[212:215], v[196:199], v[44:47]\n"
        "v_exp_f32 v107, v107\n"
        "v_mfma_f32_16x16x32_bf16 v[108:111], v[228:231], v[204:207], v[108:111]\n"
        "v_cvt_pk_bf16_f32 v78, v104, v105\n"
        "v_cvt_pk_bf16_f32 v79, v106, v107\n"
        "v_mfma_f32_16x16x32_bf16 v[112:115], a[0:3], a[236:239], 0\n"
        "s_nop 0\n"
        "v_cvt_pk_bf16_f32 v150, v120, v121\n"
        "v_cvt_pk_bf16_f32 v151, v122, v123\n"
        "v_mfma_f32_16x16x32_bf16 v[112:115], a[4:7], a[240:243], v[112:115]\n"
        "v_mfma_f32_16x16x32_bf16 v[112:115], a[8:11], a[244:247], v[112:115]\n"
        "v_exp_f32 v108, v108\n"
        "v_mfma_f32_16x16x32_bf16 v[112:115], a[12:15], a[248:251], v[112:115]\n"
        "v_exp_f32 v109, v109\n"
        "v_mfma_f32_16x16x32_bf16 v[112:115], a[16:19], a[216:219], v[112:115]\n"
        "v_exp_f32 v110, v110\n"
        "v_mfma_f32_16x16x32_bf16 v[112:115], a[20:23], a[220:223], v[112:115]\n"
        "v_exp_f32 v111, v111\n"
        "v_smfmac_f32_16x16x64_bf16 v[96:99], a[252:255], v[64:71], v237\n"
        "v_cvt_pk_bf16_f32 v86, v108, v109\n"
        "v_mfma_f32_16x16x32_bf16 v[100:103], v[132:135], v[68:71], 0\n"
        "v_mov_b64 v[124:125], 0\n"
        "v_cvt_pk_bf16_f32 v87, v110, v111\n"
        "v_mfma_f32_16x16x32_bf16 v[100:103], v[128:131], v[64:67], v[100:103]\n"
        "s_nop 0\n"
        "v_cvt_pk_bf16_f32 v216, v112, v113\n"
        "v_cvt_pk_bf16_f32 v217, v114, v115\n"
        "v_mfma_f32_16x16x32_bf16 v[104:107], v[136:139], v[64:67], 0\n"
        "ds_read_b128 v[222:225], v251 offset:4608\n"
        "v_add_f32 v97, v96, v97\n"
        "v_mfma_f32_16x16x32_bf16 v[104:107], v[140:143], v[68:71], v[104:107]\n"
        "v_rcp_f32 v96, v97\n"
        "v_mfma_f32_16x16x32_bf16 v[108:111], v[148:151], v[68:71], 0\n"
        "v_mul_f32 v100, v96, v100\n"
        "v_mul_f32 v101, v96, v101\n"
        "v_mfma_f32_16x16x32_bf16 v[108:111], v[144:147], v[64:67], v[108:111]\n"
        "v_mul_f32 v102, v96, v102\n"
        "v_mul_f32 v103, v96, v103\n"
        "v_mfma_f32_16x16x32_bf16 v[116:119], a[44:47], a[220:223], 0\n"
        "v_cvt_pk_bf16_f32 v100, v100, v101\n"
        "v_cvt_pk_bf16_f32 v101, v102, v103\n"
        "v_mfma_f32_16x16x32_bf16 v[116:119], a[40:43], a[216:219], v[116:119]\n"
        "v_mul_f32 v104, v96, v104\n"
        "v_mul_f32 v105, v96, v105\n"
        "v_mfma_f32_16x16x32_bf16 v[116:119], a[36:39], a[248:251], v[116:119]\n"
        "v_mul_f32 v106, v96, v106\n"
        "v_mul_f32 v107, v96, v107\n"
        "v_mfma_f32_16x16x32_bf16 v[116:119], a[32:35], a[244:247], v[116:119]\n"
        "v_cvt_pk_bf16_f32 v102, v104, v105\n"
        "v_cvt_pk_bf16_f32 v103, v106, v107\n"
        "v_mfma_f32_16x16x32_bf16 v[116:119], a[28:31], a[240:243], v[116:119]\n"
        "v_mul_f32 v108, v96, v108\n"
        "v_mul_f32 v109, v96, v109\n"
        "v_mfma_f32_16x16x32_bf16 v[116:119], a[24:27], a[236:239], v[116:119]\n"
        "v_mul_f32 v110, v96, v110\n"
        "v_mul_f32 v111, v96, v111\n"
        "v_smfmac_f32_16x16x64_bf16 v[124:127], a[252:255], v[72:79], v237\n"
        "v_cvt_pk_bf16_f32 v108, v108, v109\n"
        "v_mfma_f32_16x16x32_bf16 v[64:67], v[132:135], v[76:79], 0\n"
        "ds_write_b32 v245, v108 offset:4096\n"
        "v_mfma_f32_16x16x32_bf16 v[64:67], v[128:131], v[72:75], v[64:67]\n"
        "ds_read_b128 v[156:159], v247 offset:0\n"
        "v_cvt_pk_bf16_f32 v218, v116, v117\n"
        "v_mfma_f32_16x16x32_bf16 v[68:71], v[136:139], v[72:75], 0\n"
        "ds_read_b128 v[160:163], v247 offset:4096\n"
        "v_cvt_pk_bf16_f32 v219, v118, v119\n"
        "v_mfma_f32_16x16x32_bf16 v[68:71], v[140:143], v[76:79], v[68:71]\n"
        "ds_read_b128 v[164:167], v247 offset:4352\n"
        "v_cvt_pk_bf16_f32 v109, v110, v111\n"
        "v_mfma_f32_16x16x32_bf16 v[120:123], v[148:151], v[76:79], 0\n"
        "ds_write_b32 v245, v109 offset:5120\n"
        "v_mfma_f32_16x16x32_bf16 v[120:123], v[144:147], v[72:75], v[120:123]\n"
        "v_max_u32 v236, v96, v97\n"
        "v_mov_b64 v[116:117], 0\n"
        "v_mfma_f32_16x16x32_bf16 v[112:115], a[48:51], a[236:239], 0\n"
        "v_add_f32 v125, v124, v125\n"
        "v_mfma_f32_16x16x32_bf16 v[112:115], a[52:55], a[240:243], v[112:115]\n"
        "v_rcp_f32 v124, v125\n"
        "v_mfma_f32_16x16x32_bf16 v[112:115], a[56:59], a[244:247], v[112:115]\n"
        "v_mul_f32 v64, v124, v64\n"
        "v_mul_f32 v65, v124, v65\n"
        "v_mfma_f32_16x16x32_bf16 v[112:115], a[60:63], a[248:251], v[112:115]\n"
        "v_mul_f32 v66, v124, v66\n"
        "v_mul_f32 v67, v124, v67\n"
        "v_mfma_f32_16x16x32_bf16 v[112:115], a[64:67], a[216:219], v[112:115]\n"
        "v_cvt_pk_bf16_f32 v64, v64, v65\n"
        "v_cvt_pk_bf16_f32 v65, v66, v67\n"
        "v_mfma_f32_16x16x32_bf16 v[112:115], a[68:71], a[220:223], v[112:115]\n"
        "ds_read_b128 a[216:219], v248 offset:0\n"
        "v_mul_f32 v68, v124, v68\n"
        "v_smfmac_f32_16x16x64_bf16 v[116:119], a[252:255], v[80:87], v237\n"
        "ds_read_b128 a[220:223], v248 offset:3072\n"
        "v_mfma_f32_16x16x32_bf16 v[72:75], v[132:135], v[84:87], 0\n"
        "v_mul_f32 v69, v124, v69\n"
        "v_mul_f32 v70, v124, v70\n"
        "v_mfma_f32_16x16x32_bf16 v[72:75], v[128:131], v[80:83], v[72:75]\n"
        "v_cvt_pk_bf16_f32 v220, v112, v113\n"
        "v_cvt_pk_bf16_f32 v221, v114, v115\n"
        "v_mfma_f32_16x16x32_bf16 v[76:79], v[136:139], v[80:83], 0\n"
        "ds_read_b128 a[224:227], v248 offset:32\n"
        "v_cvt_pk_bf16_f32 v226, v112, v113\n"
        "v_mfma_f32_16x16x32_bf16 v[76:79], v[140:143], v[84:87], v[76:79]\n"
        "ds_read_b128 a[228:231], v248 offset:3104\n"
        "v_cvt_pk_bf16_f32 v227, v114, v115\n"
        "v_mfma_f32_16x16x32_bf16 v[96:99], v[148:151], v[84:87], 0\n"
        "v_mul_f32 v71, v124, v71\n"
        "v_cvt_pk_bf16_f32 v66, v68, v69\n"
        "v_mfma_f32_16x16x32_bf16 v[96:99], v[144:147], v[80:83], v[96:99]\n"
        "v_cvt_pk_bf16_f32 v67, v70, v71\n"
        "v_mul_f32 v120, v124, v120\n"
        "v_mfma_f32_16x16x32_bf16 v[104:107], v[152:155], v[216:219], v[48:51]\n"
        "s_waitcnt lgkmcnt(2)\n"
        "ds_read_b128 v[152:155], v247 offset:256\n"
        "v_mul_f32 v121, v124, v121\n"
        "v_mfma_f32_16x16x32_bf16 v[104:107], v[168:171], v[220:223], v[104:107]\n"
        "v_mul_f32 v122, v124, v122\n"
        "v_mul_f32 v123, v124, v123\n"
        "v_mfma_f32_16x16x32_bf16 v[108:111], v[172:175], v[216:219], v[52:55]\n"
        "v_cvt_pk_bf16_f32 v120, v120, v121\n"
        "v_cvt_pk_bf16_f32 v121, v122, v123\n"
        "v_mfma_f32_16x16x32_bf16 v[108:111], v[188:191], v[224:227], v[108:111]\n"
        "ds_read_b128 v[222:225], v252 offset:4608\n"
        "v_max3_u32 v236, v236, v124, v125\n"
        "v_mfma_f32_16x16x32_bf16 v[176:179], v[156:159], a[216:219], 0\n"
        "ds_write_b32 v245, v120 offset:4352\n"
        "v_mfma_f32_16x16x32_bf16 v[176:179], v[160:163], a[220:223], v[176:179]\n"
        "ds_write_b32 v245, v121 offset:5376\n"
        "v_mfma_f32_16x16x32_bf16 v[196:199], v[164:167], a[220:223], 0\n"
        "s_waitcnt lgkmcnt(3)\n"
        "ds_read_b128 a[232:235], v254 offset:0\n"
        "v_add_f32 v117, v116, v117\n"
        "v_mfma_f32_16x16x32_bf16 v[196:199], v[152:155], a[216:219], v[196:199]\n"
        "ds_read_b128 a[220:223], v254 offset:3072\n"
        "v_mfma_f32_16x16x32_bf16 v[200:203], v[152:155], a[224:227], 0\n"
        "ds_read_b128 a[216:219], v254 offset:32\n"
        "v_mfma_f32_16x16x32_bf16 v[200:203], v[164:167], a[228:231], v[200:203]\n"
        "v_rcp_f32 v116, v117\n"
        "v_mfma_f32_16x16x32_bf16 v[180:183], v[160:163], a[228:231], 0\n"
        "ds_read_b128 a[228:231], v254 offset:3104\n"
        "v_mul_f32 v72, v116, v72\n"
        "v_mfma_f32_16x16x32_bf16 v[180:183], v[156:159], a[224:227], v[180:183]\n"
        "ds_read_b128 a[236:239], v247 offset:8192\n"
        "v_mul_f32 v73, v116, v73\n"
        "v_mfma_f32_16x16x32_bf16 v[112:115], v[192:195], v[216:219], v[56:59]\n"
        "s_waitcnt lgkmcnt(7)\n"
        "ds_read_b128 a[224:227], v248 offset:16\n"
        "v_mul_f32 v74, v116, v74\n"
        "v_mfma_f32_16x16x32_bf16 v[112:115], v[208:211], v[220:223], v[112:115]\n"
        "ds_read_b128 a[240:243], v247 offset:12288\n"
        "v_mul_f32 v75, v116, v75\n"
        "v_mfma_f32_16x16x32_bf16 v[68:71], v[212:215], v[216:219], v[60:63]\n"
        "ds_read_b128 a[244:247], v247 offset:12544\n"
        "v_cvt_pk_bf16_f32 v72, v72, v73\n"
        "v_mfma_f32_16x16x32_bf16 v[68:71], v[228:231], v[224:227], v[68:71]\n"
        "s_waitcnt lgkmcnt(4)\n"
        "ds_read_b128 a[248:251], v247 offset:8448\n"
        "v_cvt_pk_bf16_f32 v73, v74, v75\n"
        "v_mfma_f32_16x16x32_bf16 v[184:187], v[156:159], a[232:235], 0\n"
        "v_mul_f32 v76, v116, v76\n"
        "v_mul_f32 v77, v116, v77\n"
        "v_mfma_f32_16x16x32_bf16 v[184:187], v[160:163], a[220:223], v[184:187]\n"
        "v_mul_f32 v78, v116, v78\n"
        "v_mul_f32 v79, v116, v79\n"
        "v_mfma_f32_16x16x32_bf16 v[204:207], v[164:167], a[220:223], 0\n"
        "ds_read_b128 a[220:223], v248 offset:3088\n"
        "v_cvt_pk_bf16_f32 v74, v76, v77\n"
        "v_mfma_f32_16x16x32_bf16 v[204:207], v[152:155], a[232:235], v[204:207]\n"
        "ds_read_b128 a[232:235], v254 offset:16\n"
        "v_cvt_pk_bf16_f32 v75, v78, v79\n"
        "v_mfma_f32_16x16x32_bf16 v[192:195], v[152:155], a[216:219], 0\n"
        "v_exp_f32 v104, v104\n"
        "v_mfma_f32_16x16x32_bf16 v[192:195], v[164:167], a[228:231], v[192:195]\n"
        "v_mul_f32 v96, v116, v96\n"
        "v_mul_f32 v97, v116, v97\n"
        "v_mfma_f32_16x16x32_bf16 v[172:175], v[160:163], a[228:231], 0\n"
        "ds_read_b128 a[228:231], v254 offset:3088\n"
        "v_mul_f32 v98, v116, v98\n"
        "v_mfma_f32_16x16x32_bf16 v[172:175], v[156:159], a[216:219], v[172:175]\n"
        "s_waitcnt lgkmcnt(2)\n"
        "ds_read_b128 a[216:219], v248 offset:6144\n"
        "v_mul_f32 v99, v116, v99\n"
        "v_mfma_f32_16x16x32_bf16 v[212:215], v[156:159], a[224:227], 0\n"
        "v_exp_f32 v105, v105\n"
        "v_mfma_f32_16x16x32_bf16 v[212:215], v[160:163], a[220:223], v[212:215]\n"
        "v_exp_f32 v106, v106\n"
        "v_mfma_f32_16x16x32_bf16 v[216:219], v[164:167], a[220:223], 0\n"
        "ds_read_b128 a[220:223], v248 offset:9216\n"
        "v_cvt_pk_bf16_f32 v88, v104, v105\n"
        "v_mfma_f32_16x16x32_bf16 v[216:219], v[152:155], a[224:227], v[216:219]\n"
        "s_waitcnt lgkmcnt(2)\n"
        "ds_read_b128 a[224:227], v248 offset:6176\n"
        "v_cvt_pk_bf16_f32 v96, v96, v97\n"
        "v_mfma_f32_16x16x32_bf16 v[224:227], v[152:155], a[232:235], 0\n"
        "v_exp_f32 v108, v108\n"
        "v_mfma_f32_16x16x32_bf16 v[224:227], v[164:167], a[228:231], v[224:227]\n"
        "v_cvt_pk_bf16_f32 v97, v98, v99\n"
        "v_max3_u32 v236, v236, v116, v117\n"
        "v_mfma_f32_16x16x32_bf16 v[220:223], v[160:163], a[228:231], 0\n"
        "ds_read_b128 a[228:231], v248 offset:9248\n"
        "v_mfma_f32_16x16x32_bf16 v[220:223], v[156:159], a[232:235], v[220:223]\n"
        "s_waitcnt lgkmcnt(2)\n"
        "ds_read_b128 a[232:235], v254 offset:6144\n"
        "v_mfma_f32_16x16x32_bf16 v[176:179], a[236:239], a[216:219], v[176:179]\n"
        "v_exp_f32 v107, v107\n"
        "v_mfma_f32_16x16x32_bf16 v[176:179], a[240:243], a[220:223], v[176:179]\n"
        "v_exp_f32 v109, v109\n"
        "v_mfma_f32_16x16x32_bf16 v[196:199], a[244:247], a[220:223], v[196:199]\n"
        "ds_read_b128 a[220:223], v254 offset:9216\n"
        "v_cvt_pk_bf16_f32 v89, v106, v107\n"
        "v_mfma_f32_16x16x32_bf16 v[196:199], a[248:251], a[216:219], v[196:199]\n"
        "s_waitcnt lgkmcnt(2)\n"
        "ds_read_b128 a[216:219], v254 offset:6176\n"
        "v_cvt_pk_bf16_f32 v90, v108, v109\n"
        "v_mfma_f32_16x16x32_bf16 v[200:203], a[248:251], a[224:227], v[200:203]\n"
        "ds_read_b128 v[156:159], v247 offset:16384\n"
        "v_mfma_f32_16x16x32_bf16 v[200:203], a[244:247], a[228:231], v[200:203]\n"
        "ds_read_b128 v[160:163], v247 offset:20480\n"
        "v_mfma_f32_16x16x32_bf16 v[180:183], a[240:243], a[228:231], v[180:183]\n"
        "ds_read_b128 a[228:231], v254 offset:9248\n"
        "v_mfma_f32_16x16x32_bf16 v[180:183], a[236:239], a[224:227], v[180:183]\n"
        "s_waitcnt lgkmcnt(4)\n"
        "ds_read_b128 a[224:227], v248 offset:6160\n"
        "v_mfma_f32_16x16x32_bf16 v[184:187], a[236:239], a[232:235], v[184:187]\n"
        "ds_read_b128 v[164:167], v247 offset:20736\n"
        "v_mfma_f32_16x16x32_bf16 v[184:187], a[240:243], a[220:223], v[184:187]\n"
        "ds_read_b128 v[152:155], v247 offset:16640\n"
        "v_mfma_f32_16x16x32_bf16 v[204:207], a[244:247], a[220:223], v[204:207]\n"
        "ds_read_b128 a[220:223], v248 offset:9232\n"
        "v_mfma_f32_16x16x32_bf16 v[204:207], a[248:251], a[232:235], v[204:207]\n"
        "s_waitcnt lgkmcnt(4)\n"
        "ds_read_b128 a[232:235], v254 offset:6160\n"
        "v_mfma_f32_16x16x32_bf16 v[192:195], a[248:251], a[216:219], v[192:195]\n"
        "v_exp_f32 v110, v110\n"
        "v_mfma_f32_16x16x32_bf16 v[192:195], a[244:247], a[228:231], v[192:195]\n"
        "v_exp_f32 v111, v111\n"
        "v_mfma_f32_16x16x32_bf16 v[172:175], a[240:243], a[228:231], v[172:175]\n"
        "ds_read_b128 a[228:231], v254 offset:9232\n"
        "v_cvt_pk_bf16_f32 v91, v110, v111\n"
        "v_mfma_f32_16x16x32_bf16 v[172:175], a[236:239], a[216:219], v[172:175]\n"
        "s_waitcnt lgkmcnt(2)\n"
        "ds_read_b128 a[216:219], v248 offset:12288\n"
        "v_mfma_f32_16x16x32_bf16 v[212:215], a[236:239], a[224:227], v[212:215]\n"
        "v_exp_f32 v112, v112\n"
        "v_mfma_f32_16x16x32_bf16 v[212:215], a[240:243], a[220:223], v[212:215]\n"
        "v_exp_f32 v113, v113\n"
        "v_mfma_f32_16x16x32_bf16 v[216:219], a[244:247], a[220:223], v[216:219]\n"
        "ds_read_b128 a[220:223], v248 offset:15360\n"
        "v_cvt_pk_bf16_f32 v92, v112, v113\n"
        "v_mfma_f32_16x16x32_bf16 v[216:219], a[248:251], a[224:227], v[216:219]\n"
        "s_waitcnt lgkmcnt(2)\n"
        "ds_read_b128 a[224:227], v248 offset:12320\n"
        "v_mfma_f32_16x16x32_bf16 v[224:227], a[248:251], a[232:235], v[224:227]\n"
        "v_exp_f32 v114, v114\n"
        "v_mfma_f32_16x16x32_bf16 v[224:227], a[244:247], a[228:231], v[224:227]\n"
        "v_exp_f32 v68, v68\n"
        "v_mfma_f32_16x16x32_bf16 v[220:223], a[240:243], a[228:231], v[220:223]\n"
        "ds_read_b128 a[228:231], v248 offset:15392\n"
        "v_mfma_f32_16x16x32_bf16 v[220:223], a[236:239], a[232:235], v[220:223]\n"
        "s_waitcnt lgkmcnt(2)\n"
        "ds_read_b128 a[232:235], v254 offset:12288\n"
        "v_mfma_f32_16x16x32_bf16 v[176:179], v[156:159], a[216:219], v[176:179]\n"
        "v_exp_f32 v115, v115\n"
        "v_mfma_f32_16x16x32_bf16 v[176:179], v[160:163], a[220:223], v[176:179]\n"
        "v_exp_f32 v69, v69\n"
        "v_mfma_f32_16x16x32_bf16 v[196:199], v[164:167], a[220:223], v[196:199]\n"
        "ds_read_b128 a[220:223], v254 offset:15360\n"
        "v_cvt_pk_bf16_f32 v93, v114, v115\n"
        "v_mfma_f32_16x16x32_bf16 v[196:199], v[152:155], a[216:219], v[196:199]\n"
        "s_waitcnt lgkmcnt(2)\n"
        "ds_read_b128 a[216:219], v254 offset:12320\n"
        "v_cvt_pk_bf16_f32 v94, v68, v69\n"
        "v_mfma_f32_16x16x32_bf16 v[200:203], v[152:155], a[224:227], v[200:203]\n"
        "v_exp_f32 v70, v70\n"
        "v_mfma_f32_16x16x32_bf16 v[200:203], v[164:167], a[228:231], v[200:203]\n"
        "s_waitcnt vmcnt(0)\n"
        "v_exp_f32 v71, v71\n"
        "v_mfma_f32_16x16x32_bf16 v[180:183], v[160:163], a[228:231], v[180:183]\n"
        "s_barrier\n"
        "ds_read_b128 a[228:231], v254 offset:15392\n"
        "v_cvt_pk_bf16_f32 v95, v70, v71\n"
        "v_mfma_f32_16x16x32_bf16 v[180:183], v[156:159], a[224:227], v[180:183]\n"
        "s_waitcnt lgkmcnt(2)\n"
        "ds_read_b128 a[224:227], v248 offset:12304\n"
        "v_mfma_f32_16x16x32_bf16 v[184:187], v[156:159], a[232:235], v[184:187]\n"
        "v_mfma_f32_16x16x32_bf16 v[184:187], v[160:163], a[220:223], v[184:187]\n"
        "v_mfma_f32_16x16x32_bf16 v[204:207], v[164:167], a[220:223], v[204:207]\n"
        "ds_read_b128 a[220:223], v248 offset:15376\n"
        "v_mfma_f32_16x16x32_bf16 v[204:207], v[152:155], a[232:235], v[204:207]\n"
        "s_waitcnt lgkmcnt(2)\n"
        "ds_read_b128 a[232:235], v254 offset:12304\n"
        "v_mfma_f32_16x16x32_bf16 v[192:195], v[152:155], a[216:219], v[192:195]\n"
        "ds_read_b128 a[236:239], v240 offset:0\n"
        "v_mfma_f32_16x16x32_bf16 v[192:195], v[164:167], a[228:231], v[192:195]\n"
        "ds_read_b128 a[240:243], v241 offset:0\n"
        "v_mfma_f32_16x16x32_bf16 v[172:175], v[160:163], a[228:231], v[172:175]\n"
        "ds_read_b128 a[228:231], v254 offset:15376\n"
        "v_mfma_f32_16x16x32_bf16 v[172:175], v[156:159], a[216:219], v[172:175]\n"
        "s_waitcnt lgkmcnt(4)\n"
        "ds_read_b128 a[244:247], v240 offset:128\n"
        "v_mfma_f32_16x16x32_bf16 v[212:215], v[156:159], a[224:227], v[212:215]\n"
        "ds_read_b128 a[248:251], v241 offset:128\n"
        "v_mfma_f32_16x16x32_bf16 v[212:215], v[160:163], a[220:223], v[212:215]\n"
        "ds_read_b128 a[216:219], v240 offset:256\n"
        "v_mfma_f32_16x16x32_bf16 v[216:219], v[164:167], a[220:223], v[216:219]\n"
        "ds_read_b128 a[220:223], v241 offset:256\n"
        "v_mfma_f32_16x16x32_bf16 v[216:219], v[152:155], a[224:227], v[216:219]\n"
        "s_waitcnt lgkmcnt(4)\n"
        "v_mfma_f32_16x16x32_bf16 v[224:227], v[152:155], a[232:235], v[224:227]\n"
        "v_mfma_f32_16x16x32_bf16 v[224:227], v[164:167], a[228:231], v[224:227]\n"
        "ds_write_b32 v245, v96 offset:4608\n"
        "v_mfma_f32_16x16x32_bf16 v[220:223], v[160:163], a[228:231], v[220:223]\n"
        "v_mfma_f32_16x16x32_bf16 v[220:223], v[156:159], a[232:235], v[220:223]\n"
        "ds_write_b32 v245, v97 offset:5632\n"
        "v_mfma_f32_16x16x32_bf16 v[112:115], a[72:75], a[236:239], 0\n"
        "v_mov_b64 v[124:125], 0\n"
        "v_cvt_pk_bf16_f32 v232, v176, v180\n"
        "v_mfma_f32_16x16x32_bf16 v[112:115], a[76:79], a[240:243], v[112:115]\n"
        "s_waitcnt lgkmcnt(2)\n"
        "v_cvt_pk_bf16_f32 v233, v184, v172\n"
        "v_cvt_pk_bf16_f32 v234, v177, v181\n"
        "v_mfma_f32_16x16x32_bf16 v[112:115], a[80:83], a[244:247], v[112:115]\n"
        "buffer_store_dwordx2 v[232:233], v250, s[56:59], 0 offen offset:0 sc1\n"
        "v_cvt_pk_bf16_f32 v235, v185, v173\n"
        "v_mfma_f32_16x16x32_bf16 v[112:115], a[84:87], a[248:251], v[112:115]\n"
        "v_cvt_pk_bf16_f32 v232, v178, v182\n"
        "v_cvt_pk_bf16_f32 v233, v186, v174\n"
        "v_mfma_f32_16x16x32_bf16 v[112:115], a[88:91], a[216:219], v[112:115]\n"
        "v_mfma_f32_16x16x32_bf16 v[112:115], a[92:95], a[220:223], v[112:115]\n"
        "ds_write_b32 v245, v100 offset:0\n"
        "v_smfmac_f32_16x16x64_bf16 v[124:127], a[252:255], v[88:95], v237\n"
        "s_nop 1\n"
        "v_mfma_f32_16x16x32_bf16 v[80:83], v[132:135], v[92:95], 0\n"
        "buffer_store_dwordx2 v[234:235], v250, s[56:59], 0 offen offset:384 sc1\n"
        "s_nop 0\n"
        "v_mfma_f32_16x16x32_bf16 v[80:83], v[128:131], v[88:91], v[80:83]\n"
        "s_add_u32 s41, s30, s40\n"
        "v_cvt_pk_bf16_f32 v152, v112, v113\n"
        "v_cvt_pk_bf16_f32 v153, v114, v115\n"
        "v_mfma_f32_16x16x32_bf16 v[84:87], v[136:139], v[88:91], 0\n"
        "v_cvt_pk_bf16_f32 v234, v179, v183\n"
        "v_cvt_pk_bf16_f32 v235, v187, v175\n"
        "v_mfma_f32_16x16x32_bf16 v[84:87], v[140:143], v[92:95], v[84:87]\n"
        "v_add_f32 v125, v124, v125\n"
        "v_mfma_f32_16x16x32_bf16 v[96:99], v[148:151], v[92:95], 0\n"
        "buffer_store_dwordx2 v[232:233], v250, s[56:59], 0 offen offset:768 sc1\n"
        "v_mfma_f32_16x16x32_bf16 v[96:99], v[144:147], v[88:91], v[96:99]\n"
        "s_add_u32 s35, s47, 786432\n"
        "v_rcp_f32 v124, v125\n"
        "v_mfma_f32_16x16x32_bf16 v[116:119], a[116:119], a[220:223], 0\n"
        "s_sub_u32 s46, s35, 100663296\n"
        "v_mul_f32 v80, v124, v80\n"
        "v_mul_f32 v81, v124, v81\n"
        "v_mfma_f32_16x16x32_bf16 v[116:119], a[112:115], a[216:219], v[116:119]\n"
        "s_cmp_lt_u32 s46, 786432\n"
        "v_mul_f32 v82, v124, v82\n"
        "v_mul_f32 v83, v124, v83\n"
        "v_mfma_f32_16x16x32_bf16 v[116:119], a[108:111], a[248:251], v[116:119]\n"
        "buffer_store_dwordx2 v[234:235], v250, s[56:59], 0 offen offset:1152 sc1\n"
        "v_cvt_pk_bf16_f32 v80, v80, v81\n"
        "v_mfma_f32_16x16x32_bf16 v[116:119], a[104:107], a[244:247], v[116:119]\n"
        "s_cselect_b32 s35, s46, s35\n"
        "v_cvt_pk_bf16_f32 v81, v82, v83\n"
        "v_cvt_pk_bf16_f32 v232, v196, v200\n"
        "v_mfma_f32_16x16x32_bf16 v[116:119], a[100:103], a[240:243], v[116:119]\n"
        "v_cvt_pk_bf16_f32 v233, v204, v192\n"
        "v_mul_f32 v84, v124, v84\n"
        "v_mfma_f32_16x16x32_bf16 v[116:119], a[96:99], a[236:239], v[116:119]\n"
        "v_mul_f32 v85, v124, v85\n"
        "v_mul_f32 v86, v124, v86\n"
        "v_mfma_f32_16x16x32_bf16 v[120:123], a[120:123], a[236:239], 0\n"
        "ds_write_b32 v245, v101 offset:1024\n"
        "v_mfma_f32_16x16x32_bf16 v[120:123], a[124:127], a[240:243], v[120:123]\n"
        "buffer_store_dwordx2 v[232:233], v250, s[60:63], 0 offen offset:0 sc1\n"
        "v_mul_f32 v87, v124, v87\n"
        "v_mfma_f32_16x16x32_bf16 v[120:123], a[128:131], a[244:247], v[120:123]\n"
        "v_cvt_pk_bf16_f32 v82, v84, v85\n"
        "v_cvt_pk_bf16_f32 v83, v86, v87\n"
        "v_mfma_f32_16x16x32_bf16 v[120:123], a[132:135], a[248:251], v[120:123]\n"
        "v_mul_f32 v96, v124, v96\n"
        "v_mul_f32 v97, v124, v97\n"
        "v_mfma_f32_16x16x32_bf16 v[120:123], a[136:139], a[216:219], v[120:123]\n"
        "v_mul_f32 v98, v124, v98\n"
        "v_mul_f32 v99, v124, v99\n"
        "v_mfma_f32_16x16x32_bf16 v[120:123], a[140:143], a[220:223], v[120:123]\n"
        "v_cvt_pk_bf16_f32 v234, v197, v201\n"
        "v_cvt_pk_bf16_f32 v235, v205, v193\n"
        "v_mfma_f32_16x16x32_bf16 v[112:115], a[20:23], a[220:223], 0\n"
        "buffer_store_dwordx2 v[234:235], v250, s[60:63], 0 offen offset:384 sc1\n"
        "v_cvt_pk_bf16_f32 v154, v116, v117\n"
        "v_mfma_f32_16x16x32_bf16 v[112:115], a[16:19], a[216:219], v[112:115]\n"
        "v_cvt_pk_bf16_f32 v155, v118, v119\n"
        "v_cvt_pk_bf16_f32 v96, v96, v97\n"
        "v_mfma_f32_16x16x32_bf16 v[112:115], a[12:15], a[248:251], v[112:115]\n"
        "ds_write_b32 v245, v96 offset:4864\n"
        "v_mfma_f32_16x16x32_bf16 v[112:115], a[8:11], a[244:247], v[112:115]\n"
        "v_cvt_pk_bf16_f32 v97, v98, v99\n"
        "v_max3_u32 v236, v236, v124, v125\n"
        "v_mfma_f32_16x16x32_bf16 v[112:115], a[4:7], a[240:243], v[112:115]\n"
        "v_cvt_pk_bf16_f32 v232, v198, v202\n"
        "v_cvt_pk_bf16_f32 v233, v206, v194\n"
        "v_mfma_f32_16x16x32_bf16 v[112:115], a[0:3], a[236:239], v[112:115]\n"
        "buffer_store_dwordx2 v[232:233], v250, s[60:63], 0 offen offset:768 sc1\n"
        "v_cmp_lt_u32 vcc, s24, v236\n"
        "v_mfma_f32_16x16x32_bf16 v[116:119], a[24:27], a[236:239], 0\n"
        "v_cvt_pk_bf16_f32 v168, v120, v121\n"
        "v_cvt_pk_bf16_f32 v169, v122, v123\n"
        "v_mfma_f32_16x16x32_bf16 v[116:119], a[28:31], a[240:243], v[116:119]\n"
        "s_cmp_lg_u64 vcc, 0\n"
        "s_addc_u32 s25, s25, s25\n"
        "v_cvt_pk_bf16_f32 v234, v199, v203\n"
        "v_mfma_f32_16x16x32_bf16 v[116:119], a[32:35], a[244:247], v[116:119]\n"
        "v_cvt_pk_bf16_f32 v235, v207, v195\n"
        "v_cvt_pk_bf16_f32 v156, v112, v113\n"
        "v_mfma_f32_16x16x32_bf16 v[116:119], a[36:39], a[248:251], v[116:119]\n"
        "buffer_store_dwordx2 v[234:235], v250, s[60:63], 0 offen offset:1152 sc1\n"
        "v_cvt_pk_bf16_f32 v157, v114, v115\n"
        "v_mfma_f32_16x16x32_bf16 v[116:119], a[40:43], a[216:219], v[116:119]\n"
        "v_cvt_pk_bf16_f32 v232, v212, v220\n"
        "v_cvt_pk_bf16_f32 v234, v213, v221\n"
        "v_mfma_f32_16x16x32_bf16 v[116:119], a[44:47], a[220:223], v[116:119]\n"
        "ds_write_b32 v245, v102 offset:2048\n"
        "v_mfma_f32_16x16x32_bf16 v[120:123], a[68:71], a[220:223], 0\n"
        "v_xor_b32 v251, 0x2000, v251\n"
        "v_xor_b32 v252, 0x2000, v252\n"
        "v_mfma_f32_16x16x32_bf16 v[120:123], a[64:67], a[216:219], v[120:123]\n"
        "buffer_store_dword v232, v238, s[56:59], 0 offen offset:0 sc1\n"
        "v_mfma_f32_16x16x32_bf16 v[120:123], a[60:63], a[248:251], v[120:123]\n"
        "ds_read_b128 a[224:227], v240 offset:1536\n"
        "v_cvt_pk_bf16_f32 v158, v116, v117\n"
        "v_mfma_f32_16x16x32_bf16 v[120:123], a[56:59], a[244:247], v[120:123]\n"
        "ds_read_b128 a[228:231], v241 offset:1536\n"
        "v_cvt_pk_bf16_f32 v159, v118, v119\n"
        "v_mfma_f32_16x16x32_bf16 v[120:123], a[52:55], a[240:243], v[120:123]\n"
        "ds_read_b128 a[232:235], v240 offset:1664\n"
        "v_cvt_pk_bf16_f32 v232, v214, v222\n"
        "v_mfma_f32_16x16x32_bf16 v[120:123], a[48:51], a[236:239], v[120:123]\n"
        "buffer_store_dword v234, v238, s[56:59], 0 offen offset:384 sc1\n"
        "v_mfma_f32_16x16x32_bf16 v[112:115], a[236:239], a[144:147], 0\n"
        "v_add_u32 v247, s45, v247\n"
        "v_cvt_pk_bf16_f32 v234, v215, v223\n"
        "v_mfma_f32_16x16x32_bf16 v[112:115], a[240:243], a[148:151], v[112:115]\n"
        "ds_write_b32 v245, v103 offset:3072\n"
        "v_mfma_f32_16x16x32_bf16 v[112:115], a[244:247], a[152:155], v[112:115]\n"
        "s_nop 0\n"
        "v_cvt_pk_bf16_f32 v160, v120, v121\n"
        "v_cvt_pk_bf16_f32 v161, v122, v123\n"
        "v_mfma_f32_16x16x32_bf16 v[112:115], a[248:251], a[156:159], v[112:115]\n"
        "v_cvt_pk_bf16_f32 v166, v120, v121\n"
        "v_cvt_pk_bf16_f32 v167, v122, v123\n"
        "v_mfma_f32_16x16x32_bf16 v[112:115], a[216:219], a[160:163], v[112:115]\n"
        "buffer_store_dword v232, v238, s[56:59], 0 offen offset:768 sc1\n"
        "v_mfma_f32_16x16x32_bf16 v[112:115], a[220:223], a[164:167], v[112:115]\n"
        "ds_write_b32 v245, v97 offset:5888\n"
        "v_mfma_f32_16x16x32_bf16 v[116:119], a[220:223], a[188:191], 0\n"
        "v_cvt_pk_bf16_f32 v232, v216, v224\n"
        "s_nop 0\n"
        "v_mfma_f32_16x16x32_bf16 v[116:119], a[216:219], a[184:187], v[116:119]\n"
        "s_nop 2\n"
        "v_mfma_f32_16x16x32_bf16 v[116:119], a[248:251], a[180:183], v[116:119]\n"
        "buffer_store_dword v234, v238, s[56:59], 0 offen offset:1152 sc1\n"
        "v_mfma_f32_16x16x32_bf16 v[116:119], a[244:247], a[176:179], v[116:119]\n"
        "v_cvt_pk_bf16_f32 v128, v112, v113\n"
        "v_cvt_pk_bf16_f32 v129, v114, v115\n"
        "v_mfma_f32_16x16x32_bf16 v[116:119], a[240:243], a[172:175], v[116:119]\n"
        "ds_write_b32 v245, v64 offset:256\n"
        "v_mfma_f32_16x16x32_bf16 v[116:119], a[236:239], a[168:171], v[116:119]\n"
        "v_cvt_pk_bf16_f32 v234, v217, v225\n"
        "s_nop 0\n"
        "v_mfma_f32_16x16x32_bf16 v[120:123], a[236:239], a[192:195], 0\n"
        "ds_read_b128 a[236:239], v241 offset:1664\n"
        "s_nop 0\n"
        "v_mfma_f32_16x16x32_bf16 v[120:123], a[240:243], a[196:199], v[120:123]\n"
        "buffer_store_dword v232, v238, s[60:63], 0 offen offset:0 sc1\n"
        "v_mfma_f32_16x16x32_bf16 v[120:123], a[244:247], a[200:203], v[120:123]\n"
        "ds_read_b128 a[240:243], v240 offset:1792\n"
        "ds_read_b128 a[244:247], v241 offset:1792\n"
        "v_mfma_f32_16x16x32_bf16 v[120:123], a[248:251], a[204:207], v[120:123]\n"
        "v_cvt_pk_bf16_f32 v136, v116, v117\n"
        "v_cvt_pk_bf16_f32 v137, v118, v119\n"
        "v_mfma_f32_16x16x32_bf16 v[120:123], a[216:219], a[208:211], v[120:123]\n"
        "v_cvt_pk_bf16_f32 v232, v218, v226\n"
        "v_mfma_f32_16x16x32_bf16 v[120:123], a[220:223], a[212:215], v[120:123]\n"
        "s_waitcnt lgkmcnt(4)\n"
        "buffer_store_dword v234, v238, s[60:63], 0 offen offset:384 sc1\n"
        "v_mfma_f32_16x16x32_bf16 v[112:115], a[72:75], a[224:227], 0\n"
        "s_nop 2\n"
        "v_mfma_f32_16x16x32_bf16 v[112:115], a[76:79], a[228:231], v[112:115]\n"
        ".long 0xbec200ff\n"
        "46:\n"
        "s_branch 61b\n"
        "v_cvt_pk_bf16_f32 v234, v219, v227\n"
        "s_nop 0\n"
        "v_mfma_f32_16x16x32_bf16 v[112:115], a[80:83], a[232:235], v[112:115]\n"
        "s_waitcnt lgkmcnt(0)\n"
        "v_cvt_pk_bf16_f32 v144, v120, v121\n"
        "v_cvt_pk_bf16_f32 v145, v122, v123\n"
        "v_mfma_f32_16x16x32_bf16 v[112:115], a[84:87], a[236:239], v[112:115]\n"
        "ds_write_b32 v245, v65 offset:1280\n"
        "v_mfma_f32_16x16x32_bf16 v[112:115], a[88:91], a[240:243], v[112:115]\n"
        "buffer_store_dword v232, v238, s[60:63], 0 offen offset:768 sc1\n"
        "v_mfma_f32_16x16x32_bf16 v[112:115], a[92:95], a[244:247], v[112:115]\n"
        "s_nop 2\n"
        "v_mfma_f32_16x16x32_bf16 v[116:119], a[116:119], a[244:247], 0\n"
        "s_nop 2\n"
        "v_mfma_f32_16x16x32_bf16 v[116:119], a[112:115], a[240:243], v[116:119]\n"
        "v_cvt_pk_bf16_f32 v172, v112, v113\n"
        "v_cvt_pk_bf16_f32 v173, v114, v115\n"
        "v_mfma_f32_16x16x32_bf16 v[116:119], a[108:111], a[236:239], v[116:119]\n"
        "buffer_store_dword v234, v238, s[60:63], 0 offen offset:1152 sc1\n"
        "v_mfma_f32_16x16x32_bf16 v[116:119], a[104:107], a[232:235], v[116:119]\n"
        "ds_write_b32 v245, v66 offset:2304\n"
        "v_mfma_f32_16x16x32_bf16 v[116:119], a[100:103], a[228:231], v[116:119]\n"
        "v_mfma_f32_16x16x32_bf16 v[116:119], a[96:99], a[224:227], v[116:119]\n"
        "s_mov_b32 m0, s48\n"
        "s_nop 1\n"
        "v_mfma_f32_16x16x32_bf16 v[120:123], a[120:123], a[224:227], 0\n"
        "ds_read_b128 v[162:165], v251 offset:0\n"
        "s_nop 0\n"
        "v_mfma_f32_16x16x32_bf16 v[120:123], a[124:127], a[228:231], v[120:123]\n"
        "buffer_load_dwordx4 v249, s[12:15], s36 offen offset:0 lds\n"
        "v_mfma_f32_16x16x32_bf16 v[120:123], a[128:131], a[232:235], v[120:123]\n"
        "v_cvt_pk_bf16_f32 v174, v116, v117\n"
        "v_cvt_pk_bf16_f32 v175, v118, v119\n"
        "v_mfma_f32_16x16x32_bf16 v[120:123], a[132:135], a[236:239], v[120:123]\n"
        "ds_write_b32 v245, v67 offset:3328\n"
        "v_mfma_f32_16x16x32_bf16 v[120:123], a[136:139], a[240:243], v[120:123]\n"
        "ds_write_b32 v245, v72 offset:512\n"
        "v_mfma_f32_16x16x32_bf16 v[120:123], a[140:143], a[244:247], v[120:123]\n"
        "s_nop 2\n"
        "v_mfma_f32_16x16x32_bf16 v[112:115], a[20:23], a[244:247], 0\n"
        "s_nop 2\n"
        "v_mfma_f32_16x16x32_bf16 v[112:115], a[16:19], a[240:243], v[112:115]\n"
        "v_cvt_pk_bf16_f32 v190, v120, v121\n"
        "v_cvt_pk_bf16_f32 v191, v122, v123\n"
        "v_mfma_f32_16x16x32_bf16 v[112:115], a[12:15], a[236:239], v[112:115]\n"
        "ds_write_b32 v245, v73 offset:1536\n"
        "v_mfma_f32_16x16x32_bf16 v[112:115], a[8:11], a[232:235], v[112:115]\n"
        "ds_write_b32 v245, v74 offset:2560\n"
        "v_mfma_f32_16x16x32_bf16 v[112:115], a[4:7], a[228:231], v[112:115]\n"
        "ds_read_b128 v[182:185], v251 offset:512\n"
        "v_mfma_f32_16x16x32_bf16 v[112:115], a[0:3], a[224:227], v[112:115]\n"
        "s_nop 2\n"
        "v_mfma_f32_16x16x32_bf16 v[116:119], a[24:27], a[224:227], 0\n"
        "s_nop 2\n"
        "v_mfma_f32_16x16x32_bf16 v[116:119], a[28:31], a[228:231], v[116:119]\n"
        "v_cvt_pk_bf16_f32 v176, v112, v113\n"
        "v_cvt_pk_bf16_f32 v177, v114, v115\n"
        "v_mfma_f32_16x16x32_bf16 v[116:119], a[32:35], a[232:235], v[116:119]\n"
        "ds_write_b32 v245, v75 offset:3584\n"
        "v_mfma_f32_16x16x32_bf16 v[116:119], a[36:39], a[236:239], v[116:119]\n"
        "buffer_load_dwordx4 v253, s[12:15], s36 offen offset:1024 lds\n"
        "v_mfma_f32_16x16x32_bf16 v[116:119], a[40:43], a[240:243], v[116:119]\n"
        "ds_write_b32 v245, v80 offset:768\n"
        "v_mfma_f32_16x16x32_bf16 v[116:119], a[44:47], a[244:247], v[116:119]\n"
        "s_nop 2\n"
        "v_mfma_f32_16x16x32_bf16 v[120:123], a[68:71], a[244:247], 0\n"
        "s_nop 2\n"
        "v_mfma_f32_16x16x32_bf16 v[120:123], a[64:67], a[240:243], v[120:123]\n"
        "v_cvt_pk_bf16_f32 v178, v116, v117\n"
        "v_cvt_pk_bf16_f32 v179, v118, v119\n"
        "v_mfma_f32_16x16x32_bf16 v[120:123], a[60:63], a[236:239], v[120:123]\n"
        "ds_write_b32 v245, v81 offset:1792\n"
        "v_mfma_f32_16x16x32_bf16 v[120:123], a[56:59], a[232:235], v[120:123]\n"
        "ds_write_b32 v245, v82 offset:2816\n"
        "v_mfma_f32_16x16x32_bf16 v[120:123], a[52:55], a[228:231], v[120:123]\n"
        "ds_write_b32 v245, v83 offset:3840\n"
        "v_mfma_f32_16x16x32_bf16 v[120:123], a[48:51], a[224:227], v[120:123]\n"
        "s_nop 2\n"
        "v_mfma_f32_16x16x32_bf16 v[112:115], a[224:227], a[144:147], 0\n"
        "s_nop 2\n"
        "v_mfma_f32_16x16x32_bf16 v[112:115], a[228:231], a[148:151], v[112:115]\n"
        "v_cvt_pk_bf16_f32 v180, v120, v121\n"
        "v_cvt_pk_bf16_f32 v181, v122, v123\n"
        "v_mfma_f32_16x16x32_bf16 v[112:115], a[232:235], a[152:155], v[112:115]\n"
        "v_cvt_pk_bf16_f32 v186, v120, v121\n"
        "v_cvt_pk_bf16_f32 v187, v122, v123\n"
        "v_mfma_f32_16x16x32_bf16 v[112:115], a[236:239], a[156:159], v[112:115]\n"
        "s_add_u32 m0, s41, 0\n"
        "v_mfma_f32_16x16x32_bf16 v[112:115], a[240:243], a[160:163], v[112:115]\n"
        "ds_read_b128 a[248:251], v240 offset:12288\n"
        "v_mfma_f32_16x16x32_bf16 v[112:115], a[244:247], a[164:167], v[112:115]\n"
        "buffer_load_dwordx4 v242, s[8:11], s47 offen lds\n"
        "v_mfma_f32_16x16x32_bf16 v[96:99], v[152:155], v[156:159], v[0:3]\n"
        "s_waitcnt lgkmcnt(2)\n"
        "ds_read_b128 a[216:219], v241 offset:12288\n"
        "s_nop 0\n"
        "v_mfma_f32_16x16x32_bf16 v[96:99], v[168:171], v[160:163], v[96:99]\n"
        "ds_read_b128 a[220:223], v240 offset:12416\n"
        "s_nop 0\n"
        "v_mfma_f32_16x16x32_bf16 v[100:103], v[172:175], v[156:159], v[4:7]\n"
        "s_sub_u32 s31, s30, s28\n"
        "v_cvt_pk_bf16_f32 v130, v112, v113\n"
        "v_cvt_pk_bf16_f32 v131, v114, v115\n"
        "v_mfma_f32_16x16x32_bf16 v[100:103], v[188:191], v[164:167], v[100:103]\n"
        "s_sub_u32 s45, s28, s29\n"
        "ds_read_b128 v[162:165], v252 offset:0\n"
        "v_mfma_f32_16x16x32_bf16 v[116:119], a[244:247], a[188:191], 0\n"
        "s_add_u32 s28, s28, s31\n"
        "v_exp_f32 v96, v96\n"
        "v_mfma_f32_16x16x32_bf16 v[116:119], a[240:243], a[184:187], v[116:119]\n"
        "s_mov_b32 s30, s29\n"
        "v_exp_f32 v97, v97\n"
        "v_mfma_f32_16x16x32_bf16 v[116:119], a[236:239], a[180:183], v[116:119]\n"
        "s_add_u32 s29, s29, s45\n"
        "v_exp_f32 v98, v98\n"
        "v_mfma_f32_16x16x32_bf16 v[116:119], a[232:235], a[176:179], v[116:119]\n"
        "v_exp_f32 v100, v100\n"
        "v_mfma_f32_16x16x32_bf16 v[116:119], a[228:231], a[172:175], v[116:119]\n"
        "v_exp_f32 v99, v99\n"
        "v_mfma_f32_16x16x32_bf16 v[116:119], a[224:227], a[168:171], v[116:119]\n"
        "v_exp_f32 v101, v101\n"
        "v_mfma_f32_16x16x32_bf16 v[104:107], v[152:155], v[176:179], v[16:19]\n"
        "v_exp_f32 v102, v102\n"
        "v_mfma_f32_16x16x32_bf16 v[104:107], v[168:171], v[180:183], v[104:107]\n"
        "s_add_u32 m0, s41, 1024\n"
        "v_cvt_pk_bf16_f32 v64, v96, v97\n"
        "v_cvt_pk_bf16_f32 v65, v98, v99\n"
        "v_mfma_f32_16x16x32_bf16 v[108:111], v[172:175], v[176:179], v[20:23]\n"
        "v_cvt_pk_bf16_f32 v138, v116, v117\n"
        "v_cvt_pk_bf16_f32 v139, v118, v119\n"
        "v_mfma_f32_16x16x32_bf16 v[108:111], v[188:191], v[184:187], v[108:111]\n"
        "buffer_load_dwordx4 v243, s[8:11], s47 offen lds\n"
        "v_mfma_f32_16x16x32_bf16 v[120:123], a[224:227], a[192:195], 0\n"
        "ds_read_b128 a[224:227], v241 offset:12416\n"
        "v_cvt_pk_bf16_f32 v66, v100, v101\n"
        "v_mfma_f32_16x16x32_bf16 v[120:123], a[228:231], a[196:199], v[120:123]\n"
        "ds_read_b128 a[228:231], v240 offset:12544\n"
        "v_mfma_f32_16x16x32_bf16 v[120:123], a[232:235], a[200:203], v[120:123]\n"
        "ds_read_b128 a[232:235], v241 offset:12544\n"
        "v_mfma_f32_16x16x32_bf16 v[120:123], a[236:239], a[204:207], v[120:123]\n"
        "ds_read_b128 v[202:205], v251 offset:4096\n"
        "v_mfma_f32_16x16x32_bf16 v[120:123], a[240:243], a[208:211], v[120:123]\n"
        "ds_read_b128 v[182:185], v252 offset:512\n"
        "v_mfma_f32_16x16x32_bf16 v[120:123], a[244:247], a[212:215], v[120:123]\n"
        "s_waitcnt lgkmcnt(4)\n"
        "v_exp_f32 v103, v103\n"
        "v_mfma_f32_16x16x32_bf16 v[112:115], a[0:3], a[248:251], 0\n"
        "v_cvt_pk_bf16_f32 v67, v102, v103\n"
        "s_nop 0\n"
        "v_mfma_f32_16x16x32_bf16 v[112:115], a[4:7], a[216:219], v[112:115]\n"
        "v_exp_f32 v104, v104\n"
        "v_mfma_f32_16x16x32_bf16 v[112:115], a[8:11], a[220:223], v[112:115]\n"
        "v_exp_f32 v105, v105\n"
        "v_mfma_f32_16x16x32_bf16 v[112:115], a[12:15], a[224:227], v[112:115]\n"
        "s_waitcnt lgkmcnt(0)\n"
        "v_exp_f32 v106, v106\n"
        "v_mfma_f32_16x16x32_bf16 v[112:115], a[16:19], a[228:231], v[112:115]\n"
        "v_cvt_pk_bf16_f32 v72, v104, v105\n"
        "v_cvt_pk_bf16_f32 v146, v120, v121\n"
        "v_mfma_f32_16x16x32_bf16 v[112:115], a[20:23], a[232:235], v[112:115]\n"
        "s_add_u32 m0, s41, 2048\n"
        "v_exp_f32 v108, v108\n"
        "v_mfma_f32_16x16x32_bf16 v[116:119], a[44:47], a[232:235], 0\n"
        "v_cvt_pk_bf16_f32 v147, v122, v123\n"
        "v_add_u32 v245, s45, v245\n"
        "v_mfma_f32_16x16x32_bf16 v[116:119], a[40:43], a[228:231], v[116:119]\n"
        "buffer_load_dwordx4 v244, s[8:11], s47 offen lds\n"
        "v_mfma_f32_16x16x32_bf16 v[116:119], a[36:39], a[224:227], v[116:119]\n"
        "v_exp_f32 v107, v107\n"
        "v_mfma_f32_16x16x32_bf16 v[116:119], a[32:35], a[220:223], v[116:119]\n"
        "v_exp_f32 v109, v109\n"
        "v_mfma_f32_16x16x32_bf16 v[116:119], a[28:31], a[216:219], v[116:119]\n"
        "v_cvt_pk_bf16_f32 v73, v106, v107\n"
        "v_cvt_pk_bf16_f32 v74, v108, v109\n"
        "v_mfma_f32_16x16x32_bf16 v[116:119], a[24:27], a[248:251], v[116:119]\n"
        "v_exp_f32 v110, v110\n"
        "v_mfma_f32_16x16x32_bf16 v[120:123], a[48:51], a[248:251], 0\n"
        "v_exp_f32 v111, v111\n"
        "v_mfma_f32_16x16x32_bf16 v[120:123], a[52:55], a[216:219], v[120:123]\n"
        "v_cvt_pk_bf16_f32 v75, v110, v111\n"
        "v_cvt_pk_bf16_f32 v196, v112, v113\n"
        "v_mfma_f32_16x16x32_bf16 v[120:123], a[56:59], a[220:223], v[120:123]\n"
        "v_cvt_pk_bf16_f32 v197, v114, v115\n"
        "v_cvt_pk_bf16_f32 v198, v116, v117\n"
        "v_mfma_f32_16x16x32_bf16 v[120:123], a[60:63], a[224:227], v[120:123]\n"
        "v_cvt_pk_bf16_f32 v199, v118, v119\n"
        "v_mfma_f32_16x16x32_bf16 v[120:123], a[64:67], a[228:231], v[120:123]\n"
        "v_mfma_f32_16x16x32_bf16 v[120:123], a[68:71], a[232:235], v[120:123]\n"
        "s_nop 2\n"
        "v_mfma_f32_16x16x32_bf16 v[112:115], a[92:95], a[232:235], 0\n"
        "s_nop 2\n"
        "v_mfma_f32_16x16x32_bf16 v[112:115], a[88:91], a[228:231], v[112:115]\n"
        "s_add_u32 m0, s41, 12288\n"
        "v_cvt_pk_bf16_f32 v200, v120, v121\n"
        "v_cvt_pk_bf16_f32 v201, v122, v123\n"
        "v_mfma_f32_16x16x32_bf16 v[112:115], a[84:87], a[224:227], v[112:115]\n"
        "v_cvt_pk_bf16_f32 v206, v120, v121\n"
        "v_cvt_pk_bf16_f32 v207, v122, v123\n"
        "v_mfma_f32_16x16x32_bf16 v[112:115], a[80:83], a[220:223], v[112:115]\n"
        "buffer_load_dwordx4 v242, s[8:11], s35 offen lds\n"
        "v_mfma_f32_16x16x32_bf16 v[112:115], a[76:79], a[216:219], v[112:115]\n"
        "v_mfma_f32_16x16x32_bf16 v[112:115], a[72:75], a[248:251], v[112:115]\n"
        "s_nop 2\n"
        "v_mfma_f32_16x16x32_bf16 v[96:99], v[152:155], v[196:199], v[32:35]\n"
        "s_nop 2\n"
        "v_mfma_f32_16x16x32_bf16 v[96:99], v[168:171], v[200:203], v[96:99]\n"
        "v_cvt_pk_bf16_f32 v192, v112, v113\n"
        "v_cvt_pk_bf16_f32 v193, v114, v115\n"
        "v_mfma_f32_16x16x32_bf16 v[100:103], v[172:175], v[196:199], v[36:39]\n"
        "v_mfma_f32_16x16x32_bf16 v[100:103], v[188:191], v[204:207], v[100:103]\n"
        "ds_read_b128 v[202:205], v252 offset:4096\n"
        "v_mfma_f32_16x16x32_bf16 v[116:119], a[96:99], a[248:251], 0\n"
        "s_cmp_eq_u32 s37, 100663296\n"
        "v_mfma_f32_16x16x32_bf16 v[116:119], a[100:103], a[216:219], v[116:119]\n"
        "s_cselect_b32 s46, 0, s37\n"
        "v_exp_f32 v96, v96\n"
        "v_mfma_f32_16x16x32_bf16 v[116:119], a[104:107], a[220:223], v[116:119]\n"
        "s_add_u32 s38, s46, s43\n"
        "v_exp_f32 v97, v97\n"
        "v_mfma_f32_16x16x32_bf16 v[116:119], a[108:111], a[224:227], v[116:119]\n"
        "s_add_u32 s39, s46, s44\n"
        "v_exp_f32 v98, v98\n"
        "v_mfma_f32_16x16x32_bf16 v[116:119], a[112:115], a[228:231], v[116:119]\n"
        "s_add_u32 s37, s37, 6291456\n"
        "v_exp_f32 v100, v100\n"
        "v_mfma_f32_16x16x32_bf16 v[116:119], a[116:119], a[232:235], v[116:119]\n"
        "s_add_u32 m0, s41, 13312\n"
        "v_exp_f32 v99, v99\n"
        "v_mfma_f32_16x16x32_bf16 v[120:123], a[140:143], a[232:235], 0\n"
        "v_exp_f32 v101, v101\n"
        "v_mfma_f32_16x16x32_bf16 v[120:123], a[136:139], a[228:231], v[120:123]\n"
        "buffer_load_dwordx4 v243, s[8:11], s35 offen lds\n"
        "v_mfma_f32_16x16x32_bf16 v[120:123], a[132:135], a[224:227], v[120:123]\n"
        "v_cvt_pk_bf16_f32 v80, v96, v97\n"
        "v_cvt_pk_bf16_f32 v194, v116, v117\n"
        "v_mfma_f32_16x16x32_bf16 v[120:123], a[128:131], a[220:223], v[120:123]\n"
        "s_mov_b64 s[58:59], s[18:19]\n"
        "v_cvt_pk_bf16_f32 v195, v118, v119\n"
        "v_cvt_pk_bf16_f32 v81, v98, v99\n"
        "v_mfma_f32_16x16x32_bf16 v[120:123], a[124:127], a[216:219], v[120:123]\n"
        "s_mov_b64 s[62:63], s[18:19]\n"
        "v_exp_f32 v102, v102\n"
        "v_mfma_f32_16x16x32_bf16 v[120:123], a[120:123], a[248:251], v[120:123]\n"
        "v_cvt_pk_bf16_f32 v82, v100, v101\n"
        "s_nop 0\n"
        "v_mfma_f32_16x16x32_bf16 v[112:115], a[248:251], a[144:147], 0\n"
        "v_exp_f32 v103, v103\n"
        "v_mfma_f32_16x16x32_bf16 v[112:115], a[216:219], a[148:151], v[112:115]\n"
        "v_cvt_pk_bf16_f32 v83, v102, v103\n"
        "s_nop 0\n"
        "v_mfma_f32_16x16x32_bf16 v[112:115], a[220:223], a[152:155], v[112:115]\n"
        "ds_read_b128 a[236:239], v240 offset:13824\n"
        "v_cvt_pk_bf16_f32 v208, v120, v121\n"
        "v_mfma_f32_16x16x32_bf16 v[112:115], a[224:227], a[156:159], v[112:115]\n"
        "ds_read_b128 a[240:243], v241 offset:13824\n"
        "v_cvt_pk_bf16_f32 v209, v122, v123\n"
        "v_mfma_f32_16x16x32_bf16 v[112:115], a[228:231], a[160:163], v[112:115]\n"
        "ds_read_b128 a[244:247], v240 offset:13952\n"
        "v_mfma_f32_16x16x32_bf16 v[112:115], a[232:235], a[164:167], v[112:115]\n"
        "s_nop 2\n"
        "v_mfma_f32_16x16x32_bf16 v[104:107], v[192:195], v[156:159], v[8:11]\n"
        "s_nop 2\n"
        "v_mfma_f32_16x16x32_bf16 v[104:107], v[208:211], v[160:163], v[104:107]\n"
        "s_add_u32 m0, s41, 14336\n"
        "v_cvt_pk_bf16_f32 v132, v112, v113\n"
        "v_cvt_pk_bf16_f32 v133, v114, v115\n"
        "v_mfma_f32_16x16x32_bf16 v[116:119], a[232:235], a[188:191], 0\n"
        "v_mfma_f32_16x16x32_bf16 v[116:119], a[228:231], a[184:187], v[116:119]\n"
        "buffer_load_dwordx4 v244, s[8:11], s35 offen lds\n"
        "v_mfma_f32_16x16x32_bf16 v[116:119], a[224:227], a[180:183], v[116:119]\n"
        "v_mfma_f32_16x16x32_bf16 v[116:119], a[220:223], a[176:179], v[116:119]\n"
        "v_exp_f32 v104, v104\n"
        "v_mfma_f32_16x16x32_bf16 v[116:119], a[216:219], a[172:175], v[116:119]\n"
        "v_exp_f32 v105, v105\n"
        "v_mfma_f32_16x16x32_bf16 v[116:119], a[248:251], a[168:171], v[116:119]\n"
        "v_exp_f32 v106, v106\n"
        "v_mfma_f32_16x16x32_bf16 v[108:111], v[192:195], v[176:179], v[24:27]\n"
        "v_exp_f32 v107, v107\n"
        "v_mfma_f32_16x16x32_bf16 v[108:111], v[208:211], v[180:183], v[108:111]\n"
        "v_cvt_pk_bf16_f32 v68, v104, v105\n"
        "v_cvt_pk_bf16_f32 v69, v106, v107\n"
        "v_mfma_f32_16x16x32_bf16 v[120:123], a[248:251], a[192:195], 0\n"
        "ds_read_b128 a[248:251], v241 offset:13952\n"
        "v_cvt_pk_bf16_f32 v140, v116, v117\n"
        "v_mfma_f32_16x16x32_bf16 v[120:123], a[216:219], a[196:199], v[120:123]\n"
        "ds_read_b128 a[216:219], v240 offset:14080\n"
        "v_cvt_pk_bf16_f32 v141, v118, v119\n"
        "v_mfma_f32_16x16x32_bf16 v[120:123], a[220:223], a[200:203], v[120:123]\n"
        "ds_read_b128 a[220:223], v241 offset:14080\n"
        "v_mfma_f32_16x16x32_bf16 v[120:123], a[224:227], a[204:207], v[120:123]\n"
        "v_exp_f32 v108, v108\n"
        "v_mfma_f32_16x16x32_bf16 v[120:123], a[228:231], a[208:211], v[120:123]\n"
        "v_exp_f32 v109, v109\n"
        "v_mfma_f32_16x16x32_bf16 v[120:123], a[232:235], a[212:215], v[120:123]\n"
        "v_exp_f32 v110, v110\n"
        "v_mfma_f32_16x16x32_bf16 v[96:99], v[192:195], v[196:199], v[40:43]\n"
        "s_waitcnt lgkmcnt(1)\n"
        "v_exp_f32 v111, v111\n"
        "v_mfma_f32_16x16x32_bf16 v[96:99], v[208:211], v[200:203], v[96:99]\n"
        "v_cvt_pk_bf16_f32 v76, v108, v109\n"
        "v_cvt_pk_bf16_f32 v77, v110, v111\n"
        "v_mfma_f32_16x16x32_bf16 v[112:115], a[72:75], a[236:239], 0\n"
        "s_nop 0\n"
        "v_cvt_pk_bf16_f32 v148, v120, v121\n"
        "v_cvt_pk_bf16_f32 v149, v122, v123\n"
        "v_mfma_f32_16x16x32_bf16 v[112:115], a[76:79], a[240:243], v[112:115]\n"
        "v_mfma_f32_16x16x32_bf16 v[112:115], a[80:83], a[244:247], v[112:115]\n"
        "v_exp_f32 v96, v96\n"
        "v_mfma_f32_16x16x32_bf16 v[112:115], a[84:87], a[248:251], v[112:115]\n"
        "v_exp_f32 v97, v97\n"
        "v_mfma_f32_16x16x32_bf16 v[112:115], a[88:91], a[216:219], v[112:115]\n"
        "s_waitcnt lgkmcnt(0)\n"
        "v_exp_f32 v98, v98\n"
        "v_mfma_f32_16x16x32_bf16 v[112:115], a[92:95], a[220:223], v[112:115]\n"
        "v_exp_f32 v99, v99\n"
        "v_mfma_f32_16x16x32_bf16 v[116:119], a[116:119], a[220:223], 0\n"
        "v_cvt_pk_bf16_f32 v84, v96, v97\n"
        "v_cvt_pk_bf16_f32 v85, v98, v99\n"
        "v_mfma_f32_16x16x32_bf16 v[116:119], a[112:115], a[216:219], v[116:119]\n"
        "v_add_u32 v240, s31, v240\n"
        "v_add_u32 v241, s31, v241\n"
        "v_mfma_f32_16x16x32_bf16 v[116:119], a[108:111], a[248:251], v[116:119]\n"
        "v_cvt_pk_bf16_f32 v212, v112, v113\n"
        "v_cvt_pk_bf16_f32 v213, v114, v115\n"
        "v_mfma_f32_16x16x32_bf16 v[116:119], a[104:107], a[244:247], v[116:119]\n"
        "s_add_u32 s56, s16, s38\n"
        "s_addc_u32 s57, s17, 0\n"
        "v_mfma_f32_16x16x32_bf16 v[116:119], a[100:103], a[240:243], v[116:119]\n"
        "s_add_u32 s60, s16, s39\n"
        "s_addc_u32 s61, s17, 0\n"
        "v_mfma_f32_16x16x32_bf16 v[116:119], a[96:99], a[236:239], v[116:119]\n"
        "s_nop 2\n"
        "v_mfma_f32_16x16x32_bf16 v[120:123], a[120:123], a[236:239], 0\n"
        "s_nop 2\n"
        "v_mfma_f32_16x16x32_bf16 v[120:123], a[124:127], a[240:243], v[120:123]\n"
        "s_add_u32 s47, s47, 6291456\n"
        "v_cvt_pk_bf16_f32 v214, v116, v117\n"
        "v_cvt_pk_bf16_f32 v215, v118, v119\n"
        "v_mfma_f32_16x16x32_bf16 v[120:123], a[128:131], a[244:247], v[120:123]\n"
        "s_add_u32 s36, s36, 2097152\n"
        "v_mfma_f32_16x16x32_bf16 v[120:123], a[132:135], a[248:251], v[120:123]\n"
        "s_xor_b32 s48, s48, 8192\n"
        "v_mfma_f32_16x16x32_bf16 v[120:123], a[136:139], a[216:219], v[120:123]\n"
        "s_barrier\n"
        "v_mfma_f32_16x16x32_bf16 v[120:123], a[140:143], a[220:223], v[120:123]\n"
        "s_nop 2\n"
        "v_mfma_f32_16x16x32_bf16 v[112:115], a[220:223], a[164:167], 0\n"
        "s_nop 2\n"
        "v_mfma_f32_16x16x32_bf16 v[112:115], a[216:219], a[160:163], v[112:115]\n"
        "v_cvt_pk_bf16_f32 v230, v120, v121\n"
        "v_cvt_pk_bf16_f32 v231, v122, v123\n"
        "v_mfma_f32_16x16x32_bf16 v[112:115], a[248:251], a[156:159], v[112:115]\n"
        "v_mfma_f32_16x16x32_bf16 v[112:115], a[244:247], a[152:155], v[112:115]\n"
        "v_mfma_f32_16x16x32_bf16 v[112:115], a[240:243], a[148:151], v[112:115]\n"
        "v_mfma_f32_16x16x32_bf16 v[112:115], a[236:239], a[144:147], v[112:115]\n"
        "s_nop 2\n"
        "v_mfma_f32_16x16x32_bf16 v[100:103], v[212:215], v[156:159], v[12:15]\n"
        "s_nop 2\n"
        "v_mfma_f32_16x16x32_bf16 v[100:103], v[228:231], v[164:167], v[100:103]\n"
        "v_cvt_pk_bf16_f32 v134, v112, v113\n"
        "v_cvt_pk_bf16_f32 v135, v114, v115\n"
        "v_mfma_f32_16x16x32_bf16 v[116:119], a[236:239], a[168:171], 0\n"
        "s_sub_u32 s27, s27, 1\n"
        "v_mfma_f32_16x16x32_bf16 v[116:119], a[240:243], a[172:175], v[116:119]\n"
        "s_cmp_lg_u32 s27, 0\n"
        "v_mfma_f32_16x16x32_bf16 v[116:119], a[244:247], a[176:179], v[116:119]\n"
        "s_cbranch_scc1 10b\n"
        "19:\n"
        "v_mfma_f32_16x16x32_bf16 v[116:119], a[248:251], a[180:183], v[116:119]\n"
        "ds_read_b128 a[224:227], v247 offset:0\n"
        "ds_read_b128 a[228:231], v247 offset:4096\n"
        "v_mfma_f32_16x16x32_bf16 v[116:119], a[216:219], a[184:187], v[116:119]\n"
        "ds_read_b128 a[232:235], v247 offset:8192\n"
        "ds_read_b128 v[156:159], v247 offset:12288\n"
        "v_mfma_f32_16x16x32_bf16 v[116:119], a[220:223], a[188:191], v[116:119]\n"
        "ds_read_b128 v[160:163], v247 offset:16384\n"
        "ds_read_b128 v[164:167], v247 offset:20480\n"
        "v_mfma_f32_16x16x32_bf16 v[104:107], v[212:215], v[176:179], v[28:31]\n"
        "s_and_b32 s46, s42, 1\n"
        "v_exp_f32 v100, v100\n"
        "v_mfma_f32_16x16x32_bf16 v[104:107], v[228:231], v[184:187], v[104:107]\n"
        "s_lshl_b32 s31, s46, 8\n"
        "v_exp_f32 v101, v101\n"
        "v_mfma_f32_16x16x32_bf16 v[120:123], a[220:223], a[212:215], 0\n"
        "s_sub_u32 s31, s45, s31\n"
        "v_cvt_pk_bf16_f32 v142, v116, v117\n"
        "v_cvt_pk_bf16_f32 v143, v118, v119\n"
        "v_mfma_f32_16x16x32_bf16 v[120:123], a[216:219], a[208:211], v[120:123]\n"
        "ds_read_b128 a[96:99], v248 offset:32\n"
        "v_cvt_pk_bf16_f32 v70, v100, v101\n"
        "v_mfma_f32_16x16x32_bf16 v[120:123], a[248:251], a[204:207], v[120:123]\n"
        "ds_read_b128 a[100:103], v248 offset:3104\n"
        "v_mov_b32 v241, 0\n"
        "v_mfma_f32_16x16x32_bf16 v[120:123], a[244:247], a[200:203], v[120:123]\n"
        "ds_read_b128 a[104:107], v248 offset:6176\n"
        "v_mfma_f32_16x16x32_bf16 v[120:123], a[240:243], a[196:199], v[120:123]\n"
        "ds_read_b128 a[108:111], v248 offset:9248\n"
        "v_mfma_f32_16x16x32_bf16 v[120:123], a[236:239], a[192:195], v[120:123]\n"
        "ds_read_b128 a[112:115], v248 offset:12320\n"
        "s_nop 0\n"
        "v_mfma_f32_16x16x32_bf16 v[108:111], v[212:215], v[196:199], v[44:47]\n"
        "ds_read_b128 a[116:119], v248 offset:15392\n"
        "s_nop 0\n"
        "v_mfma_f32_16x16x32_bf16 v[108:111], v[228:231], v[204:207], v[108:111]\n"
        "ds_read_b128 a[92:95], v248 offset:15360\n"
        "s_nop 0\n"
        "v_mfma_f32_16x16x32_bf16 v[116:119], a[0:3], a[236:239], 0\n"
        "ds_read_b128 a[88:91], v248 offset:12288\n"
        "v_cvt_pk_bf16_f32 v150, v120, v121\n"
        "v_mfma_f32_16x16x32_bf16 v[116:119], a[4:7], a[240:243], v[116:119]\n"
        "ds_read_b128 a[84:87], v248 offset:9216\n"
        "v_cvt_pk_bf16_f32 v151, v122, v123\n"
        "v_mfma_f32_16x16x32_bf16 v[116:119], a[8:11], a[244:247], v[116:119]\n"
        "ds_read_b128 a[80:83], v248 offset:6144\n"
        "v_mfma_f32_16x16x32_bf16 v[116:119], a[12:15], a[248:251], v[116:119]\n"
        "ds_read_b128 a[76:79], v248 offset:3072\n"
        "v_mfma_f32_16x16x32_bf16 v[116:119], a[16:19], a[216:219], v[116:119]\n"
        "ds_read_b128 a[72:75], v248 offset:0\n"
        "v_mfma_f32_16x16x32_bf16 v[116:119], a[20:23], a[220:223], v[116:119]\n"
        "s_waitcnt lgkmcnt(6)\n"
        "ds_read_b128 a[120:123], v254 offset:0\n"
        "v_mfma_f32_16x16x32_bf16 v[4:7], a[224:227], a[96:99], 0\n"
        "ds_read_b128 a[124:127], v254 offset:3072\n"
        "v_mfma_f32_16x16x32_bf16 v[4:7], a[228:231], a[100:103], v[4:7]\n"
        "ds_read_b128 a[128:131], v254 offset:6144\n"
        "v_mfma_f32_16x16x32_bf16 v[4:7], a[232:235], a[104:107], v[4:7]\n"
        "ds_read_b128 a[132:135], v254 offset:9216\n"
        "s_nop 0\n"
        "v_mfma_f32_16x16x32_bf16 v[4:7], v[156:159], a[108:111], v[4:7]\n"
        "ds_read_b128 a[136:139], v254 offset:12288\n"
        "v_cvt_pk_bf16_f32 v216, v116, v117\n"
        "v_mfma_f32_16x16x32_bf16 v[4:7], v[160:163], a[112:115], v[4:7]\n"
        "ds_read_b128 a[140:143], v254 offset:15360\n"
        "v_cvt_pk_bf16_f32 v217, v118, v119\n"
        "v_mfma_f32_16x16x32_bf16 v[4:7], v[164:167], a[116:119], v[4:7]\n"
        "s_waitcnt lgkmcnt(6)\n"
        "v_exp_f32 v102, v102\n"
        "v_mfma_f32_16x16x32_bf16 v[0:3], v[164:167], a[92:95], 0\n"
        "s_mul_i32 s45, s46, 786432\n"
        "v_mov_b64 v[96:97], 0\n"
        "v_mfma_f32_16x16x32_bf16 v[0:3], v[160:163], a[88:91], v[0:3]\n"
        "s_sub_u32 s37, s37, s45\n"
        "v_exp_f32 v103, v103\n"
        "v_mfma_f32_16x16x32_bf16 v[0:3], v[156:159], a[84:87], v[0:3]\n"
        "s_cmp_eq_u32 s46, 0\n"
        "s_cselect_b32 s43, s43, s44\n"
        "v_cvt_pk_bf16_f32 v71, v102, v103\n"
        "v_mfma_f32_16x16x32_bf16 v[0:3], a[232:235], a[80:83], v[0:3]\n"
        "s_add_u32 s28, s37, 786432\n"
        "v_exp_f32 v104, v104\n"
        "v_mfma_f32_16x16x32_bf16 v[0:3], a[228:231], a[76:79], v[0:3]\n"
        "s_cmp_eq_u32 s28, 100663296\n"
        "s_cselect_b32 s28, 0, s28\n"
        "v_mfma_f32_16x16x32_bf16 v[0:3], a[224:227], a[72:75], v[0:3]\n"
        "s_waitcnt lgkmcnt(0)\n"
        "v_exp_f32 v105, v105\n"
        "v_mfma_f32_16x16x32_bf16 v[8:11], a[224:227], a[120:123], 0\n"
        "s_lshl_b32 s29, s42, 2\n"
        "v_exp_f32 v106, v106\n"
        "v_mfma_f32_16x16x32_bf16 v[8:11], a[228:231], a[124:127], v[8:11]\n"
        "s_add_u32 s47, s37, s43\n"
        "v_exp_f32 v107, v107\n"
        "v_mfma_f32_16x16x32_bf16 v[8:11], a[232:235], a[128:131], v[8:11]\n"
        "s_add_u32 s35, s28, s43\n"
        "v_exp_f32 v108, v108\n"
        "v_mfma_f32_16x16x32_bf16 v[8:11], v[156:159], a[132:135], v[8:11]\n"
        "v_exp_f32 v109, v109\n"
        "v_mfma_f32_16x16x32_bf16 v[8:11], v[160:163], a[136:139], v[8:11]\n"
        "v_exp_f32 v110, v110\n"
        "v_mfma_f32_16x16x32_bf16 v[8:11], v[164:167], a[140:143], v[8:11]\n"
        "v_exp_f32 v111, v111\n"
        "v_smfmac_f32_16x16x64_bf16 v[96:99], a[252:255], v[64:71], v237\n"
        "ds_read_b128 a[164:167], v254 offset:15392\n"
        "v_mfma_f32_16x16x32_bf16 v[100:103], v[132:135], v[68:71], 0\n"
        "ds_read_b128 a[160:163], v254 offset:12320\n"
        "v_cvt_pk_bf16_f32 v78, v104, v105\n"
        "v_mfma_f32_16x16x32_bf16 v[100:103], v[128:131], v[64:67], v[100:103]\n"
        "ds_read_b128 a[156:159], v254 offset:9248\n"
        "v_cvt_pk_bf16_f32 v79, v106, v107\n"
        "v_mfma_f32_16x16x32_bf16 v[104:107], v[136:139], v[64:67], 0\n"
        "ds_read_b128 a[152:155], v254 offset:6176\n"
        "v_cvt_pk_bf16_f32 v86, v108, v109\n"
        "v_mfma_f32_16x16x32_bf16 v[104:107], v[140:143], v[68:71], v[104:107]\n"
        "ds_read_b128 a[148:151], v254 offset:3104\n"
        "v_cvt_pk_bf16_f32 v87, v110, v111\n"
        "v_mfma_f32_16x16x32_bf16 v[108:111], v[148:151], v[68:71], 0\n"
        "ds_read_b128 a[144:147], v254 offset:32\n"
        "v_add_f32 v97, v96, v97\n"
        "v_mfma_f32_16x16x32_bf16 v[108:111], v[144:147], v[64:67], v[108:111]\n"
        "v_rcp_f32 v96, v97\n"
        "v_mfma_f32_16x16x32_bf16 v[120:123], a[44:47], a[220:223], 0\n"
        "v_mul_f32 v100, v96, v100\n"
        "v_mul_f32 v101, v96, v101\n"
        "v_mfma_f32_16x16x32_bf16 v[120:123], a[40:43], a[216:219], v[120:123]\n"
        "ds_read_b128 v[176:179], v247 offset:256\n"
        "v_mul_f32 v102, v96, v102\n"
        "v_mfma_f32_16x16x32_bf16 v[120:123], a[36:39], a[248:251], v[120:123]\n"
        "ds_read_b128 v[180:183], v247 offset:4352\n"
        "v_mul_f32 v103, v96, v103\n"
        "v_mfma_f32_16x16x32_bf16 v[120:123], a[32:35], a[244:247], v[120:123]\n"
        "ds_read_b128 v[184:187], v247 offset:8448\n"
        "v_cvt_pk_bf16_f32 v100, v100, v101\n"
        "v_mfma_f32_16x16x32_bf16 v[120:123], a[28:31], a[240:243], v[120:123]\n"
        "ds_read_b128 v[196:199], v247 offset:12544\n"
        "v_cvt_pk_bf16_f32 v232, v0, v4\n"
        "v_mfma_f32_16x16x32_bf16 v[120:123], a[24:27], a[236:239], v[120:123]\n"
        "s_waitcnt lgkmcnt(4)\n"
        "ds_read_b128 v[200:203], v247 offset:16640\n"
        "v_cvt_pk_bf16_f32 v101, v102, v103\n"
        "v_mfma_f32_16x16x32_bf16 v[12:15], v[164:167], a[164:167], 0\n"
        "ds_read_b128 v[204:207], v247 offset:20736\n"
        "v_mov_b64 v[124:125], 0\n"
        "v_mfma_f32_16x16x32_bf16 v[12:15], v[160:163], a[160:163], v[12:15]\n"
        "ds_write_b32 v245, v100 offset:0\n"
        "v_mfma_f32_16x16x32_bf16 v[12:15], v[156:159], a[156:159], v[12:15]\n"
        "ds_write_b32 v245, v101 offset:1024\n"
        "v_mfma_f32_16x16x32_bf16 v[12:15], a[232:235], a[152:155], v[12:15]\n"
        "v_cvt_pk_bf16_f32 v218, v120, v121\n"
        "v_cvt_pk_bf16_f32 v219, v122, v123\n"
        "v_mfma_f32_16x16x32_bf16 v[12:15], a[228:231], a[148:151], v[12:15]\n"
        "v_cvt_pk_bf16_f32 v234, v1, v5\n"
        "v_mul_f32 v104, v96, v104\n"
        "v_mfma_f32_16x16x32_bf16 v[12:15], a[224:227], a[144:147], v[12:15]\n"
        "v_mul_f32 v105, v96, v105\n"
        "v_mul_f32 v106, v96, v106\n"
        "v_mfma_f32_16x16x32_bf16 v[116:119], a[48:51], a[236:239], 0\n"
        "v_mul_f32 v107, v96, v107\n"
        "v_cvt_pk_bf16_f32 v242, v2, v6\n"
        "v_mfma_f32_16x16x32_bf16 v[116:119], a[52:55], a[240:243], v[116:119]\n"
        "ds_read_b128 v[222:225], v251 offset:4608\n"
        "v_cvt_pk_bf16_f32 v102, v104, v105\n"
        "v_mfma_f32_16x16x32_bf16 v[116:119], a[56:59], a[244:247], v[116:119]\n"
        "v_cvt_pk_bf16_f32 v233, v8, v12\n"
        "v_cvt_pk_bf16_f32 v235, v9, v13\n"
        "v_mfma_f32_16x16x32_bf16 v[116:119], a[60:63], a[248:251], v[116:119]\n"
        "buffer_store_dwordx2 v[232:233], v250, s[56:59], 0 offen offset:0 sc1\n"
        "v_cvt_pk_bf16_f32 v243, v10, v14\n"
        "v_mfma_f32_16x16x32_bf16 v[116:119], a[64:67], a[216:219], v[116:119]\n"
        "v_cvt_pk_bf16_f32 v103, v106, v107\n"
        "v_mul_f32 v108, v96, v108\n"
        "v_mfma_f32_16x16x32_bf16 v[116:119], a[68:71], a[220:223], v[116:119]\n"
        "ds_write2st64_b32 v245, v102, v103 offset0:8 offset1:12\n"
        "v_smfmac_f32_16x16x64_bf16 v[124:127], a[252:255], v[72:79], v237\n"
        "ds_read_b128 a[212:215], v254 offset:15376\n"
        "v_mfma_f32_16x16x32_bf16 v[64:67], v[132:135], v[76:79], 0\n"
        "buffer_store_dwordx2 v[234:235], v250, s[56:59], 0 offen offset:384 sc1\n"
        "ds_read_b128 a[208:211], v254 offset:12304\n"
        "v_mfma_f32_16x16x32_bf16 v[64:67], v[128:131], v[72:75], v[64:67]\n"
        "ds_read_b128 a[204:207], v254 offset:9232\n"
        "v_mul_f32 v109, v96, v109\n"
        "v_mfma_f32_16x16x32_bf16 v[68:71], v[136:139], v[72:75], 0\n"
        "buffer_store_dwordx2 v[242:243], v250, s[56:59], 0 offen offset:768 sc1\n"
        "ds_read_b128 a[200:203], v254 offset:6160\n"
        "v_mfma_f32_16x16x32_bf16 v[68:71], v[140:143], v[76:79], v[68:71]\n"
        "ds_read_b128 a[196:199], v254 offset:3088\n"
        "v_mul_f32 v110, v96, v110\n"
        "v_mfma_f32_16x16x32_bf16 v[112:115], v[148:151], v[76:79], 0\n"
        "ds_read_b128 a[192:195], v254 offset:16\n"
        "v_mul_f32 v111, v96, v111\n"
        "v_mfma_f32_16x16x32_bf16 v[112:115], v[144:147], v[72:75], v[112:115]\n"
        "s_waitcnt lgkmcnt(7)\n"
        "ds_read_b128 a[168:171], v248 offset:16\n"
        "v_cvt_pk_bf16_f32 v232, v3, v7\n"
        "v_mfma_f32_16x16x32_bf16 v[24:27], v[176:179], a[72:75], 0\n"
        "ds_read_b128 a[172:175], v248 offset:3088\n"
        "v_cvt_pk_bf16_f32 v233, v11, v15\n"
        "v_mfma_f32_16x16x32_bf16 v[24:27], v[180:183], a[76:79], v[24:27]\n"
        "buffer_store_dwordx2 v[232:233], v250, s[56:59], 0 offen offset:1152 sc1\n"
        "ds_read_b128 a[176:179], v248 offset:6160\n"
        "v_mfma_f32_16x16x32_bf16 v[24:27], v[184:187], a[80:83], v[24:27]\n"
        "ds_read_b128 a[180:183], v248 offset:9232\n"
        "v_cvt_pk_bf16_f32 v108, v108, v109\n"
        "v_mfma_f32_16x16x32_bf16 v[24:27], v[196:199], a[84:87], v[24:27]\n"
        "ds_read_b128 a[184:187], v248 offset:12304\n"
        "v_cvt_pk_bf16_f32 v109, v110, v111\n"
        "v_mfma_f32_16x16x32_bf16 v[24:27], v[200:203], a[88:91], v[24:27]\n"
        "ds_write2st64_b32 v245, v108, v109 offset0:16 offset1:20\n"
        "v_mfma_f32_16x16x32_bf16 v[24:27], v[204:207], a[92:95], v[24:27]\n"
        "s_waitcnt lgkmcnt(6)\n"
        "ds_read_b128 a[188:191], v248 offset:15376\n"
        "v_add_f32 v125, v124, v125\n"
        "v_mfma_f32_16x16x32_bf16 v[20:23], v[164:167], a[212:215], 0\n"
        "v_rcp_f32 v124, v125\n"
        "v_mfma_f32_16x16x32_bf16 v[20:23], v[160:163], a[208:211], v[20:23]\n"
        ".long 0xbec200ff\n"
        "47:\n"
        "s_branch 61b\n"
        "v_add_u32 v240, s31, v247\n"
        "v_max_u32 v236, v96, v97\n"
        "v_mfma_f32_16x16x32_bf16 v[20:23], v[156:159], a[204:207], v[20:23]\n"
        "v_cvt_pk_bf16_f32 v220, v116, v117\n"
        "v_cvt_pk_bf16_f32 v221, v118, v119\n"
        "v_mfma_f32_16x16x32_bf16 v[20:23], a[232:235], a[200:203], v[20:23]\n"
        "v_cvt_pk_bf16_f32 v226, v116, v117\n"
        "v_cvt_pk_bf16_f32 v227, v118, v119\n"
        "v_mfma_f32_16x16x32_bf16 v[20:23], a[228:231], a[196:199], v[20:23]\n"
        "v_mul_f32 v64, v124, v64\n"
        "v_mul_f32 v65, v124, v65\n"
        "v_mfma_f32_16x16x32_bf16 v[20:23], a[224:227], a[192:195], v[20:23]\n"
        "s_waitcnt lgkmcnt(2)\n"
        "v_mul_f32 v66, v124, v66\n"
        "v_mul_f32 v67, v124, v67\n"
        "v_mfma_f32_16x16x32_bf16 v[16:19], a[224:227], a[168:171], 0\n"
        "v_cvt_pk_bf16_f32 v64, v64, v65\n"
        "v_cvt_pk_bf16_f32 v65, v66, v67\n"
        "v_mfma_f32_16x16x32_bf16 v[16:19], a[228:231], a[172:175], v[16:19]\n"
        "ds_write2st64_b32 v245, v64, v65 offset0:1 offset1:5\n"
        "v_mfma_f32_16x16x32_bf16 v[16:19], a[232:235], a[176:179], v[16:19]\n"
        "v_mul_f32 v68, v124, v68\n"
        "v_mul_f32 v69, v124, v69\n"
        "v_mfma_f32_16x16x32_bf16 v[16:19], v[156:159], a[180:183], v[16:19]\n"
        "v_mul_f32 v70, v124, v70\n"
        "v_mul_f32 v71, v124, v71\n"
        "v_mfma_f32_16x16x32_bf16 v[16:19], v[160:163], a[184:187], v[16:19]\n"
        "s_waitcnt lgkmcnt(1)\n"
        "v_cvt_pk_bf16_f32 v66, v68, v69\n"
        "v_cvt_pk_bf16_f32 v67, v70, v71\n"
        "v_mfma_f32_16x16x32_bf16 v[16:19], v[164:167], a[188:191], v[16:19]\n"
        "ds_write2st64_b32 v245, v66, v67 offset0:9 offset1:13\n"
        "v_mfma_f32_16x16x32_bf16 v[0:3], v[152:155], v[216:219], v[48:51]\n"
        "v_mul_f32 v112, v124, v112\n"
        "v_mul_f32 v113, v124, v113\n"
        "v_mfma_f32_16x16x32_bf16 v[0:3], v[168:171], v[220:223], v[0:3]\n"
        "v_mul_f32 v114, v124, v114\n"
        "v_mul_f32 v115, v124, v115\n"
        "v_mfma_f32_16x16x32_bf16 v[4:7], v[172:175], v[216:219], v[52:55]\n"
        "v_cvt_pk_bf16_f32 v112, v112, v113\n"
        "v_cvt_pk_bf16_f32 v113, v114, v115\n"
        "v_mfma_f32_16x16x32_bf16 v[4:7], v[188:191], v[224:227], v[4:7]\n"
        "ds_write_b32 v245, v112 offset:4352\n"
        "v_mfma_f32_16x16x32_bf16 v[36:39], v[204:207], a[164:167], 0\n"
        "ds_write_b32 v245, v113 offset:5376\n"
        "v_mfma_f32_16x16x32_bf16 v[36:39], v[200:203], a[160:163], v[36:39]\n"
        "ds_read_b128 v[222:225], v252 offset:4608\n"
        "v_max3_u32 v236, v236, v124, v125\n"
        "v_mfma_f32_16x16x32_bf16 v[36:39], v[196:199], a[156:159], v[36:39]\n"
        "v_cvt_pk_bf16_f32 v244, v16, v20\n"
        "v_cvt_pk_bf16_f32 v239, v17, v21\n"
        "v_mfma_f32_16x16x32_bf16 v[36:39], v[184:187], a[152:155], v[36:39]\n"
        "buffer_store_dword v244, v238, s[56:59], 0 offen offset:0 sc1\n"
        "v_mfma_f32_16x16x32_bf16 v[36:39], v[180:183], a[148:151], v[36:39]\n"
        "buffer_store_dword v239, v238, s[56:59], 0 offen offset:384 sc1\n"
        "v_mfma_f32_16x16x32_bf16 v[36:39], v[176:179], a[144:147], v[36:39]\n"
        "v_exp_f32 v0, v0\n"
        "v_mfma_f32_16x16x32_bf16 v[32:35], v[176:179], a[120:123], 0\n"
        "v_exp_f32 v1, v1\n"
        "v_mfma_f32_16x16x32_bf16 v[32:35], v[180:183], a[124:127], v[32:35]\n"
        "v_exp_f32 v2, v2\n"
        "v_mfma_f32_16x16x32_bf16 v[32:35], v[184:187], a[128:131], v[32:35]\n"
        "v_cvt_pk_bf16_f32 v88, v0, v1\n"
        "v_cvt_pk_bf16_f32 v255, v18, v22\n"
        "v_mfma_f32_16x16x32_bf16 v[32:35], v[196:199], a[132:135], v[32:35]\n"
        "buffer_store_dword v255, v238, s[56:59], 0 offen offset:768 sc1\n"
        "v_mfma_f32_16x16x32_bf16 v[32:35], v[200:203], a[136:139], v[32:35]\n"
        "v_exp_f32 v4, v4\n"
        "v_mfma_f32_16x16x32_bf16 v[32:35], v[204:207], a[140:143], v[32:35]\n"
        "v_exp_f32 v3, v3\n"
        "v_mfma_f32_16x16x32_bf16 v[28:31], v[204:207], a[116:119], 0\n"
        "v_exp_f32 v5, v5\n"
        "v_mfma_f32_16x16x32_bf16 v[28:31], v[200:203], a[112:115], v[28:31]\n"
        "v_cvt_pk_bf16_f32 v89, v2, v3\n"
        "v_cvt_pk_bf16_f32 v90, v4, v5\n"
        "v_mfma_f32_16x16x32_bf16 v[28:31], v[196:199], a[108:111], v[28:31]\n"
        "v_exp_f32 v6, v6\n"
        "v_mfma_f32_16x16x32_bf16 v[28:31], v[184:187], a[104:107], v[28:31]\n"
        "v_exp_f32 v7, v7\n"
        "v_mfma_f32_16x16x32_bf16 v[28:31], v[180:183], a[100:103], v[28:31]\n"
        "v_cvt_pk_bf16_f32 v91, v6, v7\n"
        "v_cvt_pk_bf16_f32 v244, v19, v23\n"
        "v_mfma_f32_16x16x32_bf16 v[28:31], v[176:179], a[96:99], v[28:31]\n"
        "buffer_store_dword v244, v238, s[56:59], 0 offen offset:1152 sc1\n"
        "v_mfma_f32_16x16x32_bf16 v[8:11], v[192:195], v[216:219], v[56:59]\n"
        "s_waitcnt lgkmcnt(0)\n"
        "v_mov_b64 v[96:97], 0\n"
        "v_cvt_pk_bf16_f32 v235, v32, v36\n"
        "v_mfma_f32_16x16x32_bf16 v[8:11], v[208:211], v[220:223], v[8:11]\n"
        "v_cvt_pk_bf16_f32 v243, v33, v37\n"
        "v_cvt_pk_bf16_f32 v233, v34, v38\n"
        "v_mfma_f32_16x16x32_bf16 v[40:43], v[176:179], a[168:171], 0\n"
        "v_cvt_pk_bf16_f32 v234, v24, v28\n"
        "v_cvt_pk_bf16_f32 v242, v25, v29\n"
        "v_mfma_f32_16x16x32_bf16 v[40:43], v[180:183], a[172:175], v[40:43]\n"
        "buffer_store_dwordx2 v[234:235], v250, s[60:63], 0 offen offset:0 sc1\n"
        "v_cvt_pk_bf16_f32 v232, v26, v30\n"
        "v_mfma_f32_16x16x32_bf16 v[40:43], v[184:187], a[176:179], v[40:43]\n"
        "v_exp_f32 v8, v8\n"
        "v_mfma_f32_16x16x32_bf16 v[40:43], v[196:199], a[180:183], v[40:43]\n"
        "s_barrier\n"
        "ds_read_b128 a[0:3], v240 offset:0\n"
        "ds_read_b128 a[4:7], v240 offset:4096\n"
        "v_mfma_f32_16x16x32_bf16 v[40:43], v[200:203], a[184:187], v[40:43]\n"
        "buffer_store_dwordx2 v[242:243], v250, s[60:63], 0 offen offset:384 sc1\n"
        "ds_read_b128 a[8:11], v240 offset:8192\n"
        "v_mfma_f32_16x16x32_bf16 v[40:43], v[204:207], a[188:191], v[40:43]\n"
        "ds_read_b128 a[12:15], v240 offset:12288\n"
        "ds_read_b128 a[16:19], v240 offset:16384\n"
        "v_smfmac_f32_16x16x64_bf16 v[96:99], a[252:255], v[80:87], v237\n"
        "ds_read_b128 a[20:23], v240 offset:20480\n"
        "v_mfma_f32_16x16x32_bf16 v[72:75], v[132:135], v[84:87], 0\n"
        "buffer_store_dwordx2 v[232:233], v250, s[60:63], 0 offen offset:768 sc1\n"
        "v_cvt_pk_bf16_f32 v234, v27, v31\n"
        "v_mfma_f32_16x16x32_bf16 v[72:75], v[128:131], v[80:83], v[72:75]\n"
        "v_exp_f32 v9, v9\n"
        "v_mfma_f32_16x16x32_bf16 v[76:79], v[136:139], v[80:83], 0\n"
        "v_exp_f32 v10, v10\n"
        "v_mfma_f32_16x16x32_bf16 v[76:79], v[140:143], v[84:87], v[76:79]\n"
        "v_cvt_pk_bf16_f32 v92, v8, v9\n"
        "v_cvt_pk_bf16_f32 v235, v35, v39\n"
        "v_mfma_f32_16x16x32_bf16 v[100:103], v[148:151], v[84:87], 0\n"
        "buffer_store_dwordx2 v[234:235], v250, s[60:63], 0 offen offset:1152 sc1\n"
        "v_add_f32 v97, v96, v97\n"
        "v_mfma_f32_16x16x32_bf16 v[100:103], v[144:147], v[80:83], v[100:103]\n"
        "v_exp_f32 v11, v11\n"
        "v_mfma_f32_16x16x32_bf16 v[12:15], v[212:215], v[216:219], v[60:63]\n"
        "v_cvt_pk_bf16_f32 v93, v10, v11\n"
        "v_mov_b32 v249, s29\n"
        "v_mfma_f32_16x16x32_bf16 v[12:15], v[228:231], v[224:227], v[12:15]\n"
        "s_waitcnt lgkmcnt(0)\n"
        "v_rcp_f32 v96, v97\n"
        "v_mfma_f32_16x16x32_bf16 v[156:159], a[20:23], a[92:95], 0\n"
        "v_mul_f32 v72, v96, v72\n"
        "v_mul_f32 v73, v96, v73\n"
        "v_mfma_f32_16x16x32_bf16 v[156:159], a[16:19], a[88:91], v[156:159]\n"
        "v_mul_f32 v74, v96, v74\n"
        "v_mul_f32 v75, v96, v75\n"
        "v_mfma_f32_16x16x32_bf16 v[156:159], a[12:15], a[84:87], v[156:159]\n"
        "v_exp_f32 v12, v12\n"
        "v_mfma_f32_16x16x32_bf16 v[156:159], a[8:11], a[80:83], v[156:159]\n"
        "v_exp_f32 v13, v13\n"
        "v_mfma_f32_16x16x32_bf16 v[156:159], a[4:7], a[76:79], v[156:159]\n"
        "v_exp_f32 v14, v14\n"
        "v_mfma_f32_16x16x32_bf16 v[156:159], a[0:3], a[72:75], v[156:159]\n"
        "v_cvt_pk_bf16_f32 v94, v12, v13\n"
        "v_cvt_pk_bf16_f32 v72, v72, v73\n"
        "v_mfma_f32_16x16x32_bf16 v[44:47], v[176:179], a[192:195], 0\n"
        "v_exp_f32 v15, v15\n"
        "v_mfma_f32_16x16x32_bf16 v[44:47], v[180:183], a[196:199], v[44:47]\n"
        "v_cvt_pk_bf16_f32 v95, v14, v15\n"
        "v_mov_b64 v[124:125], 0\n"
        "v_mfma_f32_16x16x32_bf16 v[44:47], v[184:187], a[200:203], v[44:47]\n"
        "v_cvt_pk_bf16_f32 v73, v74, v75\n"
        "v_mul_f32 v76, v96, v76\n"
        "v_mfma_f32_16x16x32_bf16 v[44:47], v[196:199], a[204:207], v[44:47]\n"
        "ds_write2st64_b32 v245, v72, v73 offset0:2 offset1:6\n"
        "v_mfma_f32_16x16x32_bf16 v[44:47], v[200:203], a[208:211], v[44:47]\n"
        "v_mul_f32 v77, v96, v77\n"
        "v_mul_f32 v78, v96, v78\n"
        "v_mfma_f32_16x16x32_bf16 v[44:47], v[204:207], a[212:215], v[44:47]\n"
        "v_mul_f32 v79, v96, v79\n"
        "v_cvt_pk_bf16_f32 v74, v76, v77\n"
        "v_mfma_f32_16x16x32_bf16 v[160:163], a[20:23], a[116:119], 0\n"
        "v_cvt_pk_bf16_f32 v75, v78, v79\n"
        "v_mul_f32 v100, v96, v100\n"
        "v_mfma_f32_16x16x32_bf16 v[160:163], a[16:19], a[112:115], v[160:163]\n"
        "v_mul_f32 v101, v96, v101\n"
        "v_mul_f32 v102, v96, v102\n"
        "v_mfma_f32_16x16x32_bf16 v[160:163], a[12:15], a[108:111], v[160:163]\n"
        "ds_write2st64_b32 v245, v74, v75 offset0:10 offset1:14\n"
        "v_mfma_f32_16x16x32_bf16 v[160:163], a[8:11], a[104:107], v[160:163]\n"
        "v_cvt_pk_bf16_f32 v239, v40, v44\n"
        "v_cvt_pk_bf16_f32 v255, v41, v45\n"
        "v_mfma_f32_16x16x32_bf16 v[160:163], a[4:7], a[100:103], v[160:163]\n"
        "buffer_store_dword v239, v238, s[60:63], 0 offen offset:0 sc1\n"
        "v_mfma_f32_16x16x32_bf16 v[160:163], a[0:3], a[96:99], v[160:163]\n"
        "v_mul_f32 v103, v96, v103\n"
        "v_cvt_pk_bf16_f32 v244, v42, v46\n"
        "v_smfmac_f32_16x16x64_bf16 v[124:127], a[252:255], v[88:95], v237\n"
        "v_cvt_pk_bf16_f32 v100, v100, v101\n"
        "v_mfma_f32_16x16x32_bf16 v[104:107], v[132:135], v[92:95], 0\n"
        "buffer_store_dword v255, v238, s[60:63], 0 offen offset:384 sc1\n"
        "v_mfma_f32_16x16x32_bf16 v[104:107], v[128:131], v[88:91], v[104:107]\n"
        "v_cvt_pk_bf16_f32 v101, v102, v103\n"
        "v_cvt_pk_bf16_f32 v239, v43, v47\n"
        "v_mfma_f32_16x16x32_bf16 v[108:111], v[136:139], v[88:91], 0\n"
        "buffer_store_dword v244, v238, s[60:63], 0 offen offset:768 sc1\n"
        "v_mfma_f32_16x16x32_bf16 v[108:111], v[140:143], v[92:95], v[108:111]\n"
        "ds_write2st64_b32 v245, v100, v101 offset0:18 offset1:22\n"
        "v_mfma_f32_16x16x32_bf16 v[112:115], v[148:151], v[92:95], 0\n"
        "v_max3_u32 v236, v236, v96, v97\n"
        "v_add_f32 v125, v124, v125\n"
        "v_mfma_f32_16x16x32_bf16 v[112:115], v[144:147], v[88:91], v[112:115]\n"
        "buffer_store_dword v239, v238, s[60:63], 0 offen offset:1152 sc1\n"
        "v_mfma_f32_16x16x32_bf16 v[176:179], a[0:3], a[144:147], 0\n"
        "v_rcp_f32 v124, v125\n"
        "v_mfma_f32_16x16x32_bf16 v[176:179], a[4:7], a[148:151], v[176:179]\n"
        "v_mul_f32 v104, v124, v104\n"
        "v_mul_f32 v105, v124, v105\n"
        "v_mfma_f32_16x16x32_bf16 v[176:179], a[8:11], a[152:155], v[176:179]\n"
        "v_mul_f32 v106, v124, v106\n"
        "v_mul_f32 v107, v124, v107\n"
        "v_mfma_f32_16x16x32_bf16 v[176:179], a[12:15], a[156:159], v[176:179]\n"
        "v_cvt_pk_bf16_f32 v104, v104, v105\n"
        "v_cvt_pk_bf16_f32 v105, v106, v107\n"
        "v_mfma_f32_16x16x32_bf16 v[176:179], a[16:19], a[160:163], v[176:179]\n"
        "ds_write2st64_b32 v245, v104, v105 offset0:3 offset1:7\n"
        "v_mfma_f32_16x16x32_bf16 v[176:179], a[20:23], a[164:167], v[176:179]\n"
        "v_mul_f32 v108, v124, v108\n"
        "v_mul_f32 v109, v124, v109\n"
        "v_mfma_f32_16x16x32_bf16 v[164:167], a[20:23], a[140:143], 0\n"
        "v_mul_f32 v110, v124, v110\n"
        "v_mul_f32 v111, v124, v111\n"
        "v_mfma_f32_16x16x32_bf16 v[164:167], a[16:19], a[136:139], v[164:167]\n"
        "v_cvt_pk_bf16_f32 v106, v108, v109\n"
        "v_cvt_pk_bf16_f32 v107, v110, v111\n"
        "v_mfma_f32_16x16x32_bf16 v[164:167], a[12:15], a[132:135], v[164:167]\n"
        "v_mul_f32 v112, v124, v112\n"
        "v_mul_f32 v113, v124, v113\n"
        "v_mfma_f32_16x16x32_bf16 v[164:167], a[8:11], a[128:131], v[164:167]\n"
        "ds_write2st64_b32 v245, v106, v107 offset0:11 offset1:15\n"
        "v_mfma_f32_16x16x32_bf16 v[164:167], a[4:7], a[124:127], v[164:167]\n"
        "v_mul_f32 v114, v124, v114\n"
        "v_mul_f32 v115, v124, v115\n"
        "v_mfma_f32_16x16x32_bf16 v[164:167], a[0:3], a[120:123], v[164:167]\n"
        "v_cvt_pk_bf16_f32 v112, v112, v113\n"
        "v_cvt_pk_bf16_f32 v113, v114, v115\n"
        "v_mfma_f32_16x16x32_bf16 v[180:183], a[0:3], a[168:171], 0\n"
        "v_max3_u32 v236, v236, v124, v125\n"
        "v_cvt_pk_bf16_f32 v242, v156, v160\n"
        "v_mfma_f32_16x16x32_bf16 v[180:183], a[4:7], a[172:175], v[180:183]\n"
        "ds_write2st64_b32 v245, v112, v113 offset0:19 offset1:23\n"
        "v_mfma_f32_16x16x32_bf16 v[180:183], a[8:11], a[176:179], v[180:183]\n"
        "v_cvt_pk_bf16_f32 v243, v164, v176\n"
        "v_cmp_lt_u32 vcc, s24, v236\n"
        "v_mfma_f32_16x16x32_bf16 v[180:183], a[12:15], a[180:183], v[180:183]\n"
        "buffer_store_dwordx2 v[242:243], v250, s[16:19], s47 offen offset:0 sc1\n"
        "v_cvt_pk_bf16_f32 v232, v157, v161\n"
        "v_mfma_f32_16x16x32_bf16 v[180:183], a[16:19], a[184:187], v[180:183]\n"
        "s_cmp_lg_u64 vcc, 0\n"
        "s_addc_u32 s25, s25, s25\n"
        "v_mov_b32 v253, s25\n"
        "v_mfma_f32_16x16x32_bf16 v[180:183], a[20:23], a[188:191], v[180:183]\n"
        "ds_write_b32 v249, v253\n"
        "v_mfma_f32_16x16x32_bf16 v[184:187], a[20:23], a[212:215], 0\n"
        "v_cvt_pk_bf16_f32 v233, v165, v177\n"
        "v_cvt_pk_bf16_f32 v234, v158, v162\n"
        "v_mfma_f32_16x16x32_bf16 v[184:187], a[16:19], a[208:211], v[184:187]\n"
        "buffer_store_dwordx2 v[232:233], v250, s[16:19], s47 offen offset:384 sc1\n"
        "v_cvt_pk_bf16_f32 v235, v166, v178\n"
        "v_mfma_f32_16x16x32_bf16 v[184:187], a[12:15], a[204:207], v[184:187]\n"
        "s_waitcnt lgkmcnt(0)\n"
        "s_barrier\n"
        "ds_read_b128 a[24:27], v240 offset:512\n"
        "ds_read_b128 a[28:31], v240 offset:4608\n"
        "v_mfma_f32_16x16x32_bf16 v[184:187], a[8:11], a[200:203], v[184:187]\n"
        "buffer_store_dwordx2 v[234:235], v250, s[16:19], s47 offen offset:768 sc1\n"
        "ds_read_b128 a[32:35], v240 offset:8704\n"
        "v_mfma_f32_16x16x32_bf16 v[184:187], a[4:7], a[196:199], v[184:187]\n"
        "ds_read_b128 a[36:39], v240 offset:12800\n"
        "ds_read_b128 a[40:43], v240 offset:16896\n"
        "v_mfma_f32_16x16x32_bf16 v[184:187], a[0:3], a[192:195], v[184:187]\n"
        "s_waitcnt lgkmcnt(4)\n"
        "ds_read_b128 a[44:47], v240 offset:20992\n"
        "v_cvt_pk_bf16_f32 v242, v159, v163\n"
        "v_mfma_f32_16x16x32_bf16 v[52:55], a[24:27], a[168:171], 0\n"
        "s_waitcnt lgkmcnt(4)\n"
        "v_cvt_pk_bf16_f32 v243, v167, v179\n"
        "s_nop 0\n"
        "v_mfma_f32_16x16x32_bf16 v[52:55], a[28:31], a[172:175], v[52:55]\n"
        "s_waitcnt lgkmcnt(3)\n"
        "buffer_store_dwordx2 v[242:243], v250, s[16:19], s47 offen offset:1152 sc1\n"
        "s_nop 0\n"
        "v_mfma_f32_16x16x32_bf16 v[52:55], a[32:35], a[176:179], v[52:55]\n"
        "s_waitcnt lgkmcnt(2)\n"
        "v_cvt_pk_bf16_f32 v255, v180, v184\n"
        "v_cvt_pk_bf16_f32 v244, v181, v185\n"
        "v_mfma_f32_16x16x32_bf16 v[52:55], a[36:39], a[180:183], v[52:55]\n"
        "s_waitcnt lgkmcnt(1)\n"
        "buffer_store_dword v255, v238, s[16:19], s47 offen offset:0 sc1\n"
        "v_mfma_f32_16x16x32_bf16 v[52:55], a[40:43], a[184:187], v[52:55]\n"
        "s_waitcnt lgkmcnt(0)\n"
        "ds_read_b128 v[60:63], v241\n"
        "v_cvt_pk_bf16_f32 v239, v182, v186\n"
        "v_mfma_f32_16x16x32_bf16 v[52:55], a[44:47], a[188:191], v[52:55]\n"
        "v_cvt_pk_bf16_f32 v255, v183, v187\n"
        "v_mfma_f32_16x16x32_bf16 v[56:59], a[44:47], a[212:215], 0\n"
        "buffer_store_dword v244, v238, s[16:19], s47 offen offset:384 sc1\n"
        "v_mfma_f32_16x16x32_bf16 v[56:59], a[40:43], a[208:211], v[56:59]\n"
        "v_mfma_f32_16x16x32_bf16 v[56:59], a[36:39], a[204:207], v[56:59]\n"
        "v_mfma_f32_16x16x32_bf16 v[56:59], a[32:35], a[200:203], v[56:59]\n"
        "buffer_store_dword v239, v238, s[16:19], s47 offen offset:768 sc1\n"
        "v_mfma_f32_16x16x32_bf16 v[56:59], a[28:31], a[196:199], v[56:59]\n"
        "v_mfma_f32_16x16x32_bf16 v[56:59], a[24:27], a[192:195], v[56:59]\n"
        "s_nop 2\n"
        "v_mfma_f32_16x16x32_bf16 v[196:199], a[24:27], a[72:75], 0\n"
        "buffer_store_dword v255, v238, s[16:19], s47 offen offset:1152 sc1\n"
        "v_mfma_f32_16x16x32_bf16 v[196:199], a[28:31], a[76:79], v[196:199]\n"
        "s_nop 1\n"
        "v_cvt_pk_bf16_f32 v88, v52, v56\n"
        "v_mfma_f32_16x16x32_bf16 v[196:199], a[32:35], a[80:83], v[196:199]\n"
        "v_cvt_pk_bf16_f32 v90, v53, v57\n"
        "v_cvt_pk_bf16_f32 v92, v54, v58\n"
        "v_mfma_f32_16x16x32_bf16 v[196:199], a[36:39], a[84:87], v[196:199]\n"
        "buffer_store_dword v88, v238, s[16:19], s35 offen offset:0 sc1\n"
        "v_mfma_f32_16x16x32_bf16 v[196:199], a[40:43], a[88:91], v[196:199]\n"
        "v_cvt_pk_bf16_f32 v94, v55, v59\n"
        "v_mfma_f32_16x16x32_bf16 v[196:199], a[44:47], a[92:95], v[196:199]\n"
        "v_mfma_f32_16x16x32_bf16 v[48:51], a[44:47], a[164:167], 0\n"
        "buffer_store_dword v90, v238, s[16:19], s35 offen offset:384 sc1\n"
        "v_mfma_f32_16x16x32_bf16 v[48:51], a[40:43], a[160:163], v[48:51]\n"
        "s_waitcnt lgkmcnt(0)\n"
        "v_or3_b32 v60, v60, v61, v62\n"
        "v_or_b32 v60, v60, v63\n"
        "v_mfma_f32_16x16x32_bf16 v[48:51], a[36:39], a[156:159], v[48:51]\n"
        "s_nop 0\n"
        "v_readfirstlane_b32 s25, v60\n"
        "v_mfma_f32_16x16x32_bf16 v[48:51], a[32:35], a[152:155], v[48:51]\n"
        "buffer_store_dword v92, v238, s[16:19], s35 offen offset:768 sc1\n"
        "v_mfma_f32_16x16x32_bf16 v[48:51], a[28:31], a[148:151], v[48:51]\n"
        "v_mfma_f32_16x16x32_bf16 v[48:51], a[24:27], a[144:147], v[48:51]\n"
        "v_mfma_f32_16x16x32_bf16 v[204:207], a[24:27], a[120:123], 0\n"
        "buffer_store_dword v94, v238, s[16:19], s35 offen offset:1152 sc1\n"
        "v_mfma_f32_16x16x32_bf16 v[204:207], a[28:31], a[124:127], v[204:207]\n"
        "v_mfma_f32_16x16x32_bf16 v[204:207], a[32:35], a[128:131], v[204:207]\n"
        "v_mfma_f32_16x16x32_bf16 v[204:207], a[36:39], a[132:135], v[204:207]\n"
        "v_mfma_f32_16x16x32_bf16 v[204:207], a[40:43], a[136:139], v[204:207]\n"
        "v_mfma_f32_16x16x32_bf16 v[204:207], a[44:47], a[140:143], v[204:207]\n"
        "v_mfma_f32_16x16x32_bf16 v[200:203], a[44:47], a[116:119], 0\n"
        "v_mfma_f32_16x16x32_bf16 v[200:203], a[40:43], a[112:115], v[200:203]\n"
        "s_nop 2\n"
        "v_mfma_f32_16x16x32_bf16 v[200:203], a[36:39], a[108:111], v[200:203]\n"
        "s_nop 1\n"
        "v_cvt_pk_bf16_f32 v81, v204, v48\n"
        "v_mfma_f32_16x16x32_bf16 v[200:203], a[32:35], a[104:107], v[200:203]\n"
        "v_cvt_pk_bf16_f32 v83, v205, v49\n"
        "v_mfma_f32_16x16x32_bf16 v[200:203], a[28:31], a[100:103], v[200:203]\n"
        "v_cvt_pk_bf16_f32 v85, v206, v50\n"
        "v_mfma_f32_16x16x32_bf16 v[200:203], a[24:27], a[96:99], v[200:203]\n"
        "s_nop 2\n"
        "v_cvt_pk_bf16_f32 v87, v207, v51\n"
        "s_nop 3\n"
        "v_cvt_pk_bf16_f32 v80, v196, v200\n"
        "buffer_store_dwordx2 v[80:81], v250, s[16:19], s35 offen offset:0 sc1\n"
        "v_cvt_pk_bf16_f32 v82, v197, v201\n"
        "buffer_store_dwordx2 v[82:83], v250, s[16:19], s35 offen offset:384 sc1\n"
        "v_cvt_pk_bf16_f32 v84, v198, v202\n"
        "buffer_store_dwordx2 v[84:85], v250, s[16:19], s35 offen offset:768 sc1\n"
        "v_cvt_pk_bf16_f32 v86, v199, v203\n"
        "buffer_store_dwordx2 v[86:87], v250, s[16:19], s35 offen offset:1152 sc1\n"
        "s_cmp_lg_u32 s25, 0\n"
        "s_cbranch_scc1 30f\n"
        "s_endpgm\n"
        "30:\n"
        : "={s25}"(flg), "={s42}"(w), "+{s[0:1]}"(ka), "+{s2}"(bid), "+{v0}"(tid)
        : "s"(ldsb)
        : "v1", "v2", "v3", "v4", "v5", "v6", "v7", "v8", "v9", "v10", "v11", "v12", "v13", "v14", "v15", "v16", "v17", "v18", "v19", "v20", "v21", "v22", "v23", "v24", "v25", "v26", "v27", "v28", "v29", "v30", "v31", "v32", "v33", "v34", "v35", "v36", "v37", "v38", "v39", "v40", "v41", "v42", "v43", "v44", "v45", "v46", "v47", "v48", "v49", "v50", "v51", "v52", "v53", "v54", "v55", "v56", "v57", "v58", "v59", "v60", "v61", "v62", "v63", "v64", "v65", "v66", "v67", "v68", "v69", "v70", "v71", "v72", "v73", "v74", "v75", "v76", "v77", "v78", "v79", "v80", "v81", "v82", "v83", "v84", "v85", "v86", "v87", "v88", "v89", "v90", "v91", "v92", "v93", "v94", "v95", "v96", "v97", "v98", "v99", "v100", "v101", "v102", "v103", "v104", "v105", "v106", "v107", "v108", "v109", "v110", "v111", "v112", "v113", "v114", "v115", "v116", "v117", "v118", "v119", "v120", "v121", "v122", "v123", "v124", "v125", "v126", "v127", "v128", "v129", "v130", "v131", "v132", "v133", "v134", "v135", "v136", "v137", "v138", "v139", "v140", "v141", "v142", "v143", "v144", "v145", "v146", "v147", "v148", "v149", "v150", "v151", "v152", "v153", "v154", "v155", "v156", "v157", "v158", "v159", "v160", "v161", "v162", "v163", "v164", "v165", "v166", "v167", "v168", "v169", "v170", "v171", "v172", "v173", "v174", "v175", "v176", "v177", "v178", "v179", "v180", "v181", "v182", "v183", "v184", "v185", "v186", "v187", "v188", "v189", "v190", "v191", "v192", "v193", "v194", "v195", "v196", "v197", "v198", "v199", "v200", "v201", "v202", "v203", "v204", "v205", "v206", "v207", "v208", "v209", "v210", "v211", "v212", "v213", "v214", "v215", "v216", "v217", "v218", "v219", "v220", "v221", "v222", "v223", "v224", "v225", "v226", "v227", "v228", "v229", "v230", "v231", "v232", "v233", "v234", "v235", "v236", "v237", "v238", "v239", "v240", "v241", "v242", "v243", "v244", "v245", "v246", "v247", "v248", "v249", "v250", "v251", "v252", "v253", "v254", "v255", "a0", "a1", "a2", "a3", "a4", "a5", "a6", "a7", "a8", "a9", "a10", "a11", "a12", "a13", "a14", "a15", "a16", "a17", "a18", "a19", "a20", "a21", "a22", "a23", "a24", "a25", "a26", "a27", "a28", "a29", "a30", "a31", "a32", "a33", "a34", "a35", "a36", "a37", "a38", "a39", "a40", "a41", "a42", "a43", "a44", "a45", "a46", "a47", "a48", "a49", "a50", "a51", "a52", "a53", "a54", "a55", "a56", "a57", "a58", "a59", "a60", "a61", "a62", "a63", "a64", "a65", "a66", "a67", "a68", "a69", "a70", "a71", "a72", "a73", "a74", "a75", "a76", "a77", "a78", "a79", "a80", "a81", "a82", "a83", "a84", "a85", "a86", "a87", "a88", "a89", "a90", "a91", "a92", "a93", "a94", "a95", "a96", "a97", "a98", "a99", "a100", "a101", "a102", "a103", "a104", "a105", "a106", "a107", "a108", "a109", "a110", "a111", "a112", "a113", "a114", "a115", "a116", "a117", "a118", "a119", "a120", "a121", "a122", "a123", "a124", "a125", "a126", "a127", "a128", "a129", "a130", "a131", "a132", "a133", "a134", "a135", "a136", "a137", "a138", "a139", "a140", "a141", "a142", "a143", "a144", "a145", "a146", "a147", "a148", "a149", "a150", "a151", "a152", "a153", "a154", "a155", "a156", "a157", "a158", "a159", "a160", "a161", "a162", "a163", "a164", "a165", "a166", "a167", "a168", "a169", "a170", "a171", "a172", "a173", "a174", "a175", "a176", "a177", "a178", "a179", "a180", "a181", "a182", "a183", "a184", "a185", "a186", "a187", "a188", "a189", "a190", "a191", "a192", "a193", "a194", "a195", "a196", "a197", "a198", "a199", "a200", "a201", "a202", "a203", "a204", "a205", "a206", "a207", "a208", "a209", "a210", "a211", "a212", "a213", "a214", "a215", "a216", "a217", "a218", "a219", "a220", "a221", "a222", "a223", "a224", "a225", "a226", "a227", "a228", "a229", "a230", "a231", "a232", "a233", "a234", "a235", "a236", "a237", "a238", "a239", "a240", "a241", "a242", "a243", "a244", "a245", "a246", "a247", "a248", "a249", "a250", "a251", "a252", "a253", "a254", "a255", "s4", "s5", "s6", "s7", "s8", "s9", "s10", "s11", "s12", "s13", "s14", "s15", "s16", "s17", "s18", "s19", "s20", "s21", "s22", "s23", "s24", "s26", "s27", "s28", "s29", "s30", "s31", "s34", "s35", "s36", "s37", "s38", "s39", "s40", "s41", "s43", "s44", "s45", "s46", "s47", "s48", "s49", "s50", "s51", "s52", "s53", "s54", "s55", "s56", "s57", "s58", "s59", "s60", "s61", "s62", "s63", "s64", "s65", "s66", "s67", "s68", "s69", "s70", "s71", "s72", "s73", "s74", "s75", "s76", "s77", "s78", "s79", "s80", "s81", "s82", "s83", "vcc", "scc", "memory");
    if (flg) {
        unsigned ln;
        asm volatile("s_waitcnt vmcnt(0)\n v_mbcnt_lo_u32_b32 %0, -1, 0\n v_mbcnt_hi_u32_b32 %0, -1, %0" : "=v"(ln) :: "memory");
        const unsigned t = w * 64u + ln, wx = bid & 63u, wy0 = bid >> 6;
        typedef const char* __attribute__((address_space(4))) const* kargp;
        const char *x = ((kargp)ka)[0], *wqkv = ((kargp)ka)[1], *wo = ((kargp)ka)[2], *rpb = ((kargp)ka)[3], *rpi = ((kargp)ka)[4],
                   *mask = ((kargp)ka)[5];
        char* out = (char*)((kargp)ka)[6];
        unsigned short* xs = (unsigned short*)smem;
        unsigned short* qs = xs + 64 * 192;
        const unsigned short* W1 = (const unsigned short*)wqkv;
        const unsigned short* W2 = (const unsigned short*)wo;
        for (unsigned i = 0; i < 16u; ++i) {
            if (!((flg >> (15u - i)) & 1u)) continue;
            const unsigned wy = wy0 + 4u * i;
            __syncthreads();
            for (unsigned e = t; e < 64u * 96u; e += 256u) {
                const unsigned tok = e / 96u, c2 = e % 96u;
                const unsigned pix = ((8u * wy + 4u + (tok >> 3)) & 511u) * 512u + ((8u * wx + 4u + (tok & 7u)) & 511u);
                ((unsigned*)xs)[e] = ((const unsigned*)x)[pix * 96u + c2];
            }
            __syncthreads();
            for (unsigned col = t; col < 576u; col += 256u)
                for (unsigned tb = 0; tb < 64u; tb += 8u) {
                    float a[8] = {0.f, 0.f, 0.f, 0.f, 0.f, 0.f, 0.f, 0.f};
                    for (unsigned c = 0; c < 192u; ++c) {
                        const float wt = bf2f(W1[c * 576u + col]);
#pragma unroll
                        for (int j = 0; j < 8; ++j) a[j] += bf2f(xs[(tb + j) * 192u + c]) * wt;
                    }
#pragma unroll
                    for (int j = 0; j < 8; ++j) qs[(tb + j) * 578u + col] = f2bf(a[j]);
                }
            __syncthreads();
            {
                float qv[48], o[48];
#pragma unroll
                for (int d = 0; d < 48; ++d) { qv[d] = bf2f(qs[ln * 578u + w * 48u + d]); o[d] = 0.f; }
                const unsigned short* mrow = (const unsigned short*)mask + (size_t)(wy * 64u + wx) * 4096u + ln * 64u;
                float mx = -__builtin_inff(), l = 0.f;
                for (int pass = 0; pass < 2; ++pass)
                    for (unsigned k = 0; k < 64u; ++k) {
                        float s = 0.f;
#pragma unroll
                        for (int d = 0; d < 48; ++d) s += qv[d] * bf2f(qs[k * 578u + 192u + w * 48u + d]);
                        s = s * 0.14433756729740643f + bf2f(((const unsigned short*)rpb)[((const unsigned*)rpi)[ln * 64u + k] * 4u + w]) + bf2f(mrow[k]);
                        if (pass == 0) mx = fmaxf(mx, s);
                        else {
                            const float p = __expf(s - mx);
                            l += p;
#pragma unroll
                            for (int d = 0; d < 48; ++d) o[d] += p * bf2f(qs[k * 578u + 384u + w * 48u + d]);
                        }
                    }
#pragma unroll
                for (int d = 0; d < 48; ++d) xs[ln * 192u + w * 48u + d] = f2bf(o[d] / l);
            }
            __syncthreads();
            if (t < 192u)
                for (unsigned tb = 0; tb < 64u; tb += 8u) {
                    float a[8] = {0.f, 0.f, 0.f, 0.f, 0.f, 0.f, 0.f, 0.f};
                    for (unsigned c = 0; c < 192u; ++c) {
                        const float wt = bf2f(W2[c * 192u + t]);
#pragma unroll
                        for (int j = 0; j < 8; ++j) a[j] += bf2f(xs[(tb + j) * 192u + c]) * wt;
                    }
                    const unsigned row = (8u * wy + 4u + (tb >> 3)) & 511u;
#pragma unroll
                    for (int j = 0; j < 8; ++j)
                        ((unsigned short*)out)[(row * 512u + ((8u * wx + 4u + j) & 511u)) * 192u + t] = f2bf(a[j]);
                }
        }
    }
}


extern "C" void kernel_launch(void* const* d_in, const int* in_sizes, int n_in, void* d_out, int out_size, void* d_ws,
                              size_t ws_size, hipStream_t stream) {
    win_attn_kernel<<<256, 256, 0, stream>>>((const char*)d_in[0], (const char*)d_in[1], (const char*)d_in[2],
                                             (const char*)d_in[3], (const char*)d_in[4], (const char*)d_in[5],
                                             (char*)d_out, (char*)d_ws);

}
```

```cpp
#include <hip/hip_runtime.h>
#include <hip/hip_bf16.h>

typedef unsigned int u32x4 __attribute__((ext_vector_type(4)));

static __device__ __forceinline__ float bf2f(unsigned short u) { return __uint_as_float((unsigned)u << 16); }
static __device__ __forceinline__ unsigned short f2bf(float f) { return __builtin_bit_cast(unsigned short, (__bf16)f); }

__global__ __launch_bounds__(256, 1) __attribute__((aligned(4096))) void win_attn_kernel(const char* x_, const char* wqkv_, const char* wo_, const char* rpb_,
                                                          const char* rpi_, const char* mask_, char* out_, char* ws) {
    __shared__ __attribute__((aligned(16))) char smem[163840];
    const void* ka = (const void*)__builtin_amdgcn_kernarg_segment_ptr();
    unsigned bid = blockIdx.x, tid = threadIdx.x, flg, w;
    unsigned ldsb = (unsigned)(size_t)smem;
    asm volatile(
        "s_load_dwordx16 s[68:83], s[0:1], 0\n"
        "v_readfirstlane_b32 s42, v0\n"
        "v_and_b32 v1, 63, v0\n"
        "s_lshr_b32 s31, s2, 1\n"
        "s_lshr_b32 s42, s42, 6\n"
        "s_and_b32 s31, s31, 0x7c\n"
        "v_lshlrev_b32 v2, 4, v1\n"
        "s_add_u32 s31, s31, s42\n"
        "s_mul_i32 s35, s31, 256\n"
        "v_min_u32 v10, 3, v1\n"
        "s_getpc_b64 s[66:67]\n"
        "v_lshl_add_u32 v10, v10, 6, s35\n"
        "global_load_dword v60, v10, s[66:67]\n"
        "s_lshl_b32 s50, s31, 6\n"
        "s_mul_i32 s50, s50, 27\n"
        "v_min_u32 v136, 26, v1\n"
        "v_lshl_add_u32 v136, v136, 6, s50\n"
        "v_mul_u32_u24 v11, 171, v1\n"
        "v_lshrrev_b32 v4, 10, v11\n"
        "v_bfe_u32 v7, v11, 13, 1\n"
        "v_add_u32 v11, 10944, v11\n"
        "v_lshrrev_b32 v5, 10, v11\n"
        "v_bfe_u32 v8, v11, 13, 1\n"
        "v_add_u32 v11, 10944, v11\n"
        "v_lshrrev_b32 v6, 10, v11\n"
        "v_bfe_u32 v9, v11, 13, 1\n"
        "s_mul_i32 s40, s42, 3072\n"
        "s_mov_b32 s10, 100663296\n"
        "s_mov_b32 s11, 0x20000\n"
        "s_mov_b32 s6, 16384\n"
        "s_mov_b32 s7, 0x20000\n"
        "s_mov_b32 s62, 1800\n"
        "s_mov_b32 s63, 0x20000\n"
        "s_mov_b32 s22, 221184\n"
        "s_mov_b32 s23, 0x20000\n"
        "s_and_b32 s51, s42, 1\n"
        "v_lshlrev_b32 v134, 3, v0\n"
        "s_lshl_b32 s35, s51, 7\n"
        "v_and_b32 v10, 48, v1\n"
        "v_xor_b32 v131, s35, v2\n"
        "s_lshl_b32 s46, s42, 12\n"
        "v_xor_b32 v131, v131, v10\n"
        "v_xor_b32 v132, 64, v131\n"
        "s_and_b32 s44, s2, 63\n"
        "s_lshl3_add_u32 s44, s44, 4\n"
        "s_lshl_b32 s35, s42, 5\n"
        "s_movk_i32 s41, 384\n"
        "s_movk_i32 s45, -384\n"
        "v_lshrrev_b32 v10, 2, v4\n"
        "v_mad_i32_i24 v11, v10, s45, v2\n"
        "v_add_u32 v10, s44, v10\n"
        "v_lshl_or_b32 v242, v7, 4, s35\n"
        "v_and_b32 v10, 511, v10\n"
        "v_xor_b32 v11, v11, v242\n"
        "v_mad_u32_u24 v242, v10, s41, v11\n"
        "v_lshrrev_b32 v10, 2, v5\n"
        "v_mad_i32_i24 v11, v10, s45, v2\n"
        "v_add_u32 v10, s44, v10\n"
        "v_add_u32 v11, 1024, v11\n"
        "v_lshl_or_b32 v243, v8, 4, s35\n"
        "v_and_b32 v10, 511, v10\n"
        "v_xor_b32 v11, v11, v243\n"
        "v_mad_u32_u24 v243, v10, s41, v11\n"
        "v_lshrrev_b32 v10, 2, v6\n"
        "v_mad_i32_i24 v11, v10, s45, v2\n"
        "v_add_u32 v10, s44, v10\n"
        "v_add_u32 v11, 2048, v11\n"
        "v_lshl_or_b32 v244, v9, 4, s35\n"
        "v_and_b32 v10, 511, v10\n"
        "v_xor_b32 v11, v11, v244\n"
        "v_mad_u32_u24 v244, v10, s41, v11\n"
        "s_mul_i32 s43, s44, 384\n"
        "s_add_u32 s44, s44, 4\n"
        "s_and_b32 s44, s44, 511\n"
        "s_mul_i32 s44, s44, 384\n"
        "s_lshr_b32 s37, s2, 6\n"
        "s_lshl3_add_u32 s47, s37, s42\n"
        "s_add_u32 s47, s47, 68\n"
        "s_mul_i32 s47, s47, 196608\n"
        "s_lshl1_add_u32 s37, s37, s51\n"
        "s_add_u32 s37, s37, 1\n"
        "s_mul_i32 s37, s37, 786432\n"
        "s_mul_i32 s65, s42, 18432\n"
        "s_mov_b32 s14, 33554432\n"
        "s_mov_b32 s15, 0x20000\n"
        "s_mul_i32 s35, s42, 96\n"
        "s_movk_i32 s31, 1056\n"
        "s_movk_i32 s45, 4608\n"
        "v_add_u32 v3, s35, v2\n"
        "s_waitcnt lgkmcnt(0)\n"
        "s_mov_b64 s[60:61], s[74:75]\n"
        "s_mov_b64 s[20:21], s[70:71]\n"
        "s_mov_b64 s[4:5], s[76:77]\n"
        "s_mov_b64 s[8:9], s[68:69]\n"
        "buffer_load_dwordx2 v[144:145], v134, s[60:63], 0 offen\n"
        "s_add_u32 m0, s46, 122880\n"
        "s_mov_b32 s64, s46\n"
        "buffer_load_dword v136, s[20:23], 0 offen lds\n"
        "s_mov_b64 s[52:53], s[72:73]\n"
        "s_mov_b64 s[12:13], s[78:79]\n"
        "buffer_load_dwordx4 v131, s[4:7], s64 offen lds\n"
        "s_mov_b64 s[16:17], s[80:81]\n"
        "v_bfe_u32 v12, v4, 2, 1\n"
        "v_mad_u32_u24 v128, v4, s31, v3\n"
        "v_sub_u32 v12, v12, v7\n"
        "v_mad_i32_i24 v128, v12, s45, v128\n"
        "v_bfe_u32 v13, v5, 2, 1\n"
        "buffer_load_dwordx4 v131, s[4:7], s64 offen offset:1024 lds\n"
        "v_mad_u32_u24 v129, v5, s31, v3\n"
        "v_sub_u32 v13, v13, v8\n"
        "v_mad_i32_i24 v129, v13, s45, v129\n"
        "v_bfe_u32 v14, v6, 2, 1\n"
        "v_mad_u32_u24 v130, v6, s31, v3\n"
        "v_sub_u32 v14, v14, v9\n"
        "v_mad_i32_i24 v130, v14, s45, v130\n"
        "s_lshl_b32 s48, s42, 11\n"
        "s_lshl_b32 s36, s2, 13\n"
        "v_and_b32 v19, 16, v1\n"
        "buffer_load_dwordx4 v132, s[4:7], s64 offen offset:2048 lds\n"
        "s_add_u32 s48, s48, 155648\n"
        "s_add_u32 s36, s36, s48\n"
        "s_add_u32 s36, s36, 4038656\n"
        "s_lshl_b32 s35, s51, 6\n"
        "v_xor_b32 v249, v2, v19\n"
        "v_xor_b32 v249, s35, v249\n"
        "v_xor_b32 v253, 32, v249\n"
        "v_and_b32 v15, 15, v1\n"
        "v_lshrrev_b32 v16, 4, v1\n"
        "v_and_b32 v17, 12, v1\n"
        "buffer_load_dwordx4 v132, s[4:7], s64 offen offset:3072 lds\n"
        "v_and_b32 v18, 0xf0, v2\n"
        "v_add_u32 v17, v17, v15\n"
        "v_bfe_u32 v10, v1, 2, 3\n"
        "v_lshrrev_b32 v11, 5, v1\n"
        "v_lshl_or_b32 v10, v11, 4, v10\n"
        "v_and_b32 v255, 3, v1\n"
        "v_mul_u32_u24 v10, 96, v10\n"
        "v_lshl_add_u32 v255, v255, 3, s65\n"
        "v_add_u32 v255, v255, v10\n"
        "v_lshrrev_b32 v20, 1, v15\n"
        "s_add_u32 m0, s40, 73728\n"
        "s_sub_u32 s64, s47, 12582912\n"
        "buffer_load_dwordx4 v242, s[8:11], s64 offen lds\n"
        "v_xor_b32 v20, v20, v16\n"
        "v_lshlrev_b32 v20, 4, v20\n"
        "v_mad_u32_u24 v240, v17, s41, v20\n"
        "v_lshl_or_b32 v251, v17, 7, v20\n"
        "v_add_u32 v240, 73728, v240\n"
        "v_add_u32 v251, 155648, v251\n"
        "v_xor_b32 v241, 64, v240\n"
        "v_xor_b32 v252, 64, v251\n"
        "s_lshl_b32 s67, s42, 10\n"
        "s_add_u32 s67, s67, 139264\n"
        "s_add_u32 m0, s40, 74752\n"
        "s_nop 0\n"
        "buffer_load_dwordx4 v243, s[8:11], s64 offen lds\n"
        "v_lshlrev_b32 v135, 2, v0\n"
        "v_add_u32 v135, 139264, v135\n"
        "v_lshlrev_b32 v11, 5, v16\n"
        "v_xor_b32 v11, v11, v18\n"
        "v_lshl_or_b32 v11, v17, 8, v11\n"
        "v_add_u32 v140, 122880, v11\n"
        "v_xor_b32 v141, 16, v140\n"
        "v_xor_b32 v142, 128, v140\n"
        "v_xor_b32 v143, 144, v140\n"
        "v_lshrrev_b32 v10, 2, v15\n"
        "s_add_u32 m0, s40, 75776\n"
        "s_nop 0\n"
        "buffer_load_dwordx4 v244, s[8:11], s64 offen lds\n"
        "v_cmp_eq_u32 vcc, v10, v16\n"
        "v_mov_b32 v11, 0x3fb9\n"
        "v_mov_b32 v189, 0\n"
        "v_and_b32 v10, 48, v2\n"
        "v_cndmask_b32 v188, 0, v11, vcc\n"
        "v_lshlrev_b64 v[188:189], v10, v[188:189]\n"
        "v_cmp_gt_u32 vcc, 32, v1\n"
        "v_mov_b32 v11, 15957\n"
        "v_lshrrev_b32 v149, 2, v15\n"
        "v_lshrrev_b32 v151, 3, v19\n"
        "v_mov_b32 v148, 14702\n"
        "v_cndmask_b32 v11, v148, v11, vcc\n"
        "v_xor_b32 v151, v149, v151\n"
        "v_cmp_eq_u32 vcc, 0, v151\n"
        "v_mov_b32 v149, 0\n"
        "v_cndmask_b32 v148, 0, v11, vcc\n"
        "v_lshlrev_b64 v[148:149], v10, v[148:149]\n"
        "v_cmp_eq_u32 vcc, 1, v151\n"
        "v_mov_b32 v151, 0\n"
        "v_cndmask_b32 v150, 0, v11, vcc\n"
        "v_lshlrev_b64 v[150:151], v10, v[150:151]\n"
        "v_and_b32 v10, 8, v1\n"
        "v_add_u32 v10, v10, v15\n"
        "v_mul_u32_u24 v10, 96, v10\n"
        "v_add3_u32 v137, v10, v19, s65\n"
        "v_and_b32 v237, 1, v1\n"
        "v_mul_u32_u24 v237, 0xaaaa, v237\n"
        "v_mov_b64 v[170:171], v[188:189]\n"
        "v_mov_b64 v[210:211], v[188:189]\n"
        "v_mov_b64 v[228:229], v[188:189]\n"
        "v_add_u32 v237, 0x4444, v237\n"
        "v_mov_b32 v11, 0x3f803f80\n"
        "v_accvgpr_write_b32 a252, v11\n"
        "v_accvgpr_write_b32 a253, v11\n"
        "v_accvgpr_write_b32 a254, v11\n"
        "v_accvgpr_write_b32 a255, v11\n"
        "v_lshl_or_b32 v10, v16, 10, v18\n"
        "s_mul_i32 s31, s42, 6144\n"
        "s_add_u32 s31, s31, 73728\n"
        "s_lshl_b32 s35, s51, 9\n"
        "s_add_u32 s35, s35, 73728\n"
        "v_lshl_or_b32 v11, v16, 2, v18\n"
        "v_add_u32 v247, s35, v10\n"
        "v_add_u32 v245, s31, v11\n"
        "v_mul_u32_u24 v10, 384, v16\n"
        "s_movk_i32 s45, 1728\n"
        "v_lshrrev_b32 v235, 2, v15\n"
        "v_lshrrev_b32 v254, 1, v15\n"
        "v_and_b32 v11, 1, v1\n"
        "v_mad_u32_u24 v235, v235, 48, v10\n"
        "v_and_b32 v248, 1, v254\n"
        "v_mad_u32_u24 v235, v248, s45, v235\n"
        "v_mul_u32_u24 v248, 36, v11\n"
        "v_lshlrev_b32 v11, 5, v11\n"
        "v_xor_b32 v254, v254, v248\n"
        "v_add3_u32 v235, v235, v11, s65\n"
        "v_mad_u32_u24 v254, v254, 48, v10\n"
        "v_add3_u32 v254, v254, 16, s65\n"
        "s_lshr_b32 s31, s42, 1\n"
        "s_mul_i32 s35, s31, 36864\n"
        "s_movk_i32 s45, 18048\n"
        "v_mad_u32_u24 v10, v15, 48, v10\n"
        "v_lshrrev_b32 v248, 3, v15\n"
        "v_mad_u32_u24 v10, v248, s45, v10\n"
        "v_add_u32 v248, s35, v10\n"
        "s_lshr_b32 s49, s42, 1\n"
        "s_mul_i32 s49, s49, -192\n"
        "s_add_u32 s49, s49, 128\n"
        "s_mul_i32 s31, s31, 256\n"
        "v_mul_u32_u24 v10, 196608, v16\n"
        "v_mul_u32_u24 v11, 8, v15\n"
        "v_add3_u32 v250, v10, v11, s31\n"
        "s_lshl_b32 s35, s51, 6\n"
        "s_add_u32 s66, s31, s35\n"
        "s_lshl_b32 s35, s42, 5\n"
        "s_sub_u32 s35, s35, s66\n"
        "s_add_u32 s27, s35, 64\n"
        "s_movk_i32 s31, 288\n"
        "s_movk_i32 s35, 768\n"
        "v_mad_u32_u24 v10, v4, s31, v2\n"
        "v_bfe_u32 v11, v10, 6, 1\n"
        "v_add_u32 v10, s66, v10\n"
        "v_mad_i32_i24 v10, v11, s27, v10\n"
        "v_bfe_u32 v11, v4, 1, 1\n"
        "v_bfe_u32 v232, v4, 2, 1\n"
        "v_sub_u32 v11, v11, v232\n"
        "v_mad_i32_i24 v232, v11, s35, v10\n"
        "v_mad_u32_u24 v10, v5, s31, v2\n"
        "v_bfe_u32 v11, v10, 6, 1\n"
        "v_add_u32 v10, s66, v10\n"
        "v_mad_i32_i24 v10, v11, s27, v10\n"
        "v_bfe_u32 v11, v5, 1, 1\n"
        "v_bfe_u32 v233, v5, 2, 1\n"
        "v_sub_u32 v11, v11, v233\n"
        "v_mad_i32_i24 v233, v11, s35, v10\n"
        "v_mad_u32_u24 v10, v6, s31, v2\n"
        "v_bfe_u32 v11, v10, 6, 1\n"
        "v_add_u32 v10, s66, v10\n"
        "v_mad_i32_i24 v10, v11, s27, v10\n"
        "v_bfe_u32 v11, v6, 1, 1\n"
        "v_bfe_u32 v234, v6, 2, 1\n"
        "v_sub_u32 v11, v11, v234\n"
        "v_mad_i32_i24 v234, v11, s35, v10\n"
        "s_mov_b32 s26, 0x3fb8aa3b\n"
        "s_mov_b32 s24, 0x5d800000\n"
        "s_mov_b32 s27, 14\n"
        "s_mov_b32 s28, 98304\n"
        "s_mov_b32 s29, 73728\n"
        "s_mov_b32 s30, 122880\n"
        "s_mov_b32 s45, 0\n"
        "s_mov_b32 s25, 0\n"
        "s_mov_b32 s54, 73728\n"
        "s_mov_b32 s55, 0x20000\n"
        "s_mov_b32 s18, 100663296\n"
        "s_mov_b32 s19, 0x20000\n"
        "s_mov_b32 s56, 36864\n"
        "s_mov_b32 s57, 73728\n"
        "s_mov_b32 s58, 110592\n"
        "s_mov_b32 s59, 147456\n"
        "s_mov_b32 s60, 184320\n"
        "s_mov_b32 s61, 24576\n"
        "s_mov_b32 s62, 61440\n"
        "s_mov_b32 s63, 12288\n"
        "s_mov_b32 s34, 49152\n"
        "s_add_u32 m0, s40, 86016\n"
        "s_sub_u32 s64, s47, 11796480\n"
        "buffer_load_dwordx4 v242, s[8:11], s64 offen lds\n"
        "s_waitcnt vmcnt(9)\n"
        "v_and_b32 v146, 0xffff0000, v144\n"
        "v_and_b32 v147, 0xffff0000, v145\n"
        "v_lshlrev_b32 v144, 16, v144\n"
        "v_lshlrev_b32 v145, 16, v145\n"
        "s_add_u32 m0, s40, 87040\n"
        "v_mul_f32 v146, s26, v146\n"
        "buffer_load_dwordx4 v243, s[8:11], s64 offen lds\n"
        "v_mul_f32 v147, s26, v147\n"
        "v_mul_f32 v144, s26, v144\n"
        "v_mul_f32 v145, s26, v145\n"
        "s_mov_b32 s38, s67\n"
        "s_mov_b32 s39, s67\n"
        "ds_write2st64_b32 v135, v144, v146 offset0:0 offset1:4\n"
        "ds_write2st64_b32 v135, v145, v147 offset0:8 offset1:12\n"
        "s_cmp_eq_u32 s42, 0\n"
        "s_cbranch_scc1 41f\n"
        "s_cmp_eq_u32 s42, 1\n"
        "s_cbranch_scc1 42f\n"
        "s_cmp_eq_u32 s42, 2\n"
        "s_cbranch_scc1 43f\n"
        "s_cmp_eq_u32 s42, 3\n"
        "s_cbranch_scc1 44f\n"
        "60:\n"
        "s_add_u32 m0, s40, 88064\n"
        "s_nop 0\n"
        "buffer_load_dwordx4 v244, s[8:11], s64 offen lds\n"
        "s_waitcnt lgkmcnt(0)\n"
        "s_waitcnt vmcnt(6)\n"
        "s_barrier\n"
        "s_add_u32 m0, s65, 0\n"
        "s_mov_b32 s64, 384\n"
        "buffer_load_dwordx4 v128, s[20:23], s64 offen lds\n"
        "buffer_load_dwordx4 v129, s[20:23], s64 offen offset:1024 lds\n"
        "ds_read_b128 v[0:3], v140 offset:0\n"
        "ds_read_b128 v[4:7], v141 offset:0\n"
        "ds_read_b128 v[16:19], v140 offset:1024\n"
        "ds_read_b128 v[20:23], v141 offset:1024\n"
        "ds_read_b128 v[8:11], v142 offset:0\n"
        "ds_read_b128 v[24:27], v142 offset:1024\n"
        "ds_read_b128 v[12:15], v143 offset:0\n"
        "ds_read_b128 v[28:31], v143 offset:1024\n"
        "ds_read_b128 v[32:35], v140 offset:8192\n"
        "ds_read_b128 v[36:39], v141 offset:8192\n"
        "ds_read_b128 v[40:43], v142 offset:8192\n"
        "ds_read_b128 v[44:47], v143 offset:8192\n"
        "ds_read_b128 v[48:51], v140 offset:9216\n"
        "s_cmp_eq_u32 s42, 0\n"
        "s_cbranch_scc1 51f\n"
        "s_cmp_eq_u32 s42, 1\n"
        "s_cbranch_scc1 45f\n"
        "s_cmp_eq_u32 s42, 2\n"
        "s_cbranch_scc1 46f\n"
        "s_cmp_eq_u32 s42, 3\n"
        "s_cbranch_scc1 47f\n"
        "s_nop 0\n"
        "s_nop 0\n"
        "s_nop 0\n"
        "s_nop 0\n"
        "s_nop 0\n"
        "s_nop 0\n"
        "s_nop 0\n"
        "s_nop 0\n"
        "s_nop 0\n"
        "s_nop 0\n"
        "s_nop 0\n"
        "s_nop 0\n"
        "s_nop 0\n"
        "61:\n"
        "buffer_load_dwordx4 v130, s[20:23], s64 offen offset:2048 lds\n"
        "s_add_u32 m0, s65, 3072\n"
        "s_waitcnt lgkmcnt(12)\n"
        "v_lshl_add_u64 v[0:1], v[0:1], 2, s[38:39]\n"
        "buffer_load_dwordx4 v128, s[20:23], 0 offen lds\n"
        "v_lshl_add_u64 v[2:3], v[2:3], 2, s[38:39]\n"
        "s_waitcnt lgkmcnt(11)\n"
        "v_lshl_add_u64 v[4:5], v[4:5], 2, s[38:39]\n"
        "v_lshl_add_u64 v[6:7], v[6:7], 2, s[38:39]\n"
        "s_waitcnt lgkmcnt(10)\n"
        "v_lshl_add_u64 v[16:17], v[16:17], 2, s[38:39]\n"
        "v_lshl_add_u64 v[18:19], v[18:19], 2, s[38:39]\n"
        "buffer_load_dwordx4 v129, s[20:23], 0 offen offset:1024 lds\n"
        "ds_read_b32 v0, v0\n"
        "ds_read_b32 v1, v1\n"
        "ds_read_b32 v2, v2\n"
        "ds_read_b32 v3, v3\n"
        "ds_read_b32 v4, v4\n"
        "ds_read_b32 v5, v5\n"
        "ds_read_b32 v6, v6\n"
        "ds_read_b32 v7, v7\n"
        "ds_read_b32 v16, v16\n"
        "ds_read_b32 v17, v17\n"
        "ds_read_b32 v18, v18\n"
        "ds_read_b32 v19, v19\n"
        "buffer_load_dwordx4 v130, s[20:23], 0 offen offset:2048 lds\n"
        "s_waitcnt lgkmcnt(15)\n"
        "v_lshl_add_u64 v[20:21], v[20:21], 2, s[38:39]\n"
        "v_lshl_add_u64 v[22:23], v[22:23], 2, s[38:39]\n"
        "v_lshl_add_u64 v[8:9], v[8:9], 2, s[38:39]\n"
        "v_lshl_add_u64 v[10:11], v[10:11], 2, s[38:39]\n"
        "v_lshl_add_u64 v[24:25], v[24:25], 2, s[38:39]\n"
        "v_lshl_add_u64 v[26:27], v[26:27], 2, s[38:39]\n"
        "ds_read_b32 v20, v20\n"
        "ds_read_b32 v21, v21\n"
        "ds_read_b32 v22, v22\n"
        "ds_read_b32 v23, v23\n"
        "ds_read_b32 v8, v8\n"
        "ds_read_b32 v9, v9\n"
        "ds_read_b32 v10, v10\n"
        "ds_read_b32 v11, v11\n"
        "ds_read_b32 v24, v24\n"
        "ds_read_b32 v25, v25\n"
        "ds_read_b32 v26, v26\n"
        "ds_read_b32 v27, v27\n"
        "s_add_u32 m0, s65, 6144\n"
        "v_lshl_add_u64 v[12:13], v[12:13], 2, s[38:39]\n"
        "buffer_load_dwordx4 v128, s[20:23], s56 offen lds\n"
        "v_lshl_add_u64 v[14:15], v[14:15], 2, s[38:39]\n"
        "v_lshl_add_u64 v[28:29], v[28:29], 2, s[38:39]\n"
        "v_lshl_add_u64 v[30:31], v[30:31], 2, s[38:39]\n"
        "v_lshl_add_u64 v[32:33], v[32:33], 2, s[38:39]\n"
        "v_lshl_add_u64 v[34:35], v[34:35], 2, s[38:39]\n"
        "ds_read_b32 v12, v12\n"
        "ds_read_b32 v13, v13\n"
        "ds_read_b32 v14, v14\n"
        "ds_read_b32 v15, v15\n"
        "ds_read_b32 v28, v28\n"
        "ds_read_b32 v29, v29\n"
        "ds_read_b32 v30, v30\n"
        "ds_read_b32 v31, v31\n"
        "ds_read_b32 v32, v32\n"
        "ds_read_b32 v33, v33\n"
        "ds_read_b32 v34, v34\n"
        "ds_read_b32 v35, v35\n"
        "buffer_load_dwordx4 v129, s[20:23], s56 offen offset:1024 lds\n"
        "v_lshl_add_u64 v[36:37], v[36:37], 2, s[38:39]\n"
        "v_lshl_add_u64 v[38:39], v[38:39], 2, s[38:39]\n"
        "s_waitcnt lgkmcnt(15)\n"
        "v_lshl_add_u64 v[40:41], v[40:41], 2, s[38:39]\n"
        "v_lshl_add_u64 v[42:43], v[42:43], 2, s[38:39]\n"
        "v_lshl_add_u64 v[44:45], v[44:45], 2, s[38:39]\n"
        "v_lshl_add_u64 v[46:47], v[46:47], 2, s[38:39]\n"
        "ds_read_b32 v36, v36\n"
        "ds_read_b32 v37, v37\n"
        "ds_read_b32 v38, v38\n"
        "ds_read_b32 v39, v39\n"
        "ds_read_b32 v40, v40\n"
        "ds_read_b32 v41, v41\n"
        "ds_read_b32 v42, v42\n"
        "ds_read_b32 v43, v43\n"
        "ds_read_b32 v44, v44\n"
        "ds_read_b32 v45, v45\n"
        "ds_read_b32 v46, v46\n"
        "ds_read_b32 v47, v47\n"
        "buffer_load_dwordx4 v130, s[20:23], s56 offen offset:2048 lds\n"
        "v_lshl_add_u64 v[48:49], v[48:49], 2, s[38:39]\n"
        "v_lshl_add_u64 v[50:51], v[50:51], 2, s[38:39]\n"
        "ds_read_b32 v48, v48\n"
        "ds_read_b32 v49, v49\n"
        "ds_read_b32 v50, v50\n"
        "ds_read_b32 v51, v51\n"
        "s_waitcnt vmcnt(9)\n"
        "s_barrier\n"
        "ds_read_b128 a[216:219], v240 offset:0\n"
        "ds_read_b128 a[220:223], v240 offset:1536\n"
        "ds_read_b128 a[224:227], v240 offset:12288\n"
        "ds_read_b128 a[228:231], v240 offset:13824\n"
        "s_waitcnt vmcnt(6)\n"
        "ds_read_b64_tr_b16 a[72:73], v255 offset:0\n"
        "ds_read_b64_tr_b16 a[74:75], v255 offset:768\n"
        "ds_read_b64_tr_b16 a[96:97], v255 offset:32\n"
        "ds_read_b64_tr_b16 a[98:99], v255 offset:800\n"
        "ds_read_b64_tr_b16 a[120:121], v255 offset:64\n"
        "ds_read_b64_tr_b16 a[122:123], v255 offset:832\n"
        "s_add_u32 m0, s65, 8832\n"
        "s_nop 0\n"
        "buffer_load_dwordx4 v128, s[20:23], s56 offen offset:384 lds\n"
        "s_waitcnt lgkmcnt(4)\n"
        "v_mfma_f32_16x16x32_bf16 v[152:155], a[72:75], a[216:219], 0\n"
        "ds_read_b128 a[232:235], v241 offset:0\n"
        "ds_read_b128 a[236:239], v241 offset:1536\n"
        "v_mfma_f32_16x16x32_bf16 v[172:175], a[72:75], a[220:223], 0\n"
        "ds_read_b128 a[240:243], v241 offset:12288\n"
        "ds_read_b128 a[244:247], v241 offset:13824\n"
        "v_mfma_f32_16x16x32_bf16 v[192:195], a[72:75], a[224:227], 0\n"
        "v_mfma_f32_16x16x32_bf16 v[212:215], a[72:75], a[228:231], 0\n"
        "s_waitcnt lgkmcnt(6)\n"
        "buffer_load_dwordx4 v129, s[20:23], s56 offen offset:1408 lds\n"
        "v_mfma_f32_16x16x32_bf16 v[88:91], a[96:99], a[228:231], 0\n"
        "v_mfma_f32_16x16x32_bf16 v[80:83], a[96:99], a[224:227], 0\n"
        "buffer_load_dwordx4 v130, s[20:23], s56 offen offset:2432 lds\n"
        "s_waitcnt vmcnt(6)\n"
        "ds_read_b128 a[212:215], v137 offset:3072\n"
        "v_mfma_f32_16x16x32_bf16 v[68:71], a[96:99], a[220:223], 0\n"
        "s_add_u32 m0, s65, 12288\n"
        "ds_read_b128 a[208:211], v137 offset:3840\n"
        "ds_read_b128 a[204:207], v137 offset:3104\n"
        "v_mfma_f32_16x16x32_bf16 v[64:67], a[96:99], a[216:219], 0\n"
        "s_waitcnt lgkmcnt(3)\n"
        "ds_read_b128 a[200:203], v137 offset:3872\n"
        "ds_read_b128 a[196:199], v137 offset:3136\n"
        "v_mfma_f32_16x16x32_bf16 v[72:75], a[120:123], a[216:219], 0\n"
        "buffer_load_dwordx4 v128, s[20:23], s57 offen lds\n"
        "v_mfma_f32_16x16x32_bf16 v[76:79], a[120:123], a[220:223], 0\n"
        "s_waitcnt lgkmcnt(4)\n"
        "ds_read_b128 a[192:195], v137 offset:3904\n"
        "v_mfma_f32_16x16x32_bf16 v[112:115], a[212:215], v[148:151], 0\n"
        "s_waitcnt lgkmcnt(4)\n"
        "s_waitcnt vmcnt(4)\n"
        "ds_read_b128 a[188:191], v137 offset:6144\n"
        "ds_read_b128 a[184:187], v137 offset:6912\n"
        "v_mfma_f32_16x16x32_bf16 v[116:119], a[208:211], v[148:151], 0\n"
        "buffer_load_dwordx4 v129, s[20:23], s57 offen offset:1024 lds\n"
        "v_mfma_f32_16x16x32_bf16 v[84:87], a[120:123], a[224:227], 0\n"
        "ds_read_b128 a[180:183], v137 offset:6176\n"
        "ds_read_b128 a[176:179], v137 offset:6944\n"
        "v_mfma_f32_16x16x32_bf16 v[92:95], a[120:123], a[228:231], 0\n"
        "s_waitcnt lgkmcnt(5)\n"
        "ds_read_b128 a[172:175], v137 offset:6208\n"
        "ds_read_b128 a[168:171], v137 offset:6976\n"
        "v_mfma_f32_16x16x32_bf16 v[120:123], a[204:207], v[148:151], 0\n"
        "v_cvt_pk_bf16_f32 v112, v112, v113\n"
        "v_accvgpr_write_b32 a0, v112\n"
        "v_mfma_f32_16x16x32_bf16 v[124:127], a[200:203], v[148:151], 0\n"
        "buffer_load_dwordx4 v130, s[20:23], s57 offen offset:2048 lds\n"
        "v_mfma_f32_16x16x32_bf16 v[96:99], a[196:199], v[148:151], 0\n"
        "s_waitcnt lgkmcnt(4)\n"
        "v_cvt_pk_bf16_f32 v114, v114, v115\n"
        "v_accvgpr_write_b32 a1, v114\n"
        "v_mfma_f32_16x16x32_bf16 v[100:103], a[192:195], v[148:151], 0\n"
        "s_add_u32 m0, s65, 14976\n"
        "v_cvt_pk_bf16_f32 v116, v116, v117\n"
        "v_accvgpr_write_b32 a2, v116\n"
        "v_mfma_f32_16x16x32_bf16 v[104:107], a[188:191], v[148:151], 0\n"
        "v_cvt_pk_bf16_f32 v118, v118, v119\n"
        "v_accvgpr_write_b32 a3, v118\n"
        "v_mfma_f32_16x16x32_bf16 v[108:111], a[184:187], v[148:151], 0\n"
        "buffer_load_dwordx4 v128, s[20:23], s57 offen offset:384 lds\n"
        "v_mfma_f32_16x16x32_bf16 v[156:159], a[0:3], a[216:219], 0\n"
        "v_cvt_pk_bf16_f32 v120, v120, v121\n"
        "v_accvgpr_write_b32 a24, v120\n"
        "v_mfma_f32_16x16x32_bf16 v[176:179], a[0:3], a[220:223], 0\n"
        "v_cvt_pk_bf16_f32 v122, v122, v123\n"
        "v_accvgpr_write_b32 a25, v122\n"
        "v_mfma_f32_16x16x32_bf16 v[196:199], a[0:3], a[224:227], 0\n"
        "v_cvt_pk_bf16_f32 v124, v124, v125\n"
        "v_accvgpr_write_b32 a26, v124\n"
        "v_mfma_f32_16x16x32_bf16 v[216:219], a[0:3], a[228:231], 0\n"
        "buffer_load_dwordx4 v129, s[20:23], s57 offen offset:1408 lds\n"
        "v_cvt_pk_bf16_f32 v126, v126, v127\n"
        "v_accvgpr_write_b32 a27, v126\n"
        "s_nop 1\n"
        "v_mfma_f32_16x16x32_bf16 v[224:227], a[24:27], a[228:231], 0\n"
        "v_cvt_pk_bf16_f32 v96, v96, v97\n"
        "v_accvgpr_write_b32 a48, v96\n"
        "v_mfma_f32_16x16x32_bf16 v[204:207], a[24:27], a[224:227], 0\n"
        "s_waitcnt vmcnt(5)\n"
        "ds_read_b64_tr_b16 a[76:77], v255 offset:9216\n"
        "ds_read_b64_tr_b16 a[78:79], v255 offset:9984\n"
        "v_mfma_f32_16x16x32_bf16 v[184:187], a[24:27], a[220:223], 0\n"
        "ds_read_b64_tr_b16 a[100:101], v255 offset:9248\n"
        "ds_read_b64_tr_b16 a[102:103], v255 offset:10016\n"
        "v_mfma_f32_16x16x32_bf16 v[164:167], a[24:27], a[216:219], 0\n"
        "buffer_load_dwordx4 v130, s[20:23], s57 offen offset:2432 lds\n"
        "ds_read_b64_tr_b16 a[124:125], v255 offset:9280\n"
        "ds_read_b64_tr_b16 a[126:127], v255 offset:10048\n"
        "v_cvt_pk_bf16_f32 v98, v98, v99\n"
        "v_accvgpr_write_b32 a49, v98\n"
        "v_cvt_pk_bf16_f32 v100, v100, v101\n"
        "v_accvgpr_write_b32 a50, v100\n"
        "v_cvt_pk_bf16_f32 v102, v102, v103\n"
        "v_accvgpr_write_b32 a51, v102\n"
        "s_nop 1\n"
        "v_mfma_f32_16x16x32_bf16 v[160:163], a[48:51], a[216:219], 0\n"
        "s_add_u32 m0, s65, 0\n"
        "ds_read_b128 a[248:251], v240 offset:128\n"
        "v_cvt_pk_bf16_f32 v104, v104, v105\n"
        "v_mfma_f32_16x16x32_bf16 v[180:183], a[48:51], a[220:223], 0\n"
        "ds_read_b128 a[216:219], v240 offset:1664\n"
        "v_accvgpr_write_b32 a4, v104\n"
        "v_mfma_f32_16x16x32_bf16 v[200:203], a[48:51], a[224:227], 0\n"
        "buffer_load_dwordx4 v128, s[20:23], s58 offen lds\n"
        "v_mfma_f32_16x16x32_bf16 v[220:223], a[48:51], a[228:231], 0\n"
        "s_waitcnt lgkmcnt(6)\n"
        "ds_read_b128 a[220:223], v240 offset:12416\n"
        "ds_read_b128 a[224:227], v240 offset:13952\n"
        "v_mfma_f32_16x16x32_bf16 v[112:115], a[180:183], v[148:151], 0\n"
        "v_cvt_pk_bf16_f32 v106, v106, v107\n"
        "v_accvgpr_write_b32 a5, v106\n"
        "v_mfma_f32_16x16x32_bf16 v[116:119], a[176:179], v[148:151], 0\n"
        "v_cvt_pk_bf16_f32 v108, v108, v109\n"
        "v_accvgpr_write_b32 a6, v108\n"
        "v_mfma_f32_16x16x32_bf16 v[152:155], a[76:79], a[232:235], v[152:155]\n"
        "buffer_load_dwordx4 v129, s[20:23], s58 offen offset:1024 lds\n"
        "v_mfma_f32_16x16x32_bf16 v[172:175], a[76:79], a[236:239], v[172:175]\n"
        "v_cvt_pk_bf16_f32 v110, v110, v111\n"
        "v_accvgpr_write_b32 a7, v110\n"
        "v_mfma_f32_16x16x32_bf16 v[192:195], a[76:79], a[240:243], v[192:195]\n"
        "v_cvt_pk_bf16_f32 v112, v112, v113\n"
        "v_accvgpr_write_b32 a28, v112\n"
        "v_mfma_f32_16x16x32_bf16 v[212:215], a[76:79], a[244:247], v[212:215]\n"
        "v_cvt_pk_bf16_f32 v114, v114, v115\n"
        "v_accvgpr_write_b32 a29, v114\n"
        "v_mfma_f32_16x16x32_bf16 v[156:159], a[4:7], a[232:235], v[156:159]\n"
        "buffer_load_dwordx4 v130, s[20:23], s58 offen offset:2048 lds\n"
        "v_mfma_f32_16x16x32_bf16 v[176:179], a[4:7], a[236:239], v[176:179]\n"
        "s_add_u32 m0, s65, 2688\n"
        "v_cvt_pk_bf16_f32 v116, v116, v117\n"
        "v_accvgpr_write_b32 a30, v116\n"
        "v_mfma_f32_16x16x32_bf16 v[196:199], a[4:7], a[240:243], v[196:199]\n"
        "v_cvt_pk_bf16_f32 v118, v118, v119\n"
        "v_accvgpr_write_b32 a31, v118\n"
        "v_mfma_f32_16x16x32_bf16 v[216:219], a[4:7], a[244:247], v[216:219]\n"
        "s_waitcnt vmcnt(6)\n"
        "ds_read_b128 a[212:215], v137 offset:12288\n"
        "ds_read_b128 a[208:211], v137 offset:13056\n"
        "v_mfma_f32_16x16x32_bf16 v[120:123], a[172:175], v[148:151], 0\n"
        "buffer_load_dwordx4 v128, s[20:23], s58 offen offset:384 lds\n"
        "v_mfma_f32_16x16x32_bf16 v[124:127], a[168:171], v[148:151], 0\n"
        "ds_read_b128 a[204:207], v137 offset:12320\n"
        "ds_read_b128 a[200:203], v137 offset:13088\n"
        "v_mfma_f32_16x16x32_bf16 v[224:227], a[28:31], a[244:247], v[224:227]\n"
        ".long 0xbec200ff\n"
        "41:\n"
        "s_branch 60b\n"
        "ds_read_b128 a[196:199], v137 offset:12352\n"
        "ds_read_b128 a[192:195], v137 offset:13120\n"
        "v_mfma_f32_16x16x32_bf16 v[204:207], a[28:31], a[240:243], v[204:207]\n"
        "buffer_load_dwordx4 v129, s[20:23], s58 offen offset:1408 lds\n"
        "v_mfma_f32_16x16x32_bf16 v[184:187], a[28:31], a[236:239], v[184:187]\n"
        "v_cvt_pk_bf16_f32 v120, v120, v121\n"
        "v_accvgpr_write_b32 a52, v120\n"
        "v_mfma_f32_16x16x32_bf16 v[164:167], a[28:31], a[232:235], v[164:167]\n"
        "s_waitcnt lgkmcnt(4)\n"
        "v_cvt_pk_bf16_f32 v122, v122, v123\n"
        "v_accvgpr_write_b32 a53, v122\n"
        "v_mfma_f32_16x16x32_bf16 v[96:99], a[212:215], v[148:151], 0\n"
        "v_cvt_pk_bf16_f32 v124, v124, v125\n"
        "v_accvgpr_write_b32 a54, v124\n"
        "v_mfma_f32_16x16x32_bf16 v[100:103], a[208:211], v[148:151], 0\n"
        "buffer_load_dwordx4 v130, s[20:23], s58 offen offset:2432 lds\n"
        "v_mfma_f32_16x16x32_bf16 v[88:91], a[100:103], a[244:247], v[88:91]\n"
        "s_add_u32 m0, s65, 6144\n"
        "v_cvt_pk_bf16_f32 v126, v126, v127\n"
        "v_accvgpr_write_b32 a55, v126\n"
        "v_mfma_f32_16x16x32_bf16 v[80:83], a[100:103], a[240:243], v[80:83]\n"
        "v_cvt_pk_bf16_f32 v96, v96, v97\n"
        "v_accvgpr_write_b32 a8, v96\n"
        "v_mfma_f32_16x16x32_bf16 v[68:71], a[100:103], a[236:239], v[68:71]\n"
        "v_cvt_pk_bf16_f32 v98, v98, v99\n"
        "v_accvgpr_write_b32 a9, v98\n"
        "v_mfma_f32_16x16x32_bf16 v[64:67], a[100:103], a[232:235], v[64:67]\n"
        "buffer_load_dwordx4 v128, s[20:23], s59 offen lds\n"
        "v_mfma_f32_16x16x32_bf16 v[160:163], a[52:55], a[232:235], v[160:163]\n"
        "s_waitcnt vmcnt(7)\n"
        "ds_read_b64_tr_b16 a[80:81], v255 offset:15360\n"
        "ds_read_b64_tr_b16 a[82:83], v255 offset:16128\n"
        "v_mfma_f32_16x16x32_bf16 v[180:183], a[52:55], a[236:239], v[180:183]\n"
        "ds_read_b64_tr_b16 a[104:105], v255 offset:15392\n"
        "ds_read_b64_tr_b16 a[106:107], v255 offset:16160\n"
        "v_mfma_f32_16x16x32_bf16 v[200:203], a[52:55], a[240:243], v[200:203]\n"
        "ds_read_b64_tr_b16 a[128:129], v255 offset:15424\n"
        "ds_read_b64_tr_b16 a[130:131], v255 offset:16192\n"
        "v_mfma_f32_16x16x32_bf16 v[220:223], a[52:55], a[244:247], v[220:223]\n"
        "s_waitcnt lgkmcnt(6)\n"
        "v_cvt_pk_bf16_f32 v100, v100, v101\n"
        "v_accvgpr_write_b32 a10, v100\n"
        "v_mfma_f32_16x16x32_bf16 v[104:107], a[204:207], v[148:151], 0\n"
        "buffer_load_dwordx4 v129, s[20:23], s59 offen offset:1024 lds\n"
        "v_mfma_f32_16x16x32_bf16 v[108:111], a[200:203], v[148:151], 0\n"
        "ds_read_b128 a[228:231], v241 offset:128\n"
        "v_cvt_pk_bf16_f32 v102, v102, v103\n"
        "v_mfma_f32_16x16x32_bf16 v[72:75], a[124:127], a[232:235], v[72:75]\n"
        "ds_read_b128 a[232:235], v241 offset:1664\n"
        "v_accvgpr_write_b32 a11, v102\n"
        "v_mfma_f32_16x16x32_bf16 v[76:79], a[124:127], a[236:239], v[76:79]\n"
        "buffer_load_dwordx4 v130, s[20:23], s59 offen offset:2048 lds\n"
        "v_mfma_f32_16x16x32_bf16 v[84:87], a[124:127], a[240:243], v[84:87]\n"
        "s_add_u32 m0, s65, 8832\n"
        "ds_read_b128 a[236:239], v241 offset:12416\n"
        "ds_read_b128 a[240:243], v241 offset:13952\n"
        "v_mfma_f32_16x16x32_bf16 v[92:95], a[124:127], a[244:247], v[92:95]\n"
        "s_waitcnt vmcnt(6)\n"
        "ds_read_b128 a[188:191], v137 offset:0\n"
        "ds_read_b128 a[184:187], v137 offset:768\n"
        "v_mfma_f32_16x16x32_bf16 v[156:159], a[8:11], a[248:251], v[156:159]\n"
        "buffer_load_dwordx4 v128, s[20:23], s59 offen offset:384 lds\n"
        "v_mfma_f32_16x16x32_bf16 v[176:179], a[8:11], a[216:219], v[176:179]\n"
        "ds_read_b128 a[180:183], v137 offset:32\n"
        "ds_read_b128 a[176:179], v137 offset:800\n"
        "v_mfma_f32_16x16x32_bf16 v[196:199], a[8:11], a[220:223], v[196:199]\n"
        "ds_read_b128 a[172:175], v137 offset:64\n"
        "ds_read_b128 a[168:171], v137 offset:832\n"
        "v_mfma_f32_16x16x32_bf16 v[216:219], a[8:11], a[224:227], v[216:219]\n"
        "v_cvt_pk_bf16_f32 v104, v104, v105\n"
        "v_accvgpr_write_b32 a32, v104\n"
        "v_mfma_f32_16x16x32_bf16 v[112:115], a[196:199], v[148:151], 0\n"
        "buffer_load_dwordx4 v129, s[20:23], s59 offen offset:1408 lds\n"
        "v_mfma_f32_16x16x32_bf16 v[116:119], a[192:195], v[148:151], 0\n"
        "s_waitcnt lgkmcnt(4)\n"
        "v_cvt_pk_bf16_f32 v106, v106, v107\n"
        "v_accvgpr_write_b32 a33, v106\n"
        "v_mfma_f32_16x16x32_bf16 v[152:155], a[80:83], a[248:251], v[152:155]\n"
        "v_cvt_pk_bf16_f32 v108, v108, v109\n"
        "v_accvgpr_write_b32 a34, v108\n"
        "v_mfma_f32_16x16x32_bf16 v[172:175], a[80:83], a[216:219], v[172:175]\n"
        "v_cvt_pk_bf16_f32 v110, v110, v111\n"
        "v_accvgpr_write_b32 a35, v110\n"
        "v_mfma_f32_16x16x32_bf16 v[192:195], a[80:83], a[220:223], v[192:195]\n"
        "buffer_load_dwordx4 v130, s[20:23], s59 offen offset:2432 lds\n"
        "v_mfma_f32_16x16x32_bf16 v[212:215], a[80:83], a[224:227], v[212:215]\n"
        "s_add_u32 m0, s65, 12288\n"
        "v_cvt_pk_bf16_f32 v112, v112, v113\n"
        "v_accvgpr_write_b32 a56, v112\n"
        "v_mfma_f32_16x16x32_bf16 v[224:227], a[32:35], a[224:227], v[224:227]\n"
        "ds_read_b128 v[52:55], v141 offset:9216\n"
        "v_cvt_pk_bf16_f32 v114, v114, v115\n"
        "v_mfma_f32_16x16x32_bf16 v[204:207], a[32:35], a[220:223], v[204:207]\n"
        "v_accvgpr_write_b32 a57, v114\n"
        "v_cvt_pk_bf16_f32 v116, v116, v117\n"
        "v_mfma_f32_16x16x32_bf16 v[184:187], a[32:35], a[216:219], v[184:187]\n"
        "buffer_load_dwordx4 v128, s[20:23], s60 offen lds\n"
        "v_mfma_f32_16x16x32_bf16 v[164:167], a[32:35], a[248:251], v[164:167]\n"
        "v_accvgpr_write_b32 a58, v116\n"
        "v_cvt_pk_bf16_f32 v118, v118, v119\n"
        "v_mfma_f32_16x16x32_bf16 v[120:123], a[188:191], v[148:151], 0\n"
        "s_waitcnt vmcnt(7)\n"
        "ds_read_b64_tr_b16 a[84:85], v255 offset:3072\n"
        "ds_read_b64_tr_b16 a[86:87], v255 offset:3840\n"
        "v_mfma_f32_16x16x32_bf16 v[124:127], a[184:187], v[148:151], 0\n"
        "ds_read_b64_tr_b16 a[108:109], v255 offset:3104\n"
        "ds_read_b64_tr_b16 a[110:111], v255 offset:3872\n"
        "v_mfma_f32_16x16x32_bf16 v[88:91], a[104:107], a[224:227], v[88:91]\n"
        "buffer_load_dwordx4 v129, s[20:23], s60 offen offset:1024 lds\n"
        "ds_read_b64_tr_b16 a[132:133], v255 offset:3136\n"
        "ds_read_b64_tr_b16 a[134:135], v255 offset:3904\n"
        "v_mfma_f32_16x16x32_bf16 v[80:83], a[104:107], a[220:223], v[80:83]\n"
        "v_accvgpr_write_b32 a59, v118\n"
        "v_cvt_pk_bf16_f32 v120, v120, v121\n"
        "v_mfma_f32_16x16x32_bf16 v[68:71], a[104:107], a[216:219], v[68:71]\n"
        "v_accvgpr_write_b32 a12, v120\n"
        "v_cvt_pk_bf16_f32 v122, v122, v123\n"
        "v_mfma_f32_16x16x32_bf16 v[64:67], a[104:107], a[248:251], v[64:67]\n"
        "buffer_load_dwordx4 v130, s[20:23], s60 offen offset:2048 lds\n"
        "v_mfma_f32_16x16x32_bf16 v[160:163], a[56:59], a[248:251], v[160:163]\n"
        "s_add_u32 m0, s65, 14976\n"
        "v_accvgpr_write_b32 a13, v122\n"
        "v_cvt_pk_bf16_f32 v124, v124, v125\n"
        "v_mfma_f32_16x16x32_bf16 v[180:183], a[56:59], a[216:219], v[180:183]\n"
        "s_waitcnt lgkmcnt(2)\n"
        "v_lshl_add_u64 v[52:53], v[52:53], 2, s[38:39]\n"
        "v_lshl_add_u64 v[54:55], v[54:55], 2, s[38:39]\n"
        "v_mfma_f32_16x16x32_bf16 v[200:203], a[56:59], a[220:223], v[200:203]\n"
        "ds_read_b32 v52, v52\n"
        "ds_read_b32 v53, v53\n"
        "v_mfma_f32_16x16x32_bf16 v[220:223], a[56:59], a[224:227], v[220:223]\n"
        "ds_read_b32 v54, v54\n"
        "ds_read_b32 v55, v55\n"
        "v_mfma_f32_16x16x32_bf16 v[96:99], a[180:183], v[148:151], 0\n"
        "buffer_load_dwordx4 v128, s[20:23], s60 offen offset:384 lds\n"
        "v_mfma_f32_16x16x32_bf16 v[100:103], a[176:179], v[148:151], 0\n"
        "ds_read_b128 a[244:247], v240 offset:256\n"
        "v_accvgpr_write_b32 a14, v124\n"
        "v_mfma_f32_16x16x32_bf16 v[72:75], a[128:131], a[248:251], v[72:75]\n"
        "ds_read_b128 a[248:251], v240 offset:1792\n"
        "v_cvt_pk_bf16_f32 v126, v126, v127\n"
        "v_mfma_f32_16x16x32_bf16 v[76:79], a[128:131], a[216:219], v[76:79]\n"
        "buffer_load_dwordx4 v129, s[20:23], s60 offen offset:1408 lds\n"
        "v_mfma_f32_16x16x32_bf16 v[84:87], a[128:131], a[220:223], v[84:87]\n"
        "ds_read_b128 a[216:219], v240 offset:12544\n"
        "ds_read_b128 a[220:223], v240 offset:14080\n"
        "v_mfma_f32_16x16x32_bf16 v[92:95], a[128:131], a[224:227], v[92:95]\n"
        "v_accvgpr_write_b32 a15, v126\n"
        "v_cvt_pk_bf16_f32 v96, v96, v97\n"
        "s_nop 0\n"
        "v_mfma_f32_16x16x32_bf16 v[156:159], a[12:15], a[228:231], v[156:159]\n"
        "v_accvgpr_write_b32 a36, v96\n"
        "v_cvt_pk_bf16_f32 v98, v98, v99\n"
        "v_mfma_f32_16x16x32_bf16 v[176:179], a[12:15], a[232:235], v[176:179]\n"
        "buffer_load_dwordx4 v130, s[20:23], s60 offen offset:2432 lds\n"
        "v_mfma_f32_16x16x32_bf16 v[196:199], a[12:15], a[236:239], v[196:199]\n"
        "v_accvgpr_write_b32 a37, v98\n"
        "v_cvt_pk_bf16_f32 v100, v100, v101\n"
        "v_mfma_f32_16x16x32_bf16 v[216:219], a[12:15], a[240:243], v[216:219]\n"
        "s_waitcnt vmcnt(9)\n"
        "ds_read_b128 a[212:215], v137 offset:6144\n"
        "ds_read_b128 a[208:211], v137 offset:6912\n"
        "v_mfma_f32_16x16x32_bf16 v[104:107], a[172:175], v[148:151], 0\n"
        "ds_read_b128 a[204:207], v137 offset:6176\n"
        "ds_read_b128 a[200:203], v137 offset:6944\n"
        "v_mfma_f32_16x16x32_bf16 v[108:111], a[168:171], v[148:151], 0\n"
        "ds_read_b128 a[196:199], v137 offset:6208\n"
        "ds_read_b128 a[192:195], v137 offset:6976\n"
        "v_mfma_f32_16x16x32_bf16 v[152:155], a[84:87], a[228:231], v[152:155]\n"
        "v_accvgpr_write_b32 a38, v100\n"
        "v_cvt_pk_bf16_f32 v102, v102, v103\n"
        "v_mfma_f32_16x16x32_bf16 v[172:175], a[84:87], a[232:235], v[172:175]\n"
        "s_mov_b32 m0, s48\n"
        "v_accvgpr_write_b32 a39, v102\n"
        "v_cvt_pk_bf16_f32 v104, v104, v105\n"
        "v_mfma_f32_16x16x32_bf16 v[192:195], a[84:87], a[236:239], v[192:195]\n"
        "s_sub_u32 s64, s36, 4194304\n"
        "v_accvgpr_write_b32 a60, v104\n"
        "v_cvt_pk_bf16_f32 v106, v106, v107\n"
        "v_mfma_f32_16x16x32_bf16 v[212:215], a[84:87], a[240:243], v[212:215]\n"
        "buffer_load_dwordx4 v249, s[12:15], s64 offen lds\n"
        "v_mfma_f32_16x16x32_bf16 v[224:227], a[36:39], a[240:243], v[224:227]\n"
        "v_accvgpr_write_b32 a61, v106\n"
        "v_cvt_pk_bf16_f32 v108, v108, v109\n"
        "v_mfma_f32_16x16x32_bf16 v[204:207], a[36:39], a[236:239], v[204:207]\n"
        "ds_read_b128 v[56:59], v142 offset:9216\n"
        "v_accvgpr_write_b32 a62, v108\n"
        "v_mfma_f32_16x16x32_bf16 v[184:187], a[36:39], a[232:235], v[184:187]\n"
        "s_waitcnt vmcnt(7)\n"
        "ds_read_b64_tr_b16 a[88:89], v255 offset:9216\n"
        "ds_read_b64_tr_b16 a[90:91], v255 offset:9984\n"
        "v_mfma_f32_16x16x32_bf16 v[164:167], a[36:39], a[228:231], v[164:167]\n"
        "s_waitcnt lgkmcnt(3)\n"
        "ds_read_b64_tr_b16 a[112:113], v255 offset:9248\n"
        "ds_read_b64_tr_b16 a[114:115], v255 offset:10016\n"
        "v_mfma_f32_16x16x32_bf16 v[112:115], a[212:215], v[148:151], 0\n"
        "buffer_load_dwordx4 v253, s[12:15], s64 offen offset:1024 lds\n"
        "ds_read_b64_tr_b16 a[136:137], v255 offset:9280\n"
        "v_mfma_f32_16x16x32_bf16 v[116:119], a[208:211], v[148:151], 0\n"
        "ds_read_b64_tr_b16 a[138:139], v255 offset:10048\n"
        "s_add_u32 m0, s65, 0\n"
        "v_cvt_pk_bf16_f32 v110, v110, v111\n"
        "v_mfma_f32_16x16x32_bf16 v[88:91], a[108:111], a[240:243], v[88:91]\n"
        "s_mov_b32 s64, 768\n"
        "v_accvgpr_write_b32 a63, v110\n"
        "v_cvt_pk_bf16_f32 v112, v112, v113\n"
        "v_mfma_f32_16x16x32_bf16 v[80:83], a[108:111], a[236:239], v[80:83]\n"
        "buffer_load_dwordx4 v128, s[20:23], s64 offen lds\n"
        "v_mfma_f32_16x16x32_bf16 v[68:71], a[108:111], a[232:235], v[68:71]\n"
        "v_accvgpr_write_b32 a16, v112\n"
        "v_cvt_pk_bf16_f32 v114, v114, v115\n"
        "v_mfma_f32_16x16x32_bf16 v[64:67], a[108:111], a[228:231], v[64:67]\n"
        "v_accvgpr_write_b32 a17, v114\n"
        "v_cvt_pk_bf16_f32 v116, v116, v117\n"
        "v_mfma_f32_16x16x32_bf16 v[160:163], a[60:63], a[228:231], v[160:163]\n"
        "v_accvgpr_write_b32 a18, v116\n"
        "v_cvt_pk_bf16_f32 v118, v118, v119\n"
        "v_mfma_f32_16x16x32_bf16 v[180:183], a[60:63], a[232:235], v[180:183]\n"
        "buffer_load_dwordx4 v129, s[20:23], s64 offen offset:1024 lds\n"
        "v_mfma_f32_16x16x32_bf16 v[200:203], a[60:63], a[236:239], v[200:203]\n"
        "ds_read_b128 a[224:227], v241 offset:256\n"
        "s_waitcnt lgkmcnt(1)\n"
        "v_lshl_add_u64 v[56:57], v[56:57], 2, s[38:39]\n"
        "v_mfma_f32_16x16x32_bf16 v[220:223], a[60:63], a[240:243], v[220:223]\n"
        "ds_read_b32 v56, v56\n"
        "ds_read_b32 v57, v57\n"
        "v_mfma_f32_16x16x32_bf16 v[120:123], a[204:207], v[148:151], 0\n"
        "v_lshl_add_u64 v[58:59], v[58:59], 2, s[38:39]\n"
        "v_accvgpr_write_b32 a19, v118\n"
        "v_mfma_f32_16x16x32_bf16 v[124:127], a[200:203], v[148:151], 0\n"
        "buffer_load_dwordx4 v130, s[20:23], s64 offen offset:2048 lds\n"
        "v_mfma_f32_16x16x32_bf16 v[72:75], a[132:135], a[228:231], v[72:75]\n"
        "s_add_u32 m0, s65, 2304\n"
        "ds_read_b32 v58, v58\n"
        "ds_read_b32 v59, v59\n"
        "v_mfma_f32_16x16x32_bf16 v[76:79], a[132:135], a[232:235], v[76:79]\n"
        "ds_read_b128 a[228:231], v241 offset:1792\n"
        "ds_read_b128 a[232:235], v241 offset:12544\n"
        "v_mfma_f32_16x16x32_bf16 v[84:87], a[132:135], a[236:239], v[84:87]\n"
        "ds_read_b128 a[236:239], v241 offset:14080\n"
        "v_cvt_pk_bf16_f32 v120, v120, v121\n"
        "v_mfma_f32_16x16x32_bf16 v[92:95], a[132:135], a[240:243], v[92:95]\n"
        "buffer_load_dwordx4 v128, s[20:23], s56 offen offset:768 lds\n"
        "v_mfma_f32_16x16x32_bf16 v[156:159], a[16:19], a[244:247], v[156:159]\n"
        "v_accvgpr_write_b32 a40, v120\n"
        "v_cvt_pk_bf16_f32 v122, v122, v123\n"
        "v_mfma_f32_16x16x32_bf16 v[176:179], a[16:19], a[248:251], v[176:179]\n"
        "v_accvgpr_write_b32 a41, v122\n"
        "v_cvt_pk_bf16_f32 v124, v124, v125\n"
        "v_mfma_f32_16x16x32_bf16 v[196:199], a[16:19], a[216:219], v[196:199]\n"
        "v_accvgpr_write_b32 a42, v124\n"
        "v_cvt_pk_bf16_f32 v126, v126, v127\n"
        "v_mfma_f32_16x16x32_bf16 v[216:219], a[16:19], a[220:223], v[216:219]\n"
        "buffer_load_dwordx4 v129, s[20:23], s56 offen offset:1792 lds\n"
        "v_mfma_f32_16x16x32_bf16 v[96:99], a[196:199], v[148:151], 0\n"
        "v_accvgpr_write_b32 a43, v126\n"
        "v_mfma_f32_16x16x32_bf16 v[100:103], a[192:195], v[148:151], 0\n"
        "s_waitcnt vmcnt(10)\n"
        "ds_read_b128 a[188:191], v137 offset:12288\n"
        "ds_read_b128 a[184:187], v137 offset:13056\n"
        "v_mfma_f32_16x16x32_bf16 v[152:155], a[88:91], a[244:247], v[152:155]\n"
        "ds_read_b128 a[180:183], v137 offset:12320\n"
        "ds_read_b128 a[176:179], v137 offset:13088\n"
        "v_mfma_f32_16x16x32_bf16 v[172:175], a[88:91], a[248:251], v[172:175]\n"
        "buffer_load_dwordx4 v130, s[20:23], s56 offen offset:2816 lds\n"
        "v_mfma_f32_16x16x32_bf16 v[192:195], a[88:91], a[216:219], v[192:195]\n"
        "s_add_u32 m0, s65, 5376\n"
        "ds_read_b128 a[172:175], v137 offset:12352\n"
        "ds_read_b128 a[168:171], v137 offset:13120\n"
        "v_mfma_f32_16x16x32_bf16 v[212:215], a[88:91], a[220:223], v[212:215]\n"
        "v_cvt_pk_bf16_f32 v96, v96, v97\n"
        "v_accvgpr_write_b32 a64, v96\n"
        "v_mfma_f32_16x16x32_bf16 v[224:227], a[40:43], a[220:223], v[224:227]\n"
        "v_cvt_pk_bf16_f32 v98, v98, v99\n"
        "v_accvgpr_write_b32 a65, v98\n"
        "v_mfma_f32_16x16x32_bf16 v[204:207], a[40:43], a[216:219], v[204:207]\n"
        "buffer_load_dwordx4 v128, s[20:23], s57 offen offset:768 lds\n"
        "v_mfma_f32_16x16x32_bf16 v[184:187], a[40:43], a[248:251], v[184:187]\n"
        "v_cvt_pk_bf16_f32 v100, v100, v101\n"
        "v_accvgpr_write_b32 a66, v100\n"
        "v_mfma_f32_16x16x32_bf16 v[164:167], a[40:43], a[244:247], v[164:167]\n"
        "s_waitcnt lgkmcnt(2)\n"
        "ds_read_b128 v[60:63], v143 offset:9216\n"
        "v_cvt_pk_bf16_f32 v102, v102, v103\n"
        "v_mfma_f32_16x16x32_bf16 v[104:107], a[188:191], v[148:151], 0\n"
        "v_accvgpr_write_b32 a67, v102\n"
        "v_mfma_f32_16x16x32_bf16 v[108:111], a[184:187], v[148:151], 0\n"
        "buffer_load_dwordx4 v129, s[20:23], s57 offen offset:1792 lds\n"
        "v_mfma_f32_16x16x32_bf16 v[88:91], a[112:115], a[220:223], v[88:91]\n"
        "v_mfma_f32_16x16x32_bf16 v[80:83], a[112:115], a[216:219], v[80:83]\n"
        "v_mfma_f32_16x16x32_bf16 v[68:71], a[112:115], a[248:251], v[68:71]\n"
        "v_mfma_f32_16x16x32_bf16 v[64:67], a[112:115], a[244:247], v[64:67]\n"
        "s_waitcnt vmcnt(10)\n"
        "ds_read_b64_tr_b16 a[92:93], v255 offset:15360\n"
        "ds_read_b64_tr_b16 a[94:95], v255 offset:16128\n"
        "v_mfma_f32_16x16x32_bf16 v[160:163], a[64:67], a[244:247], v[160:163]\n"
        "ds_read_b64_tr_b16 a[116:117], v255 offset:15392\n"
        "ds_read_b64_tr_b16 a[118:119], v255 offset:16160\n"
        "v_mfma_f32_16x16x32_bf16 v[180:183], a[64:67], a[248:251], v[180:183]\n"
        "ds_read_b64_tr_b16 a[140:141], v255 offset:15424\n"
        "ds_read_b64_tr_b16 a[142:143], v255 offset:16192\n"
        "v_mfma_f32_16x16x32_bf16 v[200:203], a[64:67], a[216:219], v[200:203]\n"
        "v_cvt_pk_bf16_f32 v104, v104, v105\n"
        "v_accvgpr_write_b32 a20, v104\n"
        "v_mfma_f32_16x16x32_bf16 v[220:223], a[64:67], a[220:223], v[220:223]\n"
        "v_cvt_pk_bf16_f32 v106, v106, v107\n"
        "v_accvgpr_write_b32 a21, v106\n"
        "v_mfma_f32_16x16x32_bf16 v[112:115], a[180:183], v[148:151], 0\n"
        "s_waitcnt lgkmcnt(6)\n"
        "v_lshl_add_u64 v[60:61], v[60:61], 2, s[38:39]\n"
        "v_lshl_add_u64 v[62:63], v[62:63], 2, s[38:39]\n"
        "v_mfma_f32_16x16x32_bf16 v[116:119], a[176:179], v[148:151], 0\n"
        "ds_read_b32 v60, v60\n"
        "ds_read_b32 v61, v61\n"
        "v_mfma_f32_16x16x32_bf16 v[72:75], a[136:139], a[244:247], v[72:75]\n"
        "ds_read_b32 v62, v62\n"
        "ds_read_b32 v63, v63\n"
        "v_mfma_f32_16x16x32_bf16 v[76:79], a[136:139], a[248:251], v[76:79]\n"
        "v_cvt_pk_bf16_f32 v108, v108, v109\n"
        "v_accvgpr_write_b32 a22, v108\n"
        "v_cvt_pk_bf16_f32 v110, v110, v111\n"
        "v_accvgpr_write_b32 a23, v110\n"
        "s_nop 1\n"
        "v_mfma_f32_16x16x32_bf16 v[156:159], a[20:23], a[224:227], v[156:159]\n"
        "v_cvt_pk_bf16_f32 v112, v112, v113\n"
        "v_accvgpr_write_b32 a44, v112\n"
        "v_mfma_f32_16x16x32_bf16 v[176:179], a[20:23], a[228:231], v[176:179]\n"
        "v_cvt_pk_bf16_f32 v114, v114, v115\n"
        "v_accvgpr_write_b32 a45, v114\n"
        "v_mfma_f32_16x16x32_bf16 v[196:199], a[20:23], a[232:235], v[196:199]\n"
        "v_cvt_pk_bf16_f32 v116, v116, v117\n"
        "v_accvgpr_write_b32 a46, v116\n"
        "v_cvt_pk_bf16_f32 v156, v156, v157\n"
        "v_mfma_f32_16x16x32_bf16 v[216:219], a[20:23], a[236:239], v[216:219]\n"
        "v_cvt_pk_bf16_f32 v157, v158, v159\n"
        "v_cvt_pk_bf16_f32 v176, v176, v177\n"
        "v_cvt_pk_bf16_f32 v177, v178, v179\n"
        "v_mfma_f32_16x16x32_bf16 v[120:123], a[172:175], v[148:151], 0\n"
        "v_cvt_pk_bf16_f32 v196, v196, v197\n"
        "v_cvt_pk_bf16_f32 v197, v198, v199\n"
        "v_mfma_f32_16x16x32_bf16 v[124:127], a[168:171], v[148:151], 0\n"
        "v_cvt_pk_bf16_f32 v118, v118, v119\n"
        "v_cvt_pk_bf16_f32 v216, v216, v217\n"
        "v_cvt_pk_bf16_f32 v217, v218, v219\n"
        "v_mfma_f32_16x16x32_bf16 v[84:87], a[136:139], a[216:219], v[84:87]\n"
        "v_accvgpr_write_b32 a47, v118\n"
        "v_cvt_pk_bf16_f32 v120, v120, v121\n"
        "v_mfma_f32_16x16x32_bf16 v[92:95], a[136:139], a[220:223], v[92:95]\n"
        "v_accvgpr_write_b32 a68, v120\n"
        "v_cvt_pk_bf16_f32 v122, v122, v123\n"
        "v_mfma_f32_16x16x32_bf16 v[224:227], a[44:47], a[236:239], v[224:227]\n"
        "v_accvgpr_write_b32 a69, v122\n"
        "v_cvt_pk_bf16_f32 v124, v124, v125\n"
        "v_mfma_f32_16x16x32_bf16 v[204:207], a[44:47], a[232:235], v[204:207]\n"
        "v_accvgpr_write_b32 a70, v124\n"
        "v_cvt_pk_bf16_f32 v126, v126, v127\n"
        "v_mfma_f32_16x16x32_bf16 v[184:187], a[44:47], a[228:231], v[184:187]\n"
        "ds_read_b128 a[240:243], v240 offset:0\n"
        "v_accvgpr_write_b32 a71, v126\n"
        "v_cvt_pk_bf16_f32 v218, v224, v225\n"
        "v_mfma_f32_16x16x32_bf16 v[164:167], a[44:47], a[224:227], v[164:167]\n"
        "s_waitcnt lgkmcnt(1)\n"
        "ds_read_b128 a[244:247], v241 offset:0\n"
        "v_cvt_pk_bf16_f32 v219, v226, v227\n"
        "v_cvt_pk_bf16_f32 v198, v204, v205\n"
        "v_mfma_f32_16x16x32_bf16 v[152:155], a[92:95], a[224:227], v[152:155]\n"
        "ds_read_b128 a[248:251], v240 offset:128\n"
        "v_cvt_pk_bf16_f32 v199, v206, v207\n"
        "v_cvt_pk_bf16_f32 v178, v184, v185\n"
        "v_mfma_f32_16x16x32_bf16 v[64:67], a[116:119], a[224:227], v[64:67]\n"
        "buffer_load_dwordx4 v130, s[20:23], s57 offen offset:2816 lds\n"
        "v_cvt_pk_bf16_f32 v179, v186, v187\n"
        "v_mfma_f32_16x16x32_bf16 v[72:75], a[140:143], a[224:227], v[72:75]\n"
        "s_add_u32 m0, s65, 8448\n"
        "v_cvt_pk_bf16_f32 v158, v164, v165\n"
        "v_cvt_pk_bf16_f32 v159, v166, v167\n"
        "v_mfma_f32_16x16x32_bf16 v[172:175], a[92:95], a[228:231], v[172:175]\n"
        "v_cvt_pk_bf16_f32 v152, v152, v153\n"
        "v_cvt_pk_bf16_f32 v153, v154, v155\n"
        "v_mfma_f32_16x16x32_bf16 v[160:163], a[68:71], a[224:227], v[160:163]\n"
        "v_cvt_pk_bf16_f32 v154, v64, v65\n"
        "v_cvt_pk_bf16_f32 v155, v66, v67\n"
        "v_mfma_f32_16x16x32_bf16 v[180:183], a[68:71], a[228:231], v[180:183]\n"
        "buffer_load_dwordx4 v128, s[20:23], s58 offen offset:768 lds\n"
        "v_mfma_f32_16x16x32_bf16 v[200:203], a[68:71], a[232:235], v[200:203]\n"
        "v_cvt_pk_bf16_f32 v168, v72, v73\n"
        "v_cvt_pk_bf16_f32 v169, v74, v75\n"
        "v_mfma_f32_16x16x32_bf16 v[220:223], a[68:71], a[236:239], v[220:223]\n"
        "v_cvt_pk_bf16_f32 v166, v160, v161\n"
        "v_cvt_pk_bf16_f32 v160, v160, v161\n"
        "v_cvt_pk_bf16_f32 v161, v162, v163\n"
        "v_mfma_f32_16x16x32_bf16 v[68:71], a[116:119], a[228:231], v[68:71]\n"
        "buffer_load_dwordx4 v129, s[20:23], s58 offen offset:1792 lds\n"
        "v_cvt_pk_bf16_f32 v167, v162, v163\n"
        "v_mfma_f32_16x16x32_bf16 v[76:79], a[140:143], a[228:231], v[76:79]\n"
        "s_waitcnt vmcnt(11)\n"
        "s_barrier\n"
        "ds_read_b128 v[162:165], v251 offset:0\n"
        "ds_read_b128 a[216:219], v240 offset:1536\n"
        "v_cvt_pk_bf16_f32 v186, v180, v181\n"
        "v_mfma_f32_16x16x32_bf16 v[192:195], a[92:95], a[232:235], v[192:195]\n"
        "ds_read_b128 a[220:223], v241 offset:1536\n"
        "v_cvt_pk_bf16_f32 v180, v180, v181\n"
        "v_cvt_pk_bf16_f32 v181, v182, v183\n"
        "v_mfma_f32_16x16x32_bf16 v[80:83], a[116:119], a[232:235], v[80:83]\n"
        "buffer_load_dwordx4 v130, s[20:23], s58 offen offset:2816 lds\n"
        "v_cvt_pk_bf16_f32 v187, v182, v183\n"
        "v_mfma_f32_16x16x32_bf16 v[84:87], a[140:143], a[232:235], v[84:87]\n"
        "ds_read_b128 v[182:185], v251 offset:512\n"
        "s_add_u32 m0, s65, 11520\n"
        "ds_read_b128 a[224:227], v240 offset:1664\n"
        "v_cvt_pk_bf16_f32 v206, v200, v201\n"
        "v_mfma_f32_16x16x32_bf16 v[212:215], a[92:95], a[236:239], v[212:215]\n"
        "v_cvt_pk_bf16_f32 v200, v200, v201\n"
        "v_cvt_pk_bf16_f32 v201, v202, v203\n"
        "v_cvt_pk_bf16_f32 v207, v202, v203\n"
        "v_cvt_pk_bf16_f32 v226, v220, v221\n"
        "v_cvt_pk_bf16_f32 v220, v220, v221\n"
        "v_cvt_pk_bf16_f32 v221, v222, v223\n"
        "v_mfma_f32_16x16x32_bf16 v[88:91], a[116:119], a[236:239], v[88:91]\n"
        "buffer_load_dwordx4 v128, s[20:23], s59 offen offset:768 lds\n"
        "v_cvt_pk_bf16_f32 v227, v222, v223\n"
        "v_cvt_pk_bf16_f32 v172, v172, v173\n"
        "v_cvt_pk_bf16_f32 v173, v174, v175\n"
        "v_mfma_f32_16x16x32_bf16 v[92:95], a[140:143], a[236:239], v[92:95]\n"
        "s_waitcnt vmcnt(10)\n"
        "ds_read_b64_tr_b16 a[144:145], v255 offset:0\n"
        "ds_read_b64_tr_b16 a[146:147], v255 offset:768\n"
        "v_cvt_pk_bf16_f32 v174, v68, v69\n"
        "v_cvt_pk_bf16_f32 v175, v70, v71\n"
        "v_mfma_f32_16x16x32_bf16 v[64:67], v[152:155], v[156:159], v[0:3]\n"
        "s_waitcnt lgkmcnt(4)\n"
        "ds_read_b64_tr_b16 a[168:169], v255 offset:32\n"
        "ds_read_b64_tr_b16 a[170:171], v255 offset:800\n"
        "v_cvt_pk_bf16_f32 v190, v76, v77\n"
        "v_cvt_pk_bf16_f32 v191, v78, v79\n"
        "v_mfma_f32_16x16x32_bf16 v[64:67], v[168:171], v[160:163], v[64:67]\n"
        "ds_read_b64_tr_b16 a[192:193], v255 offset:64\n"
        "ds_read_b64_tr_b16 a[194:195], v255 offset:832\n"
        "v_cvt_pk_bf16_f32 v192, v192, v193\n"
        "v_cvt_pk_bf16_f32 v193, v194, v195\n"
        "v_mfma_f32_16x16x32_bf16 v[96:99], v[172:175], v[156:159], v[4:7]\n"
        "buffer_load_dwordx4 v129, s[20:23], s59 offen offset:1792 lds\n"
        "v_cvt_pk_bf16_f32 v194, v80, v81\n"
        "v_cvt_pk_bf16_f32 v195, v82, v83\n"
        "v_exp_f32 v64, v64\n"
        "v_mfma_f32_16x16x32_bf16 v[96:99], v[188:191], v[164:167], v[96:99]\n"
        "ds_read_b128 v[162:165], v252 offset:0\n"
        "ds_read_b128 v[202:205], v251 offset:4096\n"
        "v_exp_f32 v65, v65\n"
        "v_exp_f32 v66, v66\n"
        "v_mfma_f32_16x16x32_bf16 v[72:75], v[152:155], v[176:179], v[16:19]\n"
        "s_waitcnt lgkmcnt(8)\n"
        "buffer_load_dwordx4 v130, s[20:23], s59 offen offset:2816 lds\n"
        "v_cvt_pk_bf16_f32 v64, v64, v65\n"
        "v_cvt_pk_bf16_f32 v208, v84, v85\n"
        "v_exp_f32 v96, v96\n"
        "v_mfma_f32_16x16x32_bf16 v[72:75], v[168:171], v[180:183], v[72:75]\n"
        "s_add_u32 m0, s65, 14592\n"
        "s_waitcnt vmcnt(9)\n"
        "ds_read_b64_tr_b16 a[148:149], v255 offset:3072\n"
        "ds_read_b64_tr_b16 a[150:151], v255 offset:3840\n"
        "v_cvt_pk_bf16_f32 v209, v86, v87\n"
        "v_exp_f32 v67, v67\n"
        "v_cvt_pk_bf16_f32 v212, v212, v213\n"
        "v_mfma_f32_16x16x32_bf16 v[100:103], v[172:175], v[176:179], v[20:23]\n"
        "buffer_load_dwordx4 v128, s[20:23], s60 offen offset:768 lds\n"
        "ds_read_b64_tr_b16 a[172:173], v255 offset:3104\n"
        "ds_read_b64_tr_b16 a[174:175], v255 offset:3872\n"
        "v_exp_f32 v97, v97\n"
        "v_mfma_f32_16x16x32_bf16 v[100:103], v[188:191], v[184:187], v[100:103]\n"
        "ds_read_b128 v[182:185], v252 offset:512\n"
        "ds_read_b64_tr_b16 a[196:197], v255 offset:3136\n"
        "ds_read_b64_tr_b16 a[198:199], v255 offset:3904\n"
        "v_cvt_pk_bf16_f32 v213, v214, v215\n"
        "v_cvt_pk_bf16_f32 v65, v66, v67\n"
        "v_cvt_pk_bf16_f32 v66, v96, v97\n"
        "v_mfma_f32_16x16x32_bf16 v[68:71], v[192:195], v[156:159], v[8:11]\n"
        "s_waitcnt lgkmcnt(8)\n"
        "ds_read_b128 a[228:231], v240 offset:12288\n"
        "ds_read_b128 a[232:235], v241 offset:12288\n"
        "v_exp_f32 v98, v98\n"
        "v_exp_f32 v72, v72\n"
        "v_mfma_f32_16x16x32_bf16 v[68:71], v[208:211], v[160:163], v[68:71]\n"
        "buffer_load_dwordx4 v129, s[20:23], s60 offen offset:1792 lds\n"
        "v_cvt_pk_bf16_f32 v214, v88, v89\n"
        "v_cvt_pk_bf16_f32 v215, v90, v91\n"
        "v_exp_f32 v99, v99\n"
        "v_mfma_f32_16x16x32_bf16 v[76:79], v[192:195], v[176:179], v[24:27]\n"
        "s_waitcnt lgkmcnt(4)\n"
        "s_waitcnt vmcnt(8)\n"
        "ds_read_b64_tr_b16 a[152:153], v255 offset:6144\n"
        "ds_read_b64_tr_b16 a[154:155], v255 offset:6912\n"
        "v_exp_f32 v73, v73\n"
        "v_cvt_pk_bf16_f32 v230, v92, v93\n"
        "v_cvt_pk_bf16_f32 v231, v94, v95\n"
        "v_mfma_f32_16x16x32_bf16 v[76:79], v[208:211], v[180:183], v[76:79]\n"
        "buffer_load_dwordx4 v130, s[20:23], s60 offen offset:2816 lds\n"
        "ds_read_b64_tr_b16 a[176:177], v255 offset:6176\n"
        "ds_read_b64_tr_b16 a[178:179], v255 offset:6944\n"
        "v_cvt_pk_bf16_f32 v67, v98, v99\n"
        "v_cvt_pk_bf16_f32 v72, v72, v73\n"
        "v_mfma_f32_16x16x32_bf16 v[104:107], v[212:215], v[156:159], v[12:15]\n"
        "s_add_u32 m0, s40, 98304\n"
        "ds_read_b64_tr_b16 a[200:201], v255 offset:6208\n"
        "ds_read_b64_tr_b16 a[202:203], v255 offset:6976\n"
        "v_mfma_f32_16x16x32_bf16 v[104:107], v[228:231], v[164:167], v[104:107]\n"
        "s_sub_u32 s64, s47, 6291456\n"
        "ds_read_b128 a[236:239], v240 offset:12416\n"
        "v_exp_f32 v74, v74\n"
        "v_mfma_f32_16x16x32_bf16 v[108:111], v[212:215], v[176:179], v[28:31]\n"
        "buffer_load_dwordx4 v242, s[8:11], s64 offen lds\n"
        "v_mfma_f32_16x16x32_bf16 v[108:111], v[228:231], v[184:187], v[108:111]\n"
        "s_add_u32 m0, s40, 99328\n"
        "v_exp_f32 v100, v100\n"
        "v_mfma_f32_16x16x32_bf16 v[80:83], v[152:155], v[196:199], v[32:35]\n"
        "v_exp_f32 v75, v75\n"
        "v_mfma_f32_16x16x32_bf16 v[80:83], v[168:171], v[200:203], v[80:83]\n"
        "buffer_load_dwordx4 v243, s[8:11], s64 offen lds\n"
        "v_cvt_pk_bf16_f32 v73, v74, v75\n"
        "v_mfma_f32_16x16x32_bf16 v[112:115], a[240:243], a[144:147], 0\n"
        "s_add_u32 m0, s40, 100352\n"
        "v_exp_f32 v101, v101\n"
        "v_mfma_f32_16x16x32_bf16 v[112:115], a[244:247], a[148:151], v[112:115]\n"
        "s_waitcnt lgkmcnt(0)\n"
        "v_exp_f32 v102, v102\n"
        "v_cvt_pk_bf16_f32 v74, v100, v101\n"
        "v_mfma_f32_16x16x32_bf16 v[112:115], a[248:251], a[152:155], v[112:115]\n"
        "buffer_load_dwordx4 v244, s[8:11], s64 offen lds\n"
        "v_mfma_f32_16x16x32_bf16 v[116:119], a[248:251], a[176:179], 0\n"
        "s_add_u32 m0, s40, 110592\n"
        "v_exp_f32 v68, v68\n"
        "v_mfma_f32_16x16x32_bf16 v[116:119], a[244:247], a[172:175], v[116:119]\n"
        "s_sub_u32 s64, s47, 5505024\n"
        "v_exp_f32 v103, v103\n"
        "v_mfma_f32_16x16x32_bf16 v[116:119], a[240:243], a[168:171], v[116:119]\n"
        ".long 0xbec200ff\n"
        "42:\n"
        "s_branch 60b\n"
        "v_exp_f32 v69, v69\n"
        "v_cvt_pk_bf16_f32 v75, v102, v103\n"
        "v_mfma_f32_16x16x32_bf16 v[96:99], v[172:175], v[196:199], v[36:39]\n"
        "buffer_load_dwordx4 v242, s[8:11], s64 offen lds\n"
        "s_waitcnt vmcnt(10)\n"
        "ds_read_b64_tr_b16 a[156:157], v255 offset:9216\n"
        "v_mfma_f32_16x16x32_bf16 v[96:99], v[188:191], v[204:207], v[96:99]\n"
        "s_add_u32 m0, s40, 111616\n"
        "ds_read_b64_tr_b16 a[158:159], v255 offset:9984\n"
        "ds_read_b64_tr_b16 a[180:181], v255 offset:9248\n"
        "ds_read_b64_tr_b16 a[182:183], v255 offset:10016\n"
        "v_mfma_f32_16x16x32_bf16 v[120:123], a[240:243], a[192:195], 0\n"
        "ds_read_b128 v[202:205], v252 offset:4096\n"
        "ds_read_b64_tr_b16 a[204:205], v255 offset:9280\n"
        "ds_read_b64_tr_b16 a[206:207], v255 offset:10048\n"
        "v_mfma_f32_16x16x32_bf16 v[120:123], a[244:247], a[196:199], v[120:123]\n"
        "buffer_load_dwordx4 v243, s[8:11], s64 offen lds\n"
        "v_cvt_pk_bf16_f32 v68, v68, v69\n"
        "v_mfma_f32_16x16x32_bf16 v[120:123], a[248:251], a[200:203], v[120:123]\n"
        "s_add_u32 m0, s40, 112640\n"
        "ds_read_b128 a[240:243], v240 offset:13824\n"
        "ds_read_b128 a[244:247], v241 offset:13824\n"
        "v_mfma_f32_16x16x32_bf16 v[124:127], a[216:219], a[144:147], 0\n"
        "buffer_load_dwordx4 v244, s[8:11], s64 offen lds\n"
        "v_mfma_f32_16x16x32_bf16 v[124:127], a[220:223], a[148:151], v[124:127]\n"
        "s_xor_b32 m0, s48, 8192\n"
        "s_waitcnt vmcnt(9)\n"
        "ds_read_b64_tr_b16 a[160:161], v255 offset:12288\n"
        "ds_read_b64_tr_b16 a[162:163], v255 offset:13056\n"
        "v_mfma_f32_16x16x32_bf16 v[124:127], a[224:227], a[152:155], v[124:127]\n"
        "s_sub_u32 s64, s36, 2097152\n"
        "ds_read_b64_tr_b16 a[184:185], v255 offset:12320\n"
        "ds_read_b64_tr_b16 a[186:187], v255 offset:13088\n"
        "v_mfma_f32_16x16x32_bf16 v[156:159], a[224:227], a[176:179], 0\n"
        "buffer_load_dwordx4 v249, s[12:15], s64 offen lds\n"
        "ds_read_b64_tr_b16 a[208:209], v255 offset:12352\n"
        "v_mfma_f32_16x16x32_bf16 v[156:159], a[220:223], a[172:175], v[156:159]\n"
        "ds_read_b64_tr_b16 a[210:211], v255 offset:13120\n"
        "ds_read_b128 a[248:251], v240 offset:13952\n"
        "v_mfma_f32_16x16x32_bf16 v[156:159], a[216:219], a[168:171], v[156:159]\n"
        "ds_read_b128 v[222:225], v251 offset:4608\n"
        "v_exp_f32 v70, v70\n"
        "v_mfma_f32_16x16x32_bf16 v[84:87], v[192:195], v[196:199], v[40:43]\n"
        "s_waitcnt lgkmcnt(8)\n"
        "v_exp_f32 v76, v76\n"
        "v_mfma_f32_16x16x32_bf16 v[84:87], v[208:211], v[200:203], v[84:87]\n"
        "buffer_load_dwordx4 v253, s[12:15], s64 offen offset:1024 lds\n"
        "v_mfma_f32_16x16x32_bf16 v[160:163], a[216:219], a[192:195], 0\n"
        "s_add_u32 m0, s65, 12288\n"
        "ds_read_b128 a[216:219], v241 offset:128\n"
        "v_exp_f32 v71, v71\n"
        "v_mfma_f32_16x16x32_bf16 v[160:163], a[220:223], a[196:199], v[160:163]\n"
        "ds_read_b128 a[220:223], v240 offset:256\n"
        "v_exp_f32 v77, v77\n"
        "v_mfma_f32_16x16x32_bf16 v[160:163], a[224:227], a[200:203], v[160:163]\n"
        "ds_read_b128 a[224:227], v241 offset:256\n"
        "v_cvt_pk_bf16_f32 v69, v70, v71\n"
        "v_cvt_pk_bf16_f32 v76, v76, v77\n"
        "v_mfma_f32_16x16x32_bf16 v[164:167], a[228:231], a[144:147], 0\n"
        "s_waitcnt lgkmcnt(3)\n"
        "buffer_load_dword v136, s[52:55], 0 offen lds\n"
        "v_mfma_f32_16x16x32_bf16 v[164:167], a[232:235], a[148:151], v[164:167]\n"
        "v_exp_f32 v78, v78\n"
        "v_mfma_f32_16x16x32_bf16 v[164:167], a[236:239], a[152:155], v[164:167]\n"
        "v_exp_f32 v104, v104\n"
        "v_mfma_f32_16x16x32_bf16 v[100:103], v[212:215], v[196:199], v[44:47]\n"
        "v_exp_f32 v79, v79\n"
        "v_mfma_f32_16x16x32_bf16 v[100:103], v[228:231], v[204:207], v[100:103]\n"
        "v_exp_f32 v105, v105\n"
        "v_cvt_pk_bf16_f32 v77, v78, v79\n"
        "v_mfma_f32_16x16x32_bf16 v[176:179], a[236:239], a[176:179], 0\n"
        "v_exp_f32 v106, v106\n"
        "v_cvt_pk_bf16_f32 v70, v104, v105\n"
        "v_mfma_f32_16x16x32_bf16 v[176:179], a[232:235], a[172:175], v[176:179]\n"
        "v_exp_f32 v108, v108\n"
        "v_mfma_f32_16x16x32_bf16 v[176:179], a[228:231], a[168:171], v[176:179]\n"
        "s_waitcnt vmcnt(9)\n"
        "ds_read_b64_tr_b16 a[164:165], v255 offset:15360\n"
        "ds_read_b64_tr_b16 a[166:167], v255 offset:16128\n"
        "v_mfma_f32_16x16x32_bf16 v[180:183], a[228:231], a[192:195], 0\n"
        "ds_read_b64_tr_b16 a[188:189], v255 offset:15392\n"
        "ds_read_b64_tr_b16 a[190:191], v255 offset:16160\n"
        "v_mfma_f32_16x16x32_bf16 v[180:183], a[232:235], a[196:199], v[180:183]\n"
        "ds_read_b64_tr_b16 a[212:213], v255 offset:15424\n"
        "ds_read_b64_tr_b16 a[214:215], v255 offset:16192\n"
        "v_mfma_f32_16x16x32_bf16 v[180:183], a[236:239], a[200:203], v[180:183]\n"
        "ds_read_b128 a[228:231], v241 offset:1664\n"
        "ds_read_b128 a[232:235], v240 offset:1792\n"
        "v_mfma_f32_16x16x32_bf16 v[184:187], a[240:243], a[144:147], 0\n"
        "ds_read_b128 a[236:239], v241 offset:1792\n"
        "v_exp_f32 v107, v107\n"
        "v_mfma_f32_16x16x32_bf16 v[184:187], a[244:247], a[148:151], v[184:187]\n"
        "v_exp_f32 v109, v109\n"
        "v_cvt_pk_bf16_f32 v71, v106, v107\n"
        "v_mfma_f32_16x16x32_bf16 v[184:187], a[248:251], a[152:155], v[184:187]\n"
        "v_exp_f32 v110, v110\n"
        "v_cvt_pk_bf16_f32 v78, v108, v109\n"
        "v_mfma_f32_16x16x32_bf16 v[196:199], a[248:251], a[176:179], 0\n"
        "v_exp_f32 v111, v111\n"
        "v_mfma_f32_16x16x32_bf16 v[196:199], a[244:247], a[172:175], v[196:199]\n"
        "v_cvt_pk_bf16_f32 v79, v110, v111\n"
        "v_exp_f32 v80, v80\n"
        "v_mfma_f32_16x16x32_bf16 v[196:199], a[240:243], a[168:171], v[196:199]\n"
        "v_exp_f32 v81, v81\n"
        "v_mfma_f32_16x16x32_bf16 v[200:203], a[240:243], a[192:195], 0\n"
        "ds_read_b128 a[240:243], v241 offset:12416\n"
        "v_exp_f32 v82, v82\n"
        "v_mfma_f32_16x16x32_bf16 v[200:203], a[244:247], a[196:199], v[200:203]\n"
        "ds_read_b128 a[244:247], v240 offset:12544\n"
        "v_cvt_pk_bf16_f32 v80, v80, v81\n"
        "v_mfma_f32_16x16x32_bf16 v[200:203], a[248:251], a[200:203], v[200:203]\n"
        "ds_read_b128 a[248:251], v241 offset:12544\n"
        "v_exp_f32 v83, v83\n"
        "v_mfma_f32_16x16x32_bf16 v[88:91], v[152:155], v[216:219], v[48:51]\n"
        "v_cvt_pk_bf16_f32 v81, v82, v83\n"
        "v_exp_f32 v96, v96\n"
        "v_mfma_f32_16x16x32_bf16 v[88:91], v[168:171], v[220:223], v[88:91]\n"
        "s_waitcnt lgkmcnt(3)\n"
        "v_exp_f32 v97, v97\n"
        "v_mfma_f32_16x16x32_bf16 v[112:115], a[216:219], a[156:159], v[112:115]\n"
        "v_exp_f32 v98, v98\n"
        "v_cvt_pk_bf16_f32 v82, v96, v97\n"
        "v_mfma_f32_16x16x32_bf16 v[112:115], a[220:223], a[160:163], v[112:115]\n"
        "v_exp_f32 v99, v99\n"
        "v_mfma_f32_16x16x32_bf16 v[112:115], a[224:227], a[164:167], v[112:115]\n"
        "v_cvt_pk_bf16_f32 v83, v98, v99\n"
        "v_exp_f32 v84, v84\n"
        "v_mfma_f32_16x16x32_bf16 v[116:119], a[224:227], a[188:191], v[116:119]\n"
        "v_exp_f32 v85, v85\n"
        "v_mfma_f32_16x16x32_bf16 v[116:119], a[220:223], a[184:187], v[116:119]\n"
        "v_exp_f32 v86, v86\n"
        "v_cvt_pk_bf16_f32 v84, v84, v85\n"
        "v_mfma_f32_16x16x32_bf16 v[116:119], a[216:219], a[180:183], v[116:119]\n"
        "v_exp_f32 v87, v87\n"
        "v_mfma_f32_16x16x32_bf16 v[120:123], a[216:219], a[204:207], v[120:123]\n"
        "ds_read_b128 a[216:219], v241 offset:13952\n"
        "v_cvt_pk_bf16_f32 v85, v86, v87\n"
        "v_mfma_f32_16x16x32_bf16 v[120:123], a[220:223], a[208:211], v[120:123]\n"
        "ds_read_b128 a[220:223], v240 offset:14080\n"
        "v_exp_f32 v100, v100\n"
        "v_mfma_f32_16x16x32_bf16 v[120:123], a[224:227], a[212:215], v[120:123]\n"
        "ds_read_b128 a[224:227], v241 offset:14080\n"
        "v_exp_f32 v101, v101\n"
        "v_mfma_f32_16x16x32_bf16 v[124:127], a[228:231], a[156:159], v[124:127]\n"
        "v_exp_f32 v102, v102\n"
        "v_cvt_pk_bf16_f32 v86, v100, v101\n"
        "v_mfma_f32_16x16x32_bf16 v[124:127], a[232:235], a[160:163], v[124:127]\n"
        "v_exp_f32 v103, v103\n"
        "v_cvt_pk_bf16_f32 v128, v112, v113\n"
        "v_mfma_f32_16x16x32_bf16 v[124:127], a[236:239], a[164:167], v[124:127]\n"
        "v_cvt_pk_bf16_f32 v87, v102, v103\n"
        "v_cvt_pk_bf16_f32 v129, v114, v115\n"
        "v_mfma_f32_16x16x32_bf16 v[104:107], v[172:175], v[216:219], v[52:55]\n"
        "v_cvt_pk_bf16_f32 v136, v116, v117\n"
        "v_cvt_pk_bf16_f32 v137, v118, v119\n"
        "v_mfma_f32_16x16x32_bf16 v[104:107], v[188:191], v[224:227], v[104:107]\n"
        "ds_read_b128 v[222:225], v252 offset:4608\n"
        "v_cvt_pk_bf16_f32 v144, v120, v121\n"
        "v_mfma_f32_16x16x32_bf16 v[156:159], a[236:239], a[188:191], v[156:159]\n"
        "v_cvt_pk_bf16_f32 v145, v122, v123\n"
        "v_cvt_pk_bf16_f32 v130, v124, v125\n"
        "v_mfma_f32_16x16x32_bf16 v[156:159], a[232:235], a[184:187], v[156:159]\n"
        "v_cvt_pk_bf16_f32 v131, v126, v127\n"
        "v_mfma_f32_16x16x32_bf16 v[156:159], a[228:231], a[180:183], v[156:159]\n"
        "v_exp_f32 v88, v88\n"
        "v_mfma_f32_16x16x32_bf16 v[160:163], a[228:231], a[204:207], v[160:163]\n"
        "v_exp_f32 v89, v89\n"
        "v_mfma_f32_16x16x32_bf16 v[160:163], a[232:235], a[208:211], v[160:163]\n"
        "v_exp_f32 v90, v90\n"
        "v_mfma_f32_16x16x32_bf16 v[160:163], a[236:239], a[212:215], v[160:163]\n"
        "s_waitcnt lgkmcnt(0)\n"
        "v_exp_f32 v91, v91\n"
        "v_mfma_f32_16x16x32_bf16 v[164:167], a[240:243], a[156:159], v[164:167]\n"
        "v_cvt_pk_bf16_f32 v138, v156, v157\n"
        "v_cvt_pk_bf16_f32 v139, v158, v159\n"
        "v_mfma_f32_16x16x32_bf16 v[164:167], a[244:247], a[160:163], v[164:167]\n"
        "v_cvt_pk_bf16_f32 v88, v88, v89\n"
        "v_cvt_pk_bf16_f32 v89, v90, v91\n"
        "v_mfma_f32_16x16x32_bf16 v[164:167], a[248:251], a[164:167], v[164:167]\n"
        "v_exp_f32 v104, v104\n"
        "v_mfma_f32_16x16x32_bf16 v[92:95], v[192:195], v[216:219], v[56:59]\n"
        "v_cvt_pk_bf16_f32 v146, v160, v161\n"
        "v_cvt_pk_bf16_f32 v147, v162, v163\n"
        "v_mfma_f32_16x16x32_bf16 v[92:95], v[208:211], v[220:223], v[92:95]\n"
        "v_exp_f32 v105, v105\n"
        "v_mfma_f32_16x16x32_bf16 v[176:179], a[248:251], a[188:191], v[176:179]\n"
        "v_exp_f32 v106, v106\n"
        "v_mfma_f32_16x16x32_bf16 v[176:179], a[244:247], a[184:187], v[176:179]\n"
        "v_cvt_pk_bf16_f32 v132, v164, v165\n"
        "v_cvt_pk_bf16_f32 v133, v166, v167\n"
        "v_mfma_f32_16x16x32_bf16 v[176:179], a[240:243], a[180:183], v[176:179]\n"
        "v_exp_f32 v107, v107\n"
        "v_mfma_f32_16x16x32_bf16 v[180:183], a[240:243], a[204:207], v[180:183]\n"
        "v_exp_f32 v92, v92\n"
        "v_mfma_f32_16x16x32_bf16 v[180:183], a[244:247], a[208:211], v[180:183]\n"
        "v_exp_f32 v93, v93\n"
        "v_mfma_f32_16x16x32_bf16 v[180:183], a[248:251], a[212:215], v[180:183]\n"
        "v_mov_b64 v[96:97], 0\n"
        "v_cvt_pk_bf16_f32 v90, v104, v105\n"
        "v_mfma_f32_16x16x32_bf16 v[184:187], a[216:219], a[156:159], v[184:187]\n"
        "v_cvt_pk_bf16_f32 v140, v176, v177\n"
        "v_cvt_pk_bf16_f32 v141, v178, v179\n"
        "v_mfma_f32_16x16x32_bf16 v[184:187], a[220:223], a[160:163], v[184:187]\n"
        "v_exp_f32 v94, v94\n"
        "v_mfma_f32_16x16x32_bf16 v[184:187], a[224:227], a[164:167], v[184:187]\n"
        "v_exp_f32 v95, v95\n"
        "v_mfma_f32_16x16x32_bf16 v[108:111], v[212:215], v[216:219], v[60:63]\n"
        "v_cvt_pk_bf16_f32 v148, v180, v181\n"
        "v_cvt_pk_bf16_f32 v149, v182, v183\n"
        "v_mfma_f32_16x16x32_bf16 v[108:111], v[228:231], v[224:227], v[108:111]\n"
        "v_cvt_pk_bf16_f32 v91, v106, v107\n"
        "v_add_u32 v240, 24576, v240\n"
        "v_mfma_f32_16x16x32_bf16 v[196:199], a[224:227], a[188:191], v[196:199]\n"
        "v_cvt_pk_bf16_f32 v134, v184, v185\n"
        "v_cvt_pk_bf16_f32 v135, v186, v187\n"
        "v_mfma_f32_16x16x32_bf16 v[196:199], a[220:223], a[184:187], v[196:199]\n"
        "v_mov_b64 v[172:173], 0\n"
        "v_add_u32 v241, 24576, v241\n"
        "v_mfma_f32_16x16x32_bf16 v[196:199], a[216:219], a[180:183], v[196:199]\n"
        "v_exp_f32 v108, v108\n"
        "v_mfma_f32_16x16x32_bf16 v[200:203], a[216:219], a[204:207], v[200:203]\n"
        "v_exp_f32 v109, v109\n"
        "v_mfma_f32_16x16x32_bf16 v[200:203], a[220:223], a[208:211], v[200:203]\n"
        "v_exp_f32 v110, v110\n"
        "v_mfma_f32_16x16x32_bf16 v[200:203], a[224:227], a[212:215], v[200:203]\n"
        "v_exp_f32 v111, v111\n"
        "v_smfmac_f32_16x16x64_bf16 v[96:99], a[252:255], v[64:71], v237\n"
        "v_cvt_pk_bf16_f32 v142, v196, v197\n"
        "v_mfma_f32_16x16x32_bf16 v[100:103], v[132:135], v[68:71], 0\n"
        "v_cvt_pk_bf16_f32 v143, v198, v199\n"
        "v_cvt_pk_bf16_f32 v92, v92, v93\n"
        "v_mfma_f32_16x16x32_bf16 v[100:103], v[128:131], v[64:67], v[100:103]\n"
        "v_mov_b64 v[192:193], 0\n"
        "v_cvt_pk_bf16_f32 v150, v200, v201\n"
        "v_mfma_f32_16x16x32_bf16 v[104:107], v[136:139], v[64:67], 0\n"
        "v_cvt_pk_bf16_f32 v151, v202, v203\n"
        "v_cvt_pk_bf16_f32 v93, v94, v95\n"
        "v_mfma_f32_16x16x32_bf16 v[104:107], v[140:143], v[68:71], v[104:107]\n"
        "v_add_f32 v97, v96, v97\n"
        "v_cvt_pk_bf16_f32 v94, v108, v109\n"
        "v_cvt_pk_bf16_f32 v95, v110, v111\n"
        "v_mfma_f32_16x16x32_bf16 v[108:111], v[148:151], v[68:71], 0\n"
        "v_rcp_f32 v96, v97\n"
        "v_mfma_f32_16x16x32_bf16 v[108:111], v[144:147], v[64:67], v[108:111]\n"
        "v_mul_f32 v100, v96, v100\n"
        "v_mul_f32 v101, v96, v101\n"
        "v_smfmac_f32_16x16x64_bf16 v[172:175], a[252:255], v[72:79], v237\n"
        "s_waitcnt vmcnt(6)\n"
        "s_barrier\n"
        "ds_read_b128 a[236:239], v240 offset:0\n"
        "v_mfma_f32_16x16x32_bf16 v[176:179], v[132:135], v[76:79], 0\n"
        "ds_read_b128 a[240:243], v241 offset:0\n"
        "ds_read_b128 a[244:247], v240 offset:128\n"
        "v_mfma_f32_16x16x32_bf16 v[176:179], v[128:131], v[72:75], v[176:179]\n"
        "ds_read_b128 a[248:251], v241 offset:128\n"
        "ds_read_b128 a[216:219], v240 offset:256\n"
        "v_mfma_f32_16x16x32_bf16 v[180:183], v[136:139], v[72:75], 0\n"
        "ds_read_b128 a[220:223], v241 offset:256\n"
        "v_mul_f32 v102, v96, v102\n"
        "v_mfma_f32_16x16x32_bf16 v[180:183], v[140:143], v[76:79], v[180:183]\n"
        "v_mul_f32 v103, v96, v103\n"
        "v_cvt_pk_bf16_f32 v100, v100, v101\n"
        "v_mfma_f32_16x16x32_bf16 v[184:187], v[148:151], v[76:79], 0\n"
        "v_cvt_pk_bf16_f32 v101, v102, v103\n"
        "v_mul_f32 v104, v96, v104\n"
        "v_mfma_f32_16x16x32_bf16 v[184:187], v[144:147], v[72:75], v[184:187]\n"
        "ds_write2st64_b32 v245, v100, v101 offset0:0 offset1:4\n"
        "v_smfmac_f32_16x16x64_bf16 v[192:195], a[252:255], v[80:87], v237\n"
        "v_mul_f32 v105, v96, v105\n"
        "v_mfma_f32_16x16x32_bf16 v[196:199], v[132:135], v[84:87], 0\n"
        "v_mul_f32 v106, v96, v106\n"
        "v_mul_f32 v107, v96, v107\n"
        "v_mfma_f32_16x16x32_bf16 v[196:199], v[128:131], v[80:83], v[196:199]\n"
        "buffer_load_dwordx4 v232, s[52:55], s34 offen lds\n"
        "v_mfma_f32_16x16x32_bf16 v[200:203], v[136:139], v[80:83], 0\n"
        "v_cvt_pk_bf16_f32 v102, v104, v105\n"
        "v_cvt_pk_bf16_f32 v103, v106, v107\n"
        "v_mov_b64 v[124:125], 0\n"
        "v_mfma_f32_16x16x32_bf16 v[200:203], v[140:143], v[84:87], v[200:203]\n"
        "ds_write2st64_b32 v245, v102, v103 offset0:8 offset1:12\n"
        "v_mfma_f32_16x16x32_bf16 v[204:207], v[148:151], v[84:87], 0\n"
        "v_mul_f32 v108, v96, v108\n"
        "v_mul_f32 v109, v96, v109\n"
        "v_mul_f32 v110, v96, v110\n"
        "v_mfma_f32_16x16x32_bf16 v[204:207], v[144:147], v[80:83], v[204:207]\n"
        "s_waitcnt lgkmcnt(2)\n"
        "v_mul_f32 v111, v96, v111\n"
        "v_cvt_pk_bf16_f32 v108, v108, v109\n"
        "v_cvt_pk_bf16_f32 v109, v110, v111\n"
        "v_mfma_f32_16x16x32_bf16 v[112:115], a[72:75], a[236:239], 0\n"
        "v_max_u32 v236, v96, v97\n"
        "v_add_f32 v173, v172, v173\n"
        "v_mfma_f32_16x16x32_bf16 v[112:115], a[76:79], a[240:243], v[112:115]\n"
        "v_rcp_f32 v172, v173\n"
        "v_mfma_f32_16x16x32_bf16 v[112:115], a[80:83], a[244:247], v[112:115]\n"
        "ds_write2st64_b32 v245, v108, v109 offset0:16 offset1:20\n"
        "v_mfma_f32_16x16x32_bf16 v[112:115], a[84:87], a[248:251], v[112:115]\n"
        "buffer_load_dwordx4 v233, s[52:55], s34 offen offset:1024 lds\n"
        "v_mfma_f32_16x16x32_bf16 v[112:115], a[88:91], a[216:219], v[112:115]\n"
        "v_mul_f32 v176, v172, v176\n"
        "v_mul_f32 v177, v172, v177\n"
        "v_mfma_f32_16x16x32_bf16 v[112:115], a[92:95], a[220:223], v[112:115]\n"
        "v_mul_f32 v178, v172, v178\n"
        "v_mul_f32 v179, v172, v179\n"
        "v_smfmac_f32_16x16x64_bf16 v[124:127], a[252:255], v[88:95], v237\n"
        "v_cvt_pk_bf16_f32 v176, v176, v177\n"
        "v_mfma_f32_16x16x32_bf16 v[80:83], v[132:135], v[92:95], 0\n"
        "v_cvt_pk_bf16_f32 v177, v178, v179\n"
        "v_mul_f32 v180, v172, v180\n"
        "v_mfma_f32_16x16x32_bf16 v[80:83], v[128:131], v[88:91], v[80:83]\n"
        "ds_write2st64_b32 v245, v176, v177 offset0:1 offset1:5\n"
        "v_mfma_f32_16x16x32_bf16 v[84:87], v[136:139], v[88:91], 0\n"
        "v_cvt_pk_bf16_f32 v152, v112, v113\n"
        "v_cvt_pk_bf16_f32 v153, v114, v115\n"
        "v_mfma_f32_16x16x32_bf16 v[84:87], v[140:143], v[92:95], v[84:87]\n"
        "v_mul_f32 v181, v172, v181\n"
        "v_mul_f32 v182, v172, v182\n"
        "v_mfma_f32_16x16x32_bf16 v[96:99], v[148:151], v[92:95], 0\n"
        "buffer_load_dwordx4 v234, s[52:55], s34 offen offset:2048 lds\n"
        "v_mfma_f32_16x16x32_bf16 v[96:99], v[144:147], v[88:91], v[96:99]\n"
        "v_mul_f32 v183, v172, v183\n"
        "v_cvt_pk_bf16_f32 v178, v180, v181\n"
        "v_mfma_f32_16x16x32_bf16 v[116:119], a[116:119], a[220:223], 0\n"
        "v_cvt_pk_bf16_f32 v179, v182, v183\n"
        "v_mul_f32 v184, v172, v184\n"
        "v_mfma_f32_16x16x32_bf16 v[116:119], a[112:115], a[216:219], v[116:119]\n"
        "ds_write2st64_b32 v245, v178, v179 offset0:9 offset1:13\n"
        "v_mfma_f32_16x16x32_bf16 v[116:119], a[108:111], a[248:251], v[116:119]\n"
        "v_mul_f32 v185, v172, v185\n"
        "v_mul_f32 v186, v172, v186\n"
        "v_mfma_f32_16x16x32_bf16 v[116:119], a[104:107], a[244:247], v[116:119]\n"
        "s_add_u32 m0, s65, 15360\n"
        "v_mul_f32 v187, v172, v187\n"
        "v_add_f32 v125, v124, v125\n"
        "v_mfma_f32_16x16x32_bf16 v[116:119], a[100:103], a[240:243], v[116:119]\n"
        "v_rcp_f32 v124, v125\n"
        "v_mfma_f32_16x16x32_bf16 v[116:119], a[96:99], a[236:239], v[116:119]\n"
        "v_cvt_pk_bf16_f32 v184, v184, v185\n"
        "v_cvt_pk_bf16_f32 v185, v186, v187\n"
        "v_mfma_f32_16x16x32_bf16 v[120:123], a[120:123], a[236:239], 0\n"
        "buffer_load_dwordx4 v232, s[52:55], s62 offen lds\n"
        "v_mfma_f32_16x16x32_bf16 v[120:123], a[124:127], a[240:243], v[120:123]\n"
        "ds_write2st64_b32 v245, v184, v185 offset0:17 offset1:21\n"
        "v_mfma_f32_16x16x32_bf16 v[120:123], a[128:131], a[244:247], v[120:123]\n"
        "v_mul_f32 v80, v124, v80\n"
        "v_mul_f32 v81, v124, v81\n"
        "v_mfma_f32_16x16x32_bf16 v[120:123], a[132:135], a[248:251], v[120:123]\n"
        "v_mul_f32 v82, v124, v82\n"
        "v_mul_f32 v83, v124, v83\n"
        "v_mfma_f32_16x16x32_bf16 v[120:123], a[136:139], a[216:219], v[120:123]\n"
        "v_max3_u32 v236, v236, v172, v173\n"
        "v_cvt_pk_bf16_f32 v80, v80, v81\n"
        "v_mfma_f32_16x16x32_bf16 v[120:123], a[140:143], a[220:223], v[120:123]\n"
        "v_cvt_pk_bf16_f32 v81, v82, v83\n"
        "v_mul_f32 v84, v124, v84\n"
        "v_mfma_f32_16x16x32_bf16 v[112:115], a[20:23], a[220:223], 0\n"
        "v_mul_f32 v85, v124, v85\n"
        "v_mul_f32 v86, v124, v86\n"
        "v_mfma_f32_16x16x32_bf16 v[112:115], a[16:19], a[216:219], v[112:115]\n"
        "v_mul_f32 v87, v124, v87\n"
        "v_cvt_pk_bf16_f32 v82, v84, v85\n"
        "v_mfma_f32_16x16x32_bf16 v[112:115], a[12:15], a[248:251], v[112:115]\n"
        "buffer_load_dwordx4 v233, s[52:55], s62 offen offset:1024 lds\n"
        "v_mfma_f32_16x16x32_bf16 v[112:115], a[8:11], a[244:247], v[112:115]\n"
        "v_cvt_pk_bf16_f32 v83, v86, v87\n"
        "v_mul_f32 v96, v124, v96\n"
        "v_mfma_f32_16x16x32_bf16 v[112:115], a[4:7], a[240:243], v[112:115]\n"
        "v_mul_f32 v97, v124, v97\n"
        "v_mul_f32 v98, v124, v98\n"
        "v_mfma_f32_16x16x32_bf16 v[112:115], a[0:3], a[236:239], v[112:115]\n"
        "v_mul_f32 v99, v124, v99\n"
        "v_cvt_pk_bf16_f32 v154, v116, v117\n"
        "v_cvt_pk_bf16_f32 v155, v118, v119\n"
        "v_mfma_f32_16x16x32_bf16 v[116:119], a[24:27], a[236:239], 0\n"
        "v_cvt_pk_bf16_f32 v96, v96, v97\n"
        "v_cvt_pk_bf16_f32 v97, v98, v99\n"
        "v_mfma_f32_16x16x32_bf16 v[116:119], a[28:31], a[240:243], v[116:119]\n"
        "ds_write_b32 v245, v96 offset:4864\n"
        "v_mfma_f32_16x16x32_bf16 v[116:119], a[32:35], a[244:247], v[116:119]\n"
        "v_cvt_pk_bf16_f32 v168, v120, v121\n"
        "v_cvt_pk_bf16_f32 v169, v122, v123\n"
        "v_mfma_f32_16x16x32_bf16 v[116:119], a[36:39], a[248:251], v[116:119]\n"
        "v_add_f32 v193, v192, v193\n"
        "v_cvt_pk_bf16_f32 v156, v112, v113\n"
        "v_mfma_f32_16x16x32_bf16 v[116:119], a[40:43], a[216:219], v[116:119]\n"
        "buffer_load_dwordx4 v234, s[52:55], s62 offen offset:2048 lds\n"
        "v_mfma_f32_16x16x32_bf16 v[116:119], a[44:47], a[220:223], v[116:119]\n"
        "v_rcp_f32 v192, v193\n"
        "v_mfma_f32_16x16x32_bf16 v[120:123], a[68:71], a[220:223], 0\n"
        "v_mul_f32 v196, v192, v196\n"
        "v_mul_f32 v197, v192, v197\n"
        "v_mfma_f32_16x16x32_bf16 v[120:123], a[64:67], a[216:219], v[120:123]\n"
        "v_mul_f32 v198, v192, v198\n"
        "v_mul_f32 v199, v192, v199\n"
        "v_mfma_f32_16x16x32_bf16 v[120:123], a[60:63], a[248:251], v[120:123]\n"
        "ds_read_b128 a[224:227], v240 offset:1536\n"
        "v_cvt_pk_bf16_f32 v157, v114, v115\n"
        "v_mfma_f32_16x16x32_bf16 v[120:123], a[56:59], a[244:247], v[120:123]\n"
        "s_add_u32 m0, s65, 0\n"
        "ds_read_b128 a[228:231], v241 offset:1536\n"
        "v_cvt_pk_bf16_f32 v196, v196, v197\n"
        "v_mfma_f32_16x16x32_bf16 v[120:123], a[52:55], a[240:243], v[120:123]\n"
        "ds_read_b128 a[232:235], v240 offset:1664\n"
        "v_cvt_pk_bf16_f32 v197, v198, v199\n"
        "v_mfma_f32_16x16x32_bf16 v[120:123], a[48:51], a[236:239], v[120:123]\n"
        "ds_write2st64_b32 v245, v196, v197 offset0:2 offset1:6\n"
        "v_mfma_f32_16x16x32_bf16 v[112:115], a[236:239], a[144:147], 0\n"
        "buffer_load_dwordx4 v232, s[52:55], 0 offen lds\n"
        "v_mfma_f32_16x16x32_bf16 v[112:115], a[240:243], a[148:151], v[112:115]\n"
        "v_mul_f32 v200, v192, v200\n"
        "v_mul_f32 v201, v192, v201\n"
        "v_mfma_f32_16x16x32_bf16 v[112:115], a[244:247], a[152:155], v[112:115]\n"
        "v_mul_f32 v202, v192, v202\n"
        "v_mul_f32 v203, v192, v203\n"
        "v_mfma_f32_16x16x32_bf16 v[112:115], a[248:251], a[156:159], v[112:115]\n"
        "v_cvt_pk_bf16_f32 v198, v200, v201\n"
        "v_cvt_pk_bf16_f32 v199, v202, v203\n"
        "v_mfma_f32_16x16x32_bf16 v[112:115], a[216:219], a[160:163], v[112:115]\n"
        "ds_write2st64_b32 v245, v198, v199 offset0:10 offset1:14\n"
        "v_mfma_f32_16x16x32_bf16 v[112:115], a[220:223], a[164:167], v[112:115]\n"
        "v_mul_f32 v204, v192, v204\n"
        "v_mul_f32 v205, v192, v205\n"
        "v_cvt_pk_bf16_f32 v158, v116, v117\n"
        "v_cvt_pk_bf16_f32 v159, v118, v119\n"
        "v_mfma_f32_16x16x32_bf16 v[116:119], a[220:223], a[188:191], 0\n"
        "s_waitcnt vmcnt(4)\n"
        "ds_read_b64_tr_b16 v[64:65], v255 offset:12288\n"
        "ds_read_b64_tr_b16 v[66:67], v255 offset:13056\n"
        "v_mfma_f32_16x16x32_bf16 v[116:119], a[216:219], a[184:187], v[116:119]\n"
        "ds_write_b32 v245, v97 offset:5888\n"
        "v_mfma_f32_16x16x32_bf16 v[116:119], a[248:251], a[180:183], v[116:119]\n"
        "buffer_load_dwordx4 v233, s[52:55], 0 offen offset:1024 lds\n"
        "v_mfma_f32_16x16x32_bf16 v[116:119], a[244:247], a[176:179], v[116:119]\n"
        "ds_read_b64_tr_b16 v[68:69], v255 offset:12320\n"
        "ds_read_b64_tr_b16 v[70:71], v255 offset:13088\n"
        "v_mfma_f32_16x16x32_bf16 v[116:119], a[240:243], a[172:175], v[116:119]\n"
        "ds_read_b64_tr_b16 v[72:73], v255 offset:12352\n"
        "ds_read_b64_tr_b16 v[74:75], v255 offset:13120\n"
        "v_mfma_f32_16x16x32_bf16 v[116:119], a[236:239], a[168:171], v[116:119]\n"
        "v_mul_f32 v206, v192, v206\n"
        "v_mul_f32 v207, v192, v207\n"
        "v_cvt_pk_bf16_f32 v160, v120, v121\n"
        "v_cvt_pk_bf16_f32 v161, v122, v123\n"
        "v_cvt_pk_bf16_f32 v166, v120, v121\n"
        "v_cvt_pk_bf16_f32 v167, v122, v123\n"
        "v_mfma_f32_16x16x32_bf16 v[120:123], a[236:239], a[192:195], 0\n"
        "ds_read_b128 a[236:239], v241 offset:1664\n"
        "v_cvt_pk_bf16_f32 v204, v204, v205\n"
        "v_mfma_f32_16x16x32_bf16 v[120:123], a[240:243], a[196:199], v[120:123]\n"
        "ds_read_b128 a[240:243], v240 offset:1792\n"
        "v_cvt_pk_bf16_f32 v205, v206, v207\n"
        "v_mfma_f32_16x16x32_bf16 v[120:123], a[244:247], a[200:203], v[120:123]\n"
        "ds_write2st64_b32 v245, v204, v205 offset0:18 offset1:22\n"
        "v_mfma_f32_16x16x32_bf16 v[120:123], a[248:251], a[204:207], v[120:123]\n"
        "ds_read_b128 a[244:247], v241 offset:1792\n"
        "v_max3_u32 v236, v236, v192, v193\n"
        "v_mfma_f32_16x16x32_bf16 v[120:123], a[216:219], a[208:211], v[120:123]\n"
        "buffer_load_dwordx4 v234, s[52:55], 0 offen offset:2048 lds\n"
        "v_mfma_f32_16x16x32_bf16 v[120:123], a[220:223], a[212:215], v[120:123]\n"
        "s_waitcnt lgkmcnt(4)\n"
        "ds_write_b64 v235, v[64:65] offset:12288\n"
        "v_cvt_pk_bf16_f32 v128, v112, v113\n"
        "v_cvt_pk_bf16_f32 v129, v114, v115\n"
        "v_mfma_f32_16x16x32_bf16 v[112:115], a[72:75], a[224:227], 0\n"
        "s_barrier\n"
        "s_waitcnt vmcnt(3)\n"
        "ds_read_b64_tr_b16 v[76:77], v255 offset:15360\n"
        "ds_read_b64_tr_b16 v[78:79], v255 offset:16128\n"
        "v_mfma_f32_16x16x32_bf16 v[112:115], a[76:79], a[228:231], v[112:115]\n"
        "v_max3_u32 v236, v236, v124, v125\n"
        "v_cmp_lt_u32 vcc, s24, v236\n"
        "v_mfma_f32_16x16x32_bf16 v[112:115], a[80:83], a[232:235], v[112:115]\n"
        "s_waitcnt lgkmcnt(5)\n"
        "ds_write_b64 v235, v[66:67] offset:12296\n"
        "s_cmp_lg_u64 vcc, 0\n"
        "s_addc_u32 s25, s25, s25\n"
        "v_mfma_f32_16x16x32_bf16 v[112:115], a[84:87], a[236:239], v[112:115]\n"
        "s_add_u32 m0, s65, 3072\n"
        "v_and_b32 v239, 32, v245\n"
        "v_cvt_pk_bf16_f32 v136, v116, v117\n"
        "v_mfma_f32_16x16x32_bf16 v[112:115], a[88:91], a[240:243], v[112:115]\n"
        "s_waitcnt lgkmcnt(4)\n"
        "v_cvt_pk_bf16_f32 v137, v118, v119\n"
        "v_xor_b32 v251, 0x2000, v251\n"
        "v_mfma_f32_16x16x32_bf16 v[112:115], a[92:95], a[244:247], v[112:115]\n"
        "v_xor_b32 v252, 0x2000, v252\n"
        "v_mad_i32_i24 v239, v239, -12, v235\n"
        "v_mfma_f32_16x16x32_bf16 v[116:119], a[116:119], a[244:247], 0\n"
        "ds_write_b64 v239, v[68:69] offset:12480\n"
        "v_mfma_f32_16x16x32_bf16 v[116:119], a[112:115], a[240:243], v[116:119]\n"
        "buffer_load_dwordx4 v232, s[52:55], s63 offen lds\n"
        "v_mfma_f32_16x16x32_bf16 v[116:119], a[108:111], a[236:239], v[116:119]\n"
        "ds_read_b64_tr_b16 v[88:89], v255 offset:15392\n"
        "ds_read_b64_tr_b16 v[90:91], v255 offset:16160\n"
        "v_mfma_f32_16x16x32_bf16 v[116:119], a[104:107], a[232:235], v[116:119]\n"
        "ds_write_b64 v239, v[70:71] offset:12488\n"
        "v_mfma_f32_16x16x32_bf16 v[116:119], a[100:103], a[228:231], v[116:119]\n"
        "v_cvt_pk_bf16_f32 v144, v120, v121\n"
        "v_cvt_pk_bf16_f32 v145, v122, v123\n"
        "v_mfma_f32_16x16x32_bf16 v[116:119], a[96:99], a[224:227], v[116:119]\n"
        "v_cvt_pk_bf16_f32 v172, v112, v113\n"
        "v_cvt_pk_bf16_f32 v173, v114, v115\n"
        "v_mfma_f32_16x16x32_bf16 v[120:123], a[120:123], a[224:227], 0\n"
        "ds_write_b64 v254, v[72:73] offset:12288\n"
        "v_mfma_f32_16x16x32_bf16 v[120:123], a[124:127], a[228:231], v[120:123]\n"
        "ds_read_b128 v[162:165], v251 offset:0\n"
        "v_add_u32 v247, s45, v247\n"
        "v_mfma_f32_16x16x32_bf16 v[120:123], a[128:131], a[232:235], v[120:123]\n"
        "buffer_load_dwordx4 v233, s[52:55], s63 offen offset:1024 lds\n"
        "v_mfma_f32_16x16x32_bf16 v[120:123], a[132:135], a[236:239], v[120:123]\n"
        "ds_write_b64 v254, v[74:75] offset:12296\n"
        "v_mfma_f32_16x16x32_bf16 v[120:123], a[136:139], a[240:243], v[120:123]\n"
        "ds_read_b64_tr_b16 v[92:93], v255 offset:15424\n"
        "ds_read_b64_tr_b16 v[94:95], v255 offset:16192\n"
        "v_mfma_f32_16x16x32_bf16 v[120:123], a[140:143], a[244:247], v[120:123]\n"
        "v_cvt_pk_bf16_f32 v174, v116, v117\n"
        "v_cvt_pk_bf16_f32 v175, v118, v119\n"
        "v_mfma_f32_16x16x32_bf16 v[112:115], a[20:23], a[244:247], 0\n"
        "s_nop 2\n"
        "v_mfma_f32_16x16x32_bf16 v[112:115], a[16:19], a[240:243], v[112:115]\n"
        "s_nop 0\n"
        "v_cvt_pk_bf16_f32 v190, v120, v121\n"
        "v_cvt_pk_bf16_f32 v191, v122, v123\n"
        "v_mfma_f32_16x16x32_bf16 v[112:115], a[12:15], a[236:239], v[112:115]\n"
        "v_mfma_f32_16x16x32_bf16 v[112:115], a[8:11], a[232:235], v[112:115]\n"
        "s_waitcnt vmcnt(2)\n"
        "ds_read_b64_tr_b16 v[212:213], v255 offset:0\n"
        "ds_read_b64_tr_b16 v[214:215], v255 offset:768\n"
        "v_mfma_f32_16x16x32_bf16 v[112:115], a[4:7], a[228:231], v[112:115]\n"
        "buffer_load_dwordx4 v234, s[52:55], s63 offen offset:2048 lds\n"
        "v_mfma_f32_16x16x32_bf16 v[112:115], a[0:3], a[224:227], v[112:115]\n"
        "ds_read_b128 v[182:185], v251 offset:512\n"
        "s_nop 0\n"
        "v_mfma_f32_16x16x32_bf16 v[116:119], a[24:27], a[224:227], 0\n"
        "s_nop 2\n"
        "v_mfma_f32_16x16x32_bf16 v[116:119], a[28:31], a[228:231], v[116:119]\n"
        "s_waitcnt lgkmcnt(3)\n"
        "ds_write_b128 v235, v[76:79] offset:15360\n"
        "v_mfma_f32_16x16x32_bf16 v[116:119], a[32:35], a[232:235], v[116:119]\n"
        "v_cvt_pk_bf16_f32 v176, v112, v113\n"
        "v_cvt_pk_bf16_f32 v177, v114, v115\n"
        "v_mfma_f32_16x16x32_bf16 v[116:119], a[36:39], a[236:239], v[116:119]\n"
        "s_add_u32 m0, s65, 6144\n"
        "v_mfma_f32_16x16x32_bf16 v[116:119], a[40:43], a[240:243], v[116:119]\n"
        "ds_write_b32 v245, v80 offset:768\n"
        "v_mfma_f32_16x16x32_bf16 v[116:119], a[44:47], a[244:247], v[116:119]\n"
        "ds_read_b64_tr_b16 v[216:217], v255 offset:32\n"
        "ds_read_b64_tr_b16 v[218:219], v255 offset:800\n"
        "v_mfma_f32_16x16x32_bf16 v[120:123], a[68:71], a[244:247], 0\n"
        "buffer_load_dwordx4 v232, s[52:55], s61 offen lds\n"
        "v_mfma_f32_16x16x32_bf16 v[120:123], a[64:67], a[240:243], v[120:123]\n"
        "s_nop 2\n"
        "v_mfma_f32_16x16x32_bf16 v[120:123], a[60:63], a[236:239], v[120:123]\n"
        "ds_write_b128 v239, v[88:91] offset:15552\n"
        "v_mfma_f32_16x16x32_bf16 v[120:123], a[56:59], a[232:235], v[120:123]\n"
        "v_cvt_pk_bf16_f32 v178, v116, v117\n"
        "v_cvt_pk_bf16_f32 v179, v118, v119\n"
        "v_mfma_f32_16x16x32_bf16 v[120:123], a[52:55], a[228:231], v[120:123]\n"
        "v_mfma_f32_16x16x32_bf16 v[120:123], a[48:51], a[224:227], v[120:123]\n"
        "ds_write_b32 v245, v81 offset:1792\n"
        "v_mfma_f32_16x16x32_bf16 v[112:115], a[224:227], a[144:147], 0\n"
        "ds_read_b64_tr_b16 v[220:221], v255 offset:64\n"
        "ds_read_b64_tr_b16 v[222:223], v255 offset:832\n"
        "v_mfma_f32_16x16x32_bf16 v[112:115], a[228:231], a[148:151], v[112:115]\n"
        "ds_write_b32 v245, v82 offset:2816\n"
        "v_mfma_f32_16x16x32_bf16 v[112:115], a[232:235], a[152:155], v[112:115]\n"
        "ds_write_b32 v245, v83 offset:3840\n"
        "v_mfma_f32_16x16x32_bf16 v[112:115], a[236:239], a[156:159], v[112:115]\n"
        "buffer_load_dwordx4 v233, s[52:55], s61 offen offset:1024 lds\n"
        "v_mfma_f32_16x16x32_bf16 v[112:115], a[240:243], a[160:163], v[112:115]\n"
        "ds_read_b128 a[248:251], v240 offset:12288\n"
        "v_cvt_pk_bf16_f32 v180, v120, v121\n"
        "v_mfma_f32_16x16x32_bf16 v[112:115], a[244:247], a[164:167], v[112:115]\n"
        "ds_write_b128 v254, v[92:95] offset:15360\n"
        "v_mfma_f32_16x16x32_bf16 v[96:99], v[152:155], v[156:159], v[0:3]\n"
        "v_cvt_pk_bf16_f32 v181, v122, v123\n"
        "v_cvt_pk_bf16_f32 v186, v120, v121\n"
        "v_mfma_f32_16x16x32_bf16 v[96:99], v[168:171], v[160:163], v[96:99]\n"
        "s_sub_u32 s31, s30, s28\n"
        "v_cvt_pk_bf16_f32 v187, v122, v123\n"
        "s_nop 0\n"
        "v_mfma_f32_16x16x32_bf16 v[100:103], v[172:175], v[156:159], v[4:7]\n"
        ".long 0xbec200ff\n"
        "43:\n"
        "s_branch 60b\n"
        "s_sub_u32 s45, s28, s29\n"
        "v_cvt_pk_bf16_f32 v130, v112, v113\n"
        "v_cvt_pk_bf16_f32 v131, v114, v115\n"
        "v_mfma_f32_16x16x32_bf16 v[100:103], v[188:191], v[164:167], v[100:103]\n"
        "s_waitcnt vmcnt(2)\n"
        "ds_read_b64_tr_b16 v[224:225], v255 offset:3072\n"
        "ds_read_b64_tr_b16 v[226:227], v255 offset:3840\n"
        "s_add_u32 s28, s28, s31\n"
        "v_mfma_f32_16x16x32_bf16 v[116:119], a[244:247], a[188:191], 0\n"
        "buffer_load_dwordx4 v234, s[52:55], s61 offen offset:2048 lds\n"
        "v_mfma_f32_16x16x32_bf16 v[116:119], a[240:243], a[184:187], v[116:119]\n"
        "s_waitcnt lgkmcnt(5)\n"
        "ds_write_b128 v235, v[212:215] offset:0\n"
        "v_mfma_f32_16x16x32_bf16 v[116:119], a[236:239], a[180:183], v[116:119]\n"
        "v_exp_f32 v96, v96\n"
        "v_mfma_f32_16x16x32_bf16 v[116:119], a[232:235], a[176:179], v[116:119]\n"
        "v_exp_f32 v97, v97\n"
        "v_mfma_f32_16x16x32_bf16 v[116:119], a[228:231], a[172:175], v[116:119]\n"
        "v_exp_f32 v98, v98\n"
        "v_mfma_f32_16x16x32_bf16 v[116:119], a[224:227], a[168:171], v[116:119]\n"
        "ds_read_b64_tr_b16 v[88:89], v255 offset:3104\n"
        "ds_read_b64_tr_b16 v[90:91], v255 offset:3872\n"
        "s_add_u32 m0, s65, 9216\n"
        "v_mfma_f32_16x16x32_bf16 v[104:107], v[152:155], v[176:179], v[16:19]\n"
        "ds_read_b128 a[216:219], v241 offset:12288\n"
        "v_cvt_pk_bf16_f32 v64, v96, v97\n"
        "v_mfma_f32_16x16x32_bf16 v[104:107], v[168:171], v[180:183], v[104:107]\n"
        "ds_write_b128 v239, v[216:219] offset:192\n"
        "v_mfma_f32_16x16x32_bf16 v[108:111], v[172:175], v[176:179], v[20:23]\n"
        "buffer_load_dwordx4 v232, s[52:55], s56 offen lds\n"
        "v_mfma_f32_16x16x32_bf16 v[108:111], v[188:191], v[184:187], v[108:111]\n"
        "v_exp_f32 v100, v100\n"
        "v_mfma_f32_16x16x32_bf16 v[120:123], a[224:227], a[192:195], 0\n"
        "v_cvt_pk_bf16_f32 v138, v116, v117\n"
        "v_cvt_pk_bf16_f32 v139, v118, v119\n"
        "v_mfma_f32_16x16x32_bf16 v[120:123], a[228:231], a[196:199], v[120:123]\n"
        "ds_read_b128 a[220:223], v240 offset:12416\n"
        "ds_read_b128 a[224:227], v241 offset:12416\n"
        "v_mfma_f32_16x16x32_bf16 v[120:123], a[232:235], a[200:203], v[120:123]\n"
        "ds_read_b64_tr_b16 v[92:93], v255 offset:3136\n"
        "ds_read_b64_tr_b16 v[94:95], v255 offset:3904\n"
        "v_mfma_f32_16x16x32_bf16 v[120:123], a[236:239], a[204:207], v[120:123]\n"
        "ds_read_b128 a[228:231], v240 offset:12544\n"
        "ds_read_b128 a[232:235], v241 offset:12544\n"
        "v_mfma_f32_16x16x32_bf16 v[120:123], a[240:243], a[208:211], v[120:123]\n"
        "ds_write_b128 v254, v[220:223] offset:0\n"
        "v_mfma_f32_16x16x32_bf16 v[120:123], a[244:247], a[212:215], v[120:123]\n"
        "s_waitcnt lgkmcnt(8)\n"
        "v_exp_f32 v99, v99\n"
        "v_mfma_f32_16x16x32_bf16 v[112:115], a[0:3], a[248:251], 0\n"
        "buffer_load_dwordx4 v233, s[52:55], s56 offen offset:1024 lds\n"
        "v_mfma_f32_16x16x32_bf16 v[112:115], a[4:7], a[216:219], v[112:115]\n"
        "s_waitcnt lgkmcnt(5)\n"
        "v_exp_f32 v101, v101\n"
        "v_mfma_f32_16x16x32_bf16 v[112:115], a[8:11], a[220:223], v[112:115]\n"
        "ds_read_b128 v[202:205], v251 offset:4096\n"
        "v_cvt_pk_bf16_f32 v65, v98, v99\n"
        "v_mfma_f32_16x16x32_bf16 v[112:115], a[12:15], a[224:227], v[112:115]\n"
        "s_waitcnt lgkmcnt(2)\n"
        "ds_read_b128 v[162:165], v252 offset:0\n"
        "v_cvt_pk_bf16_f32 v66, v100, v101\n"
        "v_mfma_f32_16x16x32_bf16 v[112:115], a[16:19], a[228:231], v[112:115]\n"
        "s_waitcnt vmcnt(2)\n"
        "ds_read_b64_tr_b16 v[212:213], v255 offset:6144\n"
        "ds_read_b64_tr_b16 v[214:215], v255 offset:6912\n"
        "v_mfma_f32_16x16x32_bf16 v[112:115], a[20:23], a[232:235], v[112:115]\n"
        "ds_write_b128 v235, v[224:227] offset:3072\n"
        "v_mfma_f32_16x16x32_bf16 v[116:119], a[44:47], a[232:235], 0\n"
        "v_exp_f32 v102, v102\n"
        "v_mfma_f32_16x16x32_bf16 v[116:119], a[40:43], a[228:231], v[116:119]\n"
        "v_exp_f32 v103, v103\n"
        "v_mfma_f32_16x16x32_bf16 v[116:119], a[36:39], a[224:227], v[116:119]\n"
        "buffer_load_dwordx4 v234, s[52:55], s56 offen offset:2048 lds\n"
        "s_add_u32 s35, s47, 786432\n"
        "s_sub_u32 s46, s35, 100663296\n"
        "s_cmp_lt_u32 s46, 786432\n"
        "v_mfma_f32_16x16x32_bf16 v[116:119], a[32:35], a[220:223], v[116:119]\n"
        "s_cselect_b32 s35, s46, s35\n"
        "s_add_u32 s41, s30, s40\n"
        "s_mov_b32 s30, s29\n"
        "s_add_u32 s29, s29, s45\n"
        "v_mfma_f32_16x16x32_bf16 v[116:119], a[28:31], a[216:219], v[116:119]\n"
        "ds_read_b64_tr_b16 v[216:217], v255 offset:6176\n"
        "ds_read_b64_tr_b16 v[218:219], v255 offset:6944\n"
        "v_mfma_f32_16x16x32_bf16 v[116:119], a[24:27], a[248:251], v[116:119]\n"
        "ds_read_b128 v[182:185], v252 offset:512\n"
        "v_cvt_pk_bf16_f32 v67, v102, v103\n"
        "v_cvt_pk_bf16_f32 v146, v120, v121\n"
        "v_cvt_pk_bf16_f32 v147, v122, v123\n"
        "v_mfma_f32_16x16x32_bf16 v[120:123], a[48:51], a[248:251], 0\n"
        "ds_write_b128 v239, v[88:91] offset:3264\n"
        "v_mfma_f32_16x16x32_bf16 v[120:123], a[52:55], a[216:219], v[120:123]\n"
        "v_exp_f32 v104, v104\n"
        "v_mfma_f32_16x16x32_bf16 v[120:123], a[56:59], a[220:223], v[120:123]\n"
        "s_mov_b32 m0, s48\n"
        "v_exp_f32 v105, v105\n"
        "v_mfma_f32_16x16x32_bf16 v[120:123], a[60:63], a[224:227], v[120:123]\n"
        "v_exp_f32 v106, v106\n"
        "v_mfma_f32_16x16x32_bf16 v[120:123], a[64:67], a[228:231], v[120:123]\n"
        "ds_read_b64_tr_b16 v[220:221], v255 offset:6208\n"
        "ds_read_b64_tr_b16 v[222:223], v255 offset:6976\n"
        "v_mfma_f32_16x16x32_bf16 v[120:123], a[68:71], a[232:235], v[120:123]\n"
        "buffer_load_dwordx4 v249, s[12:15], s36 offen offset:0 lds\n"
        "v_cvt_pk_bf16_f32 v196, v112, v113\n"
        "v_cvt_pk_bf16_f32 v197, v114, v115\n"
        "v_mfma_f32_16x16x32_bf16 v[112:115], a[92:95], a[232:235], 0\n"
        "v_cvt_pk_bf16_f32 v72, v104, v105\n"
        "v_cvt_pk_bf16_f32 v198, v116, v117\n"
        "v_mfma_f32_16x16x32_bf16 v[112:115], a[88:91], a[228:231], v[112:115]\n"
        "ds_write_b128 v254, v[92:95] offset:3072\n"
        "v_mfma_f32_16x16x32_bf16 v[112:115], a[84:87], a[224:227], v[112:115]\n"
        "v_exp_f32 v108, v108\n"
        "v_mfma_f32_16x16x32_bf16 v[112:115], a[80:83], a[220:223], v[112:115]\n"
        "v_exp_f32 v107, v107\n"
        "v_mfma_f32_16x16x32_bf16 v[112:115], a[76:79], a[216:219], v[112:115]\n"
        "v_exp_f32 v109, v109\n"
        "v_mfma_f32_16x16x32_bf16 v[112:115], a[72:75], a[248:251], v[112:115]\n"
        "s_waitcnt vmcnt(1)\n"
        "ds_read_b64_tr_b16 v[224:225], v255 offset:9216\n"
        "ds_read_b64_tr_b16 v[226:227], v255 offset:9984\n"
        "v_cvt_pk_bf16_f32 v199, v118, v119\n"
        "s_nop 1\n"
        "v_mfma_f32_16x16x32_bf16 v[96:99], v[152:155], v[196:199], v[32:35]\n"
        "s_waitcnt lgkmcnt(3)\n"
        "v_cvt_pk_bf16_f32 v73, v106, v107\n"
        "v_cvt_pk_bf16_f32 v74, v108, v109\n"
        "v_cvt_pk_bf16_f32 v200, v120, v121\n"
        "v_cvt_pk_bf16_f32 v201, v122, v123\n"
        "s_nop 1\n"
        "v_mfma_f32_16x16x32_bf16 v[96:99], v[168:171], v[200:203], v[96:99]\n"
        "buffer_load_dwordx4 v253, s[12:15], s36 offen offset:1024 lds\n"
        "v_mfma_f32_16x16x32_bf16 v[100:103], v[172:175], v[196:199], v[36:39]\n"
        "ds_write_b128 v235, v[212:215] offset:6144\n"
        "v_cvt_pk_bf16_f32 v206, v120, v121\n"
        "v_cvt_pk_bf16_f32 v207, v122, v123\n"
        "s_nop 1\n"
        "v_mfma_f32_16x16x32_bf16 v[100:103], v[188:191], v[204:207], v[100:103]\n"
        "s_cmp_eq_u32 s37, 100663296\n"
        "v_exp_f32 v110, v110\n"
        "v_mfma_f32_16x16x32_bf16 v[116:119], a[96:99], a[248:251], 0\n"
        "s_cselect_b32 s46, 0, s37\n"
        "v_exp_f32 v111, v111\n"
        "v_mfma_f32_16x16x32_bf16 v[116:119], a[100:103], a[216:219], v[116:119]\n"
        "s_add_u32 m0, s41, 0\n"
        "v_cvt_pk_bf16_f32 v75, v110, v111\n"
        "v_cvt_pk_bf16_f32 v192, v112, v113\n"
        "v_mfma_f32_16x16x32_bf16 v[116:119], a[104:107], a[220:223], v[116:119]\n"
        "ds_read_b64_tr_b16 v[88:89], v255 offset:9248\n"
        "ds_read_b64_tr_b16 v[90:91], v255 offset:10016\n"
        "s_add_u32 s38, s46, s43\n"
        "v_mfma_f32_16x16x32_bf16 v[116:119], a[108:111], a[224:227], v[116:119]\n"
        "buffer_load_dwordx4 v242, s[8:11], s47 offen lds\n"
        "v_mfma_f32_16x16x32_bf16 v[116:119], a[112:115], a[228:231], v[116:119]\n"
        "ds_read_b128 v[202:205], v252 offset:4096\n"
        "s_add_u32 s39, s46, s44\n"
        "v_cvt_pk_bf16_f32 v193, v114, v115\n"
        "v_mfma_f32_16x16x32_bf16 v[116:119], a[116:119], a[232:235], v[116:119]\n"
        "ds_write_b128 v239, v[216:219] offset:6336\n"
        "s_add_u32 s37, s37, 6291456\n"
        "v_mfma_f32_16x16x32_bf16 v[120:123], a[140:143], a[232:235], 0\n"
        "v_exp_f32 v96, v96\n"
        "v_mfma_f32_16x16x32_bf16 v[120:123], a[136:139], a[228:231], v[120:123]\n"
        "s_add_u32 m0, s41, 1024\n"
        "v_exp_f32 v97, v97\n"
        "v_mfma_f32_16x16x32_bf16 v[120:123], a[132:135], a[224:227], v[120:123]\n"
        "v_exp_f32 v98, v98\n"
        "v_mfma_f32_16x16x32_bf16 v[120:123], a[128:131], a[220:223], v[120:123]\n"
        "ds_read_b64_tr_b16 v[92:93], v255 offset:9280\n"
        "ds_read_b64_tr_b16 v[94:95], v255 offset:10048\n"
        "s_mov_b64 s[58:59], s[18:19]\n"
        "v_mfma_f32_16x16x32_bf16 v[120:123], a[124:127], a[216:219], v[120:123]\n"
        "buffer_load_dwordx4 v243, s[8:11], s47 offen lds\n"
        "v_mfma_f32_16x16x32_bf16 v[120:123], a[120:123], a[248:251], v[120:123]\n"
        "s_mov_b64 s[62:63], s[18:19]\n"
        "v_cvt_pk_bf16_f32 v80, v96, v97\n"
        "v_cvt_pk_bf16_f32 v194, v116, v117\n"
        "v_mfma_f32_16x16x32_bf16 v[112:115], a[248:251], a[144:147], 0\n"
        "ds_write_b128 v254, v[220:223] offset:6144\n"
        "v_mfma_f32_16x16x32_bf16 v[112:115], a[216:219], a[148:151], v[112:115]\n"
        "v_exp_f32 v100, v100\n"
        "v_mfma_f32_16x16x32_bf16 v[112:115], a[220:223], a[152:155], v[112:115]\n"
        "s_add_u32 m0, s41, 2048\n"
        "v_cvt_pk_bf16_f32 v195, v118, v119\n"
        "v_cvt_pk_bf16_f32 v208, v120, v121\n"
        "v_mfma_f32_16x16x32_bf16 v[112:115], a[224:227], a[156:159], v[112:115]\n"
        "v_exp_f32 v99, v99\n"
        "v_mfma_f32_16x16x32_bf16 v[112:115], a[228:231], a[160:163], v[112:115]\n"
        "buffer_load_dwordx4 v244, s[8:11], s47 offen lds\n"
        "v_mfma_f32_16x16x32_bf16 v[112:115], a[232:235], a[164:167], v[112:115]\n"
        "ds_read_b128 a[236:239], v240 offset:13824\n"
        "v_cvt_pk_bf16_f32 v81, v98, v99\n"
        "v_mfma_f32_16x16x32_bf16 v[104:107], v[192:195], v[156:159], v[8:11]\n"
        "ds_read_b128 a[240:243], v241 offset:13824\n"
        "v_cvt_pk_bf16_f32 v209, v122, v123\n"
        "s_nop 1\n"
        "v_mfma_f32_16x16x32_bf16 v[104:107], v[208:211], v[160:163], v[104:107]\n"
        "s_waitcnt lgkmcnt(3)\n"
        "ds_write_b128 v235, v[224:227] offset:9216\n"
        "v_mfma_f32_16x16x32_bf16 v[116:119], a[232:235], a[188:191], 0\n"
        "v_exp_f32 v101, v101\n"
        "v_mfma_f32_16x16x32_bf16 v[116:119], a[228:231], a[184:187], v[116:119]\n"
        "s_add_u32 m0, s41, 12288\n"
        "v_exp_f32 v102, v102\n"
        "v_mfma_f32_16x16x32_bf16 v[116:119], a[224:227], a[180:183], v[116:119]\n"
        "v_cvt_pk_bf16_f32 v82, v100, v101\n"
        "v_cvt_pk_bf16_f32 v132, v112, v113\n"
        "v_mfma_f32_16x16x32_bf16 v[116:119], a[220:223], a[176:179], v[116:119]\n"
        "buffer_load_dwordx4 v242, s[8:11], s35 offen lds\n"
        "v_mfma_f32_16x16x32_bf16 v[116:119], a[216:219], a[172:175], v[116:119]\n"
        "ds_read_b128 a[244:247], v240 offset:13952\n"
        "v_cvt_pk_bf16_f32 v133, v114, v115\n"
        "v_mfma_f32_16x16x32_bf16 v[116:119], a[248:251], a[168:171], v[116:119]\n"
        "v_exp_f32 v103, v103\n"
        "v_mfma_f32_16x16x32_bf16 v[108:111], v[192:195], v[176:179], v[24:27]\n"
        "ds_write_b128 v239, v[88:91] offset:9408\n"
        "v_mfma_f32_16x16x32_bf16 v[108:111], v[208:211], v[180:183], v[108:111]\n"
        "v_cvt_pk_bf16_f32 v83, v102, v103\n"
        "v_add_u32 v245, s45, v245\n"
        "v_mfma_f32_16x16x32_bf16 v[120:123], a[248:251], a[192:195], 0\n"
        "s_add_u32 m0, s41, 13312\n"
        "v_exp_f32 v104, v104\n"
        "v_mfma_f32_16x16x32_bf16 v[120:123], a[216:219], a[196:199], v[120:123]\n"
        "v_cvt_pk_bf16_f32 v140, v116, v117\n"
        "v_cvt_pk_bf16_f32 v141, v118, v119\n"
        "v_mfma_f32_16x16x32_bf16 v[120:123], a[220:223], a[200:203], v[120:123]\n"
        "ds_read_b128 a[248:251], v241 offset:13952\n"
        "ds_read_b128 a[216:219], v240 offset:14080\n"
        "v_mfma_f32_16x16x32_bf16 v[120:123], a[224:227], a[204:207], v[120:123]\n"
        "buffer_load_dwordx4 v243, s[8:11], s35 offen lds\n"
        "v_mfma_f32_16x16x32_bf16 v[120:123], a[228:231], a[208:211], v[120:123]\n"
        "ds_read_b128 a[220:223], v241 offset:14080\n"
        "v_bfe_u32 v238, v250, 3, 4\n"
        "v_mfma_f32_16x16x32_bf16 v[120:123], a[232:235], a[212:215], v[120:123]\n"
        "ds_write_b128 v254, v[92:95] offset:9216\n"
        "v_mfma_f32_16x16x32_bf16 v[96:99], v[192:195], v[196:199], v[40:43]\n"
        "v_exp_f32 v105, v105\n"
        "v_mfma_f32_16x16x32_bf16 v[96:99], v[208:211], v[200:203], v[96:99]\n"
        "s_waitcnt lgkmcnt(5)\n"
        "s_add_u32 m0, s41, 14336\n"
        "v_exp_f32 v106, v106\n"
        "v_mfma_f32_16x16x32_bf16 v[112:115], a[72:75], a[236:239], 0\n"
        "v_cvt_pk_bf16_f32 v68, v104, v105\n"
        "v_cvt_pk_bf16_f32 v148, v120, v121\n"
        "v_mfma_f32_16x16x32_bf16 v[112:115], a[76:79], a[240:243], v[112:115]\n"
        "buffer_load_dwordx4 v244, s[8:11], s35 offen lds\n"
        "v_mfma_f32_16x16x32_bf16 v[112:115], a[80:83], a[244:247], v[112:115]\n"
        "s_waitcnt lgkmcnt(1)\n"
        "v_exp_f32 v107, v107\n"
        "v_mfma_f32_16x16x32_bf16 v[112:115], a[84:87], a[248:251], v[112:115]\n"
        "v_cvt_pk_bf16_f32 v69, v106, v107\n"
        "v_cvt_pk_bf16_f32 v149, v122, v123\n"
        "v_mfma_f32_16x16x32_bf16 v[112:115], a[88:91], a[216:219], v[112:115]\n"
        "v_exp_f32 v108, v108\n"
        "v_mfma_f32_16x16x32_bf16 v[112:115], a[92:95], a[220:223], v[112:115]\n"
        "v_exp_f32 v109, v109\n"
        "v_mfma_f32_16x16x32_bf16 v[116:119], a[116:119], a[220:223], 0\n"
        "v_exp_f32 v110, v110\n"
        "v_mfma_f32_16x16x32_bf16 v[116:119], a[112:115], a[216:219], v[116:119]\n"
        "v_cvt_pk_bf16_f32 v76, v108, v109\n"
        "v_and_b32 v254, 32, v250\n"
        "v_mfma_f32_16x16x32_bf16 v[116:119], a[108:111], a[248:251], v[116:119]\n"
        "v_exp_f32 v111, v111\n"
        "v_mfma_f32_16x16x32_bf16 v[116:119], a[104:107], a[244:247], v[116:119]\n"
        "s_add_u32 s56, s16, s38\n"
        "s_addc_u32 s57, s17, 0\n"
        "v_cvt_pk_bf16_f32 v212, v112, v113\n"
        "v_mfma_f32_16x16x32_bf16 v[116:119], a[100:103], a[240:243], v[116:119]\n"
        "s_add_u32 s60, s16, s39\n"
        "s_addc_u32 s61, s17, 0\n"
        "v_cvt_pk_bf16_f32 v213, v114, v115\n"
        "v_mfma_f32_16x16x32_bf16 v[116:119], a[96:99], a[236:239], v[116:119]\n"
        "v_cvt_pk_bf16_f32 v77, v110, v111\n"
        "v_mad_i32_i24 v238, v238, -4, v250\n"
        "v_mfma_f32_16x16x32_bf16 v[120:123], a[120:123], a[236:239], 0\n"
        "v_exp_f32 v96, v96\n"
        "v_mfma_f32_16x16x32_bf16 v[120:123], a[124:127], a[240:243], v[120:123]\n"
        "s_add_u32 s47, s47, 6291456\n"
        "v_exp_f32 v97, v97\n"
        "v_mfma_f32_16x16x32_bf16 v[120:123], a[128:131], a[244:247], v[120:123]\n"
        "s_add_u32 s36, s36, 2097152\n"
        "v_cvt_pk_bf16_f32 v214, v116, v117\n"
        "v_cvt_pk_bf16_f32 v215, v118, v119\n"
        "v_mfma_f32_16x16x32_bf16 v[120:123], a[132:135], a[248:251], v[120:123]\n"
        "s_xor_b32 s48, s48, 8192\n"
        "v_exp_f32 v98, v98\n"
        "v_mfma_f32_16x16x32_bf16 v[120:123], a[136:139], a[216:219], v[120:123]\n"
        "s_waitcnt lgkmcnt(0)\n"
        "s_barrier\n"
        "v_cvt_pk_bf16_f32 v84, v96, v97\n"
        "v_mad_i32_i24 v254, v254, -12, v248\n"
        "v_mfma_f32_16x16x32_bf16 v[120:123], a[140:143], a[220:223], v[120:123]\n"
        "v_exp_f32 v99, v99\n"
        "v_mfma_f32_16x16x32_bf16 v[112:115], a[220:223], a[164:167], 0\n"
        "v_cvt_pk_bf16_f32 v85, v98, v99\n"
        "v_add_u32 v254, 1728, v254\n"
        "v_mfma_f32_16x16x32_bf16 v[112:115], a[216:219], a[160:163], v[112:115]\n"
        "v_add_u32 v238, s49, v238\n"
        "v_add_u32 v240, s31, v240\n"
        "v_mfma_f32_16x16x32_bf16 v[112:115], a[248:251], a[156:159], v[112:115]\n"
        "v_cvt_pk_bf16_f32 v230, v120, v121\n"
        "v_cvt_pk_bf16_f32 v231, v122, v123\n"
        "v_mfma_f32_16x16x32_bf16 v[112:115], a[244:247], a[152:155], v[112:115]\n"
        "v_add_u32 v241, s31, v241\n"
        "v_mfma_f32_16x16x32_bf16 v[112:115], a[240:243], a[148:151], v[112:115]\n"
        "v_mfma_f32_16x16x32_bf16 v[112:115], a[236:239], a[144:147], v[112:115]\n"
        "s_nop 2\n"
        "v_mfma_f32_16x16x32_bf16 v[100:103], v[212:215], v[156:159], v[12:15]\n"
        "s_nop 2\n"
        "v_mfma_f32_16x16x32_bf16 v[100:103], v[228:231], v[164:167], v[100:103]\n"
        "v_cvt_pk_bf16_f32 v134, v112, v113\n"
        "v_cvt_pk_bf16_f32 v135, v114, v115\n"
        "v_mfma_f32_16x16x32_bf16 v[116:119], a[236:239], a[168:171], 0\n"
        "s_waitcnt lgkmcnt(0)\n"
        "v_mfma_f32_16x16x32_bf16 v[116:119], a[240:243], a[172:175], v[116:119]\n"
        "s_bitcmp1_b32 5, s42\n"
        "v_mfma_f32_16x16x32_bf16 v[116:119], a[244:247], a[176:179], v[116:119]\n"
        "s_cbranch_scc1 12f\n"
        "11:\n"
        "10:\n"
        "v_mfma_f32_16x16x32_bf16 v[116:119], a[248:251], a[180:183], v[116:119]\n"
        "v_mfma_f32_16x16x32_bf16 v[116:119], a[216:219], a[184:187], v[116:119]\n"
        "s_nop 0\n"
        "v_exp_f32 v100, v100\n"
        "v_mfma_f32_16x16x32_bf16 v[116:119], a[220:223], a[188:191], v[116:119]\n"
        "v_exp_f32 v101, v101\n"
        "v_mfma_f32_16x16x32_bf16 v[104:107], v[212:215], v[176:179], v[28:31]\n"
        "v_exp_f32 v102, v102\n"
        "v_mfma_f32_16x16x32_bf16 v[104:107], v[228:231], v[184:187], v[104:107]\n"
        "v_exp_f32 v103, v103\n"
        "v_mfma_f32_16x16x32_bf16 v[120:123], a[220:223], a[212:215], 0\n"
        "v_cvt_pk_bf16_f32 v70, v100, v101\n"
        "v_cvt_pk_bf16_f32 v71, v102, v103\n"
        "v_mfma_f32_16x16x32_bf16 v[120:123], a[216:219], a[208:211], v[120:123]\n"
        "v_cvt_pk_bf16_f32 v142, v116, v117\n"
        "v_cvt_pk_bf16_f32 v143, v118, v119\n"
        "v_mfma_f32_16x16x32_bf16 v[120:123], a[248:251], a[204:207], v[120:123]\n"
        "v_mov_b64 v[96:97], 0\n"
        "v_mfma_f32_16x16x32_bf16 v[120:123], a[244:247], a[200:203], v[120:123]\n"
        "v_exp_f32 v104, v104\n"
        "v_mfma_f32_16x16x32_bf16 v[120:123], a[240:243], a[196:199], v[120:123]\n"
        "v_exp_f32 v105, v105\n"
        "v_mfma_f32_16x16x32_bf16 v[120:123], a[236:239], a[192:195], v[120:123]\n"
        "v_exp_f32 v106, v106\n"
        "v_mfma_f32_16x16x32_bf16 v[108:111], v[212:215], v[196:199], v[44:47]\n"
        "v_exp_f32 v107, v107\n"
        "v_mfma_f32_16x16x32_bf16 v[108:111], v[228:231], v[204:207], v[108:111]\n"
        "v_cvt_pk_bf16_f32 v78, v104, v105\n"
        "v_cvt_pk_bf16_f32 v79, v106, v107\n"
        "v_mfma_f32_16x16x32_bf16 v[112:115], a[0:3], a[236:239], 0\n"
        "s_nop 0\n"
        "v_cvt_pk_bf16_f32 v150, v120, v121\n"
        "v_cvt_pk_bf16_f32 v151, v122, v123\n"
        "v_mfma_f32_16x16x32_bf16 v[112:115], a[4:7], a[240:243], v[112:115]\n"
        "v_mfma_f32_16x16x32_bf16 v[112:115], a[8:11], a[244:247], v[112:115]\n"
        "v_exp_f32 v108, v108\n"
        "v_mfma_f32_16x16x32_bf16 v[112:115], a[12:15], a[248:251], v[112:115]\n"
        "v_exp_f32 v109, v109\n"
        "v_mfma_f32_16x16x32_bf16 v[112:115], a[16:19], a[216:219], v[112:115]\n"
        "v_exp_f32 v110, v110\n"
        "v_mfma_f32_16x16x32_bf16 v[112:115], a[20:23], a[220:223], v[112:115]\n"
        "v_exp_f32 v111, v111\n"
        "v_smfmac_f32_16x16x64_bf16 v[96:99], a[252:255], v[64:71], v237\n"
        "v_cvt_pk_bf16_f32 v86, v108, v109\n"
        "v_mfma_f32_16x16x32_bf16 v[100:103], v[132:135], v[68:71], 0\n"
        "v_mov_b64 v[124:125], 0\n"
        "v_cvt_pk_bf16_f32 v87, v110, v111\n"
        "v_mfma_f32_16x16x32_bf16 v[100:103], v[128:131], v[64:67], v[100:103]\n"
        "s_nop 0\n"
        "v_cvt_pk_bf16_f32 v216, v112, v113\n"
        "v_cvt_pk_bf16_f32 v217, v114, v115\n"
        "v_mfma_f32_16x16x32_bf16 v[104:107], v[136:139], v[64:67], 0\n"
        "ds_read_b128 v[222:225], v251 offset:4608\n"
        "v_add_f32 v97, v96, v97\n"
        "v_mfma_f32_16x16x32_bf16 v[104:107], v[140:143], v[68:71], v[104:107]\n"
        "v_rcp_f32 v96, v97\n"
        "v_mfma_f32_16x16x32_bf16 v[108:111], v[148:151], v[68:71], 0\n"
        "v_mul_f32 v100, v96, v100\n"
        "v_mul_f32 v101, v96, v101\n"
        "v_mfma_f32_16x16x32_bf16 v[108:111], v[144:147], v[64:67], v[108:111]\n"
        "v_mul_f32 v102, v96, v102\n"
        "v_mul_f32 v103, v96, v103\n"
        "v_mfma_f32_16x16x32_bf16 v[116:119], a[44:47], a[220:223], 0\n"
        "v_cvt_pk_bf16_f32 v100, v100, v101\n"
        "v_cvt_pk_bf16_f32 v101, v102, v103\n"
        "v_mfma_f32_16x16x32_bf16 v[116:119], a[40:43], a[216:219], v[116:119]\n"
        "v_mul_f32 v104, v96, v104\n"
        "v_mul_f32 v105, v96, v105\n"
        "v_mfma_f32_16x16x32_bf16 v[116:119], a[36:39], a[248:251], v[116:119]\n"
        "v_mul_f32 v106, v96, v106\n"
        "v_mul_f32 v107, v96, v107\n"
        "v_mfma_f32_16x16x32_bf16 v[116:119], a[32:35], a[244:247], v[116:119]\n"
        "v_cvt_pk_bf16_f32 v102, v104, v105\n"
        "v_cvt_pk_bf16_f32 v103, v106, v107\n"
        "v_mfma_f32_16x16x32_bf16 v[116:119], a[28:31], a[240:243], v[116:119]\n"
        "v_mul_f32 v108, v96, v108\n"
        "v_mul_f32 v109, v96, v109\n"
        "v_mfma_f32_16x16x32_bf16 v[116:119], a[24:27], a[236:239], v[116:119]\n"
        "v_mul_f32 v110, v96, v110\n"
        "v_mul_f32 v111, v96, v111\n"
        "v_smfmac_f32_16x16x64_bf16 v[124:127], a[252:255], v[72:79], v237\n"
        "v_cvt_pk_bf16_f32 v108, v108, v109\n"
        "v_mfma_f32_16x16x32_bf16 v[64:67], v[132:135], v[76:79], 0\n"
        "ds_write_b32 v245, v108 offset:4096\n"
        "v_mfma_f32_16x16x32_bf16 v[64:67], v[128:131], v[72:75], v[64:67]\n"
        "ds_read_b128 v[156:159], v247 offset:0\n"
        "v_cvt_pk_bf16_f32 v218, v116, v117\n"
        "v_mfma_f32_16x16x32_bf16 v[68:71], v[136:139], v[72:75], 0\n"
        "ds_read_b128 v[160:163], v247 offset:4096\n"
        "v_cvt_pk_bf16_f32 v219, v118, v119\n"
        "v_mfma_f32_16x16x32_bf16 v[68:71], v[140:143], v[76:79], v[68:71]\n"
        "ds_read_b128 v[164:167], v247 offset:4352\n"
        "v_cvt_pk_bf16_f32 v109, v110, v111\n"
        "v_mfma_f32_16x16x32_bf16 v[120:123], v[148:151], v[76:79], 0\n"
        "ds_write_b32 v245, v109 offset:5120\n"
        "v_mfma_f32_16x16x32_bf16 v[120:123], v[144:147], v[72:75], v[120:123]\n"
        "v_max_u32 v236, v96, v97\n"
        "v_mov_b64 v[116:117], 0\n"
        "v_mfma_f32_16x16x32_bf16 v[112:115], a[48:51], a[236:239], 0\n"
        "v_add_f32 v125, v124, v125\n"
        "v_mfma_f32_16x16x32_bf16 v[112:115], a[52:55], a[240:243], v[112:115]\n"
        "v_rcp_f32 v124, v125\n"
        "v_mfma_f32_16x16x32_bf16 v[112:115], a[56:59], a[244:247], v[112:115]\n"
        "v_mul_f32 v64, v124, v64\n"
        "v_mul_f32 v65, v124, v65\n"
        "v_mfma_f32_16x16x32_bf16 v[112:115], a[60:63], a[248:251], v[112:115]\n"
        "v_mul_f32 v66, v124, v66\n"
        "v_mul_f32 v67, v124, v67\n"
        "v_mfma_f32_16x16x32_bf16 v[112:115], a[64:67], a[216:219], v[112:115]\n"
        "v_cvt_pk_bf16_f32 v64, v64, v65\n"
        "v_cvt_pk_bf16_f32 v65, v66, v67\n"
        "v_mfma_f32_16x16x32_bf16 v[112:115], a[68:71], a[220:223], v[112:115]\n"
        "ds_read_b128 a[216:219], v248 offset:0\n"
        "v_mul_f32 v68, v124, v68\n"
        "v_smfmac_f32_16x16x64_bf16 v[116:119], a[252:255], v[80:87], v237\n"
        "ds_read_b128 a[220:223], v248 offset:3072\n"
        "v_mfma_f32_16x16x32_bf16 v[72:75], v[132:135], v[84:87], 0\n"
        "v_mul_f32 v69, v124, v69\n"
        "v_mul_f32 v70, v124, v70\n"
        "v_mfma_f32_16x16x32_bf16 v[72:75], v[128:131], v[80:83], v[72:75]\n"
        "v_cvt_pk_bf16_f32 v220, v112, v113\n"
        "v_cvt_pk_bf16_f32 v221, v114, v115\n"
        "v_mfma_f32_16x16x32_bf16 v[76:79], v[136:139], v[80:83], 0\n"
        "ds_read_b128 a[224:227], v248 offset:32\n"
        "v_cvt_pk_bf16_f32 v226, v112, v113\n"
        "v_mfma_f32_16x16x32_bf16 v[76:79], v[140:143], v[84:87], v[76:79]\n"
        "ds_read_b128 a[228:231], v248 offset:3104\n"
        "v_cvt_pk_bf16_f32 v227, v114, v115\n"
        "v_mfma_f32_16x16x32_bf16 v[96:99], v[148:151], v[84:87], 0\n"
        "v_mul_f32 v71, v124, v71\n"
        "v_cvt_pk_bf16_f32 v66, v68, v69\n"
        "v_mfma_f32_16x16x32_bf16 v[96:99], v[144:147], v[80:83], v[96:99]\n"
        "v_cvt_pk_bf16_f32 v67, v70, v71\n"
        "v_mul_f32 v120, v124, v120\n"
        "v_mfma_f32_16x16x32_bf16 v[104:107], v[152:155], v[216:219], v[48:51]\n"
        "s_waitcnt lgkmcnt(2)\n"
        "ds_read_b128 v[152:155], v247 offset:256\n"
        "v_mul_f32 v121, v124, v121\n"
        "v_mfma_f32_16x16x32_bf16 v[104:107], v[168:171], v[220:223], v[104:107]\n"
        "v_mul_f32 v122, v124, v122\n"
        "v_mul_f32 v123, v124, v123\n"
        "v_mfma_f32_16x16x32_bf16 v[108:111], v[172:175], v[216:219], v[52:55]\n"
        "v_cvt_pk_bf16_f32 v120, v120, v121\n"
        "v_cvt_pk_bf16_f32 v121, v122, v123\n"
        "v_mfma_f32_16x16x32_bf16 v[108:111], v[188:191], v[224:227], v[108:111]\n"
        "ds_read_b128 v[222:225], v252 offset:4608\n"
        "v_max3_u32 v236, v236, v124, v125\n"
        "v_mfma_f32_16x16x32_bf16 v[176:179], v[156:159], a[216:219], 0\n"
        "ds_write_b32 v245, v120 offset:4352\n"
        "v_mfma_f32_16x16x32_bf16 v[176:179], v[160:163], a[220:223], v[176:179]\n"
        "ds_write_b32 v245, v121 offset:5376\n"
        "v_mfma_f32_16x16x32_bf16 v[196:199], v[164:167], a[220:223], 0\n"
        "s_waitcnt lgkmcnt(3)\n"
        "ds_read_b128 a[232:235], v254 offset:0\n"
        "v_add_f32 v117, v116, v117\n"
        "v_mfma_f32_16x16x32_bf16 v[196:199], v[152:155], a[216:219], v[196:199]\n"
        "ds_read_b128 a[220:223], v254 offset:3072\n"
        "v_mfma_f32_16x16x32_bf16 v[200:203], v[152:155], a[224:227], 0\n"
        "ds_read_b128 a[216:219], v254 offset:32\n"
        "v_mfma_f32_16x16x32_bf16 v[200:203], v[164:167], a[228:231], v[200:203]\n"
        "v_rcp_f32 v116, v117\n"
        "v_mfma_f32_16x16x32_bf16 v[180:183], v[160:163], a[228:231], 0\n"
        "ds_read_b128 a[228:231], v254 offset:3104\n"
        "v_mul_f32 v72, v116, v72\n"
        "v_mfma_f32_16x16x32_bf16 v[180:183], v[156:159], a[224:227], v[180:183]\n"
        "ds_read_b128 a[236:239], v247 offset:8192\n"
        "v_mul_f32 v73, v116, v73\n"
        "v_mfma_f32_16x16x32_bf16 v[112:115], v[192:195], v[216:219], v[56:59]\n"
        "s_waitcnt lgkmcnt(7)\n"
        "ds_read_b128 a[224:227], v248 offset:16\n"
        "v_mul_f32 v74, v116, v74\n"
        "v_mfma_f32_16x16x32_bf16 v[112:115], v[208:211], v[220:223], v[112:115]\n"
        "ds_read_b128 a[240:243], v247 offset:12288\n"
        "v_mul_f32 v75, v116, v75\n"
        "v_mfma_f32_16x16x32_bf16 v[68:71], v[212:215], v[216:219], v[60:63]\n"
        "ds_read_b128 a[244:247], v247 offset:12544\n"
        "v_cvt_pk_bf16_f32 v72, v72, v73\n"
        "v_mfma_f32_16x16x32_bf16 v[68:71], v[228:231], v[224:227], v[68:71]\n"
        "s_waitcnt lgkmcnt(4)\n"
        "ds_read_b128 a[248:251], v247 offset:8448\n"
        "v_cvt_pk_bf16_f32 v73, v74, v75\n"
        "v_mfma_f32_16x16x32_bf16 v[184:187], v[156:159], a[232:235], 0\n"
        "v_mul_f32 v76, v116, v76\n"
        "v_mul_f32 v77, v116, v77\n"
        "v_mfma_f32_16x16x32_bf16 v[184:187], v[160:163], a[220:223], v[184:187]\n"
        "v_mul_f32 v78, v116, v78\n"
        "v_mul_f32 v79, v116, v79\n"
        "v_mfma_f32_16x16x32_bf16 v[204:207], v[164:167], a[220:223], 0\n"
        "ds_read_b128 a[220:223], v248 offset:3088\n"
        "v_cvt_pk_bf16_f32 v74, v76, v77\n"
        "v_mfma_f32_16x16x32_bf16 v[204:207], v[152:155], a[232:235], v[204:207]\n"
        "ds_read_b128 a[232:235], v254 offset:16\n"
        "v_cvt_pk_bf16_f32 v75, v78, v79\n"
        "v_mfma_f32_16x16x32_bf16 v[192:195], v[152:155], a[216:219], 0\n"
        "v_exp_f32 v104, v104\n"
        "v_mfma_f32_16x16x32_bf16 v[192:195], v[164:167], a[228:231], v[192:195]\n"
        "v_mul_f32 v96, v116, v96\n"
        "v_mul_f32 v97, v116, v97\n"
        "v_mfma_f32_16x16x32_bf16 v[172:175], v[160:163], a[228:231], 0\n"
        "ds_read_b128 a[228:231], v254 offset:3088\n"
        "v_mul_f32 v98, v116, v98\n"
        "v_mfma_f32_16x16x32_bf16 v[172:175], v[156:159], a[216:219], v[172:175]\n"
        "s_waitcnt lgkmcnt(2)\n"
        "ds_read_b128 a[216:219], v248 offset:6144\n"
        "v_mul_f32 v99, v116, v99\n"
        "v_mfma_f32_16x16x32_bf16 v[212:215], v[156:159], a[224:227], 0\n"
        "v_exp_f32 v105, v105\n"
        "v_mfma_f32_16x16x32_bf16 v[212:215], v[160:163], a[220:223], v[212:215]\n"
        "v_exp_f32 v106, v106\n"
        "v_mfma_f32_16x16x32_bf16 v[216:219], v[164:167], a[220:223], 0\n"
        "ds_read_b128 a[220:223], v248 offset:9216\n"
        "v_cvt_pk_bf16_f32 v88, v104, v105\n"
        "v_mfma_f32_16x16x32_bf16 v[216:219], v[152:155], a[224:227], v[216:219]\n"
        "s_waitcnt lgkmcnt(2)\n"
        "ds_read_b128 a[224:227], v248 offset:6176\n"
        "v_cvt_pk_bf16_f32 v96, v96, v97\n"
        "v_mfma_f32_16x16x32_bf16 v[224:227], v[152:155], a[232:235], 0\n"
        "v_exp_f32 v108, v108\n"
        "v_mfma_f32_16x16x32_bf16 v[224:227], v[164:167], a[228:231], v[224:227]\n"
        "v_cvt_pk_bf16_f32 v97, v98, v99\n"
        "v_max3_u32 v236, v236, v116, v117\n"
        "v_mfma_f32_16x16x32_bf16 v[220:223], v[160:163], a[228:231], 0\n"
        "ds_read_b128 a[228:231], v248 offset:9248\n"
        "v_mfma_f32_16x16x32_bf16 v[220:223], v[156:159], a[232:235], v[220:223]\n"
        "s_waitcnt lgkmcnt(2)\n"
        "ds_read_b128 a[232:235], v254 offset:6144\n"
        "v_mfma_f32_16x16x32_bf16 v[176:179], a[236:239], a[216:219], v[176:179]\n"
        "v_exp_f32 v107, v107\n"
        "v_mfma_f32_16x16x32_bf16 v[176:179], a[240:243], a[220:223], v[176:179]\n"
        "v_exp_f32 v109, v109\n"
        "v_mfma_f32_16x16x32_bf16 v[196:199], a[244:247], a[220:223], v[196:199]\n"
        "ds_read_b128 a[220:223], v254 offset:9216\n"
        "v_cvt_pk_bf16_f32 v89, v106, v107\n"
        "v_mfma_f32_16x16x32_bf16 v[196:199], a[248:251], a[216:219], v[196:199]\n"
        "s_waitcnt lgkmcnt(2)\n"
        "ds_read_b128 a[216:219], v254 offset:6176\n"
        "v_cvt_pk_bf16_f32 v90, v108, v109\n"
        "v_mfma_f32_16x16x32_bf16 v[200:203], a[248:251], a[224:227], v[200:203]\n"
        "ds_read_b128 v[156:159], v247 offset:16384\n"
        "v_mfma_f32_16x16x32_bf16 v[200:203], a[244:247], a[228:231], v[200:203]\n"
        "ds_read_b128 v[160:163], v247 offset:20480\n"
        "v_mfma_f32_16x16x32_bf16 v[180:183], a[240:243], a[228:231], v[180:183]\n"
        "ds_read_b128 a[228:231], v254 offset:9248\n"
        "v_mfma_f32_16x16x32_bf16 v[180:183], a[236:239], a[224:227], v[180:183]\n"
        "s_waitcnt lgkmcnt(4)\n"
        "ds_read_b128 a[224:227], v248 offset:6160\n"
        "v_mfma_f32_16x16x32_bf16 v[184:187], a[236:239], a[232:235], v[184:187]\n"
        "ds_read_b128 v[164:167], v247 offset:20736\n"
        "v_mfma_f32_16x16x32_bf16 v[184:187], a[240:243], a[220:223], v[184:187]\n"
        "ds_read_b128 v[152:155], v247 offset:16640\n"
        "v_mfma_f32_16x16x32_bf16 v[204:207], a[244:247], a[220:223], v[204:207]\n"
        "ds_read_b128 a[220:223], v248 offset:9232\n"
        "v_mfma_f32_16x16x32_bf16 v[204:207], a[248:251], a[232:235], v[204:207]\n"
        "s_waitcnt lgkmcnt(4)\n"
        "ds_read_b128 a[232:235], v254 offset:6160\n"
        "v_mfma_f32_16x16x32_bf16 v[192:195], a[248:251], a[216:219], v[192:195]\n"
        "v_exp_f32 v110, v110\n"
        "v_mfma_f32_16x16x32_bf16 v[192:195], a[244:247], a[228:231], v[192:195]\n"
        "v_exp_f32 v111, v111\n"
        "v_mfma_f32_16x16x32_bf16 v[172:175], a[240:243], a[228:231], v[172:175]\n"
        "ds_read_b128 a[228:231], v254 offset:9232\n"
        "v_cvt_pk_bf16_f32 v91, v110, v111\n"
        "v_mfma_f32_16x16x32_bf16 v[172:175], a[236:239], a[216:219], v[172:175]\n"
        "s_waitcnt lgkmcnt(2)\n"
        "ds_read_b128 a[216:219], v248 offset:12288\n"
        "v_mfma_f32_16x16x32_bf16 v[212:215], a[236:239], a[224:227], v[212:215]\n"
        "v_exp_f32 v112, v112\n"
        "v_mfma_f32_16x16x32_bf16 v[212:215], a[240:243], a[220:223], v[212:215]\n"
        "v_exp_f32 v113, v113\n"
        "v_mfma_f32_16x16x32_bf16 v[216:219], a[244:247], a[220:223], v[216:219]\n"
        ".long 0xbec200ff\n"
        "44:\n"
        "s_branch 60b\n"
        "ds_read_b128 a[220:223], v248 offset:15360\n"
        "v_cvt_pk_bf16_f32 v92, v112, v113\n"
        "v_mfma_f32_16x16x32_bf16 v[216:219], a[248:251], a[224:227], v[216:219]\n"
        "s_waitcnt lgkmcnt(2)\n"
        "ds_read_b128 a[224:227], v248 offset:12320\n"
        "v_mfma_f32_16x16x32_bf16 v[224:227], a[248:251], a[232:235], v[224:227]\n"
        "v_exp_f32 v114, v114\n"
        "v_mfma_f32_16x16x32_bf16 v[224:227], a[244:247], a[228:231], v[224:227]\n"
        "v_exp_f32 v68, v68\n"
        "v_mfma_f32_16x16x32_bf16 v[220:223], a[240:243], a[228:231], v[220:223]\n"
        "ds_read_b128 a[228:231], v248 offset:15392\n"
        "v_mfma_f32_16x16x32_bf16 v[220:223], a[236:239], a[232:235], v[220:223]\n"
        "s_waitcnt lgkmcnt(2)\n"
        "ds_read_b128 a[232:235], v254 offset:12288\n"
        "v_mfma_f32_16x16x32_bf16 v[176:179], v[156:159], a[216:219], v[176:179]\n"
        "v_exp_f32 v115, v115\n"
        "v_mfma_f32_16x16x32_bf16 v[176:179], v[160:163], a[220:223], v[176:179]\n"
        "v_exp_f32 v69, v69\n"
        "v_mfma_f32_16x16x32_bf16 v[196:199], v[164:167], a[220:223], v[196:199]\n"
        "ds_read_b128 a[220:223], v254 offset:15360\n"
        "v_cvt_pk_bf16_f32 v93, v114, v115\n"
        "v_mfma_f32_16x16x32_bf16 v[196:199], v[152:155], a[216:219], v[196:199]\n"
        "s_waitcnt lgkmcnt(2)\n"
        "ds_read_b128 a[216:219], v254 offset:12320\n"
        "v_cvt_pk_bf16_f32 v94, v68, v69\n"
        "v_mfma_f32_16x16x32_bf16 v[200:203], v[152:155], a[224:227], v[200:203]\n"
        "v_exp_f32 v70, v70\n"
        "v_mfma_f32_16x16x32_bf16 v[200:203], v[164:167], a[228:231], v[200:203]\n"
        "s_waitcnt vmcnt(0)\n"
        "v_exp_f32 v71, v71\n"
        "v_mfma_f32_16x16x32_bf16 v[180:183], v[160:163], a[228:231], v[180:183]\n"
        "ds_read_b128 a[228:231], v254 offset:15392\n"
        "v_cvt_pk_bf16_f32 v95, v70, v71\n"
        "v_mfma_f32_16x16x32_bf16 v[180:183], v[156:159], a[224:227], v[180:183]\n"
        "s_waitcnt lgkmcnt(2)\n"
        "ds_read_b128 a[224:227], v248 offset:12304\n"
        "v_mfma_f32_16x16x32_bf16 v[184:187], v[156:159], a[232:235], v[184:187]\n"
        "v_mfma_f32_16x16x32_bf16 v[184:187], v[160:163], a[220:223], v[184:187]\n"
        "s_barrier\n"
        "v_mfma_f32_16x16x32_bf16 v[204:207], v[164:167], a[220:223], v[204:207]\n"
        "ds_read_b128 a[220:223], v248 offset:15376\n"
        "v_mfma_f32_16x16x32_bf16 v[204:207], v[152:155], a[232:235], v[204:207]\n"
        "s_waitcnt lgkmcnt(2)\n"
        "ds_read_b128 a[232:235], v254 offset:12304\n"
        "v_mfma_f32_16x16x32_bf16 v[192:195], v[152:155], a[216:219], v[192:195]\n"
        "ds_read_b128 a[236:239], v240 offset:0\n"
        "v_mfma_f32_16x16x32_bf16 v[192:195], v[164:167], a[228:231], v[192:195]\n"
        "ds_read_b128 a[240:243], v241 offset:0\n"
        "v_mfma_f32_16x16x32_bf16 v[172:175], v[160:163], a[228:231], v[172:175]\n"
        "ds_read_b128 a[228:231], v254 offset:15376\n"
        "v_mfma_f32_16x16x32_bf16 v[172:175], v[156:159], a[216:219], v[172:175]\n"
        "s_waitcnt lgkmcnt(4)\n"
        "ds_read_b128 a[244:247], v240 offset:128\n"
        "v_mfma_f32_16x16x32_bf16 v[212:215], v[156:159], a[224:227], v[212:215]\n"
        "ds_read_b128 a[248:251], v241 offset:128\n"
        "v_mfma_f32_16x16x32_bf16 v[212:215], v[160:163], a[220:223], v[212:215]\n"
        "ds_read_b128 a[216:219], v240 offset:256\n"
        "v_mfma_f32_16x16x32_bf16 v[216:219], v[164:167], a[220:223], v[216:219]\n"
        "ds_read_b128 a[220:223], v241 offset:256\n"
        "v_mfma_f32_16x16x32_bf16 v[216:219], v[152:155], a[224:227], v[216:219]\n"
        "s_waitcnt lgkmcnt(4)\n"
        "v_mfma_f32_16x16x32_bf16 v[224:227], v[152:155], a[232:235], v[224:227]\n"
        "v_mfma_f32_16x16x32_bf16 v[224:227], v[164:167], a[228:231], v[224:227]\n"
        "ds_write_b32 v245, v96 offset:4608\n"
        "v_mfma_f32_16x16x32_bf16 v[220:223], v[160:163], a[228:231], v[220:223]\n"
        "v_mfma_f32_16x16x32_bf16 v[220:223], v[156:159], a[232:235], v[220:223]\n"
        "ds_write_b32 v245, v97 offset:5632\n"
        "v_mfma_f32_16x16x32_bf16 v[112:115], a[72:75], a[236:239], 0\n"
        "v_mov_b64 v[124:125], 0\n"
        "v_cvt_pk_bf16_f32 v232, v176, v180\n"
        "v_mfma_f32_16x16x32_bf16 v[112:115], a[76:79], a[240:243], v[112:115]\n"
        "s_waitcnt lgkmcnt(2)\n"
        "v_cvt_pk_bf16_f32 v233, v184, v172\n"
        "v_cvt_pk_bf16_f32 v234, v177, v181\n"
        "v_mfma_f32_16x16x32_bf16 v[112:115], a[80:83], a[244:247], v[112:115]\n"
        "buffer_store_dwordx2 v[232:233], v250, s[56:59], 0 offen offset:0 sc1\n"
        "v_cvt_pk_bf16_f32 v235, v185, v173\n"
        "v_mfma_f32_16x16x32_bf16 v[112:115], a[84:87], a[248:251], v[112:115]\n"
        "v_cvt_pk_bf16_f32 v232, v178, v182\n"
        "v_cvt_pk_bf16_f32 v233, v186, v174\n"
        "v_mfma_f32_16x16x32_bf16 v[112:115], a[88:91], a[216:219], v[112:115]\n"
        "v_mfma_f32_16x16x32_bf16 v[112:115], a[92:95], a[220:223], v[112:115]\n"
        "ds_write_b32 v245, v100 offset:0\n"
        "v_smfmac_f32_16x16x64_bf16 v[124:127], a[252:255], v[88:95], v237\n"
        "s_nop 1\n"
        "v_mfma_f32_16x16x32_bf16 v[80:83], v[132:135], v[92:95], 0\n"
        "buffer_store_dwordx2 v[234:235], v250, s[56:59], 0 offen offset:384 sc1\n"
        "s_nop 0\n"
        "v_mfma_f32_16x16x32_bf16 v[80:83], v[128:131], v[88:91], v[80:83]\n"
        "s_add_u32 s41, s30, s40\n"
        "v_cvt_pk_bf16_f32 v152, v112, v113\n"
        "v_cvt_pk_bf16_f32 v153, v114, v115\n"
        "v_mfma_f32_16x16x32_bf16 v[84:87], v[136:139], v[88:91], 0\n"
        "v_cvt_pk_bf16_f32 v234, v179, v183\n"
        "v_cvt_pk_bf16_f32 v235, v187, v175\n"
        "v_mfma_f32_16x16x32_bf16 v[84:87], v[140:143], v[92:95], v[84:87]\n"
        "v_add_f32 v125, v124, v125\n"
        "v_mfma_f32_16x16x32_bf16 v[96:99], v[148:151], v[92:95], 0\n"
        "buffer_store_dwordx2 v[232:233], v250, s[56:59], 0 offen offset:768 sc1\n"
        "v_mfma_f32_16x16x32_bf16 v[96:99], v[144:147], v[88:91], v[96:99]\n"
        "s_add_u32 s35, s47, 786432\n"
        "v_rcp_f32 v124, v125\n"
        "v_mfma_f32_16x16x32_bf16 v[116:119], a[116:119], a[220:223], 0\n"
        "s_sub_u32 s46, s35, 100663296\n"
        "v_mul_f32 v80, v124, v80\n"
        "v_mul_f32 v81, v124, v81\n"
        "v_mfma_f32_16x16x32_bf16 v[116:119], a[112:115], a[216:219], v[116:119]\n"
        "s_cmp_lt_u32 s46, 786432\n"
        "v_mul_f32 v82, v124, v82\n"
        "v_mul_f32 v83, v124, v83\n"
        "v_mfma_f32_16x16x32_bf16 v[116:119], a[108:111], a[248:251], v[116:119]\n"
        "buffer_store_dwordx2 v[234:235], v250, s[56:59], 0 offen offset:1152 sc1\n"
        "v_cvt_pk_bf16_f32 v80, v80, v81\n"
        "v_mfma_f32_16x16x32_bf16 v[116:119], a[104:107], a[244:247], v[116:119]\n"
        "s_cselect_b32 s35, s46, s35\n"
        "v_cvt_pk_bf16_f32 v81, v82, v83\n"
        "v_cvt_pk_bf16_f32 v232, v196, v200\n"
        "v_mfma_f32_16x16x32_bf16 v[116:119], a[100:103], a[240:243], v[116:119]\n"
        "v_cvt_pk_bf16_f32 v233, v204, v192\n"
        "v_mul_f32 v84, v124, v84\n"
        "v_mfma_f32_16x16x32_bf16 v[116:119], a[96:99], a[236:239], v[116:119]\n"
        "v_mul_f32 v85, v124, v85\n"
        "v_mul_f32 v86, v124, v86\n"
        "v_mfma_f32_16x16x32_bf16 v[120:123], a[120:123], a[236:239], 0\n"
        "ds_write_b32 v245, v101 offset:1024\n"
        "v_mfma_f32_16x16x32_bf16 v[120:123], a[124:127], a[240:243], v[120:123]\n"
        "buffer_store_dwordx2 v[232:233], v250, s[60:63], 0 offen offset:0 sc1\n"
        "v_mul_f32 v87, v124, v87\n"
        "v_mfma_f32_16x16x32_bf16 v[120:123], a[128:131], a[244:247], v[120:123]\n"
        "v_cvt_pk_bf16_f32 v82, v84, v85\n"
        "v_cvt_pk_bf16_f32 v83, v86, v87\n"
        "v_mfma_f32_16x16x32_bf16 v[120:123], a[132:135], a[248:251], v[120:123]\n"
        "v_mul_f32 v96, v124, v96\n"
        "v_mul_f32 v97, v124, v97\n"
        "v_mfma_f32_16x16x32_bf16 v[120:123], a[136:139], a[216:219], v[120:123]\n"
        "v_mul_f32 v98, v124, v98\n"
        "v_mul_f32 v99, v124, v99\n"
        "v_mfma_f32_16x16x32_bf16 v[120:123], a[140:143], a[220:223], v[120:123]\n"
        "v_cvt_pk_bf16_f32 v234, v197, v201\n"
        "v_cvt_pk_bf16_f32 v235, v205, v193\n"
        "v_mfma_f32_16x16x32_bf16 v[112:115], a[20:23], a[220:223], 0\n"
        "buffer_store_dwordx2 v[234:235], v250, s[60:63], 0 offen offset:384 sc1\n"
        "v_cvt_pk_bf16_f32 v154, v116, v117\n"
        "v_mfma_f32_16x16x32_bf16 v[112:115], a[16:19], a[216:219], v[112:115]\n"
        "v_cvt_pk_bf16_f32 v155, v118, v119\n"
        "v_cvt_pk_bf16_f32 v96, v96, v97\n"
        "v_mfma_f32_16x16x32_bf16 v[112:115], a[12:15], a[248:251], v[112:115]\n"
        "ds_write_b32 v245, v96 offset:4864\n"
        "v_mfma_f32_16x16x32_bf16 v[112:115], a[8:11], a[244:247], v[112:115]\n"
        "v_cvt_pk_bf16_f32 v97, v98, v99\n"
        "v_max3_u32 v236, v236, v124, v125\n"
        "v_mfma_f32_16x16x32_bf16 v[112:115], a[4:7], a[240:243], v[112:115]\n"
        "v_cvt_pk_bf16_f32 v232, v198, v202\n"
        "v_cvt_pk_bf16_f32 v233, v206, v194\n"
        "v_mfma_f32_16x16x32_bf16 v[112:115], a[0:3], a[236:239], v[112:115]\n"
        "buffer_store_dwordx2 v[232:233], v250, s[60:63], 0 offen offset:768 sc1\n"
        "v_cmp_lt_u32 vcc, s24, v236\n"
        "v_mfma_f32_16x16x32_bf16 v[116:119], a[24:27], a[236:239], 0\n"
        "v_cvt_pk_bf16_f32 v168, v120, v121\n"
        "v_cvt_pk_bf16_f32 v169, v122, v123\n"
        "v_mfma_f32_16x16x32_bf16 v[116:119], a[28:31], a[240:243], v[116:119]\n"
        "s_cmp_lg_u64 vcc, 0\n"
        "s_addc_u32 s25, s25, s25\n"
        "v_cvt_pk_bf16_f32 v234, v199, v203\n"
        "v_mfma_f32_16x16x32_bf16 v[116:119], a[32:35], a[244:247], v[116:119]\n"
        "v_cvt_pk_bf16_f32 v235, v207, v195\n"
        "v_cvt_pk_bf16_f32 v156, v112, v113\n"
        "v_mfma_f32_16x16x32_bf16 v[116:119], a[36:39], a[248:251], v[116:119]\n"
        "buffer_store_dwordx2 v[234:235], v250, s[60:63], 0 offen offset:1152 sc1\n"
        "v_cvt_pk_bf16_f32 v157, v114, v115\n"
        "v_mfma_f32_16x16x32_bf16 v[116:119], a[40:43], a[216:219], v[116:119]\n"
        "v_cvt_pk_bf16_f32 v232, v212, v220\n"
        "v_cvt_pk_bf16_f32 v234, v213, v221\n"
        "v_mfma_f32_16x16x32_bf16 v[116:119], a[44:47], a[220:223], v[116:119]\n"
        "ds_write_b32 v245, v102 offset:2048\n"
        "v_mfma_f32_16x16x32_bf16 v[120:123], a[68:71], a[220:223], 0\n"
        "v_xor_b32 v251, 0x2000, v251\n"
        "v_xor_b32 v252, 0x2000, v252\n"
        "v_mfma_f32_16x16x32_bf16 v[120:123], a[64:67], a[216:219], v[120:123]\n"
        "buffer_store_dword v232, v238, s[56:59], 0 offen offset:0 sc1\n"
        "v_mfma_f32_16x16x32_bf16 v[120:123], a[60:63], a[248:251], v[120:123]\n"
        "ds_read_b128 a[224:227], v240 offset:1536\n"
        "v_cvt_pk_bf16_f32 v158, v116, v117\n"
        "v_mfma_f32_16x16x32_bf16 v[120:123], a[56:59], a[244:247], v[120:123]\n"
        "ds_read_b128 a[228:231], v241 offset:1536\n"
        "v_cvt_pk_bf16_f32 v159, v118, v119\n"
        "v_mfma_f32_16x16x32_bf16 v[120:123], a[52:55], a[240:243], v[120:123]\n"
        "ds_read_b128 a[232:235], v240 offset:1664\n"
        "v_cvt_pk_bf16_f32 v232, v214, v222\n"
        "v_mfma_f32_16x16x32_bf16 v[120:123], a[48:51], a[236:239], v[120:123]\n"
        "buffer_store_dword v234, v238, s[56:59], 0 offen offset:384 sc1\n"
        "v_mfma_f32_16x16x32_bf16 v[112:115], a[236:239], a[144:147], 0\n"
        "v_add_u32 v247, s45, v247\n"
        "v_cvt_pk_bf16_f32 v234, v215, v223\n"
        "v_mfma_f32_16x16x32_bf16 v[112:115], a[240:243], a[148:151], v[112:115]\n"
        "ds_write_b32 v245, v103 offset:3072\n"
        "v_mfma_f32_16x16x32_bf16 v[112:115], a[244:247], a[152:155], v[112:115]\n"
        "s_nop 0\n"
        "v_cvt_pk_bf16_f32 v160, v120, v121\n"
        "v_cvt_pk_bf16_f32 v161, v122, v123\n"
        "v_mfma_f32_16x16x32_bf16 v[112:115], a[248:251], a[156:159], v[112:115]\n"
        "v_cvt_pk_bf16_f32 v166, v120, v121\n"
        "v_cvt_pk_bf16_f32 v167, v122, v123\n"
        "v_mfma_f32_16x16x32_bf16 v[112:115], a[216:219], a[160:163], v[112:115]\n"
        "buffer_store_dword v232, v238, s[56:59], 0 offen offset:768 sc1\n"
        "v_mfma_f32_16x16x32_bf16 v[112:115], a[220:223], a[164:167], v[112:115]\n"
        "ds_write_b32 v245, v97 offset:5888\n"
        "v_mfma_f32_16x16x32_bf16 v[116:119], a[220:223], a[188:191], 0\n"
        "v_cvt_pk_bf16_f32 v232, v216, v224\n"
        "s_nop 0\n"
        "v_mfma_f32_16x16x32_bf16 v[116:119], a[216:219], a[184:187], v[116:119]\n"
        "s_nop 2\n"
        "v_mfma_f32_16x16x32_bf16 v[116:119], a[248:251], a[180:183], v[116:119]\n"
        "buffer_store_dword v234, v238, s[56:59], 0 offen offset:1152 sc1\n"
        "v_mfma_f32_16x16x32_bf16 v[116:119], a[244:247], a[176:179], v[116:119]\n"
        "v_cvt_pk_bf16_f32 v128, v112, v113\n"
        "v_cvt_pk_bf16_f32 v129, v114, v115\n"
        "v_mfma_f32_16x16x32_bf16 v[116:119], a[240:243], a[172:175], v[116:119]\n"
        "ds_write_b32 v245, v64 offset:256\n"
        "v_mfma_f32_16x16x32_bf16 v[116:119], a[236:239], a[168:171], v[116:119]\n"
        "v_cvt_pk_bf16_f32 v234, v217, v225\n"
        "s_nop 0\n"
        "v_mfma_f32_16x16x32_bf16 v[120:123], a[236:239], a[192:195], 0\n"
        "ds_read_b128 a[236:239], v241 offset:1664\n"
        "s_nop 0\n"
        "v_mfma_f32_16x16x32_bf16 v[120:123], a[240:243], a[196:199], v[120:123]\n"
        "buffer_store_dword v232, v238, s[60:63], 0 offen offset:0 sc1\n"
        "v_mfma_f32_16x16x32_bf16 v[120:123], a[244:247], a[200:203], v[120:123]\n"
        "ds_read_b128 a[240:243], v240 offset:1792\n"
        "ds_read_b128 a[244:247], v241 offset:1792\n"
        "v_mfma_f32_16x16x32_bf16 v[120:123], a[248:251], a[204:207], v[120:123]\n"
        "v_cvt_pk_bf16_f32 v136, v116, v117\n"
        "v_cvt_pk_bf16_f32 v137, v118, v119\n"
        "v_mfma_f32_16x16x32_bf16 v[120:123], a[216:219], a[208:211], v[120:123]\n"
        "v_cvt_pk_bf16_f32 v232, v218, v226\n"
        "v_mfma_f32_16x16x32_bf16 v[120:123], a[220:223], a[212:215], v[120:123]\n"
        "s_waitcnt lgkmcnt(4)\n"
        "buffer_store_dword v234, v238, s[60:63], 0 offen offset:384 sc1\n"
        "v_mfma_f32_16x16x32_bf16 v[112:115], a[72:75], a[224:227], 0\n"
        "s_nop 2\n"
        "v_mfma_f32_16x16x32_bf16 v[112:115], a[76:79], a[228:231], v[112:115]\n"
        "v_cvt_pk_bf16_f32 v234, v219, v227\n"
        "s_nop 0\n"
        "v_mfma_f32_16x16x32_bf16 v[112:115], a[80:83], a[232:235], v[112:115]\n"
        "s_waitcnt lgkmcnt(0)\n"
        "v_cvt_pk_bf16_f32 v144, v120, v121\n"
        "v_cvt_pk_bf16_f32 v145, v122, v123\n"
        "v_mfma_f32_16x16x32_bf16 v[112:115], a[84:87], a[236:239], v[112:115]\n"
        "ds_write_b32 v245, v65 offset:1280\n"
        "v_mfma_f32_16x16x32_bf16 v[112:115], a[88:91], a[240:243], v[112:115]\n"
        "buffer_store_dword v232, v238, s[60:63], 0 offen offset:768 sc1\n"
        "v_mfma_f32_16x16x32_bf16 v[112:115], a[92:95], a[244:247], v[112:115]\n"
        "s_nop 2\n"
        "v_mfma_f32_16x16x32_bf16 v[116:119], a[116:119], a[244:247], 0\n"
        "s_nop 2\n"
        "v_mfma_f32_16x16x32_bf16 v[116:119], a[112:115], a[240:243], v[116:119]\n"
        "v_cvt_pk_bf16_f32 v172, v112, v113\n"
        "v_cvt_pk_bf16_f32 v173, v114, v115\n"
        "v_mfma_f32_16x16x32_bf16 v[116:119], a[108:111], a[236:239], v[116:119]\n"
        "buffer_store_dword v234, v238, s[60:63], 0 offen offset:1152 sc1\n"
        "v_mfma_f32_16x16x32_bf16 v[116:119], a[104:107], a[232:235], v[116:119]\n"
        "ds_write_b32 v245, v66 offset:2304\n"
        "v_mfma_f32_16x16x32_bf16 v[116:119], a[100:103], a[228:231], v[116:119]\n"
        "v_mfma_f32_16x16x32_bf16 v[116:119], a[96:99], a[224:227], v[116:119]\n"
        "s_mov_b32 m0, s48\n"
        "s_nop 1\n"
        "v_mfma_f32_16x16x32_bf16 v[120:123], a[120:123], a[224:227], 0\n"
        "ds_read_b128 v[162:165], v251 offset:0\n"
        "s_nop 0\n"
        "v_mfma_f32_16x16x32_bf16 v[120:123], a[124:127], a[228:231], v[120:123]\n"
        "buffer_load_dwordx4 v249, s[12:15], s36 offen offset:0 lds\n"
        "v_mfma_f32_16x16x32_bf16 v[120:123], a[128:131], a[232:235], v[120:123]\n"
        "v_cvt_pk_bf16_f32 v174, v116, v117\n"
        "v_cvt_pk_bf16_f32 v175, v118, v119\n"
        "v_mfma_f32_16x16x32_bf16 v[120:123], a[132:135], a[236:239], v[120:123]\n"
        "ds_write_b32 v245, v67 offset:3328\n"
        "v_mfma_f32_16x16x32_bf16 v[120:123], a[136:139], a[240:243], v[120:123]\n"
        "ds_write_b32 v245, v72 offset:512\n"
        "v_mfma_f32_16x16x32_bf16 v[120:123], a[140:143], a[244:247], v[120:123]\n"
        "s_nop 2\n"
        "v_mfma_f32_16x16x32_bf16 v[112:115], a[20:23], a[244:247], 0\n"
        "s_nop 2\n"
        "v_mfma_f32_16x16x32_bf16 v[112:115], a[16:19], a[240:243], v[112:115]\n"
        "v_cvt_pk_bf16_f32 v190, v120, v121\n"
        "v_cvt_pk_bf16_f32 v191, v122, v123\n"
        "v_mfma_f32_16x16x32_bf16 v[112:115], a[12:15], a[236:239], v[112:115]\n"
        "ds_write_b32 v245, v73 offset:1536\n"
        "v_mfma_f32_16x16x32_bf16 v[112:115], a[8:11], a[232:235], v[112:115]\n"
        "ds_write_b32 v245, v74 offset:2560\n"
        "v_mfma_f32_16x16x32_bf16 v[112:115], a[4:7], a[228:231], v[112:115]\n"
        "ds_read_b128 v[182:185], v251 offset:512\n"
        "v_mfma_f32_16x16x32_bf16 v[112:115], a[0:3], a[224:227], v[112:115]\n"
        "s_nop 2\n"
        "v_mfma_f32_16x16x32_bf16 v[116:119], a[24:27], a[224:227], 0\n"
        "s_nop 2\n"
        "v_mfma_f32_16x16x32_bf16 v[116:119], a[28:31], a[228:231], v[116:119]\n"
        "v_cvt_pk_bf16_f32 v176, v112, v113\n"
        "v_cvt_pk_bf16_f32 v177, v114, v115\n"
        "v_mfma_f32_16x16x32_bf16 v[116:119], a[32:35], a[232:235], v[116:119]\n"
        "ds_write_b32 v245, v75 offset:3584\n"
        "v_mfma_f32_16x16x32_bf16 v[116:119], a[36:39], a[236:239], v[116:119]\n"
        "buffer_load_dwordx4 v253, s[12:15], s36 offen offset:1024 lds\n"
        "v_mfma_f32_16x16x32_bf16 v[116:119], a[40:43], a[240:243], v[116:119]\n"
        "ds_write_b32 v245, v80 offset:768\n"
        "v_mfma_f32_16x16x32_bf16 v[116:119], a[44:47], a[244:247], v[116:119]\n"
        "s_nop 2\n"
        "v_mfma_f32_16x16x32_bf16 v[120:123], a[68:71], a[244:247], 0\n"
        "s_nop 2\n"
        "v_mfma_f32_16x16x32_bf16 v[120:123], a[64:67], a[240:243], v[120:123]\n"
        "v_cvt_pk_bf16_f32 v178, v116, v117\n"
        "v_cvt_pk_bf16_f32 v179, v118, v119\n"
        "v_mfma_f32_16x16x32_bf16 v[120:123], a[60:63], a[236:239], v[120:123]\n"
        "ds_write_b32 v245, v81 offset:1792\n"
        "v_mfma_f32_16x16x32_bf16 v[120:123], a[56:59], a[232:235], v[120:123]\n"
        "ds_write_b32 v245, v82 offset:2816\n"
        "v_mfma_f32_16x16x32_bf16 v[120:123], a[52:55], a[228:231], v[120:123]\n"
        "ds_write_b32 v245, v83 offset:3840\n"
        "v_mfma_f32_16x16x32_bf16 v[120:123], a[48:51], a[224:227], v[120:123]\n"
        "s_nop 2\n"
        "v_mfma_f32_16x16x32_bf16 v[112:115], a[224:227], a[144:147], 0\n"
        "s_nop 2\n"
        "v_mfma_f32_16x16x32_bf16 v[112:115], a[228:231], a[148:151], v[112:115]\n"
        "v_cvt_pk_bf16_f32 v180, v120, v121\n"
        "v_cvt_pk_bf16_f32 v181, v122, v123\n"
        "v_mfma_f32_16x16x32_bf16 v[112:115], a[232:235], a[152:155], v[112:115]\n"
        "v_cvt_pk_bf16_f32 v186, v120, v121\n"
        "v_cvt_pk_bf16_f32 v187, v122, v123\n"
        "v_mfma_f32_16x16x32_bf16 v[112:115], a[236:239], a[156:159], v[112:115]\n"
        "s_add_u32 m0, s41, 0\n"
        "v_mfma_f32_16x16x32_bf16 v[112:115], a[240:243], a[160:163], v[112:115]\n"
        "ds_read_b128 a[248:251], v240 offset:12288\n"
        "v_mfma_f32_16x16x32_bf16 v[112:115], a[244:247], a[164:167], v[112:115]\n"
        "buffer_load_dwordx4 v242, s[8:11], s47 offen lds\n"
        "v_mfma_f32_16x16x32_bf16 v[96:99], v[152:155], v[156:159], v[0:3]\n"
        "s_waitcnt lgkmcnt(2)\n"
        "ds_read_b128 a[216:219], v241 offset:12288\n"
        "s_nop 0\n"
        "v_mfma_f32_16x16x32_bf16 v[96:99], v[168:171], v[160:163], v[96:99]\n"
        "ds_read_b128 a[220:223], v240 offset:12416\n"
        "s_nop 0\n"
        "v_mfma_f32_16x16x32_bf16 v[100:103], v[172:175], v[156:159], v[4:7]\n"
        "s_sub_u32 s31, s30, s28\n"
        "v_cvt_pk_bf16_f32 v130, v112, v113\n"
        "v_cvt_pk_bf16_f32 v131, v114, v115\n"
        "v_mfma_f32_16x16x32_bf16 v[100:103], v[188:191], v[164:167], v[100:103]\n"
        "s_sub_u32 s45, s28, s29\n"
        "ds_read_b128 v[162:165], v252 offset:0\n"
        "v_mfma_f32_16x16x32_bf16 v[116:119], a[244:247], a[188:191], 0\n"
        "s_add_u32 s28, s28, s31\n"
        "v_exp_f32 v96, v96\n"
        "v_mfma_f32_16x16x32_bf16 v[116:119], a[240:243], a[184:187], v[116:119]\n"
        "s_mov_b32 s30, s29\n"
        "v_exp_f32 v97, v97\n"
        "v_mfma_f32_16x16x32_bf16 v[116:119], a[236:239], a[180:183], v[116:119]\n"
        "s_add_u32 s29, s29, s45\n"
        "v_exp_f32 v98, v98\n"
        "v_mfma_f32_16x16x32_bf16 v[116:119], a[232:235], a[176:179], v[116:119]\n"
        "v_exp_f32 v100, v100\n"
        "v_mfma_f32_16x16x32_bf16 v[116:119], a[228:231], a[172:175], v[116:119]\n"
        "v_exp_f32 v99, v99\n"
        "v_mfma_f32_16x16x32_bf16 v[116:119], a[224:227], a[168:171], v[116:119]\n"
        "v_exp_f32 v101, v101\n"
        "v_mfma_f32_16x16x32_bf16 v[104:107], v[152:155], v[176:179], v[16:19]\n"
        "v_exp_f32 v102, v102\n"
        "v_mfma_f32_16x16x32_bf16 v[104:107], v[168:171], v[180:183], v[104:107]\n"
        "s_add_u32 m0, s41, 1024\n"
        "v_cvt_pk_bf16_f32 v64, v96, v97\n"
        "v_cvt_pk_bf16_f32 v65, v98, v99\n"
        "v_mfma_f32_16x16x32_bf16 v[108:111], v[172:175], v[176:179], v[20:23]\n"
        "v_cvt_pk_bf16_f32 v138, v116, v117\n"
        "v_cvt_pk_bf16_f32 v139, v118, v119\n"
        "v_mfma_f32_16x16x32_bf16 v[108:111], v[188:191], v[184:187], v[108:111]\n"
        "buffer_load_dwordx4 v243, s[8:11], s47 offen lds\n"
        "v_mfma_f32_16x16x32_bf16 v[120:123], a[224:227], a[192:195], 0\n"
        "ds_read_b128 a[224:227], v241 offset:12416\n"
        "v_cvt_pk_bf16_f32 v66, v100, v101\n"
        "v_mfma_f32_16x16x32_bf16 v[120:123], a[228:231], a[196:199], v[120:123]\n"
        "ds_read_b128 a[228:231], v240 offset:12544\n"
        "v_mfma_f32_16x16x32_bf16 v[120:123], a[232:235], a[200:203], v[120:123]\n"
        "ds_read_b128 a[232:235], v241 offset:12544\n"
        "v_mfma_f32_16x16x32_bf16 v[120:123], a[236:239], a[204:207], v[120:123]\n"
        "ds_read_b128 v[202:205], v251 offset:4096\n"
        "v_mfma_f32_16x16x32_bf16 v[120:123], a[240:243], a[208:211], v[120:123]\n"
        "ds_read_b128 v[182:185], v252 offset:512\n"
        "v_mfma_f32_16x16x32_bf16 v[120:123], a[244:247], a[212:215], v[120:123]\n"
        "s_waitcnt lgkmcnt(4)\n"
        "v_exp_f32 v103, v103\n"
        "v_mfma_f32_16x16x32_bf16 v[112:115], a[0:3], a[248:251], 0\n"
        "v_cvt_pk_bf16_f32 v67, v102, v103\n"
        "s_nop 0\n"
        "v_mfma_f32_16x16x32_bf16 v[112:115], a[4:7], a[216:219], v[112:115]\n"
        "v_exp_f32 v104, v104\n"
        "v_mfma_f32_16x16x32_bf16 v[112:115], a[8:11], a[220:223], v[112:115]\n"
        "v_exp_f32 v105, v105\n"
        "v_mfma_f32_16x16x32_bf16 v[112:115], a[12:15], a[224:227], v[112:115]\n"
        "s_waitcnt lgkmcnt(0)\n"
        "v_exp_f32 v106, v106\n"
        "v_mfma_f32_16x16x32_bf16 v[112:115], a[16:19], a[228:231], v[112:115]\n"
        "v_cvt_pk_bf16_f32 v72, v104, v105\n"
        "v_cvt_pk_bf16_f32 v146, v120, v121\n"
        "v_mfma_f32_16x16x32_bf16 v[112:115], a[20:23], a[232:235], v[112:115]\n"
        "s_add_u32 m0, s41, 2048\n"
        "v_exp_f32 v108, v108\n"
        "v_mfma_f32_16x16x32_bf16 v[116:119], a[44:47], a[232:235], 0\n"
        "v_cvt_pk_bf16_f32 v147, v122, v123\n"
        "v_add_u32 v245, s45, v245\n"
        "v_mfma_f32_16x16x32_bf16 v[116:119], a[40:43], a[228:231], v[116:119]\n"
        "buffer_load_dwordx4 v244, s[8:11], s47 offen lds\n"
        "v_mfma_f32_16x16x32_bf16 v[116:119], a[36:39], a[224:227], v[116:119]\n"
        "v_exp_f32 v107, v107\n"
        "v_mfma_f32_16x16x32_bf16 v[116:119], a[32:35], a[220:223], v[116:119]\n"
        "v_exp_f32 v109, v109\n"
        "v_mfma_f32_16x16x32_bf16 v[116:119], a[28:31], a[216:219], v[116:119]\n"
        "v_cvt_pk_bf16_f32 v73, v106, v107\n"
        "v_cvt_pk_bf16_f32 v74, v108, v109\n"
        "v_mfma_f32_16x16x32_bf16 v[116:119], a[24:27], a[248:251], v[116:119]\n"
        "v_exp_f32 v110, v110\n"
        "v_mfma_f32_16x16x32_bf16 v[120:123], a[48:51], a[248:251], 0\n"
        "v_exp_f32 v111, v111\n"
        "v_mfma_f32_16x16x32_bf16 v[120:123], a[52:55], a[216:219], v[120:123]\n"
        "v_cvt_pk_bf16_f32 v75, v110, v111\n"
        "v_cvt_pk_bf16_f32 v196, v112, v113\n"
        "v_mfma_f32_16x16x32_bf16 v[120:123], a[56:59], a[220:223], v[120:123]\n"
        "v_cvt_pk_bf16_f32 v197, v114, v115\n"
        "v_cvt_pk_bf16_f32 v198, v116, v117\n"
        "v_mfma_f32_16x16x32_bf16 v[120:123], a[60:63], a[224:227], v[120:123]\n"
        "v_cvt_pk_bf16_f32 v199, v118, v119\n"
        "v_mfma_f32_16x16x32_bf16 v[120:123], a[64:67], a[228:231], v[120:123]\n"
        "v_mfma_f32_16x16x32_bf16 v[120:123], a[68:71], a[232:235], v[120:123]\n"
        "s_nop 2\n"
        "v_mfma_f32_16x16x32_bf16 v[112:115], a[92:95], a[232:235], 0\n"
        "s_nop 2\n"
        "v_mfma_f32_16x16x32_bf16 v[112:115], a[88:91], a[228:231], v[112:115]\n"
        "s_add_u32 m0, s41, 12288\n"
        "v_cvt_pk_bf16_f32 v200, v120, v121\n"
        "v_cvt_pk_bf16_f32 v201, v122, v123\n"
        "v_mfma_f32_16x16x32_bf16 v[112:115], a[84:87], a[224:227], v[112:115]\n"
        "v_cvt_pk_bf16_f32 v206, v120, v121\n"
        "v_cvt_pk_bf16_f32 v207, v122, v123\n"
        "v_mfma_f32_16x16x32_bf16 v[112:115], a[80:83], a[220:223], v[112:115]\n"
        "buffer_load_dwordx4 v242, s[8:11], s35 offen lds\n"
        "v_mfma_f32_16x16x32_bf16 v[112:115], a[76:79], a[216:219], v[112:115]\n"
        "v_mfma_f32_16x16x32_bf16 v[112:115], a[72:75], a[248:251], v[112:115]\n"
        "s_nop 2\n"
        "v_mfma_f32_16x16x32_bf16 v[96:99], v[152:155], v[196:199], v[32:35]\n"
        "s_nop 2\n"
        "v_mfma_f32_16x16x32_bf16 v[96:99], v[168:171], v[200:203], v[96:99]\n"
        "v_cvt_pk_bf16_f32 v192, v112, v113\n"
        "v_cvt_pk_bf16_f32 v193, v114, v115\n"
        "v_mfma_f32_16x16x32_bf16 v[100:103], v[172:175], v[196:199], v[36:39]\n"
        "v_mfma_f32_16x16x32_bf16 v[100:103], v[188:191], v[204:207], v[100:103]\n"
        "ds_read_b128 v[202:205], v252 offset:4096\n"
        "v_mfma_f32_16x16x32_bf16 v[116:119], a[96:99], a[248:251], 0\n"
        "s_cmp_eq_u32 s37, 100663296\n"
        "v_mfma_f32_16x16x32_bf16 v[116:119], a[100:103], a[216:219], v[116:119]\n"
        "s_cselect_b32 s46, 0, s37\n"
        "v_exp_f32 v96, v96\n"
        "v_mfma_f32_16x16x32_bf16 v[116:119], a[104:107], a[220:223], v[116:119]\n"
        "s_add_u32 s38, s46, s43\n"
        "v_exp_f32 v97, v97\n"
        "v_mfma_f32_16x16x32_bf16 v[116:119], a[108:111], a[224:227], v[116:119]\n"
        "s_add_u32 s39, s46, s44\n"
        "v_exp_f32 v98, v98\n"
        "v_mfma_f32_16x16x32_bf16 v[116:119], a[112:115], a[228:231], v[116:119]\n"
        "s_add_u32 s37, s37, 6291456\n"
        "v_exp_f32 v100, v100\n"
        "v_mfma_f32_16x16x32_bf16 v[116:119], a[116:119], a[232:235], v[116:119]\n"
        "s_add_u32 m0, s41, 13312\n"
        "v_exp_f32 v99, v99\n"
        "v_mfma_f32_16x16x32_bf16 v[120:123], a[140:143], a[232:235], 0\n"
        "v_exp_f32 v101, v101\n"
        "v_mfma_f32_16x16x32_bf16 v[120:123], a[136:139], a[228:231], v[120:123]\n"
        "buffer_load_dwordx4 v243, s[8:11], s35 offen lds\n"
        "v_mfma_f32_16x16x32_bf16 v[120:123], a[132:135], a[224:227], v[120:123]\n"
        "v_cvt_pk_bf16_f32 v80, v96, v97\n"
        "v_cvt_pk_bf16_f32 v194, v116, v117\n"
        "v_mfma_f32_16x16x32_bf16 v[120:123], a[128:131], a[220:223], v[120:123]\n"
        "s_mov_b64 s[58:59], s[18:19]\n"
        "v_cvt_pk_bf16_f32 v195, v118, v119\n"
        "v_cvt_pk_bf16_f32 v81, v98, v99\n"
        "v_mfma_f32_16x16x32_bf16 v[120:123], a[124:127], a[216:219], v[120:123]\n"
        "s_mov_b64 s[62:63], s[18:19]\n"
        "v_exp_f32 v102, v102\n"
        "v_mfma_f32_16x16x32_bf16 v[120:123], a[120:123], a[248:251], v[120:123]\n"
        "v_cvt_pk_bf16_f32 v82, v100, v101\n"
        "s_nop 0\n"
        "v_mfma_f32_16x16x32_bf16 v[112:115], a[248:251], a[144:147], 0\n"
        "v_exp_f32 v103, v103\n"
        "v_mfma_f32_16x16x32_bf16 v[112:115], a[216:219], a[148:151], v[112:115]\n"
        "v_cvt_pk_bf16_f32 v83, v102, v103\n"
        "s_nop 0\n"
        "v_mfma_f32_16x16x32_bf16 v[112:115], a[220:223], a[152:155], v[112:115]\n"
        "ds_read_b128 a[236:239], v240 offset:13824\n"
        "v_cvt_pk_bf16_f32 v208, v120, v121\n"
        "v_mfma_f32_16x16x32_bf16 v[112:115], a[224:227], a[156:159], v[112:115]\n"
        "ds_read_b128 a[240:243], v241 offset:13824\n"
        "v_cvt_pk_bf16_f32 v209, v122, v123\n"
        "v_mfma_f32_16x16x32_bf16 v[112:115], a[228:231], a[160:163], v[112:115]\n"
        "ds_read_b128 a[244:247], v240 offset:13952\n"
        "v_mfma_f32_16x16x32_bf16 v[112:115], a[232:235], a[164:167], v[112:115]\n"
        "s_nop 2\n"
        "v_mfma_f32_16x16x32_bf16 v[104:107], v[192:195], v[156:159], v[8:11]\n"
        "s_nop 2\n"
        "v_mfma_f32_16x16x32_bf16 v[104:107], v[208:211], v[160:163], v[104:107]\n"
        "s_add_u32 m0, s41, 14336\n"
        "v_cvt_pk_bf16_f32 v132, v112, v113\n"
        "v_cvt_pk_bf16_f32 v133, v114, v115\n"
        "v_mfma_f32_16x16x32_bf16 v[116:119], a[232:235], a[188:191], 0\n"
        "v_mfma_f32_16x16x32_bf16 v[116:119], a[228:231], a[184:187], v[116:119]\n"
        "buffer_load_dwordx4 v244, s[8:11], s35 offen lds\n"
        "v_mfma_f32_16x16x32_bf16 v[116:119], a[224:227], a[180:183], v[116:119]\n"
        "v_mfma_f32_16x16x32_bf16 v[116:119], a[220:223], a[176:179], v[116:119]\n"
        "v_exp_f32 v104, v104\n"
        "v_mfma_f32_16x16x32_bf16 v[116:119], a[216:219], a[172:175], v[116:119]\n"
        "v_exp_f32 v105, v105\n"
        "v_mfma_f32_16x16x32_bf16 v[116:119], a[248:251], a[168:171], v[116:119]\n"
        "v_exp_f32 v106, v106\n"
        "v_mfma_f32_16x16x32_bf16 v[108:111], v[192:195], v[176:179], v[24:27]\n"
        "v_exp_f32 v107, v107\n"
        "v_mfma_f32_16x16x32_bf16 v[108:111], v[208:211], v[180:183], v[108:111]\n"
        "v_cvt_pk_bf16_f32 v68, v104, v105\n"
        "v_cvt_pk_bf16_f32 v69, v106, v107\n"
        "v_mfma_f32_16x16x32_bf16 v[120:123], a[248:251], a[192:195], 0\n"
        "ds_read_b128 a[248:251], v241 offset:13952\n"
        "v_cvt_pk_bf16_f32 v140, v116, v117\n"
        "v_mfma_f32_16x16x32_bf16 v[120:123], a[216:219], a[196:199], v[120:123]\n"
        "ds_read_b128 a[216:219], v240 offset:14080\n"
        "v_cvt_pk_bf16_f32 v141, v118, v119\n"
        "v_mfma_f32_16x16x32_bf16 v[120:123], a[220:223], a[200:203], v[120:123]\n"
        "ds_read_b128 a[220:223], v241 offset:14080\n"
        "v_mfma_f32_16x16x32_bf16 v[120:123], a[224:227], a[204:207], v[120:123]\n"
        "v_exp_f32 v108, v108\n"
        "v_mfma_f32_16x16x32_bf16 v[120:123], a[228:231], a[208:211], v[120:123]\n"
        "v_exp_f32 v109, v109\n"
        "v_mfma_f32_16x16x32_bf16 v[120:123], a[232:235], a[212:215], v[120:123]\n"
        "v_exp_f32 v110, v110\n"
        "v_mfma_f32_16x16x32_bf16 v[96:99], v[192:195], v[196:199], v[40:43]\n"
        "s_waitcnt lgkmcnt(1)\n"
        "v_exp_f32 v111, v111\n"
        "v_mfma_f32_16x16x32_bf16 v[96:99], v[208:211], v[200:203], v[96:99]\n"
        "v_cvt_pk_bf16_f32 v76, v108, v109\n"
        "v_cvt_pk_bf16_f32 v77, v110, v111\n"
        "v_mfma_f32_16x16x32_bf16 v[112:115], a[72:75], a[236:239], 0\n"
        "s_nop 0\n"
        "v_cvt_pk_bf16_f32 v148, v120, v121\n"
        "v_cvt_pk_bf16_f32 v149, v122, v123\n"
        "v_mfma_f32_16x16x32_bf16 v[112:115], a[76:79], a[240:243], v[112:115]\n"
        "v_mfma_f32_16x16x32_bf16 v[112:115], a[80:83], a[244:247], v[112:115]\n"
        "v_exp_f32 v96, v96\n"
        "v_mfma_f32_16x16x32_bf16 v[112:115], a[84:87], a[248:251], v[112:115]\n"
        "v_exp_f32 v97, v97\n"
        "v_mfma_f32_16x16x32_bf16 v[112:115], a[88:91], a[216:219], v[112:115]\n"
        "s_waitcnt lgkmcnt(0)\n"
        "v_exp_f32 v98, v98\n"
        "v_mfma_f32_16x16x32_bf16 v[112:115], a[92:95], a[220:223], v[112:115]\n"
        "v_exp_f32 v99, v99\n"
        "v_mfma_f32_16x16x32_bf16 v[116:119], a[116:119], a[220:223], 0\n"
        "v_cvt_pk_bf16_f32 v84, v96, v97\n"
        "v_cvt_pk_bf16_f32 v85, v98, v99\n"
        "v_mfma_f32_16x16x32_bf16 v[116:119], a[112:115], a[216:219], v[116:119]\n"
        "v_add_u32 v240, s31, v240\n"
        "v_add_u32 v241, s31, v241\n"
        "v_mfma_f32_16x16x32_bf16 v[116:119], a[108:111], a[248:251], v[116:119]\n"
        "v_cvt_pk_bf16_f32 v212, v112, v113\n"
        "v_cvt_pk_bf16_f32 v213, v114, v115\n"
        "v_mfma_f32_16x16x32_bf16 v[116:119], a[104:107], a[244:247], v[116:119]\n"
        "s_add_u32 s56, s16, s38\n"
        "s_addc_u32 s57, s17, 0\n"
        "v_mfma_f32_16x16x32_bf16 v[116:119], a[100:103], a[240:243], v[116:119]\n"
        "s_add_u32 s60, s16, s39\n"
        "s_addc_u32 s61, s17, 0\n"
        "v_mfma_f32_16x16x32_bf16 v[116:119], a[96:99], a[236:239], v[116:119]\n"
        "s_nop 2\n"
        "v_mfma_f32_16x16x32_bf16 v[120:123], a[120:123], a[236:239], 0\n"
        "s_nop 2\n"
        "v_mfma_f32_16x16x32_bf16 v[120:123], a[124:127], a[240:243], v[120:123]\n"
        "s_add_u32 s47, s47, 6291456\n"
        "v_cvt_pk_bf16_f32 v214, v116, v117\n"
        "v_cvt_pk_bf16_f32 v215, v118, v119\n"
        "v_mfma_f32_16x16x32_bf16 v[120:123], a[128:131], a[244:247], v[120:123]\n"
        "s_add_u32 s36, s36, 2097152\n"
        "v_mfma_f32_16x16x32_bf16 v[120:123], a[132:135], a[248:251], v[120:123]\n"
        ".long 0xbec200ff\n"
        "45:\n"
        "s_branch 61b\n"
        "s_xor_b32 s48, s48, 8192\n"
        "v_mfma_f32_16x16x32_bf16 v[120:123], a[136:139], a[216:219], v[120:123]\n"
        "v_mfma_f32_16x16x32_bf16 v[120:123], a[140:143], a[220:223], v[120:123]\n"
        "s_nop 2\n"
        "v_mfma_f32_16x16x32_bf16 v[112:115], a[220:223], a[164:167], 0\n"
        "s_nop 2\n"
        "v_mfma_f32_16x16x32_bf16 v[112:115], a[216:219], a[160:163], v[112:115]\n"
        "s_barrier\n"
        "v_cvt_pk_bf16_f32 v230, v120, v121\n"
        "v_cvt_pk_bf16_f32 v231, v122, v123\n"
        "v_mfma_f32_16x16x32_bf16 v[112:115], a[248:251], a[156:159], v[112:115]\n"
        "v_mfma_f32_16x16x32_bf16 v[112:115], a[244:247], a[152:155], v[112:115]\n"
        "v_mfma_f32_16x16x32_bf16 v[112:115], a[240:243], a[148:151], v[112:115]\n"
        "v_mfma_f32_16x16x32_bf16 v[112:115], a[236:239], a[144:147], v[112:115]\n"
        "s_nop 2\n"
        "v_mfma_f32_16x16x32_bf16 v[100:103], v[212:215], v[156:159], v[12:15]\n"
        "s_nop 2\n"
        "v_mfma_f32_16x16x32_bf16 v[100:103], v[228:231], v[164:167], v[100:103]\n"
        "v_cvt_pk_bf16_f32 v134, v112, v113\n"
        "v_cvt_pk_bf16_f32 v135, v114, v115\n"
        "v_mfma_f32_16x16x32_bf16 v[116:119], a[236:239], a[168:171], 0\n"
        "s_sub_u32 s27, s27, 1\n"
        "v_mfma_f32_16x16x32_bf16 v[116:119], a[240:243], a[172:175], v[116:119]\n"
        "s_cmp_lg_u32 s27, 0\n"
        "v_mfma_f32_16x16x32_bf16 v[116:119], a[244:247], a[176:179], v[116:119]\n"
        "s_cbranch_scc1 10b\n"
        "s_branch 19f\n"
        ".p2align 6\n"
        "51:\n"
        "s_branch 61b\n"
        "s_nop 0\n"
        "s_nop 0\n"
        "s_nop 0\n"
        "s_nop 0\n"
        "s_nop 0\n"
        "12:\n"
        "10:\n"
        "v_mfma_f32_16x16x32_bf16 v[116:119], a[248:251], a[180:183], v[116:119]\n"
        "v_mfma_f32_16x16x32_bf16 v[116:119], a[216:219], a[184:187], v[116:119]\n"
        "s_nop 0\n"
        "v_exp_f32 v100, v100\n"
        "v_mfma_f32_16x16x32_bf16 v[116:119], a[220:223], a[188:191], v[116:119]\n"
        "v_exp_f32 v101, v101\n"
        "v_mfma_f32_16x16x32_bf16 v[104:107], v[212:215], v[176:179], v[28:31]\n"
        "v_exp_f32 v102, v102\n"
        "v_mfma_f32_16x16x32_bf16 v[104:107], v[228:231], v[184:187], v[104:107]\n"
        "v_exp_f32 v103, v103\n"
        "v_mfma_f32_16x16x32_bf16 v[120:123], a[220:223], a[212:215], 0\n"
        "v_cvt_pk_bf16_f32 v70, v100, v101\n"
        "v_cvt_pk_bf16_f32 v71, v102, v103\n"
        "v_mfma_f32_16x16x32_bf16 v[120:123], a[216:219], a[208:211], v[120:123]\n"
        "v_cvt_pk_bf16_f32 v142, v116, v117\n"
        "v_cvt_pk_bf16_f32 v143, v118, v119\n"
        "v_mfma_f32_16x16x32_bf16 v[120:123], a[248:251], a[204:207], v[120:123]\n"
        "v_mov_b64 v[96:97], 0\n"
        "v_mfma_f32_16x16x32_bf16 v[120:123], a[244:247], a[200:203], v[120:123]\n"
        "v_exp_f32 v104, v104\n"
        "v_mfma_f32_16x16x32_bf16 v[120:123], a[240:243], a[196:199], v[120:123]\n"
        "v_exp_f32 v105, v105\n"
        "v_mfma_f32_16x16x32_bf16 v[120:123], a[236:239], a[192:195], v[120:123]\n"
        "v_exp_f32 v106, v106\n"
        "v_mfma_f32_16x16x32_bf16 v[108:111], v[212:215], v[196:199], v[44:47]\n"
        "v_exp_f32 v107, v107\n"
        "v_mfma_f32_16x16x32_bf16 v[108:111], v[228:231], v[204:207], v[108:111]\n"
        "v_cvt_pk_bf16_f32 v78, v104, v105\n"
        "v_cvt_pk_bf16_f32 v79, v106, v107\n"
        "v_mfma_f32_16x16x32_bf16 v[112:115], a[0:3], a[236:239], 0\n"
        "s_nop 0\n"
        "v_cvt_pk_bf16_f32 v150, v120, v121\n"
        "v_cvt_pk_bf16_f32 v151, v122, v123\n"
        "v_mfma_f32_16x16x32_bf16 v[112:115], a[4:7], a[240:243], v[112:115]\n"
        "v_mfma_f32_16x16x32_bf16 v[112:115], a[8:11], a[244:247], v[112:115]\n"
        "v_exp_f32 v108, v108\n"
        "v_mfma_f32_16x16x32_bf16 v[112:115], a[12:15], a[248:251], v[112:115]\n"
        "v_exp_f32 v109, v109\n"
        "v_mfma_f32_16x16x32_bf16 v[112:115], a[16:19], a[216:219], v[112:115]\n"
        "v_exp_f32 v110, v110\n"
        "v_mfma_f32_16x16x32_bf16 v[112:115], a[20:23], a[220:223], v[112:115]\n"
        "v_exp_f32 v111, v111\n"
        "v_smfmac_f32_16x16x64_bf16 v[96:99], a[252:255], v[64:71], v237\n"
        "v_cvt_pk_bf16_f32 v86, v108, v109\n"
        "v_mfma_f32_16x16x32_bf16 v[100:103], v[132:135], v[68:71], 0\n"
        "v_mov_b64 v[124:125], 0\n"
        "v_cvt_pk_bf16_f32 v87, v110, v111\n"
        "v_mfma_f32_16x16x32_bf16 v[100:103], v[128:131], v[64:67], v[100:103]\n"
        "s_nop 0\n"
        "v_cvt_pk_bf16_f32 v216, v112, v113\n"
        "v_cvt_pk_bf16_f32 v217, v114, v115\n"
        "v_mfma_f32_16x16x32_bf16 v[104:107], v[136:139], v[64:67], 0\n"
        "ds_read_b128 v[222:225], v251 offset:4608\n"
        "v_add_f32 v97, v96, v97\n"
        "v_mfma_f32_16x16x32_bf16 v[104:107], v[140:143], v[68:71], v[104:107]\n"
        "v_rcp_f32 v96, v97\n"
        "v_mfma_f32_16x16x32_bf16 v[108:111], v[148:151], v[68:71], 0\n"
        "v_mul_f32 v100, v96, v100\n"
        "v_mul_f32 v101, v96, v101\n"
        "v_mfma_f32_16x16x32_bf16 v[108:111], v[144:147], v[64:67], v[108:111]\n"
        "v_mul_f32 v102, v96, v102\n"
        "v_mul_f32 v103, v96, v103\n"
        "v_mfma_f32_16x16x32_bf16 v[116:119], a[44:47], a[220:223], 0\n"
        "v_cvt_pk_bf16_f32 v100, v100, v101\n"
        "v_cvt_pk_bf16_f32 v101, v102, v103\n"
        "v_mfma_f32_16x16x32_bf16 v[116:119], a[40:43], a[216:219], v[116:119]\n"
        "v_mul_f32 v104, v96, v104\n"
        "v_mul_f32 v105, v96, v105\n"
        "v_mfma_f32_16x16x32_bf16 v[116:119], a[36:39], a[248:251], v[116:119]\n"
        "v_mul_f32 v106, v96, v106\n"
        "v_mul_f32 v107, v96, v107\n"
        "v_mfma_f32_16x16x32_bf16 v[116:119], a[32:35], a[244:247], v[116:119]\n"
        "v_cvt_pk_bf16_f32 v102, v104, v105\n"
        "v_cvt_pk_bf16_f32 v103, v106, v107\n"
        "v_mfma_f32_16x16x32_bf16 v[116:119], a[28:31], a[240:243], v[116:119]\n"
        "v_mul_f32 v108, v96, v108\n"
        "v_mul_f32 v109, v96, v109\n"
        "v_mfma_f32_16x16x32_bf16 v[116:119], a[24:27], a[236:239], v[116:119]\n"
        "v_mul_f32 v110, v96, v110\n"
        "v_mul_f32 v111, v96, v111\n"
        "v_smfmac_f32_16x16x64_bf16 v[124:127], a[252:255], v[72:79], v237\n"
        "v_cvt_pk_bf16_f32 v108, v108, v109\n"
        "v_mfma_f32_16x16x32_bf16 v[64:67], v[132:135], v[76:79], 0\n"
        "ds_write_b32 v245, v108 offset:4096\n"
        "v_mfma_f32_16x16x32_bf16 v[64:67], v[128:131], v[72:75], v[64:67]\n"
        "ds_read_b128 v[156:159], v247 offset:0\n"
        "v_cvt_pk_bf16_f32 v218, v116, v117\n"
        "v_mfma_f32_16x16x32_bf16 v[68:71], v[136:139], v[72:75], 0\n"
        "ds_read_b128 v[160:163], v247 offset:4096\n"
        "v_cvt_pk_bf16_f32 v219, v118, v119\n"
        "v_mfma_f32_16x16x32_bf16 v[68:71], v[140:143], v[76:79], v[68:71]\n"
        "ds_read_b128 v[164:167], v247 offset:4352\n"
        "v_cvt_pk_bf16_f32 v109, v110, v111\n"
        "v_mfma_f32_16x16x32_bf16 v[120:123], v[148:151], v[76:79], 0\n"
        "ds_write_b32 v245, v109 offset:5120\n"
        "v_mfma_f32_16x16x32_bf16 v[120:123], v[144:147], v[72:75], v[120:123]\n"
        "v_max_u32 v236, v96, v97\n"
        "v_mov_b64 v[116:117], 0\n"
        "v_mfma_f32_16x16x32_bf16 v[112:115], a[48:51], a[236:239], 0\n"
        "v_add_f32 v125, v124, v125\n"
        "v_mfma_f32_16x16x32_bf16 v[112:115], a[52:55], a[240:243], v[112:115]\n"
        "v_rcp_f32 v124, v125\n"
        "v_mfma_f32_16x16x32_bf16 v[112:115], a[56:59], a[244:247], v[112:115]\n"
        "v_mul_f32 v64, v124, v64\n"
        "v_mul_f32 v65, v124, v65\n"
        "v_mfma_f32_16x16x32_bf16 v[112:115], a[60:63], a[248:251], v[112:115]\n"
        "v_mul_f32 v66, v124, v66\n"
        "v_mul_f32 v67, v124, v67\n"
        "v_mfma_f32_16x16x32_bf16 v[112:115], a[64:67], a[216:219], v[112:115]\n"
        "v_cvt_pk_bf16_f32 v64, v64, v65\n"
        "v_cvt_pk_bf16_f32 v65, v66, v67\n"
        "v_mfma_f32_16x16x32_bf16 v[112:115], a[68:71], a[220:223], v[112:115]\n"
        "ds_read_b128 a[216:219], v248 offset:0\n"
        "v_mul_f32 v68, v124, v68\n"
        "v_smfmac_f32_16x16x64_bf16 v[116:119], a[252:255], v[80:87], v237\n"
        "ds_read_b128 a[220:223], v248 offset:3072\n"
        "v_mfma_f32_16x16x32_bf16 v[72:75], v[132:135], v[84:87], 0\n"
        "v_mul_f32 v69, v124, v69\n"
        "v_mul_f32 v70, v124, v70\n"
        "v_mfma_f32_16x16x32_bf16 v[72:75], v[128:131], v[80:83], v[72:75]\n"
        "v_cvt_pk_bf16_f32 v220, v112, v113\n"
        "v_cvt_pk_bf16_f32 v221, v114, v115\n"
        "v_mfma_f32_16x16x32_bf16 v[76:79], v[136:139], v[80:83], 0\n"
        "ds_read_b128 a[224:227], v248 offset:32\n"
        "v_cvt_pk_bf16_f32 v226, v112, v113\n"
        "v_mfma_f32_16x16x32_bf16 v[76:79], v[140:143], v[84:87], v[76:79]\n"
        "ds_read_b128 a[228:231], v248 offset:3104\n"
        "v_cvt_pk_bf16_f32 v227, v114, v115\n"
        "v_mfma_f32_16x16x32_bf16 v[96:99], v[148:151], v[84:87], 0\n"
        "v_mul_f32 v71, v124, v71\n"
        "v_cvt_pk_bf16_f32 v66, v68, v69\n"
        "v_mfma_f32_16x16x32_bf16 v[96:99], v[144:147], v[80:83], v[96:99]\n"
        "v_cvt_pk_bf16_f32 v67, v70, v71\n"
        "v_mul_f32 v120, v124, v120\n"
        "v_mfma_f32_16x16x32_bf16 v[104:107], v[152:155], v[216:219], v[48:51]\n"
        "s_waitcnt lgkmcnt(2)\n"
        "ds_read_b128 v[152:155], v247 offset:256\n"
        "v_mul_f32 v121, v124, v121\n"
        "v_mfma_f32_16x16x32_bf16 v[104:107], v[168:171], v[220:223], v[104:107]\n"
        "v_mul_f32 v122, v124, v122\n"
        "v_mul_f32 v123, v124, v123\n"
        "v_mfma_f32_16x16x32_bf16 v[108:111], v[172:175], v[216:219], v[52:55]\n"
        "v_cvt_pk_bf16_f32 v120, v120, v121\n"
        "v_cvt_pk_bf16_f32 v121, v122, v123\n"
        "v_mfma_f32_16x16x32_bf16 v[108:111], v[188:191], v[224:227], v[108:111]\n"
        "ds_read_b128 v[222:225], v252 offset:4608\n"
        "v_max3_u32 v236, v236, v124, v125\n"
        "v_mfma_f32_16x16x32_bf16 v[176:179], v[156:159], a[216:219], 0\n"
        "ds_write_b32 v245, v120 offset:4352\n"
        "v_mfma_f32_16x16x32_bf16 v[176:179], v[160:163], a[220:223], v[176:179]\n"
        "ds_write_b32 v245, v121 offset:5376\n"
        "v_mfma_f32_16x16x32_bf16 v[196:199], v[164:167], a[220:223], 0\n"
        "s_waitcnt lgkmcnt(3)\n"
        "ds_read_b128 a[232:235], v254 offset:0\n"
        "v_add_f32 v117, v116, v117\n"
        "v_mfma_f32_16x16x32_bf16 v[196:199], v[152:155], a[216:219], v[196:199]\n"
        "ds_read_b128 a[220:223], v254 offset:3072\n"
        "v_mfma_f32_16x16x32_bf16 v[200:203], v[152:155], a[224:227], 0\n"
        "ds_read_b128 a[216:219], v254 offset:32\n"
        "v_mfma_f32_16x16x32_bf16 v[200:203], v[164:167], a[228:231], v[200:203]\n"
        "v_rcp_f32 v116, v117\n"
        "v_mfma_f32_16x16x32_bf16 v[180:183], v[160:163], a[228:231], 0\n"
        "ds_read_b128 a[228:231], v254 offset:3104\n"
        "v_mul_f32 v72, v116, v72\n"
        "v_mfma_f32_16x16x32_bf16 v[180:183], v[156:159], a[224:227], v[180:183]\n"
        "ds_read_b128 a[236:239], v247 offset:8192\n"
        "v_mul_f32 v73, v116, v73\n"
        "v_mfma_f32_16x16x32_bf16 v[112:115], v[192:195], v[216:219], v[56:59]\n"
        "s_waitcnt lgkmcnt(7)\n"
        "ds_read_b128 a[224:227], v248 offset:16\n"
        "v_mul_f32 v74, v116, v74\n"
        "v_mfma_f32_16x16x32_bf16 v[112:115], v[208:211], v[220:223], v[112:115]\n"
        "ds_read_b128 a[240:243], v247 offset:12288\n"
        "v_mul_f32 v75, v116, v75\n"
        "v_mfma_f32_16x16x32_bf16 v[68:71], v[212:215], v[216:219], v[60:63]\n"
        "ds_read_b128 a[244:247], v247 offset:12544\n"
        "v_cvt_pk_bf16_f32 v72, v72, v73\n"
        "v_mfma_f32_16x16x32_bf16 v[68:71], v[228:231], v[224:227], v[68:71]\n"
        "s_waitcnt lgkmcnt(4)\n"
        "ds_read_b128 a[248:251], v247 offset:8448\n"
        "v_cvt_pk_bf16_f32 v73, v74, v75\n"
        "v_mfma_f32_16x16x32_bf16 v[184:187], v[156:159], a[232:235], 0\n"
        "v_mul_f32 v76, v116, v76\n"
        "v_mul_f32 v77, v116, v77\n"
        "v_mfma_f32_16x16x32_bf16 v[184:187], v[160:163], a[220:223], v[184:187]\n"
        "v_mul_f32 v78, v116, v78\n"
        "v_mul_f32 v79, v116, v79\n"
        "v_mfma_f32_16x16x32_bf16 v[204:207], v[164:167], a[220:223], 0\n"
        "ds_read_b128 a[220:223], v248 offset:3088\n"
        "v_cvt_pk_bf16_f32 v74, v76, v77\n"
        "v_mfma_f32_16x16x32_bf16 v[204:207], v[152:155], a[232:235], v[204:207]\n"
        "ds_read_b128 a[232:235], v254 offset:16\n"
        "v_cvt_pk_bf16_f32 v75, v78, v79\n"
        "v_mfma_f32_16x16x32_bf16 v[192:195], v[152:155], a[216:219], 0\n"
        "v_exp_f32 v104, v104\n"
        "v_mfma_f32_16x16x32_bf16 v[192:195], v[164:167], a[228:231], v[192:195]\n"
        "v_mul_f32 v96, v116, v96\n"
        "v_mul_f32 v97, v116, v97\n"
        "v_mfma_f32_16x16x32_bf16 v[172:175], v[160:163], a[228:231], 0\n"
        "ds_read_b128 a[228:231], v254 offset:3088\n"
        "v_mul_f32 v98, v116, v98\n"
        "v_mfma_f32_16x16x32_bf16 v[172:175], v[156:159], a[216:219], v[172:175]\n"
        "s_waitcnt lgkmcnt(2)\n"
        "ds_read_b128 a[216:219], v248 offset:6144\n"
        "v_mul_f32 v99, v116, v99\n"
        "v_mfma_f32_16x16x32_bf16 v[212:215], v[156:159], a[224:227], 0\n"
        "v_exp_f32 v105, v105\n"
        "v_mfma_f32_16x16x32_bf16 v[212:215], v[160:163], a[220:223], v[212:215]\n"
        "v_exp_f32 v106, v106\n"
        "v_mfma_f32_16x16x32_bf16 v[216:219], v[164:167], a[220:223], 0\n"
        "ds_read_b128 a[220:223], v248 offset:9216\n"
        "v_cvt_pk_bf16_f32 v88, v104, v105\n"
        "v_mfma_f32_16x16x32_bf16 v[216:219], v[152:155], a[224:227], v[216:219]\n"
        "s_waitcnt lgkmcnt(2)\n"
        "ds_read_b128 a[224:227], v248 offset:6176\n"
        "v_cvt_pk_bf16_f32 v96, v96, v97\n"
        "v_mfma_f32_16x16x32_bf16 v[224:227], v[152:155], a[232:235], 0\n"
        "v_exp_f32 v108, v108\n"
        "v_mfma_f32_16x16x32_bf16 v[224:227], v[164:167], a[228:231], v[224:227]\n"
        "v_cvt_pk_bf16_f32 v97, v98, v99\n"
        "v_max3_u32 v236, v236, v116, v117\n"
        "v_mfma_f32_16x16x32_bf16 v[220:223], v[160:163], a[228:231], 0\n"
        "ds_read_b128 a[228:231], v248 offset:9248\n"
        "v_mfma_f32_16x16x32_bf16 v[220:223], v[156:159], a[232:235], v[220:223]\n"
        "s_waitcnt lgkmcnt(2)\n"
        "ds_read_b128 a[232:235], v254 offset:6144\n"
        "v_mfma_f32_16x16x32_bf16 v[176:179], a[236:239], a[216:219], v[176:179]\n"
        "v_exp_f32 v107, v107\n"
        "v_mfma_f32_16x16x32_bf16 v[176:179], a[240:243], a[220:223], v[176:179]\n"
        "v_exp_f32 v109, v109\n"
        "v_mfma_f32_16x16x32_bf16 v[196:199], a[244:247], a[220:223], v[196:199]\n"
        "ds_read_b128 a[220:223], v254 offset:9216\n"
        "v_cvt_pk_bf16_f32 v89, v106, v107\n"
        "v_mfma_f32_16x16x32_bf16 v[196:199], a[248:251], a[216:219], v[196:199]\n"
        "s_waitcnt lgkmcnt(2)\n"
        "ds_read_b128 a[216:219], v254 offset:6176\n"
        "v_cvt_pk_bf16_f32 v90, v108, v109\n"
        "v_mfma_f32_16x16x32_bf16 v[200:203], a[248:251], a[224:227], v[200:203]\n"
        "ds_read_b128 v[156:159], v247 offset:16384\n"
        "v_mfma_f32_16x16x32_bf16 v[200:203], a[244:247], a[228:231], v[200:203]\n"
        "ds_read_b128 v[160:163], v247 offset:20480\n"
        "v_mfma_f32_16x16x32_bf16 v[180:183], a[240:243], a[228:231], v[180:183]\n"
        "ds_read_b128 a[228:231], v254 offset:9248\n"
        "v_mfma_f32_16x16x32_bf16 v[180:183], a[236:239], a[224:227], v[180:183]\n"
        "s_waitcnt lgkmcnt(4)\n"
        "ds_read_b128 a[224:227], v248 offset:6160\n"
        "v_mfma_f32_16x16x32_bf16 v[184:187], a[236:239], a[232:235], v[184:187]\n"
        "ds_read_b128 v[164:167], v247 offset:20736\n"
        "v_mfma_f32_16x16x32_bf16 v[184:187], a[240:243], a[220:223], v[184:187]\n"
        "ds_read_b128 v[152:155], v247 offset:16640\n"
        "v_mfma_f32_16x16x32_bf16 v[204:207], a[244:247], a[220:223], v[204:207]\n"
        "ds_read_b128 a[220:223], v248 offset:9232\n"
        "v_mfma_f32_16x16x32_bf16 v[204:207], a[248:251], a[232:235], v[204:207]\n"
        "s_waitcnt lgkmcnt(4)\n"
        "ds_read_b128 a[232:235], v254 offset:6160\n"
        "v_mfma_f32_16x16x32_bf16 v[192:195], a[248:251], a[216:219], v[192:195]\n"
        "v_exp_f32 v110, v110\n"
        "v_mfma_f32_16x16x32_bf16 v[192:195], a[244:247], a[228:231], v[192:195]\n"
        "v_exp_f32 v111, v111\n"
        "v_mfma_f32_16x16x32_bf16 v[172:175], a[240:243], a[228:231], v[172:175]\n"
        "ds_read_b128 a[228:231], v254 offset:9232\n"
        "v_cvt_pk_bf16_f32 v91, v110, v111\n"
        "v_mfma_f32_16x16x32_bf16 v[172:175], a[236:239], a[216:219], v[172:175]\n"
        "s_waitcnt lgkmcnt(2)\n"
        "ds_read_b128 a[216:219], v248 offset:12288\n"
        "v_mfma_f32_16x16x32_bf16 v[212:215], a[236:239], a[224:227], v[212:215]\n"
        "v_exp_f32 v112, v112\n"
        "v_mfma_f32_16x16x32_bf16 v[212:215], a[240:243], a[220:223], v[212:215]\n"
        "v_exp_f32 v113, v113\n"
        "v_mfma_f32_16x16x32_bf16 v[216:219], a[244:247], a[220:223], v[216:219]\n"
        "ds_read_b128 a[220:223], v248 offset:15360\n"
        "v_cvt_pk_bf16_f32 v92, v112, v113\n"
        "v_mfma_f32_16x16x32_bf16 v[216:219], a[248:251], a[224:227], v[216:219]\n"
        "s_waitcnt lgkmcnt(2)\n"
        "ds_read_b128 a[224:227], v248 offset:12320\n"
        "v_mfma_f32_16x16x32_bf16 v[224:227], a[248:251], a[232:235], v[224:227]\n"
        "v_exp_f32 v114, v114\n"
        "v_mfma_f32_16x16x32_bf16 v[224:227], a[244:247], a[228:231], v[224:227]\n"
        "v_exp_f32 v68, v68\n"
        "v_mfma_f32_16x16x32_bf16 v[220:223], a[240:243], a[228:231], v[220:223]\n"
        "ds_read_b128 a[228:231], v248 offset:15392\n"
        "v_mfma_f32_16x16x32_bf16 v[220:223], a[236:239], a[232:235], v[220:223]\n"
        "s_waitcnt lgkmcnt(2)\n"
        "ds_read_b128 a[232:235], v254 offset:12288\n"
        "v_mfma_f32_16x16x32_bf16 v[176:179], v[156:159], a[216:219], v[176:179]\n"
        "v_exp_f32 v115, v115\n"
        "v_mfma_f32_16x16x32_bf16 v[176:179], v[160:163], a[220:223], v[176:179]\n"
        "v_exp_f32 v69, v69\n"
        "v_mfma_f32_16x16x32_bf16 v[196:199], v[164:167], a[220:223], v[196:199]\n"
        "ds_read_b128 a[220:223], v254 offset:15360\n"
        "v_cvt_pk_bf16_f32 v93, v114, v115\n"
        "v_mfma_f32_16x16x32_bf16 v[196:199], v[152:155], a[216:219], v[196:199]\n"
        "s_waitcnt lgkmcnt(2)\n"
        "ds_read_b128 a[216:219], v254 offset:12320\n"
        "v_cvt_pk_bf16_f32 v94, v68, v69\n"
        "v_mfma_f32_16x16x32_bf16 v[200:203], v[152:155], a[224:227], v[200:203]\n"
        "v_exp_f32 v70, v70\n"
        "v_mfma_f32_16x16x32_bf16 v[200:203], v[164:167], a[228:231], v[200:203]\n"
        "s_waitcnt vmcnt(0)\n"
        "v_exp_f32 v71, v71\n"
        "v_mfma_f32_16x16x32_bf16 v[180:183], v[160:163], a[228:231], v[180:183]\n"
        "s_barrier\n"
        "ds_read_b128 a[228:231], v254 offset:15392\n"
        "v_cvt_pk_bf16_f32 v95, v70, v71\n"
        "v_mfma_f32_16x16x32_bf16 v[180:183], v[156:159], a[224:227], v[180:183]\n"
        "s_waitcnt lgkmcnt(2)\n"
        "ds_read_b128 a[224:227], v248 offset:12304\n"
        "v_mfma_f32_16x16x32_bf16 v[184:187], v[156:159], a[232:235], v[184:187]\n"
        "v_mfma_f32_16x16x32_bf16 v[184:187], v[160:163], a[220:223], v[184:187]\n"
        "v_mfma_f32_16x16x32_bf16 v[204:207], v[164:167], a[220:223], v[204:207]\n"
        "ds_read_b128 a[220:223], v248 offset:15376\n"
        "v_mfma_f32_16x16x32_bf16 v[204:207], v[152:155], a[232:235], v[204:207]\n"
        "s_waitcnt lgkmcnt(2)\n"
        "ds_read_b128 a[232:235], v254 offset:12304\n"
        "v_mfma_f32_16x16x32_bf16 v[192:195], v[152:155], a[216:219], v[192:195]\n"
        "ds_read_b128 a[236:239], v240 offset:0\n"
        "v_mfma_f32_16x16x32_bf16 v[192:195], v[164:167], a[228:231], v[192:195]\n"
        "ds_read_b128 a[240:243], v241 offset:0\n"
        "v_mfma_f32_16x16x32_bf16 v[172:175], v[160:163], a[228:231], v[172:175]\n"
        "ds_read_b128 a[228:231], v254 offset:15376\n"
        "v_mfma_f32_16x16x32_bf16 v[172:175], v[156:159], a[216:219], v[172:175]\n"
        "s_waitcnt lgkmcnt(4)\n"
        "ds_read_b128 a[244:247], v240 offset:128\n"
        "v_mfma_f32_16x16x32_bf16 v[212:215], v[156:159], a[224:227], v[212:215]\n"
        "ds_read_b128 a[248:251], v241 offset:128\n"
        "v_mfma_f32_16x16x32_bf16 v[212:215], v[160:163], a[220:223], v[212:215]\n"
        "ds_read_b128 a[216:219], v240 offset:256\n"
        "v_mfma_f32_16x16x32_bf16 v[216:219], v[164:167], a[220:223], v[216:219]\n"
        "ds_read_b128 a[220:223], v241 offset:256\n"
        "v_mfma_f32_16x16x32_bf16 v[216:219], v[152:155], a[224:227], v[216:219]\n"
        "s_waitcnt lgkmcnt(4)\n"
        "v_mfma_f32_16x16x32_bf16 v[224:227], v[152:155], a[232:235], v[224:227]\n"
        "v_mfma_f32_16x16x32_bf16 v[224:227], v[164:167], a[228:231], v[224:227]\n"
        "ds_write_b32 v245, v96 offset:4608\n"
        "v_mfma_f32_16x16x32_bf16 v[220:223], v[160:163], a[228:231], v[220:223]\n"
        "v_mfma_f32_16x16x32_bf16 v[220:223], v[156:159], a[232:235], v[220:223]\n"
        "ds_write_b32 v245, v97 offset:5632\n"
        "v_mfma_f32_16x16x32_bf16 v[112:115], a[72:75], a[236:239], 0\n"
        "v_mov_b64 v[124:125], 0\n"
        "v_cvt_pk_bf16_f32 v232, v176, v180\n"
        "v_mfma_f32_16x16x32_bf16 v[112:115], a[76:79], a[240:243], v[112:115]\n"
        "s_waitcnt lgkmcnt(2)\n"
        "v_cvt_pk_bf16_f32 v233, v184, v172\n"
        "v_cvt_pk_bf16_f32 v234, v177, v181\n"
        "v_mfma_f32_16x16x32_bf16 v[112:115], a[80:83], a[244:247], v[112:115]\n"
        "buffer_store_dwordx2 v[232:233], v250, s[56:59], 0 offen offset:0 sc1\n"
        "v_cvt_pk_bf16_f32 v235, v185, v173\n"
        "v_mfma_f32_16x16x32_bf16 v[112:115], a[84:87], a[248:251], v[112:115]\n"
        "v_cvt_pk_bf16_f32 v232, v178, v182\n"
        "v_cvt_pk_bf16_f32 v233, v186, v174\n"
        "v_mfma_f32_16x16x32_bf16 v[112:115], a[88:91], a[216:219], v[112:115]\n"
        "v_mfma_f32_16x16x32_bf16 v[112:115], a[92:95], a[220:223], v[112:115]\n"
        "ds_write_b32 v245, v100 offset:0\n"
        "v_smfmac_f32_16x16x64_bf16 v[124:127], a[252:255], v[88:95], v237\n"
        "s_nop 1\n"
        "v_mfma_f32_16x16x32_bf16 v[80:83], v[132:135], v[92:95], 0\n"
        "buffer_store_dwordx2 v[234:235], v250, s[56:59], 0 offen offset:384 sc1\n"
        "s_nop 0\n"
        "v_mfma_f32_16x16x32_bf16 v[80:83], v[128:131], v[88:91], v[80:83]\n"
        "s_add_u32 s41, s30, s40\n"
        "v_cvt_pk_bf16_f32 v152, v112, v113\n"
        "v_cvt_pk_bf16_f32 v153, v114, v115\n"
        "v_mfma_f32_16x16x32_bf16 v[84:87], v[136:139], v[88:91], 0\n"
        "v_cvt_pk_bf16_f32 v234, v179, v183\n"
        "v_cvt_pk_bf16_f32 v235, v187, v175\n"
        "v_mfma_f32_16x16x32_bf16 v[84:87], v[140:143], v[92:95], v[84:87]\n"
        "v_add_f32 v125, v124, v125\n"
        "v_mfma_f32_16x16x32_bf16 v[96:99], v[148:151], v[92:95], 0\n"
        "buffer_store_dwordx2 v[232:233], v250, s[56:59], 0 offen offset:768 sc1\n"
        "v_mfma_f32_16x16x32_bf16 v[96:99], v[144:147], v[88:91], v[96:99]\n"
        "s_add_u32 s35, s47, 786432\n"
        "v_rcp_f32 v124, v125\n"
        "v_mfma_f32_16x16x32_bf16 v[116:119], a[116:119], a[220:223], 0\n"
        "s_sub_u32 s46, s35, 100663296\n"
        "v_mul_f32 v80, v124, v80\n"
        "v_mul_f32 v81, v124, v81\n"
        "v_mfma_f32_16x16x32_bf16 v[116:119], a[112:115], a[216:219], v[116:119]\n"
        "s_cmp_lt_u32 s46, 786432\n"
        "v_mul_f32 v82, v124, v82\n"
        "v_mul_f32 v83, v124, v83\n"
        "v_mfma_f32_16x16x32_bf16 v[116:119], a[108:111], a[248:251], v[116:119]\n"
        "buffer_store_dwordx2 v[234:235], v250, s[56:59], 0 offen offset:1152 sc1\n"
        "v_cvt_pk_bf16_f32 v80, v80, v81\n"
        "v_mfma_f32_16x16x32_bf16 v[116:119], a[104:107], a[244:247], v[116:119]\n"
        "s_cselect_b32 s35, s46, s35\n"
        "v_cvt_pk_bf16_f32 v81, v82, v83\n"
        "v_cvt_pk_bf16_f32 v232, v196, v200\n"
        "v_mfma_f32_16x16x32_bf16 v[116:119], a[100:103], a[240:243], v[116:119]\n"
        "v_cvt_pk_bf16_f32 v233, v204, v192\n"
        "v_mul_f32 v84, v124, v84\n"
        "v_mfma_f32_16x16x32_bf16 v[116:119], a[96:99], a[236:239], v[116:119]\n"
        "v_mul_f32 v85, v124, v85\n"
        "v_mul_f32 v86, v124, v86\n"
        "v_mfma_f32_16x16x32_bf16 v[120:123], a[120:123], a[236:239], 0\n"
        "ds_write_b32 v245, v101 offset:1024\n"
        "v_mfma_f32_16x16x32_bf16 v[120:123], a[124:127], a[240:243], v[120:123]\n"
        "buffer_store_dwordx2 v[232:233], v250, s[60:63], 0 offen offset:0 sc1\n"
        "v_mul_f32 v87, v124, v87\n"
        "v_mfma_f32_16x16x32_bf16 v[120:123], a[128:131], a[244:247], v[120:123]\n"
        "v_cvt_pk_bf16_f32 v82, v84, v85\n"
        "v_cvt_pk_bf16_f32 v83, v86, v87\n"
        "v_mfma_f32_16x16x32_bf16 v[120:123], a[132:135], a[248:251], v[120:123]\n"
        "v_mul_f32 v96, v124, v96\n"
        "v_mul_f32 v97, v124, v97\n"
        "v_mfma_f32_16x16x32_bf16 v[120:123], a[136:139], a[216:219], v[120:123]\n"
        "v_mul_f32 v98, v124, v98\n"
        "v_mul_f32 v99, v124, v99\n"
        "v_mfma_f32_16x16x32_bf16 v[120:123], a[140:143], a[220:223], v[120:123]\n"
        "v_cvt_pk_bf16_f32 v234, v197, v201\n"
        "v_cvt_pk_bf16_f32 v235, v205, v193\n"
        "v_mfma_f32_16x16x32_bf16 v[112:115], a[20:23], a[220:223], 0\n"
        "buffer_store_dwordx2 v[234:235], v250, s[60:63], 0 offen offset:384 sc1\n"
        "v_cvt_pk_bf16_f32 v154, v116, v117\n"
        "v_mfma_f32_16x16x32_bf16 v[112:115], a[16:19], a[216:219], v[112:115]\n"
        "v_cvt_pk_bf16_f32 v155, v118, v119\n"
        "v_cvt_pk_bf16_f32 v96, v96, v97\n"
        "v_mfma_f32_16x16x32_bf16 v[112:115], a[12:15], a[248:251], v[112:115]\n"
        "ds_write_b32 v245, v96 offset:4864\n"
        "v_mfma_f32_16x16x32_bf16 v[112:115], a[8:11], a[244:247], v[112:115]\n"
        "v_cvt_pk_bf16_f32 v97, v98, v99\n"
        "v_max3_u32 v236, v236, v124, v125\n"
        "v_mfma_f32_16x16x32_bf16 v[112:115], a[4:7], a[240:243], v[112:115]\n"
        "v_cvt_pk_bf16_f32 v232, v198, v202\n"
        "v_cvt_pk_bf16_f32 v233, v206, v194\n"
        "v_mfma_f32_16x16x32_bf16 v[112:115], a[0:3], a[236:239], v[112:115]\n"
        "buffer_store_dwordx2 v[232:233], v250, s[60:63], 0 offen offset:768 sc1\n"
        "v_cmp_lt_u32 vcc, s24, v236\n"
        "v_mfma_f32_16x16x32_bf16 v[116:119], a[24:27], a[236:239], 0\n"
        "v_cvt_pk_bf16_f32 v168, v120, v121\n"
        "v_cvt_pk_bf16_f32 v169, v122, v123\n"
        "v_mfma_f32_16x16x32_bf16 v[116:119], a[28:31], a[240:243], v[116:119]\n"
        "s_cmp_lg_u64 vcc, 0\n"
        "s_addc_u32 s25, s25, s25\n"
        "v_cvt_pk_bf16_f32 v234, v199, v203\n"
        "v_mfma_f32_16x16x32_bf16 v[116:119], a[32:35], a[244:247], v[116:119]\n"
        "v_cvt_pk_bf16_f32 v235, v207, v195\n"
        "v_cvt_pk_bf16_f32 v156, v112, v113\n"
        "v_mfma_f32_16x16x32_bf16 v[116:119], a[36:39], a[248:251], v[116:119]\n"
        "buffer_store_dwordx2 v[234:235], v250, s[60:63], 0 offen offset:1152 sc1\n"
        "v_cvt_pk_bf16_f32 v157, v114, v115\n"
        "v_mfma_f32_16x16x32_bf16 v[116:119], a[40:43], a[216:219], v[116:119]\n"
        "v_cvt_pk_bf16_f32 v232, v212, v220\n"
        "v_cvt_pk_bf16_f32 v234, v213, v221\n"
        "v_mfma_f32_16x16x32_bf16 v[116:119], a[44:47], a[220:223], v[116:119]\n"
        "ds_write_b32 v245, v102 offset:2048\n"
        "v_mfma_f32_16x16x32_bf16 v[120:123], a[68:71], a[220:223], 0\n"
        "v_xor_b32 v251, 0x2000, v251\n"
        "v_xor_b32 v252, 0x2000, v252\n"
        "v_mfma_f32_16x16x32_bf16 v[120:123], a[64:67], a[216:219], v[120:123]\n"
        "buffer_store_dword v232, v238, s[56:59], 0 offen offset:0 sc1\n"
        "v_mfma_f32_16x16x32_bf16 v[120:123], a[60:63], a[248:251], v[120:123]\n"
        "ds_read_b128 a[224:227], v240 offset:1536\n"
        "v_cvt_pk_bf16_f32 v158, v116, v117\n"
        "v_mfma_f32_16x16x32_bf16 v[120:123], a[56:59], a[244:247], v[120:123]\n"
        "ds_read_b128 a[228:231], v241 offset:1536\n"
        "v_cvt_pk_bf16_f32 v159, v118, v119\n"
        "v_mfma_f32_16x16x32_bf16 v[120:123], a[52:55], a[240:243], v[120:123]\n"
        "ds_read_b128 a[232:235], v240 offset:1664\n"
        "v_cvt_pk_bf16_f32 v232, v214, v222\n"
        "v_mfma_f32_16x16x32_bf16 v[120:123], a[48:51], a[236:239], v[120:123]\n"
        "buffer_store_dword v234, v238, s[56:59], 0 offen offset:384 sc1\n"
        "v_mfma_f32_16x16x32_bf16 v[112:115], a[236:239], a[144:147], 0\n"
        "v_add_u32 v247, s45, v247\n"
        "v_cvt_pk_bf16_f32 v234, v215, v223\n"
        "v_mfma_f32_16x16x32_bf16 v[112:115], a[240:243], a[148:151], v[112:115]\n"
        "ds_write_b32 v245, v103 offset:3072\n"
        "v_mfma_f32_16x16x32_bf16 v[112:115], a[244:247], a[152:155], v[112:115]\n"
        "s_nop 0\n"
        "v_cvt_pk_bf16_f32 v160, v120, v121\n"
        "v_cvt_pk_bf16_f32 v161, v122, v123\n"
        "v_mfma_f32_16x16x32_bf16 v[112:115], a[248:251], a[156:159], v[112:115]\n"
        "v_cvt_pk_bf16_f32 v166, v120, v121\n"
        "v_cvt_pk_bf16_f32 v167, v122, v123\n"
        "v_mfma_f32_16x16x32_bf16 v[112:115], a[216:219], a[160:163], v[112:115]\n"
        "buffer_store_dword v232, v238, s[56:59], 0 offen offset:768 sc1\n"
        "v_mfma_f32_16x16x32_bf16 v[112:115], a[220:223], a[164:167], v[112:115]\n"
        "ds_write_b32 v245, v97 offset:5888\n"
        "v_mfma_f32_16x16x32_bf16 v[116:119], a[220:223], a[188:191], 0\n"
        "v_cvt_pk_bf16_f32 v232, v216, v224\n"
        "s_nop 0\n"
        "v_mfma_f32_16x16x32_bf16 v[116:119], a[216:219], a[184:187], v[116:119]\n"
        "s_nop 2\n"
        "v_mfma_f32_16x16x32_bf16 v[116:119], a[248:251], a[180:183], v[116:119]\n"
        "buffer_store_dword v234, v238, s[56:59], 0 offen offset:1152 sc1\n"
        "v_mfma_f32_16x16x32_bf16 v[116:119], a[244:247], a[176:179], v[116:119]\n"
        "v_cvt_pk_bf16_f32 v128, v112, v113\n"
        "v_cvt_pk_bf16_f32 v129, v114, v115\n"
        "v_mfma_f32_16x16x32_bf16 v[116:119], a[240:243], a[172:175], v[116:119]\n"
        "ds_write_b32 v245, v64 offset:256\n"
        "v_mfma_f32_16x16x32_bf16 v[116:119], a[236:239], a[168:171], v[116:119]\n"
        "v_cvt_pk_bf16_f32 v234, v217, v225\n"
        "s_nop 0\n"
        "v_mfma_f32_16x16x32_bf16 v[120:123], a[236:239], a[192:195], 0\n"
        "ds_read_b128 a[236:239], v241 offset:1664\n"
        "s_nop 0\n"
        "v_mfma_f32_16x16x32_bf16 v[120:123], a[240:243], a[196:199], v[120:123]\n"
        "buffer_store_dword v232, v238, s[60:63], 0 offen offset:0 sc1\n"
        "v_mfma_f32_16x16x32_bf16 v[120:123], a[244:247], a[200:203], v[120:123]\n"
        "ds_read_b128 a[240:243], v240 offset:1792\n"
        "ds_read_b128 a[244:247], v241 offset:1792\n"
        "v_mfma_f32_16x16x32_bf16 v[120:123], a[248:251], a[204:207], v[120:123]\n"
        "v_cvt_pk_bf16_f32 v136, v116, v117\n"
        "v_cvt_pk_bf16_f32 v137, v118, v119\n"
        "v_mfma_f32_16x16x32_bf16 v[120:123], a[216:219], a[208:211], v[120:123]\n"
        "v_cvt_pk_bf16_f32 v232, v218, v226\n"
        "v_mfma_f32_16x16x32_bf16 v[120:123], a[220:223], a[212:215], v[120:123]\n"
        "s_waitcnt lgkmcnt(4)\n"
        "buffer_store_dword v234, v238, s[60:63], 0 offen offset:384 sc1\n"
        "v_mfma_f32_16x16x32_bf16 v[112:115], a[72:75], a[224:227], 0\n"
        "s_nop 2\n"
        "v_mfma_f32_16x16x32_bf16 v[112:115], a[76:79], a[228:231], v[112:115]\n"
        "v_cvt_pk_bf16_f32 v234, v219, v227\n"
        "s_nop 0\n"
        "v_mfma_f32_16x16x32_bf16 v[112:115], a[80:83], a[232:235], v[112:115]\n"
        "s_waitcnt lgkmcnt(0)\n"
        "v_cvt_pk_bf16_f32 v144, v120, v121\n"
        "v_cvt_pk_bf16_f32 v145, v122, v123\n"
        "v_mfma_f32_16x16x32_bf16 v[112:115], a[84:87], a[236:239], v[112:115]\n"
        "ds_write_b32 v245, v65 offset:1280\n"
        "v_mfma_f32_16x16x32_bf16 v[112:115], a[88:91], a[240:243], v[112:115]\n"
        "buffer_store_dword v232, v238, s[60:63], 0 offen offset:768 sc1\n"
        "v_mfma_f32_16x16x32_bf16 v[112:115], a[92:95], a[244:247], v[112:115]\n"
        "s_nop 2\n"
        "v_mfma_f32_16x16x32_bf16 v[116:119], a[116:119], a[244:247], 0\n"
        "s_nop 2\n"
        "v_mfma_f32_16x16x32_bf16 v[116:119], a[112:115], a[240:243], v[116:119]\n"
        "v_cvt_pk_bf16_f32 v172, v112, v113\n"
        "v_cvt_pk_bf16_f32 v173, v114, v115\n"
        "v_mfma_f32_16x16x32_bf16 v[116:119], a[108:111], a[236:239], v[116:119]\n"
        "buffer_store_dword v234, v238, s[60:63], 0 offen offset:1152 sc1\n"
        "v_mfma_f32_16x16x32_bf16 v[116:119], a[104:107], a[232:235], v[116:119]\n"
        "ds_write_b32 v245, v66 offset:2304\n"
        "v_mfma_f32_16x16x32_bf16 v[116:119], a[100:103], a[228:231], v[116:119]\n"
        "v_mfma_f32_16x16x32_bf16 v[116:119], a[96:99], a[224:227], v[116:119]\n"
        "s_mov_b32 m0, s48\n"
        "s_nop 1\n"
        "v_mfma_f32_16x16x32_bf16 v[120:123], a[120:123], a[224:227], 0\n"
        "ds_read_b128 v[162:165], v251 offset:0\n"
        "s_nop 0\n"
        "v_mfma_f32_16x16x32_bf16 v[120:123], a[124:127], a[228:231], v[120:123]\n"
        "buffer_load_dwordx4 v249, s[12:15], s36 offen offset:0 lds\n"
        "v_mfma_f32_16x16x32_bf16 v[120:123], a[128:131], a[232:235], v[120:123]\n"
        "v_cvt_pk_bf16_f32 v174, v116, v117\n"
        "v_cvt_pk_bf16_f32 v175, v118, v119\n"
        "v_mfma_f32_16x16x32_bf16 v[120:123], a[132:135], a[236:239], v[120:123]\n"
        ".long 0xbec200ff\n"
        "46:\n"
        "s_branch 61b\n"
        "ds_write_b32 v245, v67 offset:3328\n"
        "v_mfma_f32_16x16x32_bf16 v[120:123], a[136:139], a[240:243], v[120:123]\n"
        "ds_write_b32 v245, v72 offset:512\n"
        "v_mfma_f32_16x16x32_bf16 v[120:123], a[140:143], a[244:247], v[120:123]\n"
        "s_nop 2\n"
        "v_mfma_f32_16x16x32_bf16 v[112:115], a[20:23], a[244:247], 0\n"
        "s_nop 2\n"
        "v_mfma_f32_16x16x32_bf16 v[112:115], a[16:19], a[240:243], v[112:115]\n"
        "v_cvt_pk_bf16_f32 v190, v120, v121\n"
        "v_cvt_pk_bf16_f32 v191, v122, v123\n"
        "v_mfma_f32_16x16x32_bf16 v[112:115], a[12:15], a[236:239], v[112:115]\n"
        "ds_write_b32 v245, v73 offset:1536\n"
        "v_mfma_f32_16x16x32_bf16 v[112:115], a[8:11], a[232:235], v[112:115]\n"
        "ds_write_b32 v245, v74 offset:2560\n"
        "v_mfma_f32_16x16x32_bf16 v[112:115], a[4:7], a[228:231], v[112:115]\n"
        "ds_read_b128 v[182:185], v251 offset:512\n"
        "v_mfma_f32_16x16x32_bf16 v[112:115], a[0:3], a[224:227], v[112:115]\n"
        "s_nop 2\n"
        "v_mfma_f32_16x16x32_bf16 v[116:119], a[24:27], a[224:227], 0\n"
        "s_nop 2\n"
        "v_mfma_f32_16x16x32_bf16 v[116:119], a[28:31], a[228:231], v[116:119]\n"
        "v_cvt_pk_bf16_f32 v176, v112, v113\n"
        "v_cvt_pk_bf16_f32 v177, v114, v115\n"
        "v_mfma_f32_16x16x32_bf16 v[116:119], a[32:35], a[232:235], v[116:119]\n"
        "ds_write_b32 v245, v75 offset:3584\n"
        "v_mfma_f32_16x16x32_bf16 v[116:119], a[36:39], a[236:239], v[116:119]\n"
        "buffer_load_dwordx4 v253, s[12:15], s36 offen offset:1024 lds\n"
        "v_mfma_f32_16x16x32_bf16 v[116:119], a[40:43], a[240:243], v[116:119]\n"
        "ds_write_b32 v245, v80 offset:768\n"
        "v_mfma_f32_16x16x32_bf16 v[116:119], a[44:47], a[244:247], v[116:119]\n"
        "s_nop 2\n"
        "v_mfma_f32_16x16x32_bf16 v[120:123], a[68:71], a[244:247], 0\n"
        "s_nop 2\n"
        "v_mfma_f32_16x16x32_bf16 v[120:123], a[64:67], a[240:243], v[120:123]\n"
        "v_cvt_pk_bf16_f32 v178, v116, v117\n"
        "v_cvt_pk_bf16_f32 v179, v118, v119\n"
        "v_mfma_f32_16x16x32_bf16 v[120:123], a[60:63], a[236:239], v[120:123]\n"
        "ds_write_b32 v245, v81 offset:1792\n"
        "v_mfma_f32_16x16x32_bf16 v[120:123], a[56:59], a[232:235], v[120:123]\n"
        "ds_write_b32 v245, v82 offset:2816\n"
        "v_mfma_f32_16x16x32_bf16 v[120:123], a[52:55], a[228:231], v[120:123]\n"
        "ds_write_b32 v245, v83 offset:3840\n"
        "v_mfma_f32_16x16x32_bf16 v[120:123], a[48:51], a[224:227], v[120:123]\n"
        "s_nop 2\n"
        "v_mfma_f32_16x16x32_bf16 v[112:115], a[224:227], a[144:147], 0\n"
        "s_nop 2\n"
        "v_mfma_f32_16x16x32_bf16 v[112:115], a[228:231], a[148:151], v[112:115]\n"
        "v_cvt_pk_bf16_f32 v180, v120, v121\n"
        "v_cvt_pk_bf16_f32 v181, v122, v123\n"
        "v_mfma_f32_16x16x32_bf16 v[112:115], a[232:235], a[152:155], v[112:115]\n"
        "v_cvt_pk_bf16_f32 v186, v120, v121\n"
        "v_cvt_pk_bf16_f32 v187, v122, v123\n"
        "v_mfma_f32_16x16x32_bf16 v[112:115], a[236:239], a[156:159], v[112:115]\n"
        "s_add_u32 m0, s41, 0\n"
        "v_mfma_f32_16x16x32_bf16 v[112:115], a[240:243], a[160:163], v[112:115]\n"
        "ds_read_b128 a[248:251], v240 offset:12288\n"
        "v_mfma_f32_16x16x32_bf16 v[112:115], a[244:247], a[164:167], v[112:115]\n"
        "buffer_load_dwordx4 v242, s[8:11], s47 offen lds\n"
        "v_mfma_f32_16x16x32_bf16 v[96:99], v[152:155], v[156:159], v[0:3]\n"
        "s_waitcnt lgkmcnt(2)\n"
        "ds_read_b128 a[216:219], v241 offset:12288\n"
        "s_nop 0\n"
        "v_mfma_f32_16x16x32_bf16 v[96:99], v[168:171], v[160:163], v[96:99]\n"
        "ds_read_b128 a[220:223], v240 offset:12416\n"
        "s_nop 0\n"
        "v_mfma_f32_16x16x32_bf16 v[100:103], v[172:175], v[156:159], v[4:7]\n"
        "s_sub_u32 s31, s30, s28\n"
        "v_cvt_pk_bf16_f32 v130, v112, v113\n"
        "v_cvt_pk_bf16_f32 v131, v114, v115\n"
        "v_mfma_f32_16x16x32_bf16 v[100:103], v[188:191], v[164:167], v[100:103]\n"
        "s_sub_u32 s45, s28, s29\n"
        "ds_read_b128 v[162:165], v252 offset:0\n"
        "v_mfma_f32_16x16x32_bf16 v[116:119], a[244:247], a[188:191], 0\n"
        "s_add_u32 s28, s28, s31\n"
        "v_exp_f32 v96, v96\n"
        "v_mfma_f32_16x16x32_bf16 v[116:119], a[240:243], a[184:187], v[116:119]\n"
        "s_mov_b32 s30, s29\n"
        "v_exp_f32 v97, v97\n"
        "v_mfma_f32_16x16x32_bf16 v[116:119], a[236:239], a[180:183], v[116:119]\n"
        "s_add_u32 s29, s29, s45\n"
        "v_exp_f32 v98, v98\n"
        "v_mfma_f32_16x16x32_bf16 v[116:119], a[232:235], a[176:179], v[116:119]\n"
        "v_exp_f32 v100, v100\n"
        "v_mfma_f32_16x16x32_bf16 v[116:119], a[228:231], a[172:175], v[116:119]\n"
        "v_exp_f32 v99, v99\n"
        "v_mfma_f32_16x16x32_bf16 v[116:119], a[224:227], a[168:171], v[116:119]\n"
        "v_exp_f32 v101, v101\n"
        "v_mfma_f32_16x16x32_bf16 v[104:107], v[152:155], v[176:179], v[16:19]\n"
        "v_exp_f32 v102, v102\n"
        "v_mfma_f32_16x16x32_bf16 v[104:107], v[168:171], v[180:183], v[104:107]\n"
        "s_add_u32 m0, s41, 1024\n"
        "v_cvt_pk_bf16_f32 v64, v96, v97\n"
        "v_cvt_pk_bf16_f32 v65, v98, v99\n"
        "v_mfma_f32_16x16x32_bf16 v[108:111], v[172:175], v[176:179], v[20:23]\n"
        "v_cvt_pk_bf16_f32 v138, v116, v117\n"
        "v_cvt_pk_bf16_f32 v139, v118, v119\n"
        "v_mfma_f32_16x16x32_bf16 v[108:111], v[188:191], v[184:187], v[108:111]\n"
        "buffer_load_dwordx4 v243, s[8:11], s47 offen lds\n"
        "v_mfma_f32_16x16x32_bf16 v[120:123], a[224:227], a[192:195], 0\n"
        "ds_read_b128 a[224:227], v241 offset:12416\n"
        "v_cvt_pk_bf16_f32 v66, v100, v101\n"
        "v_mfma_f32_16x16x32_bf16 v[120:123], a[228:231], a[196:199], v[120:123]\n"
        "ds_read_b128 a[228:231], v240 offset:12544\n"
        "v_mfma_f32_16x16x32_bf16 v[120:123], a[232:235], a[200:203], v[120:123]\n"
        "ds_read_b128 a[232:235], v241 offset:12544\n"
        "v_mfma_f32_16x16x32_bf16 v[120:123], a[236:239], a[204:207], v[120:123]\n"
        "ds_read_b128 v[202:205], v251 offset:4096\n"
        "v_mfma_f32_16x16x32_bf16 v[120:123], a[240:243], a[208:211], v[120:123]\n"
        "ds_read_b128 v[182:185], v252 offset:512\n"
        "v_mfma_f32_16x16x32_bf16 v[120:123], a[244:247], a[212:215], v[120:123]\n"
        "s_waitcnt lgkmcnt(4)\n"
        "v_exp_f32 v103, v103\n"
        "v_mfma_f32_16x16x32_bf16 v[112:115], a[0:3], a[248:251], 0\n"
        "v_cvt_pk_bf16_f32 v67, v102, v103\n"
        "s_nop 0\n"
        "v_mfma_f32_16x16x32_bf16 v[112:115], a[4:7], a[216:219], v[112:115]\n"
        "v_exp_f32 v104, v104\n"
        "v_mfma_f32_16x16x32_bf16 v[112:115], a[8:11], a[220:223], v[112:115]\n"
        "v_exp_f32 v105, v105\n"
        "v_mfma_f32_16x16x32_bf16 v[112:115], a[12:15], a[224:227], v[112:115]\n"
        "s_waitcnt lgkmcnt(0)\n"
        "v_exp_f32 v106, v106\n"
        "v_mfma_f32_16x16x32_bf16 v[112:115], a[16:19], a[228:231], v[112:115]\n"
        "v_cvt_pk_bf16_f32 v72, v104, v105\n"
        "v_cvt_pk_bf16_f32 v146, v120, v121\n"
        "v_mfma_f32_16x16x32_bf16 v[112:115], a[20:23], a[232:235], v[112:115]\n"
        "s_add_u32 m0, s41, 2048\n"
        "v_exp_f32 v108, v108\n"
        "v_mfma_f32_16x16x32_bf16 v[116:119], a[44:47], a[232:235], 0\n"
        "v_cvt_pk_bf16_f32 v147, v122, v123\n"
        "v_add_u32 v245, s45, v245\n"
        "v_mfma_f32_16x16x32_bf16 v[116:119], a[40:43], a[228:231], v[116:119]\n"
        "buffer_load_dwordx4 v244, s[8:11], s47 offen lds\n"
        "v_mfma_f32_16x16x32_bf16 v[116:119], a[36:39], a[224:227], v[116:119]\n"
        "v_exp_f32 v107, v107\n"
        "v_mfma_f32_16x16x32_bf16 v[116:119], a[32:35], a[220:223], v[116:119]\n"
        "v_exp_f32 v109, v109\n"
        "v_mfma_f32_16x16x32_bf16 v[116:119], a[28:31], a[216:219], v[116:119]\n"
        "v_cvt_pk_bf16_f32 v73, v106, v107\n"
        "v_cvt_pk_bf16_f32 v74, v108, v109\n"
        "v_mfma_f32_16x16x32_bf16 v[116:119], a[24:27], a[248:251], v[116:119]\n"
        "v_exp_f32 v110, v110\n"
        "v_mfma_f32_16x16x32_bf16 v[120:123], a[48:51], a[248:251], 0\n"
        "v_exp_f32 v111, v111\n"
        "v_mfma_f32_16x16x32_bf16 v[120:123], a[52:55], a[216:219], v[120:123]\n"
        "v_cvt_pk_bf16_f32 v75, v110, v111\n"
        "v_cvt_pk_bf16_f32 v196, v112, v113\n"
        "v_mfma_f32_16x16x32_bf16 v[120:123], a[56:59], a[220:223], v[120:123]\n"
        "v_cvt_pk_bf16_f32 v197, v114, v115\n"
        "v_cvt_pk_bf16_f32 v198, v116, v117\n"
        "v_mfma_f32_16x16x32_bf16 v[120:123], a[60:63], a[224:227], v[120:123]\n"
        "v_cvt_pk_bf16_f32 v199, v118, v119\n"
        "v_mfma_f32_16x16x32_bf16 v[120:123], a[64:67], a[228:231], v[120:123]\n"
        "v_mfma_f32_16x16x32_bf16 v[120:123], a[68:71], a[232:235], v[120:123]\n"
        "s_nop 2\n"
        "v_mfma_f32_16x16x32_bf16 v[112:115], a[92:95], a[232:235], 0\n"
        "s_nop 2\n"
        "v_mfma_f32_16x16x32_bf16 v[112:115], a[88:91], a[228:231], v[112:115]\n"
        "s_add_u32 m0, s41, 12288\n"
        "v_cvt_pk_bf16_f32 v200, v120, v121\n"
        "v_cvt_pk_bf16_f32 v201, v122, v123\n"
        "v_mfma_f32_16x16x32_bf16 v[112:115], a[84:87], a[224:227], v[112:115]\n"
        "v_cvt_pk_bf16_f32 v206, v120, v121\n"
        "v_cvt_pk_bf16_f32 v207, v122, v123\n"
        "v_mfma_f32_16x16x32_bf16 v[112:115], a[80:83], a[220:223], v[112:115]\n"
        "buffer_load_dwordx4 v242, s[8:11], s35 offen lds\n"
        "v_mfma_f32_16x16x32_bf16 v[112:115], a[76:79], a[216:219], v[112:115]\n"
        "v_mfma_f32_16x16x32_bf16 v[112:115], a[72:75], a[248:251], v[112:115]\n"
        "s_nop 2\n"
        "v_mfma_f32_16x16x32_bf16 v[96:99], v[152:155], v[196:199], v[32:35]\n"
        "s_nop 2\n"
        "v_mfma_f32_16x16x32_bf16 v[96:99], v[168:171], v[200:203], v[96:99]\n"
        "v_cvt_pk_bf16_f32 v192, v112, v113\n"
        "v_cvt_pk_bf16_f32 v193, v114, v115\n"
        "v_mfma_f32_16x16x32_bf16 v[100:103], v[172:175], v[196:199], v[36:39]\n"
        "v_mfma_f32_16x16x32_bf16 v[100:103], v[188:191], v[204:207], v[100:103]\n"
        "ds_read_b128 v[202:205], v252 offset:4096\n"
        "v_mfma_f32_16x16x32_bf16 v[116:119], a[96:99], a[248:251], 0\n"
        "s_cmp_eq_u32 s37, 100663296\n"
        "v_mfma_f32_16x16x32_bf16 v[116:119], a[100:103], a[216:219], v[116:119]\n"
        "s_cselect_b32 s46, 0, s37\n"
        "v_exp_f32 v96, v96\n"
        "v_mfma_f32_16x16x32_bf16 v[116:119], a[104:107], a[220:223], v[116:119]\n"
        "s_add_u32 s38, s46, s43\n"
        "v_exp_f32 v97, v97\n"
        "v_mfma_f32_16x16x32_bf16 v[116:119], a[108:111], a[224:227], v[116:119]\n"
        "s_add_u32 s39, s46, s44\n"
        "v_exp_f32 v98, v98\n"
        "v_mfma_f32_16x16x32_bf16 v[116:119], a[112:115], a[228:231], v[116:119]\n"
        "s_add_u32 s37, s37, 6291456\n"
        "v_exp_f32 v100, v100\n"
        "v_mfma_f32_16x16x32_bf16 v[116:119], a[116:119], a[232:235], v[116:119]\n"
        "s_add_u32 m0, s41, 13312\n"
        "v_exp_f32 v99, v99\n"
        "v_mfma_f32_16x16x32_bf16 v[120:123], a[140:143], a[232:235], 0\n"
        "v_exp_f32 v101, v101\n"
        "v_mfma_f32_16x16x32_bf16 v[120:123], a[136:139], a[228:231], v[120:123]\n"
        "buffer_load_dwordx4 v243, s[8:11], s35 offen lds\n"
        "v_mfma_f32_16x16x32_bf16 v[120:123], a[132:135], a[224:227], v[120:123]\n"
        "v_cvt_pk_bf16_f32 v80, v96, v97\n"
        "v_cvt_pk_bf16_f32 v194, v116, v117\n"
        "v_mfma_f32_16x16x32_bf16 v[120:123], a[128:131], a[220:223], v[120:123]\n"
        "s_mov_b64 s[58:59], s[18:19]\n"
        "v_cvt_pk_bf16_f32 v195, v118, v119\n"
        "v_cvt_pk_bf16_f32 v81, v98, v99\n"
        "v_mfma_f32_16x16x32_bf16 v[120:123], a[124:127], a[216:219], v[120:123]\n"
        "s_mov_b64 s[62:63], s[18:19]\n"
        "v_exp_f32 v102, v102\n"
        "v_mfma_f32_16x16x32_bf16 v[120:123], a[120:123], a[248:251], v[120:123]\n"
        "v_cvt_pk_bf16_f32 v82, v100, v101\n"
        "s_nop 0\n"
        "v_mfma_f32_16x16x32_bf16 v[112:115], a[248:251], a[144:147], 0\n"
        "v_exp_f32 v103, v103\n"
        "v_mfma_f32_16x16x32_bf16 v[112:115], a[216:219], a[148:151], v[112:115]\n"
        "v_cvt_pk_bf16_f32 v83, v102, v103\n"
        "s_nop 0\n"
        "v_mfma_f32_16x16x32_bf16 v[112:115], a[220:223], a[152:155], v[112:115]\n"
        "ds_read_b128 a[236:239], v240 offset:13824\n"
        "v_cvt_pk_bf16_f32 v208, v120, v121\n"
        "v_mfma_f32_16x16x32_bf16 v[112:115], a[224:227], a[156:159], v[112:115]\n"
        "ds_read_b128 a[240:243], v241 offset:13824\n"
        "v_cvt_pk_bf16_f32 v209, v122, v123\n"
        "v_mfma_f32_16x16x32_bf16 v[112:115], a[228:231], a[160:163], v[112:115]\n"
        "ds_read_b128 a[244:247], v240 offset:13952\n"
        "v_mfma_f32_16x16x32_bf16 v[112:115], a[232:235], a[164:167], v[112:115]\n"
        "s_nop 2\n"
        "v_mfma_f32_16x16x32_bf16 v[104:107], v[192:195], v[156:159], v[8:11]\n"
        "s_nop 2\n"
        "v_mfma_f32_16x16x32_bf16 v[104:107], v[208:211], v[160:163], v[104:107]\n"
        "s_add_u32 m0, s41, 14336\n"
        "v_cvt_pk_bf16_f32 v132, v112, v113\n"
        "v_cvt_pk_bf16_f32 v133, v114, v115\n"
        "v_mfma_f32_16x16x32_bf16 v[116:119], a[232:235], a[188:191], 0\n"
        "v_mfma_f32_16x16x32_bf16 v[116:119], a[228:231], a[184:187], v[116:119]\n"
        "buffer_load_dwordx4 v244, s[8:11], s35 offen lds\n"
        "v_mfma_f32_16x16x32_bf16 v[116:119], a[224:227], a[180:183], v[116:119]\n"
        "v_mfma_f32_16x16x32_bf16 v[116:119], a[220:223], a[176:179], v[116:119]\n"
        "v_exp_f32 v104, v104\n"
        "v_mfma_f32_16x16x32_bf16 v[116:119], a[216:219], a[172:175], v[116:119]\n"
        "v_exp_f32 v105, v105\n"
        "v_mfma_f32_16x16x32_bf16 v[116:119], a[248:251], a[168:171], v[116:119]\n"
        "v_exp_f32 v106, v106\n"
        "v_mfma_f32_16x16x32_bf16 v[108:111], v[192:195], v[176:179], v[24:27]\n"
        "v_exp_f32 v107, v107\n"
        "v_mfma_f32_16x16x32_bf16 v[108:111], v[208:211], v[180:183], v[108:111]\n"
        "v_cvt_pk_bf16_f32 v68, v104, v105\n"
        "v_cvt_pk_bf16_f32 v69, v106, v107\n"
        "v_mfma_f32_16x16x32_bf16 v[120:123], a[248:251], a[192:195], 0\n"
        "ds_read_b128 a[248:251], v241 offset:13952\n"
        "v_cvt_pk_bf16_f32 v140, v116, v117\n"
        "v_mfma_f32_16x16x32_bf16 v[120:123], a[216:219], a[196:199], v[120:123]\n"
        "ds_read_b128 a[216:219], v240 offset:14080\n"
        "v_cvt_pk_bf16_f32 v141, v118, v119\n"
        "v_mfma_f32_16x16x32_bf16 v[120:123], a[220:223], a[200:203], v[120:123]\n"
        "ds_read_b128 a[220:223], v241 offset:14080\n"
        "v_mfma_f32_16x16x32_bf16 v[120:123], a[224:227], a[204:207], v[120:123]\n"
        "v_exp_f32 v108, v108\n"
        "v_mfma_f32_16x16x32_bf16 v[120:123], a[228:231], a[208:211], v[120:123]\n"
        "v_exp_f32 v109, v109\n"
        "v_mfma_f32_16x16x32_bf16 v[120:123], a[232:235], a[212:215], v[120:123]\n"
        "v_exp_f32 v110, v110\n"
        "v_mfma_f32_16x16x32_bf16 v[96:99], v[192:195], v[196:199], v[40:43]\n"
        "s_waitcnt lgkmcnt(1)\n"
        "v_exp_f32 v111, v111\n"
        "v_mfma_f32_16x16x32_bf16 v[96:99], v[208:211], v[200:203], v[96:99]\n"
        "v_cvt_pk_bf16_f32 v76, v108, v109\n"
        "v_cvt_pk_bf16_f32 v77, v110, v111\n"
        "v_mfma_f32_16x16x32_bf16 v[112:115], a[72:75], a[236:239], 0\n"
        "s_nop 0\n"
        "v_cvt_pk_bf16_f32 v148, v120, v121\n"
        "v_cvt_pk_bf16_f32 v149, v122, v123\n"
        "v_mfma_f32_16x16x32_bf16 v[112:115], a[76:79], a[240:243], v[112:115]\n"
        "v_mfma_f32_16x16x32_bf16 v[112:115], a[80:83], a[244:247], v[112:115]\n"
        "v_exp_f32 v96, v96\n"
        "v_mfma_f32_16x16x32_bf16 v[112:115], a[84:87], a[248:251], v[112:115]\n"
        "v_exp_f32 v97, v97\n"
        "v_mfma_f32_16x16x32_bf16 v[112:115], a[88:91], a[216:219], v[112:115]\n"
        "s_waitcnt lgkmcnt(0)\n"
        "v_exp_f32 v98, v98\n"
        "v_mfma_f32_16x16x32_bf16 v[112:115], a[92:95], a[220:223], v[112:115]\n"
        "v_exp_f32 v99, v99\n"
        "v_mfma_f32_16x16x32_bf16 v[116:119], a[116:119], a[220:223], 0\n"
        "v_cvt_pk_bf16_f32 v84, v96, v97\n"
        "v_cvt_pk_bf16_f32 v85, v98, v99\n"
        "v_mfma_f32_16x16x32_bf16 v[116:119], a[112:115], a[216:219], v[116:119]\n"
        "v_add_u32 v240, s31, v240\n"
        "v_add_u32 v241, s31, v241\n"
        "v_mfma_f32_16x16x32_bf16 v[116:119], a[108:111], a[248:251], v[116:119]\n"
        "v_cvt_pk_bf16_f32 v212, v112, v113\n"
        "v_cvt_pk_bf16_f32 v213, v114, v115\n"
        "v_mfma_f32_16x16x32_bf16 v[116:119], a[104:107], a[244:247], v[116:119]\n"
        "s_add_u32 s56, s16, s38\n"
        "s_addc_u32 s57, s17, 0\n"
        "v_mfma_f32_16x16x32_bf16 v[116:119], a[100:103], a[240:243], v[116:119]\n"
        "s_add_u32 s60, s16, s39\n"
        "s_addc_u32 s61, s17, 0\n"
        "v_mfma_f32_16x16x32_bf16 v[116:119], a[96:99], a[236:239], v[116:119]\n"
        "s_nop 2\n"
        "v_mfma_f32_16x16x32_bf16 v[120:123], a[120:123], a[236:239], 0\n"
        "s_nop 2\n"
        "v_mfma_f32_16x16x32_bf16 v[120:123], a[124:127], a[240:243], v[120:123]\n"
        "s_add_u32 s47, s47, 6291456\n"
        "v_cvt_pk_bf16_f32 v214, v116, v117\n"
        "v_cvt_pk_bf16_f32 v215, v118, v119\n"
        "v_mfma_f32_16x16x32_bf16 v[120:123], a[128:131], a[244:247], v[120:123]\n"
        "s_add_u32 s36, s36, 2097152\n"
        "v_mfma_f32_16x16x32_bf16 v[120:123], a[132:135], a[248:251], v[120:123]\n"
        "s_xor_b32 s48, s48, 8192\n"
        "v_mfma_f32_16x16x32_bf16 v[120:123], a[136:139], a[216:219], v[120:123]\n"
        "s_barrier\n"
        "v_mfma_f32_16x16x32_bf16 v[120:123], a[140:143], a[220:223], v[120:123]\n"
        "s_nop 2\n"
        "v_mfma_f32_16x16x32_bf16 v[112:115], a[220:223], a[164:167], 0\n"
        "s_nop 2\n"
        "v_mfma_f32_16x16x32_bf16 v[112:115], a[216:219], a[160:163], v[112:115]\n"
        "v_cvt_pk_bf16_f32 v230, v120, v121\n"
        "v_cvt_pk_bf16_f32 v231, v122, v123\n"
        "v_mfma_f32_16x16x32_bf16 v[112:115], a[248:251], a[156:159], v[112:115]\n"
        "v_mfma_f32_16x16x32_bf16 v[112:115], a[244:247], a[152:155], v[112:115]\n"
        "v_mfma_f32_16x16x32_bf16 v[112:115], a[240:243], a[148:151], v[112:115]\n"
        "v_mfma_f32_16x16x32_bf16 v[112:115], a[236:239], a[144:147], v[112:115]\n"
        "s_nop 2\n"
        "v_mfma_f32_16x16x32_bf16 v[100:103], v[212:215], v[156:159], v[12:15]\n"
        "s_nop 2\n"
        "v_mfma_f32_16x16x32_bf16 v[100:103], v[228:231], v[164:167], v[100:103]\n"
        "v_cvt_pk_bf16_f32 v134, v112, v113\n"
        "v_cvt_pk_bf16_f32 v135, v114, v115\n"
        "v_mfma_f32_16x16x32_bf16 v[116:119], a[236:239], a[168:171], 0\n"
        "s_sub_u32 s27, s27, 1\n"
        "v_mfma_f32_16x16x32_bf16 v[116:119], a[240:243], a[172:175], v[116:119]\n"
        "s_cmp_lg_u32 s27, 0\n"
        "v_mfma_f32_16x16x32_bf16 v[116:119], a[244:247], a[176:179], v[116:119]\n"
        "s_cbranch_scc1 10b\n"
        "19:\n"
        "v_mfma_f32_16x16x32_bf16 v[116:119], a[248:251], a[180:183], v[116:119]\n"
        "ds_read_b128 a[224:227], v247 offset:0\n"
        "ds_read_b128 a[228:231], v247 offset:4096\n"
        "v_mfma_f32_16x16x32_bf16 v[116:119], a[216:219], a[184:187], v[116:119]\n"
        "ds_read_b128 a[232:235], v247 offset:8192\n"
        "ds_read_b128 v[156:159], v247 offset:12288\n"
        "v_mfma_f32_16x16x32_bf16 v[116:119], a[220:223], a[188:191], v[116:119]\n"
        "ds_read_b128 v[160:163], v247 offset:16384\n"
        "ds_read_b128 v[164:167], v247 offset:20480\n"
        "v_mfma_f32_16x16x32_bf16 v[104:107], v[212:215], v[176:179], v[28:31]\n"
        "s_and_b32 s46, s42, 1\n"
        "v_exp_f32 v100, v100\n"
        "v_mfma_f32_16x16x32_bf16 v[104:107], v[228:231], v[184:187], v[104:107]\n"
        "s_lshl_b32 s31, s46, 8\n"
        "v_exp_f32 v101, v101\n"
        "v_mfma_f32_16x16x32_bf16 v[120:123], a[220:223], a[212:215], 0\n"
        "s_sub_u32 s31, s45, s31\n"
        "v_cvt_pk_bf16_f32 v142, v116, v117\n"
        "v_cvt_pk_bf16_f32 v143, v118, v119\n"
        "v_mfma_f32_16x16x32_bf16 v[120:123], a[216:219], a[208:211], v[120:123]\n"
        "ds_read_b128 a[96:99], v248 offset:32\n"
        "v_cvt_pk_bf16_f32 v70, v100, v101\n"
        "v_mfma_f32_16x16x32_bf16 v[120:123], a[248:251], a[204:207], v[120:123]\n"
        "ds_read_b128 a[100:103], v248 offset:3104\n"
        "v_mov_b32 v241, 0\n"
        "v_mfma_f32_16x16x32_bf16 v[120:123], a[244:247], a[200:203], v[120:123]\n"
        "ds_read_b128 a[104:107], v248 offset:6176\n"
        "v_mfma_f32_16x16x32_bf16 v[120:123], a[240:243], a[196:199], v[120:123]\n"
        "ds_read_b128 a[108:111], v248 offset:9248\n"
        "v_mfma_f32_16x16x32_bf16 v[120:123], a[236:239], a[192:195], v[120:123]\n"
        "ds_read_b128 a[112:115], v248 offset:12320\n"
        "s_nop 0\n"
        "v_mfma_f32_16x16x32_bf16 v[108:111], v[212:215], v[196:199], v[44:47]\n"
        "ds_read_b128 a[116:119], v248 offset:15392\n"
        "s_nop 0\n"
        "v_mfma_f32_16x16x32_bf16 v[108:111], v[228:231], v[204:207], v[108:111]\n"
        "ds_read_b128 a[92:95], v248 offset:15360\n"
        "s_nop 0\n"
        "v_mfma_f32_16x16x32_bf16 v[116:119], a[0:3], a[236:239], 0\n"
        "ds_read_b128 a[88:91], v248 offset:12288\n"
        "v_cvt_pk_bf16_f32 v150, v120, v121\n"
        "v_mfma_f32_16x16x32_bf16 v[116:119], a[4:7], a[240:243], v[116:119]\n"
        "ds_read_b128 a[84:87], v248 offset:9216\n"
        "v_cvt_pk_bf16_f32 v151, v122, v123\n"
        "v_mfma_f32_16x16x32_bf16 v[116:119], a[8:11], a[244:247], v[116:119]\n"
        "ds_read_b128 a[80:83], v248 offset:6144\n"
        "v_mfma_f32_16x16x32_bf16 v[116:119], a[12:15], a[248:251], v[116:119]\n"
        "ds_read_b128 a[76:79], v248 offset:3072\n"
        "v_mfma_f32_16x16x32_bf16 v[116:119], a[16:19], a[216:219], v[116:119]\n"
        "ds_read_b128 a[72:75], v248 offset:0\n"
        "v_mfma_f32_16x16x32_bf16 v[116:119], a[20:23], a[220:223], v[116:119]\n"
        "s_waitcnt lgkmcnt(6)\n"
        "ds_read_b128 a[120:123], v254 offset:0\n"
        "v_mfma_f32_16x16x32_bf16 v[4:7], a[224:227], a[96:99], 0\n"
        "ds_read_b128 a[124:127], v254 offset:3072\n"
        "v_mfma_f32_16x16x32_bf16 v[4:7], a[228:231], a[100:103], v[4:7]\n"
        "ds_read_b128 a[128:131], v254 offset:6144\n"
        "v_mfma_f32_16x16x32_bf16 v[4:7], a[232:235], a[104:107], v[4:7]\n"
        "ds_read_b128 a[132:135], v254 offset:9216\n"
        "s_nop 0\n"
        "v_mfma_f32_16x16x32_bf16 v[4:7], v[156:159], a[108:111], v[4:7]\n"
        "ds_read_b128 a[136:139], v254 offset:12288\n"
        "v_cvt_pk_bf16_f32 v216, v116, v117\n"
        "v_mfma_f32_16x16x32_bf16 v[4:7], v[160:163], a[112:115], v[4:7]\n"
        "ds_read_b128 a[140:143], v254 offset:15360\n"
        "v_cvt_pk_bf16_f32 v217, v118, v119\n"
        "v_mfma_f32_16x16x32_bf16 v[4:7], v[164:167], a[116:119], v[4:7]\n"
        "s_waitcnt lgkmcnt(6)\n"
        "v_exp_f32 v102, v102\n"
        "v_mfma_f32_16x16x32_bf16 v[0:3], v[164:167], a[92:95], 0\n"
        "s_mul_i32 s45, s46, 786432\n"
        "v_mov_b64 v[96:97], 0\n"
        "v_mfma_f32_16x16x32_bf16 v[0:3], v[160:163], a[88:91], v[0:3]\n"
        "s_sub_u32 s37, s37, s45\n"
        "v_exp_f32 v103, v103\n"
        "v_mfma_f32_16x16x32_bf16 v[0:3], v[156:159], a[84:87], v[0:3]\n"
        "s_cmp_eq_u32 s46, 0\n"
        "s_cselect_b32 s43, s43, s44\n"
        "v_cvt_pk_bf16_f32 v71, v102, v103\n"
        "v_mfma_f32_16x16x32_bf16 v[0:3], a[232:235], a[80:83], v[0:3]\n"
        "s_add_u32 s28, s37, 786432\n"
        "v_exp_f32 v104, v104\n"
        "v_mfma_f32_16x16x32_bf16 v[0:3], a[228:231], a[76:79], v[0:3]\n"
        "s_cmp_eq_u32 s28, 100663296\n"
        "s_cselect_b32 s28, 0, s28\n"
        "v_mfma_f32_16x16x32_bf16 v[0:3], a[224:227], a[72:75], v[0:3]\n"
        "s_waitcnt lgkmcnt(0)\n"
        "v_exp_f32 v105, v105\n"
        "v_mfma_f32_16x16x32_bf16 v[8:11], a[224:227], a[120:123], 0\n"
        "s_lshl_b32 s29, s42, 2\n"
        "v_exp_f32 v106, v106\n"
        "v_mfma_f32_16x16x32_bf16 v[8:11], a[228:231], a[124:127], v[8:11]\n"
        "s_add_u32 s47, s37, s43\n"
        "v_exp_f32 v107, v107\n"
        "v_mfma_f32_16x16x32_bf16 v[8:11], a[232:235], a[128:131], v[8:11]\n"
        "s_add_u32 s35, s28, s43\n"
        "v_exp_f32 v108, v108\n"
        "v_mfma_f32_16x16x32_bf16 v[8:11], v[156:159], a[132:135], v[8:11]\n"
        "v_exp_f32 v109, v109\n"
        "v_mfma_f32_16x16x32_bf16 v[8:11], v[160:163], a[136:139], v[8:11]\n"
        "v_exp_f32 v110, v110\n"
        "v_mfma_f32_16x16x32_bf16 v[8:11], v[164:167], a[140:143], v[8:11]\n"
        "v_exp_f32 v111, v111\n"
        "v_smfmac_f32_16x16x64_bf16 v[96:99], a[252:255], v[64:71], v237\n"
        "ds_read_b128 a[164:167], v254 offset:15392\n"
        "v_mfma_f32_16x16x32_bf16 v[100:103], v[132:135], v[68:71], 0\n"
        "ds_read_b128 a[160:163], v254 offset:12320\n"
        "v_cvt_pk_bf16_f32 v78, v104, v105\n"
        "v_mfma_f32_16x16x32_bf16 v[100:103], v[128:131], v[64:67], v[100:103]\n"
        "ds_read_b128 a[156:159], v254 offset:9248\n"
        "v_cvt_pk_bf16_f32 v79, v106, v107\n"
        "v_mfma_f32_16x16x32_bf16 v[104:107], v[136:139], v[64:67], 0\n"
        "ds_read_b128 a[152:155], v254 offset:6176\n"
        "v_cvt_pk_bf16_f32 v86, v108, v109\n"
        "v_mfma_f32_16x16x32_bf16 v[104:107], v[140:143], v[68:71], v[104:107]\n"
        "ds_read_b128 a[148:151], v254 offset:3104\n"
        "v_cvt_pk_bf16_f32 v87, v110, v111\n"
        "v_mfma_f32_16x16x32_bf16 v[108:111], v[148:151], v[68:71], 0\n"
        "ds_read_b128 a[144:147], v254 offset:32\n"
        "v_add_f32 v97, v96, v97\n"
        "v_mfma_f32_16x16x32_bf16 v[108:111], v[144:147], v[64:67], v[108:111]\n"
        "v_rcp_f32 v96, v97\n"
        "v_mfma_f32_16x16x32_bf16 v[120:123], a[44:47], a[220:223], 0\n"
        "v_mul_f32 v100, v96, v100\n"
        "v_mul_f32 v101, v96, v101\n"
        "v_mfma_f32_16x16x32_bf16 v[120:123], a[40:43], a[216:219], v[120:123]\n"
        "ds_read_b128 v[176:179], v247 offset:256\n"
        "v_mul_f32 v102, v96, v102\n"
        "v_mfma_f32_16x16x32_bf16 v[120:123], a[36:39], a[248:251], v[120:123]\n"
        "ds_read_b128 v[180:183], v247 offset:4352\n"
        "v_mul_f32 v103, v96, v103\n"
        "v_mfma_f32_16x16x32_bf16 v[120:123], a[32:35], a[244:247], v[120:123]\n"
        "ds_read_b128 v[184:187], v247 offset:8448\n"
        "v_cvt_pk_bf16_f32 v100, v100, v101\n"
        "v_mfma_f32_16x16x32_bf16 v[120:123], a[28:31], a[240:243], v[120:123]\n"
        "ds_read_b128 v[196:199], v247 offset:12544\n"
        "v_cvt_pk_bf16_f32 v232, v0, v4\n"
        "v_mfma_f32_16x16x32_bf16 v[120:123], a[24:27], a[236:239], v[120:123]\n"
        "s_waitcnt lgkmcnt(4)\n"
        "ds_read_b128 v[200:203], v247 offset:16640\n"
        "v_cvt_pk_bf16_f32 v101, v102, v103\n"
        "v_mfma_f32_16x16x32_bf16 v[12:15], v[164:167], a[164:167], 0\n"
        "ds_read_b128 v[204:207], v247 offset:20736\n"
        "v_mov_b64 v[124:125], 0\n"
        "v_mfma_f32_16x16x32_bf16 v[12:15], v[160:163], a[160:163], v[12:15]\n"
        "ds_write_b32 v245, v100 offset:0\n"
        "v_mfma_f32_16x16x32_bf16 v[12:15], v[156:159], a[156:159], v[12:15]\n"
        "ds_write_b32 v245, v101 offset:1024\n"
        "v_mfma_f32_16x16x32_bf16 v[12:15], a[232:235], a[152:155], v[12:15]\n"
        "v_cvt_pk_bf16_f32 v218, v120, v121\n"
        "v_cvt_pk_bf16_f32 v219, v122, v123\n"
        "v_mfma_f32_16x16x32_bf16 v[12:15], a[228:231], a[148:151], v[12:15]\n"
        "v_cvt_pk_bf16_f32 v234, v1, v5\n"
        "v_mul_f32 v104, v96, v104\n"
        "v_mfma_f32_16x16x32_bf16 v[12:15], a[224:227], a[144:147], v[12:15]\n"
        "v_mul_f32 v105, v96, v105\n"
        "v_mul_f32 v106, v96, v106\n"
        "v_mfma_f32_16x16x32_bf16 v[116:119], a[48:51], a[236:239], 0\n"
        "v_mul_f32 v107, v96, v107\n"
        "v_cvt_pk_bf16_f32 v242, v2, v6\n"
        "v_mfma_f32_16x16x32_bf16 v[116:119], a[52:55], a[240:243], v[116:119]\n"
        "ds_read_b128 v[222:225], v251 offset:4608\n"
        "v_cvt_pk_bf16_f32 v102, v104, v105\n"
        "v_mfma_f32_16x16x32_bf16 v[116:119], a[56:59], a[244:247], v[116:119]\n"
        "v_cvt_pk_bf16_f32 v233, v8, v12\n"
        "v_cvt_pk_bf16_f32 v235, v9, v13\n"
        "v_mfma_f32_16x16x32_bf16 v[116:119], a[60:63], a[248:251], v[116:119]\n"
        "buffer_store_dwordx2 v[232:233], v250, s[56:59], 0 offen offset:0 sc1\n"
        "v_cvt_pk_bf16_f32 v243, v10, v14\n"
        "v_mfma_f32_16x16x32_bf16 v[116:119], a[64:67], a[216:219], v[116:119]\n"
        "v_cvt_pk_bf16_f32 v103, v106, v107\n"
        "v_mul_f32 v108, v96, v108\n"
        "v_mfma_f32_16x16x32_bf16 v[116:119], a[68:71], a[220:223], v[116:119]\n"
        "ds_write2st64_b32 v245, v102, v103 offset0:8 offset1:12\n"
        "v_smfmac_f32_16x16x64_bf16 v[124:127], a[252:255], v[72:79], v237\n"
        "ds_read_b128 a[212:215], v254 offset:15376\n"
        "v_mfma_f32_16x16x32_bf16 v[64:67], v[132:135], v[76:79], 0\n"
        "buffer_store_dwordx2 v[234:235], v250, s[56:59], 0 offen offset:384 sc1\n"
        "ds_read_b128 a[208:211], v254 offset:12304\n"
        "v_mfma_f32_16x16x32_bf16 v[64:67], v[128:131], v[72:75], v[64:67]\n"
        "ds_read_b128 a[204:207], v254 offset:9232\n"
        "v_mul_f32 v109, v96, v109\n"
        "v_mfma_f32_16x16x32_bf16 v[68:71], v[136:139], v[72:75], 0\n"
        "buffer_store_dwordx2 v[242:243], v250, s[56:59], 0 offen offset:768 sc1\n"
        "ds_read_b128 a[200:203], v254 offset:6160\n"
        "v_mfma_f32_16x16x32_bf16 v[68:71], v[140:143], v[76:79], v[68:71]\n"
        "ds_read_b128 a[196:199], v254 offset:3088\n"
        "v_mul_f32 v110, v96, v110\n"
        "v_mfma_f32_16x16x32_bf16 v[112:115], v[148:151], v[76:79], 0\n"
        "ds_read_b128 a[192:195], v254 offset:16\n"
        "v_mul_f32 v111, v96, v111\n"
        "v_mfma_f32_16x16x32_bf16 v[112:115], v[144:147], v[72:75], v[112:115]\n"
        "s_waitcnt lgkmcnt(7)\n"
        "ds_read_b128 a[168:171], v248 offset:16\n"
        "v_cvt_pk_bf16_f32 v232, v3, v7\n"
        "v_mfma_f32_16x16x32_bf16 v[24:27], v[176:179], a[72:75], 0\n"
        "ds_read_b128 a[172:175], v248 offset:3088\n"
        "v_cvt_pk_bf16_f32 v233, v11, v15\n"
        "v_mfma_f32_16x16x32_bf16 v[24:27], v[180:183], a[76:79], v[24:27]\n"
        "buffer_store_dwordx2 v[232:233], v250, s[56:59], 0 offen offset:1152 sc1\n"
        "ds_read_b128 a[176:179], v248 offset:6160\n"
        "v_mfma_f32_16x16x32_bf16 v[24:27], v[184:187], a[80:83], v[24:27]\n"
        "ds_read_b128 a[180:183], v248 offset:9232\n"
        "v_cvt_pk_bf16_f32 v108, v108, v109\n"
        "v_mfma_f32_16x16x32_bf16 v[24:27], v[196:199], a[84:87], v[24:27]\n"
        "ds_read_b128 a[184:187], v248 offset:12304\n"
        "v_cvt_pk_bf16_f32 v109, v110, v111\n"
        "v_mfma_f32_16x16x32_bf16 v[24:27], v[200:203], a[88:91], v[24:27]\n"
        "ds_write2st64_b32 v245, v108, v109 offset0:16 offset1:20\n"
        "v_mfma_f32_16x16x32_bf16 v[24:27], v[204:207], a[92:95], v[24:27]\n"
        "s_waitcnt lgkmcnt(6)\n"
        "ds_read_b128 a[188:191], v248 offset:15376\n"
        "v_add_f32 v125, v124, v125\n"
        "v_mfma_f32_16x16x32_bf16 v[20:23], v[164:167], a[212:215], 0\n"
        "v_rcp_f32 v124, v125\n"
        "v_mfma_f32_16x16x32_bf16 v[20:23], v[160:163], a[208:211], v[20:23]\n"
        "v_add_u32 v240, s31, v247\n"
        "v_max_u32 v236, v96, v97\n"
        "v_mfma_f32_16x16x32_bf16 v[20:23], v[156:159], a[204:207], v[20:23]\n"
        "v_cvt_pk_bf16_f32 v220, v116, v117\n"
        "v_cvt_pk_bf16_f32 v221, v118, v119\n"
        "v_mfma_f32_16x16x32_bf16 v[20:23], a[232:235], a[200:203], v[20:23]\n"
        "v_cvt_pk_bf16_f32 v226, v116, v117\n"
        "v_cvt_pk_bf16_f32 v227, v118, v119\n"
        "v_mfma_f32_16x16x32_bf16 v[20:23], a[228:231], a[196:199], v[20:23]\n"
        "v_mul_f32 v64, v124, v64\n"
        "v_mul_f32 v65, v124, v65\n"
        "v_mfma_f32_16x16x32_bf16 v[20:23], a[224:227], a[192:195], v[20:23]\n"
        "s_waitcnt lgkmcnt(2)\n"
        "v_mul_f32 v66, v124, v66\n"
        "v_mul_f32 v67, v124, v67\n"
        "v_mfma_f32_16x16x32_bf16 v[16:19], a[224:227], a[168:171], 0\n"
        "v_cvt_pk_bf16_f32 v64, v64, v65\n"
        "v_cvt_pk_bf16_f32 v65, v66, v67\n"
        "v_mfma_f32_16x16x32_bf16 v[16:19], a[228:231], a[172:175], v[16:19]\n"
        "ds_write2st64_b32 v245, v64, v65 offset0:1 offset1:5\n"
        "v_mfma_f32_16x16x32_bf16 v[16:19], a[232:235], a[176:179], v[16:19]\n"
        "v_mul_f32 v68, v124, v68\n"
        "v_mul_f32 v69, v124, v69\n"
        "v_mfma_f32_16x16x32_bf16 v[16:19], v[156:159], a[180:183], v[16:19]\n"
        "v_mul_f32 v70, v124, v70\n"
        "v_mul_f32 v71, v124, v71\n"
        "v_mfma_f32_16x16x32_bf16 v[16:19], v[160:163], a[184:187], v[16:19]\n"
        "s_waitcnt lgkmcnt(1)\n"
        "v_cvt_pk_bf16_f32 v66, v68, v69\n"
        "v_cvt_pk_bf16_f32 v67, v70, v71\n"
        "v_mfma_f32_16x16x32_bf16 v[16:19], v[164:167], a[188:191], v[16:19]\n"
        "ds_write2st64_b32 v245, v66, v67 offset0:9 offset1:13\n"
        "v_mfma_f32_16x16x32_bf16 v[0:3], v[152:155], v[216:219], v[48:51]\n"
        "v_mul_f32 v112, v124, v112\n"
        "v_mul_f32 v113, v124, v113\n"
        "v_mfma_f32_16x16x32_bf16 v[0:3], v[168:171], v[220:223], v[0:3]\n"
        "v_mul_f32 v114, v124, v114\n"
        "v_mul_f32 v115, v124, v115\n"
        "v_mfma_f32_16x16x32_bf16 v[4:7], v[172:175], v[216:219], v[52:55]\n"
        ".long 0xbec200ff\n"
        "47:\n"
        "s_branch 61b\n"
        "v_cvt_pk_bf16_f32 v112, v112, v113\n"
        "v_cvt_pk_bf16_f32 v113, v114, v115\n"
        "v_mfma_f32_16x16x32_bf16 v[4:7], v[188:191], v[224:227], v[4:7]\n"
        "ds_write_b32 v245, v112 offset:4352\n"
        "v_mfma_f32_16x16x32_bf16 v[36:39], v[204:207], a[164:167], 0\n"
        "ds_write_b32 v245, v113 offset:5376\n"
        "v_mfma_f32_16x16x32_bf16 v[36:39], v[200:203], a[160:163], v[36:39]\n"
        "ds_read_b128 v[222:225], v252 offset:4608\n"
        "v_max3_u32 v236, v236, v124, v125\n"
        "v_mfma_f32_16x16x32_bf16 v[36:39], v[196:199], a[156:159], v[36:39]\n"
        "v_cvt_pk_bf16_f32 v244, v16, v20\n"
        "v_cvt_pk_bf16_f32 v239, v17, v21\n"
        "v_mfma_f32_16x16x32_bf16 v[36:39], v[184:187], a[152:155], v[36:39]\n"
        "buffer_store_dword v244, v238, s[56:59], 0 offen offset:0 sc1\n"
        "v_mfma_f32_16x16x32_bf16 v[36:39], v[180:183], a[148:151], v[36:39]\n"
        "buffer_store_dword v239, v238, s[56:59], 0 offen offset:384 sc1\n"
        "v_mfma_f32_16x16x32_bf16 v[36:39], v[176:179], a[144:147], v[36:39]\n"
        "v_exp_f32 v0, v0\n"
        "v_mfma_f32_16x16x32_bf16 v[32:35], v[176:179], a[120:123], 0\n"
        "v_exp_f32 v1, v1\n"
        "v_mfma_f32_16x16x32_bf16 v[32:35], v[180:183], a[124:127], v[32:35]\n"
        "v_exp_f32 v2, v2\n"
        "v_mfma_f32_16x16x32_bf16 v[32:35], v[184:187], a[128:131], v[32:35]\n"
        "v_cvt_pk_bf16_f32 v88, v0, v1\n"
        "v_cvt_pk_bf16_f32 v255, v18, v22\n"
        "v_mfma_f32_16x16x32_bf16 v[32:35], v[196:199], a[132:135], v[32:35]\n"
        "buffer_store_dword v255, v238, s[56:59], 0 offen offset:768 sc1\n"
        "v_mfma_f32_16x16x32_bf16 v[32:35], v[200:203], a[136:139], v[32:35]\n"
        "v_exp_f32 v4, v4\n"
        "v_mfma_f32_16x16x32_bf16 v[32:35], v[204:207], a[140:143], v[32:35]\n"
        "v_exp_f32 v3, v3\n"
        "v_mfma_f32_16x16x32_bf16 v[28:31], v[204:207], a[116:119], 0\n"
        "v_exp_f32 v5, v5\n"
        "v_mfma_f32_16x16x32_bf16 v[28:31], v[200:203], a[112:115], v[28:31]\n"
        "v_cvt_pk_bf16_f32 v89, v2, v3\n"
        "v_cvt_pk_bf16_f32 v90, v4, v5\n"
        "v_mfma_f32_16x16x32_bf16 v[28:31], v[196:199], a[108:111], v[28:31]\n"
        "v_exp_f32 v6, v6\n"
        "v_mfma_f32_16x16x32_bf16 v[28:31], v[184:187], a[104:107], v[28:31]\n"
        "v_exp_f32 v7, v7\n"
        "v_mfma_f32_16x16x32_bf16 v[28:31], v[180:183], a[100:103], v[28:31]\n"
        "v_cvt_pk_bf16_f32 v91, v6, v7\n"
        "v_cvt_pk_bf16_f32 v244, v19, v23\n"
        "v_mfma_f32_16x16x32_bf16 v[28:31], v[176:179], a[96:99], v[28:31]\n"
        "buffer_store_dword v244, v238, s[56:59], 0 offen offset:1152 sc1\n"
        "v_mfma_f32_16x16x32_bf16 v[8:11], v[192:195], v[216:219], v[56:59]\n"
        "s_waitcnt lgkmcnt(0)\n"
        "v_mov_b64 v[96:97], 0\n"
        "v_cvt_pk_bf16_f32 v235, v32, v36\n"
        "v_mfma_f32_16x16x32_bf16 v[8:11], v[208:211], v[220:223], v[8:11]\n"
        "v_cvt_pk_bf16_f32 v243, v33, v37\n"
        "v_cvt_pk_bf16_f32 v233, v34, v38\n"
        "v_mfma_f32_16x16x32_bf16 v[40:43], v[176:179], a[168:171], 0\n"
        "v_cvt_pk_bf16_f32 v234, v24, v28\n"
        "v_cvt_pk_bf16_f32 v242, v25, v29\n"
        "v_mfma_f32_16x16x32_bf16 v[40:43], v[180:183], a[172:175], v[40:43]\n"
        "buffer_store_dwordx2 v[234:235], v250, s[60:63], 0 offen offset:0 sc1\n"
        "v_cvt_pk_bf16_f32 v232, v26, v30\n"
        "v_mfma_f32_16x16x32_bf16 v[40:43], v[184:187], a[176:179], v[40:43]\n"
        "v_exp_f32 v8, v8\n"
        "v_mfma_f32_16x16x32_bf16 v[40:43], v[196:199], a[180:183], v[40:43]\n"
        "s_barrier\n"
        "ds_read_b128 a[0:3], v240 offset:0\n"
        "ds_read_b128 a[4:7], v240 offset:4096\n"
        "v_mfma_f32_16x16x32_bf16 v[40:43], v[200:203], a[184:187], v[40:43]\n"
        "buffer_store_dwordx2 v[242:243], v250, s[60:63], 0 offen offset:384 sc1\n"
        "ds_read_b128 a[8:11], v240 offset:8192\n"
        "v_mfma_f32_16x16x32_bf16 v[40:43], v[204:207], a[188:191], v[40:43]\n"
        "ds_read_b128 a[12:15], v240 offset:12288\n"
        "ds_read_b128 a[16:19], v240 offset:16384\n"
        "v_smfmac_f32_16x16x64_bf16 v[96:99], a[252:255], v[80:87], v237\n"
        "ds_read_b128 a[20:23], v240 offset:20480\n"
        "v_mfma_f32_16x16x32_bf16 v[72:75], v[132:135], v[84:87], 0\n"
        "buffer_store_dwordx2 v[232:233], v250, s[60:63], 0 offen offset:768 sc1\n"
        "v_cvt_pk_bf16_f32 v234, v27, v31\n"
        "v_mfma_f32_16x16x32_bf16 v[72:75], v[128:131], v[80:83], v[72:75]\n"
        "v_exp_f32 v9, v9\n"
        "v_mfma_f32_16x16x32_bf16 v[76:79], v[136:139], v[80:83], 0\n"
        "v_exp_f32 v10, v10\n"
        "v_mfma_f32_16x16x32_bf16 v[76:79], v[140:143], v[84:87], v[76:79]\n"
        "v_cvt_pk_bf16_f32 v92, v8, v9\n"
        "v_cvt_pk_bf16_f32 v235, v35, v39\n"
        "v_mfma_f32_16x16x32_bf16 v[100:103], v[148:151], v[84:87], 0\n"
        "buffer_store_dwordx2 v[234:235], v250, s[60:63], 0 offen offset:1152 sc1\n"
        "v_add_f32 v97, v96, v97\n"
        "v_mfma_f32_16x16x32_bf16 v[100:103], v[144:147], v[80:83], v[100:103]\n"
        "v_exp_f32 v11, v11\n"
        "v_mfma_f32_16x16x32_bf16 v[12:15], v[212:215], v[216:219], v[60:63]\n"
        "v_cvt_pk_bf16_f32 v93, v10, v11\n"
        "v_mov_b32 v249, s29\n"
        "v_mfma_f32_16x16x32_bf16 v[12:15], v[228:231], v[224:227], v[12:15]\n"
        "s_waitcnt lgkmcnt(0)\n"
        "v_rcp_f32 v96, v97\n"
        "v_mfma_f32_16x16x32_bf16 v[156:159], a[20:23], a[92:95], 0\n"
        "v_mul_f32 v72, v96, v72\n"
        "v_mul_f32 v73, v96, v73\n"
        "v_mfma_f32_16x16x32_bf16 v[156:159], a[16:19], a[88:91], v[156:159]\n"
        "v_mul_f32 v74, v96, v74\n"
        "v_mul_f32 v75, v96, v75\n"
        "v_mfma_f32_16x16x32_bf16 v[156:159], a[12:15], a[84:87], v[156:159]\n"
        "v_exp_f32 v12, v12\n"
        "v_mfma_f32_16x16x32_bf16 v[156:159], a[8:11], a[80:83], v[156:159]\n"
        "v_exp_f32 v13, v13\n"
        "v_mfma_f32_16x16x32_bf16 v[156:159], a[4:7], a[76:79], v[156:159]\n"
        "v_exp_f32 v14, v14\n"
        "v_mfma_f32_16x16x32_bf16 v[156:159], a[0:3], a[72:75], v[156:159]\n"
        "v_cvt_pk_bf16_f32 v94, v12, v13\n"
        "v_cvt_pk_bf16_f32 v72, v72, v73\n"
        "v_mfma_f32_16x16x32_bf16 v[44:47], v[176:179], a[192:195], 0\n"
        "v_exp_f32 v15, v15\n"
        "v_mfma_f32_16x16x32_bf16 v[44:47], v[180:183], a[196:199], v[44:47]\n"
        "v_cvt_pk_bf16_f32 v95, v14, v15\n"
        "v_mov_b64 v[124:125], 0\n"
        "v_mfma_f32_16x16x32_bf16 v[44:47], v[184:187], a[200:203], v[44:47]\n"
        "v_cvt_pk_bf16_f32 v73, v74, v75\n"
        "v_mul_f32 v76, v96, v76\n"
        "v_mfma_f32_16x16x32_bf16 v[44:47], v[196:199], a[204:207], v[44:47]\n"
        "ds_write2st64_b32 v245, v72, v73 offset0:2 offset1:6\n"
        "v_mfma_f32_16x16x32_bf16 v[44:47], v[200:203], a[208:211], v[44:47]\n"
        "v_mul_f32 v77, v96, v77\n"
        "v_mul_f32 v78, v96, v78\n"
        "v_mfma_f32_16x16x32_bf16 v[44:47], v[204:207], a[212:215], v[44:47]\n"
        "v_mul_f32 v79, v96, v79\n"
        "v_cvt_pk_bf16_f32 v74, v76, v77\n"
        "v_mfma_f32_16x16x32_bf16 v[160:163], a[20:23], a[116:119], 0\n"
        "v_cvt_pk_bf16_f32 v75, v78, v79\n"
        "v_mul_f32 v100, v96, v100\n"
        "v_mfma_f32_16x16x32_bf16 v[160:163], a[16:19], a[112:115], v[160:163]\n"
        "v_mul_f32 v101, v96, v101\n"
        "v_mul_f32 v102, v96, v102\n"
        "v_mfma_f32_16x16x32_bf16 v[160:163], a[12:15], a[108:111], v[160:163]\n"
        "ds_write2st64_b32 v245, v74, v75 offset0:10 offset1:14\n"
        "v_mfma_f32_16x16x32_bf16 v[160:163], a[8:11], a[104:107], v[160:163]\n"
        "v_cvt_pk_bf16_f32 v239, v40, v44\n"
        "v_cvt_pk_bf16_f32 v255, v41, v45\n"
        "v_mfma_f32_16x16x32_bf16 v[160:163], a[4:7], a[100:103], v[160:163]\n"
        "buffer_store_dword v239, v238, s[60:63], 0 offen offset:0 sc1\n"
        "v_mfma_f32_16x16x32_bf16 v[160:163], a[0:3], a[96:99], v[160:163]\n"
        "v_mul_f32 v103, v96, v103\n"
        "v_cvt_pk_bf16_f32 v244, v42, v46\n"
        "v_smfmac_f32_16x16x64_bf16 v[124:127], a[252:255], v[88:95], v237\n"
        "v_cvt_pk_bf16_f32 v100, v100, v101\n"
        "v_mfma_f32_16x16x32_bf16 v[104:107], v[132:135], v[92:95], 0\n"
        "buffer_store_dword v255, v238, s[60:63], 0 offen offset:384 sc1\n"
        "v_mfma_f32_16x16x32_bf16 v[104:107], v[128:131], v[88:91], v[104:107]\n"
        "v_cvt_pk_bf16_f32 v101, v102, v103\n"
        "v_cvt_pk_bf16_f32 v239, v43, v47\n"
        "v_mfma_f32_16x16x32_bf16 v[108:111], v[136:139], v[88:91], 0\n"
        "buffer_store_dword v244, v238, s[60:63], 0 offen offset:768 sc1\n"
        "v_mfma_f32_16x16x32_bf16 v[108:111], v[140:143], v[92:95], v[108:111]\n"
        "ds_write2st64_b32 v245, v100, v101 offset0:18 offset1:22\n"
        "v_mfma_f32_16x16x32_bf16 v[112:115], v[148:151], v[92:95], 0\n"
        "v_max3_u32 v236, v236, v96, v97\n"
        "v_add_f32 v125, v124, v125\n"
        "v_mfma_f32_16x16x32_bf16 v[112:115], v[144:147], v[88:91], v[112:115]\n"
        "buffer_store_dword v239, v238, s[60:63], 0 offen offset:1152 sc1\n"
        "v_mfma_f32_16x16x32_bf16 v[176:179], a[0:3], a[144:147], 0\n"
        "v_rcp_f32 v124, v125\n"
        "v_mfma_f32_16x16x32_bf16 v[176:179], a[4:7], a[148:151], v[176:179]\n"
        "v_mul_f32 v104, v124, v104\n"
        "v_mul_f32 v105, v124, v105\n"
        "v_mfma_f32_16x16x32_bf16 v[176:179], a[8:11], a[152:155], v[176:179]\n"
        "v_mul_f32 v106, v124, v106\n"
        "v_mul_f32 v107, v124, v107\n"
        "v_mfma_f32_16x16x32_bf16 v[176:179], a[12:15], a[156:159], v[176:179]\n"
        "v_cvt_pk_bf16_f32 v104, v104, v105\n"
        "v_cvt_pk_bf16_f32 v105, v106, v107\n"
        "v_mfma_f32_16x16x32_bf16 v[176:179], a[16:19], a[160:163], v[176:179]\n"
        "ds_write2st64_b32 v245, v104, v105 offset0:3 offset1:7\n"
        "v_mfma_f32_16x16x32_bf16 v[176:179], a[20:23], a[164:167], v[176:179]\n"
        "v_mul_f32 v108, v124, v108\n"
        "v_mul_f32 v109, v124, v109\n"
        "v_mfma_f32_16x16x32_bf16 v[164:167], a[20:23], a[140:143], 0\n"
        "v_mul_f32 v110, v124, v110\n"
        "v_mul_f32 v111, v124, v111\n"
        "v_mfma_f32_16x16x32_bf16 v[164:167], a[16:19], a[136:139], v[164:167]\n"
        "v_cvt_pk_bf16_f32 v106, v108, v109\n"
        "v_cvt_pk_bf16_f32 v107, v110, v111\n"
        "v_mfma_f32_16x16x32_bf16 v[164:167], a[12:15], a[132:135], v[164:167]\n"
        "v_mul_f32 v112, v124, v112\n"
        "v_mul_f32 v113, v124, v113\n"
        "v_mfma_f32_16x16x32_bf16 v[164:167], a[8:11], a[128:131], v[164:167]\n"
        "ds_write2st64_b32 v245, v106, v107 offset0:11 offset1:15\n"
        "v_mfma_f32_16x16x32_bf16 v[164:167], a[4:7], a[124:127], v[164:167]\n"
        "v_mul_f32 v114, v124, v114\n"
        "v_mul_f32 v115, v124, v115\n"
        "v_mfma_f32_16x16x32_bf16 v[164:167], a[0:3], a[120:123], v[164:167]\n"
        "v_cvt_pk_bf16_f32 v112, v112, v113\n"
        "v_cvt_pk_bf16_f32 v113, v114, v115\n"
        "v_mfma_f32_16x16x32_bf16 v[180:183], a[0:3], a[168:171], 0\n"
        "v_max3_u32 v236, v236, v124, v125\n"
        "v_cvt_pk_bf16_f32 v242, v156, v160\n"
        "v_mfma_f32_16x16x32_bf16 v[180:183], a[4:7], a[172:175], v[180:183]\n"
        "ds_write2st64_b32 v245, v112, v113 offset0:19 offset1:23\n"
        "v_mfma_f32_16x16x32_bf16 v[180:183], a[8:11], a[176:179], v[180:183]\n"
        "v_cvt_pk_bf16_f32 v243, v164, v176\n"
        "v_cmp_lt_u32 vcc, s24, v236\n"
        "v_mfma_f32_16x16x32_bf16 v[180:183], a[12:15], a[180:183], v[180:183]\n"
        "buffer_store_dwordx2 v[242:243], v250, s[16:19], s47 offen offset:0 sc1\n"
        "v_cvt_pk_bf16_f32 v232, v157, v161\n"
        "v_mfma_f32_16x16x32_bf16 v[180:183], a[16:19], a[184:187], v[180:183]\n"
        "s_cmp_lg_u64 vcc, 0\n"
        "s_addc_u32 s25, s25, s25\n"
        "v_mov_b32 v253, s25\n"
        "v_mfma_f32_16x16x32_bf16 v[180:183], a[20:23], a[188:191], v[180:183]\n"
        "ds_write_b32 v249, v253\n"
        "v_mfma_f32_16x16x32_bf16 v[184:187], a[20:23], a[212:215], 0\n"
        "v_cvt_pk_bf16_f32 v233, v165, v177\n"
        "v_cvt_pk_bf16_f32 v234, v158, v162\n"
        "v_mfma_f32_16x16x32_bf16 v[184:187], a[16:19], a[208:211], v[184:187]\n"
        "buffer_store_dwordx2 v[232:233], v250, s[16:19], s47 offen offset:384 sc1\n"
        "v_cvt_pk_bf16_f32 v235, v166, v178\n"
        "v_mfma_f32_16x16x32_bf16 v[184:187], a[12:15], a[204:207], v[184:187]\n"
        "s_waitcnt lgkmcnt(0)\n"
        "s_barrier\n"
        "ds_read_b128 a[24:27], v240 offset:512\n"
        "ds_read_b128 a[28:31], v240 offset:4608\n"
        "v_mfma_f32_16x16x32_bf16 v[184:187], a[8:11], a[200:203], v[184:187]\n"
        "buffer_store_dwordx2 v[234:235], v250, s[16:19], s47 offen offset:768 sc1\n"
        "ds_read_b128 a[32:35], v240 offset:8704\n"
        "v_mfma_f32_16x16x32_bf16 v[184:187], a[4:7], a[196:199], v[184:187]\n"
        "ds_read_b128 a[36:39], v240 offset:12800\n"
        "ds_read_b128 a[40:43], v240 offset:16896\n"
        "v_mfma_f32_16x16x32_bf16 v[184:187], a[0:3], a[192:195], v[184:187]\n"
        "s_waitcnt lgkmcnt(4)\n"
        "ds_read_b128 a[44:47], v240 offset:20992\n"
        "v_cvt_pk_bf16_f32 v242, v159, v163\n"
        "v_mfma_f32_16x16x32_bf16 v[52:55], a[24:27], a[168:171], 0\n"
        "s_waitcnt lgkmcnt(4)\n"
        "v_cvt_pk_bf16_f32 v243, v167, v179\n"
        "s_nop 0\n"
        "v_mfma_f32_16x16x32_bf16 v[52:55], a[28:31], a[172:175], v[52:55]\n"
        "s_waitcnt lgkmcnt(3)\n"
        "buffer_store_dwordx2 v[242:243], v250, s[16:19], s47 offen offset:1152 sc1\n"
        "s_nop 0\n"
        "v_mfma_f32_16x16x32_bf16 v[52:55], a[32:35], a[176:179], v[52:55]\n"
        "s_waitcnt lgkmcnt(2)\n"
        "v_cvt_pk_bf16_f32 v255, v180, v184\n"
        "v_cvt_pk_bf16_f32 v244, v181, v185\n"
        "v_mfma_f32_16x16x32_bf16 v[52:55], a[36:39], a[180:183], v[52:55]\n"
        "s_waitcnt lgkmcnt(1)\n"
        "buffer_store_dword v255, v238, s[16:19], s47 offen offset:0 sc1\n"
        "v_mfma_f32_16x16x32_bf16 v[52:55], a[40:43], a[184:187], v[52:55]\n"
        "s_waitcnt lgkmcnt(0)\n"
        "ds_read_b128 v[60:63], v241\n"
        "v_cvt_pk_bf16_f32 v239, v182, v186\n"
        "v_mfma_f32_16x16x32_bf16 v[52:55], a[44:47], a[188:191], v[52:55]\n"
        "v_cvt_pk_bf16_f32 v255, v183, v187\n"
        "v_mfma_f32_16x16x32_bf16 v[56:59], a[44:47], a[212:215], 0\n"
        "buffer_store_dword v244, v238, s[16:19], s47 offen offset:384 sc1\n"
        "v_mfma_f32_16x16x32_bf16 v[56:59], a[40:43], a[208:211], v[56:59]\n"
        "v_mfma_f32_16x16x32_bf16 v[56:59], a[36:39], a[204:207], v[56:59]\n"
        "v_mfma_f32_16x16x32_bf16 v[56:59], a[32:35], a[200:203], v[56:59]\n"
        "buffer_store_dword v239, v238, s[16:19], s47 offen offset:768 sc1\n"
        "v_mfma_f32_16x16x32_bf16 v[56:59], a[28:31], a[196:199], v[56:59]\n"
        "v_mfma_f32_16x16x32_bf16 v[56:59], a[24:27], a[192:195], v[56:59]\n"
        "s_nop 2\n"
        "v_mfma_f32_16x16x32_bf16 v[196:199], a[24:27], a[72:75], 0\n"
        "buffer_store_dword v255, v238, s[16:19], s47 offen offset:1152 sc1\n"
        "v_mfma_f32_16x16x32_bf16 v[196:199], a[28:31], a[76:79], v[196:199]\n"
        "s_nop 1\n"
        "v_cvt_pk_bf16_f32 v88, v52, v56\n"
        "v_mfma_f32_16x16x32_bf16 v[196:199], a[32:35], a[80:83], v[196:199]\n"
        "v_cvt_pk_bf16_f32 v90, v53, v57\n"
        "v_cvt_pk_bf16_f32 v92, v54, v58\n"
        "v_mfma_f32_16x16x32_bf16 v[196:199], a[36:39], a[84:87], v[196:199]\n"
        "buffer_store_dword v88, v238, s[16:19], s35 offen offset:0 sc1\n"
        "v_mfma_f32_16x16x32_bf16 v[196:199], a[40:43], a[88:91], v[196:199]\n"
        "v_cvt_pk_bf16_f32 v94, v55, v59\n"
        "v_mfma_f32_16x16x32_bf16 v[196:199], a[44:47], a[92:95], v[196:199]\n"
        "v_mfma_f32_16x16x32_bf16 v[48:51], a[44:47], a[164:167], 0\n"
        "buffer_store_dword v90, v238, s[16:19], s35 offen offset:384 sc1\n"
        "v_mfma_f32_16x16x32_bf16 v[48:51], a[40:43], a[160:163], v[48:51]\n"
        "s_waitcnt lgkmcnt(0)\n"
        "v_or3_b32 v60, v60, v61, v62\n"
        "v_or_b32 v60, v60, v63\n"
        "v_mfma_f32_16x16x32_bf16 v[48:51], a[36:39], a[156:159], v[48:51]\n"
        "s_nop 0\n"
        "v_readfirstlane_b32 s25, v60\n"
        "v_mfma_f32_16x16x32_bf16 v[48:51], a[32:35], a[152:155], v[48:51]\n"
        "buffer_store_dword v92, v238, s[16:19], s35 offen offset:768 sc1\n"
        "v_mfma_f32_16x16x32_bf16 v[48:51], a[28:31], a[148:151], v[48:51]\n"
        "v_mfma_f32_16x16x32_bf16 v[48:51], a[24:27], a[144:147], v[48:51]\n"
        "v_mfma_f32_16x16x32_bf16 v[204:207], a[24:27], a[120:123], 0\n"
        "buffer_store_dword v94, v238, s[16:19], s35 offen offset:1152 sc1\n"
        "v_mfma_f32_16x16x32_bf16 v[204:207], a[28:31], a[124:127], v[204:207]\n"
        "v_mfma_f32_16x16x32_bf16 v[204:207], a[32:35], a[128:131], v[204:207]\n"
        "v_mfma_f32_16x16x32_bf16 v[204:207], a[36:39], a[132:135], v[204:207]\n"
        "v_mfma_f32_16x16x32_bf16 v[204:207], a[40:43], a[136:139], v[204:207]\n"
        "v_mfma_f32_16x16x32_bf16 v[204:207], a[44:47], a[140:143], v[204:207]\n"
        "v_mfma_f32_16x16x32_bf16 v[200:203], a[44:47], a[116:119], 0\n"
        "v_mfma_f32_16x16x32_bf16 v[200:203], a[40:43], a[112:115], v[200:203]\n"
        "s_nop 2\n"
        "v_mfma_f32_16x16x32_bf16 v[200:203], a[36:39], a[108:111], v[200:203]\n"
        "s_nop 1\n"
        "v_cvt_pk_bf16_f32 v81, v204, v48\n"
        "v_mfma_f32_16x16x32_bf16 v[200:203], a[32:35], a[104:107], v[200:203]\n"
        "v_cvt_pk_bf16_f32 v83, v205, v49\n"
        "v_mfma_f32_16x16x32_bf16 v[200:203], a[28:31], a[100:103], v[200:203]\n"
        "v_cvt_pk_bf16_f32 v85, v206, v50\n"
        "v_mfma_f32_16x16x32_bf16 v[200:203], a[24:27], a[96:99], v[200:203]\n"
        "s_nop 2\n"
        "v_cvt_pk_bf16_f32 v87, v207, v51\n"
        "s_nop 3\n"
        "v_cvt_pk_bf16_f32 v80, v196, v200\n"
        "buffer_store_dwordx2 v[80:81], v250, s[16:19], s35 offen offset:0 sc1\n"
        "v_cvt_pk_bf16_f32 v82, v197, v201\n"
        "buffer_store_dwordx2 v[82:83], v250, s[16:19], s35 offen offset:384 sc1\n"
        "v_cvt_pk_bf16_f32 v84, v198, v202\n"
        "buffer_store_dwordx2 v[84:85], v250, s[16:19], s35 offen offset:768 sc1\n"
        "v_cvt_pk_bf16_f32 v86, v199, v203\n"
        "buffer_store_dwordx2 v[86:87], v250, s[16:19], s35 offen offset:1152 sc1\n"
        "s_cmp_lg_u32 s25, 0\n"
        "s_cbranch_scc1 30f\n"
        "s_endpgm\n"
        "30:\n"
        : "={s25}"(flg), "={s42}"(w), "+{s[0:1]}"(ka), "+{s2}"(bid), "+{v0}"(tid)
        : "s"(ldsb)
        : "v1", "v2", "v3", "v4", "v5", "v6", "v7", "v8", "v9", "v10", "v11", "v12", "v13", "v14", "v15", "v16", "v17", "v18", "v19", "v20", "v21", "v22", "v23", "v24", "v25", "v26", "v27", "v28", "v29", "v30", "v31", "v32", "v33", "v34", "v35", "v36", "v37", "v38", "v39", "v40", "v41", "v42", "v43", "v44", "v45", "v46", "v47", "v48", "v49", "v50", "v51", "v52", "v53", "v54", "v55", "v56", "v57", "v58", "v59", "v60", "v61", "v62", "v63", "v64", "v65", "v66", "v67", "v68", "v69", "v70", "v71", "v72", "v73", "v74", "v75", "v76", "v77", "v78", "v79", "v80", "v81", "v82", "v83", "v84", "v85", "v86", "v87", "v88", "v89", "v90", "v91", "v92", "v93", "v94", "v95", "v96", "v97", "v98", "v99", "v100", "v101", "v102", "v103", "v104", "v105", "v106", "v107", "v108", "v109", "v110", "v111", "v112", "v113", "v114", "v115", "v116", "v117", "v118", "v119", "v120", "v121", "v122", "v123", "v124", "v125", "v126", "v127", "v128", "v129", "v130", "v131", "v132", "v133", "v134", "v135", "v136", "v137", "v138", "v139", "v140", "v141", "v142", "v143", "v144", "v145", "v146", "v147", "v148", "v149", "v150", "v151", "v152", "v153", "v154", "v155", "v156", "v157", "v158", "v159", "v160", "v161", "v162", "v163", "v164", "v165", "v166", "v167", "v168", "v169", "v170", "v171", "v172", "v173", "v174", "v175", "v176", "v177", "v178", "v179", "v180", "v181", "v182", "v183", "v184", "v185", "v186", "v187", "v188", "v189", "v190", "v191", "v192", "v193", "v194", "v195", "v196", "v197", "v198", "v199", "v200", "v201", "v202", "v203", "v204", "v205", "v206", "v207", "v208", "v209", "v210", "v211", "v212", "v213", "v214", "v215", "v216", "v217", "v218", "v219", "v220", "v221", "v222", "v223", "v224", "v225", "v226", "v227", "v228", "v229", "v230", "v231", "v232", "v233", "v234", "v235", "v236", "v237", "v238", "v239", "v240", "v241", "v242", "v243", "v244", "v245", "v246", "v247", "v248", "v249", "v250", "v251", "v252", "v253", "v254", "v255", "a0", "a1", "a2", "a3", "a4", "a5", "a6", "a7", "a8", "a9", "a10", "a11", "a12", "a13", "a14", "a15", "a16", "a17", "a18", "a19", "a20", "a21", "a22", "a23", "a24", "a25", "a26", "a27", "a28", "a29", "a30", "a31", "a32", "a33", "a34", "a35", "a36", "a37", "a38", "a39", "a40", "a41", "a42", "a43", "a44", "a45", "a46", "a47", "a48", "a49", "a50", "a51", "a52", "a53", "a54", "a55", "a56", "a57", "a58", "a59", "a60", "a61", "a62", "a63", "a64", "a65", "a66", "a67", "a68", "a69", "a70", "a71", "a72", "a73", "a74", "a75", "a76", "a77", "a78", "a79", "a80", "a81", "a82", "a83", "a84", "a85", "a86", "a87", "a88", "a89", "a90", "a91", "a92", "a93", "a94", "a95", "a96", "a97", "a98", "a99", "a100", "a101", "a102", "a103", "a104", "a105", "a106", "a107", "a108", "a109", "a110", "a111", "a112", "a113", "a114", "a115", "a116", "a117", "a118", "a119", "a120", "a121", "a122", "a123", "a124", "a125", "a126", "a127", "a128", "a129", "a130", "a131", "a132", "a133", "a134", "a135", "a136", "a137", "a138", "a139", "a140", "a141", "a142", "a143", "a144", "a145", "a146", "a147", "a148", "a149", "a150", "a151", "a152", "a153", "a154", "a155", "a156", "a157", "a158", "a159", "a160", "a161", "a162", "a163", "a164", "a165", "a166", "a167", "a168", "a169", "a170", "a171", "a172", "a173", "a174", "a175", "a176", "a177", "a178", "a179", "a180", "a181", "a182", "a183", "a184", "a185", "a186", "a187", "a188", "a189", "a190", "a191", "a192", "a193", "a194", "a195", "a196", "a197", "a198", "a199", "a200", "a201", "a202", "a203", "a204", "a205", "a206", "a207", "a208", "a209", "a210", "a211", "a212", "a213", "a214", "a215", "a216", "a217", "a218", "a219", "a220", "a221", "a222", "a223", "a224", "a225", "a226", "a227", "a228", "a229", "a230", "a231", "a232", "a233", "a234", "a235", "a236", "a237", "a238", "a239", "a240", "a241", "a242", "a243", "a244", "a245", "a246", "a247", "a248", "a249", "a250", "a251", "a252", "a253", "a254", "a255", "s4", "s5", "s6", "s7", "s8", "s9", "s10", "s11", "s12", "s13", "s14", "s15", "s16", "s17", "s18", "s19", "s20", "s21", "s22", "s23", "s24", "s26", "s27", "s28", "s29", "s30", "s31", "s34", "s35", "s36", "s37", "s38", "s39", "s40", "s41", "s43", "s44", "s45", "s46", "s47", "s48", "s49", "s50", "s51", "s52", "s53", "s54", "s55", "s56", "s57", "s58", "s59", "s60", "s61", "s62", "s63", "s64", "s65", "s66", "s67", "s68", "s69", "s70", "s71", "s72", "s73", "s74", "s75", "s76", "s77", "s78", "s79", "s80", "s81", "s82", "s83", "vcc", "scc", "memory");
    if (flg) {
        unsigned ln;
        asm volatile("s_waitcnt vmcnt(0)\n v_mbcnt_lo_u32_b32 %0, -1, 0\n v_mbcnt_hi_u32_b32 %0, -1, %0" : "=v"(ln) :: "memory");
        const unsigned t = w * 64u + ln, wx = bid & 63u, wy0 = bid >> 6;
        typedef const char* __attribute__((address_space(4))) const* kargp;
        const char *x = ((kargp)ka)[0], *wqkv = ((kargp)ka)[1], *wo = ((kargp)ka)[2], *rpb = ((kargp)ka)[3], *rpi = ((kargp)ka)[4],
                   *mask = ((kargp)ka)[5];
        char* out = (char*)((kargp)ka)[6];
        unsigned short* xs = (unsigned short*)smem;
        unsigned short* qs = xs + 64 * 192;
        const unsigned short* W1 = (const unsigned short*)wqkv;
        const unsigned short* W2 = (const unsigned short*)wo;
        for (unsigned i = 0; i < 16u; ++i) {
            if (!((flg >> (15u - i)) & 1u)) continue;
            const unsigned wy = wy0 + 4u * i;
            __syncthreads();
            for (unsigned e = t; e < 64u * 96u; e += 256u) {
                const unsigned tok = e / 96u, c2 = e % 96u;
                const unsigned pix = ((8u * wy + 4u + (tok >> 3)) & 511u) * 512u + ((8u * wx + 4u + (tok & 7u)) & 511u);
                ((unsigned*)xs)[e] = ((const unsigned*)x)[pix * 96u + c2];
            }
            __syncthreads();
            for (unsigned col = t; col < 576u; col += 256u)
                for (unsigned tb = 0; tb < 64u; tb += 8u) {
                    float a[8] = {0.f, 0.f, 0.f, 0.f, 0.f, 0.f, 0.f, 0.f};
                    for (unsigned c = 0; c < 192u; ++c) {
                        const float wt = bf2f(W1[c * 576u + col]);
#pragma unroll
                        for (int j = 0; j < 8; ++j) a[j] += bf2f(xs[(tb + j) * 192u + c]) * wt;
                    }
#pragma unroll
                    for (int j = 0; j < 8; ++j) qs[(tb + j) * 578u + col] = f2bf(a[j]);
                }
            __syncthreads();
            {
                float qv[48], o[48];
#pragma unroll
                for (int d = 0; d < 48; ++d) { qv[d] = bf2f(qs[ln * 578u + w * 48u + d]); o[d] = 0.f; }
                const unsigned short* mrow = (const unsigned short*)mask + (size_t)(wy * 64u + wx) * 4096u + ln * 64u;
                float mx = -__builtin_inff(), l = 0.f;
                for (int pass = 0; pass < 2; ++pass)
                    for (unsigned k = 0; k < 64u; ++k) {
                        float s = 0.f;
#pragma unroll
                        for (int d = 0; d < 48; ++d) s += qv[d] * bf2f(qs[k * 578u + 192u + w * 48u + d]);
                        s = s * 0.14433756729740643f + bf2f(((const unsigned short*)rpb)[((const unsigned*)rpi)[ln * 64u + k] * 4u + w]) + bf2f(mrow[k]);
                        if (pass == 0) mx = fmaxf(mx, s);
                        else {
                            const float p = __expf(s - mx);
                            l += p;
#pragma unroll
                            for (int d = 0; d < 48; ++d) o[d] += p * bf2f(qs[k * 578u + 384u + w * 48u + d]);
                        }
                    }
#pragma unroll
                for (int d = 0; d < 48; ++d) xs[ln * 192u + w * 48u + d] = f2bf(o[d] / l);
            }
            __syncthreads();
            if (t < 192u)
                for (unsigned tb = 0; tb < 64u; tb += 8u) {
                    float a[8] = {0.f, 0.f, 0.f, 0.f, 0.f, 0.f, 0.f, 0.f};
                    for (unsigned c = 0; c < 192u; ++c) {
                        const float wt = bf2f(W2[c * 192u + t]);
#pragma unroll
                        for (int j = 0; j < 8; ++j) a[j] += bf2f(xs[(tb + j) * 192u + c]) * wt;
                    }
                    const unsigned row = (8u * wy + 4u + (tb >> 3)) & 511u;
#pragma unroll
                    for (int j = 0; j < 8; ++j)
                        ((unsigned short*)out)[(row * 512u + ((8u * wx + 4u + j) & 511u)) * 192u + t] = f2bf(a[j]);
                }
        }
    }
}


extern "C" void kernel_launch(void* const* d_in, const int* in_sizes, int n_in, void* d_out, int out_size, void* d_ws,
                              size_t ws_size, hipStream_t stream) {
    win_attn_kernel<<<256, 256, 0, stream>>>((const char*)d_in[0], (const char*)d_in[1], (const char*)d_in[2],
                                             (const char*)d_in[3], (const char*)d_in[4], (const char*)d_in[5],
                                             (char*)d_out, (char*)d_ws);

}
```

```cpp
#include <hip/hip_runtime.h>
#include <hip/hip_bf16.h>

typedef unsigned int u32x4 __attribute__((ext_vector_type(4)));

static __device__ __forceinline__ float bf2f(unsigned short u) { return __uint_as_float((unsigned)u << 16); }
static __device__ __forceinline__ unsigned short f2bf(float f) { return __builtin_bit_cast(unsigned short, (__bf16)f); }

__global__ __launch_bounds__(256, 1) __attribute__((aligned(4096))) void win_attn_kernel(const char* x_, const char* wqkv_, const char* wo_, const char* rpb_,
                                                          const char* rpi_, const char* mask_, char* out_, char* ws) {
    __shared__ __attribute__((aligned(16))) char smem[163840];
    const void* ka = (const void*)__builtin_amdgcn_kernarg_segment_ptr();
    unsigned bid = blockIdx.x, tid = threadIdx.x, flg, w;
    unsigned ldsb = (unsigned)(size_t)smem;
    asm volatile(
        "s_load_dwordx16 s[68:83], s[0:1], 0\n"
        "v_readfirstlane_b32 s42, v0\n"
        "v_and_b32 v1, 63, v0\n"
        "s_lshr_b32 s31, s2, 1\n"
        "s_lshr_b32 s42, s42, 6\n"
        "s_and_b32 s31, s31, 0x7c\n"
        "v_lshlrev_b32 v2, 4, v1\n"
        "s_add_u32 s31, s31, s42\n"
        "s_mul_i32 s35, s31, 256\n"
        "v_min_u32 v10, 3, v1\n"
        "s_getpc_b64 s[66:67]\n"
        "v_lshl_add_u32 v10, v10, 6, s35\n"
        "global_load_dword v60, v10, s[66:67]\n"
        "s_mul_i32 s50, s31, 9\n"
        "v_min_u32 v136, 8, v1\n"
        "v_add_u32 v136, s50, v136\n"
        "v_mul_u32_u24 v133, 10923, v136\n"
        "v_lshlrev_b32 v136, 6, v136\n"
        "v_lshrrev_b32 v133, 16, v133\n"
        "v_mul_u32_u24 v133, 384, v133\n"
        "v_lshl_add_u32 v133, v136, 1, v133\n"
        "v_mul_u32_u24 v11, 171, v1\n"
        "v_lshrrev_b32 v4, 10, v11\n"
        "v_bfe_u32 v7, v11, 13, 1\n"
        "v_add_u32 v11, 10944, v11\n"
        "v_lshrrev_b32 v5, 10, v11\n"
        "v_bfe_u32 v8, v11, 13, 1\n"
        "v_add_u32 v11, 10944, v11\n"
        "v_lshrrev_b32 v6, 10, v11\n"
        "v_bfe_u32 v9, v11, 13, 1\n"
        "s_mul_i32 s40, s42, 3072\n"
        "s_mov_b32 s10, 100663296\n"
        "s_mov_b32 s11, 0x20000\n"
        "s_mov_b32 s6, 16384\n"
        "s_mov_b32 s7, 0x20000\n"
        "s_mov_b32 s62, 1800\n"
        "s_mov_b32 s63, 0x20000\n"
        "s_mov_b32 s22, 221184\n"
        "s_mov_b32 s23, 0x20000\n"
        "s_and_b32 s51, s42, 1\n"
        "v_lshlrev_b32 v134, 3, v0\n"
        "s_lshl_b32 s35, s51, 7\n"
        "v_and_b32 v10, 48, v1\n"
        "v_xor_b32 v131, s35, v2\n"
        "s_lshl_b32 s46, s42, 12\n"
        "v_xor_b32 v131, v131, v10\n"
        "v_xor_b32 v132, 64, v131\n"
        "s_and_b32 s44, s2, 63\n"
        "s_lshl3_add_u32 s44, s44, 4\n"
        "s_lshl_b32 s35, s42, 5\n"
        "s_movk_i32 s41, 384\n"
        "s_movk_i32 s45, -384\n"
        "v_lshrrev_b32 v10, 2, v4\n"
        "v_mad_i32_i24 v11, v10, s45, v2\n"
        "v_add_u32 v10, s44, v10\n"
        "v_lshl_or_b32 v242, v7, 4, s35\n"
        "v_and_b32 v10, 511, v10\n"
        "v_xor_b32 v11, v11, v242\n"
        "v_mad_u32_u24 v242, v10, s41, v11\n"
        "v_lshrrev_b32 v10, 2, v5\n"
        "v_mad_i32_i24 v11, v10, s45, v2\n"
        "v_add_u32 v10, s44, v10\n"
        "v_add_u32 v11, 1024, v11\n"
        "v_lshl_or_b32 v243, v8, 4, s35\n"
        "v_and_b32 v10, 511, v10\n"
        "v_xor_b32 v11, v11, v243\n"
        "v_mad_u32_u24 v243, v10, s41, v11\n"
        "v_lshrrev_b32 v10, 2, v6\n"
        "v_mad_i32_i24 v11, v10, s45, v2\n"
        "v_add_u32 v10, s44, v10\n"
        "v_add_u32 v11, 2048, v11\n"
        "v_lshl_or_b32 v244, v9, 4, s35\n"
        "v_and_b32 v10, 511, v10\n"
        "v_xor_b32 v11, v11, v244\n"
        "v_mad_u32_u24 v244, v10, s41, v11\n"
        "s_mul_i32 s43, s44, 384\n"
        "s_add_u32 s44, s44, 4\n"
        "s_and_b32 s44, s44, 511\n"
        "s_mul_i32 s44, s44, 384\n"
        "s_lshr_b32 s37, s2, 6\n"
        "s_lshl3_add_u32 s47, s37, s42\n"
        "s_add_u32 s47, s47, 68\n"
        "s_mul_i32 s47, s47, 196608\n"
        "s_lshl1_add_u32 s37, s37, s51\n"
        "s_add_u32 s37, s37, 1\n"
        "s_mul_i32 s37, s37, 786432\n"
        "s_mul_i32 s65, s42, 18432\n"
        "s_mov_b32 s14, 33554432\n"
        "s_mov_b32 s15, 0x20000\n"
        "s_mul_i32 s35, s42, 96\n"
        "s_movk_i32 s31, 1056\n"
        "s_movk_i32 s45, 4608\n"
        "v_add_u32 v3, s35, v2\n"
        "s_waitcnt lgkmcnt(0)\n"
        "s_mov_b64 s[60:61], s[74:75]\n"
        "s_mov_b64 s[20:21], s[70:71]\n"
        "s_mov_b64 s[4:5], s[76:77]\n"
        "s_mov_b64 s[8:9], s[68:69]\n"
        "buffer_load_dwordx2 v[144:145], v134, s[60:63], 0 offen\n"
        "s_add_u32 m0, s46, 122880\n"
        "s_mov_b32 s64, s46\n"
        "buffer_load_dword v133, s[20:23], 0 offen lds\n"
        "s_mov_b64 s[52:53], s[72:73]\n"
        "s_mov_b64 s[12:13], s[78:79]\n"
        "buffer_load_dwordx4 v131, s[4:7], s64 offen lds\n"
        "s_mov_b64 s[16:17], s[80:81]\n"
        "v_bfe_u32 v12, v4, 2, 1\n"
        "v_mad_u32_u24 v128, v4, s31, v3\n"
        "v_sub_u32 v12, v12, v7\n"
        "v_mad_i32_i24 v128, v12, s45, v128\n"
        "v_bfe_u32 v13, v5, 2, 1\n"
        "buffer_load_dwordx4 v131, s[4:7], s64 offen offset:1024 lds\n"
        "v_mad_u32_u24 v129, v5, s31, v3\n"
        "v_sub_u32 v13, v13, v8\n"
        "v_mad_i32_i24 v129, v13, s45, v129\n"
        "v_bfe_u32 v14, v6, 2, 1\n"
        "v_mad_u32_u24 v130, v6, s31, v3\n"
        "v_sub_u32 v14, v14, v9\n"
        "v_mad_i32_i24 v130, v14, s45, v130\n"
        "s_lshl_b32 s48, s42, 11\n"
        "s_lshl_b32 s36, s2, 13\n"
        "v_and_b32 v19, 16, v1\n"
        "buffer_load_dwordx4 v132, s[4:7], s64 offen offset:2048 lds\n"
        "s_add_u32 s48, s48, 155648\n"
        "s_add_u32 s36, s36, s48\n"
        "s_add_u32 s36, s36, 4038656\n"
        "s_lshl_b32 s35, s51, 6\n"
        "v_xor_b32 v249, v2, v19\n"
        "v_xor_b32 v249, s35, v249\n"
        "v_xor_b32 v253, 32, v249\n"
        "v_and_b32 v15, 15, v1\n"
        "v_lshrrev_b32 v16, 4, v1\n"
        "v_and_b32 v17, 12, v1\n"
        "buffer_load_dwordx4 v132, s[4:7], s64 offen offset:3072 lds\n"
        "v_and_b32 v18, 0xf0, v2\n"
        "v_add_u32 v17, v17, v15\n"
        "v_bfe_u32 v10, v1, 2, 3\n"
        "v_lshrrev_b32 v11, 5, v1\n"
        "v_lshl_or_b32 v10, v11, 4, v10\n"
        "v_and_b32 v255, 3, v1\n"
        "v_mul_u32_u24 v10, 96, v10\n"
        "v_lshl_add_u32 v255, v255, 3, s65\n"
        "v_add_u32 v255, v255, v10\n"
        "v_lshrrev_b32 v20, 1, v15\n"
        "s_add_u32 m0, s40, 73728\n"
        "s_sub_u32 s64, s47, 12582912\n"
        "buffer_load_dwordx4 v242, s[8:11], s64 offen lds\n"
        "v_xor_b32 v20, v20, v16\n"
        "v_lshlrev_b32 v20, 4, v20\n"
        "v_mad_u32_u24 v240, v17, s41, v20\n"
        "v_lshl_or_b32 v251, v17, 7, v20\n"
        "v_add_u32 v240, 73728, v240\n"
        "v_add_u32 v251, 155648, v251\n"
        "v_xor_b32 v241, 64, v240\n"
        "v_xor_b32 v252, 64, v251\n"
        "s_lshl_b32 s67, s42, 10\n"
        "s_add_u32 s67, s67, 139264\n"
        "s_add_u32 m0, s40, 74752\n"
        "s_nop 0\n"
        "buffer_load_dwordx4 v243, s[8:11], s64 offen lds\n"
        "v_lshlrev_b32 v135, 2, v0\n"
        "v_add_u32 v135, 139264, v135\n"
        "v_lshlrev_b32 v11, 5, v16\n"
        "v_xor_b32 v11, v11, v18\n"
        "v_lshl_or_b32 v11, v17, 8, v11\n"
        "v_add_u32 v140, 122880, v11\n"
        "v_xor_b32 v141, 16, v140\n"
        "v_xor_b32 v142, 128, v140\n"
        "v_xor_b32 v143, 144, v140\n"
        "v_lshrrev_b32 v10, 2, v15\n"
        "s_add_u32 m0, s40, 75776\n"
        "s_nop 0\n"
        "buffer_load_dwordx4 v244, s[8:11], s64 offen lds\n"
        "v_cmp_eq_u32 vcc, v10, v16\n"
        "v_mov_b32 v11, 0x3fb9\n"
        "v_mov_b32 v189, 0\n"
        "v_and_b32 v10, 48, v2\n"
        "v_cndmask_b32 v188, 0, v11, vcc\n"
        "v_lshlrev_b64 v[188:189], v10, v[188:189]\n"
        "v_cmp_gt_u32 vcc, 32, v1\n"
        "v_mov_b32 v11, 15957\n"
        "v_lshrrev_b32 v149, 2, v15\n"
        "v_lshrrev_b32 v151, 3, v19\n"
        "v_mov_b32 v148, 14702\n"
        "v_cndmask_b32 v11, v148, v11, vcc\n"
        "v_xor_b32 v151, v149, v151\n"
        "v_cmp_eq_u32 vcc, 0, v151\n"
        "v_mov_b32 v149, 0\n"
        "v_cndmask_b32 v148, 0, v11, vcc\n"
        "v_lshlrev_b64 v[148:149], v10, v[148:149]\n"
        "v_cmp_eq_u32 vcc, 1, v151\n"
        "v_mov_b32 v151, 0\n"
        "v_cndmask_b32 v150, 0, v11, vcc\n"
        "v_lshlrev_b64 v[150:151], v10, v[150:151]\n"
        "v_and_b32 v10, 8, v1\n"
        "v_add_u32 v10, v10, v15\n"
        "v_mul_u32_u24 v10, 96, v10\n"
        "v_add3_u32 v137, v10, v19, s65\n"
        "v_and_b32 v237, 1, v1\n"
        "v_mul_u32_u24 v237, 0xaaaa, v237\n"
        "v_mov_b64 v[170:171], v[188:189]\n"
        "v_mov_b64 v[210:211], v[188:189]\n"
        "v_mov_b64 v[228:229], v[188:189]\n"
        "v_add_u32 v237, 0x4444, v237\n"
        "v_mov_b32 v11, 0x3f803f80\n"
        "v_accvgpr_write_b32 a252, v11\n"
        "v_accvgpr_write_b32 a253, v11\n"
        "v_accvgpr_write_b32 a254, v11\n"
        "v_accvgpr_write_b32 a255, v11\n"
        "v_lshl_or_b32 v10, v16, 10, v18\n"
        "s_mul_i32 s31, s42, 6144\n"
        "s_add_u32 s31, s31, 73728\n"
        "s_lshl_b32 s35, s51, 9\n"
        "s_add_u32 s35, s35, 73728\n"
        "v_lshl_or_b32 v11, v16, 2, v18\n"
        "v_add_u32 v247, s35, v10\n"
        "v_add_u32 v245, s31, v11\n"
        "v_mul_u32_u24 v10, 384, v16\n"
        "s_movk_i32 s45, 1728\n"
        "v_lshrrev_b32 v235, 2, v15\n"
        "v_lshrrev_b32 v254, 1, v15\n"
        "v_and_b32 v11, 1, v1\n"
        "v_mad_u32_u24 v235, v235, 48, v10\n"
        "v_and_b32 v248, 1, v254\n"
        "v_mad_u32_u24 v235, v248, s45, v235\n"
        "v_mul_u32_u24 v248, 36, v11\n"
        "v_lshlrev_b32 v11, 5, v11\n"
        "v_xor_b32 v254, v254, v248\n"
        "v_add3_u32 v235, v235, v11, s65\n"
        "v_mad_u32_u24 v254, v254, 48, v10\n"
        "v_add3_u32 v254, v254, 16, s65\n"
        "s_lshr_b32 s31, s42, 1\n"
        "s_mul_i32 s35, s31, 36864\n"
        "s_movk_i32 s45, 18048\n"
        "v_mad_u32_u24 v10, v15, 48, v10\n"
        "v_lshrrev_b32 v248, 3, v15\n"
        "v_mad_u32_u24 v10, v248, s45, v10\n"
        "v_add_u32 v248, s35, v10\n"
        "s_lshr_b32 s49, s42, 1\n"
        "s_mul_i32 s49, s49, -192\n"
        "s_add_u32 s49, s49, 128\n"
        "s_mul_i32 s31, s31, 256\n"
        "v_mul_u32_u24 v10, 196608, v16\n"
        "v_mul_u32_u24 v11, 8, v15\n"
        "v_add3_u32 v250, v10, v11, s31\n"
        "s_lshl_b32 s35, s51, 6\n"
        "s_add_u32 s66, s31, s35\n"
        "s_lshl_b32 s35, s42, 5\n"
        "s_sub_u32 s35, s35, s66\n"
        "s_add_u32 s27, s35, 64\n"
        "s_movk_i32 s31, 288\n"
        "s_movk_i32 s35, 768\n"
        "v_mad_u32_u24 v10, v4, s31, v2\n"
        "v_bfe_u32 v11, v10, 6, 1\n"
        "v_add_u32 v10, s66, v10\n"
        "v_mad_i32_i24 v10, v11, s27, v10\n"
        "v_bfe_u32 v11, v4, 1, 1\n"
        "v_bfe_u32 v232, v4, 2, 1\n"
        "v_sub_u32 v11, v11, v232\n"
        "v_mad_i32_i24 v232, v11, s35, v10\n"
        "v_mad_u32_u24 v10, v5, s31, v2\n"
        "v_bfe_u32 v11, v10, 6, 1\n"
        "v_add_u32 v10, s66, v10\n"
        "v_mad_i32_i24 v10, v11, s27, v10\n"
        "v_bfe_u32 v11, v5, 1, 1\n"
        "v_bfe_u32 v233, v5, 2, 1\n"
        "v_sub_u32 v11, v11, v233\n"
        "v_mad_i32_i24 v233, v11, s35, v10\n"
        "v_mad_u32_u24 v10, v6, s31, v2\n"
        "v_bfe_u32 v11, v10, 6, 1\n"
        "v_add_u32 v10, s66, v10\n"
        "v_mad_i32_i24 v10, v11, s27, v10\n"
        "v_bfe_u32 v11, v6, 1, 1\n"
        "v_bfe_u32 v234, v6, 2, 1\n"
        "v_sub_u32 v11, v11, v234\n"
        "v_mad_i32_i24 v234, v11, s35, v10\n"
        "v_mad_i32_i24 v138, v136, -3, v133\n"
        "v_add_u32 v138, v138, v133\n"
        "s_mov_b32 s26, 0x3fb8aa3b\n"
        "s_mov_b32 s24, 0x5d800000\n"
        "s_mov_b32 s27, 14\n"
        "s_mov_b32 s28, 98304\n"
        "s_mov_b32 s29, 73728\n"
        "s_mov_b32 s30, 122880\n"
        "s_mov_b32 s45, 0\n"
        "s_mov_b32 s25, 0\n"
        "s_mov_b32 s54, 73728\n"
        "s_mov_b32 s55, 0x20000\n"
        "s_mov_b32 s18, 100663296\n"
        "s_mov_b32 s19, 0x20000\n"
        "s_mov_b32 s56, 36864\n"
        "s_mov_b32 s57, 73728\n"
        "s_mov_b32 s58, 110592\n"
        "s_mov_b32 s59, 147456\n"
        "s_mov_b32 s60, 184320\n"
        "s_mov_b32 s61, 24576\n"
        "s_mov_b32 s62, 61440\n"
        "s_mov_b32 s63, 12288\n"
        "s_mov_b32 s34, 49152\n"
        "s_add_u32 m0, s40, 86016\n"
        "s_sub_u32 s64, s47, 11796480\n"
        "buffer_load_dwordx4 v242, s[8:11], s64 offen lds\n"
        "s_add_u32 m0, s40, 87040\n"
        "s_nop 0\n"
        "buffer_load_dwordx4 v243, s[8:11], s64 offen lds\n"
        "s_waitcnt vmcnt(10)\n"
        "v_and_b32 v146, 0xffff0000, v144\n"
        "v_and_b32 v147, 0xffff0000, v145\n"
        "v_lshlrev_b32 v144, 16, v144\n"
        "v_lshlrev_b32 v145, 16, v145\n"
        "s_add_u32 m0, s40, 88064\n"
        "v_mul_f32 v146, s26, v146\n"
        "buffer_load_dwordx4 v244, s[8:11], s64 offen lds\n"
        "v_mul_f32 v147, s26, v147\n"
        "v_mul_f32 v144, s26, v144\n"
        "v_mul_f32 v145, s26, v145\n"
        "s_mov_b32 s38, s67\n"
        "s_mov_b32 s39, s67\n"
        "ds_write2st64_b32 v135, v144, v146 offset0:0 offset1:4\n"
        "ds_write2st64_b32 v135, v145, v147 offset0:8 offset1:12\n"
        "s_cmp_eq_u32 s42, 0\n"
        "s_cbranch_scc1 41f\n"
        "s_cmp_eq_u32 s42, 1\n"
        "s_cbranch_scc1 42f\n"
        "s_cmp_eq_u32 s42, 2\n"
        "s_cbranch_scc1 43f\n"
        "s_cmp_eq_u32 s42, 3\n"
        "s_cbranch_scc1 44f\n"
        "60:\n"
        "s_waitcnt lgkmcnt(0)\n"
        "s_waitcnt vmcnt(6)\n"
        "s_barrier\n"
        "s_add_u32 m0, s65, 0\n"
        "s_mov_b32 s64, 384\n"
        "buffer_load_dwordx4 v128, s[20:23], s64 offen lds\n"
        "buffer_load_dwordx4 v129, s[20:23], s64 offen offset:1024 lds\n"
        "ds_read_b128 v[0:3], v140 offset:0\n"
        "ds_read_b128 v[4:7], v141 offset:0\n"
        "ds_read_b128 v[16:19], v140 offset:1024\n"
        "ds_read_b128 v[20:23], v141 offset:1024\n"
        "ds_read_b128 v[8:11], v142 offset:0\n"
        "ds_read_b128 v[24:27], v142 offset:1024\n"
        "ds_read_b128 v[12:15], v143 offset:0\n"
        "ds_read_b128 v[28:31], v143 offset:1024\n"
        "ds_read_b128 v[32:35], v140 offset:8192\n"
        "ds_read_b128 v[36:39], v141 offset:8192\n"
        "ds_read_b128 v[40:43], v142 offset:8192\n"
        "ds_read_b128 v[44:47], v143 offset:8192\n"
        "ds_read_b128 v[48:51], v140 offset:9216\n"
        "s_cmp_eq_u32 s42, 0\n"
        "s_cbranch_scc1 51f\n"
        "s_cmp_eq_u32 s42, 1\n"
        "s_cbranch_scc1 45f\n"
        "s_cmp_eq_u32 s42, 2\n"
        "s_cbranch_scc1 46f\n"
        "s_cmp_eq_u32 s42, 3\n"
        "s_cbranch_scc1 47f\n"
        "s_nop 0\n"
        "61:\n"
        "buffer_load_dwordx4 v130, s[20:23], s64 offen offset:2048 lds\n"
        "s_add_u32 m0, s65, 3072\n"
        "s_waitcnt lgkmcnt(12)\n"
        "v_lshl_add_u64 v[0:1], v[0:1], 2, s[38:39]\n"
        "buffer_load_dwordx4 v128, s[20:23], 0 offen lds\n"
        "v_lshl_add_u64 v[2:3], v[2:3], 2, s[38:39]\n"
        "s_waitcnt lgkmcnt(11)\n"
        "v_lshl_add_u64 v[4:5], v[4:5], 2, s[38:39]\n"
        "v_lshl_add_u64 v[6:7], v[6:7], 2, s[38:39]\n"
        "s_waitcnt lgkmcnt(10)\n"
        "v_lshl_add_u64 v[16:17], v[16:17], 2, s[38:39]\n"
        "v_lshl_add_u64 v[18:19], v[18:19], 2, s[38:39]\n"
        "buffer_load_dwordx4 v129, s[20:23], 0 offen offset:1024 lds\n"
        "ds_read_b32 v0, v0\n"
        "ds_read_b32 v1, v1\n"
        "ds_read_b32 v2, v2\n"
        "ds_read_b32 v3, v3\n"
        "ds_read_b32 v4, v4\n"
        "ds_read_b32 v5, v5\n"
        "ds_read_b32 v6, v6\n"
        "ds_read_b32 v7, v7\n"
        "ds_read_b32 v16, v16\n"
        "ds_read_b32 v17, v17\n"
        "ds_read_b32 v18, v18\n"
        "ds_read_b32 v19, v19\n"
        "buffer_load_dwordx4 v130, s[20:23], 0 offen offset:2048 lds\n"
        "s_waitcnt lgkmcnt(15)\n"
        "v_lshl_add_u64 v[20:21], v[20:21], 2, s[38:39]\n"
        "v_lshl_add_u64 v[22:23], v[22:23], 2, s[38:39]\n"
        "v_lshl_add_u64 v[8:9], v[8:9], 2, s[38:39]\n"
        "v_lshl_add_u64 v[10:11], v[10:11], 2, s[38:39]\n"
        "v_lshl_add_u64 v[24:25], v[24:25], 2, s[38:39]\n"
        "v_lshl_add_u64 v[26:27], v[26:27], 2, s[38:39]\n"
        "ds_read_b32 v20, v20\n"
        "ds_read_b32 v21, v21\n"
        "ds_read_b32 v22, v22\n"
        "ds_read_b32 v23, v23\n"
        "ds_read_b32 v8, v8\n"
        "ds_read_b32 v9, v9\n"
        "ds_read_b32 v10, v10\n"
        "ds_read_b32 v11, v11\n"
        "ds_read_b32 v24, v24\n"
        "ds_read_b32 v25, v25\n"
        "ds_read_b32 v26, v26\n"
        "ds_read_b32 v27, v27\n"
        "s_add_u32 m0, s65, 6144\n"
        "v_lshl_add_u64 v[12:13], v[12:13], 2, s[38:39]\n"
        "buffer_load_dwordx4 v128, s[20:23], s56 offen lds\n"
        "v_lshl_add_u64 v[14:15], v[14:15], 2, s[38:39]\n"
        "v_lshl_add_u64 v[28:29], v[28:29], 2, s[38:39]\n"
        "v_lshl_add_u64 v[30:31], v[30:31], 2, s[38:39]\n"
        "v_lshl_add_u64 v[32:33], v[32:33], 2, s[38:39]\n"
        "v_lshl_add_u64 v[34:35], v[34:35], 2, s[38:39]\n"
        "ds_read_b32 v12, v12\n"
        "ds_read_b32 v13, v13\n"
        "ds_read_b32 v14, v14\n"
        "ds_read_b32 v15, v15\n"
        "ds_read_b32 v28, v28\n"
        "ds_read_b32 v29, v29\n"
        "ds_read_b32 v30, v30\n"
        "ds_read_b32 v31, v31\n"
        "ds_read_b32 v32, v32\n"
        "ds_read_b32 v33, v33\n"
        "ds_read_b32 v34, v34\n"
        "ds_read_b32 v35, v35\n"
        "buffer_load_dwordx4 v129, s[20:23], s56 offen offset:1024 lds\n"
        "v_lshl_add_u64 v[36:37], v[36:37], 2, s[38:39]\n"
        "v_lshl_add_u64 v[38:39], v[38:39], 2, s[38:39]\n"
        "s_waitcnt lgkmcnt(15)\n"
        "v_lshl_add_u64 v[40:41], v[40:41], 2, s[38:39]\n"
        "v_lshl_add_u64 v[42:43], v[42:43], 2, s[38:39]\n"
        "v_lshl_add_u64 v[44:45], v[44:45], 2, s[38:39]\n"
        "v_lshl_add_u64 v[46:47], v[46:47], 2, s[38:39]\n"
        "ds_read_b32 v36, v36\n"
        "ds_read_b32 v37, v37\n"
        "ds_read_b32 v38, v38\n"
        "ds_read_b32 v39, v39\n"
        "ds_read_b32 v40, v40\n"
        "ds_read_b32 v41, v41\n"
        "ds_read_b32 v42, v42\n"
        "ds_read_b32 v43, v43\n"
        "ds_read_b32 v44, v44\n"
        "ds_read_b32 v45, v45\n"
        "ds_read_b32 v46, v46\n"
        "ds_read_b32 v47, v47\n"
        "buffer_load_dwordx4 v130, s[20:23], s56 offen offset:2048 lds\n"
        "v_lshl_add_u64 v[48:49], v[48:49], 2, s[38:39]\n"
        "v_lshl_add_u64 v[50:51], v[50:51], 2, s[38:39]\n"
        "ds_read_b32 v48, v48\n"
        "ds_read_b32 v49, v49\n"
        "ds_read_b32 v50, v50\n"
        "ds_read_b32 v51, v51\n"
        "s_waitcnt vmcnt(9)\n"
        "s_barrier\n"
        "ds_read_b128 a[216:219], v240 offset:0\n"
        "ds_read_b128 a[220:223], v240 offset:1536\n"
        "ds_read_b128 a[224:227], v240 offset:12288\n"
        "ds_read_b128 a[228:231], v240 offset:13824\n"
        "s_waitcnt vmcnt(6)\n"
        "ds_read_b64_tr_b16 a[72:73], v255 offset:0\n"
        "ds_read_b64_tr_b16 a[74:75], v255 offset:768\n"
        "ds_read_b64_tr_b16 a[96:97], v255 offset:32\n"
        "ds_read_b64_tr_b16 a[98:99], v255 offset:800\n"
        "ds_read_b64_tr_b16 a[120:121], v255 offset:64\n"
        "ds_read_b64_tr_b16 a[122:123], v255 offset:832\n"
        "s_add_u32 m0, s65, 8832\n"
        "s_nop 0\n"
        "buffer_load_dwordx4 v128, s[20:23], s56 offen offset:384 lds\n"
        "s_waitcnt lgkmcnt(4)\n"
        "v_mfma_f32_16x16x32_bf16 v[152:155], a[72:75], a[216:219], 0\n"
        "ds_read_b128 a[232:235], v241 offset:0\n"
        "ds_read_b128 a[236:239], v241 offset:1536\n"
        "v_mfma_f32_16x16x32_bf16 v[172:175], a[72:75], a[220:223], 0\n"
        "ds_read_b128 a[240:243], v241 offset:12288\n"
        "ds_read_b128 a[244:247], v241 offset:13824\n"
        "v_mfma_f32_16x16x32_bf16 v[192:195], a[72:75], a[224:227], 0\n"
        "v_mfma_f32_16x16x32_bf16 v[212:215], a[72:75], a[228:231], 0\n"
        "s_waitcnt lgkmcnt(6)\n"
        "buffer_load_dwordx4 v129, s[20:23], s56 offen offset:1408 lds\n"
        "v_mfma_f32_16x16x32_bf16 v[88:91], a[96:99], a[228:231], 0\n"
        "v_mfma_f32_16x16x32_bf16 v[80:83], a[96:99], a[224:227], 0\n"
        "buffer_load_dwordx4 v130, s[20:23], s56 offen offset:2432 lds\n"
        "s_waitcnt vmcnt(6)\n"
        "ds_read_b128 a[212:215], v137 offset:3072\n"
        "v_mfma_f32_16x16x32_bf16 v[68:71], a[96:99], a[220:223], 0\n"
        "s_add_u32 m0, s65, 12288\n"
        "ds_read_b128 a[208:211], v137 offset:3840\n"
        "ds_read_b128 a[204:207], v137 offset:3104\n"
        "v_mfma_f32_16x16x32_bf16 v[64:67], a[96:99], a[216:219], 0\n"
        "s_waitcnt lgkmcnt(3)\n"
        "ds_read_b128 a[200:203], v137 offset:3872\n"
        "ds_read_b128 a[196:199], v137 offset:3136\n"
        "v_mfma_f32_16x16x32_bf16 v[72:75], a[120:123], a[216:219], 0\n"
        "buffer_load_dwordx4 v128, s[20:23], s57 offen lds\n"
        "v_mfma_f32_16x16x32_bf16 v[76:79], a[120:123], a[220:223], 0\n"
        "s_waitcnt lgkmcnt(4)\n"
        "ds_read_b128 a[192:195], v137 offset:3904\n"
        "v_mfma_f32_16x16x32_bf16 v[112:115], a[212:215], v[148:151], 0\n"
        "s_waitcnt lgkmcnt(4)\n"
        "s_waitcnt vmcnt(4)\n"
        "ds_read_b128 a[188:191], v137 offset:6144\n"
        "ds_read_b128 a[184:187], v137 offset:6912\n"
        "v_mfma_f32_16x16x32_bf16 v[116:119], a[208:211], v[148:151], 0\n"
        "buffer_load_dwordx4 v129, s[20:23], s57 offen offset:1024 lds\n"
        "v_mfma_f32_16x16x32_bf16 v[84:87], a[120:123], a[224:227], 0\n"
        "ds_read_b128 a[180:183], v137 offset:6176\n"
        "ds_read_b128 a[176:179], v137 offset:6944\n"
        "v_mfma_f32_16x16x32_bf16 v[92:95], a[120:123], a[228:231], 0\n"
        "s_waitcnt lgkmcnt(5)\n"
        "ds_read_b128 a[172:175], v137 offset:6208\n"
        "ds_read_b128 a[168:171], v137 offset:6976\n"
        "v_mfma_f32_16x16x32_bf16 v[120:123], a[204:207], v[148:151], 0\n"
        "v_cvt_pk_bf16_f32 v112, v112, v113\n"
        "v_accvgpr_write_b32 a0, v112\n"
        "v_mfma_f32_16x16x32_bf16 v[124:127], a[200:203], v[148:151], 0\n"
        "buffer_load_dwordx4 v130, s[20:23], s57 offen offset:2048 lds\n"
        "v_mfma_f32_16x16x32_bf16 v[96:99], a[196:199], v[148:151], 0\n"
        "s_waitcnt lgkmcnt(4)\n"
        "v_cvt_pk_bf16_f32 v114, v114, v115\n"
        "v_accvgpr_write_b32 a1, v114\n"
        "v_mfma_f32_16x16x32_bf16 v[100:103], a[192:195], v[148:151], 0\n"
        "s_add_u32 m0, s65, 14976\n"
        "v_cvt_pk_bf16_f32 v116, v116, v117\n"
        "v_accvgpr_write_b32 a2, v116\n"
        "v_mfma_f32_16x16x32_bf16 v[104:107], a[188:191], v[148:151], 0\n"
        "v_cvt_pk_bf16_f32 v118, v118, v119\n"
        "v_accvgpr_write_b32 a3, v118\n"
        "v_mfma_f32_16x16x32_bf16 v[108:111], a[184:187], v[148:151], 0\n"
        "buffer_load_dwordx4 v128, s[20:23], s57 offen offset:384 lds\n"
        "v_mfma_f32_16x16x32_bf16 v[156:159], a[0:3], a[216:219], 0\n"
        "v_cvt_pk_bf16_f32 v120, v120, v121\n"
        "v_accvgpr_write_b32 a24, v120\n"
        "v_mfma_f32_16x16x32_bf16 v[176:179], a[0:3], a[220:223], 0\n"
        "v_cvt_pk_bf16_f32 v122, v122, v123\n"
        "v_accvgpr_write_b32 a25, v122\n"
        "v_mfma_f32_16x16x32_bf16 v[196:199], a[0:3], a[224:227], 0\n"
        "v_cvt_pk_bf16_f32 v124, v124, v125\n"
        "v_accvgpr_write_b32 a26, v124\n"
        "v_mfma_f32_16x16x32_bf16 v[216:219], a[0:3], a[228:231], 0\n"
        "buffer_load_dwordx4 v129, s[20:23], s57 offen offset:1408 lds\n"
        "v_cvt_pk_bf16_f32 v126, v126, v127\n"
        "v_accvgpr_write_b32 a27, v126\n"
        "s_nop 1\n"
        "v_mfma_f32_16x16x32_bf16 v[224:227], a[24:27], a[228:231], 0\n"
        "v_cvt_pk_bf16_f32 v96, v96, v97\n"
        "v_accvgpr_write_b32 a48, v96\n"
        "v_mfma_f32_16x16x32_bf16 v[204:207], a[24:27], a[224:227], 0\n"
        "s_waitcnt vmcnt(5)\n"
        "ds_read_b64_tr_b16 a[76:77], v255 offset:9216\n"
        "ds_read_b64_tr_b16 a[78:79], v255 offset:9984\n"
        "v_mfma_f32_16x16x32_bf16 v[184:187], a[24:27], a[220:223], 0\n"
        "ds_read_b64_tr_b16 a[100:101], v255 offset:9248\n"
        "ds_read_b64_tr_b16 a[102:103], v255 offset:10016\n"
        "v_mfma_f32_16x16x32_bf16 v[164:167], a[24:27], a[216:219], 0\n"
        "buffer_load_dwordx4 v130, s[20:23], s57 offen offset:2432 lds\n"
        "ds_read_b64_tr_b16 a[124:125], v255 offset:9280\n"
        "ds_read_b64_tr_b16 a[126:127], v255 offset:10048\n"
        "v_cvt_pk_bf16_f32 v98, v98, v99\n"
        "v_accvgpr_write_b32 a49, v98\n"
        "v_cvt_pk_bf16_f32 v100, v100, v101\n"
        "v_accvgpr_write_b32 a50, v100\n"
        "v_cvt_pk_bf16_f32 v102, v102, v103\n"
        "v_accvgpr_write_b32 a51, v102\n"
        "s_nop 1\n"
        "v_mfma_f32_16x16x32_bf16 v[160:163], a[48:51], a[216:219], 0\n"
        "s_add_u32 m0, s65, 0\n"
        "ds_read_b128 a[248:251], v240 offset:128\n"
        "v_cvt_pk_bf16_f32 v104, v104, v105\n"
        "v_mfma_f32_16x16x32_bf16 v[180:183], a[48:51], a[220:223], 0\n"
        "ds_read_b128 a[216:219], v240 offset:1664\n"
        "v_accvgpr_write_b32 a4, v104\n"
        "v_mfma_f32_16x16x32_bf16 v[200:203], a[48:51], a[224:227], 0\n"
        "buffer_load_dwordx4 v128, s[20:23], s58 offen lds\n"
        "v_mfma_f32_16x16x32_bf16 v[220:223], a[48:51], a[228:231], 0\n"
        "s_waitcnt lgkmcnt(6)\n"
        "ds_read_b128 a[220:223], v240 offset:12416\n"
        "ds_read_b128 a[224:227], v240 offset:13952\n"
        "v_mfma_f32_16x16x32_bf16 v[112:115], a[180:183], v[148:151], 0\n"
        "v_cvt_pk_bf16_f32 v106, v106, v107\n"
        "v_accvgpr_write_b32 a5, v106\n"
        "v_mfma_f32_16x16x32_bf16 v[116:119], a[176:179], v[148:151], 0\n"
        "v_cvt_pk_bf16_f32 v108, v108, v109\n"
        "v_accvgpr_write_b32 a6, v108\n"
        "v_mfma_f32_16x16x32_bf16 v[152:155], a[76:79], a[232:235], v[152:155]\n"
        "buffer_load_dwordx4 v129, s[20:23], s58 offen offset:1024 lds\n"
        "v_mfma_f32_16x16x32_bf16 v[172:175], a[76:79], a[236:239], v[172:175]\n"
        "v_cvt_pk_bf16_f32 v110, v110, v111\n"
        "v_accvgpr_write_b32 a7, v110\n"
        "v_mfma_f32_16x16x32_bf16 v[192:195], a[76:79], a[240:243], v[192:195]\n"
        "v_cvt_pk_bf16_f32 v112, v112, v113\n"
        "v_accvgpr_write_b32 a28, v112\n"
        "v_mfma_f32_16x16x32_bf16 v[212:215], a[76:79], a[244:247], v[212:215]\n"
        "v_cvt_pk_bf16_f32 v114, v114, v115\n"
        "v_accvgpr_write_b32 a29, v114\n"
        "v_mfma_f32_16x16x32_bf16 v[156:159], a[4:7], a[232:235], v[156:159]\n"
        "buffer_load_dwordx4 v130, s[20:23], s58 offen offset:2048 lds\n"
        "v_mfma_f32_16x16x32_bf16 v[176:179], a[4:7], a[236:239], v[176:179]\n"
        "s_add_u32 m0, s65, 2688\n"
        "v_cvt_pk_bf16_f32 v116, v116, v117\n"
        "v_accvgpr_write_b32 a30, v116\n"
        "v_mfma_f32_16x16x32_bf16 v[196:199], a[4:7], a[240:243], v[196:199]\n"
        "v_cvt_pk_bf16_f32 v118, v118, v119\n"
        "v_accvgpr_write_b32 a31, v118\n"
        "v_mfma_f32_16x16x32_bf16 v[216:219], a[4:7], a[244:247], v[216:219]\n"
        "s_waitcnt vmcnt(6)\n"
        "ds_read_b128 a[212:215], v137 offset:12288\n"
        "ds_read_b128 a[208:211], v137 offset:13056\n"
        "v_mfma_f32_16x16x32_bf16 v[120:123], a[172:175], v[148:151], 0\n"
        "buffer_load_dwordx4 v128, s[20:23], s58 offen offset:384 lds\n"
        "v_mfma_f32_16x16x32_bf16 v[124:127], a[168:171], v[148:151], 0\n"
        "ds_read_b128 a[204:207], v137 offset:12320\n"
        "ds_read_b128 a[200:203], v137 offset:13088\n"
        "v_mfma_f32_16x16x32_bf16 v[224:227], a[28:31], a[244:247], v[224:227]\n"
        ".long 0xbec200ff\n"
        "41:\n"
        "s_branch 60b\n"
        "ds_read_b128 a[196:199], v137 offset:12352\n"
        "ds_read_b128 a[192:195], v137 offset:13120\n"
        "v_mfma_f32_16x16x32_bf16 v[204:207], a[28:31], a[240:243], v[204:207]\n"
        "buffer_load_dwordx4 v129, s[20:23], s58 offen offset:1408 lds\n"
        "v_mfma_f32_16x16x32_bf16 v[184:187], a[28:31], a[236:239], v[184:187]\n"
        "v_cvt_pk_bf16_f32 v120, v120, v121\n"
        "v_accvgpr_write_b32 a52, v120\n"
        "v_mfma_f32_16x16x32_bf16 v[164:167], a[28:31], a[232:235], v[164:167]\n"
        "s_waitcnt lgkmcnt(4)\n"
        "v_cvt_pk_bf16_f32 v122, v122, v123\n"
        "v_accvgpr_write_b32 a53, v122\n"
        "v_mfma_f32_16x16x32_bf16 v[96:99], a[212:215], v[148:151], 0\n"
        "v_cvt_pk_bf16_f32 v124, v124, v125\n"
        "v_accvgpr_write_b32 a54, v124\n"
        "v_mfma_f32_16x16x32_bf16 v[100:103], a[208:211], v[148:151], 0\n"
        "buffer_load_dwordx4 v130, s[20:23], s58 offen offset:2432 lds\n"
        "v_mfma_f32_16x16x32_bf16 v[88:91], a[100:103], a[244:247], v[88:91]\n"
        "s_add_u32 m0, s65, 6144\n"
        "v_cvt_pk_bf16_f32 v126, v126, v127\n"
        "v_accvgpr_write_b32 a55, v126\n"
        "v_mfma_f32_16x16x32_bf16 v[80:83], a[100:103], a[240:243], v[80:83]\n"
        "v_cvt_pk_bf16_f32 v96, v96, v97\n"
        "v_accvgpr_write_b32 a8, v96\n"
        "v_mfma_f32_16x16x32_bf16 v[68:71], a[100:103], a[236:239], v[68:71]\n"
        "v_cvt_pk_bf16_f32 v98, v98, v99\n"
        "v_accvgpr_write_b32 a9, v98\n"
        "v_mfma_f32_16x16x32_bf16 v[64:67], a[100:103], a[232:235], v[64:67]\n"
        "buffer_load_dwordx4 v128, s[20:23], s59 offen lds\n"
        "v_mfma_f32_16x16x32_bf16 v[160:163], a[52:55], a[232:235], v[160:163]\n"
        "s_waitcnt vmcnt(7)\n"
        "ds_read_b64_tr_b16 a[80:81], v255 offset:15360\n"
        "ds_read_b64_tr_b16 a[82:83], v255 offset:16128\n"
        "v_mfma_f32_16x16x32_bf16 v[180:183], a[52:55], a[236:239], v[180:183]\n"
        "ds_read_b64_tr_b16 a[104:105], v255 offset:15392\n"
        "ds_read_b64_tr_b16 a[106:107], v255 offset:16160\n"
        "v_mfma_f32_16x16x32_bf16 v[200:203], a[52:55], a[240:243], v[200:203]\n"
        "ds_read_b64_tr_b16 a[128:129], v255 offset:15424\n"
        "ds_read_b64_tr_b16 a[130:131], v255 offset:16192\n"
        "v_mfma_f32_16x16x32_bf16 v[220:223], a[52:55], a[244:247], v[220:223]\n"
        "s_waitcnt lgkmcnt(6)\n"
        "v_cvt_pk_bf16_f32 v100, v100, v101\n"
        "v_accvgpr_write_b32 a10, v100\n"
        "v_mfma_f32_16x16x32_bf16 v[104:107], a[204:207], v[148:151], 0\n"
        "buffer_load_dwordx4 v129, s[20:23], s59 offen offset:1024 lds\n"
        "v_mfma_f32_16x16x32_bf16 v[108:111], a[200:203], v[148:151], 0\n"
        "ds_read_b128 a[228:231], v241 offset:128\n"
        "v_cvt_pk_bf16_f32 v102, v102, v103\n"
        "v_mfma_f32_16x16x32_bf16 v[72:75], a[124:127], a[232:235], v[72:75]\n"
        "ds_read_b128 a[232:235], v241 offset:1664\n"
        "v_accvgpr_write_b32 a11, v102\n"
        "v_mfma_f32_16x16x32_bf16 v[76:79], a[124:127], a[236:239], v[76:79]\n"
        "buffer_load_dwordx4 v130, s[20:23], s59 offen offset:2048 lds\n"
        "v_mfma_f32_16x16x32_bf16 v[84:87], a[124:127], a[240:243], v[84:87]\n"
        "s_add_u32 m0, s65, 8832\n"
        "ds_read_b128 a[236:239], v241 offset:12416\n"
        "ds_read_b128 a[240:243], v241 offset:13952\n"
        "v_mfma_f32_16x16x32_bf16 v[92:95], a[124:127], a[244:247], v[92:95]\n"
        "s_waitcnt vmcnt(6)\n"
        "ds_read_b128 a[188:191], v137 offset:0\n"
        "ds_read_b128 a[184:187], v137 offset:768\n"
        "v_mfma_f32_16x16x32_bf16 v[156:159], a[8:11], a[248:251], v[156:159]\n"
        "buffer_load_dwordx4 v128, s[20:23], s59 offen offset:384 lds\n"
        "v_mfma_f32_16x16x32_bf16 v[176:179], a[8:11], a[216:219], v[176:179]\n"
        "ds_read_b128 a[180:183], v137 offset:32\n"
        "ds_read_b128 a[176:179], v137 offset:800\n"
        "v_mfma_f32_16x16x32_bf16 v[196:199], a[8:11], a[220:223], v[196:199]\n"
        "ds_read_b128 a[172:175], v137 offset:64\n"
        "ds_read_b128 a[168:171], v137 offset:832\n"
        "v_mfma_f32_16x16x32_bf16 v[216:219], a[8:11], a[224:227], v[216:219]\n"
        "v_cvt_pk_bf16_f32 v104, v104, v105\n"
        "v_accvgpr_write_b32 a32, v104\n"
        "v_mfma_f32_16x16x32_bf16 v[112:115], a[196:199], v[148:151], 0\n"
        "buffer_load_dwordx4 v129, s[20:23], s59 offen offset:1408 lds\n"
        "v_mfma_f32_16x16x32_bf16 v[116:119], a[192:195], v[148:151], 0\n"
        "s_waitcnt lgkmcnt(4)\n"
        "v_cvt_pk_bf16_f32 v106, v106, v107\n"
        "v_accvgpr_write_b32 a33, v106\n"
        "v_mfma_f32_16x16x32_bf16 v[152:155], a[80:83], a[248:251], v[152:155]\n"
        "v_cvt_pk_bf16_f32 v108, v108, v109\n"
        "v_accvgpr_write_b32 a34, v108\n"
        "v_mfma_f32_16x16x32_bf16 v[172:175], a[80:83], a[216:219], v[172:175]\n"
        "v_cvt_pk_bf16_f32 v110, v110, v111\n"
        "v_accvgpr_write_b32 a35, v110\n"
        "v_mfma_f32_16x16x32_bf16 v[192:195], a[80:83], a[220:223], v[192:195]\n"
        "buffer_load_dwordx4 v130, s[20:23], s59 offen offset:2432 lds\n"
        "v_mfma_f32_16x16x32_bf16 v[212:215], a[80:83], a[224:227], v[212:215]\n"
        "s_add_u32 m0, s65, 12288\n"
        "v_cvt_pk_bf16_f32 v112, v112, v113\n"
        "v_accvgpr_write_b32 a56, v112\n"
        "v_mfma_f32_16x16x32_bf16 v[224:227], a[32:35], a[224:227], v[224:227]\n"
        "ds_read_b128 v[52:55], v141 offset:9216\n"
        "v_cvt_pk_bf16_f32 v114, v114, v115\n"
        "v_mfma_f32_16x16x32_bf16 v[204:207], a[32:35], a[220:223], v[204:207]\n"
        "v_accvgpr_write_b32 a57, v114\n"
        "v_cvt_pk_bf16_f32 v116, v116, v117\n"
        "v_mfma_f32_16x16x32_bf16 v[184:187], a[32:35], a[216:219], v[184:187]\n"
        "buffer_load_dwordx4 v128, s[20:23], s60 offen lds\n"
        "v_mfma_f32_16x16x32_bf16 v[164:167], a[32:35], a[248:251], v[164:167]\n"
        "v_accvgpr_write_b32 a58, v116\n"
        "v_cvt_pk_bf16_f32 v118, v118, v119\n"
        "v_mfma_f32_16x16x32_bf16 v[120:123], a[188:191], v[148:151], 0\n"
        "s_waitcnt vmcnt(7)\n"
        "ds_read_b64_tr_b16 a[84:85], v255 offset:3072\n"
        "ds_read_b64_tr_b16 a[86:87], v255 offset:3840\n"
        "v_mfma_f32_16x16x32_bf16 v[124:127], a[184:187], v[148:151], 0\n"
        "ds_read_b64_tr_b16 a[108:109], v255 offset:3104\n"
        "ds_read_b64_tr_b16 a[110:111], v255 offset:3872\n"
        "v_mfma_f32_16x16x32_bf16 v[88:91], a[104:107], a[224:227], v[88:91]\n"
        "buffer_load_dwordx4 v129, s[20:23], s60 offen offset:1024 lds\n"
        "ds_read_b64_tr_b16 a[132:133], v255 offset:3136\n"
        "ds_read_b64_tr_b16 a[134:135], v255 offset:3904\n"
        "v_mfma_f32_16x16x32_bf16 v[80:83], a[104:107], a[220:223], v[80:83]\n"
        "v_accvgpr_write_b32 a59, v118\n"
        "v_cvt_pk_bf16_f32 v120, v120, v121\n"
        "v_mfma_f32_16x16x32_bf16 v[68:71], a[104:107], a[216:219], v[68:71]\n"
        "v_accvgpr_write_b32 a12, v120\n"
        "v_cvt_pk_bf16_f32 v122, v122, v123\n"
        "v_mfma_f32_16x16x32_bf16 v[64:67], a[104:107], a[248:251], v[64:67]\n"
        "buffer_load_dwordx4 v130, s[20:23], s60 offen offset:2048 lds\n"
        "v_mfma_f32_16x16x32_bf16 v[160:163], a[56:59], a[248:251], v[160:163]\n"
        "s_add_u32 m0, s65, 14976\n"
        "v_accvgpr_write_b32 a13, v122\n"
        "v_cvt_pk_bf16_f32 v124, v124, v125\n"
        "v_mfma_f32_16x16x32_bf16 v[180:183], a[56:59], a[216:219], v[180:183]\n"
        "s_waitcnt lgkmcnt(2)\n"
        "v_lshl_add_u64 v[52:53], v[52:53], 2, s[38:39]\n"
        "v_lshl_add_u64 v[54:55], v[54:55], 2, s[38:39]\n"
        "v_mfma_f32_16x16x32_bf16 v[200:203], a[56:59], a[220:223], v[200:203]\n"
        "ds_read_b32 v52, v52\n"
        "ds_read_b32 v53, v53\n"
        "v_mfma_f32_16x16x32_bf16 v[220:223], a[56:59], a[224:227], v[220:223]\n"
        "ds_read_b32 v54, v54\n"
        "ds_read_b32 v55, v55\n"
        "v_mfma_f32_16x16x32_bf16 v[96:99], a[180:183], v[148:151], 0\n"
        "buffer_load_dwordx4 v128, s[20:23], s60 offen offset:384 lds\n"
        "v_mfma_f32_16x16x32_bf16 v[100:103], a[176:179], v[148:151], 0\n"
        "ds_read_b128 a[244:247], v240 offset:256\n"
        "v_accvgpr_write_b32 a14, v124\n"
        "v_mfma_f32_16x16x32_bf16 v[72:75], a[128:131], a[248:251], v[72:75]\n"
        "ds_read_b128 a[248:251], v240 offset:1792\n"
        "v_cvt_pk_bf16_f32 v126, v126, v127\n"
        "v_mfma_f32_16x16x32_bf16 v[76:79], a[128:131], a[216:219], v[76:79]\n"
        "buffer_load_dwordx4 v129, s[20:23], s60 offen offset:1408 lds\n"
        "v_mfma_f32_16x16x32_bf16 v[84:87], a[128:131], a[220:223], v[84:87]\n"
        "ds_read_b128 a[216:219], v240 offset:12544\n"
        "ds_read_b128 a[220:223], v240 offset:14080\n"
        "v_mfma_f32_16x16x32_bf16 v[92:95], a[128:131], a[224:227], v[92:95]\n"
        "v_accvgpr_write_b32 a15, v126\n"
        "v_cvt_pk_bf16_f32 v96, v96, v97\n"
        "s_nop 0\n"
        "v_mfma_f32_16x16x32_bf16 v[156:159], a[12:15], a[228:231], v[156:159]\n"
        "v_accvgpr_write_b32 a36, v96\n"
        "v_cvt_pk_bf16_f32 v98, v98, v99\n"
        "v_mfma_f32_16x16x32_bf16 v[176:179], a[12:15], a[232:235], v[176:179]\n"
        "buffer_load_dwordx4 v130, s[20:23], s60 offen offset:2432 lds\n"
        "v_mfma_f32_16x16x32_bf16 v[196:199], a[12:15], a[236:239], v[196:199]\n"
        "v_accvgpr_write_b32 a37, v98\n"
        "v_cvt_pk_bf16_f32 v100, v100, v101\n"
        "v_mfma_f32_16x16x32_bf16 v[216:219], a[12:15], a[240:243], v[216:219]\n"
        "s_waitcnt vmcnt(9)\n"
        "ds_read_b128 a[212:215], v137 offset:6144\n"
        "ds_read_b128 a[208:211], v137 offset:6912\n"
        "v_mfma_f32_16x16x32_bf16 v[104:107], a[172:175], v[148:151], 0\n"
        "s_mov_b32 m0, s48\n"
        "ds_read_b128 a[204:207], v137 offset:6176\n"
        "ds_read_b128 a[200:203], v137 offset:6944\n"
        "v_mfma_f32_16x16x32_bf16 v[108:111], a[168:171], v[148:151], 0\n"
        "s_sub_u32 s64, s36, 4194304\n"
        "ds_read_b128 a[196:199], v137 offset:6208\n"
        "ds_read_b128 a[192:195], v137 offset:6976\n"
        "v_mfma_f32_16x16x32_bf16 v[152:155], a[84:87], a[228:231], v[152:155]\n"
        "buffer_load_dword v138, s[20:23], 0 offen offset:768 lds\n"
        "v_mfma_f32_16x16x32_bf16 v[172:175], a[84:87], a[232:235], v[172:175]\n"
        "v_accvgpr_write_b32 a38, v100\n"
        "v_cvt_pk_bf16_f32 v102, v102, v103\n"
        "v_mfma_f32_16x16x32_bf16 v[192:195], a[84:87], a[236:239], v[192:195]\n"
        "v_accvgpr_write_b32 a39, v102\n"
        "v_cvt_pk_bf16_f32 v104, v104, v105\n"
        "v_mfma_f32_16x16x32_bf16 v[212:215], a[84:87], a[240:243], v[212:215]\n"
        "buffer_load_dwordx4 v249, s[12:15], s64 offen lds\n"
        "v_mfma_f32_16x16x32_bf16 v[224:227], a[36:39], a[240:243], v[224:227]\n"
        "v_accvgpr_write_b32 a60, v104\n"
        "v_cvt_pk_bf16_f32 v106, v106, v107\n"
        "v_mfma_f32_16x16x32_bf16 v[204:207], a[36:39], a[236:239], v[204:207]\n"
        "ds_read_b128 v[56:59], v142 offset:9216\n"
        "v_accvgpr_write_b32 a61, v106\n"
        "v_mfma_f32_16x16x32_bf16 v[184:187], a[36:39], a[232:235], v[184:187]\n"
        "s_waitcnt vmcnt(8)\n"
        "ds_read_b64_tr_b16 a[88:89], v255 offset:9216\n"
        "ds_read_b64_tr_b16 a[90:91], v255 offset:9984\n"
        "v_mfma_f32_16x16x32_bf16 v[164:167], a[36:39], a[228:231], v[164:167]\n"
        "s_waitcnt lgkmcnt(3)\n"
        "ds_read_b64_tr_b16 a[112:113], v255 offset:9248\n"
        "ds_read_b64_tr_b16 a[114:115], v255 offset:10016\n"
        "v_mfma_f32_16x16x32_bf16 v[112:115], a[212:215], v[148:151], 0\n"
        "buffer_load_dwordx4 v253, s[12:15], s64 offen offset:1024 lds\n"
        "ds_read_b64_tr_b16 a[136:137], v255 offset:9280\n"
        "v_mfma_f32_16x16x32_bf16 v[116:119], a[208:211], v[148:151], 0\n"
        "ds_read_b64_tr_b16 a[138:139], v255 offset:10048\n"
        "s_add_u32 m0, s65, 0\n"
        "v_cvt_pk_bf16_f32 v108, v108, v109\n"
        "v_mfma_f32_16x16x32_bf16 v[88:91], a[108:111], a[240:243], v[88:91]\n"
        "s_mov_b32 s64, 768\n"
        "v_accvgpr_write_b32 a62, v108\n"
        "v_cvt_pk_bf16_f32 v110, v110, v111\n"
        "v_mfma_f32_16x16x32_bf16 v[80:83], a[108:111], a[236:239], v[80:83]\n"
        "buffer_load_dwordx4 v128, s[20:23], s64 offen lds\n"
        "v_mfma_f32_16x16x32_bf16 v[68:71], a[108:111], a[232:235], v[68:71]\n"
        "v_accvgpr_write_b32 a63, v110\n"
        "v_cvt_pk_bf16_f32 v112, v112, v113\n"
        "v_mfma_f32_16x16x32_bf16 v[64:67], a[108:111], a[228:231], v[64:67]\n"
        "v_accvgpr_write_b32 a16, v112\n"
        "v_cvt_pk_bf16_f32 v114, v114, v115\n"
        "v_mfma_f32_16x16x32_bf16 v[160:163], a[60:63], a[228:231], v[160:163]\n"
        "v_accvgpr_write_b32 a17, v114\n"
        "v_cvt_pk_bf16_f32 v116, v116, v117\n"
        "v_mfma_f32_16x16x32_bf16 v[180:183], a[60:63], a[232:235], v[180:183]\n"
        "buffer_load_dwordx4 v129, s[20:23], s64 offen offset:1024 lds\n"
        "v_mfma_f32_16x16x32_bf16 v[200:203], a[60:63], a[236:239], v[200:203]\n"
        "ds_read_b128 a[224:227], v241 offset:256\n"
        "s_waitcnt lgkmcnt(1)\n"
        "v_lshl_add_u64 v[56:57], v[56:57], 2, s[38:39]\n"
        "v_mfma_f32_16x16x32_bf16 v[220:223], a[60:63], a[240:243], v[220:223]\n"
        "ds_read_b32 v56, v56\n"
        "ds_read_b32 v57, v57\n"
        "v_mfma_f32_16x16x32_bf16 v[120:123], a[204:207], v[148:151], 0\n"
        "v_lshl_add_u64 v[58:59], v[58:59], 2, s[38:39]\n"
        "v_accvgpr_write_b32 a18, v116\n"
        "v_mfma_f32_16x16x32_bf16 v[124:127], a[200:203], v[148:151], 0\n"
        "buffer_load_dwordx4 v130, s[20:23], s64 offen offset:2048 lds\n"
        "v_mfma_f32_16x16x32_bf16 v[72:75], a[132:135], a[228:231], v[72:75]\n"
        "s_add_u32 m0, s65, 2304\n"
        "ds_read_b32 v58, v58\n"
        "ds_read_b32 v59, v59\n"
        "v_mfma_f32_16x16x32_bf16 v[76:79], a[132:135], a[232:235], v[76:79]\n"
        "ds_read_b128 a[228:231], v241 offset:1792\n"
        "ds_read_b128 a[232:235], v241 offset:12544\n"
        "v_mfma_f32_16x16x32_bf16 v[84:87], a[132:135], a[236:239], v[84:87]\n"
        "ds_read_b128 a[236:239], v241 offset:14080\n"
        "v_cvt_pk_bf16_f32 v118, v118, v119\n"
        "v_mfma_f32_16x16x32_bf16 v[92:95], a[132:135], a[240:243], v[92:95]\n"
        "buffer_load_dwordx4 v128, s[20:23], s56 offen offset:768 lds\n"
        "v_accvgpr_write_b32 a19, v118\n"
        "s_nop 1\n"
        "v_mfma_f32_16x16x32_bf16 v[156:159], a[16:19], a[244:247], v[156:159]\n"
        "v_cvt_pk_bf16_f32 v120, v120, v121\n"
        "v_accvgpr_write_b32 a40, v120\n"
        "v_mfma_f32_16x16x32_bf16 v[176:179], a[16:19], a[248:251], v[176:179]\n"
        "v_cvt_pk_bf16_f32 v122, v122, v123\n"
        "v_accvgpr_write_b32 a41, v122\n"
        "v_mfma_f32_16x16x32_bf16 v[196:199], a[16:19], a[216:219], v[196:199]\n"
        "v_cvt_pk_bf16_f32 v124, v124, v125\n"
        "v_accvgpr_write_b32 a42, v124\n"
        "v_mfma_f32_16x16x32_bf16 v[216:219], a[16:19], a[220:223], v[216:219]\n"
        "buffer_load_dwordx4 v129, s[20:23], s56 offen offset:1792 lds\n"
        "v_mfma_f32_16x16x32_bf16 v[96:99], a[196:199], v[148:151], 0\n"
        "v_cvt_pk_bf16_f32 v126, v126, v127\n"
        "v_accvgpr_write_b32 a43, v126\n"
        "v_mfma_f32_16x16x32_bf16 v[100:103], a[192:195], v[148:151], 0\n"
        "s_waitcnt vmcnt(11)\n"
        "ds_read_b128 a[188:191], v137 offset:12288\n"
        "ds_read_b128 a[184:187], v137 offset:13056\n"
        "v_mfma_f32_16x16x32_bf16 v[152:155], a[88:91], a[244:247], v[152:155]\n"
        "ds_read_b128 a[180:183], v137 offset:12320\n"
        "ds_read_b128 a[176:179], v137 offset:13088\n"
        "v_mfma_f32_16x16x32_bf16 v[172:175], a[88:91], a[248:251], v[172:175]\n"
        "buffer_load_dwordx4 v130, s[20:23], s56 offen offset:2816 lds\n"
        "v_mfma_f32_16x16x32_bf16 v[192:195], a[88:91], a[216:219], v[192:195]\n"
        "s_add_u32 m0, s65, 5376\n"
        "ds_read_b128 a[172:175], v137 offset:12352\n"
        "ds_read_b128 a[168:171], v137 offset:13120\n"
        "v_mfma_f32_16x16x32_bf16 v[212:215], a[88:91], a[220:223], v[212:215]\n"
        "v_cvt_pk_bf16_f32 v96, v96, v97\n"
        "v_accvgpr_write_b32 a64, v96\n"
        "v_mfma_f32_16x16x32_bf16 v[224:227], a[40:43], a[220:223], v[224:227]\n"
        "v_cvt_pk_bf16_f32 v98, v98, v99\n"
        "v_accvgpr_write_b32 a65, v98\n"
        "v_mfma_f32_16x16x32_bf16 v[204:207], a[40:43], a[216:219], v[204:207]\n"
        "buffer_load_dwordx4 v128, s[20:23], s57 offen offset:768 lds\n"
        "v_mfma_f32_16x16x32_bf16 v[184:187], a[40:43], a[248:251], v[184:187]\n"
        "v_cvt_pk_bf16_f32 v100, v100, v101\n"
        "v_accvgpr_write_b32 a66, v100\n"
        "v_mfma_f32_16x16x32_bf16 v[164:167], a[40:43], a[244:247], v[164:167]\n"
        "s_waitcnt lgkmcnt(2)\n"
        "ds_read_b128 v[60:63], v143 offset:9216\n"
        "v_cvt_pk_bf16_f32 v102, v102, v103\n"
        "v_mfma_f32_16x16x32_bf16 v[104:107], a[188:191], v[148:151], 0\n"
        "v_accvgpr_write_b32 a67, v102\n"
        "v_mfma_f32_16x16x32_bf16 v[108:111], a[184:187], v[148:151], 0\n"
        "buffer_load_dwordx4 v129, s[20:23], s57 offen offset:1792 lds\n"
        "v_mfma_f32_16x16x32_bf16 v[88:91], a[112:115], a[220:223], v[88:91]\n"
        "v_mfma_f32_16x16x32_bf16 v[80:83], a[112:115], a[216:219], v[80:83]\n"
        "v_mfma_f32_16x16x32_bf16 v[68:71], a[112:115], a[248:251], v[68:71]\n"
        "v_mfma_f32_16x16x32_bf16 v[64:67], a[112:115], a[244:247], v[64:67]\n"
        "s_waitcnt vmcnt(11)\n"
        "ds_read_b64_tr_b16 a[92:93], v255 offset:15360\n"
        "ds_read_b64_tr_b16 a[94:95], v255 offset:16128\n"
        "v_mfma_f32_16x16x32_bf16 v[160:163], a[64:67], a[244:247], v[160:163]\n"
        "ds_read_b64_tr_b16 a[116:117], v255 offset:15392\n"
        "ds_read_b64_tr_b16 a[118:119], v255 offset:16160\n"
        "v_mfma_f32_16x16x32_bf16 v[180:183], a[64:67], a[248:251], v[180:183]\n"
        "ds_read_b64_tr_b16 a[140:141], v255 offset:15424\n"
        "ds_read_b64_tr_b16 a[142:143], v255 offset:16192\n"
        "v_mfma_f32_16x16x32_bf16 v[200:203], a[64:67], a[216:219], v[200:203]\n"
        "v_cvt_pk_bf16_f32 v104, v104, v105\n"
        "v_accvgpr_write_b32 a20, v104\n"
        "v_mfma_f32_16x16x32_bf16 v[220:223], a[64:67], a[220:223], v[220:223]\n"
        "v_cvt_pk_bf16_f32 v106, v106, v107\n"
        "v_accvgpr_write_b32 a21, v106\n"
        "v_mfma_f32_16x16x32_bf16 v[112:115], a[180:183], v[148:151], 0\n"
        "s_waitcnt lgkmcnt(6)\n"
        "v_lshl_add_u64 v[60:61], v[60:61], 2, s[38:39]\n"
        "v_lshl_add_u64 v[62:63], v[62:63], 2, s[38:39]\n"
        "v_mfma_f32_16x16x32_bf16 v[116:119], a[176:179], v[148:151], 0\n"
        "ds_read_b32 v60, v60\n"
        "ds_read_b32 v61, v61\n"
        "v_mfma_f32_16x16x32_bf16 v[72:75], a[136:139], a[244:247], v[72:75]\n"
        "ds_read_b32 v62, v62\n"
        "ds_read_b32 v63, v63\n"
        "v_mfma_f32_16x16x32_bf16 v[76:79], a[136:139], a[248:251], v[76:79]\n"
        "v_cvt_pk_bf16_f32 v108, v108, v109\n"
        "v_accvgpr_write_b32 a22, v108\n"
        "v_cvt_pk_bf16_f32 v110, v110, v111\n"
        "v_accvgpr_write_b32 a23, v110\n"
        "s_nop 1\n"
        "v_mfma_f32_16x16x32_bf16 v[156:159], a[20:23], a[224:227], v[156:159]\n"
        "v_cvt_pk_bf16_f32 v112, v112, v113\n"
        "v_accvgpr_write_b32 a44, v112\n"
        "v_mfma_f32_16x16x32_bf16 v[176:179], a[20:23], a[228:231], v[176:179]\n"
        "v_cvt_pk_bf16_f32 v114, v114, v115\n"
        "v_accvgpr_write_b32 a45, v114\n"
        "v_mfma_f32_16x16x32_bf16 v[196:199], a[20:23], a[232:235], v[196:199]\n"
        "v_cvt_pk_bf16_f32 v116, v116, v117\n"
        "v_accvgpr_write_b32 a46, v116\n"
        "v_cvt_pk_bf16_f32 v156, v156, v157\n"
        "v_mfma_f32_16x16x32_bf16 v[216:219], a[20:23], a[236:239], v[216:219]\n"
        "v_cvt_pk_bf16_f32 v157, v158, v159\n"
        "v_cvt_pk_bf16_f32 v176, v176, v177\n"
        "v_cvt_pk_bf16_f32 v177, v178, v179\n"
        "v_mfma_f32_16x16x32_bf16 v[120:123], a[172:175], v[148:151], 0\n"
        "v_cvt_pk_bf16_f32 v196, v196, v197\n"
        "v_cvt_pk_bf16_f32 v197, v198, v199\n"
        "v_mfma_f32_16x16x32_bf16 v[124:127], a[168:171], v[148:151], 0\n"
        "v_cvt_pk_bf16_f32 v118, v118, v119\n"
        "v_cvt_pk_bf16_f32 v216, v216, v217\n"
        "v_cvt_pk_bf16_f32 v217, v218, v219\n"
        "v_mfma_f32_16x16x32_bf16 v[84:87], a[136:139], a[216:219], v[84:87]\n"
        "v_accvgpr_write_b32 a47, v118\n"
        "v_cvt_pk_bf16_f32 v120, v120, v121\n"
        "v_mfma_f32_16x16x32_bf16 v[92:95], a[136:139], a[220:223], v[92:95]\n"
        "v_accvgpr_write_b32 a68, v120\n"
        "v_cvt_pk_bf16_f32 v122, v122, v123\n"
        "v_mfma_f32_16x16x32_bf16 v[224:227], a[44:47], a[236:239], v[224:227]\n"
        "v_accvgpr_write_b32 a69, v122\n"
        "v_cvt_pk_bf16_f32 v124, v124, v125\n"
        "v_mfma_f32_16x16x32_bf16 v[204:207], a[44:47], a[232:235], v[204:207]\n"
        "v_accvgpr_write_b32 a70, v124\n"
        "v_cvt_pk_bf16_f32 v126, v126, v127\n"
        "v_mfma_f32_16x16x32_bf16 v[184:187], a[44:47], a[228:231], v[184:187]\n"
        "ds_read_b128 a[240:243], v240 offset:0\n"
        "v_accvgpr_write_b32 a71, v126\n"
        "v_cvt_pk_bf16_f32 v218, v224, v225\n"
        "v_mfma_f32_16x16x32_bf16 v[164:167], a[44:47], a[224:227], v[164:167]\n"
        "s_waitcnt lgkmcnt(1)\n"
        "ds_read_b128 a[244:247], v241 offset:0\n"
        "v_cvt_pk_bf16_f32 v219, v226, v227\n"
        "v_cvt_pk_bf16_f32 v198, v204, v205\n"
        "v_mfma_f32_16x16x32_bf16 v[152:155], a[92:95], a[224:227], v[152:155]\n"
        "ds_read_b128 a[248:251], v240 offset:128\n"
        "v_cvt_pk_bf16_f32 v199, v206, v207\n"
        "v_cvt_pk_bf16_f32 v178, v184, v185\n"
        "v_mfma_f32_16x16x32_bf16 v[64:67], a[116:119], a[224:227], v[64:67]\n"
        "buffer_load_dwordx4 v130, s[20:23], s57 offen offset:2816 lds\n"
        "v_cvt_pk_bf16_f32 v179, v186, v187\n"
        "v_mfma_f32_16x16x32_bf16 v[72:75], a[140:143], a[224:227], v[72:75]\n"
        "s_add_u32 m0, s65, 8448\n"
        "v_cvt_pk_bf16_f32 v158, v164, v165\n"
        "v_cvt_pk_bf16_f32 v159, v166, v167\n"
        "v_mfma_f32_16x16x32_bf16 v[172:175], a[92:95], a[228:231], v[172:175]\n"
        "v_cvt_pk_bf16_f32 v152, v152, v153\n"
        "v_cvt_pk_bf16_f32 v153, v154, v155\n"
        "v_mfma_f32_16x16x32_bf16 v[160:163], a[68:71], a[224:227], v[160:163]\n"
        "v_cvt_pk_bf16_f32 v154, v64, v65\n"
        "v_cvt_pk_bf16_f32 v155, v66, v67\n"
        "v_mfma_f32_16x16x32_bf16 v[180:183], a[68:71], a[228:231], v[180:183]\n"
        "buffer_load_dwordx4 v128, s[20:23], s58 offen offset:768 lds\n"
        "v_mfma_f32_16x16x32_bf16 v[200:203], a[68:71], a[232:235], v[200:203]\n"
        "v_cvt_pk_bf16_f32 v168, v72, v73\n"
        "v_cvt_pk_bf16_f32 v169, v74, v75\n"
        "v_mfma_f32_16x16x32_bf16 v[220:223], a[68:71], a[236:239], v[220:223]\n"
        "v_cvt_pk_bf16_f32 v166, v160, v161\n"
        "v_cvt_pk_bf16_f32 v160, v160, v161\n"
        "v_cvt_pk_bf16_f32 v161, v162, v163\n"
        "v_mfma_f32_16x16x32_bf16 v[68:71], a[116:119], a[228:231], v[68:71]\n"
        "buffer_load_dwordx4 v129, s[20:23], s58 offen offset:1792 lds\n"
        "v_cvt_pk_bf16_f32 v167, v162, v163\n"
        "v_mfma_f32_16x16x32_bf16 v[76:79], a[140:143], a[228:231], v[76:79]\n"
        "s_waitcnt vmcnt(11)\n"
        "s_barrier\n"
        "ds_read_b128 v[162:165], v251 offset:0\n"
        "ds_read_b128 a[216:219], v240 offset:1536\n"
        "v_cvt_pk_bf16_f32 v186, v180, v181\n"
        "v_mfma_f32_16x16x32_bf16 v[192:195], a[92:95], a[232:235], v[192:195]\n"
        "ds_read_b128 a[220:223], v241 offset:1536\n"
        "v_cvt_pk_bf16_f32 v180, v180, v181\n"
        "v_cvt_pk_bf16_f32 v181, v182, v183\n"
        "v_mfma_f32_16x16x32_bf16 v[80:83], a[116:119], a[232:235], v[80:83]\n"
        "buffer_load_dwordx4 v130, s[20:23], s58 offen offset:2816 lds\n"
        "v_cvt_pk_bf16_f32 v187, v182, v183\n"
        "v_mfma_f32_16x16x32_bf16 v[84:87], a[140:143], a[232:235], v[84:87]\n"
        "ds_read_b128 v[182:185], v251 offset:512\n"
        "s_add_u32 m0, s65, 11520\n"
        "ds_read_b128 a[224:227], v240 offset:1664\n"
        "v_cvt_pk_bf16_f32 v206, v200, v201\n"
        "v_mfma_f32_16x16x32_bf16 v[212:215], a[92:95], a[236:239], v[212:215]\n"
        "v_cvt_pk_bf16_f32 v200, v200, v201\n"
        "v_cvt_pk_bf16_f32 v201, v202, v203\n"
        "v_cvt_pk_bf16_f32 v207, v202, v203\n"
        "v_cvt_pk_bf16_f32 v226, v220, v221\n"
        "v_cvt_pk_bf16_f32 v220, v220, v221\n"
        "v_cvt_pk_bf16_f32 v221, v222, v223\n"
        "v_mfma_f32_16x16x32_bf16 v[88:91], a[116:119], a[236:239], v[88:91]\n"
        "buffer_load_dwordx4 v128, s[20:23], s59 offen offset:768 lds\n"
        "v_cvt_pk_bf16_f32 v227, v222, v223\n"
        "v_cvt_pk_bf16_f32 v172, v172, v173\n"
        "v_cvt_pk_bf16_f32 v173, v174, v175\n"
        "v_mfma_f32_16x16x32_bf16 v[92:95], a[140:143], a[236:239], v[92:95]\n"
        "s_waitcnt vmcnt(10)\n"
        "ds_read_b64_tr_b16 a[144:145], v255 offset:0\n"
        "ds_read_b64_tr_b16 a[146:147], v255 offset:768\n"
        "v_cvt_pk_bf16_f32 v174, v68, v69\n"
        "v_cvt_pk_bf16_f32 v175, v70, v71\n"
        "v_mfma_f32_16x16x32_bf16 v[64:67], v[152:155], v[156:159], v[0:3]\n"
        "s_waitcnt lgkmcnt(4)\n"
        "ds_read_b64_tr_b16 a[168:169], v255 offset:32\n"
        "ds_read_b64_tr_b16 a[170:171], v255 offset:800\n"
        "v_cvt_pk_bf16_f32 v190, v76, v77\n"
        "v_cvt_pk_bf16_f32 v191, v78, v79\n"
        "v_mfma_f32_16x16x32_bf16 v[64:67], v[168:171], v[160:163], v[64:67]\n"
        "ds_read_b64_tr_b16 a[192:193], v255 offset:64\n"
        "ds_read_b64_tr_b16 a[194:195], v255 offset:832\n"
        "v_cvt_pk_bf16_f32 v192, v192, v193\n"
        "v_cvt_pk_bf16_f32 v193, v194, v195\n"
        "v_mfma_f32_16x16x32_bf16 v[96:99], v[172:175], v[156:159], v[4:7]\n"
        "buffer_load_dwordx4 v129, s[20:23], s59 offen offset:1792 lds\n"
        "v_cvt_pk_bf16_f32 v194, v80, v81\n"
        "v_cvt_pk_bf16_f32 v195, v82, v83\n"
        "v_exp_f32 v64, v64\n"
        "v_mfma_f32_16x16x32_bf16 v[96:99], v[188:191], v[164:167], v[96:99]\n"
        "ds_read_b128 v[162:165], v252 offset:0\n"
        "ds_read_b128 v[202:205], v251 offset:4096\n"
        "v_exp_f32 v65, v65\n"
        "v_exp_f32 v66, v66\n"
        "v_mfma_f32_16x16x32_bf16 v[72:75], v[152:155], v[176:179], v[16:19]\n"
        "s_waitcnt lgkmcnt(8)\n"
        "buffer_load_dwordx4 v130, s[20:23], s59 offen offset:2816 lds\n"
        "v_cvt_pk_bf16_f32 v64, v64, v65\n"
        "v_cvt_pk_bf16_f32 v208, v84, v85\n"
        "v_exp_f32 v96, v96\n"
        "v_mfma_f32_16x16x32_bf16 v[72:75], v[168:171], v[180:183], v[72:75]\n"
        "s_add_u32 m0, s65, 14592\n"
        "s_waitcnt vmcnt(9)\n"
        "ds_read_b64_tr_b16 a[148:149], v255 offset:3072\n"
        "ds_read_b64_tr_b16 a[150:151], v255 offset:3840\n"
        "v_cvt_pk_bf16_f32 v209, v86, v87\n"
        "v_exp_f32 v67, v67\n"
        "v_cvt_pk_bf16_f32 v212, v212, v213\n"
        "v_mfma_f32_16x16x32_bf16 v[100:103], v[172:175], v[176:179], v[20:23]\n"
        "buffer_load_dwordx4 v128, s[20:23], s60 offen offset:768 lds\n"
        "ds_read_b64_tr_b16 a[172:173], v255 offset:3104\n"
        "ds_read_b64_tr_b16 a[174:175], v255 offset:3872\n"
        "v_exp_f32 v97, v97\n"
        "v_mfma_f32_16x16x32_bf16 v[100:103], v[188:191], v[184:187], v[100:103]\n"
        "ds_read_b128 v[182:185], v252 offset:512\n"
        "ds_read_b64_tr_b16 a[196:197], v255 offset:3136\n"
        "ds_read_b64_tr_b16 a[198:199], v255 offset:3904\n"
        "v_cvt_pk_bf16_f32 v213, v214, v215\n"
        "v_cvt_pk_bf16_f32 v65, v66, v67\n"
        "v_cvt_pk_bf16_f32 v66, v96, v97\n"
        "v_mfma_f32_16x16x32_bf16 v[68:71], v[192:195], v[156:159], v[8:11]\n"
        "s_waitcnt lgkmcnt(8)\n"
        "ds_read_b128 a[228:231], v240 offset:12288\n"
        "ds_read_b128 a[232:235], v241 offset:12288\n"
        "v_exp_f32 v98, v98\n"
        "v_exp_f32 v72, v72\n"
        "v_mfma_f32_16x16x32_bf16 v[68:71], v[208:211], v[160:163], v[68:71]\n"
        "buffer_load_dwordx4 v129, s[20:23], s60 offen offset:1792 lds\n"
        "v_cvt_pk_bf16_f32 v214, v88, v89\n"
        "v_cvt_pk_bf16_f32 v215, v90, v91\n"
        "v_exp_f32 v99, v99\n"
        "v_mfma_f32_16x16x32_bf16 v[76:79], v[192:195], v[176:179], v[24:27]\n"
        "s_waitcnt lgkmcnt(4)\n"
        "s_waitcnt vmcnt(8)\n"
        "ds_read_b64_tr_b16 a[152:153], v255 offset:6144\n"
        "ds_read_b64_tr_b16 a[154:155], v255 offset:6912\n"
        "v_exp_f32 v73, v73\n"
        "v_cvt_pk_bf16_f32 v230, v92, v93\n"
        "v_cvt_pk_bf16_f32 v231, v94, v95\n"
        "v_mfma_f32_16x16x32_bf16 v[76:79], v[208:211], v[180:183], v[76:79]\n"
        "buffer_load_dwordx4 v130, s[20:23], s60 offen offset:2816 lds\n"
        "ds_read_b64_tr_b16 a[176:177], v255 offset:6176\n"
        "ds_read_b64_tr_b16 a[178:179], v255 offset:6944\n"
        "v_cvt_pk_bf16_f32 v67, v98, v99\n"
        "v_cvt_pk_bf16_f32 v72, v72, v73\n"
        "v_mfma_f32_16x16x32_bf16 v[104:107], v[212:215], v[156:159], v[12:15]\n"
        "s_add_u32 m0, s40, 98304\n"
        "ds_read_b64_tr_b16 a[200:201], v255 offset:6208\n"
        "ds_read_b64_tr_b16 a[202:203], v255 offset:6976\n"
        "v_mfma_f32_16x16x32_bf16 v[104:107], v[228:231], v[164:167], v[104:107]\n"
        "s_sub_u32 s64, s47, 6291456\n"
        "ds_read_b128 a[236:239], v240 offset:12416\n"
        "v_exp_f32 v74, v74\n"
        "v_mfma_f32_16x16x32_bf16 v[108:111], v[212:215], v[176:179], v[28:31]\n"
        "buffer_load_dwordx4 v242, s[8:11], s64 offen lds\n"
        "v_mfma_f32_16x16x32_bf16 v[108:111], v[228:231], v[184:187], v[108:111]\n"
        "s_add_u32 m0, s40, 99328\n"
        "v_exp_f32 v100, v100\n"
        "v_mfma_f32_16x16x32_bf16 v[80:83], v[152:155], v[196:199], v[32:35]\n"
        "v_exp_f32 v75, v75\n"
        "v_mfma_f32_16x16x32_bf16 v[80:83], v[168:171], v[200:203], v[80:83]\n"
        "buffer_load_dwordx4 v243, s[8:11], s64 offen lds\n"
        "v_cvt_pk_bf16_f32 v73, v74, v75\n"
        "v_mfma_f32_16x16x32_bf16 v[112:115], a[240:243], a[144:147], 0\n"
        "s_add_u32 m0, s40, 100352\n"
        "v_exp_f32 v101, v101\n"
        "v_mfma_f32_16x16x32_bf16 v[112:115], a[244:247], a[148:151], v[112:115]\n"
        "s_waitcnt lgkmcnt(0)\n"
        "v_exp_f32 v102, v102\n"
        "v_cvt_pk_bf16_f32 v74, v100, v101\n"
        "v_mfma_f32_16x16x32_bf16 v[112:115], a[248:251], a[152:155], v[112:115]\n"
        "buffer_load_dwordx4 v244, s[8:11], s64 offen lds\n"
        "v_mfma_f32_16x16x32_bf16 v[116:119], a[248:251], a[176:179], 0\n"
        "s_add_u32 m0, s40, 110592\n"
        "v_exp_f32 v68, v68\n"
        "v_mfma_f32_16x16x32_bf16 v[116:119], a[244:247], a[172:175], v[116:119]\n"
        "s_sub_u32 s64, s47, 5505024\n"
        "v_exp_f32 v103, v103\n"
        "v_mfma_f32_16x16x32_bf16 v[116:119], a[240:243], a[168:171], v[116:119]\n"
        ".long 0xbec200ff\n"
        "42:\n"
        "s_branch 60b\n"
        "v_exp_f32 v69, v69\n"
        "v_cvt_pk_bf16_f32 v75, v102, v103\n"
        "v_mfma_f32_16x16x32_bf16 v[96:99], v[172:175], v[196:199], v[36:39]\n"
        "buffer_load_dwordx4 v242, s[8:11], s64 offen lds\n"
        "s_waitcnt vmcnt(10)\n"
        "ds_read_b64_tr_b16 a[156:157], v255 offset:9216\n"
        "v_mfma_f32_16x16x32_bf16 v[96:99], v[188:191], v[204:207], v[96:99]\n"
        "s_add_u32 m0, s40, 111616\n"
        "ds_read_b64_tr_b16 a[158:159], v255 offset:9984\n"
        "ds_read_b64_tr_b16 a[180:181], v255 offset:9248\n"
        "ds_read_b64_tr_b16 a[182:183], v255 offset:10016\n"
        "v_mfma_f32_16x16x32_bf16 v[120:123], a[240:243], a[192:195], 0\n"
        "ds_read_b128 v[202:205], v252 offset:4096\n"
        "ds_read_b64_tr_b16 a[204:205], v255 offset:9280\n"
        "ds_read_b64_tr_b16 a[206:207], v255 offset:10048\n"
        "v_mfma_f32_16x16x32_bf16 v[120:123], a[244:247], a[196:199], v[120:123]\n"
        "buffer_load_dwordx4 v243, s[8:11], s64 offen lds\n"
        "v_cvt_pk_bf16_f32 v68, v68, v69\n"
        "v_mfma_f32_16x16x32_bf16 v[120:123], a[248:251], a[200:203], v[120:123]\n"
        "s_add_u32 m0, s40, 112640\n"
        "ds_read_b128 a[240:243], v240 offset:13824\n"
        "ds_read_b128 a[244:247], v241 offset:13824\n"
        "v_mfma_f32_16x16x32_bf16 v[124:127], a[216:219], a[144:147], 0\n"
        "buffer_load_dwordx4 v244, s[8:11], s64 offen lds\n"
        "v_mfma_f32_16x16x32_bf16 v[124:127], a[220:223], a[148:151], v[124:127]\n"
        "s_xor_b32 m0, s48, 8192\n"
        "s_waitcnt vmcnt(9)\n"
        "ds_read_b64_tr_b16 a[160:161], v255 offset:12288\n"
        "ds_read_b64_tr_b16 a[162:163], v255 offset:13056\n"
        "v_mfma_f32_16x16x32_bf16 v[124:127], a[224:227], a[152:155], v[124:127]\n"
        "s_sub_u32 s64, s36, 2097152\n"
        "ds_read_b64_tr_b16 a[184:185], v255 offset:12320\n"
        "ds_read_b64_tr_b16 a[186:187], v255 offset:13088\n"
        "v_mfma_f32_16x16x32_bf16 v[156:159], a[224:227], a[176:179], 0\n"
        "buffer_load_dwordx4 v249, s[12:15], s64 offen lds\n"
        "ds_read_b64_tr_b16 a[208:209], v255 offset:12352\n"
        "v_mfma_f32_16x16x32_bf16 v[156:159], a[220:223], a[172:175], v[156:159]\n"
        "ds_read_b64_tr_b16 a[210:211], v255 offset:13120\n"
        "ds_read_b128 a[248:251], v240 offset:13952\n"
        "v_mfma_f32_16x16x32_bf16 v[156:159], a[216:219], a[168:171], v[156:159]\n"
        "ds_read_b128 v[222:225], v251 offset:4608\n"
        "v_exp_f32 v70, v70\n"
        "v_mfma_f32_16x16x32_bf16 v[84:87], v[192:195], v[196:199], v[40:43]\n"
        "s_waitcnt lgkmcnt(8)\n"
        "v_exp_f32 v76, v76\n"
        "v_mfma_f32_16x16x32_bf16 v[84:87], v[208:211], v[200:203], v[84:87]\n"
        "buffer_load_dwordx4 v253, s[12:15], s64 offen offset:1024 lds\n"
        "v_mfma_f32_16x16x32_bf16 v[160:163], a[216:219], a[192:195], 0\n"
        "s_add_u32 m0, s65, 12288\n"
        "ds_read_b128 a[216:219], v241 offset:128\n"
        "v_exp_f32 v71, v71\n"
        "v_mfma_f32_16x16x32_bf16 v[160:163], a[220:223], a[196:199], v[160:163]\n"
        "ds_read_b128 a[220:223], v240 offset:256\n"
        "v_exp_f32 v77, v77\n"
        "v_mfma_f32_16x16x32_bf16 v[160:163], a[224:227], a[200:203], v[160:163]\n"
        "ds_read_b128 a[224:227], v241 offset:256\n"
        "v_cvt_pk_bf16_f32 v69, v70, v71\n"
        "v_cvt_pk_bf16_f32 v76, v76, v77\n"
        "v_mfma_f32_16x16x32_bf16 v[164:167], a[228:231], a[144:147], 0\n"
        "s_waitcnt lgkmcnt(3)\n"
        "buffer_load_dword v136, s[52:55], 0 offen lds\n"
        "v_mfma_f32_16x16x32_bf16 v[164:167], a[232:235], a[148:151], v[164:167]\n"
        "v_exp_f32 v78, v78\n"
        "v_mfma_f32_16x16x32_bf16 v[164:167], a[236:239], a[152:155], v[164:167]\n"
        "v_exp_f32 v104, v104\n"
        "v_mfma_f32_16x16x32_bf16 v[100:103], v[212:215], v[196:199], v[44:47]\n"
        "v_exp_f32 v79, v79\n"
        "v_mfma_f32_16x16x32_bf16 v[100:103], v[228:231], v[204:207], v[100:103]\n"
        "v_exp_f32 v105, v105\n"
        "v_cvt_pk_bf16_f32 v77, v78, v79\n"
        "v_mfma_f32_16x16x32_bf16 v[176:179], a[236:239], a[176:179], 0\n"
        "v_exp_f32 v106, v106\n"
        "v_cvt_pk_bf16_f32 v70, v104, v105\n"
        "v_mfma_f32_16x16x32_bf16 v[176:179], a[232:235], a[172:175], v[176:179]\n"
        "v_exp_f32 v108, v108\n"
        "v_mfma_f32_16x16x32_bf16 v[176:179], a[228:231], a[168:171], v[176:179]\n"
        "s_waitcnt vmcnt(9)\n"
        "ds_read_b64_tr_b16 a[164:165], v255 offset:15360\n"
        "ds_read_b64_tr_b16 a[166:167], v255 offset:16128\n"
        "v_mfma_f32_16x16x32_bf16 v[180:183], a[228:231], a[192:195], 0\n"
        "ds_read_b64_tr_b16 a[188:189], v255 offset:15392\n"
        "ds_read_b64_tr_b16 a[190:191], v255 offset:16160\n"
        "v_mfma_f32_16x16x32_bf16 v[180:183], a[232:235], a[196:199], v[180:183]\n"
        "ds_read_b64_tr_b16 a[212:213], v255 offset:15424\n"
        "ds_read_b64_tr_b16 a[214:215], v255 offset:16192\n"
        "v_mfma_f32_16x16x32_bf16 v[180:183], a[236:239], a[200:203], v[180:183]\n"
        "ds_read_b128 a[228:231], v241 offset:1664\n"
        "ds_read_b128 a[232:235], v240 offset:1792\n"
        "v_mfma_f32_16x16x32_bf16 v[184:187], a[240:243], a[144:147], 0\n"
        "ds_read_b128 a[236:239], v241 offset:1792\n"
        "v_exp_f32 v107, v107\n"
        "v_mfma_f32_16x16x32_bf16 v[184:187], a[244:247], a[148:151], v[184:187]\n"
        "v_exp_f32 v109, v109\n"
        "v_cvt_pk_bf16_f32 v71, v106, v107\n"
        "v_mfma_f32_16x16x32_bf16 v[184:187], a[248:251], a[152:155], v[184:187]\n"
        "v_exp_f32 v110, v110\n"
        "v_cvt_pk_bf16_f32 v78, v108, v109\n"
        "v_mfma_f32_16x16x32_bf16 v[196:199], a[248:251], a[176:179], 0\n"
        "v_exp_f32 v111, v111\n"
        "v_mfma_f32_16x16x32_bf16 v[196:199], a[244:247], a[172:175], v[196:199]\n"
        "v_cvt_pk_bf16_f32 v79, v110, v111\n"
        "v_exp_f32 v80, v80\n"
        "v_mfma_f32_16x16x32_bf16 v[196:199], a[240:243], a[168:171], v[196:199]\n"
        "v_exp_f32 v81, v81\n"
        "v_mfma_f32_16x16x32_bf16 v[200:203], a[240:243], a[192:195], 0\n"
        "ds_read_b128 a[240:243], v241 offset:12416\n"
        "v_exp_f32 v82, v82\n"
        "v_mfma_f32_16x16x32_bf16 v[200:203], a[244:247], a[196:199], v[200:203]\n"
        "ds_read_b128 a[244:247], v240 offset:12544\n"
        "v_cvt_pk_bf16_f32 v80, v80, v81\n"
        "v_mfma_f32_16x16x32_bf16 v[200:203], a[248:251], a[200:203], v[200:203]\n"
        "ds_read_b128 a[248:251], v241 offset:12544\n"
        "v_exp_f32 v83, v83\n"
        "v_mfma_f32_16x16x32_bf16 v[88:91], v[152:155], v[216:219], v[48:51]\n"
        "v_cvt_pk_bf16_f32 v81, v82, v83\n"
        "v_exp_f32 v96, v96\n"
        "v_mfma_f32_16x16x32_bf16 v[88:91], v[168:171], v[220:223], v[88:91]\n"
        "s_waitcnt lgkmcnt(3)\n"
        "v_exp_f32 v97, v97\n"
        "v_mfma_f32_16x16x32_bf16 v[112:115], a[216:219], a[156:159], v[112:115]\n"
        "v_exp_f32 v98, v98\n"
        "v_cvt_pk_bf16_f32 v82, v96, v97\n"
        "v_mfma_f32_16x16x32_bf16 v[112:115], a[220:223], a[160:163], v[112:115]\n"
        "v_exp_f32 v99, v99\n"
        "v_mfma_f32_16x16x32_bf16 v[112:115], a[224:227], a[164:167], v[112:115]\n"
        "v_cvt_pk_bf16_f32 v83, v98, v99\n"
        "v_exp_f32 v84, v84\n"
        "v_mfma_f32_16x16x32_bf16 v[116:119], a[224:227], a[188:191], v[116:119]\n"
        "v_exp_f32 v85, v85\n"
        "v_mfma_f32_16x16x32_bf16 v[116:119], a[220:223], a[184:187], v[116:119]\n"
        "v_exp_f32 v86, v86\n"
        "v_cvt_pk_bf16_f32 v84, v84, v85\n"
        "v_mfma_f32_16x16x32_bf16 v[116:119], a[216:219], a[180:183], v[116:119]\n"
        "v_exp_f32 v87, v87\n"
        "v_mfma_f32_16x16x32_bf16 v[120:123], a[216:219], a[204:207], v[120:123]\n"
        "ds_read_b128 a[216:219], v241 offset:13952\n"
        "v_cvt_pk_bf16_f32 v85, v86, v87\n"
        "v_mfma_f32_16x16x32_bf16 v[120:123], a[220:223], a[208:211], v[120:123]\n"
        "ds_read_b128 a[220:223], v240 offset:14080\n"
        "v_exp_f32 v100, v100\n"
        "v_mfma_f32_16x16x32_bf16 v[120:123], a[224:227], a[212:215], v[120:123]\n"
        "ds_read_b128 a[224:227], v241 offset:14080\n"
        "v_exp_f32 v101, v101\n"
        "v_mfma_f32_16x16x32_bf16 v[124:127], a[228:231], a[156:159], v[124:127]\n"
        "v_exp_f32 v102, v102\n"
        "v_cvt_pk_bf16_f32 v86, v100, v101\n"
        "v_mfma_f32_16x16x32_bf16 v[124:127], a[232:235], a[160:163], v[124:127]\n"
        "v_exp_f32 v103, v103\n"
        "v_cvt_pk_bf16_f32 v128, v112, v113\n"
        "v_mfma_f32_16x16x32_bf16 v[124:127], a[236:239], a[164:167], v[124:127]\n"
        "v_cvt_pk_bf16_f32 v87, v102, v103\n"
        "v_cvt_pk_bf16_f32 v129, v114, v115\n"
        "v_mfma_f32_16x16x32_bf16 v[104:107], v[172:175], v[216:219], v[52:55]\n"
        "v_cvt_pk_bf16_f32 v136, v116, v117\n"
        "v_cvt_pk_bf16_f32 v137, v118, v119\n"
        "v_mfma_f32_16x16x32_bf16 v[104:107], v[188:191], v[224:227], v[104:107]\n"
        "ds_read_b128 v[222:225], v252 offset:4608\n"
        "v_cvt_pk_bf16_f32 v144, v120, v121\n"
        "v_mfma_f32_16x16x32_bf16 v[156:159], a[236:239], a[188:191], v[156:159]\n"
        "v_cvt_pk_bf16_f32 v145, v122, v123\n"
        "v_cvt_pk_bf16_f32 v130, v124, v125\n"
        "v_mfma_f32_16x16x32_bf16 v[156:159], a[232:235], a[184:187], v[156:159]\n"
        "v_cvt_pk_bf16_f32 v131, v126, v127\n"
        "v_mfma_f32_16x16x32_bf16 v[156:159], a[228:231], a[180:183], v[156:159]\n"
        "v_exp_f32 v88, v88\n"
        "v_mfma_f32_16x16x32_bf16 v[160:163], a[228:231], a[204:207], v[160:163]\n"
        "v_exp_f32 v89, v89\n"
        "v_mfma_f32_16x16x32_bf16 v[160:163], a[232:235], a[208:211], v[160:163]\n"
        "v_exp_f32 v90, v90\n"
        "v_mfma_f32_16x16x32_bf16 v[160:163], a[236:239], a[212:215], v[160:163]\n"
        "s_waitcnt lgkmcnt(0)\n"
        "v_exp_f32 v91, v91\n"
        "v_mfma_f32_16x16x32_bf16 v[164:167], a[240:243], a[156:159], v[164:167]\n"
        "v_cvt_pk_bf16_f32 v138, v156, v157\n"
        "v_cvt_pk_bf16_f32 v139, v158, v159\n"
        "v_mfma_f32_16x16x32_bf16 v[164:167], a[244:247], a[160:163], v[164:167]\n"
        "v_cvt_pk_bf16_f32 v88, v88, v89\n"
        "v_cvt_pk_bf16_f32 v89, v90, v91\n"
        "v_mfma_f32_16x16x32_bf16 v[164:167], a[248:251], a[164:167], v[164:167]\n"
        "v_exp_f32 v104, v104\n"
        "v_mfma_f32_16x16x32_bf16 v[92:95], v[192:195], v[216:219], v[56:59]\n"
        "v_cvt_pk_bf16_f32 v146, v160, v161\n"
        "v_cvt_pk_bf16_f32 v147, v162, v163\n"
        "v_mfma_f32_16x16x32_bf16 v[92:95], v[208:211], v[220:223], v[92:95]\n"
        "v_exp_f32 v105, v105\n"
        "v_mfma_f32_16x16x32_bf16 v[176:179], a[248:251], a[188:191], v[176:179]\n"
        "v_exp_f32 v106, v106\n"
        "v_mfma_f32_16x16x32_bf16 v[176:179], a[244:247], a[184:187], v[176:179]\n"
        "v_cvt_pk_bf16_f32 v132, v164, v165\n"
        "v_cvt_pk_bf16_f32 v133, v166, v167\n"
        "v_mfma_f32_16x16x32_bf16 v[176:179], a[240:243], a[180:183], v[176:179]\n"
        "v_exp_f32 v107, v107\n"
        "v_mfma_f32_16x16x32_bf16 v[180:183], a[240:243], a[204:207], v[180:183]\n"
        "v_exp_f32 v92, v92\n"
        "v_mfma_f32_16x16x32_bf16 v[180:183], a[244:247], a[208:211], v[180:183]\n"
        "v_exp_f32 v93, v93\n"
        "v_mfma_f32_16x16x32_bf16 v[180:183], a[248:251], a[212:215], v[180:183]\n"
        "v_mov_b64 v[96:97], 0\n"
        "v_cvt_pk_bf16_f32 v90, v104, v105\n"
        "v_mfma_f32_16x16x32_bf16 v[184:187], a[216:219], a[156:159], v[184:187]\n"
        "v_cvt_pk_bf16_f32 v140, v176, v177\n"
        "v_cvt_pk_bf16_f32 v141, v178, v179\n"
        "v_mfma_f32_16x16x32_bf16 v[184:187], a[220:223], a[160:163], v[184:187]\n"
        "v_exp_f32 v94, v94\n"
        "v_mfma_f32_16x16x32_bf16 v[184:187], a[224:227], a[164:167], v[184:187]\n"
        "v_exp_f32 v95, v95\n"
        "v_mfma_f32_16x16x32_bf16 v[108:111], v[212:215], v[216:219], v[60:63]\n"
        "v_cvt_pk_bf16_f32 v148, v180, v181\n"
        "v_cvt_pk_bf16_f32 v149, v182, v183\n"
        "v_mfma_f32_16x16x32_bf16 v[108:111], v[228:231], v[224:227], v[108:111]\n"
        "v_cvt_pk_bf16_f32 v91, v106, v107\n"
        "v_add_u32 v240, 24576, v240\n"
        "v_mfma_f32_16x16x32_bf16 v[196:199], a[224:227], a[188:191], v[196:199]\n"
        "v_cvt_pk_bf16_f32 v134, v184, v185\n"
        "v_cvt_pk_bf16_f32 v135, v186, v187\n"
        "v_mfma_f32_16x16x32_bf16 v[196:199], a[220:223], a[184:187], v[196:199]\n"
        "v_mov_b64 v[172:173], 0\n"
        "v_add_u32 v241, 24576, v241\n"
        "v_mfma_f32_16x16x32_bf16 v[196:199], a[216:219], a[180:183], v[196:199]\n"
        "v_exp_f32 v108, v108\n"
        "v_mfma_f32_16x16x32_bf16 v[200:203], a[216:219], a[204:207], v[200:203]\n"
        "v_exp_f32 v109, v109\n"
        "v_mfma_f32_16x16x32_bf16 v[200:203], a[220:223], a[208:211], v[200:203]\n"
        "v_exp_f32 v110, v110\n"
        "v_mfma_f32_16x16x32_bf16 v[200:203], a[224:227], a[212:215], v[200:203]\n"
        "v_exp_f32 v111, v111\n"
        "v_smfmac_f32_16x16x64_bf16 v[96:99], a[252:255], v[64:71], v237\n"
        "v_cvt_pk_bf16_f32 v142, v196, v197\n"
        "v_mfma_f32_16x16x32_bf16 v[100:103], v[132:135], v[68:71], 0\n"
        "v_cvt_pk_bf16_f32 v143, v198, v199\n"
        "v_cvt_pk_bf16_f32 v92, v92, v93\n"
        "v_mfma_f32_16x16x32_bf16 v[100:103], v[128:131], v[64:67], v[100:103]\n"
        "v_mov_b64 v[192:193], 0\n"
        "v_cvt_pk_bf16_f32 v150, v200, v201\n"
        "v_mfma_f32_16x16x32_bf16 v[104:107], v[136:139], v[64:67], 0\n"
        "v_cvt_pk_bf16_f32 v151, v202, v203\n"
        "v_cvt_pk_bf16_f32 v93, v94, v95\n"
        "v_mfma_f32_16x16x32_bf16 v[104:107], v[140:143], v[68:71], v[104:107]\n"
        "v_add_f32 v97, v96, v97\n"
        "v_cvt_pk_bf16_f32 v94, v108, v109\n"
        "v_cvt_pk_bf16_f32 v95, v110, v111\n"
        "v_mfma_f32_16x16x32_bf16 v[108:111], v[148:151], v[68:71], 0\n"
        "v_rcp_f32 v96, v97\n"
        "v_mfma_f32_16x16x32_bf16 v[108:111], v[144:147], v[64:67], v[108:111]\n"
        "v_mul_f32 v100, v96, v100\n"
        "v_mul_f32 v101, v96, v101\n"
        "v_smfmac_f32_16x16x64_bf16 v[172:175], a[252:255], v[72:79], v237\n"
        "s_waitcnt vmcnt(6)\n"
        "s_barrier\n"
        "ds_read_b128 a[236:239], v240 offset:0\n"
        "v_mfma_f32_16x16x32_bf16 v[176:179], v[132:135], v[76:79], 0\n"
        "ds_read_b128 a[240:243], v241 offset:0\n"
        "ds_read_b128 a[244:247], v240 offset:128\n"
        "v_mfma_f32_16x16x32_bf16 v[176:179], v[128:131], v[72:75], v[176:179]\n"
        "ds_read_b128 a[248:251], v241 offset:128\n"
        "ds_read_b128 a[216:219], v240 offset:256\n"
        "v_mfma_f32_16x16x32_bf16 v[180:183], v[136:139], v[72:75], 0\n"
        "ds_read_b128 a[220:223], v241 offset:256\n"
        "v_mul_f32 v102, v96, v102\n"
        "v_mfma_f32_16x16x32_bf16 v[180:183], v[140:143], v[76:79], v[180:183]\n"
        "v_mul_f32 v103, v96, v103\n"
        "v_cvt_pk_bf16_f32 v100, v100, v101\n"
        "v_mfma_f32_16x16x32_bf16 v[184:187], v[148:151], v[76:79], 0\n"
        "v_cvt_pk_bf16_f32 v101, v102, v103\n"
        "v_mul_f32 v104, v96, v104\n"
        "v_mfma_f32_16x16x32_bf16 v[184:187], v[144:147], v[72:75], v[184:187]\n"
        "ds_write2st64_b32 v245, v100, v101 offset0:0 offset1:4\n"
        "v_smfmac_f32_16x16x64_bf16 v[192:195], a[252:255], v[80:87], v237\n"
        "v_mul_f32 v105, v96, v105\n"
        "v_mfma_f32_16x16x32_bf16 v[196:199], v[132:135], v[84:87], 0\n"
        "v_mul_f32 v106, v96, v106\n"
        "v_mul_f32 v107, v96, v107\n"
        "v_mfma_f32_16x16x32_bf16 v[196:199], v[128:131], v[80:83], v[196:199]\n"
        "buffer_load_dwordx4 v232, s[52:55], s34 offen lds\n"
        "v_mfma_f32_16x16x32_bf16 v[200:203], v[136:139], v[80:83], 0\n"
        "v_cvt_pk_bf16_f32 v102, v104, v105\n"
        "v_cvt_pk_bf16_f32 v103, v106, v107\n"
        "v_mov_b64 v[124:125], 0\n"
        "v_mfma_f32_16x16x32_bf16 v[200:203], v[140:143], v[84:87], v[200:203]\n"
        "ds_write2st64_b32 v245, v102, v103 offset0:8 offset1:12\n"
        "v_mfma_f32_16x16x32_bf16 v[204:207], v[148:151], v[84:87], 0\n"
        "v_mul_f32 v108, v96, v108\n"
        "v_mul_f32 v109, v96, v109\n"
        "v_mul_f32 v110, v96, v110\n"
        "v_mfma_f32_16x16x32_bf16 v[204:207], v[144:147], v[80:83], v[204:207]\n"
        "s_waitcnt lgkmcnt(2)\n"
        "v_mul_f32 v111, v96, v111\n"
        "v_cvt_pk_bf16_f32 v108, v108, v109\n"
        "v_cvt_pk_bf16_f32 v109, v110, v111\n"
        "v_mfma_f32_16x16x32_bf16 v[112:115], a[72:75], a[236:239], 0\n"
        "v_max_u32 v236, v96, v97\n"
        "v_add_f32 v173, v172, v173\n"
        "v_mfma_f32_16x16x32_bf16 v[112:115], a[76:79], a[240:243], v[112:115]\n"
        "v_rcp_f32 v172, v173\n"
        "v_mfma_f32_16x16x32_bf16 v[112:115], a[80:83], a[244:247], v[112:115]\n"
        "ds_write2st64_b32 v245, v108, v109 offset0:16 offset1:20\n"
        "v_mfma_f32_16x16x32_bf16 v[112:115], a[84:87], a[248:251], v[112:115]\n"
        "buffer_load_dwordx4 v233, s[52:55], s34 offen offset:1024 lds\n"
        "v_mfma_f32_16x16x32_bf16 v[112:115], a[88:91], a[216:219], v[112:115]\n"
        "v_mul_f32 v176, v172, v176\n"
        "v_mul_f32 v177, v172, v177\n"
        "v_mfma_f32_16x16x32_bf16 v[112:115], a[92:95], a[220:223], v[112:115]\n"
        "v_mul_f32 v178, v172, v178\n"
        "v_mul_f32 v179, v172, v179\n"
        "v_smfmac_f32_16x16x64_bf16 v[124:127], a[252:255], v[88:95], v237\n"
        "v_cvt_pk_bf16_f32 v176, v176, v177\n"
        "v_mfma_f32_16x16x32_bf16 v[80:83], v[132:135], v[92:95], 0\n"
        "v_cvt_pk_bf16_f32 v177, v178, v179\n"
        "v_mul_f32 v180, v172, v180\n"
        "v_mfma_f32_16x16x32_bf16 v[80:83], v[128:131], v[88:91], v[80:83]\n"
        "ds_write2st64_b32 v245, v176, v177 offset0:1 offset1:5\n"
        "v_mfma_f32_16x16x32_bf16 v[84:87], v[136:139], v[88:91], 0\n"
        "v_cvt_pk_bf16_f32 v152, v112, v113\n"
        "v_cvt_pk_bf16_f32 v153, v114, v115\n"
        "v_mfma_f32_16x16x32_bf16 v[84:87], v[140:143], v[92:95], v[84:87]\n"
        "v_mul_f32 v181, v172, v181\n"
        "v_mul_f32 v182, v172, v182\n"
        "v_mfma_f32_16x16x32_bf16 v[96:99], v[148:151], v[92:95], 0\n"
        "buffer_load_dwordx4 v234, s[52:55], s34 offen offset:2048 lds\n"
        "v_mfma_f32_16x16x32_bf16 v[96:99], v[144:147], v[88:91], v[96:99]\n"
        "v_mul_f32 v183, v172, v183\n"
        "v_cvt_pk_bf16_f32 v178, v180, v181\n"
        "v_mfma_f32_16x16x32_bf16 v[116:119], a[116:119], a[220:223], 0\n"
        "v_cvt_pk_bf16_f32 v179, v182, v183\n"
        "v_mul_f32 v184, v172, v184\n"
        "v_mfma_f32_16x16x32_bf16 v[116:119], a[112:115], a[216:219], v[116:119]\n"
        "ds_write2st64_b32 v245, v178, v179 offset0:9 offset1:13\n"
        "v_mfma_f32_16x16x32_bf16 v[116:119], a[108:111], a[248:251], v[116:119]\n"
        "v_mul_f32 v185, v172, v185\n"
        "v_mul_f32 v186, v172, v186\n"
        "v_mfma_f32_16x16x32_bf16 v[116:119], a[104:107], a[244:247], v[116:119]\n"
        "s_add_u32 m0, s65, 15360\n"
        "v_mul_f32 v187, v172, v187\n"
        "v_add_f32 v125, v124, v125\n"
        "v_mfma_f32_16x16x32_bf16 v[116:119], a[100:103], a[240:243], v[116:119]\n"
        "v_rcp_f32 v124, v125\n"
        "v_mfma_f32_16x16x32_bf16 v[116:119], a[96:99], a[236:239], v[116:119]\n"
        "v_cvt_pk_bf16_f32 v184, v184, v185\n"
        "v_cvt_pk_bf16_f32 v185, v186, v187\n"
        "v_mfma_f32_16x16x32_bf16 v[120:123], a[120:123], a[236:239], 0\n"
        "buffer_load_dwordx4 v232, s[52:55], s62 offen lds\n"
        "v_mfma_f32_16x16x32_bf16 v[120:123], a[124:127], a[240:243], v[120:123]\n"
        "ds_write2st64_b32 v245, v184, v185 offset0:17 offset1:21\n"
        "v_mfma_f32_16x16x32_bf16 v[120:123], a[128:131], a[244:247], v[120:123]\n"
        "v_mul_f32 v80, v124, v80\n"
        "v_mul_f32 v81, v124, v81\n"
        "v_mfma_f32_16x16x32_bf16 v[120:123], a[132:135], a[248:251], v[120:123]\n"
        "v_mul_f32 v82, v124, v82\n"
        "v_mul_f32 v83, v124, v83\n"
        "v_mfma_f32_16x16x32_bf16 v[120:123], a[136:139], a[216:219], v[120:123]\n"
        "v_max3_u32 v236, v236, v172, v173\n"
        "v_cvt_pk_bf16_f32 v80, v80, v81\n"
        "v_mfma_f32_16x16x32_bf16 v[120:123], a[140:143], a[220:223], v[120:123]\n"
        "v_cvt_pk_bf16_f32 v81, v82, v83\n"
        "v_mul_f32 v84, v124, v84\n"
        "v_mfma_f32_16x16x32_bf16 v[112:115], a[20:23], a[220:223], 0\n"
        "v_mul_f32 v85, v124, v85\n"
        "v_mul_f32 v86, v124, v86\n"
        "v_mfma_f32_16x16x32_bf16 v[112:115], a[16:19], a[216:219], v[112:115]\n"
        "v_mul_f32 v87, v124, v87\n"
        "v_cvt_pk_bf16_f32 v82, v84, v85\n"
        "v_mfma_f32_16x16x32_bf16 v[112:115], a[12:15], a[248:251], v[112:115]\n"
        "buffer_load_dwordx4 v233, s[52:55], s62 offen offset:1024 lds\n"
        "v_mfma_f32_16x16x32_bf16 v[112:115], a[8:11], a[244:247], v[112:115]\n"
        "v_cvt_pk_bf16_f32 v83, v86, v87\n"
        "v_mul_f32 v96, v124, v96\n"
        "v_mfma_f32_16x16x32_bf16 v[112:115], a[4:7], a[240:243], v[112:115]\n"
        "v_mul_f32 v97, v124, v97\n"
        "v_mul_f32 v98, v124, v98\n"
        "v_mfma_f32_16x16x32_bf16 v[112:115], a[0:3], a[236:239], v[112:115]\n"
        "v_mul_f32 v99, v124, v99\n"
        "v_cvt_pk_bf16_f32 v154, v116, v117\n"
        "v_cvt_pk_bf16_f32 v155, v118, v119\n"
        "v_mfma_f32_16x16x32_bf16 v[116:119], a[24:27], a[236:239], 0\n"
        "v_cvt_pk_bf16_f32 v96, v96, v97\n"
        "v_cvt_pk_bf16_f32 v97, v98, v99\n"
        "v_mfma_f32_16x16x32_bf16 v[116:119], a[28:31], a[240:243], v[116:119]\n"
        "ds_write_b32 v245, v96 offset:4864\n"
        "v_mfma_f32_16x16x32_bf16 v[116:119], a[32:35], a[244:247], v[116:119]\n"
        "v_cvt_pk_bf16_f32 v168, v120, v121\n"
        "v_cvt_pk_bf16_f32 v169, v122, v123\n"
        "v_mfma_f32_16x16x32_bf16 v[116:119], a[36:39], a[248:251], v[116:119]\n"
        "v_add_f32 v193, v192, v193\n"
        "v_cvt_pk_bf16_f32 v156, v112, v113\n"
        "v_mfma_f32_16x16x32_bf16 v[116:119], a[40:43], a[216:219], v[116:119]\n"
        "buffer_load_dwordx4 v234, s[52:55], s62 offen offset:2048 lds\n"
        "v_mfma_f32_16x16x32_bf16 v[116:119], a[44:47], a[220:223], v[116:119]\n"
        "v_rcp_f32 v192, v193\n"
        "v_mfma_f32_16x16x32_bf16 v[120:123], a[68:71], a[220:223], 0\n"
        "v_mul_f32 v196, v192, v196\n"
        "v_mul_f32 v197, v192, v197\n"
        "v_mfma_f32_16x16x32_bf16 v[120:123], a[64:67], a[216:219], v[120:123]\n"
        "v_mul_f32 v198, v192, v198\n"
        "v_mul_f32 v199, v192, v199\n"
        "v_mfma_f32_16x16x32_bf16 v[120:123], a[60:63], a[248:251], v[120:123]\n"
        "ds_read_b128 a[224:227], v240 offset:1536\n"
        "v_cvt_pk_bf16_f32 v157, v114, v115\n"
        "v_mfma_f32_16x16x32_bf16 v[120:123], a[56:59], a[244:247], v[120:123]\n"
        "s_add_u32 m0, s65, 0\n"
        "ds_read_b128 a[228:231], v241 offset:1536\n"
        "v_cvt_pk_bf16_f32 v196, v196, v197\n"
        "v_mfma_f32_16x16x32_bf16 v[120:123], a[52:55], a[240:243], v[120:123]\n"
        "ds_read_b128 a[232:235], v240 offset:1664\n"
        "v_cvt_pk_bf16_f32 v197, v198, v199\n"
        "v_mfma_f32_16x16x32_bf16 v[120:123], a[48:51], a[236:239], v[120:123]\n"
        "ds_write2st64_b32 v245, v196, v197 offset0:2 offset1:6\n"
        "v_mfma_f32_16x16x32_bf16 v[112:115], a[236:239], a[144:147], 0\n"
        "buffer_load_dwordx4 v232, s[52:55], 0 offen lds\n"
        "v_mfma_f32_16x16x32_bf16 v[112:115], a[240:243], a[148:151], v[112:115]\n"
        "v_mul_f32 v200, v192, v200\n"
        "v_mul_f32 v201, v192, v201\n"
        "v_mfma_f32_16x16x32_bf16 v[112:115], a[244:247], a[152:155], v[112:115]\n"
        "v_mul_f32 v202, v192, v202\n"
        "v_mul_f32 v203, v192, v203\n"
        "v_mfma_f32_16x16x32_bf16 v[112:115], a[248:251], a[156:159], v[112:115]\n"
        "v_cvt_pk_bf16_f32 v198, v200, v201\n"
        "v_cvt_pk_bf16_f32 v199, v202, v203\n"
        "v_mfma_f32_16x16x32_bf16 v[112:115], a[216:219], a[160:163], v[112:115]\n"
        "ds_write2st64_b32 v245, v198, v199 offset0:10 offset1:14\n"
        "v_mfma_f32_16x16x32_bf16 v[112:115], a[220:223], a[164:167], v[112:115]\n"
        "v_mul_f32 v204, v192, v204\n"
        "v_mul_f32 v205, v192, v205\n"
        "v_cvt_pk_bf16_f32 v158, v116, v117\n"
        "v_cvt_pk_bf16_f32 v159, v118, v119\n"
        "v_mfma_f32_16x16x32_bf16 v[116:119], a[220:223], a[188:191], 0\n"
        "s_waitcnt vmcnt(4)\n"
        "ds_read_b64_tr_b16 v[64:65], v255 offset:12288\n"
        "ds_read_b64_tr_b16 v[66:67], v255 offset:13056\n"
        "v_mfma_f32_16x16x32_bf16 v[116:119], a[216:219], a[184:187], v[116:119]\n"
        "ds_write_b32 v245, v97 offset:5888\n"
        "v_mfma_f32_16x16x32_bf16 v[116:119], a[248:251], a[180:183], v[116:119]\n"
        "buffer_load_dwordx4 v233, s[52:55], 0 offen offset:1024 lds\n"
        "v_mfma_f32_16x16x32_bf16 v[116:119], a[244:247], a[176:179], v[116:119]\n"
        "ds_read_b64_tr_b16 v[68:69], v255 offset:12320\n"
        "ds_read_b64_tr_b16 v[70:71], v255 offset:13088\n"
        "v_mfma_f32_16x16x32_bf16 v[116:119], a[240:243], a[172:175], v[116:119]\n"
        "ds_read_b64_tr_b16 v[72:73], v255 offset:12352\n"
        "ds_read_b64_tr_b16 v[74:75], v255 offset:13120\n"
        "v_mfma_f32_16x16x32_bf16 v[116:119], a[236:239], a[168:171], v[116:119]\n"
        "v_mul_f32 v206, v192, v206\n"
        "v_mul_f32 v207, v192, v207\n"
        "v_cvt_pk_bf16_f32 v160, v120, v121\n"
        "v_cvt_pk_bf16_f32 v161, v122, v123\n"
        "v_cvt_pk_bf16_f32 v166, v120, v121\n"
        "v_cvt_pk_bf16_f32 v167, v122, v123\n"
        "v_mfma_f32_16x16x32_bf16 v[120:123], a[236:239], a[192:195], 0\n"
        "ds_read_b128 a[236:239], v241 offset:1664\n"
        "v_cvt_pk_bf16_f32 v204, v204, v205\n"
        "v_mfma_f32_16x16x32_bf16 v[120:123], a[240:243], a[196:199], v[120:123]\n"
        "ds_read_b128 a[240:243], v240 offset:1792\n"
        "v_cvt_pk_bf16_f32 v205, v206, v207\n"
        "v_mfma_f32_16x16x32_bf16 v[120:123], a[244:247], a[200:203], v[120:123]\n"
        "ds_write2st64_b32 v245, v204, v205 offset0:18 offset1:22\n"
        "v_mfma_f32_16x16x32_bf16 v[120:123], a[248:251], a[204:207], v[120:123]\n"
        "ds_read_b128 a[244:247], v241 offset:1792\n"
        "v_max3_u32 v236, v236, v192, v193\n"
        "v_mfma_f32_16x16x32_bf16 v[120:123], a[216:219], a[208:211], v[120:123]\n"
        "buffer_load_dwordx4 v234, s[52:55], 0 offen offset:2048 lds\n"
        "v_mfma_f32_16x16x32_bf16 v[120:123], a[220:223], a[212:215], v[120:123]\n"
        "s_waitcnt lgkmcnt(4)\n"
        "ds_write_b64 v235, v[64:65] offset:12288\n"
        "v_cvt_pk_bf16_f32 v128, v112, v113\n"
        "v_cvt_pk_bf16_f32 v129, v114, v115\n"
        "v_mfma_f32_16x16x32_bf16 v[112:115], a[72:75], a[224:227], 0\n"
        "s_barrier\n"
        "s_waitcnt vmcnt(3)\n"
        "ds_read_b64_tr_b16 v[76:77], v255 offset:15360\n"
        "ds_read_b64_tr_b16 v[78:79], v255 offset:16128\n"
        "v_mfma_f32_16x16x32_bf16 v[112:115], a[76:79], a[228:231], v[112:115]\n"
        "v_max3_u32 v236, v236, v124, v125\n"
        "v_cmp_lt_u32 vcc, s24, v236\n"
        "v_mfma_f32_16x16x32_bf16 v[112:115], a[80:83], a[232:235], v[112:115]\n"
        "s_waitcnt lgkmcnt(5)\n"
        "ds_write_b64 v235, v[66:67] offset:12296\n"
        "s_cmp_lg_u64 vcc, 0\n"
        "s_addc_u32 s25, s25, s25\n"
        "v_mfma_f32_16x16x32_bf16 v[112:115], a[84:87], a[236:239], v[112:115]\n"
        "s_add_u32 m0, s65, 3072\n"
        "v_and_b32 v239, 32, v245\n"
        "v_cvt_pk_bf16_f32 v136, v116, v117\n"
        "v_mfma_f32_16x16x32_bf16 v[112:115], a[88:91], a[240:243], v[112:115]\n"
        "s_waitcnt lgkmcnt(4)\n"
        "v_cvt_pk_bf16_f32 v137, v118, v119\n"
        "v_xor_b32 v251, 0x2000, v251\n"
        "v_mfma_f32_16x16x32_bf16 v[112:115], a[92:95], a[244:247], v[112:115]\n"
        "v_xor_b32 v252, 0x2000, v252\n"
        "v_mad_i32_i24 v239, v239, -12, v235\n"
        "v_mfma_f32_16x16x32_bf16 v[116:119], a[116:119], a[244:247], 0\n"
        "ds_write_b64 v239, v[68:69] offset:12480\n"
        "v_mfma_f32_16x16x32_bf16 v[116:119], a[112:115], a[240:243], v[116:119]\n"
        "buffer_load_dwordx4 v232, s[52:55], s63 offen lds\n"
        "v_mfma_f32_16x16x32_bf16 v[116:119], a[108:111], a[236:239], v[116:119]\n"
        "ds_read_b64_tr_b16 v[88:89], v255 offset:15392\n"
        "ds_read_b64_tr_b16 v[90:91], v255 offset:16160\n"
        "v_mfma_f32_16x16x32_bf16 v[116:119], a[104:107], a[232:235], v[116:119]\n"
        "ds_write_b64 v239, v[70:71] offset:12488\n"
        "v_mfma_f32_16x16x32_bf16 v[116:119], a[100:103], a[228:231], v[116:119]\n"
        "v_cvt_pk_bf16_f32 v144, v120, v121\n"
        "v_cvt_pk_bf16_f32 v145, v122, v123\n"
        "v_mfma_f32_16x16x32_bf16 v[116:119], a[96:99], a[224:227], v[116:119]\n"
        "v_cvt_pk_bf16_f32 v172, v112, v113\n"
        "v_cvt_pk_bf16_f32 v173, v114, v115\n"
        "v_mfma_f32_16x16x32_bf16 v[120:123], a[120:123], a[224:227], 0\n"
        "ds_write_b64 v254, v[72:73] offset:12288\n"
        "v_mfma_f32_16x16x32_bf16 v[120:123], a[124:127], a[228:231], v[120:123]\n"
        "ds_read_b128 v[162:165], v251 offset:0\n"
        "v_add_u32 v247, s45, v247\n"
        "v_mfma_f32_16x16x32_bf16 v[120:123], a[128:131], a[232:235], v[120:123]\n"
        "buffer_load_dwordx4 v233, s[52:55], s63 offen offset:1024 lds\n"
        "v_mfma_f32_16x16x32_bf16 v[120:123], a[132:135], a[236:239], v[120:123]\n"
        "ds_write_b64 v254, v[74:75] offset:12296\n"
        "v_mfma_f32_16x16x32_bf16 v[120:123], a[136:139], a[240:243], v[120:123]\n"
        "ds_read_b64_tr_b16 v[92:93], v255 offset:15424\n"
        "ds_read_b64_tr_b16 v[94:95], v255 offset:16192\n"
        "v_mfma_f32_16x16x32_bf16 v[120:123], a[140:143], a[244:247], v[120:123]\n"
        "v_cvt_pk_bf16_f32 v174, v116, v117\n"
        "v_cvt_pk_bf16_f32 v175, v118, v119\n"
        "v_mfma_f32_16x16x32_bf16 v[112:115], a[20:23], a[244:247], 0\n"
        "s_nop 2\n"
        "v_mfma_f32_16x16x32_bf16 v[112:115], a[16:19], a[240:243], v[112:115]\n"
        "s_nop 0\n"
        "v_cvt_pk_bf16_f32 v190, v120, v121\n"
        "v_cvt_pk_bf16_f32 v191, v122, v123\n"
        "v_mfma_f32_16x16x32_bf16 v[112:115], a[12:15], a[236:239], v[112:115]\n"
        "v_mfma_f32_16x16x32_bf16 v[112:115], a[8:11], a[232:235], v[112:115]\n"
        "s_waitcnt vmcnt(2)\n"
        "ds_read_b64_tr_b16 v[212:213], v255 offset:0\n"
        "ds_read_b64_tr_b16 v[214:215], v255 offset:768\n"
        "v_mfma_f32_16x16x32_bf16 v[112:115], a[4:7], a[228:231], v[112:115]\n"
        "buffer_load_dwordx4 v234, s[52:55], s63 offen offset:2048 lds\n"
        "v_mfma_f32_16x16x32_bf16 v[112:115], a[0:3], a[224:227], v[112:115]\n"
        "ds_read_b128 v[182:185], v251 offset:512\n"
        "s_nop 0\n"
        "v_mfma_f32_16x16x32_bf16 v[116:119], a[24:27], a[224:227], 0\n"
        "s_nop 2\n"
        "v_mfma_f32_16x16x32_bf16 v[116:119], a[28:31], a[228:231], v[116:119]\n"
        "s_waitcnt lgkmcnt(3)\n"
        "ds_write_b128 v235, v[76:79] offset:15360\n"
        "v_mfma_f32_16x16x32_bf16 v[116:119], a[32:35], a[232:235], v[116:119]\n"
        "v_cvt_pk_bf16_f32 v176, v112, v113\n"
        "v_cvt_pk_bf16_f32 v177, v114, v115\n"
        "v_mfma_f32_16x16x32_bf16 v[116:119], a[36:39], a[236:239], v[116:119]\n"
        "s_add_u32 m0, s65, 6144\n"
        "v_mfma_f32_16x16x32_bf16 v[116:119], a[40:43], a[240:243], v[116:119]\n"
        "ds_write_b32 v245, v80 offset:768\n"
        "v_mfma_f32_16x16x32_bf16 v[116:119], a[44:47], a[244:247], v[116:119]\n"
        "ds_read_b64_tr_b16 v[216:217], v255 offset:32\n"
        "ds_read_b64_tr_b16 v[218:219], v255 offset:800\n"
        "v_mfma_f32_16x16x32_bf16 v[120:123], a[68:71], a[244:247], 0\n"
        "buffer_load_dwordx4 v232, s[52:55], s61 offen lds\n"
        "v_mfma_f32_16x16x32_bf16 v[120:123], a[64:67], a[240:243], v[120:123]\n"
        "s_nop 2\n"
        "v_mfma_f32_16x16x32_bf16 v[120:123], a[60:63], a[236:239], v[120:123]\n"
        "ds_write_b128 v239, v[88:91] offset:15552\n"
        "v_mfma_f32_16x16x32_bf16 v[120:123], a[56:59], a[232:235], v[120:123]\n"
        "v_cvt_pk_bf16_f32 v178, v116, v117\n"
        "v_cvt_pk_bf16_f32 v179, v118, v119\n"
        "v_mfma_f32_16x16x32_bf16 v[120:123], a[52:55], a[228:231], v[120:123]\n"
        "v_mfma_f32_16x16x32_bf16 v[120:123], a[48:51], a[224:227], v[120:123]\n"
        "ds_write_b32 v245, v81 offset:1792\n"
        "v_mfma_f32_16x16x32_bf16 v[112:115], a[224:227], a[144:147], 0\n"
        "ds_read_b64_tr_b16 v[220:221], v255 offset:64\n"
        "ds_read_b64_tr_b16 v[222:223], v255 offset:832\n"
        "v_mfma_f32_16x16x32_bf16 v[112:115], a[228:231], a[148:151], v[112:115]\n"
        "ds_write_b32 v245, v82 offset:2816\n"
        "v_mfma_f32_16x16x32_bf16 v[112:115], a[232:235], a[152:155], v[112:115]\n"
        "ds_write_b32 v245, v83 offset:3840\n"
        "v_mfma_f32_16x16x32_bf16 v[112:115], a[236:239], a[156:159], v[112:115]\n"
        "buffer_load_dwordx4 v233, s[52:55], s61 offen offset:1024 lds\n"
        "v_mfma_f32_16x16x32_bf16 v[112:115], a[240:243], a[160:163], v[112:115]\n"
        "ds_read_b128 a[248:251], v240 offset:12288\n"
        "v_cvt_pk_bf16_f32 v180, v120, v121\n"
        "v_mfma_f32_16x16x32_bf16 v[112:115], a[244:247], a[164:167], v[112:115]\n"
        "ds_write_b128 v254, v[92:95] offset:15360\n"
        "v_mfma_f32_16x16x32_bf16 v[96:99], v[152:155], v[156:159], v[0:3]\n"
        "v_cvt_pk_bf16_f32 v181, v122, v123\n"
        "v_cvt_pk_bf16_f32 v186, v120, v121\n"
        "v_mfma_f32_16x16x32_bf16 v[96:99], v[168:171], v[160:163], v[96:99]\n"
        "s_sub_u32 s31, s30, s28\n"
        "v_cvt_pk_bf16_f32 v187, v122, v123\n"
        "s_nop 0\n"
        "v_mfma_f32_16x16x32_bf16 v[100:103], v[172:175], v[156:159], v[4:7]\n"
        ".long 0xbec200ff\n"
        "43:\n"
        "s_branch 60b\n"
        "s_sub_u32 s45, s28, s29\n"
        "v_cvt_pk_bf16_f32 v130, v112, v113\n"
        "v_cvt_pk_bf16_f32 v131, v114, v115\n"
        "v_mfma_f32_16x16x32_bf16 v[100:103], v[188:191], v[164:167], v[100:103]\n"
        "s_waitcnt vmcnt(2)\n"
        "ds_read_b64_tr_b16 v[224:225], v255 offset:3072\n"
        "ds_read_b64_tr_b16 v[226:227], v255 offset:3840\n"
        "s_add_u32 s28, s28, s31\n"
        "v_mfma_f32_16x16x32_bf16 v[116:119], a[244:247], a[188:191], 0\n"
        "buffer_load_dwordx4 v234, s[52:55], s61 offen offset:2048 lds\n"
        "v_mfma_f32_16x16x32_bf16 v[116:119], a[240:243], a[184:187], v[116:119]\n"
        "s_waitcnt lgkmcnt(5)\n"
        "ds_write_b128 v235, v[212:215] offset:0\n"
        "v_mfma_f32_16x16x32_bf16 v[116:119], a[236:239], a[180:183], v[116:119]\n"
        "v_exp_f32 v96, v96\n"
        "v_mfma_f32_16x16x32_bf16 v[116:119], a[232:235], a[176:179], v[116:119]\n"
        "v_exp_f32 v97, v97\n"
        "v_mfma_f32_16x16x32_bf16 v[116:119], a[228:231], a[172:175], v[116:119]\n"
        "v_exp_f32 v98, v98\n"
        "v_mfma_f32_16x16x32_bf16 v[116:119], a[224:227], a[168:171], v[116:119]\n"
        "ds_read_b64_tr_b16 v[88:89], v255 offset:3104\n"
        "ds_read_b64_tr_b16 v[90:91], v255 offset:3872\n"
        "s_add_u32 m0, s65, 9216\n"
        "v_mfma_f32_16x16x32_bf16 v[104:107], v[152:155], v[176:179], v[16:19]\n"
        "ds_read_b128 a[216:219], v241 offset:12288\n"
        "v_cvt_pk_bf16_f32 v64, v96, v97\n"
        "v_mfma_f32_16x16x32_bf16 v[104:107], v[168:171], v[180:183], v[104:107]\n"
        "ds_write_b128 v239, v[216:219] offset:192\n"
        "v_mfma_f32_16x16x32_bf16 v[108:111], v[172:175], v[176:179], v[20:23]\n"
        "buffer_load_dwordx4 v232, s[52:55], s56 offen lds\n"
        "v_mfma_f32_16x16x32_bf16 v[108:111], v[188:191], v[184:187], v[108:111]\n"
        "v_exp_f32 v100, v100\n"
        "v_mfma_f32_16x16x32_bf16 v[120:123], a[224:227], a[192:195], 0\n"
        "v_cvt_pk_bf16_f32 v138, v116, v117\n"
        "v_cvt_pk_bf16_f32 v139, v118, v119\n"
        "v_mfma_f32_16x16x32_bf16 v[120:123], a[228:231], a[196:199], v[120:123]\n"
        "ds_read_b128 a[220:223], v240 offset:12416\n"
        "ds_read_b128 a[224:227], v241 offset:12416\n"
        "v_mfma_f32_16x16x32_bf16 v[120:123], a[232:235], a[200:203], v[120:123]\n"
        "ds_read_b64_tr_b16 v[92:93], v255 offset:3136\n"
        "ds_read_b64_tr_b16 v[94:95], v255 offset:3904\n"
        "v_mfma_f32_16x16x32_bf16 v[120:123], a[236:239], a[204:207], v[120:123]\n"
        "ds_read_b128 a[228:231], v240 offset:12544\n"
        "ds_read_b128 a[232:235], v241 offset:12544\n"
        "v_mfma_f32_16x16x32_bf16 v[120:123], a[240:243], a[208:211], v[120:123]\n"
        "ds_write_b128 v254, v[220:223] offset:0\n"
        "v_mfma_f32_16x16x32_bf16 v[120:123], a[244:247], a[212:215], v[120:123]\n"
        "s_waitcnt lgkmcnt(8)\n"
        "v_exp_f32 v99, v99\n"
        "v_mfma_f32_16x16x32_bf16 v[112:115], a[0:3], a[248:251], 0\n"
        "buffer_load_dwordx4 v233, s[52:55], s56 offen offset:1024 lds\n"
        "v_mfma_f32_16x16x32_bf16 v[112:115], a[4:7], a[216:219], v[112:115]\n"
        "s_waitcnt lgkmcnt(5)\n"
        "v_exp_f32 v101, v101\n"
        "v_mfma_f32_16x16x32_bf16 v[112:115], a[8:11], a[220:223], v[112:115]\n"
        "ds_read_b128 v[202:205], v251 offset:4096\n"
        "v_cvt_pk_bf16_f32 v65, v98, v99\n"
        "v_mfma_f32_16x16x32_bf16 v[112:115], a[12:15], a[224:227], v[112:115]\n"
        "s_waitcnt lgkmcnt(2)\n"
        "ds_read_b128 v[162:165], v252 offset:0\n"
        "v_cvt_pk_bf16_f32 v66, v100, v101\n"
        "v_mfma_f32_16x16x32_bf16 v[112:115], a[16:19], a[228:231], v[112:115]\n"
        "s_waitcnt vmcnt(2)\n"
        "ds_read_b64_tr_b16 v[212:213], v255 offset:6144\n"
        "ds_read_b64_tr_b16 v[214:215], v255 offset:6912\n"
        "v_mfma_f32_16x16x32_bf16 v[112:115], a[20:23], a[232:235], v[112:115]\n"
        "ds_write_b128 v235, v[224:227] offset:3072\n"
        "v_mfma_f32_16x16x32_bf16 v[116:119], a[44:47], a[232:235], 0\n"
        "v_exp_f32 v102, v102\n"
        "v_mfma_f32_16x16x32_bf16 v[116:119], a[40:43], a[228:231], v[116:119]\n"
        "v_exp_f32 v103, v103\n"
        "v_mfma_f32_16x16x32_bf16 v[116:119], a[36:39], a[224:227], v[116:119]\n"
        "buffer_load_dwordx4 v234, s[52:55], s56 offen offset:2048 lds\n"
        "s_add_u32 s35, s47, 786432\n"
        "s_sub_u32 s46, s35, 100663296\n"
        "s_cmp_lt_u32 s46, 786432\n"
        "v_mfma_f32_16x16x32_bf16 v[116:119], a[32:35], a[220:223], v[116:119]\n"
        "s_cselect_b32 s35, s46, s35\n"
        "s_add_u32 s41, s30, s40\n"
        "s_mov_b32 s30, s29\n"
        "s_add_u32 s29, s29, s45\n"
        "v_mfma_f32_16x16x32_bf16 v[116:119], a[28:31], a[216:219], v[116:119]\n"
        "ds_read_b64_tr_b16 v[216:217], v255 offset:6176\n"
        "ds_read_b64_tr_b16 v[218:219], v255 offset:6944\n"
        "v_mfma_f32_16x16x32_bf16 v[116:119], a[24:27], a[248:251], v[116:119]\n"
        "ds_read_b128 v[182:185], v252 offset:512\n"
        "v_cvt_pk_bf16_f32 v67, v102, v103\n"
        "v_cvt_pk_bf16_f32 v146, v120, v121\n"
        "v_cvt_pk_bf16_f32 v147, v122, v123\n"
        "v_mfma_f32_16x16x32_bf16 v[120:123], a[48:51], a[248:251], 0\n"
        "ds_write_b128 v239, v[88:91] offset:3264\n"
        "v_mfma_f32_16x16x32_bf16 v[120:123], a[52:55], a[216:219], v[120:123]\n"
        "v_exp_f32 v104, v104\n"
        "v_mfma_f32_16x16x32_bf16 v[120:123], a[56:59], a[220:223], v[120:123]\n"
        "s_mov_b32 m0, s48\n"
        "v_exp_f32 v105, v105\n"
        "v_mfma_f32_16x16x32_bf16 v[120:123], a[60:63], a[224:227], v[120:123]\n"
        "v_exp_f32 v106, v106\n"
        "v_mfma_f32_16x16x32_bf16 v[120:123], a[64:67], a[228:231], v[120:123]\n"
        "ds_read_b64_tr_b16 v[220:221], v255 offset:6208\n"
        "ds_read_b64_tr_b16 v[222:223], v255 offset:6976\n"
        "v_mfma_f32_16x16x32_bf16 v[120:123], a[68:71], a[232:235], v[120:123]\n"
        "buffer_load_dwordx4 v249, s[12:15], s36 offen offset:0 lds\n"
        "v_cvt_pk_bf16_f32 v196, v112, v113\n"
        "v_cvt_pk_bf16_f32 v197, v114, v115\n"
        "v_mfma_f32_16x16x32_bf16 v[112:115], a[92:95], a[232:235], 0\n"
        "v_cvt_pk_bf16_f32 v72, v104, v105\n"
        "v_cvt_pk_bf16_f32 v198, v116, v117\n"
        "v_mfma_f32_16x16x32_bf16 v[112:115], a[88:91], a[228:231], v[112:115]\n"
        "ds_write_b128 v254, v[92:95] offset:3072\n"
        "v_mfma_f32_16x16x32_bf16 v[112:115], a[84:87], a[224:227], v[112:115]\n"
        "v_exp_f32 v108, v108\n"
        "v_mfma_f32_16x16x32_bf16 v[112:115], a[80:83], a[220:223], v[112:115]\n"
        "v_exp_f32 v107, v107\n"
        "v_mfma_f32_16x16x32_bf16 v[112:115], a[76:79], a[216:219], v[112:115]\n"
        "v_exp_f32 v109, v109\n"
        "v_mfma_f32_16x16x32_bf16 v[112:115], a[72:75], a[248:251], v[112:115]\n"
        "s_waitcnt vmcnt(1)\n"
        "ds_read_b64_tr_b16 v[224:225], v255 offset:9216\n"
        "ds_read_b64_tr_b16 v[226:227], v255 offset:9984\n"
        "v_cvt_pk_bf16_f32 v199, v118, v119\n"
        "s_nop 1\n"
        "v_mfma_f32_16x16x32_bf16 v[96:99], v[152:155], v[196:199], v[32:35]\n"
        "s_waitcnt lgkmcnt(3)\n"
        "v_cvt_pk_bf16_f32 v73, v106, v107\n"
        "v_cvt_pk_bf16_f32 v74, v108, v109\n"
        "v_cvt_pk_bf16_f32 v200, v120, v121\n"
        "v_cvt_pk_bf16_f32 v201, v122, v123\n"
        "s_nop 1\n"
        "v_mfma_f32_16x16x32_bf16 v[96:99], v[168:171], v[200:203], v[96:99]\n"
        "buffer_load_dwordx4 v253, s[12:15], s36 offen offset:1024 lds\n"
        "v_mfma_f32_16x16x32_bf16 v[100:103], v[172:175], v[196:199], v[36:39]\n"
        "ds_write_b128 v235, v[212:215] offset:6144\n"
        "v_cvt_pk_bf16_f32 v206, v120, v121\n"
        "v_cvt_pk_bf16_f32 v207, v122, v123\n"
        "s_nop 1\n"
        "v_mfma_f32_16x16x32_bf16 v[100:103], v[188:191], v[204:207], v[100:103]\n"
        "s_cmp_eq_u32 s37, 100663296\n"
        "v_exp_f32 v110, v110\n"
        "v_mfma_f32_16x16x32_bf16 v[116:119], a[96:99], a[248:251], 0\n"
        "s_cselect_b32 s46, 0, s37\n"
        "v_exp_f32 v111, v111\n"
        "v_mfma_f32_16x16x32_bf16 v[116:119], a[100:103], a[216:219], v[116:119]\n"
        "s_add_u32 m0, s41, 0\n"
        "v_cvt_pk_bf16_f32 v75, v110, v111\n"
        "v_cvt_pk_bf16_f32 v192, v112, v113\n"
        "v_mfma_f32_16x16x32_bf16 v[116:119], a[104:107], a[220:223], v[116:119]\n"
        "ds_read_b64_tr_b16 v[88:89], v255 offset:9248\n"
        "ds_read_b64_tr_b16 v[90:91], v255 offset:10016\n"
        "s_add_u32 s38, s46, s43\n"
        "v_mfma_f32_16x16x32_bf16 v[116:119], a[108:111], a[224:227], v[116:119]\n"
        "buffer_load_dwordx4 v242, s[8:11], s47 offen lds\n"
        "v_mfma_f32_16x16x32_bf16 v[116:119], a[112:115], a[228:231], v[116:119]\n"
        "ds_read_b128 v[202:205], v252 offset:4096\n"
        "s_add_u32 s39, s46, s44\n"
        "v_cvt_pk_bf16_f32 v193, v114, v115\n"
        "v_mfma_f32_16x16x32_bf16 v[116:119], a[116:119], a[232:235], v[116:119]\n"
        "ds_write_b128 v239, v[216:219] offset:6336\n"
        "s_add_u32 s37, s37, 6291456\n"
        "v_mfma_f32_16x16x32_bf16 v[120:123], a[140:143], a[232:235], 0\n"
        "v_exp_f32 v96, v96\n"
        "v_mfma_f32_16x16x32_bf16 v[120:123], a[136:139], a[228:231], v[120:123]\n"
        "s_add_u32 m0, s41, 1024\n"
        "v_exp_f32 v97, v97\n"
        "v_mfma_f32_16x16x32_bf16 v[120:123], a[132:135], a[224:227], v[120:123]\n"
        "v_exp_f32 v98, v98\n"
        "v_mfma_f32_16x16x32_bf16 v[120:123], a[128:131], a[220:223], v[120:123]\n"
        "ds_read_b64_tr_b16 v[92:93], v255 offset:9280\n"
        "ds_read_b64_tr_b16 v[94:95], v255 offset:10048\n"
        "s_mov_b64 s[58:59], s[18:19]\n"
        "v_mfma_f32_16x16x32_bf16 v[120:123], a[124:127], a[216:219], v[120:123]\n"
        "buffer_load_dwordx4 v243, s[8:11], s47 offen lds\n"
        "v_mfma_f32_16x16x32_bf16 v[120:123], a[120:123], a[248:251], v[120:123]\n"
        "s_mov_b64 s[62:63], s[18:19]\n"
        "v_cvt_pk_bf16_f32 v80, v96, v97\n"
        "v_cvt_pk_bf16_f32 v194, v116, v117\n"
        "v_mfma_f32_16x16x32_bf16 v[112:115], a[248:251], a[144:147], 0\n"
        "ds_write_b128 v254, v[220:223] offset:6144\n"
        "v_mfma_f32_16x16x32_bf16 v[112:115], a[216:219], a[148:151], v[112:115]\n"
        "v_exp_f32 v100, v100\n"
        "v_mfma_f32_16x16x32_bf16 v[112:115], a[220:223], a[152:155], v[112:115]\n"
        "s_add_u32 m0, s41, 2048\n"
        "v_cvt_pk_bf16_f32 v195, v118, v119\n"
        "v_cvt_pk_bf16_f32 v208, v120, v121\n"
        "v_mfma_f32_16x16x32_bf16 v[112:115], a[224:227], a[156:159], v[112:115]\n"
        "v_exp_f32 v99, v99\n"
        "v_mfma_f32_16x16x32_bf16 v[112:115], a[228:231], a[160:163], v[112:115]\n"
        "buffer_load_dwordx4 v244, s[8:11], s47 offen lds\n"
        "v_mfma_f32_16x16x32_bf16 v[112:115], a[232:235], a[164:167], v[112:115]\n"
        "ds_read_b128 a[236:239], v240 offset:13824\n"
        "v_cvt_pk_bf16_f32 v81, v98, v99\n"
        "v_mfma_f32_16x16x32_bf16 v[104:107], v[192:195], v[156:159], v[8:11]\n"
        "ds_read_b128 a[240:243], v241 offset:13824\n"
        "v_cvt_pk_bf16_f32 v209, v122, v123\n"
        "s_nop 1\n"
        "v_mfma_f32_16x16x32_bf16 v[104:107], v[208:211], v[160:163], v[104:107]\n"
        "s_waitcnt lgkmcnt(3)\n"
        "ds_write_b128 v235, v[224:227] offset:9216\n"
        "v_mfma_f32_16x16x32_bf16 v[116:119], a[232:235], a[188:191], 0\n"
        "v_exp_f32 v101, v101\n"
        "v_mfma_f32_16x16x32_bf16 v[116:119], a[228:231], a[184:187], v[116:119]\n"
        "s_add_u32 m0, s41, 12288\n"
        "v_exp_f32 v102, v102\n"
        "v_mfma_f32_16x16x32_bf16 v[116:119], a[224:227], a[180:183], v[116:119]\n"
        "v_cvt_pk_bf16_f32 v82, v100, v101\n"
        "v_cvt_pk_bf16_f32 v132, v112, v113\n"
        "v_mfma_f32_16x16x32_bf16 v[116:119], a[220:223], a[176:179], v[116:119]\n"
        "buffer_load_dwordx4 v242, s[8:11], s35 offen lds\n"
        "v_mfma_f32_16x16x32_bf16 v[116:119], a[216:219], a[172:175], v[116:119]\n"
        "ds_read_b128 a[244:247], v240 offset:13952\n"
        "v_cvt_pk_bf16_f32 v133, v114, v115\n"
        "v_mfma_f32_16x16x32_bf16 v[116:119], a[248:251], a[168:171], v[116:119]\n"
        "v_exp_f32 v103, v103\n"
        "v_mfma_f32_16x16x32_bf16 v[108:111], v[192:195], v[176:179], v[24:27]\n"
        "ds_write_b128 v239, v[88:91] offset:9408\n"
        "v_mfma_f32_16x16x32_bf16 v[108:111], v[208:211], v[180:183], v[108:111]\n"
        "v_cvt_pk_bf16_f32 v83, v102, v103\n"
        "v_add_u32 v245, s45, v245\n"
        "v_mfma_f32_16x16x32_bf16 v[120:123], a[248:251], a[192:195], 0\n"
        "s_add_u32 m0, s41, 13312\n"
        "v_exp_f32 v104, v104\n"
        "v_mfma_f32_16x16x32_bf16 v[120:123], a[216:219], a[196:199], v[120:123]\n"
        "v_cvt_pk_bf16_f32 v140, v116, v117\n"
        "v_cvt_pk_bf16_f32 v141, v118, v119\n"
        "v_mfma_f32_16x16x32_bf16 v[120:123], a[220:223], a[200:203], v[120:123]\n"
        "ds_read_b128 a[248:251], v241 offset:13952\n"
        "ds_read_b128 a[216:219], v240 offset:14080\n"
        "v_mfma_f32_16x16x32_bf16 v[120:123], a[224:227], a[204:207], v[120:123]\n"
        "buffer_load_dwordx4 v243, s[8:11], s35 offen lds\n"
        "v_mfma_f32_16x16x32_bf16 v[120:123], a[228:231], a[208:211], v[120:123]\n"
        "ds_read_b128 a[220:223], v241 offset:14080\n"
        "v_bfe_u32 v238, v250, 3, 4\n"
        "v_mfma_f32_16x16x32_bf16 v[120:123], a[232:235], a[212:215], v[120:123]\n"
        "ds_write_b128 v254, v[92:95] offset:9216\n"
        "v_mfma_f32_16x16x32_bf16 v[96:99], v[192:195], v[196:199], v[40:43]\n"
        "v_exp_f32 v105, v105\n"
        "v_mfma_f32_16x16x32_bf16 v[96:99], v[208:211], v[200:203], v[96:99]\n"
        "s_waitcnt lgkmcnt(5)\n"
        "s_add_u32 m0, s41, 14336\n"
        "v_exp_f32 v106, v106\n"
        "v_mfma_f32_16x16x32_bf16 v[112:115], a[72:75], a[236:239], 0\n"
        "v_cvt_pk_bf16_f32 v68, v104, v105\n"
        "v_cvt_pk_bf16_f32 v148, v120, v121\n"
        "v_mfma_f32_16x16x32_bf16 v[112:115], a[76:79], a[240:243], v[112:115]\n"
        "buffer_load_dwordx4 v244, s[8:11], s35 offen lds\n"
        "v_mfma_f32_16x16x32_bf16 v[112:115], a[80:83], a[244:247], v[112:115]\n"
        "s_waitcnt lgkmcnt(1)\n"
        "v_exp_f32 v107, v107\n"
        "v_mfma_f32_16x16x32_bf16 v[112:115], a[84:87], a[248:251], v[112:115]\n"
        "v_cvt_pk_bf16_f32 v69, v106, v107\n"
        "v_cvt_pk_bf16_f32 v149, v122, v123\n"
        "v_mfma_f32_16x16x32_bf16 v[112:115], a[88:91], a[216:219], v[112:115]\n"
        "v_exp_f32 v108, v108\n"
        "v_mfma_f32_16x16x32_bf16 v[112:115], a[92:95], a[220:223], v[112:115]\n"
        "v_exp_f32 v109, v109\n"
        "v_mfma_f32_16x16x32_bf16 v[116:119], a[116:119], a[220:223], 0\n"
        "v_exp_f32 v110, v110\n"
        "v_mfma_f32_16x16x32_bf16 v[116:119], a[112:115], a[216:219], v[116:119]\n"
        "v_cvt_pk_bf16_f32 v76, v108, v109\n"
        "v_and_b32 v254, 32, v250\n"
        "v_mfma_f32_16x16x32_bf16 v[116:119], a[108:111], a[248:251], v[116:119]\n"
        "v_exp_f32 v111, v111\n"
        "v_mfma_f32_16x16x32_bf16 v[116:119], a[104:107], a[244:247], v[116:119]\n"
        "s_add_u32 s56, s16, s38\n"
        "s_addc_u32 s57, s17, 0\n"
        "v_cvt_pk_bf16_f32 v212, v112, v113\n"
        "v_mfma_f32_16x16x32_bf16 v[116:119], a[100:103], a[240:243], v[116:119]\n"
        "s_add_u32 s60, s16, s39\n"
        "s_addc_u32 s61, s17, 0\n"
        "v_cvt_pk_bf16_f32 v213, v114, v115\n"
        "v_mfma_f32_16x16x32_bf16 v[116:119], a[96:99], a[236:239], v[116:119]\n"
        "v_cvt_pk_bf16_f32 v77, v110, v111\n"
        "v_mad_i32_i24 v238, v238, -4, v250\n"
        "v_mfma_f32_16x16x32_bf16 v[120:123], a[120:123], a[236:239], 0\n"
        "v_exp_f32 v96, v96\n"
        "v_mfma_f32_16x16x32_bf16 v[120:123], a[124:127], a[240:243], v[120:123]\n"
        "s_add_u32 s47, s47, 6291456\n"
        "v_exp_f32 v97, v97\n"
        "v_mfma_f32_16x16x32_bf16 v[120:123], a[128:131], a[244:247], v[120:123]\n"
        "s_add_u32 s36, s36, 2097152\n"
        "v_cvt_pk_bf16_f32 v214, v116, v117\n"
        "v_cvt_pk_bf16_f32 v215, v118, v119\n"
        "v_mfma_f32_16x16x32_bf16 v[120:123], a[132:135], a[248:251], v[120:123]\n"
        "s_xor_b32 s48, s48, 8192\n"
        "v_exp_f32 v98, v98\n"
        "v_mfma_f32_16x16x32_bf16 v[120:123], a[136:139], a[216:219], v[120:123]\n"
        "s_waitcnt lgkmcnt(0)\n"
        "s_barrier\n"
        "v_cvt_pk_bf16_f32 v84, v96, v97\n"
        "v_mad_i32_i24 v254, v254, -12, v248\n"
        "v_mfma_f32_16x16x32_bf16 v[120:123], a[140:143], a[220:223], v[120:123]\n"
        "v_exp_f32 v99, v99\n"
        "v_mfma_f32_16x16x32_bf16 v[112:115], a[220:223], a[164:167], 0\n"
        "v_cvt_pk_bf16_f32 v85, v98, v99\n"
        "v_add_u32 v254, 1728, v254\n"
        "v_mfma_f32_16x16x32_bf16 v[112:115], a[216:219], a[160:163], v[112:115]\n"
        "v_add_u32 v238, s49, v238\n"
        "v_add_u32 v240, s31, v240\n"
        "v_mfma_f32_16x16x32_bf16 v[112:115], a[248:251], a[156:159], v[112:115]\n"
        "v_cvt_pk_bf16_f32 v230, v120, v121\n"
        "v_cvt_pk_bf16_f32 v231, v122, v123\n"
        "v_mfma_f32_16x16x32_bf16 v[112:115], a[244:247], a[152:155], v[112:115]\n"
        "v_add_u32 v241, s31, v241\n"
        "v_mfma_f32_16x16x32_bf16 v[112:115], a[240:243], a[148:151], v[112:115]\n"
        "v_mfma_f32_16x16x32_bf16 v[112:115], a[236:239], a[144:147], v[112:115]\n"
        "s_nop 2\n"
        "v_mfma_f32_16x16x32_bf16 v[100:103], v[212:215], v[156:159], v[12:15]\n"
        "s_nop 2\n"
        "v_mfma_f32_16x16x32_bf16 v[100:103], v[228:231], v[164:167], v[100:103]\n"
        "v_cvt_pk_bf16_f32 v134, v112, v113\n"
        "v_cvt_pk_bf16_f32 v135, v114, v115\n"
        "v_mfma_f32_16x16x32_bf16 v[116:119], a[236:239], a[168:171], 0\n"
        "s_waitcnt lgkmcnt(0)\n"
        "v_mfma_f32_16x16x32_bf16 v[116:119], a[240:243], a[172:175], v[116:119]\n"
        "s_bitcmp1_b32 5, s42\n"
        "v_mfma_f32_16x16x32_bf16 v[116:119], a[244:247], a[176:179], v[116:119]\n"
        "s_cbranch_scc1 12f\n"
        "11:\n"
        "10:\n"
        "v_mfma_f32_16x16x32_bf16 v[116:119], a[248:251], a[180:183], v[116:119]\n"
        "v_mfma_f32_16x16x32_bf16 v[116:119], a[216:219], a[184:187], v[116:119]\n"
        "s_nop 0\n"
        "v_exp_f32 v100, v100\n"
        "v_mfma_f32_16x16x32_bf16 v[116:119], a[220:223], a[188:191], v[116:119]\n"
        "v_exp_f32 v101, v101\n"
        "v_mfma_f32_16x16x32_bf16 v[104:107], v[212:215], v[176:179], v[28:31]\n"
        "v_exp_f32 v102, v102\n"
        "v_mfma_f32_16x16x32_bf16 v[104:107], v[228:231], v[184:187], v[104:107]\n"
        "v_exp_f32 v103, v103\n"
        "v_mfma_f32_16x16x32_bf16 v[120:123], a[220:223], a[212:215], 0\n"
        "v_cvt_pk_bf16_f32 v70, v100, v101\n"
        "v_cvt_pk_bf16_f32 v71, v102, v103\n"
        "v_mfma_f32_16x16x32_bf16 v[120:123], a[216:219], a[208:211], v[120:123]\n"
        "v_cvt_pk_bf16_f32 v142, v116, v117\n"
        "v_cvt_pk_bf16_f32 v143, v118, v119\n"
        "v_mfma_f32_16x16x32_bf16 v[120:123], a[248:251], a[204:207], v[120:123]\n"
        "v_mov_b64 v[96:97], 0\n"
        "v_mfma_f32_16x16x32_bf16 v[120:123], a[244:247], a[200:203], v[120:123]\n"
        "v_exp_f32 v104, v104\n"
        "v_mfma_f32_16x16x32_bf16 v[120:123], a[240:243], a[196:199], v[120:123]\n"
        "v_exp_f32 v105, v105\n"
        "v_mfma_f32_16x16x32_bf16 v[120:123], a[236:239], a[192:195], v[120:123]\n"
        "v_exp_f32 v106, v106\n"
        "v_mfma_f32_16x16x32_bf16 v[108:111], v[212:215], v[196:199], v[44:47]\n"
        "v_exp_f32 v107, v107\n"
        "v_mfma_f32_16x16x32_bf16 v[108:111], v[228:231], v[204:207], v[108:111]\n"
        "v_cvt_pk_bf16_f32 v78, v104, v105\n"
        "v_cvt_pk_bf16_f32 v79, v106, v107\n"
        "v_mfma_f32_16x16x32_bf16 v[112:115], a[0:3], a[236:239], 0\n"
        "s_nop 0\n"
        "v_cvt_pk_bf16_f32 v150, v120, v121\n"
        "v_cvt_pk_bf16_f32 v151, v122, v123\n"
        "v_mfma_f32_16x16x32_bf16 v[112:115], a[4:7], a[240:243], v[112:115]\n"
        "v_mfma_f32_16x16x32_bf16 v[112:115], a[8:11], a[244:247], v[112:115]\n"
        "v_exp_f32 v108, v108\n"
        "v_mfma_f32_16x16x32_bf16 v[112:115], a[12:15], a[248:251], v[112:115]\n"
        "v_exp_f32 v109, v109\n"
        "v_mfma_f32_16x16x32_bf16 v[112:115], a[16:19], a[216:219], v[112:115]\n"
        "v_exp_f32 v110, v110\n"
        "v_mfma_f32_16x16x32_bf16 v[112:115], a[20:23], a[220:223], v[112:115]\n"
        "v_exp_f32 v111, v111\n"
        "v_smfmac_f32_16x16x64_bf16 v[96:99], a[252:255], v[64:71], v237\n"
        "v_cvt_pk_bf16_f32 v86, v108, v109\n"
        "v_mfma_f32_16x16x32_bf16 v[100:103], v[132:135], v[68:71], 0\n"
        "v_mov_b64 v[124:125], 0\n"
        "v_cvt_pk_bf16_f32 v87, v110, v111\n"
        "v_mfma_f32_16x16x32_bf16 v[100:103], v[128:131], v[64:67], v[100:103]\n"
        "s_nop 0\n"
        "v_cvt_pk_bf16_f32 v216, v112, v113\n"
        "v_cvt_pk_bf16_f32 v217, v114, v115\n"
        "v_mfma_f32_16x16x32_bf16 v[104:107], v[136:139], v[64:67], 0\n"
        "ds_read_b128 v[222:225], v251 offset:4608\n"
        "v_add_f32 v97, v96, v97\n"
        "v_mfma_f32_16x16x32_bf16 v[104:107], v[140:143], v[68:71], v[104:107]\n"
        "v_rcp_f32 v96, v97\n"
        "v_mfma_f32_16x16x32_bf16 v[108:111], v[148:151], v[68:71], 0\n"
        "v_mul_f32 v100, v96, v100\n"
        "v_mul_f32 v101, v96, v101\n"
        "v_mfma_f32_16x16x32_bf16 v[108:111], v[144:147], v[64:67], v[108:111]\n"
        "v_mul_f32 v102, v96, v102\n"
        "v_mul_f32 v103, v96, v103\n"
        "v_mfma_f32_16x16x32_bf16 v[116:119], a[44:47], a[220:223], 0\n"
        "v_cvt_pk_bf16_f32 v100, v100, v101\n"
        "v_cvt_pk_bf16_f32 v101, v102, v103\n"
        "v_mfma_f32_16x16x32_bf16 v[116:119], a[40:43], a[216:219], v[116:119]\n"
        "v_mul_f32 v104, v96, v104\n"
        "v_mul_f32 v105, v96, v105\n"
        "v_mfma_f32_16x16x32_bf16 v[116:119], a[36:39], a[248:251], v[116:119]\n"
        "v_mul_f32 v106, v96, v106\n"
        "v_mul_f32 v107, v96, v107\n"
        "v_mfma_f32_16x16x32_bf16 v[116:119], a[32:35], a[244:247], v[116:119]\n"
        "v_cvt_pk_bf16_f32 v102, v104, v105\n"
        "v_cvt_pk_bf16_f32 v103, v106, v107\n"
        "v_mfma_f32_16x16x32_bf16 v[116:119], a[28:31], a[240:243], v[116:119]\n"
        "v_mul_f32 v108, v96, v108\n"
        "v_mul_f32 v109, v96, v109\n"
        "v_mfma_f32_16x16x32_bf16 v[116:119], a[24:27], a[236:239], v[116:119]\n"
        "v_mul_f32 v110, v96, v110\n"
        "v_mul_f32 v111, v96, v111\n"
        "v_smfmac_f32_16x16x64_bf16 v[124:127], a[252:255], v[72:79], v237\n"
        "v_cvt_pk_bf16_f32 v108, v108, v109\n"
        "v_mfma_f32_16x16x32_bf16 v[64:67], v[132:135], v[76:79], 0\n"
        "ds_write_b32 v245, v108 offset:4096\n"
        "v_mfma_f32_16x16x32_bf16 v[64:67], v[128:131], v[72:75], v[64:67]\n"
        "ds_read_b128 v[156:159], v247 offset:0\n"
        "v_cvt_pk_bf16_f32 v218, v116, v117\n"
        "v_mfma_f32_16x16x32_bf16 v[68:71], v[136:139], v[72:75], 0\n"
        "ds_read_b128 v[160:163], v247 offset:4096\n"
        "v_cvt_pk_bf16_f32 v219, v118, v119\n"
        "v_mfma_f32_16x16x32_bf16 v[68:71], v[140:143], v[76:79], v[68:71]\n"
        "ds_read_b128 v[164:167], v247 offset:4352\n"
        "v_cvt_pk_bf16_f32 v109, v110, v111\n"
        "v_mfma_f32_16x16x32_bf16 v[120:123], v[148:151], v[76:79], 0\n"
        "ds_write_b32 v245, v109 offset:5120\n"
        "v_mfma_f32_16x16x32_bf16 v[120:123], v[144:147], v[72:75], v[120:123]\n"
        "v_max_u32 v236, v96, v97\n"
        "v_mov_b64 v[116:117], 0\n"
        "v_mfma_f32_16x16x32_bf16 v[112:115], a[48:51], a[236:239], 0\n"
        "v_add_f32 v125, v124, v125\n"
        "v_mfma_f32_16x16x32_bf16 v[112:115], a[52:55], a[240:243], v[112:115]\n"
        "v_rcp_f32 v124, v125\n"
        "v_mfma_f32_16x16x32_bf16 v[112:115], a[56:59], a[244:247], v[112:115]\n"
        "v_mul_f32 v64, v124, v64\n"
        "v_mul_f32 v65, v124, v65\n"
        "v_mfma_f32_16x16x32_bf16 v[112:115], a[60:63], a[248:251], v[112:115]\n"
        "v_mul_f32 v66, v124, v66\n"
        "v_mul_f32 v67, v124, v67\n"
        "v_mfma_f32_16x16x32_bf16 v[112:115], a[64:67], a[216:219], v[112:115]\n"
        "v_cvt_pk_bf16_f32 v64, v64, v65\n"
        "v_cvt_pk_bf16_f32 v65, v66, v67\n"
        "v_mfma_f32_16x16x32_bf16 v[112:115], a[68:71], a[220:223], v[112:115]\n"
        "ds_read_b128 a[216:219], v248 offset:0\n"
        "v_mul_f32 v68, v124, v68\n"
        "v_smfmac_f32_16x16x64_bf16 v[116:119], a[252:255], v[80:87], v237\n"
        "ds_read_b128 a[220:223], v248 offset:3072\n"
        "v_mfma_f32_16x16x32_bf16 v[72:75], v[132:135], v[84:87], 0\n"
        "v_mul_f32 v69, v124, v69\n"
        "v_mul_f32 v70, v124, v70\n"
        "v_mfma_f32_16x16x32_bf16 v[72:75], v[128:131], v[80:83], v[72:75]\n"
        "v_cvt_pk_bf16_f32 v220, v112, v113\n"
        "v_cvt_pk_bf16_f32 v221, v114, v115\n"
        "v_mfma_f32_16x16x32_bf16 v[76:79], v[136:139], v[80:83], 0\n"
        "ds_read_b128 a[224:227], v248 offset:32\n"
        "v_cvt_pk_bf16_f32 v226, v112, v113\n"
        "v_mfma_f32_16x16x32_bf16 v[76:79], v[140:143], v[84:87], v[76:79]\n"
        "ds_read_b128 a[228:231], v248 offset:3104\n"
        "v_cvt_pk_bf16_f32 v227, v114, v115\n"
        "v_mfma_f32_16x16x32_bf16 v[96:99], v[148:151], v[84:87], 0\n"
        "v_mul_f32 v71, v124, v71\n"
        "v_cvt_pk_bf16_f32 v66, v68, v69\n"
        "v_mfma_f32_16x16x32_bf16 v[96:99], v[144:147], v[80:83], v[96:99]\n"
        "v_cvt_pk_bf16_f32 v67, v70, v71\n"
        "v_mul_f32 v120, v124, v120\n"
        "v_mfma_f32_16x16x32_bf16 v[104:107], v[152:155], v[216:219], v[48:51]\n"
        "s_waitcnt lgkmcnt(2)\n"
        "ds_read_b128 v[152:155], v247 offset:256\n"
        "v_mul_f32 v121, v124, v121\n"
        "v_mfma_f32_16x16x32_bf16 v[104:107], v[168:171], v[220:223], v[104:107]\n"
        "v_mul_f32 v122, v124, v122\n"
        "v_mul_f32 v123, v124, v123\n"
        "v_mfma_f32_16x16x32_bf16 v[108:111], v[172:175], v[216:219], v[52:55]\n"
        "v_cvt_pk_bf16_f32 v120, v120, v121\n"
        "v_cvt_pk_bf16_f32 v121, v122, v123\n"
        "v_mfma_f32_16x16x32_bf16 v[108:111], v[188:191], v[224:227], v[108:111]\n"
        "ds_read_b128 v[222:225], v252 offset:4608\n"
        "v_max3_u32 v236, v236, v124, v125\n"
        "v_mfma_f32_16x16x32_bf16 v[176:179], v[156:159], a[216:219], 0\n"
        "ds_write_b32 v245, v120 offset:4352\n"
        "v_mfma_f32_16x16x32_bf16 v[176:179], v[160:163], a[220:223], v[176:179]\n"
        "ds_write_b32 v245, v121 offset:5376\n"
        "v_mfma_f32_16x16x32_bf16 v[196:199], v[164:167], a[220:223], 0\n"
        "s_waitcnt lgkmcnt(3)\n"
        "ds_read_b128 a[232:235], v254 offset:0\n"
        "v_add_f32 v117, v116, v117\n"
        "v_mfma_f32_16x16x32_bf16 v[196:199], v[152:155], a[216:219], v[196:199]\n"
        "ds_read_b128 a[220:223], v254 offset:3072\n"
        "v_mfma_f32_16x16x32_bf16 v[200:203], v[152:155], a[224:227], 0\n"
        "ds_read_b128 a[216:219], v254 offset:32\n"
        "v_mfma_f32_16x16x32_bf16 v[200:203], v[164:167], a[228:231], v[200:203]\n"
        "v_rcp_f32 v116, v117\n"
        "v_mfma_f32_16x16x32_bf16 v[180:183], v[160:163], a[228:231], 0\n"
        "ds_read_b128 a[228:231], v254 offset:3104\n"
        "v_mul_f32 v72, v116, v72\n"
        "v_mfma_f32_16x16x32_bf16 v[180:183], v[156:159], a[224:227], v[180:183]\n"
        "ds_read_b128 a[236:239], v247 offset:8192\n"
        "v_mul_f32 v73, v116, v73\n"
        "v_mfma_f32_16x16x32_bf16 v[112:115], v[192:195], v[216:219], v[56:59]\n"
        "s_waitcnt lgkmcnt(7)\n"
        "ds_read_b128 a[224:227], v248 offset:16\n"
        "v_mul_f32 v74, v116, v74\n"
        "v_mfma_f32_16x16x32_bf16 v[112:115], v[208:211], v[220:223], v[112:115]\n"
        "ds_read_b128 a[240:243], v247 offset:12288\n"
        "v_mul_f32 v75, v116, v75\n"
        "v_mfma_f32_16x16x32_bf16 v[68:71], v[212:215], v[216:219], v[60:63]\n"
        "ds_read_b128 a[244:247], v247 offset:12544\n"
        "v_cvt_pk_bf16_f32 v72, v72, v73\n"
        "v_mfma_f32_16x16x32_bf16 v[68:71], v[228:231], v[224:227], v[68:71]\n"
        "s_waitcnt lgkmcnt(4)\n"
        "ds_read_b128 a[248:251], v247 offset:8448\n"
        "v_cvt_pk_bf16_f32 v73, v74, v75\n"
        "v_mfma_f32_16x16x32_bf16 v[184:187], v[156:159], a[232:235], 0\n"
        "v_mul_f32 v76, v116, v76\n"
        "v_mul_f32 v77, v116, v77\n"
        "v_mfma_f32_16x16x32_bf16 v[184:187], v[160:163], a[220:223], v[184:187]\n"
        "v_mul_f32 v78, v116, v78\n"
        "v_mul_f32 v79, v116, v79\n"
        "v_mfma_f32_16x16x32_bf16 v[204:207], v[164:167], a[220:223], 0\n"
        "ds_read_b128 a[220:223], v248 offset:3088\n"
        "v_cvt_pk_bf16_f32 v74, v76, v77\n"
        "v_mfma_f32_16x16x32_bf16 v[204:207], v[152:155], a[232:235], v[204:207]\n"
        "ds_read_b128 a[232:235], v254 offset:16\n"
        "v_cvt_pk_bf16_f32 v75, v78, v79\n"
        "v_mfma_f32_16x16x32_bf16 v[192:195], v[152:155], a[216:219], 0\n"
        "v_exp_f32 v104, v104\n"
        "v_mfma_f32_16x16x32_bf16 v[192:195], v[164:167], a[228:231], v[192:195]\n"
        "v_mul_f32 v96, v116, v96\n"
        "v_mul_f32 v97, v116, v97\n"
        "v_mfma_f32_16x16x32_bf16 v[172:175], v[160:163], a[228:231], 0\n"
        "ds_read_b128 a[228:231], v254 offset:3088\n"
        "v_mul_f32 v98, v116, v98\n"
        "v_mfma_f32_16x16x32_bf16 v[172:175], v[156:159], a[216:219], v[172:175]\n"
        "s_waitcnt lgkmcnt(2)\n"
        "ds_read_b128 a[216:219], v248 offset:6144\n"
        "v_mul_f32 v99, v116, v99\n"
        "v_mfma_f32_16x16x32_bf16 v[212:215], v[156:159], a[224:227], 0\n"
        "v_exp_f32 v105, v105\n"
        "v_mfma_f32_16x16x32_bf16 v[212:215], v[160:163], a[220:223], v[212:215]\n"
        "v_exp_f32 v106, v106\n"
        "v_mfma_f32_16x16x32_bf16 v[216:219], v[164:167], a[220:223], 0\n"
        "ds_read_b128 a[220:223], v248 offset:9216\n"
        "v_cvt_pk_bf16_f32 v88, v104, v105\n"
        "v_mfma_f32_16x16x32_bf16 v[216:219], v[152:155], a[224:227], v[216:219]\n"
        "s_waitcnt lgkmcnt(2)\n"
        "ds_read_b128 a[224:227], v248 offset:6176\n"
        "v_cvt_pk_bf16_f32 v96, v96, v97\n"
        "v_mfma_f32_16x16x32_bf16 v[224:227], v[152:155], a[232:235], 0\n"
        "v_exp_f32 v108, v108\n"
        "v_mfma_f32_16x16x32_bf16 v[224:227], v[164:167], a[228:231], v[224:227]\n"
        "v_cvt_pk_bf16_f32 v97, v98, v99\n"
        "v_max3_u32 v236, v236, v116, v117\n"
        "v_mfma_f32_16x16x32_bf16 v[220:223], v[160:163], a[228:231], 0\n"
        "ds_read_b128 a[228:231], v248 offset:9248\n"
        "v_mfma_f32_16x16x32_bf16 v[220:223], v[156:159], a[232:235], v[220:223]\n"
        "s_waitcnt lgkmcnt(2)\n"
        "ds_read_b128 a[232:235], v254 offset:6144\n"
        "v_mfma_f32_16x16x32_bf16 v[176:179], a[236:239], a[216:219], v[176:179]\n"
        "v_exp_f32 v107, v107\n"
        "v_mfma_f32_16x16x32_bf16 v[176:179], a[240:243], a[220:223], v[176:179]\n"
        "v_exp_f32 v109, v109\n"
        "v_mfma_f32_16x16x32_bf16 v[196:199], a[244:247], a[220:223], v[196:199]\n"
        "ds_read_b128 a[220:223], v254 offset:9216\n"
        "v_cvt_pk_bf16_f32 v89, v106, v107\n"
        "v_mfma_f32_16x16x32_bf16 v[196:199], a[248:251], a[216:219], v[196:199]\n"
        "s_waitcnt lgkmcnt(2)\n"
        "ds_read_b128 a[216:219], v254 offset:6176\n"
        "v_cvt_pk_bf16_f32 v90, v108, v109\n"
        "v_mfma_f32_16x16x32_bf16 v[200:203], a[248:251], a[224:227], v[200:203]\n"
        "ds_read_b128 v[156:159], v247 offset:16384\n"
        "v_mfma_f32_16x16x32_bf16 v[200:203], a[244:247], a[228:231], v[200:203]\n"
        "ds_read_b128 v[160:163], v247 offset:20480\n"
        "v_mfma_f32_16x16x32_bf16 v[180:183], a[240:243], a[228:231], v[180:183]\n"
        "ds_read_b128 a[228:231], v254 offset:9248\n"
        "v_mfma_f32_16x16x32_bf16 v[180:183], a[236:239], a[224:227], v[180:183]\n"
        "s_waitcnt lgkmcnt(4)\n"
        "ds_read_b128 a[224:227], v248 offset:6160\n"
        "v_mfma_f32_16x16x32_bf16 v[184:187], a[236:239], a[232:235], v[184:187]\n"
        "ds_read_b128 v[164:167], v247 offset:20736\n"
        "v_mfma_f32_16x16x32_bf16 v[184:187], a[240:243], a[220:223], v[184:187]\n"
        "ds_read_b128 v[152:155], v247 offset:16640\n"
        "v_mfma_f32_16x16x32_bf16 v[204:207], a[244:247], a[220:223], v[204:207]\n"
        "ds_read_b128 a[220:223], v248 offset:9232\n"
        "v_mfma_f32_16x16x32_bf16 v[204:207], a[248:251], a[232:235], v[204:207]\n"
        "s_waitcnt lgkmcnt(4)\n"
        "ds_read_b128 a[232:235], v254 offset:6160\n"
        "v_mfma_f32_16x16x32_bf16 v[192:195], a[248:251], a[216:219], v[192:195]\n"
        "v_exp_f32 v110, v110\n"
        "v_mfma_f32_16x16x32_bf16 v[192:195], a[244:247], a[228:231], v[192:195]\n"
        "v_exp_f32 v111, v111\n"
        "v_mfma_f32_16x16x32_bf16 v[172:175], a[240:243], a[228:231], v[172:175]\n"
        "ds_read_b128 a[228:231], v254 offset:9232\n"
        "v_cvt_pk_bf16_f32 v91, v110, v111\n"
        "v_mfma_f32_16x16x32_bf16 v[172:175], a[236:239], a[216:219], v[172:175]\n"
        "s_waitcnt lgkmcnt(2)\n"
        "ds_read_b128 a[216:219], v248 offset:12288\n"
        "v_mfma_f32_16x16x32_bf16 v[212:215], a[236:239], a[224:227], v[212:215]\n"
        "v_exp_f32 v112, v112\n"
        "v_mfma_f32_16x16x32_bf16 v[212:215], a[240:243], a[220:223], v[212:215]\n"
        "v_exp_f32 v113, v113\n"
        "v_mfma_f32_16x16x32_bf16 v[216:219], a[244:247], a[220:223], v[216:219]\n"
        ".long 0xbec200ff\n"
        "44:\n"
        "s_branch 60b\n"
        "ds_read_b128 a[220:223], v248 offset:15360\n"
        "v_cvt_pk_bf16_f32 v92, v112, v113\n"
        "v_mfma_f32_16x16x32_bf16 v[216:219], a[248:251], a[224:227], v[216:219]\n"
        "s_waitcnt lgkmcnt(2)\n"
        "ds_read_b128 a[224:227], v248 offset:12320\n"
        "v_mfma_f32_16x16x32_bf16 v[224:227], a[248:251], a[232:235], v[224:227]\n"
        "v_exp_f32 v114, v114\n"
        "v_mfma_f32_16x16x32_bf16 v[224:227], a[244:247], a[228:231], v[224:227]\n"
        "v_exp_f32 v68, v68\n"
        "v_mfma_f32_16x16x32_bf16 v[220:223], a[240:243], a[228:231], v[220:223]\n"
        "ds_read_b128 a[228:231], v248 offset:15392\n"
        "v_mfma_f32_16x16x32_bf16 v[220:223], a[236:239], a[232:235], v[220:223]\n"
        "s_waitcnt lgkmcnt(2)\n"
        "ds_read_b128 a[232:235], v254 offset:12288\n"
        "v_mfma_f32_16x16x32_bf16 v[176:179], v[156:159], a[216:219], v[176:179]\n"
        "v_exp_f32 v115, v115\n"
        "v_mfma_f32_16x16x32_bf16 v[176:179], v[160:163], a[220:223], v[176:179]\n"
        "v_exp_f32 v69, v69\n"
        "v_mfma_f32_16x16x32_bf16 v[196:199], v[164:167], a[220:223], v[196:199]\n"
        "ds_read_b128 a[220:223], v254 offset:15360\n"
        "v_cvt_pk_bf16_f32 v93, v114, v115\n"
        "v_mfma_f32_16x16x32_bf16 v[196:199], v[152:155], a[216:219], v[196:199]\n"
        "s_waitcnt lgkmcnt(2)\n"
        "ds_read_b128 a[216:219], v254 offset:12320\n"
        "v_cvt_pk_bf16_f32 v94, v68, v69\n"
        "v_mfma_f32_16x16x32_bf16 v[200:203], v[152:155], a[224:227], v[200:203]\n"
        "v_exp_f32 v70, v70\n"
        "v_mfma_f32_16x16x32_bf16 v[200:203], v[164:167], a[228:231], v[200:203]\n"
        "s_waitcnt vmcnt(0)\n"
        "v_exp_f32 v71, v71\n"
        "v_mfma_f32_16x16x32_bf16 v[180:183], v[160:163], a[228:231], v[180:183]\n"
        "ds_read_b128 a[228:231], v254 offset:15392\n"
        "v_cvt_pk_bf16_f32 v95, v70, v71\n"
        "v_mfma_f32_16x16x32_bf16 v[180:183], v[156:159], a[224:227], v[180:183]\n"
        "s_waitcnt lgkmcnt(2)\n"
        "ds_read_b128 a[224:227], v248 offset:12304\n"
        "v_mfma_f32_16x16x32_bf16 v[184:187], v[156:159], a[232:235], v[184:187]\n"
        "v_mfma_f32_16x16x32_bf16 v[184:187], v[160:163], a[220:223], v[184:187]\n"
        "s_barrier\n"
        "v_mfma_f32_16x16x32_bf16 v[204:207], v[164:167], a[220:223], v[204:207]\n"
        "ds_read_b128 a[220:223], v248 offset:15376\n"
        "v_mfma_f32_16x16x32_bf16 v[204:207], v[152:155], a[232:235], v[204:207]\n"
        "s_waitcnt lgkmcnt(2)\n"
        "ds_read_b128 a[232:235], v254 offset:12304\n"
        "v_mfma_f32_16x16x32_bf16 v[192:195], v[152:155], a[216:219], v[192:195]\n"
        "ds_read_b128 a[236:239], v240 offset:0\n"
        "v_mfma_f32_16x16x32_bf16 v[192:195], v[164:167], a[228:231], v[192:195]\n"
        "ds_read_b128 a[240:243], v241 offset:0\n"
        "v_mfma_f32_16x16x32_bf16 v[172:175], v[160:163], a[228:231], v[172:175]\n"
        "ds_read_b128 a[228:231], v254 offset:15376\n"
        "v_mfma_f32_16x16x32_bf16 v[172:175], v[156:159], a[216:219], v[172:175]\n"
        "s_waitcnt lgkmcnt(4)\n"
        "ds_read_b128 a[244:247], v240 offset:128\n"
        "v_mfma_f32_16x16x32_bf16 v[212:215], v[156:159], a[224:227], v[212:215]\n"
        "ds_read_b128 a[248:251], v241 offset:128\n"
        "v_mfma_f32_16x16x32_bf16 v[212:215], v[160:163], a[220:223], v[212:215]\n"
        "ds_read_b128 a[216:219], v240 offset:256\n"
        "v_mfma_f32_16x16x32_bf16 v[216:219], v[164:167], a[220:223], v[216:219]\n"
        "ds_read_b128 a[220:223], v241 offset:256\n"
        "v_mfma_f32_16x16x32_bf16 v[216:219], v[152:155], a[224:227], v[216:219]\n"
        "s_waitcnt lgkmcnt(4)\n"
        "v_mfma_f32_16x16x32_bf16 v[224:227], v[152:155], a[232:235], v[224:227]\n"
        "v_mfma_f32_16x16x32_bf16 v[224:227], v[164:167], a[228:231], v[224:227]\n"
        "ds_write_b32 v245, v96 offset:4608\n"
        "v_mfma_f32_16x16x32_bf16 v[220:223], v[160:163], a[228:231], v[220:223]\n"
        "v_mfma_f32_16x16x32_bf16 v[220:223], v[156:159], a[232:235], v[220:223]\n"
        "ds_write_b32 v245, v97 offset:5632\n"
        "v_mfma_f32_16x16x32_bf16 v[112:115], a[72:75], a[236:239], 0\n"
        "v_mov_b64 v[124:125], 0\n"
        "v_cvt_pk_bf16_f32 v232, v176, v180\n"
        "v_mfma_f32_16x16x32_bf16 v[112:115], a[76:79], a[240:243], v[112:115]\n"
        "s_waitcnt lgkmcnt(2)\n"
        "v_cvt_pk_bf16_f32 v233, v184, v172\n"
        "v_cvt_pk_bf16_f32 v234, v177, v181\n"
        "v_mfma_f32_16x16x32_bf16 v[112:115], a[80:83], a[244:247], v[112:115]\n"
        "buffer_store_dwordx2 v[232:233], v250, s[56:59], 0 offen offset:0 sc1\n"
        "v_cvt_pk_bf16_f32 v235, v185, v173\n"
        "v_mfma_f32_16x16x32_bf16 v[112:115], a[84:87], a[248:251], v[112:115]\n"
        "v_cvt_pk_bf16_f32 v232, v178, v182\n"
        "v_cvt_pk_bf16_f32 v233, v186, v174\n"
        "v_mfma_f32_16x16x32_bf16 v[112:115], a[88:91], a[216:219], v[112:115]\n"
        "v_mfma_f32_16x16x32_bf16 v[112:115], a[92:95], a[220:223], v[112:115]\n"
        "ds_write_b32 v245, v100 offset:0\n"
        "v_smfmac_f32_16x16x64_bf16 v[124:127], a[252:255], v[88:95], v237\n"
        "s_nop 1\n"
        "v_mfma_f32_16x16x32_bf16 v[80:83], v[132:135], v[92:95], 0\n"
        "buffer_store_dwordx2 v[234:235], v250, s[56:59], 0 offen offset:384 sc1\n"
        "s_nop 0\n"
        "v_mfma_f32_16x16x32_bf16 v[80:83], v[128:131], v[88:91], v[80:83]\n"
        "s_add_u32 s41, s30, s40\n"
        "v_cvt_pk_bf16_f32 v152, v112, v113\n"
        "v_cvt_pk_bf16_f32 v153, v114, v115\n"
        "v_mfma_f32_16x16x32_bf16 v[84:87], v[136:139], v[88:91], 0\n"
        "v_cvt_pk_bf16_f32 v234, v179, v183\n"
        "v_cvt_pk_bf16_f32 v235, v187, v175\n"
        "v_mfma_f32_16x16x32_bf16 v[84:87], v[140:143], v[92:95], v[84:87]\n"
        "v_add_f32 v125, v124, v125\n"
        "v_mfma_f32_16x16x32_bf16 v[96:99], v[148:151], v[92:95], 0\n"
        "buffer_store_dwordx2 v[232:233], v250, s[56:59], 0 offen offset:768 sc1\n"
        "v_mfma_f32_16x16x32_bf16 v[96:99], v[144:147], v[88:91], v[96:99]\n"
        "s_add_u32 s35, s47, 786432\n"
        "v_rcp_f32 v124, v125\n"
        "v_mfma_f32_16x16x32_bf16 v[116:119], a[116:119], a[220:223], 0\n"
        "s_sub_u32 s46, s35, 100663296\n"
        "v_mul_f32 v80, v124, v80\n"
        "v_mul_f32 v81, v124, v81\n"
        "v_mfma_f32_16x16x32_bf16 v[116:119], a[112:115], a[216:219], v[116:119]\n"
        "s_cmp_lt_u32 s46, 786432\n"
        "v_mul_f32 v82, v124, v82\n"
        "v_mul_f32 v83, v124, v83\n"
        "v_mfma_f32_16x16x32_bf16 v[116:119], a[108:111], a[248:251], v[116:119]\n"
        "buffer_store_dwordx2 v[234:235], v250, s[56:59], 0 offen offset:1152 sc1\n"
        "v_cvt_pk_bf16_f32 v80, v80, v81\n"
        "v_mfma_f32_16x16x32_bf16 v[116:119], a[104:107], a[244:247], v[116:119]\n"
        "s_cselect_b32 s35, s46, s35\n"
        "v_cvt_pk_bf16_f32 v81, v82, v83\n"
        "v_cvt_pk_bf16_f32 v232, v196, v200\n"
        "v_mfma_f32_16x16x32_bf16 v[116:119], a[100:103], a[240:243], v[116:119]\n"
        "v_cvt_pk_bf16_f32 v233, v204, v192\n"
        "v_mul_f32 v84, v124, v84\n"
        "v_mfma_f32_16x16x32_bf16 v[116:119], a[96:99], a[236:239], v[116:119]\n"
        "v_mul_f32 v85, v124, v85\n"
        "v_mul_f32 v86, v124, v86\n"
        "v_mfma_f32_16x16x32_bf16 v[120:123], a[120:123], a[236:239], 0\n"
        "ds_write_b32 v245, v101 offset:1024\n"
        "v_mfma_f32_16x16x32_bf16 v[120:123], a[124:127], a[240:243], v[120:123]\n"
        "buffer_store_dwordx2 v[232:233], v250, s[60:63], 0 offen offset:0 sc1\n"
        "v_mul_f32 v87, v124, v87\n"
        "v_mfma_f32_16x16x32_bf16 v[120:123], a[128:131], a[244:247], v[120:123]\n"
        "v_cvt_pk_bf16_f32 v82, v84, v85\n"
        "v_cvt_pk_bf16_f32 v83, v86, v87\n"
        "v_mfma_f32_16x16x32_bf16 v[120:123], a[132:135], a[248:251], v[120:123]\n"
        "v_mul_f32 v96, v124, v96\n"
        "v_mul_f32 v97, v124, v97\n"
        "v_mfma_f32_16x16x32_bf16 v[120:123], a[136:139], a[216:219], v[120:123]\n"
        "v_mul_f32 v98, v124, v98\n"
        "v_mul_f32 v99, v124, v99\n"
        "v_mfma_f32_16x16x32_bf16 v[120:123], a[140:143], a[220:223], v[120:123]\n"
        "v_cvt_pk_bf16_f32 v234, v197, v201\n"
        "v_cvt_pk_bf16_f32 v235, v205, v193\n"
        "v_mfma_f32_16x16x32_bf16 v[112:115], a[20:23], a[220:223], 0\n"
        "buffer_store_dwordx2 v[234:235], v250, s[60:63], 0 offen offset:384 sc1\n"
        "v_cvt_pk_bf16_f32 v154, v116, v117\n"
        "v_mfma_f32_16x16x32_bf16 v[112:115], a[16:19], a[216:219], v[112:115]\n"
        "v_cvt_pk_bf16_f32 v155, v118, v119\n"
        "v_cvt_pk_bf16_f32 v96, v96, v97\n"
        "v_mfma_f32_16x16x32_bf16 v[112:115], a[12:15], a[248:251], v[112:115]\n"
        "ds_write_b32 v245, v96 offset:4864\n"
        "v_mfma_f32_16x16x32_bf16 v[112:115], a[8:11], a[244:247], v[112:115]\n"
        "v_cvt_pk_bf16_f32 v97, v98, v99\n"
        "v_max3_u32 v236, v236, v124, v125\n"
        "v_mfma_f32_16x16x32_bf16 v[112:115], a[4:7], a[240:243], v[112:115]\n"
        "v_cvt_pk_bf16_f32 v232, v198, v202\n"
        "v_cvt_pk_bf16_f32 v233, v206, v194\n"
        "v_mfma_f32_16x16x32_bf16 v[112:115], a[0:3], a[236:239], v[112:115]\n"
        "buffer_store_dwordx2 v[232:233], v250, s[60:63], 0 offen offset:768 sc1\n"
        "v_cmp_lt_u32 vcc, s24, v236\n"
        "v_mfma_f32_16x16x32_bf16 v[116:119], a[24:27], a[236:239], 0\n"
        "v_cvt_pk_bf16_f32 v168, v120, v121\n"
        "v_cvt_pk_bf16_f32 v169, v122, v123\n"
        "v_mfma_f32_16x16x32_bf16 v[116:119], a[28:31], a[240:243], v[116:119]\n"
        "s_cmp_lg_u64 vcc, 0\n"
        "s_addc_u32 s25, s25, s25\n"
        "v_cvt_pk_bf16_f32 v234, v199, v203\n"
        "v_mfma_f32_16x16x32_bf16 v[116:119], a[32:35], a[244:247], v[116:119]\n"
        "v_cvt_pk_bf16_f32 v235, v207, v195\n"
        "v_cvt_pk_bf16_f32 v156, v112, v113\n"
        "v_mfma_f32_16x16x32_bf16 v[116:119], a[36:39], a[248:251], v[116:119]\n"
        "buffer_store_dwordx2 v[234:235], v250, s[60:63], 0 offen offset:1152 sc1\n"
        "v_cvt_pk_bf16_f32 v157, v114, v115\n"
        "v_mfma_f32_16x16x32_bf16 v[116:119], a[40:43], a[216:219], v[116:119]\n"
        "v_cvt_pk_bf16_f32 v232, v212, v220\n"
        "v_cvt_pk_bf16_f32 v234, v213, v221\n"
        "v_mfma_f32_16x16x32_bf16 v[116:119], a[44:47], a[220:223], v[116:119]\n"
        "ds_write_b32 v245, v102 offset:2048\n"
        "v_mfma_f32_16x16x32_bf16 v[120:123], a[68:71], a[220:223], 0\n"
        "v_xor_b32 v251, 0x2000, v251\n"
        "v_xor_b32 v252, 0x2000, v252\n"
        "v_mfma_f32_16x16x32_bf16 v[120:123], a[64:67], a[216:219], v[120:123]\n"
        "buffer_store_dword v232, v238, s[56:59], 0 offen offset:0 sc1\n"
        "v_mfma_f32_16x16x32_bf16 v[120:123], a[60:63], a[248:251], v[120:123]\n"
        "ds_read_b128 a[224:227], v240 offset:1536\n"
        "v_cvt_pk_bf16_f32 v158, v116, v117\n"
        "v_mfma_f32_16x16x32_bf16 v[120:123], a[56:59], a[244:247], v[120:123]\n"
        "ds_read_b128 a[228:231], v241 offset:1536\n"
        "v_cvt_pk_bf16_f32 v159, v118, v119\n"
        "v_mfma_f32_16x16x32_bf16 v[120:123], a[52:55], a[240:243], v[120:123]\n"
        "ds_read_b128 a[232:235], v240 offset:1664\n"
        "v_cvt_pk_bf16_f32 v232, v214, v222\n"
        "v_mfma_f32_16x16x32_bf16 v[120:123], a[48:51], a[236:239], v[120:123]\n"
        "buffer_store_dword v234, v238, s[56:59], 0 offen offset:384 sc1\n"
        "v_mfma_f32_16x16x32_bf16 v[112:115], a[236:239], a[144:147], 0\n"
        "v_add_u32 v247, s45, v247\n"
        "v_cvt_pk_bf16_f32 v234, v215, v223\n"
        "v_mfma_f32_16x16x32_bf16 v[112:115], a[240:243], a[148:151], v[112:115]\n"
        "ds_write_b32 v245, v103 offset:3072\n"
        "v_mfma_f32_16x16x32_bf16 v[112:115], a[244:247], a[152:155], v[112:115]\n"
        "s_nop 0\n"
        "v_cvt_pk_bf16_f32 v160, v120, v121\n"
        "v_cvt_pk_bf16_f32 v161, v122, v123\n"
        "v_mfma_f32_16x16x32_bf16 v[112:115], a[248:251], a[156:159], v[112:115]\n"
        "v_cvt_pk_bf16_f32 v166, v120, v121\n"
        "v_cvt_pk_bf16_f32 v167, v122, v123\n"
        "v_mfma_f32_16x16x32_bf16 v[112:115], a[216:219], a[160:163], v[112:115]\n"
        "buffer_store_dword v232, v238, s[56:59], 0 offen offset:768 sc1\n"
        "v_mfma_f32_16x16x32_bf16 v[112:115], a[220:223], a[164:167], v[112:115]\n"
        "ds_write_b32 v245, v97 offset:5888\n"
        "v_mfma_f32_16x16x32_bf16 v[116:119], a[220:223], a[188:191], 0\n"
        "v_cvt_pk_bf16_f32 v232, v216, v224\n"
        "s_nop 0\n"
        "v_mfma_f32_16x16x32_bf16 v[116:119], a[216:219], a[184:187], v[116:119]\n"
        "s_nop 2\n"
        "v_mfma_f32_16x16x32_bf16 v[116:119], a[248:251], a[180:183], v[116:119]\n"
        "buffer_store_dword v234, v238, s[56:59], 0 offen offset:1152 sc1\n"
        "v_mfma_f32_16x16x32_bf16 v[116:119], a[244:247], a[176:179], v[116:119]\n"
        "v_cvt_pk_bf16_f32 v128, v112, v113\n"
        "v_cvt_pk_bf16_f32 v129, v114, v115\n"
        "v_mfma_f32_16x16x32_bf16 v[116:119], a[240:243], a[172:175], v[116:119]\n"
        "ds_write_b32 v245, v64 offset:256\n"
        "v_mfma_f32_16x16x32_bf16 v[116:119], a[236:239], a[168:171], v[116:119]\n"
        "v_cvt_pk_bf16_f32 v234, v217, v225\n"
        "s_nop 0\n"
        "v_mfma_f32_16x16x32_bf16 v[120:123], a[236:239], a[192:195], 0\n"
        "ds_read_b128 a[236:239], v241 offset:1664\n"
        "s_nop 0\n"
        "v_mfma_f32_16x16x32_bf16 v[120:123], a[240:243], a[196:199], v[120:123]\n"
        "buffer_store_dword v232, v238, s[60:63], 0 offen offset:0 sc1\n"
        "v_mfma_f32_16x16x32_bf16 v[120:123], a[244:247], a[200:203], v[120:123]\n"
        "ds_read_b128 a[240:243], v240 offset:1792\n"
        "ds_read_b128 a[244:247], v241 offset:1792\n"
        "v_mfma_f32_16x16x32_bf16 v[120:123], a[248:251], a[204:207], v[120:123]\n"
        "v_cvt_pk_bf16_f32 v136, v116, v117\n"
        "v_cvt_pk_bf16_f32 v137, v118, v119\n"
        "v_mfma_f32_16x16x32_bf16 v[120:123], a[216:219], a[208:211], v[120:123]\n"
        "v_cvt_pk_bf16_f32 v232, v218, v226\n"
        "v_mfma_f32_16x16x32_bf16 v[120:123], a[220:223], a[212:215], v[120:123]\n"
        "s_waitcnt lgkmcnt(4)\n"
        "buffer_store_dword v234, v238, s[60:63], 0 offen offset:384 sc1\n"
        "v_mfma_f32_16x16x32_bf16 v[112:115], a[72:75], a[224:227], 0\n"
        "s_nop 2\n"
        "v_mfma_f32_16x16x32_bf16 v[112:115], a[76:79], a[228:231], v[112:115]\n"
        "v_cvt_pk_bf16_f32 v234, v219, v227\n"
        "s_nop 0\n"
        "v_mfma_f32_16x16x32_bf16 v[112:115], a[80:83], a[232:235], v[112:115]\n"
        "s_waitcnt lgkmcnt(0)\n"
        "v_cvt_pk_bf16_f32 v144, v120, v121\n"
        "v_cvt_pk_bf16_f32 v145, v122, v123\n"
        "v_mfma_f32_16x16x32_bf16 v[112:115], a[84:87], a[236:239], v[112:115]\n"
        "ds_write_b32 v245, v65 offset:1280\n"
        "v_mfma_f32_16x16x32_bf16 v[112:115], a[88:91], a[240:243], v[112:115]\n"
        "buffer_store_dword v232, v238, s[60:63], 0 offen offset:768 sc1\n"
        "v_mfma_f32_16x16x32_bf16 v[112:115], a[92:95], a[244:247], v[112:115]\n"
        "s_nop 2\n"
        "v_mfma_f32_16x16x32_bf16 v[116:119], a[116:119], a[244:247], 0\n"
        "s_nop 2\n"
        "v_mfma_f32_16x16x32_bf16 v[116:119], a[112:115], a[240:243], v[116:119]\n"
        "v_cvt_pk_bf16_f32 v172, v112, v113\n"
        "v_cvt_pk_bf16_f32 v173, v114, v115\n"
        "v_mfma_f32_16x16x32_bf16 v[116:119], a[108:111], a[236:239], v[116:119]\n"
        "buffer_store_dword v234, v238, s[60:63], 0 offen offset:1152 sc1\n"
        "v_mfma_f32_16x16x32_bf16 v[116:119], a[104:107], a[232:235], v[116:119]\n"
        "ds_write_b32 v245, v66 offset:2304\n"
        "v_mfma_f32_16x16x32_bf16 v[116:119], a[100:103], a[228:231], v[116:119]\n"
        "v_mfma_f32_16x16x32_bf16 v[116:119], a[96:99], a[224:227], v[116:119]\n"
        "s_mov_b32 m0, s48\n"
        "s_nop 1\n"
        "v_mfma_f32_16x16x32_bf16 v[120:123], a[120:123], a[224:227], 0\n"
        "ds_read_b128 v[162:165], v251 offset:0\n"
        "s_nop 0\n"
        "v_mfma_f32_16x16x32_bf16 v[120:123], a[124:127], a[228:231], v[120:123]\n"
        "buffer_load_dwordx4 v249, s[12:15], s36 offen offset:0 lds\n"
        "v_mfma_f32_16x16x32_bf16 v[120:123], a[128:131], a[232:235], v[120:123]\n"
        "v_cvt_pk_bf16_f32 v174, v116, v117\n"
        "v_cvt_pk_bf16_f32 v175, v118, v119\n"
        "v_mfma_f32_16x16x32_bf16 v[120:123], a[132:135], a[236:239], v[120:123]\n"
        "ds_write_b32 v245, v67 offset:3328\n"
        "v_mfma_f32_16x16x32_bf16 v[120:123], a[136:139], a[240:243], v[120:123]\n"
        "ds_write_b32 v245, v72 offset:512\n"
        "v_mfma_f32_16x16x32_bf16 v[120:123], a[140:143], a[244:247], v[120:123]\n"
        "s_nop 2\n"
        "v_mfma_f32_16x16x32_bf16 v[112:115], a[20:23], a[244:247], 0\n"
        "s_nop 2\n"
        "v_mfma_f32_16x16x32_bf16 v[112:115], a[16:19], a[240:243], v[112:115]\n"
        "v_cvt_pk_bf16_f32 v190, v120, v121\n"
        "v_cvt_pk_bf16_f32 v191, v122, v123\n"
        "v_mfma_f32_16x16x32_bf16 v[112:115], a[12:15], a[236:239], v[112:115]\n"
        "ds_write_b32 v245, v73 offset:1536\n"
        "v_mfma_f32_16x16x32_bf16 v[112:115], a[8:11], a[232:235], v[112:115]\n"
        "ds_write_b32 v245, v74 offset:2560\n"
        "v_mfma_f32_16x16x32_bf16 v[112:115], a[4:7], a[228:231], v[112:115]\n"
        "ds_read_b128 v[182:185], v251 offset:512\n"
        "v_mfma_f32_16x16x32_bf16 v[112:115], a[0:3], a[224:227], v[112:115]\n"
        "s_nop 2\n"
        "v_mfma_f32_16x16x32_bf16 v[116:119], a[24:27], a[224:227], 0\n"
        "s_nop 2\n"
        "v_mfma_f32_16x16x32_bf16 v[116:119], a[28:31], a[228:231], v[116:119]\n"
        "v_cvt_pk_bf16_f32 v176, v112, v113\n"
        "v_cvt_pk_bf16_f32 v177, v114, v115\n"
        "v_mfma_f32_16x16x32_bf16 v[116:119], a[32:35], a[232:235], v[116:119]\n"
        "ds_write_b32 v245, v75 offset:3584\n"
        "v_mfma_f32_16x16x32_bf16 v[116:119], a[36:39], a[236:239], v[116:119]\n"
        "buffer_load_dwordx4 v253, s[12:15], s36 offen offset:1024 lds\n"
        "v_mfma_f32_16x16x32_bf16 v[116:119], a[40:43], a[240:243], v[116:119]\n"
        "ds_write_b32 v245, v80 offset:768\n"
        "v_mfma_f32_16x16x32_bf16 v[116:119], a[44:47], a[244:247], v[116:119]\n"
        "s_nop 2\n"
        "v_mfma_f32_16x16x32_bf16 v[120:123], a[68:71], a[244:247], 0\n"
        "s_nop 2\n"
        "v_mfma_f32_16x16x32_bf16 v[120:123], a[64:67], a[240:243], v[120:123]\n"
        "v_cvt_pk_bf16_f32 v178, v116, v117\n"
        "v_cvt_pk_bf16_f32 v179, v118, v119\n"
        "v_mfma_f32_16x16x32_bf16 v[120:123], a[60:63], a[236:239], v[120:123]\n"
        "ds_write_b32 v245, v81 offset:1792\n"
        "v_mfma_f32_16x16x32_bf16 v[120:123], a[56:59], a[232:235], v[120:123]\n"
        "ds_write_b32 v245, v82 offset:2816\n"
        "v_mfma_f32_16x16x32_bf16 v[120:123], a[52:55], a[228:231], v[120:123]\n"
        "ds_write_b32 v245, v83 offset:3840\n"
        "v_mfma_f32_16x16x32_bf16 v[120:123], a[48:51], a[224:227], v[120:123]\n"
        "s_nop 2\n"
        "v_mfma_f32_16x16x32_bf16 v[112:115], a[224:227], a[144:147], 0\n"
        "s_nop 2\n"
        "v_mfma_f32_16x16x32_bf16 v[112:115], a[228:231], a[148:151], v[112:115]\n"
        "v_cvt_pk_bf16_f32 v180, v120, v121\n"
        "v_cvt_pk_bf16_f32 v181, v122, v123\n"
        "v_mfma_f32_16x16x32_bf16 v[112:115], a[232:235], a[152:155], v[112:115]\n"
        "v_cvt_pk_bf16_f32 v186, v120, v121\n"
        "v_cvt_pk_bf16_f32 v187, v122, v123\n"
        "v_mfma_f32_16x16x32_bf16 v[112:115], a[236:239], a[156:159], v[112:115]\n"
        "s_add_u32 m0, s41, 0\n"
        "v_mfma_f32_16x16x32_bf16 v[112:115], a[240:243], a[160:163], v[112:115]\n"
        "ds_read_b128 a[248:251], v240 offset:12288\n"
        "v_mfma_f32_16x16x32_bf16 v[112:115], a[244:247], a[164:167], v[112:115]\n"
        "buffer_load_dwordx4 v242, s[8:11], s47 offen lds\n"
        "v_mfma_f32_16x16x32_bf16 v[96:99], v[152:155], v[156:159], v[0:3]\n"
        "s_waitcnt lgkmcnt(2)\n"
        "ds_read_b128 a[216:219], v241 offset:12288\n"
        "s_nop 0\n"
        "v_mfma_f32_16x16x32_bf16 v[96:99], v[168:171], v[160:163], v[96:99]\n"
        "ds_read_b128 a[220:223], v240 offset:12416\n"
        "s_nop 0\n"
        "v_mfma_f32_16x16x32_bf16 v[100:103], v[172:175], v[156:159], v[4:7]\n"
        "s_sub_u32 s31, s30, s28\n"
        "v_cvt_pk_bf16_f32 v130, v112, v113\n"
        "v_cvt_pk_bf16_f32 v131, v114, v115\n"
        "v_mfma_f32_16x16x32_bf16 v[100:103], v[188:191], v[164:167], v[100:103]\n"
        "s_sub_u32 s45, s28, s29\n"
        "ds_read_b128 v[162:165], v252 offset:0\n"
        "v_mfma_f32_16x16x32_bf16 v[116:119], a[244:247], a[188:191], 0\n"
        "s_add_u32 s28, s28, s31\n"
        "v_exp_f32 v96, v96\n"
        "v_mfma_f32_16x16x32_bf16 v[116:119], a[240:243], a[184:187], v[116:119]\n"
        "s_mov_b32 s30, s29\n"
        "v_exp_f32 v97, v97\n"
        "v_mfma_f32_16x16x32_bf16 v[116:119], a[236:239], a[180:183], v[116:119]\n"
        "s_add_u32 s29, s29, s45\n"
        "v_exp_f32 v98, v98\n"
        "v_mfma_f32_16x16x32_bf16 v[116:119], a[232:235], a[176:179], v[116:119]\n"
        "v_exp_f32 v100, v100\n"
        "v_mfma_f32_16x16x32_bf16 v[116:119], a[228:231], a[172:175], v[116:119]\n"
        "v_exp_f32 v99, v99\n"
        "v_mfma_f32_16x16x32_bf16 v[116:119], a[224:227], a[168:171], v[116:119]\n"
        "v_exp_f32 v101, v101\n"
        "v_mfma_f32_16x16x32_bf16 v[104:107], v[152:155], v[176:179], v[16:19]\n"
        "v_exp_f32 v102, v102\n"
        "v_mfma_f32_16x16x32_bf16 v[104:107], v[168:171], v[180:183], v[104:107]\n"
        "s_add_u32 m0, s41, 1024\n"
        "v_cvt_pk_bf16_f32 v64, v96, v97\n"
        "v_cvt_pk_bf16_f32 v65, v98, v99\n"
        "v_mfma_f32_16x16x32_bf16 v[108:111], v[172:175], v[176:179], v[20:23]\n"
        "v_cvt_pk_bf16_f32 v138, v116, v117\n"
        "v_cvt_pk_bf16_f32 v139, v118, v119\n"
        "v_mfma_f32_16x16x32_bf16 v[108:111], v[188:191], v[184:187], v[108:111]\n"
        "buffer_load_dwordx4 v243, s[8:11], s47 offen lds\n"
        "v_mfma_f32_16x16x32_bf16 v[120:123], a[224:227], a[192:195], 0\n"
        "ds_read_b128 a[224:227], v241 offset:12416\n"
        "v_cvt_pk_bf16_f32 v66, v100, v101\n"
        "v_mfma_f32_16x16x32_bf16 v[120:123], a[228:231], a[196:199], v[120:123]\n"
        "ds_read_b128 a[228:231], v240 offset:12544\n"
        "v_mfma_f32_16x16x32_bf16 v[120:123], a[232:235], a[200:203], v[120:123]\n"
        "ds_read_b128 a[232:235], v241 offset:12544\n"
        "v_mfma_f32_16x16x32_bf16 v[120:123], a[236:239], a[204:207], v[120:123]\n"
        "ds_read_b128 v[202:205], v251 offset:4096\n"
        "v_mfma_f32_16x16x32_bf16 v[120:123], a[240:243], a[208:211], v[120:123]\n"
        "ds_read_b128 v[182:185], v252 offset:512\n"
        "v_mfma_f32_16x16x32_bf16 v[120:123], a[244:247], a[212:215], v[120:123]\n"
        "s_waitcnt lgkmcnt(4)\n"
        "v_exp_f32 v103, v103\n"
        "v_mfma_f32_16x16x32_bf16 v[112:115], a[0:3], a[248:251], 0\n"
        "v_cvt_pk_bf16_f32 v67, v102, v103\n"
        "s_nop 0\n"
        "v_mfma_f32_16x16x32_bf16 v[112:115], a[4:7], a[216:219], v[112:115]\n"
        "v_exp_f32 v104, v104\n"
        "v_mfma_f32_16x16x32_bf16 v[112:115], a[8:11], a[220:223], v[112:115]\n"
        "v_exp_f32 v105, v105\n"
        "v_mfma_f32_16x16x32_bf16 v[112:115], a[12:15], a[224:227], v[112:115]\n"
        "s_waitcnt lgkmcnt(0)\n"
        "v_exp_f32 v106, v106\n"
        "v_mfma_f32_16x16x32_bf16 v[112:115], a[16:19], a[228:231], v[112:115]\n"
        "v_cvt_pk_bf16_f32 v72, v104, v105\n"
        "v_cvt_pk_bf16_f32 v146, v120, v121\n"
        "v_mfma_f32_16x16x32_bf16 v[112:115], a[20:23], a[232:235], v[112:115]\n"
        "s_add_u32 m0, s41, 2048\n"
        "v_exp_f32 v108, v108\n"
        "v_mfma_f32_16x16x32_bf16 v[116:119], a[44:47], a[232:235], 0\n"
        "v_cvt_pk_bf16_f32 v147, v122, v123\n"
        "v_add_u32 v245, s45, v245\n"
        "v_mfma_f32_16x16x32_bf16 v[116:119], a[40:43], a[228:231], v[116:119]\n"
        "buffer_load_dwordx4 v244, s[8:11], s47 offen lds\n"
        "v_mfma_f32_16x16x32_bf16 v[116:119], a[36:39], a[224:227], v[116:119]\n"
        "v_exp_f32 v107, v107\n"
        "v_mfma_f32_16x16x32_bf16 v[116:119], a[32:35], a[220:223], v[116:119]\n"
        "v_exp_f32 v109, v109\n"
        "v_mfma_f32_16x16x32_bf16 v[116:119], a[28:31], a[216:219], v[116:119]\n"
        "v_cvt_pk_bf16_f32 v73, v106, v107\n"
        "v_cvt_pk_bf16_f32 v74, v108, v109\n"
        "v_mfma_f32_16x16x32_bf16 v[116:119], a[24:27], a[248:251], v[116:119]\n"
        "v_exp_f32 v110, v110\n"
        "v_mfma_f32_16x16x32_bf16 v[120:123], a[48:51], a[248:251], 0\n"
        "v_exp_f32 v111, v111\n"
        "v_mfma_f32_16x16x32_bf16 v[120:123], a[52:55], a[216:219], v[120:123]\n"
        "v_cvt_pk_bf16_f32 v75, v110, v111\n"
        "v_cvt_pk_bf16_f32 v196, v112, v113\n"
        "v_mfma_f32_16x16x32_bf16 v[120:123], a[56:59], a[220:223], v[120:123]\n"
        "v_cvt_pk_bf16_f32 v197, v114, v115\n"
        "v_cvt_pk_bf16_f32 v198, v116, v117\n"
        "v_mfma_f32_16x16x32_bf16 v[120:123], a[60:63], a[224:227], v[120:123]\n"
        "v_cvt_pk_bf16_f32 v199, v118, v119\n"
        "v_mfma_f32_16x16x32_bf16 v[120:123], a[64:67], a[228:231], v[120:123]\n"
        "v_mfma_f32_16x16x32_bf16 v[120:123], a[68:71], a[232:235], v[120:123]\n"
        "s_nop 2\n"
        "v_mfma_f32_16x16x32_bf16 v[112:115], a[92:95], a[232:235], 0\n"
        "s_nop 2\n"
        "v_mfma_f32_16x16x32_bf16 v[112:115], a[88:91], a[228:231], v[112:115]\n"
        "s_add_u32 m0, s41, 12288\n"
        "v_cvt_pk_bf16_f32 v200, v120, v121\n"
        "v_cvt_pk_bf16_f32 v201, v122, v123\n"
        "v_mfma_f32_16x16x32_bf16 v[112:115], a[84:87], a[224:227], v[112:115]\n"
        "v_cvt_pk_bf16_f32 v206, v120, v121\n"
        "v_cvt_pk_bf16_f32 v207, v122, v123\n"
        "v_mfma_f32_16x16x32_bf16 v[112:115], a[80:83], a[220:223], v[112:115]\n"
        "buffer_load_dwordx4 v242, s[8:11], s35 offen lds\n"
        "v_mfma_f32_16x16x32_bf16 v[112:115], a[76:79], a[216:219], v[112:115]\n"
        "v_mfma_f32_16x16x32_bf16 v[112:115], a[72:75], a[248:251], v[112:115]\n"
        "s_nop 2\n"
        "v_mfma_f32_16x16x32_bf16 v[96:99], v[152:155], v[196:199], v[32:35]\n"
        "s_nop 2\n"
        "v_mfma_f32_16x16x32_bf16 v[96:99], v[168:171], v[200:203], v[96:99]\n"
        "v_cvt_pk_bf16_f32 v192, v112, v113\n"
        "v_cvt_pk_bf16_f32 v193, v114, v115\n"
        "v_mfma_f32_16x16x32_bf16 v[100:103], v[172:175], v[196:199], v[36:39]\n"
        "v_mfma_f32_16x16x32_bf16 v[100:103], v[188:191], v[204:207], v[100:103]\n"
        "ds_read_b128 v[202:205], v252 offset:4096\n"
        "v_mfma_f32_16x16x32_bf16 v[116:119], a[96:99], a[248:251], 0\n"
        "s_cmp_eq_u32 s37, 100663296\n"
        "v_mfma_f32_16x16x32_bf16 v[116:119], a[100:103], a[216:219], v[116:119]\n"
        "s_cselect_b32 s46, 0, s37\n"
        "v_exp_f32 v96, v96\n"
        "v_mfma_f32_16x16x32_bf16 v[116:119], a[104:107], a[220:223], v[116:119]\n"
        "s_add_u32 s38, s46, s43\n"
        "v_exp_f32 v97, v97\n"
        "v_mfma_f32_16x16x32_bf16 v[116:119], a[108:111], a[224:227], v[116:119]\n"
        "s_add_u32 s39, s46, s44\n"
        "v_exp_f32 v98, v98\n"
        "v_mfma_f32_16x16x32_bf16 v[116:119], a[112:115], a[228:231], v[116:119]\n"
        "s_add_u32 s37, s37, 6291456\n"
        "v_exp_f32 v100, v100\n"
        "v_mfma_f32_16x16x32_bf16 v[116:119], a[116:119], a[232:235], v[116:119]\n"
        "s_add_u32 m0, s41, 13312\n"
        "v_exp_f32 v99, v99\n"
        "v_mfma_f32_16x16x32_bf16 v[120:123], a[140:143], a[232:235], 0\n"
        "v_exp_f32 v101, v101\n"
        "v_mfma_f32_16x16x32_bf16 v[120:123], a[136:139], a[228:231], v[120:123]\n"
        "buffer_load_dwordx4 v243, s[8:11], s35 offen lds\n"
        "v_mfma_f32_16x16x32_bf16 v[120:123], a[132:135], a[224:227], v[120:123]\n"
        "v_cvt_pk_bf16_f32 v80, v96, v97\n"
        "v_cvt_pk_bf16_f32 v194, v116, v117\n"
        "v_mfma_f32_16x16x32_bf16 v[120:123], a[128:131], a[220:223], v[120:123]\n"
        "s_mov_b64 s[58:59], s[18:19]\n"
        "v_cvt_pk_bf16_f32 v195, v118, v119\n"
        "v_cvt_pk_bf16_f32 v81, v98, v99\n"
        "v_mfma_f32_16x16x32_bf16 v[120:123], a[124:127], a[216:219], v[120:123]\n"
        "s_mov_b64 s[62:63], s[18:19]\n"
        "v_exp_f32 v102, v102\n"
        "v_mfma_f32_16x16x32_bf16 v[120:123], a[120:123], a[248:251], v[120:123]\n"
        "v_cvt_pk_bf16_f32 v82, v100, v101\n"
        "s_nop 0\n"
        "v_mfma_f32_16x16x32_bf16 v[112:115], a[248:251], a[144:147], 0\n"
        "v_exp_f32 v103, v103\n"
        "v_mfma_f32_16x16x32_bf16 v[112:115], a[216:219], a[148:151], v[112:115]\n"
        "v_cvt_pk_bf16_f32 v83, v102, v103\n"
        "s_nop 0\n"
        "v_mfma_f32_16x16x32_bf16 v[112:115], a[220:223], a[152:155], v[112:115]\n"
        "ds_read_b128 a[236:239], v240 offset:13824\n"
        "v_cvt_pk_bf16_f32 v208, v120, v121\n"
        "v_mfma_f32_16x16x32_bf16 v[112:115], a[224:227], a[156:159], v[112:115]\n"
        "ds_read_b128 a[240:243], v241 offset:13824\n"
        "v_cvt_pk_bf16_f32 v209, v122, v123\n"
        "v_mfma_f32_16x16x32_bf16 v[112:115], a[228:231], a[160:163], v[112:115]\n"
        "ds_read_b128 a[244:247], v240 offset:13952\n"
        "v_mfma_f32_16x16x32_bf16 v[112:115], a[232:235], a[164:167], v[112:115]\n"
        "s_nop 2\n"
        "v_mfma_f32_16x16x32_bf16 v[104:107], v[192:195], v[156:159], v[8:11]\n"
        "s_nop 2\n"
        "v_mfma_f32_16x16x32_bf16 v[104:107], v[208:211], v[160:163], v[104:107]\n"
        "s_add_u32 m0, s41, 14336\n"
        "v_cvt_pk_bf16_f32 v132, v112, v113\n"
        "v_cvt_pk_bf16_f32 v133, v114, v115\n"
        "v_mfma_f32_16x16x32_bf16 v[116:119], a[232:235], a[188:191], 0\n"
        "v_mfma_f32_16x16x32_bf16 v[116:119], a[228:231], a[184:187], v[116:119]\n"
        "buffer_load_dwordx4 v244, s[8:11], s35 offen lds\n"
        "v_mfma_f32_16x16x32_bf16 v[116:119], a[224:227], a[180:183], v[116:119]\n"
        "v_mfma_f32_16x16x32_bf16 v[116:119], a[220:223], a[176:179], v[116:119]\n"
        "v_exp_f32 v104, v104\n"
        "v_mfma_f32_16x16x32_bf16 v[116:119], a[216:219], a[172:175], v[116:119]\n"
        "v_exp_f32 v105, v105\n"
        "v_mfma_f32_16x16x32_bf16 v[116:119], a[248:251], a[168:171], v[116:119]\n"
        "v_exp_f32 v106, v106\n"
        "v_mfma_f32_16x16x32_bf16 v[108:111], v[192:195], v[176:179], v[24:27]\n"
        "v_exp_f32 v107, v107\n"
        "v_mfma_f32_16x16x32_bf16 v[108:111], v[208:211], v[180:183], v[108:111]\n"
        "v_cvt_pk_bf16_f32 v68, v104, v105\n"
        "v_cvt_pk_bf16_f32 v69, v106, v107\n"
        "v_mfma_f32_16x16x32_bf16 v[120:123], a[248:251], a[192:195], 0\n"
        "ds_read_b128 a[248:251], v241 offset:13952\n"
        "v_cvt_pk_bf16_f32 v140, v116, v117\n"
        "v_mfma_f32_16x16x32_bf16 v[120:123], a[216:219], a[196:199], v[120:123]\n"
        "ds_read_b128 a[216:219], v240 offset:14080\n"
        "v_cvt_pk_bf16_f32 v141, v118, v119\n"
        "v_mfma_f32_16x16x32_bf16 v[120:123], a[220:223], a[200:203], v[120:123]\n"
        "ds_read_b128 a[220:223], v241 offset:14080\n"
        "v_mfma_f32_16x16x32_bf16 v[120:123], a[224:227], a[204:207], v[120:123]\n"
        "v_exp_f32 v108, v108\n"
        "v_mfma_f32_16x16x32_bf16 v[120:123], a[228:231], a[208:211], v[120:123]\n"
        "v_exp_f32 v109, v109\n"
        "v_mfma_f32_16x16x32_bf16 v[120:123], a[232:235], a[212:215], v[120:123]\n"
        "v_exp_f32 v110, v110\n"
        "v_mfma_f32_16x16x32_bf16 v[96:99], v[192:195], v[196:199], v[40:43]\n"
        "s_waitcnt lgkmcnt(1)\n"
        "v_exp_f32 v111, v111\n"
        "v_mfma_f32_16x16x32_bf16 v[96:99], v[208:211], v[200:203], v[96:99]\n"
        "v_cvt_pk_bf16_f32 v76, v108, v109\n"
        "v_cvt_pk_bf16_f32 v77, v110, v111\n"
        "v_mfma_f32_16x16x32_bf16 v[112:115], a[72:75], a[236:239], 0\n"
        "s_nop 0\n"
        "v_cvt_pk_bf16_f32 v148, v120, v121\n"
        "v_cvt_pk_bf16_f32 v149, v122, v123\n"
        "v_mfma_f32_16x16x32_bf16 v[112:115], a[76:79], a[240:243], v[112:115]\n"
        "v_mfma_f32_16x16x32_bf16 v[112:115], a[80:83], a[244:247], v[112:115]\n"
        "v_exp_f32 v96, v96\n"
        "v_mfma_f32_16x16x32_bf16 v[112:115], a[84:87], a[248:251], v[112:115]\n"
        "v_exp_f32 v97, v97\n"
        "v_mfma_f32_16x16x32_bf16 v[112:115], a[88:91], a[216:219], v[112:115]\n"
        "s_waitcnt lgkmcnt(0)\n"
        "v_exp_f32 v98, v98\n"
        "v_mfma_f32_16x16x32_bf16 v[112:115], a[92:95], a[220:223], v[112:115]\n"
        "v_exp_f32 v99, v99\n"
        "v_mfma_f32_16x16x32_bf16 v[116:119], a[116:119], a[220:223], 0\n"
        "v_cvt_pk_bf16_f32 v84, v96, v97\n"
        "v_cvt_pk_bf16_f32 v85, v98, v99\n"
        "v_mfma_f32_16x16x32_bf16 v[116:119], a[112:115], a[216:219], v[116:119]\n"
        "v_add_u32 v240, s31, v240\n"
        "v_add_u32 v241, s31, v241\n"
        "v_mfma_f32_16x16x32_bf16 v[116:119], a[108:111], a[248:251], v[116:119]\n"
        "v_cvt_pk_bf16_f32 v212, v112, v113\n"
        "v_cvt_pk_bf16_f32 v213, v114, v115\n"
        "v_mfma_f32_16x16x32_bf16 v[116:119], a[104:107], a[244:247], v[116:119]\n"
        "s_add_u32 s56, s16, s38\n"
        "s_addc_u32 s57, s17, 0\n"
        "v_mfma_f32_16x16x32_bf16 v[116:119], a[100:103], a[240:243], v[116:119]\n"
        "s_add_u32 s60, s16, s39\n"
        "s_addc_u32 s61, s17, 0\n"
        "v_mfma_f32_16x16x32_bf16 v[116:119], a[96:99], a[236:239], v[116:119]\n"
        "s_nop 2\n"
        "v_mfma_f32_16x16x32_bf16 v[120:123], a[120:123], a[236:239], 0\n"
        "s_nop 2\n"
        "v_mfma_f32_16x16x32_bf16 v[120:123], a[124:127], a[240:243], v[120:123]\n"
        "s_add_u32 s47, s47, 6291456\n"
        "v_cvt_pk_bf16_f32 v214, v116, v117\n"
        "v_cvt_pk_bf16_f32 v215, v118, v119\n"
        "v_mfma_f32_16x16x32_bf16 v[120:123], a[128:131], a[244:247], v[120:123]\n"
        "s_add_u32 s36, s36, 2097152\n"
        "v_mfma_f32_16x16x32_bf16 v[120:123], a[132:135], a[248:251], v[120:123]\n"
        ".long 0xbec200ff\n"
        "45:\n"
        "s_branch 61b\n"
        "s_xor_b32 s48, s48, 8192\n"
        "v_mfma_f32_16x16x32_bf16 v[120:123], a[136:139], a[216:219], v[120:123]\n"
        "v_mfma_f32_16x16x32_bf16 v[120:123], a[140:143], a[220:223], v[120:123]\n"
        "s_nop 2\n"
        "v_mfma_f32_16x16x32_bf16 v[112:115], a[220:223], a[164:167], 0\n"
        "s_nop 2\n"
        "v_mfma_f32_16x16x32_bf16 v[112:115], a[216:219], a[160:163], v[112:115]\n"
        "s_barrier\n"
        "v_cvt_pk_bf16_f32 v230, v120, v121\n"
        "v_cvt_pk_bf16_f32 v231, v122, v123\n"
        "v_mfma_f32_16x16x32_bf16 v[112:115], a[248:251], a[156:159], v[112:115]\n"
        "v_mfma_f32_16x16x32_bf16 v[112:115], a[244:247], a[152:155], v[112:115]\n"
        "v_mfma_f32_16x16x32_bf16 v[112:115], a[240:243], a[148:151], v[112:115]\n"
        "v_mfma_f32_16x16x32_bf16 v[112:115], a[236:239], a[144:147], v[112:115]\n"
        "s_nop 2\n"
        "v_mfma_f32_16x16x32_bf16 v[100:103], v[212:215], v[156:159], v[12:15]\n"
        "s_nop 2\n"
        "v_mfma_f32_16x16x32_bf16 v[100:103], v[228:231], v[164:167], v[100:103]\n"
        "v_cvt_pk_bf16_f32 v134, v112, v113\n"
        "v_cvt_pk_bf16_f32 v135, v114, v115\n"
        "v_mfma_f32_16x16x32_bf16 v[116:119], a[236:239], a[168:171], 0\n"
        "s_sub_u32 s27, s27, 1\n"
        "v_mfma_f32_16x16x32_bf16 v[116:119], a[240:243], a[172:175], v[116:119]\n"
        "s_cmp_lg_u32 s27, 0\n"
        "v_mfma_f32_16x16x32_bf16 v[116:119], a[244:247], a[176:179], v[116:119]\n"
        "s_cbranch_scc1 10b\n"
        "s_branch 19f\n"
        ".p2align 6\n"
        "51:\n"
        "s_branch 61b\n"
        "s_nop 0\n"
        "s_nop 0\n"
        "s_nop 0\n"
        "s_nop 0\n"
        "s_nop 0\n"
        "12:\n"
        "10:\n"
        "v_mfma_f32_16x16x32_bf16 v[116:119], a[248:251], a[180:183], v[116:119]\n"
        "v_mfma_f32_16x16x32_bf16 v[116:119], a[216:219], a[184:187], v[116:119]\n"
        "s_nop 0\n"
        "v_exp_f32 v100, v100\n"
        "v_mfma_f32_16x16x32_bf16 v[116:119], a[220:223], a[188:191], v[116:119]\n"
        "v_exp_f32 v101, v101\n"
        "v_mfma_f32_16x16x32_bf16 v[104:107], v[212:215], v[176:179], v[28:31]\n"
        "v_exp_f32 v102, v102\n"
        "v_mfma_f32_16x16x32_bf16 v[104:107], v[228:231], v[184:187], v[104:107]\n"
        "v_exp_f32 v103, v103\n"
        "v_mfma_f32_16x16x32_bf16 v[120:123], a[220:223], a[212:215], 0\n"
        "v_cvt_pk_bf16_f32 v70, v100, v101\n"
        "v_cvt_pk_bf16_f32 v71, v102, v103\n"
        "v_mfma_f32_16x16x32_bf16 v[120:123], a[216:219], a[208:211], v[120:123]\n"
        "v_cvt_pk_bf16_f32 v142, v116, v117\n"
        "v_cvt_pk_bf16_f32 v143, v118, v119\n"
        "v_mfma_f32_16x16x32_bf16 v[120:123], a[248:251], a[204:207], v[120:123]\n"
        "v_mov_b64 v[96:97], 0\n"
        "v_mfma_f32_16x16x32_bf16 v[120:123], a[244:247], a[200:203], v[120:123]\n"
        "v_exp_f32 v104, v104\n"
        "v_mfma_f32_16x16x32_bf16 v[120:123], a[240:243], a[196:199], v[120:123]\n"
        "v_exp_f32 v105, v105\n"
        "v_mfma_f32_16x16x32_bf16 v[120:123], a[236:239], a[192:195], v[120:123]\n"
        "v_exp_f32 v106, v106\n"
        "v_mfma_f32_16x16x32_bf16 v[108:111], v[212:215], v[196:199], v[44:47]\n"
        "v_exp_f32 v107, v107\n"
        "v_mfma_f32_16x16x32_bf16 v[108:111], v[228:231], v[204:207], v[108:111]\n"
        "v_cvt_pk_bf16_f32 v78, v104, v105\n"
        "v_cvt_pk_bf16_f32 v79, v106, v107\n"
        "v_mfma_f32_16x16x32_bf16 v[112:115], a[0:3], a[236:239], 0\n"
        "s_nop 0\n"
        "v_cvt_pk_bf16_f32 v150, v120, v121\n"
        "v_cvt_pk_bf16_f32 v151, v122, v123\n"
        "v_mfma_f32_16x16x32_bf16 v[112:115], a[4:7], a[240:243], v[112:115]\n"
        "v_mfma_f32_16x16x32_bf16 v[112:115], a[8:11], a[244:247], v[112:115]\n"
        "v_exp_f32 v108, v108\n"
        "v_mfma_f32_16x16x32_bf16 v[112:115], a[12:15], a[248:251], v[112:115]\n"
        "v_exp_f32 v109, v109\n"
        "v_mfma_f32_16x16x32_bf16 v[112:115], a[16:19], a[216:219], v[112:115]\n"
        "v_exp_f32 v110, v110\n"
        "v_mfma_f32_16x16x32_bf16 v[112:115], a[20:23], a[220:223], v[112:115]\n"
        "v_exp_f32 v111, v111\n"
        "v_smfmac_f32_16x16x64_bf16 v[96:99], a[252:255], v[64:71], v237\n"
        "v_cvt_pk_bf16_f32 v86, v108, v109\n"
        "v_mfma_f32_16x16x32_bf16 v[100:103], v[132:135], v[68:71], 0\n"
        "v_mov_b64 v[124:125], 0\n"
        "v_cvt_pk_bf16_f32 v87, v110, v111\n"
        "v_mfma_f32_16x16x32_bf16 v[100:103], v[128:131], v[64:67], v[100:103]\n"
        "s_nop 0\n"
        "v_cvt_pk_bf16_f32 v216, v112, v113\n"
        "v_cvt_pk_bf16_f32 v217, v114, v115\n"
        "v_mfma_f32_16x16x32_bf16 v[104:107], v[136:139], v[64:67], 0\n"
        "ds_read_b128 v[222:225], v251 offset:4608\n"
        "v_add_f32 v97, v96, v97\n"
        "v_mfma_f32_16x16x32_bf16 v[104:107], v[140:143], v[68:71], v[104:107]\n"
        "v_rcp_f32 v96, v97\n"
        "v_mfma_f32_16x16x32_bf16 v[108:111], v[148:151], v[68:71], 0\n"
        "v_mul_f32 v100, v96, v100\n"
        "v_mul_f32 v101, v96, v101\n"
        "v_mfma_f32_16x16x32_bf16 v[108:111], v[144:147], v[64:67], v[108:111]\n"
        "v_mul_f32 v102, v96, v102\n"
        "v_mul_f32 v103, v96, v103\n"
        "v_mfma_f32_16x16x32_bf16 v[116:119], a[44:47], a[220:223], 0\n"
        "v_cvt_pk_bf16_f32 v100, v100, v101\n"
        "v_cvt_pk_bf16_f32 v101, v102, v103\n"
        "v_mfma_f32_16x16x32_bf16 v[116:119], a[40:43], a[216:219], v[116:119]\n"
        "v_mul_f32 v104, v96, v104\n"
        "v_mul_f32 v105, v96, v105\n"
        "v_mfma_f32_16x16x32_bf16 v[116:119], a[36:39], a[248:251], v[116:119]\n"
        "v_mul_f32 v106, v96, v106\n"
        "v_mul_f32 v107, v96, v107\n"
        "v_mfma_f32_16x16x32_bf16 v[116:119], a[32:35], a[244:247], v[116:119]\n"
        "v_cvt_pk_bf16_f32 v102, v104, v105\n"
        "v_cvt_pk_bf16_f32 v103, v106, v107\n"
        "v_mfma_f32_16x16x32_bf16 v[116:119], a[28:31], a[240:243], v[116:119]\n"
        "v_mul_f32 v108, v96, v108\n"
        "v_mul_f32 v109, v96, v109\n"
        "v_mfma_f32_16x16x32_bf16 v[116:119], a[24:27], a[236:239], v[116:119]\n"
        "v_mul_f32 v110, v96, v110\n"
        "v_mul_f32 v111, v96, v111\n"
        "v_smfmac_f32_16x16x64_bf16 v[124:127], a[252:255], v[72:79], v237\n"
        "v_cvt_pk_bf16_f32 v108, v108, v109\n"
        "v_mfma_f32_16x16x32_bf16 v[64:67], v[132:135], v[76:79], 0\n"
        "ds_write_b32 v245, v108 offset:4096\n"
        "v_mfma_f32_16x16x32_bf16 v[64:67], v[128:131], v[72:75], v[64:67]\n"
        "ds_read_b128 v[156:159], v247 offset:0\n"
        "v_cvt_pk_bf16_f32 v218, v116, v117\n"
        "v_mfma_f32_16x16x32_bf16 v[68:71], v[136:139], v[72:75], 0\n"
        "ds_read_b128 v[160:163], v247 offset:4096\n"
        "v_cvt_pk_bf16_f32 v219, v118, v119\n"
        "v_mfma_f32_16x16x32_bf16 v[68:71], v[140:143], v[76:79], v[68:71]\n"
        "ds_read_b128 v[164:167], v247 offset:4352\n"
        "v_cvt_pk_bf16_f32 v109, v110, v111\n"
        "v_mfma_f32_16x16x32_bf16 v[120:123], v[148:151], v[76:79], 0\n"
        "ds_write_b32 v245, v109 offset:5120\n"
        "v_mfma_f32_16x16x32_bf16 v[120:123], v[144:147], v[72:75], v[120:123]\n"
        "v_max_u32 v236, v96, v97\n"
        "v_mov_b64 v[116:117], 0\n"
        "v_mfma_f32_16x16x32_bf16 v[112:115], a[48:51], a[236:239], 0\n"
        "v_add_f32 v125, v124, v125\n"
        "v_mfma_f32_16x16x32_bf16 v[112:115], a[52:55], a[240:243], v[112:115]\n"
        "v_rcp_f32 v124, v125\n"
        "v_mfma_f32_16x16x32_bf16 v[112:115], a[56:59], a[244:247], v[112:115]\n"
        "v_mul_f32 v64, v124, v64\n"
        "v_mul_f32 v65, v124, v65\n"
        "v_mfma_f32_16x16x32_bf16 v[112:115], a[60:63], a[248:251], v[112:115]\n"
        "v_mul_f32 v66, v124, v66\n"
        "v_mul_f32 v67, v124, v67\n"
        "v_mfma_f32_16x16x32_bf16 v[112:115], a[64:67], a[216:219], v[112:115]\n"
        "v_cvt_pk_bf16_f32 v64, v64, v65\n"
        "v_cvt_pk_bf16_f32 v65, v66, v67\n"
        "v_mfma_f32_16x16x32_bf16 v[112:115], a[68:71], a[220:223], v[112:115]\n"
        "ds_read_b128 a[216:219], v248 offset:0\n"
        "v_mul_f32 v68, v124, v68\n"
        "v_smfmac_f32_16x16x64_bf16 v[116:119], a[252:255], v[80:87], v237\n"
        "ds_read_b128 a[220:223], v248 offset:3072\n"
        "v_mfma_f32_16x16x32_bf16 v[72:75], v[132:135], v[84:87], 0\n"
        "v_mul_f32 v69, v124, v69\n"
        "v_mul_f32 v70, v124, v70\n"
        "v_mfma_f32_16x16x32_bf16 v[72:75], v[128:131], v[80:83], v[72:75]\n"
        "v_cvt_pk_bf16_f32 v220, v112, v113\n"
        "v_cvt_pk_bf16_f32 v221, v114, v115\n"
        "v_mfma_f32_16x16x32_bf16 v[76:79], v[136:139], v[80:83], 0\n"
        "ds_read_b128 a[224:227], v248 offset:32\n"
        "v_cvt_pk_bf16_f32 v226, v112, v113\n"
        "v_mfma_f32_16x16x32_bf16 v[76:79], v[140:143], v[84:87], v[76:79]\n"
        "ds_read_b128 a[228:231], v248 offset:3104\n"
        "v_cvt_pk_bf16_f32 v227, v114, v115\n"
        "v_mfma_f32_16x16x32_bf16 v[96:99], v[148:151], v[84:87], 0\n"
        "v_mul_f32 v71, v124, v71\n"
        "v_cvt_pk_bf16_f32 v66, v68, v69\n"
        "v_mfma_f32_16x16x32_bf16 v[96:99], v[144:147], v[80:83], v[96:99]\n"
        "v_cvt_pk_bf16_f32 v67, v70, v71\n"
        "v_mul_f32 v120, v124, v120\n"
        "v_mfma_f32_16x16x32_bf16 v[104:107], v[152:155], v[216:219], v[48:51]\n"
        "s_waitcnt lgkmcnt(2)\n"
        "ds_read_b128 v[152:155], v247 offset:256\n"
        "v_mul_f32 v121, v124, v121\n"
        "v_mfma_f32_16x16x32_bf16 v[104:107], v[168:171], v[220:223], v[104:107]\n"
        "v_mul_f32 v122, v124, v122\n"
        "v_mul_f32 v123, v124, v123\n"
        "v_mfma_f32_16x16x32_bf16 v[108:111], v[172:175], v[216:219], v[52:55]\n"
        "v_cvt_pk_bf16_f32 v120, v120, v121\n"
        "v_cvt_pk_bf16_f32 v121, v122, v123\n"
        "v_mfma_f32_16x16x32_bf16 v[108:111], v[188:191], v[224:227], v[108:111]\n"
        "ds_read_b128 v[222:225], v252 offset:4608\n"
        "v_max3_u32 v236, v236, v124, v125\n"
        "v_mfma_f32_16x16x32_bf16 v[176:179], v[156:159], a[216:219], 0\n"
        "ds_write_b32 v245, v120 offset:4352\n"
        "v_mfma_f32_16x16x32_bf16 v[176:179], v[160:163], a[220:223], v[176:179]\n"
        "ds_write_b32 v245, v121 offset:5376\n"
        "v_mfma_f32_16x16x32_bf16 v[196:199], v[164:167], a[220:223], 0\n"
        "s_waitcnt lgkmcnt(3)\n"
        "ds_read_b128 a[232:235], v254 offset:0\n"
        "v_add_f32 v117, v116, v117\n"
        "v_mfma_f32_16x16x32_bf16 v[196:199], v[152:155], a[216:219], v[196:199]\n"
        "ds_read_b128 a[220:223], v254 offset:3072\n"
        "v_mfma_f32_16x16x32_bf16 v[200:203], v[152:155], a[224:227], 0\n"
        "ds_read_b128 a[216:219], v254 offset:32\n"
        "v_mfma_f32_16x16x32_bf16 v[200:203], v[164:167], a[228:231], v[200:203]\n"
        "v_rcp_f32 v116, v117\n"
        "v_mfma_f32_16x16x32_bf16 v[180:183], v[160:163], a[228:231], 0\n"
        "ds_read_b128 a[228:231], v254 offset:3104\n"
        "v_mul_f32 v72, v116, v72\n"
        "v_mfma_f32_16x16x32_bf16 v[180:183], v[156:159], a[224:227], v[180:183]\n"
        "ds_read_b128 a[236:239], v247 offset:8192\n"
        "v_mul_f32 v73, v116, v73\n"
        "v_mfma_f32_16x16x32_bf16 v[112:115], v[192:195], v[216:219], v[56:59]\n"
        "s_waitcnt lgkmcnt(7)\n"
        "ds_read_b128 a[224:227], v248 offset:16\n"
        "v_mul_f32 v74, v116, v74\n"
        "v_mfma_f32_16x16x32_bf16 v[112:115], v[208:211], v[220:223], v[112:115]\n"
        "ds_read_b128 a[240:243], v247 offset:12288\n"
        "v_mul_f32 v75, v116, v75\n"
        "v_mfma_f32_16x16x32_bf16 v[68:71], v[212:215], v[216:219], v[60:63]\n"
        "ds_read_b128 a[244:247], v247 offset:12544\n"
        "v_cvt_pk_bf16_f32 v72, v72, v73\n"
        "v_mfma_f32_16x16x32_bf16 v[68:71], v[228:231], v[224:227], v[68:71]\n"
        "s_waitcnt lgkmcnt(4)\n"
        "ds_read_b128 a[248:251], v247 offset:8448\n"
        "v_cvt_pk_bf16_f32 v73, v74, v75\n"
        "v_mfma_f32_16x16x32_bf16 v[184:187], v[156:159], a[232:235], 0\n"
        "v_mul_f32 v76, v116, v76\n"
        "v_mul_f32 v77, v116, v77\n"
        "v_mfma_f32_16x16x32_bf16 v[184:187], v[160:163], a[220:223], v[184:187]\n"
        "v_mul_f32 v78, v116, v78\n"
        "v_mul_f32 v79, v116, v79\n"
        "v_mfma_f32_16x16x32_bf16 v[204:207], v[164:167], a[220:223], 0\n"
        "ds_read_b128 a[220:223], v248 offset:3088\n"
        "v_cvt_pk_bf16_f32 v74, v76, v77\n"
        "v_mfma_f32_16x16x32_bf16 v[204:207], v[152:155], a[232:235], v[204:207]\n"
        "ds_read_b128 a[232:235], v254 offset:16\n"
        "v_cvt_pk_bf16_f32 v75, v78, v79\n"
        "v_mfma_f32_16x16x32_bf16 v[192:195], v[152:155], a[216:219], 0\n"
        "v_exp_f32 v104, v104\n"
        "v_mfma_f32_16x16x32_bf16 v[192:195], v[164:167], a[228:231], v[192:195]\n"
        "v_mul_f32 v96, v116, v96\n"
        "v_mul_f32 v97, v116, v97\n"
        "v_mfma_f32_16x16x32_bf16 v[172:175], v[160:163], a[228:231], 0\n"
        "ds_read_b128 a[228:231], v254 offset:3088\n"
        "v_mul_f32 v98, v116, v98\n"
        "v_mfma_f32_16x16x32_bf16 v[172:175], v[156:159], a[216:219], v[172:175]\n"
        "s_waitcnt lgkmcnt(2)\n"
        "ds_read_b128 a[216:219], v248 offset:6144\n"
        "v_mul_f32 v99, v116, v99\n"
        "v_mfma_f32_16x16x32_bf16 v[212:215], v[156:159], a[224:227], 0\n"
        "v_exp_f32 v105, v105\n"
        "v_mfma_f32_16x16x32_bf16 v[212:215], v[160:163], a[220:223], v[212:215]\n"
        "v_exp_f32 v106, v106\n"
        "v_mfma_f32_16x16x32_bf16 v[216:219], v[164:167], a[220:223], 0\n"
        "ds_read_b128 a[220:223], v248 offset:9216\n"
        "v_cvt_pk_bf16_f32 v88, v104, v105\n"
        "v_mfma_f32_16x16x32_bf16 v[216:219], v[152:155], a[224:227], v[216:219]\n"
        "s_waitcnt lgkmcnt(2)\n"
        "ds_read_b128 a[224:227], v248 offset:6176\n"
        "v_cvt_pk_bf16_f32 v96, v96, v97\n"
        "v_mfma_f32_16x16x32_bf16 v[224:227], v[152:155], a[232:235], 0\n"
        "v_exp_f32 v108, v108\n"
        "v_mfma_f32_16x16x32_bf16 v[224:227], v[164:167], a[228:231], v[224:227]\n"
        "v_cvt_pk_bf16_f32 v97, v98, v99\n"
        "v_max3_u32 v236, v236, v116, v117\n"
        "v_mfma_f32_16x16x32_bf16 v[220:223], v[160:163], a[228:231], 0\n"
        "ds_read_b128 a[228:231], v248 offset:9248\n"
        "v_mfma_f32_16x16x32_bf16 v[220:223], v[156:159], a[232:235], v[220:223]\n"
        "s_waitcnt lgkmcnt(2)\n"
        "ds_read_b128 a[232:235], v254 offset:6144\n"
        "v_mfma_f32_16x16x32_bf16 v[176:179], a[236:239], a[216:219], v[176:179]\n"
        "v_exp_f32 v107, v107\n"
        "v_mfma_f32_16x16x32_bf16 v[176:179], a[240:243], a[220:223], v[176:179]\n"
        "v_exp_f32 v109, v109\n"
        "v_mfma_f32_16x16x32_bf16 v[196:199], a[244:247], a[220:223], v[196:199]\n"
        "ds_read_b128 a[220:223], v254 offset:9216\n"
        "v_cvt_pk_bf16_f32 v89, v106, v107\n"
        "v_mfma_f32_16x16x32_bf16 v[196:199], a[248:251], a[216:219], v[196:199]\n"
        "s_waitcnt lgkmcnt(2)\n"
        "ds_read_b128 a[216:219], v254 offset:6176\n"
        "v_cvt_pk_bf16_f32 v90, v108, v109\n"
        "v_mfma_f32_16x16x32_bf16 v[200:203], a[248:251], a[224:227], v[200:203]\n"
        "ds_read_b128 v[156:159], v247 offset:16384\n"
        "v_mfma_f32_16x16x32_bf16 v[200:203], a[244:247], a[228:231], v[200:203]\n"
        "ds_read_b128 v[160:163], v247 offset:20480\n"
        "v_mfma_f32_16x16x32_bf16 v[180:183], a[240:243], a[228:231], v[180:183]\n"
        "ds_read_b128 a[228:231], v254 offset:9248\n"
        "v_mfma_f32_16x16x32_bf16 v[180:183], a[236:239], a[224:227], v[180:183]\n"
        "s_waitcnt lgkmcnt(4)\n"
        "ds_read_b128 a[224:227], v248 offset:6160\n"
        "v_mfma_f32_16x16x32_bf16 v[184:187], a[236:239], a[232:235], v[184:187]\n"
        "ds_read_b128 v[164:167], v247 offset:20736\n"
        "v_mfma_f32_16x16x32_bf16 v[184:187], a[240:243], a[220:223], v[184:187]\n"
        "ds_read_b128 v[152:155], v247 offset:16640\n"
        "v_mfma_f32_16x16x32_bf16 v[204:207], a[244:247], a[220:223], v[204:207]\n"
        "ds_read_b128 a[220:223], v248 offset:9232\n"
        "v_mfma_f32_16x16x32_bf16 v[204:207], a[248:251], a[232:235], v[204:207]\n"
        "s_waitcnt lgkmcnt(4)\n"
        "ds_read_b128 a[232:235], v254 offset:6160\n"
        "v_mfma_f32_16x16x32_bf16 v[192:195], a[248:251], a[216:219], v[192:195]\n"
        "v_exp_f32 v110, v110\n"
        "v_mfma_f32_16x16x32_bf16 v[192:195], a[244:247], a[228:231], v[192:195]\n"
        "v_exp_f32 v111, v111\n"
        "v_mfma_f32_16x16x32_bf16 v[172:175], a[240:243], a[228:231], v[172:175]\n"
        "ds_read_b128 a[228:231], v254 offset:9232\n"
        "v_cvt_pk_bf16_f32 v91, v110, v111\n"
        "v_mfma_f32_16x16x32_bf16 v[172:175], a[236:239], a[216:219], v[172:175]\n"
        "s_waitcnt lgkmcnt(2)\n"
        "ds_read_b128 a[216:219], v248 offset:12288\n"
        "v_mfma_f32_16x16x32_bf16 v[212:215], a[236:239], a[224:227], v[212:215]\n"
        "v_exp_f32 v112, v112\n"
        "v_mfma_f32_16x16x32_bf16 v[212:215], a[240:243], a[220:223], v[212:215]\n"
        "v_exp_f32 v113, v113\n"
        "v_mfma_f32_16x16x32_bf16 v[216:219], a[244:247], a[220:223], v[216:219]\n"
        "ds_read_b128 a[220:223], v248 offset:15360\n"
        "v_cvt_pk_bf16_f32 v92, v112, v113\n"
        "v_mfma_f32_16x16x32_bf16 v[216:219], a[248:251], a[224:227], v[216:219]\n"
        "s_waitcnt lgkmcnt(2)\n"
        "ds_read_b128 a[224:227], v248 offset:12320\n"
        "v_mfma_f32_16x16x32_bf16 v[224:227], a[248:251], a[232:235], v[224:227]\n"
        "v_exp_f32 v114, v114\n"
        "v_mfma_f32_16x16x32_bf16 v[224:227], a[244:247], a[228:231], v[224:227]\n"
        "v_exp_f32 v68, v68\n"
        "v_mfma_f32_16x16x32_bf16 v[220:223], a[240:243], a[228:231], v[220:223]\n"
        "ds_read_b128 a[228:231], v248 offset:15392\n"
        "v_mfma_f32_16x16x32_bf16 v[220:223], a[236:239], a[232:235], v[220:223]\n"
        "s_waitcnt lgkmcnt(2)\n"
        "ds_read_b128 a[232:235], v254 offset:12288\n"
        "v_mfma_f32_16x16x32_bf16 v[176:179], v[156:159], a[216:219], v[176:179]\n"
        "v_exp_f32 v115, v115\n"
        "v_mfma_f32_16x16x32_bf16 v[176:179], v[160:163], a[220:223], v[176:179]\n"
        "v_exp_f32 v69, v69\n"
        "v_mfma_f32_16x16x32_bf16 v[196:199], v[164:167], a[220:223], v[196:199]\n"
        "ds_read_b128 a[220:223], v254 offset:15360\n"
        "v_cvt_pk_bf16_f32 v93, v114, v115\n"
        "v_mfma_f32_16x16x32_bf16 v[196:199], v[152:155], a[216:219], v[196:199]\n"
        "s_waitcnt lgkmcnt(2)\n"
        "ds_read_b128 a[216:219], v254 offset:12320\n"
        "v_cvt_pk_bf16_f32 v94, v68, v69\n"
        "v_mfma_f32_16x16x32_bf16 v[200:203], v[152:155], a[224:227], v[200:203]\n"
        "v_exp_f32 v70, v70\n"
        "v_mfma_f32_16x16x32_bf16 v[200:203], v[164:167], a[228:231], v[200:203]\n"
        "s_waitcnt vmcnt(0)\n"
        "v_exp_f32 v71, v71\n"
        "v_mfma_f32_16x16x32_bf16 v[180:183], v[160:163], a[228:231], v[180:183]\n"
        "s_barrier\n"
        "ds_read_b128 a[228:231], v254 offset:15392\n"
        "v_cvt_pk_bf16_f32 v95, v70, v71\n"
        "v_mfma_f32_16x16x32_bf16 v[180:183], v[156:159], a[224:227], v[180:183]\n"
        "s_waitcnt lgkmcnt(2)\n"
        "ds_read_b128 a[224:227], v248 offset:12304\n"
        "v_mfma_f32_16x16x32_bf16 v[184:187], v[156:159], a[232:235], v[184:187]\n"
        "v_mfma_f32_16x16x32_bf16 v[184:187], v[160:163], a[220:223], v[184:187]\n"
        "v_mfma_f32_16x16x32_bf16 v[204:207], v[164:167], a[220:223], v[204:207]\n"
        "ds_read_b128 a[220:223], v248 offset:15376\n"
        "v_mfma_f32_16x16x32_bf16 v[204:207], v[152:155], a[232:235], v[204:207]\n"
        "s_waitcnt lgkmcnt(2)\n"
        "ds_read_b128 a[232:235], v254 offset:12304\n"
        "v_mfma_f32_16x16x32_bf16 v[192:195], v[152:155], a[216:219], v[192:195]\n"
        "ds_read_b128 a[236:239], v240 offset:0\n"
        "v_mfma_f32_16x16x32_bf16 v[192:195], v[164:167], a[228:231], v[192:195]\n"
        "ds_read_b128 a[240:243], v241 offset:0\n"
        "v_mfma_f32_16x16x32_bf16 v[172:175], v[160:163], a[228:231], v[172:175]\n"
        "ds_read_b128 a[228:231], v254 offset:15376\n"
        "v_mfma_f32_16x16x32_bf16 v[172:175], v[156:159], a[216:219], v[172:175]\n"
        "s_waitcnt lgkmcnt(4)\n"
        "ds_read_b128 a[244:247], v240 offset:128\n"
        "v_mfma_f32_16x16x32_bf16 v[212:215], v[156:159], a[224:227], v[212:215]\n"
        "ds_read_b128 a[248:251], v241 offset:128\n"
        "v_mfma_f32_16x16x32_bf16 v[212:215], v[160:163], a[220:223], v[212:215]\n"
        "ds_read_b128 a[216:219], v240 offset:256\n"
        "v_mfma_f32_16x16x32_bf16 v[216:219], v[164:167], a[220:223], v[216:219]\n"
        "ds_read_b128 a[220:223], v241 offset:256\n"
        "v_mfma_f32_16x16x32_bf16 v[216:219], v[152:155], a[224:227], v[216:219]\n"
        "s_waitcnt lgkmcnt(4)\n"
        "v_mfma_f32_16x16x32_bf16 v[224:227], v[152:155], a[232:235], v[224:227]\n"
        "v_mfma_f32_16x16x32_bf16 v[224:227], v[164:167], a[228:231], v[224:227]\n"
        "ds_write_b32 v245, v96 offset:4608\n"
        "v_mfma_f32_16x16x32_bf16 v[220:223], v[160:163], a[228:231], v[220:223]\n"
        "v_mfma_f32_16x16x32_bf16 v[220:223], v[156:159], a[232:235], v[220:223]\n"
        "ds_write_b32 v245, v97 offset:5632\n"
        "v_mfma_f32_16x16x32_bf16 v[112:115], a[72:75], a[236:239], 0\n"
        "v_mov_b64 v[124:125], 0\n"
        "v_cvt_pk_bf16_f32 v232, v176, v180\n"
        "v_mfma_f32_16x16x32_bf16 v[112:115], a[76:79], a[240:243], v[112:115]\n"
        "s_waitcnt lgkmcnt(2)\n"
        "v_cvt_pk_bf16_f32 v233, v184, v172\n"
        "v_cvt_pk_bf16_f32 v234, v177, v181\n"
        "v_mfma_f32_16x16x32_bf16 v[112:115], a[80:83], a[244:247], v[112:115]\n"
        "buffer_store_dwordx2 v[232:233], v250, s[56:59], 0 offen offset:0 sc1\n"
        "v_cvt_pk_bf16_f32 v235, v185, v173\n"
        "v_mfma_f32_16x16x32_bf16 v[112:115], a[84:87], a[248:251], v[112:115]\n"
        "v_cvt_pk_bf16_f32 v232, v178, v182\n"
        "v_cvt_pk_bf16_f32 v233, v186, v174\n"
        "v_mfma_f32_16x16x32_bf16 v[112:115], a[88:91], a[216:219], v[112:115]\n"
        "v_mfma_f32_16x16x32_bf16 v[112:115], a[92:95], a[220:223], v[112:115]\n"
        "ds_write_b32 v245, v100 offset:0\n"
        "v_smfmac_f32_16x16x64_bf16 v[124:127], a[252:255], v[88:95], v237\n"
        "s_nop 1\n"
        "v_mfma_f32_16x16x32_bf16 v[80:83], v[132:135], v[92:95], 0\n"
        "buffer_store_dwordx2 v[234:235], v250, s[56:59], 0 offen offset:384 sc1\n"
        "s_nop 0\n"
        "v_mfma_f32_16x16x32_bf16 v[80:83], v[128:131], v[88:91], v[80:83]\n"
        "s_add_u32 s41, s30, s40\n"
        "v_cvt_pk_bf16_f32 v152, v112, v113\n"
        "v_cvt_pk_bf16_f32 v153, v114, v115\n"
        "v_mfma_f32_16x16x32_bf16 v[84:87], v[136:139], v[88:91], 0\n"
        "v_cvt_pk_bf16_f32 v234, v179, v183\n"
        "v_cvt_pk_bf16_f32 v235, v187, v175\n"
        "v_mfma_f32_16x16x32_bf16 v[84:87], v[140:143], v[92:95], v[84:87]\n"
        "v_add_f32 v125, v124, v125\n"
        "v_mfma_f32_16x16x32_bf16 v[96:99], v[148:151], v[92:95], 0\n"
        "buffer_store_dwordx2 v[232:233], v250, s[56:59], 0 offen offset:768 sc1\n"
        "v_mfma_f32_16x16x32_bf16 v[96:99], v[144:147], v[88:91], v[96:99]\n"
        "s_add_u32 s35, s47, 786432\n"
        "v_rcp_f32 v124, v125\n"
        "v_mfma_f32_16x16x32_bf16 v[116:119], a[116:119], a[220:223], 0\n"
        "s_sub_u32 s46, s35, 100663296\n"
        "v_mul_f32 v80, v124, v80\n"
        "v_mul_f32 v81, v124, v81\n"
        "v_mfma_f32_16x16x32_bf16 v[116:119], a[112:115], a[216:219], v[116:119]\n"
        "s_cmp_lt_u32 s46, 786432\n"
        "v_mul_f32 v82, v124, v82\n"
        "v_mul_f32 v83, v124, v83\n"
        "v_mfma_f32_16x16x32_bf16 v[116:119], a[108:111], a[248:251], v[116:119]\n"
        "buffer_store_dwordx2 v[234:235], v250, s[56:59], 0 offen offset:1152 sc1\n"
        "v_cvt_pk_bf16_f32 v80, v80, v81\n"
        "v_mfma_f32_16x16x32_bf16 v[116:119], a[104:107], a[244:247], v[116:119]\n"
        "s_cselect_b32 s35, s46, s35\n"
        "v_cvt_pk_bf16_f32 v81, v82, v83\n"
        "v_cvt_pk_bf16_f32 v232, v196, v200\n"
        "v_mfma_f32_16x16x32_bf16 v[116:119], a[100:103], a[240:243], v[116:119]\n"
        "v_cvt_pk_bf16_f32 v233, v204, v192\n"
        "v_mul_f32 v84, v124, v84\n"
        "v_mfma_f32_16x16x32_bf16 v[116:119], a[96:99], a[236:239], v[116:119]\n"
        "v_mul_f32 v85, v124, v85\n"
        "v_mul_f32 v86, v124, v86\n"
        "v_mfma_f32_16x16x32_bf16 v[120:123], a[120:123], a[236:239], 0\n"
        "ds_write_b32 v245, v101 offset:1024\n"
        "v_mfma_f32_16x16x32_bf16 v[120:123], a[124:127], a[240:243], v[120:123]\n"
        "buffer_store_dwordx2 v[232:233], v250, s[60:63], 0 offen offset:0 sc1\n"
        "v_mul_f32 v87, v124, v87\n"
        "v_mfma_f32_16x16x32_bf16 v[120:123], a[128:131], a[244:247], v[120:123]\n"
        "v_cvt_pk_bf16_f32 v82, v84, v85\n"
        "v_cvt_pk_bf16_f32 v83, v86, v87\n"
        "v_mfma_f32_16x16x32_bf16 v[120:123], a[132:135], a[248:251], v[120:123]\n"
        "v_mul_f32 v96, v124, v96\n"
        "v_mul_f32 v97, v124, v97\n"
        "v_mfma_f32_16x16x32_bf16 v[120:123], a[136:139], a[216:219], v[120:123]\n"
        "v_mul_f32 v98, v124, v98\n"
        "v_mul_f32 v99, v124, v99\n"
        "v_mfma_f32_16x16x32_bf16 v[120:123], a[140:143], a[220:223], v[120:123]\n"
        "v_cvt_pk_bf16_f32 v234, v197, v201\n"
        "v_cvt_pk_bf16_f32 v235, v205, v193\n"
        "v_mfma_f32_16x16x32_bf16 v[112:115], a[20:23], a[220:223], 0\n"
        "buffer_store_dwordx2 v[234:235], v250, s[60:63], 0 offen offset:384 sc1\n"
        "v_cvt_pk_bf16_f32 v154, v116, v117\n"
        "v_mfma_f32_16x16x32_bf16 v[112:115], a[16:19], a[216:219], v[112:115]\n"
        "v_cvt_pk_bf16_f32 v155, v118, v119\n"
        "v_cvt_pk_bf16_f32 v96, v96, v97\n"
        "v_mfma_f32_16x16x32_bf16 v[112:115], a[12:15], a[248:251], v[112:115]\n"
        "ds_write_b32 v245, v96 offset:4864\n"
        "v_mfma_f32_16x16x32_bf16 v[112:115], a[8:11], a[244:247], v[112:115]\n"
        "v_cvt_pk_bf16_f32 v97, v98, v99\n"
        "v_max3_u32 v236, v236, v124, v125\n"
        "v_mfma_f32_16x16x32_bf16 v[112:115], a[4:7], a[240:243], v[112:115]\n"
        "v_cvt_pk_bf16_f32 v232, v198, v202\n"
        "v_cvt_pk_bf16_f32 v233, v206, v194\n"
        "v_mfma_f32_16x16x32_bf16 v[112:115], a[0:3], a[236:239], v[112:115]\n"
        "buffer_store_dwordx2 v[232:233], v250, s[60:63], 0 offen offset:768 sc1\n"
        "v_cmp_lt_u32 vcc, s24, v236\n"
        "v_mfma_f32_16x16x32_bf16 v[116:119], a[24:27], a[236:239], 0\n"
        "v_cvt_pk_bf16_f32 v168, v120, v121\n"
        "v_cvt_pk_bf16_f32 v169, v122, v123\n"
        "v_mfma_f32_16x16x32_bf16 v[116:119], a[28:31], a[240:243], v[116:119]\n"
        "s_cmp_lg_u64 vcc, 0\n"
        "s_addc_u32 s25, s25, s25\n"
        "v_cvt_pk_bf16_f32 v234, v199, v203\n"
        "v_mfma_f32_16x16x32_bf16 v[116:119], a[32:35], a[244:247], v[116:119]\n"
        "v_cvt_pk_bf16_f32 v235, v207, v195\n"
        "v_cvt_pk_bf16_f32 v156, v112, v113\n"
        "v_mfma_f32_16x16x32_bf16 v[116:119], a[36:39], a[248:251], v[116:119]\n"
        "buffer_store_dwordx2 v[234:235], v250, s[60:63], 0 offen offset:1152 sc1\n"
        "v_cvt_pk_bf16_f32 v157, v114, v115\n"
        "v_mfma_f32_16x16x32_bf16 v[116:119], a[40:43], a[216:219], v[116:119]\n"
        "v_cvt_pk_bf16_f32 v232, v212, v220\n"
        "v_cvt_pk_bf16_f32 v234, v213, v221\n"
        "v_mfma_f32_16x16x32_bf16 v[116:119], a[44:47], a[220:223], v[116:119]\n"
        "ds_write_b32 v245, v102 offset:2048\n"
        "v_mfma_f32_16x16x32_bf16 v[120:123], a[68:71], a[220:223], 0\n"
        "v_xor_b32 v251, 0x2000, v251\n"
        "v_xor_b32 v252, 0x2000, v252\n"
        "v_mfma_f32_16x16x32_bf16 v[120:123], a[64:67], a[216:219], v[120:123]\n"
        "buffer_store_dword v232, v238, s[56:59], 0 offen offset:0 sc1\n"
        "v_mfma_f32_16x16x32_bf16 v[120:123], a[60:63], a[248:251], v[120:123]\n"
        "ds_read_b128 a[224:227], v240 offset:1536\n"
        "v_cvt_pk_bf16_f32 v158, v116, v117\n"
        "v_mfma_f32_16x16x32_bf16 v[120:123], a[56:59], a[244:247], v[120:123]\n"
        "ds_read_b128 a[228:231], v241 offset:1536\n"
        "v_cvt_pk_bf16_f32 v159, v118, v119\n"
        "v_mfma_f32_16x16x32_bf16 v[120:123], a[52:55], a[240:243], v[120:123]\n"
        "ds_read_b128 a[232:235], v240 offset:1664\n"
        "v_cvt_pk_bf16_f32 v232, v214, v222\n"
        "v_mfma_f32_16x16x32_bf16 v[120:123], a[48:51], a[236:239], v[120:123]\n"
        "buffer_store_dword v234, v238, s[56:59], 0 offen offset:384 sc1\n"
        "v_mfma_f32_16x16x32_bf16 v[112:115], a[236:239], a[144:147], 0\n"
        "v_add_u32 v247, s45, v247\n"
        "v_cvt_pk_bf16_f32 v234, v215, v223\n"
        "v_mfma_f32_16x16x32_bf16 v[112:115], a[240:243], a[148:151], v[112:115]\n"
        "ds_write_b32 v245, v103 offset:3072\n"
        "v_mfma_f32_16x16x32_bf16 v[112:115], a[244:247], a[152:155], v[112:115]\n"
        "s_nop 0\n"
        "v_cvt_pk_bf16_f32 v160, v120, v121\n"
        "v_cvt_pk_bf16_f32 v161, v122, v123\n"
        "v_mfma_f32_16x16x32_bf16 v[112:115], a[248:251], a[156:159], v[112:115]\n"
        "v_cvt_pk_bf16_f32 v166, v120, v121\n"
        "v_cvt_pk_bf16_f32 v167, v122, v123\n"
        "v_mfma_f32_16x16x32_bf16 v[112:115], a[216:219], a[160:163], v[112:115]\n"
        "buffer_store_dword v232, v238, s[56:59], 0 offen offset:768 sc1\n"
        "v_mfma_f32_16x16x32_bf16 v[112:115], a[220:223], a[164:167], v[112:115]\n"
        "ds_write_b32 v245, v97 offset:5888\n"
        "v_mfma_f32_16x16x32_bf16 v[116:119], a[220:223], a[188:191], 0\n"
        "v_cvt_pk_bf16_f32 v232, v216, v224\n"
        "s_nop 0\n"
        "v_mfma_f32_16x16x32_bf16 v[116:119], a[216:219], a[184:187], v[116:119]\n"
        "s_nop 2\n"
        "v_mfma_f32_16x16x32_bf16 v[116:119], a[248:251], a[180:183], v[116:119]\n"
        "buffer_store_dword v234, v238, s[56:59], 0 offen offset:1152 sc1\n"
        "v_mfma_f32_16x16x32_bf16 v[116:119], a[244:247], a[176:179], v[116:119]\n"
        "v_cvt_pk_bf16_f32 v128, v112, v113\n"
        "v_cvt_pk_bf16_f32 v129, v114, v115\n"
        "v_mfma_f32_16x16x32_bf16 v[116:119], a[240:243], a[172:175], v[116:119]\n"
        "ds_write_b32 v245, v64 offset:256\n"
        "v_mfma_f32_16x16x32_bf16 v[116:119], a[236:239], a[168:171], v[116:119]\n"
        "v_cvt_pk_bf16_f32 v234, v217, v225\n"
        "s_nop 0\n"
        "v_mfma_f32_16x16x32_bf16 v[120:123], a[236:239], a[192:195], 0\n"
        "ds_read_b128 a[236:239], v241 offset:1664\n"
        "s_nop 0\n"
        "v_mfma_f32_16x16x32_bf16 v[120:123], a[240:243], a[196:199], v[120:123]\n"
        "buffer_store_dword v232, v238, s[60:63], 0 offen offset:0 sc1\n"
        "v_mfma_f32_16x16x32_bf16 v[120:123], a[244:247], a[200:203], v[120:123]\n"
        "ds_read_b128 a[240:243], v240 offset:1792\n"
        "ds_read_b128 a[244:247], v241 offset:1792\n"
        "v_mfma_f32_16x16x32_bf16 v[120:123], a[248:251], a[204:207], v[120:123]\n"
        "v_cvt_pk_bf16_f32 v136, v116, v117\n"
        "v_cvt_pk_bf16_f32 v137, v118, v119\n"
        "v_mfma_f32_16x16x32_bf16 v[120:123], a[216:219], a[208:211], v[120:123]\n"
        "v_cvt_pk_bf16_f32 v232, v218, v226\n"
        "v_mfma_f32_16x16x32_bf16 v[120:123], a[220:223], a[212:215], v[120:123]\n"
        "s_waitcnt lgkmcnt(4)\n"
        "buffer_store_dword v234, v238, s[60:63], 0 offen offset:384 sc1\n"
        "v_mfma_f32_16x16x32_bf16 v[112:115], a[72:75], a[224:227], 0\n"
        "s_nop 2\n"
        "v_mfma_f32_16x16x32_bf16 v[112:115], a[76:79], a[228:231], v[112:115]\n"
        "v_cvt_pk_bf16_f32 v234, v219, v227\n"
        "s_nop 0\n"
        "v_mfma_f32_16x16x32_bf16 v[112:115], a[80:83], a[232:235], v[112:115]\n"
        "s_waitcnt lgkmcnt(0)\n"
        "v_cvt_pk_bf16_f32 v144, v120, v121\n"
        "v_cvt_pk_bf16_f32 v145, v122, v123\n"
        "v_mfma_f32_16x16x32_bf16 v[112:115], a[84:87], a[236:239], v[112:115]\n"
        "ds_write_b32 v245, v65 offset:1280\n"
        "v_mfma_f32_16x16x32_bf16 v[112:115], a[88:91], a[240:243], v[112:115]\n"
        "buffer_store_dword v232, v238, s[60:63], 0 offen offset:768 sc1\n"
        "v_mfma_f32_16x16x32_bf16 v[112:115], a[92:95], a[244:247], v[112:115]\n"
        "s_nop 2\n"
        "v_mfma_f32_16x16x32_bf16 v[116:119], a[116:119], a[244:247], 0\n"
        "s_nop 2\n"
        "v_mfma_f32_16x16x32_bf16 v[116:119], a[112:115], a[240:243], v[116:119]\n"
        "v_cvt_pk_bf16_f32 v172, v112, v113\n"
        "v_cvt_pk_bf16_f32 v173, v114, v115\n"
        "v_mfma_f32_16x16x32_bf16 v[116:119], a[108:111], a[236:239], v[116:119]\n"
        "buffer_store_dword v234, v238, s[60:63], 0 offen offset:1152 sc1\n"
        "v_mfma_f32_16x16x32_bf16 v[116:119], a[104:107], a[232:235], v[116:119]\n"
        "ds_write_b32 v245, v66 offset:2304\n"
        "v_mfma_f32_16x16x32_bf16 v[116:119], a[100:103], a[228:231], v[116:119]\n"
        "v_mfma_f32_16x16x32_bf16 v[116:119], a[96:99], a[224:227], v[116:119]\n"
        "s_mov_b32 m0, s48\n"
        "s_nop 1\n"
        "v_mfma_f32_16x16x32_bf16 v[120:123], a[120:123], a[224:227], 0\n"
        "ds_read_b128 v[162:165], v251 offset:0\n"
        "s_nop 0\n"
        "v_mfma_f32_16x16x32_bf16 v[120:123], a[124:127], a[228:231], v[120:123]\n"
        "buffer_load_dwordx4 v249, s[12:15], s36 offen offset:0 lds\n"
        "v_mfma_f32_16x16x32_bf16 v[120:123], a[128:131], a[232:235], v[120:123]\n"
        "v_cvt_pk_bf16_f32 v174, v116, v117\n"
        "v_cvt_pk_bf16_f32 v175, v118, v119\n"
        "v_mfma_f32_16x16x32_bf16 v[120:123], a[132:135], a[236:239], v[120:123]\n"
        ".long 0xbec200ff\n"
        "46:\n"
        "s_branch 61b\n"
        "ds_write_b32 v245, v67 offset:3328\n"
        "v_mfma_f32_16x16x32_bf16 v[120:123], a[136:139], a[240:243], v[120:123]\n"
        "ds_write_b32 v245, v72 offset:512\n"
        "v_mfma_f32_16x16x32_bf16 v[120:123], a[140:143], a[244:247], v[120:123]\n"
        "s_nop 2\n"
        "v_mfma_f32_16x16x32_bf16 v[112:115], a[20:23], a[244:247], 0\n"
        "s_nop 2\n"
        "v_mfma_f32_16x16x32_bf16 v[112:115], a[16:19], a[240:243], v[112:115]\n"
        "v_cvt_pk_bf16_f32 v190, v120, v121\n"
        "v_cvt_pk_bf16_f32 v191, v122, v123\n"
        "v_mfma_f32_16x16x32_bf16 v[112:115], a[12:15], a[236:239], v[112:115]\n"
        "ds_write_b32 v245, v73 offset:1536\n"
        "v_mfma_f32_16x16x32_bf16 v[112:115], a[8:11], a[232:235], v[112:115]\n"
        "ds_write_b32 v245, v74 offset:2560\n"
        "v_mfma_f32_16x16x32_bf16 v[112:115], a[4:7], a[228:231], v[112:115]\n"
        "ds_read_b128 v[182:185], v251 offset:512\n"
        "v_mfma_f32_16x16x32_bf16 v[112:115], a[0:3], a[224:227], v[112:115]\n"
        "s_nop 2\n"
        "v_mfma_f32_16x16x32_bf16 v[116:119], a[24:27], a[224:227], 0\n"
        "s_nop 2\n"
        "v_mfma_f32_16x16x32_bf16 v[116:119], a[28:31], a[228:231], v[116:119]\n"
        "v_cvt_pk_bf16_f32 v176, v112, v113\n"
        "v_cvt_pk_bf16_f32 v177, v114, v115\n"
        "v_mfma_f32_16x16x32_bf16 v[116:119], a[32:35], a[232:235], v[116:119]\n"
        "ds_write_b32 v245, v75 offset:3584\n"
        "v_mfma_f32_16x16x32_bf16 v[116:119], a[36:39], a[236:239], v[116:119]\n"
        "buffer_load_dwordx4 v253, s[12:15], s36 offen offset:1024 lds\n"
        "v_mfma_f32_16x16x32_bf16 v[116:119], a[40:43], a[240:243], v[116:119]\n"
        "ds_write_b32 v245, v80 offset:768\n"
        "v_mfma_f32_16x16x32_bf16 v[116:119], a[44:47], a[244:247], v[116:119]\n"
        "s_nop 2\n"
        "v_mfma_f32_16x16x32_bf16 v[120:123], a[68:71], a[244:247], 0\n"
        "s_nop 2\n"
        "v_mfma_f32_16x16x32_bf16 v[120:123], a[64:67], a[240:243], v[120:123]\n"
        "v_cvt_pk_bf16_f32 v178, v116, v117\n"
        "v_cvt_pk_bf16_f32 v179, v118, v119\n"
        "v_mfma_f32_16x16x32_bf16 v[120:123], a[60:63], a[236:239], v[120:123]\n"
        "ds_write_b32 v245, v81 offset:1792\n"
        "v_mfma_f32_16x16x32_bf16 v[120:123], a[56:59], a[232:235], v[120:123]\n"
        "ds_write_b32 v245, v82 offset:2816\n"
        "v_mfma_f32_16x16x32_bf16 v[120:123], a[52:55], a[228:231], v[120:123]\n"
        "ds_write_b32 v245, v83 offset:3840\n"
        "v_mfma_f32_16x16x32_bf16 v[120:123], a[48:51], a[224:227], v[120:123]\n"
        "s_nop 2\n"
        "v_mfma_f32_16x16x32_bf16 v[112:115], a[224:227], a[144:147], 0\n"
        "s_nop 2\n"
        "v_mfma_f32_16x16x32_bf16 v[112:115], a[228:231], a[148:151], v[112:115]\n"
        "v_cvt_pk_bf16_f32 v180, v120, v121\n"
        "v_cvt_pk_bf16_f32 v181, v122, v123\n"
        "v_mfma_f32_16x16x32_bf16 v[112:115], a[232:235], a[152:155], v[112:115]\n"
        "v_cvt_pk_bf16_f32 v186, v120, v121\n"
        "v_cvt_pk_bf16_f32 v187, v122, v123\n"
        "v_mfma_f32_16x16x32_bf16 v[112:115], a[236:239], a[156:159], v[112:115]\n"
        "s_add_u32 m0, s41, 0\n"
        "v_mfma_f32_16x16x32_bf16 v[112:115], a[240:243], a[160:163], v[112:115]\n"
        "ds_read_b128 a[248:251], v240 offset:12288\n"
        "v_mfma_f32_16x16x32_bf16 v[112:115], a[244:247], a[164:167], v[112:115]\n"
        "buffer_load_dwordx4 v242, s[8:11], s47 offen lds\n"
        "v_mfma_f32_16x16x32_bf16 v[96:99], v[152:155], v[156:159], v[0:3]\n"
        "s_waitcnt lgkmcnt(2)\n"
        "ds_read_b128 a[216:219], v241 offset:12288\n"
        "s_nop 0\n"
        "v_mfma_f32_16x16x32_bf16 v[96:99], v[168:171], v[160:163], v[96:99]\n"
        "ds_read_b128 a[220:223], v240 offset:12416\n"
        "s_nop 0\n"
        "v_mfma_f32_16x16x32_bf16 v[100:103], v[172:175], v[156:159], v[4:7]\n"
        "s_sub_u32 s31, s30, s28\n"
        "v_cvt_pk_bf16_f32 v130, v112, v113\n"
        "v_cvt_pk_bf16_f32 v131, v114, v115\n"
        "v_mfma_f32_16x16x32_bf16 v[100:103], v[188:191], v[164:167], v[100:103]\n"
        "s_sub_u32 s45, s28, s29\n"
        "ds_read_b128 v[162:165], v252 offset:0\n"
        "v_mfma_f32_16x16x32_bf16 v[116:119], a[244:247], a[188:191], 0\n"
        "s_add_u32 s28, s28, s31\n"
        "v_exp_f32 v96, v96\n"
        "v_mfma_f32_16x16x32_bf16 v[116:119], a[240:243], a[184:187], v[116:119]\n"
        "s_mov_b32 s30, s29\n"
        "v_exp_f32 v97, v97\n"
        "v_mfma_f32_16x16x32_bf16 v[116:119], a[236:239], a[180:183], v[116:119]\n"
        "s_add_u32 s29, s29, s45\n"
        "v_exp_f32 v98, v98\n"
        "v_mfma_f32_16x16x32_bf16 v[116:119], a[232:235], a[176:179], v[116:119]\n"
        "v_exp_f32 v100, v100\n"
        "v_mfma_f32_16x16x32_bf16 v[116:119], a[228:231], a[172:175], v[116:119]\n"
        "v_exp_f32 v99, v99\n"
        "v_mfma_f32_16x16x32_bf16 v[116:119], a[224:227], a[168:171], v[116:119]\n"
        "v_exp_f32 v101, v101\n"
        "v_mfma_f32_16x16x32_bf16 v[104:107], v[152:155], v[176:179], v[16:19]\n"
        "v_exp_f32 v102, v102\n"
        "v_mfma_f32_16x16x32_bf16 v[104:107], v[168:171], v[180:183], v[104:107]\n"
        "s_add_u32 m0, s41, 1024\n"
        "v_cvt_pk_bf16_f32 v64, v96, v97\n"
        "v_cvt_pk_bf16_f32 v65, v98, v99\n"
        "v_mfma_f32_16x16x32_bf16 v[108:111], v[172:175], v[176:179], v[20:23]\n"
        "v_cvt_pk_bf16_f32 v138, v116, v117\n"
        "v_cvt_pk_bf16_f32 v139, v118, v119\n"
        "v_mfma_f32_16x16x32_bf16 v[108:111], v[188:191], v[184:187], v[108:111]\n"
        "buffer_load_dwordx4 v243, s[8:11], s47 offen lds\n"
        "v_mfma_f32_16x16x32_bf16 v[120:123], a[224:227], a[192:195], 0\n"
        "ds_read_b128 a[224:227], v241 offset:12416\n"
        "v_cvt_pk_bf16_f32 v66, v100, v101\n"
        "v_mfma_f32_16x16x32_bf16 v[120:123], a[228:231], a[196:199], v[120:123]\n"
        "ds_read_b128 a[228:231], v240 offset:12544\n"
        "v_mfma_f32_16x16x32_bf16 v[120:123], a[232:235], a[200:203], v[120:123]\n"
        "ds_read_b128 a[232:235], v241 offset:12544\n"
        "v_mfma_f32_16x16x32_bf16 v[120:123], a[236:239], a[204:207], v[120:123]\n"
        "ds_read_b128 v[202:205], v251 offset:4096\n"
        "v_mfma_f32_16x16x32_bf16 v[120:123], a[240:243], a[208:211], v[120:123]\n"
        "ds_read_b128 v[182:185], v252 offset:512\n"
        "v_mfma_f32_16x16x32_bf16 v[120:123], a[244:247], a[212:215], v[120:123]\n"
        "s_waitcnt lgkmcnt(4)\n"
        "v_exp_f32 v103, v103\n"
        "v_mfma_f32_16x16x32_bf16 v[112:115], a[0:3], a[248:251], 0\n"
        "v_cvt_pk_bf16_f32 v67, v102, v103\n"
        "s_nop 0\n"
        "v_mfma_f32_16x16x32_bf16 v[112:115], a[4:7], a[216:219], v[112:115]\n"
        "v_exp_f32 v104, v104\n"
        "v_mfma_f32_16x16x32_bf16 v[112:115], a[8:11], a[220:223], v[112:115]\n"
        "v_exp_f32 v105, v105\n"
        "v_mfma_f32_16x16x32_bf16 v[112:115], a[12:15], a[224:227], v[112:115]\n"
        "s_waitcnt lgkmcnt(0)\n"
        "v_exp_f32 v106, v106\n"
        "v_mfma_f32_16x16x32_bf16 v[112:115], a[16:19], a[228:231], v[112:115]\n"
        "v_cvt_pk_bf16_f32 v72, v104, v105\n"
        "v_cvt_pk_bf16_f32 v146, v120, v121\n"
        "v_mfma_f32_16x16x32_bf16 v[112:115], a[20:23], a[232:235], v[112:115]\n"
        "s_add_u32 m0, s41, 2048\n"
        "v_exp_f32 v108, v108\n"
        "v_mfma_f32_16x16x32_bf16 v[116:119], a[44:47], a[232:235], 0\n"
        "v_cvt_pk_bf16_f32 v147, v122, v123\n"
        "v_add_u32 v245, s45, v245\n"
        "v_mfma_f32_16x16x32_bf16 v[116:119], a[40:43], a[228:231], v[116:119]\n"
        "buffer_load_dwordx4 v244, s[8:11], s47 offen lds\n"
        "v_mfma_f32_16x16x32_bf16 v[116:119], a[36:39], a[224:227], v[116:119]\n"
        "v_exp_f32 v107, v107\n"
        "v_mfma_f32_16x16x32_bf16 v[116:119], a[32:35], a[220:223], v[116:119]\n"
        "v_exp_f32 v109, v109\n"
        "v_mfma_f32_16x16x32_bf16 v[116:119], a[28:31], a[216:219], v[116:119]\n"
        "v_cvt_pk_bf16_f32 v73, v106, v107\n"
        "v_cvt_pk_bf16_f32 v74, v108, v109\n"
        "v_mfma_f32_16x16x32_bf16 v[116:119], a[24:27], a[248:251], v[116:119]\n"
        "v_exp_f32 v110, v110\n"
        "v_mfma_f32_16x16x32_bf16 v[120:123], a[48:51], a[248:251], 0\n"
        "v_exp_f32 v111, v111\n"
        "v_mfma_f32_16x16x32_bf16 v[120:123], a[52:55], a[216:219], v[120:123]\n"
        "v_cvt_pk_bf16_f32 v75, v110, v111\n"
        "v_cvt_pk_bf16_f32 v196, v112, v113\n"
        "v_mfma_f32_16x16x32_bf16 v[120:123], a[56:59], a[220:223], v[120:123]\n"
        "v_cvt_pk_bf16_f32 v197, v114, v115\n"
        "v_cvt_pk_bf16_f32 v198, v116, v117\n"
        "v_mfma_f32_16x16x32_bf16 v[120:123], a[60:63], a[224:227], v[120:123]\n"
        "v_cvt_pk_bf16_f32 v199, v118, v119\n"
        "v_mfma_f32_16x16x32_bf16 v[120:123], a[64:67], a[228:231], v[120:123]\n"
        "v_mfma_f32_16x16x32_bf16 v[120:123], a[68:71], a[232:235], v[120:123]\n"
        "s_nop 2\n"
        "v_mfma_f32_16x16x32_bf16 v[112:115], a[92:95], a[232:235], 0\n"
        "s_nop 2\n"
        "v_mfma_f32_16x16x32_bf16 v[112:115], a[88:91], a[228:231], v[112:115]\n"
        "s_add_u32 m0, s41, 12288\n"
        "v_cvt_pk_bf16_f32 v200, v120, v121\n"
        "v_cvt_pk_bf16_f32 v201, v122, v123\n"
        "v_mfma_f32_16x16x32_bf16 v[112:115], a[84:87], a[224:227], v[112:115]\n"
        "v_cvt_pk_bf16_f32 v206, v120, v121\n"
        "v_cvt_pk_bf16_f32 v207, v122, v123\n"
        "v_mfma_f32_16x16x32_bf16 v[112:115], a[80:83], a[220:223], v[112:115]\n"
        "buffer_load_dwordx4 v242, s[8:11], s35 offen lds\n"
        "v_mfma_f32_16x16x32_bf16 v[112:115], a[76:79], a[216:219], v[112:115]\n"
        "v_mfma_f32_16x16x32_bf16 v[112:115], a[72:75], a[248:251], v[112:115]\n"
        "s_nop 2\n"
        "v_mfma_f32_16x16x32_bf16 v[96:99], v[152:155], v[196:199], v[32:35]\n"
        "s_nop 2\n"
        "v_mfma_f32_16x16x32_bf16 v[96:99], v[168:171], v[200:203], v[96:99]\n"
        "v_cvt_pk_bf16_f32 v192, v112, v113\n"
        "v_cvt_pk_bf16_f32 v193, v114, v115\n"
        "v_mfma_f32_16x16x32_bf16 v[100:103], v[172:175], v[196:199], v[36:39]\n"
        "v_mfma_f32_16x16x32_bf16 v[100:103], v[188:191], v[204:207], v[100:103]\n"
        "ds_read_b128 v[202:205], v252 offset:4096\n"
        "v_mfma_f32_16x16x32_bf16 v[116:119], a[96:99], a[248:251], 0\n"
        "s_cmp_eq_u32 s37, 100663296\n"
        "v_mfma_f32_16x16x32_bf16 v[116:119], a[100:103], a[216:219], v[116:119]\n"
        "s_cselect_b32 s46, 0, s37\n"
        "v_exp_f32 v96, v96\n"
        "v_mfma_f32_16x16x32_bf16 v[116:119], a[104:107], a[220:223], v[116:119]\n"
        "s_add_u32 s38, s46, s43\n"
        "v_exp_f32 v97, v97\n"
        "v_mfma_f32_16x16x32_bf16 v[116:119], a[108:111], a[224:227], v[116:119]\n"
        "s_add_u32 s39, s46, s44\n"
        "v_exp_f32 v98, v98\n"
        "v_mfma_f32_16x16x32_bf16 v[116:119], a[112:115], a[228:231], v[116:119]\n"
        "s_add_u32 s37, s37, 6291456\n"
        "v_exp_f32 v100, v100\n"
        "v_mfma_f32_16x16x32_bf16 v[116:119], a[116:119], a[232:235], v[116:119]\n"
        "s_add_u32 m0, s41, 13312\n"
        "v_exp_f32 v99, v99\n"
        "v_mfma_f32_16x16x32_bf16 v[120:123], a[140:143], a[232:235], 0\n"
        "v_exp_f32 v101, v101\n"
        "v_mfma_f32_16x16x32_bf16 v[120:123], a[136:139], a[228:231], v[120:123]\n"
        "buffer_load_dwordx4 v243, s[8:11], s35 offen lds\n"
        "v_mfma_f32_16x16x32_bf16 v[120:123], a[132:135], a[224:227], v[120:123]\n"
        "v_cvt_pk_bf16_f32 v80, v96, v97\n"
        "v_cvt_pk_bf16_f32 v194, v116, v117\n"
        "v_mfma_f32_16x16x32_bf16 v[120:123], a[128:131], a[220:223], v[120:123]\n"
        "s_mov_b64 s[58:59], s[18:19]\n"
        "v_cvt_pk_bf16_f32 v195, v118, v119\n"
        "v_cvt_pk_bf16_f32 v81, v98, v99\n"
        "v_mfma_f32_16x16x32_bf16 v[120:123], a[124:127], a[216:219], v[120:123]\n"
        "s_mov_b64 s[62:63], s[18:19]\n"
        "v_exp_f32 v102, v102\n"
        "v_mfma_f32_16x16x32_bf16 v[120:123], a[120:123], a[248:251], v[120:123]\n"
        "v_cvt_pk_bf16_f32 v82, v100, v101\n"
        "s_nop 0\n"
        "v_mfma_f32_16x16x32_bf16 v[112:115], a[248:251], a[144:147], 0\n"
        "v_exp_f32 v103, v103\n"
        "v_mfma_f32_16x16x32_bf16 v[112:115], a[216:219], a[148:151], v[112:115]\n"
        "v_cvt_pk_bf16_f32 v83, v102, v103\n"
        "s_nop 0\n"
        "v_mfma_f32_16x16x32_bf16 v[112:115], a[220:223], a[152:155], v[112:115]\n"
        "ds_read_b128 a[236:239], v240 offset:13824\n"
        "v_cvt_pk_bf16_f32 v208, v120, v121\n"
        "v_mfma_f32_16x16x32_bf16 v[112:115], a[224:227], a[156:159], v[112:115]\n"
        "ds_read_b128 a[240:243], v241 offset:13824\n"
        "v_cvt_pk_bf16_f32 v209, v122, v123\n"
        "v_mfma_f32_16x16x32_bf16 v[112:115], a[228:231], a[160:163], v[112:115]\n"
        "ds_read_b128 a[244:247], v240 offset:13952\n"
        "v_mfma_f32_16x16x32_bf16 v[112:115], a[232:235], a[164:167], v[112:115]\n"
        "s_nop 2\n"
        "v_mfma_f32_16x16x32_bf16 v[104:107], v[192:195], v[156:159], v[8:11]\n"
        "s_nop 2\n"
        "v_mfma_f32_16x16x32_bf16 v[104:107], v[208:211], v[160:163], v[104:107]\n"
        "s_add_u32 m0, s41, 14336\n"
        "v_cvt_pk_bf16_f32 v132, v112, v113\n"
        "v_cvt_pk_bf16_f32 v133, v114, v115\n"
        "v_mfma_f32_16x16x32_bf16 v[116:119], a[232:235], a[188:191], 0\n"
        "v_mfma_f32_16x16x32_bf16 v[116:119], a[228:231], a[184:187], v[116:119]\n"
        "buffer_load_dwordx4 v244, s[8:11], s35 offen lds\n"
        "v_mfma_f32_16x16x32_bf16 v[116:119], a[224:227], a[180:183], v[116:119]\n"
        "v_mfma_f32_16x16x32_bf16 v[116:119], a[220:223], a[176:179], v[116:119]\n"
        "v_exp_f32 v104, v104\n"
        "v_mfma_f32_16x16x32_bf16 v[116:119], a[216:219], a[172:175], v[116:119]\n"
        "v_exp_f32 v105, v105\n"
        "v_mfma_f32_16x16x32_bf16 v[116:119], a[248:251], a[168:171], v[116:119]\n"
        "v_exp_f32 v106, v106\n"
        "v_mfma_f32_16x16x32_bf16 v[108:111], v[192:195], v[176:179], v[24:27]\n"
        "v_exp_f32 v107, v107\n"
        "v_mfma_f32_16x16x32_bf16 v[108:111], v[208:211], v[180:183], v[108:111]\n"
        "v_cvt_pk_bf16_f32 v68, v104, v105\n"
        "v_cvt_pk_bf16_f32 v69, v106, v107\n"
        "v_mfma_f32_16x16x32_bf16 v[120:123], a[248:251], a[192:195], 0\n"
        "ds_read_b128 a[248:251], v241 offset:13952\n"
        "v_cvt_pk_bf16_f32 v140, v116, v117\n"
        "v_mfma_f32_16x16x32_bf16 v[120:123], a[216:219], a[196:199], v[120:123]\n"
        "ds_read_b128 a[216:219], v240 offset:14080\n"
        "v_cvt_pk_bf16_f32 v141, v118, v119\n"
        "v_mfma_f32_16x16x32_bf16 v[120:123], a[220:223], a[200:203], v[120:123]\n"
        "ds_read_b128 a[220:223], v241 offset:14080\n"
        "v_mfma_f32_16x16x32_bf16 v[120:123], a[224:227], a[204:207], v[120:123]\n"
        "v_exp_f32 v108, v108\n"
        "v_mfma_f32_16x16x32_bf16 v[120:123], a[228:231], a[208:211], v[120:123]\n"
        "v_exp_f32 v109, v109\n"
        "v_mfma_f32_16x16x32_bf16 v[120:123], a[232:235], a[212:215], v[120:123]\n"
        "v_exp_f32 v110, v110\n"
        "v_mfma_f32_16x16x32_bf16 v[96:99], v[192:195], v[196:199], v[40:43]\n"
        "s_waitcnt lgkmcnt(1)\n"
        "v_exp_f32 v111, v111\n"
        "v_mfma_f32_16x16x32_bf16 v[96:99], v[208:211], v[200:203], v[96:99]\n"
        "v_cvt_pk_bf16_f32 v76, v108, v109\n"
        "v_cvt_pk_bf16_f32 v77, v110, v111\n"
        "v_mfma_f32_16x16x32_bf16 v[112:115], a[72:75], a[236:239], 0\n"
        "s_nop 0\n"
        "v_cvt_pk_bf16_f32 v148, v120, v121\n"
        "v_cvt_pk_bf16_f32 v149, v122, v123\n"
        "v_mfma_f32_16x16x32_bf16 v[112:115], a[76:79], a[240:243], v[112:115]\n"
        "v_mfma_f32_16x16x32_bf16 v[112:115], a[80:83], a[244:247], v[112:115]\n"
        "v_exp_f32 v96, v96\n"
        "v_mfma_f32_16x16x32_bf16 v[112:115], a[84:87], a[248:251], v[112:115]\n"
        "v_exp_f32 v97, v97\n"
        "v_mfma_f32_16x16x32_bf16 v[112:115], a[88:91], a[216:219], v[112:115]\n"
        "s_waitcnt lgkmcnt(0)\n"
        "v_exp_f32 v98, v98\n"
        "v_mfma_f32_16x16x32_bf16 v[112:115], a[92:95], a[220:223], v[112:115]\n"
        "v_exp_f32 v99, v99\n"
        "v_mfma_f32_16x16x32_bf16 v[116:119], a[116:119], a[220:223], 0\n"
        "v_cvt_pk_bf16_f32 v84, v96, v97\n"
        "v_cvt_pk_bf16_f32 v85, v98, v99\n"
        "v_mfma_f32_16x16x32_bf16 v[116:119], a[112:115], a[216:219], v[116:119]\n"
        "v_add_u32 v240, s31, v240\n"
        "v_add_u32 v241, s31, v241\n"
        "v_mfma_f32_16x16x32_bf16 v[116:119], a[108:111], a[248:251], v[116:119]\n"
        "v_cvt_pk_bf16_f32 v212, v112, v113\n"
        "v_cvt_pk_bf16_f32 v213, v114, v115\n"
        "v_mfma_f32_16x16x32_bf16 v[116:119], a[104:107], a[244:247], v[116:119]\n"
        "s_add_u32 s56, s16, s38\n"
        "s_addc_u32 s57, s17, 0\n"
        "v_mfma_f32_16x16x32_bf16 v[116:119], a[100:103], a[240:243], v[116:119]\n"
        "s_add_u32 s60, s16, s39\n"
        "s_addc_u32 s61, s17, 0\n"
        "v_mfma_f32_16x16x32_bf16 v[116:119], a[96:99], a[236:239], v[116:119]\n"
        "s_nop 2\n"
        "v_mfma_f32_16x16x32_bf16 v[120:123], a[120:123], a[236:239], 0\n"
        "s_nop 2\n"
        "v_mfma_f32_16x16x32_bf16 v[120:123], a[124:127], a[240:243], v[120:123]\n"
        "s_add_u32 s47, s47, 6291456\n"
        "v_cvt_pk_bf16_f32 v214, v116, v117\n"
        "v_cvt_pk_bf16_f32 v215, v118, v119\n"
        "v_mfma_f32_16x16x32_bf16 v[120:123], a[128:131], a[244:247], v[120:123]\n"
        "s_add_u32 s36, s36, 2097152\n"
        "v_mfma_f32_16x16x32_bf16 v[120:123], a[132:135], a[248:251], v[120:123]\n"
        "s_xor_b32 s48, s48, 8192\n"
        "v_mfma_f32_16x16x32_bf16 v[120:123], a[136:139], a[216:219], v[120:123]\n"
        "s_barrier\n"
        "v_mfma_f32_16x16x32_bf16 v[120:123], a[140:143], a[220:223], v[120:123]\n"
        "s_nop 2\n"
        "v_mfma_f32_16x16x32_bf16 v[112:115], a[220:223], a[164:167], 0\n"
        "s_nop 2\n"
        "v_mfma_f32_16x16x32_bf16 v[112:115], a[216:219], a[160:163], v[112:115]\n"
        "v_cvt_pk_bf16_f32 v230, v120, v121\n"
        "v_cvt_pk_bf16_f32 v231, v122, v123\n"
        "v_mfma_f32_16x16x32_bf16 v[112:115], a[248:251], a[156:159], v[112:115]\n"
        "v_mfma_f32_16x16x32_bf16 v[112:115], a[244:247], a[152:155], v[112:115]\n"
        "v_mfma_f32_16x16x32_bf16 v[112:115], a[240:243], a[148:151], v[112:115]\n"
        "v_mfma_f32_16x16x32_bf16 v[112:115], a[236:239], a[144:147], v[112:115]\n"
        "s_nop 2\n"
        "v_mfma_f32_16x16x32_bf16 v[100:103], v[212:215], v[156:159], v[12:15]\n"
        "s_nop 2\n"
        "v_mfma_f32_16x16x32_bf16 v[100:103], v[228:231], v[164:167], v[100:103]\n"
        "v_cvt_pk_bf16_f32 v134, v112, v113\n"
        "v_cvt_pk_bf16_f32 v135, v114, v115\n"
        "v_mfma_f32_16x16x32_bf16 v[116:119], a[236:239], a[168:171], 0\n"
        "s_sub_u32 s27, s27, 1\n"
        "v_mfma_f32_16x16x32_bf16 v[116:119], a[240:243], a[172:175], v[116:119]\n"
        "s_cmp_lg_u32 s27, 0\n"
        "v_mfma_f32_16x16x32_bf16 v[116:119], a[244:247], a[176:179], v[116:119]\n"
        "s_cbranch_scc1 10b\n"
        "19:\n"
        "v_mfma_f32_16x16x32_bf16 v[116:119], a[248:251], a[180:183], v[116:119]\n"
        "ds_read_b128 a[224:227], v247 offset:0\n"
        "ds_read_b128 a[228:231], v247 offset:4096\n"
        "v_mfma_f32_16x16x32_bf16 v[116:119], a[216:219], a[184:187], v[116:119]\n"
        "ds_read_b128 a[232:235], v247 offset:8192\n"
        "ds_read_b128 v[156:159], v247 offset:12288\n"
        "v_mfma_f32_16x16x32_bf16 v[116:119], a[220:223], a[188:191], v[116:119]\n"
        "ds_read_b128 v[160:163], v247 offset:16384\n"
        "ds_read_b128 v[164:167], v247 offset:20480\n"
        "v_mfma_f32_16x16x32_bf16 v[104:107], v[212:215], v[176:179], v[28:31]\n"
        "s_and_b32 s46, s42, 1\n"
        "v_exp_f32 v100, v100\n"
        "v_mfma_f32_16x16x32_bf16 v[104:107], v[228:231], v[184:187], v[104:107]\n"
        "s_lshl_b32 s31, s46, 8\n"
        "v_exp_f32 v101, v101\n"
        "v_mfma_f32_16x16x32_bf16 v[120:123], a[220:223], a[212:215], 0\n"
        "s_sub_u32 s31, s45, s31\n"
        "v_cvt_pk_bf16_f32 v142, v116, v117\n"
        "v_cvt_pk_bf16_f32 v143, v118, v119\n"
        "v_mfma_f32_16x16x32_bf16 v[120:123], a[216:219], a[208:211], v[120:123]\n"
        "ds_read_b128 a[96:99], v248 offset:32\n"
        "v_cvt_pk_bf16_f32 v70, v100, v101\n"
        "v_mfma_f32_16x16x32_bf16 v[120:123], a[248:251], a[204:207], v[120:123]\n"
        "ds_read_b128 a[100:103], v248 offset:3104\n"
        "v_mov_b32 v241, 0\n"
        "v_mfma_f32_16x16x32_bf16 v[120:123], a[244:247], a[200:203], v[120:123]\n"
        "ds_read_b128 a[104:107], v248 offset:6176\n"
        "v_mfma_f32_16x16x32_bf16 v[120:123], a[240:243], a[196:199], v[120:123]\n"
        "ds_read_b128 a[108:111], v248 offset:9248\n"
        "v_mfma_f32_16x16x32_bf16 v[120:123], a[236:239], a[192:195], v[120:123]\n"
        "ds_read_b128 a[112:115], v248 offset:12320\n"
        "s_nop 0\n"
        "v_mfma_f32_16x16x32_bf16 v[108:111], v[212:215], v[196:199], v[44:47]\n"
        "ds_read_b128 a[116:119], v248 offset:15392\n"
        "s_nop 0\n"
        "v_mfma_f32_16x16x32_bf16 v[108:111], v[228:231], v[204:207], v[108:111]\n"
        "ds_read_b128 a[92:95], v248 offset:15360\n"
        "s_nop 0\n"
        "v_mfma_f32_16x16x32_bf16 v[116:119], a[0:3], a[236:239], 0\n"
        "ds_read_b128 a[88:91], v248 offset:12288\n"
        "v_cvt_pk_bf16_f32 v150, v120, v121\n"
        "v_mfma_f32_16x16x32_bf16 v[116:119], a[4:7], a[240:243], v[116:119]\n"
        "ds_read_b128 a[84:87], v248 offset:9216\n"
        "v_cvt_pk_bf16_f32 v151, v122, v123\n"
        "v_mfma_f32_16x16x32_bf16 v[116:119], a[8:11], a[244:247], v[116:119]\n"
        "ds_read_b128 a[80:83], v248 offset:6144\n"
        "v_mfma_f32_16x16x32_bf16 v[116:119], a[12:15], a[248:251], v[116:119]\n"
        "ds_read_b128 a[76:79], v248 offset:3072\n"
        "v_mfma_f32_16x16x32_bf16 v[116:119], a[16:19], a[216:219], v[116:119]\n"
        "ds_read_b128 a[72:75], v248 offset:0\n"
        "v_mfma_f32_16x16x32_bf16 v[116:119], a[20:23], a[220:223], v[116:119]\n"
        "s_waitcnt lgkmcnt(6)\n"
        "ds_read_b128 a[120:123], v254 offset:0\n"
        "v_mfma_f32_16x16x32_bf16 v[4:7], a[224:227], a[96:99], 0\n"
        "ds_read_b128 a[124:127], v254 offset:3072\n"
        "v_mfma_f32_16x16x32_bf16 v[4:7], a[228:231], a[100:103], v[4:7]\n"
        "ds_read_b128 a[128:131], v254 offset:6144\n"
        "v_mfma_f32_16x16x32_bf16 v[4:7], a[232:235], a[104:107], v[4:7]\n"
        "ds_read_b128 a[132:135], v254 offset:9216\n"
        "s_nop 0\n"
        "v_mfma_f32_16x16x32_bf16 v[4:7], v[156:159], a[108:111], v[4:7]\n"
        "ds_read_b128 a[136:139], v254 offset:12288\n"
        "v_cvt_pk_bf16_f32 v216, v116, v117\n"
        "v_mfma_f32_16x16x32_bf16 v[4:7], v[160:163], a[112:115], v[4:7]\n"
        "ds_read_b128 a[140:143], v254 offset:15360\n"
        "v_cvt_pk_bf16_f32 v217, v118, v119\n"
        "v_mfma_f32_16x16x32_bf16 v[4:7], v[164:167], a[116:119], v[4:7]\n"
        "s_waitcnt lgkmcnt(6)\n"
        "v_exp_f32 v102, v102\n"
        "v_mfma_f32_16x16x32_bf16 v[0:3], v[164:167], a[92:95], 0\n"
        "s_mul_i32 s45, s46, 786432\n"
        "v_mov_b64 v[96:97], 0\n"
        "v_mfma_f32_16x16x32_bf16 v[0:3], v[160:163], a[88:91], v[0:3]\n"
        "s_sub_u32 s37, s37, s45\n"
        "v_exp_f32 v103, v103\n"
        "v_mfma_f32_16x16x32_bf16 v[0:3], v[156:159], a[84:87], v[0:3]\n"
        "s_cmp_eq_u32 s46, 0\n"
        "s_cselect_b32 s43, s43, s44\n"
        "v_cvt_pk_bf16_f32 v71, v102, v103\n"
        "v_mfma_f32_16x16x32_bf16 v[0:3], a[232:235], a[80:83], v[0:3]\n"
        "s_add_u32 s28, s37, 786432\n"
        "v_exp_f32 v104, v104\n"
        "v_mfma_f32_16x16x32_bf16 v[0:3], a[228:231], a[76:79], v[0:3]\n"
        "s_cmp_eq_u32 s28, 100663296\n"
        "s_cselect_b32 s28, 0, s28\n"
        "v_mfma_f32_16x16x32_bf16 v[0:3], a[224:227], a[72:75], v[0:3]\n"
        "s_waitcnt lgkmcnt(0)\n"
        "v_exp_f32 v105, v105\n"
        "v_mfma_f32_16x16x32_bf16 v[8:11], a[224:227], a[120:123], 0\n"
        "s_lshl_b32 s29, s42, 2\n"
        "v_exp_f32 v106, v106\n"
        "v_mfma_f32_16x16x32_bf16 v[8:11], a[228:231], a[124:127], v[8:11]\n"
        "s_add_u32 s47, s37, s43\n"
        "v_exp_f32 v107, v107\n"
        "v_mfma_f32_16x16x32_bf16 v[8:11], a[232:235], a[128:131], v[8:11]\n"
        "s_add_u32 s35, s28, s43\n"
        "v_exp_f32 v108, v108\n"
        "v_mfma_f32_16x16x32_bf16 v[8:11], v[156:159], a[132:135], v[8:11]\n"
        "v_exp_f32 v109, v109\n"
        "v_mfma_f32_16x16x32_bf16 v[8:11], v[160:163], a[136:139], v[8:11]\n"
        "v_exp_f32 v110, v110\n"
        "v_mfma_f32_16x16x32_bf16 v[8:11], v[164:167], a[140:143], v[8:11]\n"
        "v_exp_f32 v111, v111\n"
        "v_smfmac_f32_16x16x64_bf16 v[96:99], a[252:255], v[64:71], v237\n"
        "ds_read_b128 a[164:167], v254 offset:15392\n"
        "v_mfma_f32_16x16x32_bf16 v[100:103], v[132:135], v[68:71], 0\n"
        "ds_read_b128 a[160:163], v254 offset:12320\n"
        "v_cvt_pk_bf16_f32 v78, v104, v105\n"
        "v_mfma_f32_16x16x32_bf16 v[100:103], v[128:131], v[64:67], v[100:103]\n"
        "ds_read_b128 a[156:159], v254 offset:9248\n"
        "v_cvt_pk_bf16_f32 v79, v106, v107\n"
        "v_mfma_f32_16x16x32_bf16 v[104:107], v[136:139], v[64:67], 0\n"
        "ds_read_b128 a[152:155], v254 offset:6176\n"
        "v_cvt_pk_bf16_f32 v86, v108, v109\n"
        "v_mfma_f32_16x16x32_bf16 v[104:107], v[140:143], v[68:71], v[104:107]\n"
        "ds_read_b128 a[148:151], v254 offset:3104\n"
        "v_cvt_pk_bf16_f32 v87, v110, v111\n"
        "v_mfma_f32_16x16x32_bf16 v[108:111], v[148:151], v[68:71], 0\n"
        "ds_read_b128 a[144:147], v254 offset:32\n"
        "v_add_f32 v97, v96, v97\n"
        "v_mfma_f32_16x16x32_bf16 v[108:111], v[144:147], v[64:67], v[108:111]\n"
        "v_rcp_f32 v96, v97\n"
        "v_mfma_f32_16x16x32_bf16 v[120:123], a[44:47], a[220:223], 0\n"
        "v_mul_f32 v100, v96, v100\n"
        "v_mul_f32 v101, v96, v101\n"
        "v_mfma_f32_16x16x32_bf16 v[120:123], a[40:43], a[216:219], v[120:123]\n"
        "ds_read_b128 v[176:179], v247 offset:256\n"
        "v_mul_f32 v102, v96, v102\n"
        "v_mfma_f32_16x16x32_bf16 v[120:123], a[36:39], a[248:251], v[120:123]\n"
        "ds_read_b128 v[180:183], v247 offset:4352\n"
        "v_mul_f32 v103, v96, v103\n"
        "v_mfma_f32_16x16x32_bf16 v[120:123], a[32:35], a[244:247], v[120:123]\n"
        "ds_read_b128 v[184:187], v247 offset:8448\n"
        "v_cvt_pk_bf16_f32 v100, v100, v101\n"
        "v_mfma_f32_16x16x32_bf16 v[120:123], a[28:31], a[240:243], v[120:123]\n"
        "ds_read_b128 v[196:199], v247 offset:12544\n"
        "v_cvt_pk_bf16_f32 v232, v0, v4\n"
        "v_mfma_f32_16x16x32_bf16 v[120:123], a[24:27], a[236:239], v[120:123]\n"
        "s_waitcnt lgkmcnt(4)\n"
        "ds_read_b128 v[200:203], v247 offset:16640\n"
        "v_cvt_pk_bf16_f32 v101, v102, v103\n"
        "v_mfma_f32_16x16x32_bf16 v[12:15], v[164:167], a[164:167], 0\n"
        "ds_read_b128 v[204:207], v247 offset:20736\n"
        "v_mov_b64 v[124:125], 0\n"
        "v_mfma_f32_16x16x32_bf16 v[12:15], v[160:163], a[160:163], v[12:15]\n"
        "ds_write_b32 v245, v100 offset:0\n"
        "v_mfma_f32_16x16x32_bf16 v[12:15], v[156:159], a[156:159], v[12:15]\n"
        "ds_write_b32 v245, v101 offset:1024\n"
        "v_mfma_f32_16x16x32_bf16 v[12:15], a[232:235], a[152:155], v[12:15]\n"
        "v_cvt_pk_bf16_f32 v218, v120, v121\n"
        "v_cvt_pk_bf16_f32 v219, v122, v123\n"
        "v_mfma_f32_16x16x32_bf16 v[12:15], a[228:231], a[148:151], v[12:15]\n"
        "v_cvt_pk_bf16_f32 v234, v1, v5\n"
        "v_mul_f32 v104, v96, v104\n"
        "v_mfma_f32_16x16x32_bf16 v[12:15], a[224:227], a[144:147], v[12:15]\n"
        "v_mul_f32 v105, v96, v105\n"
        "v_mul_f32 v106, v96, v106\n"
        "v_mfma_f32_16x16x32_bf16 v[116:119], a[48:51], a[236:239], 0\n"
        "v_mul_f32 v107, v96, v107\n"
        "v_cvt_pk_bf16_f32 v242, v2, v6\n"
        "v_mfma_f32_16x16x32_bf16 v[116:119], a[52:55], a[240:243], v[116:119]\n"
        "ds_read_b128 v[222:225], v251 offset:4608\n"
        "v_cvt_pk_bf16_f32 v102, v104, v105\n"
        "v_mfma_f32_16x16x32_bf16 v[116:119], a[56:59], a[244:247], v[116:119]\n"
        "v_cvt_pk_bf16_f32 v233, v8, v12\n"
        "v_cvt_pk_bf16_f32 v235, v9, v13\n"
        "v_mfma_f32_16x16x32_bf16 v[116:119], a[60:63], a[248:251], v[116:119]\n"
        "buffer_store_dwordx2 v[232:233], v250, s[56:59], 0 offen offset:0 sc1\n"
        "v_cvt_pk_bf16_f32 v243, v10, v14\n"
        "v_mfma_f32_16x16x32_bf16 v[116:119], a[64:67], a[216:219], v[116:119]\n"
        "v_cvt_pk_bf16_f32 v103, v106, v107\n"
        "v_mul_f32 v108, v96, v108\n"
        "v_mfma_f32_16x16x32_bf16 v[116:119], a[68:71], a[220:223], v[116:119]\n"
        "ds_write2st64_b32 v245, v102, v103 offset0:8 offset1:12\n"
        "v_smfmac_f32_16x16x64_bf16 v[124:127], a[252:255], v[72:79], v237\n"
        "ds_read_b128 a[212:215], v254 offset:15376\n"
        "v_mfma_f32_16x16x32_bf16 v[64:67], v[132:135], v[76:79], 0\n"
        "buffer_store_dwordx2 v[234:235], v250, s[56:59], 0 offen offset:384 sc1\n"
        "ds_read_b128 a[208:211], v254 offset:12304\n"
        "v_mfma_f32_16x16x32_bf16 v[64:67], v[128:131], v[72:75], v[64:67]\n"
        "ds_read_b128 a[204:207], v254 offset:9232\n"
        "v_mul_f32 v109, v96, v109\n"
        "v_mfma_f32_16x16x32_bf16 v[68:71], v[136:139], v[72:75], 0\n"
        "buffer_store_dwordx2 v[242:243], v250, s[56:59], 0 offen offset:768 sc1\n"
        "ds_read_b128 a[200:203], v254 offset:6160\n"
        "v_mfma_f32_16x16x32_bf16 v[68:71], v[140:143], v[76:79], v[68:71]\n"
        "ds_read_b128 a[196:199], v254 offset:3088\n"
        "v_mul_f32 v110, v96, v110\n"
        "v_mfma_f32_16x16x32_bf16 v[112:115], v[148:151], v[76:79], 0\n"
        "ds_read_b128 a[192:195], v254 offset:16\n"
        "v_mul_f32 v111, v96, v111\n"
        "v_mfma_f32_16x16x32_bf16 v[112:115], v[144:147], v[72:75], v[112:115]\n"
        "s_waitcnt lgkmcnt(7)\n"
        "ds_read_b128 a[168:171], v248 offset:16\n"
        "v_cvt_pk_bf16_f32 v232, v3, v7\n"
        "v_mfma_f32_16x16x32_bf16 v[24:27], v[176:179], a[72:75], 0\n"
        "ds_read_b128 a[172:175], v248 offset:3088\n"
        "v_cvt_pk_bf16_f32 v233, v11, v15\n"
        "v_mfma_f32_16x16x32_bf16 v[24:27], v[180:183], a[76:79], v[24:27]\n"
        "buffer_store_dwordx2 v[232:233], v250, s[56:59], 0 offen offset:1152 sc1\n"
        "ds_read_b128 a[176:179], v248 offset:6160\n"
        "v_mfma_f32_16x16x32_bf16 v[24:27], v[184:187], a[80:83], v[24:27]\n"
        "ds_read_b128 a[180:183], v248 offset:9232\n"
        "v_cvt_pk_bf16_f32 v108, v108, v109\n"
        "v_mfma_f32_16x16x32_bf16 v[24:27], v[196:199], a[84:87], v[24:27]\n"
        "ds_read_b128 a[184:187], v248 offset:12304\n"
        "v_cvt_pk_bf16_f32 v109, v110, v111\n"
        "v_mfma_f32_16x16x32_bf16 v[24:27], v[200:203], a[88:91], v[24:27]\n"
        "ds_write2st64_b32 v245, v108, v109 offset0:16 offset1:20\n"
        "v_mfma_f32_16x16x32_bf16 v[24:27], v[204:207], a[92:95], v[24:27]\n"
        "s_waitcnt lgkmcnt(6)\n"
        "ds_read_b128 a[188:191], v248 offset:15376\n"
        "v_add_f32 v125, v124, v125\n"
        "v_mfma_f32_16x16x32_bf16 v[20:23], v[164:167], a[212:215], 0\n"
        "v_rcp_f32 v124, v125\n"
        "v_mfma_f32_16x16x32_bf16 v[20:23], v[160:163], a[208:211], v[20:23]\n"
        "v_add_u32 v240, s31, v247\n"
        "v_max_u32 v236, v96, v97\n"
        "v_mfma_f32_16x16x32_bf16 v[20:23], v[156:159], a[204:207], v[20:23]\n"
        "v_cvt_pk_bf16_f32 v220, v116, v117\n"
        "v_cvt_pk_bf16_f32 v221, v118, v119\n"
        "v_mfma_f32_16x16x32_bf16 v[20:23], a[232:235], a[200:203], v[20:23]\n"
        "v_cvt_pk_bf16_f32 v226, v116, v117\n"
        "v_cvt_pk_bf16_f32 v227, v118, v119\n"
        "v_mfma_f32_16x16x32_bf16 v[20:23], a[228:231], a[196:199], v[20:23]\n"
        "v_mul_f32 v64, v124, v64\n"
        "v_mul_f32 v65, v124, v65\n"
        "v_mfma_f32_16x16x32_bf16 v[20:23], a[224:227], a[192:195], v[20:23]\n"
        "s_waitcnt lgkmcnt(2)\n"
        "v_mul_f32 v66, v124, v66\n"
        "v_mul_f32 v67, v124, v67\n"
        "v_mfma_f32_16x16x32_bf16 v[16:19], a[224:227], a[168:171], 0\n"
        "v_cvt_pk_bf16_f32 v64, v64, v65\n"
        "v_cvt_pk_bf16_f32 v65, v66, v67\n"
        "v_mfma_f32_16x16x32_bf16 v[16:19], a[228:231], a[172:175], v[16:19]\n"
        "ds_write2st64_b32 v245, v64, v65 offset0:1 offset1:5\n"
        "v_mfma_f32_16x16x32_bf16 v[16:19], a[232:235], a[176:179], v[16:19]\n"
        "v_mul_f32 v68, v124, v68\n"
        "v_mul_f32 v69, v124, v69\n"
        "v_mfma_f32_16x16x32_bf16 v[16:19], v[156:159], a[180:183], v[16:19]\n"
        "v_mul_f32 v70, v124, v70\n"
        "v_mul_f32 v71, v124, v71\n"
        "v_mfma_f32_16x16x32_bf16 v[16:19], v[160:163], a[184:187], v[16:19]\n"
        "s_waitcnt lgkmcnt(1)\n"
        "v_cvt_pk_bf16_f32 v66, v68, v69\n"
        "v_cvt_pk_bf16_f32 v67, v70, v71\n"
        "v_mfma_f32_16x16x32_bf16 v[16:19], v[164:167], a[188:191], v[16:19]\n"
        "ds_write2st64_b32 v245, v66, v67 offset0:9 offset1:13\n"
        "v_mfma_f32_16x16x32_bf16 v[0:3], v[152:155], v[216:219], v[48:51]\n"
        "v_mul_f32 v112, v124, v112\n"
        "v_mul_f32 v113, v124, v113\n"
        "v_mfma_f32_16x16x32_bf16 v[0:3], v[168:171], v[220:223], v[0:3]\n"
        "v_mul_f32 v114, v124, v114\n"
        "v_mul_f32 v115, v124, v115\n"
        "v_mfma_f32_16x16x32_bf16 v[4:7], v[172:175], v[216:219], v[52:55]\n"
        ".long 0xbec200ff\n"
        "47:\n"
        "s_branch 61b\n"
        "v_cvt_pk_bf16_f32 v112, v112, v113\n"
        "v_cvt_pk_bf16_f32 v113, v114, v115\n"
        "v_mfma_f32_16x16x32_bf16 v[4:7], v[188:191], v[224:227], v[4:7]\n"
        "ds_write_b32 v245, v112 offset:4352\n"
        "v_mfma_f32_16x16x32_bf16 v[36:39], v[204:207], a[164:167], 0\n"
        "ds_write_b32 v245, v113 offset:5376\n"
        "v_mfma_f32_16x16x32_bf16 v[36:39], v[200:203], a[160:163], v[36:39]\n"
        "ds_read_b128 v[222:225], v252 offset:4608\n"
        "v_max3_u32 v236, v236, v124, v125\n"
        "v_mfma_f32_16x16x32_bf16 v[36:39], v[196:199], a[156:159], v[36:39]\n"
        "v_cvt_pk_bf16_f32 v244, v16, v20\n"
        "v_cvt_pk_bf16_f32 v239, v17, v21\n"
        "v_mfma_f32_16x16x32_bf16 v[36:39], v[184:187], a[152:155], v[36:39]\n"
        "buffer_store_dword v244, v238, s[56:59], 0 offen offset:0 sc1\n"
        "v_mfma_f32_16x16x32_bf16 v[36:39], v[180:183], a[148:151], v[36:39]\n"
        "buffer_store_dword v239, v238, s[56:59], 0 offen offset:384 sc1\n"
        "v_mfma_f32_16x16x32_bf16 v[36:39], v[176:179], a[144:147], v[36:39]\n"
        "v_exp_f32 v0, v0\n"
        "v_mfma_f32_16x16x32_bf16 v[32:35], v[176:179], a[120:123], 0\n"
        "v_exp_f32 v1, v1\n"
        "v_mfma_f32_16x16x32_bf16 v[32:35], v[180:183], a[124:127], v[32:35]\n"
        "v_exp_f32 v2, v2\n"
        "v_mfma_f32_16x16x32_bf16 v[32:35], v[184:187], a[128:131], v[32:35]\n"
        "v_cvt_pk_bf16_f32 v88, v0, v1\n"
        "v_cvt_pk_bf16_f32 v255, v18, v22\n"
        "v_mfma_f32_16x16x32_bf16 v[32:35], v[196:199], a[132:135], v[32:35]\n"
        "buffer_store_dword v255, v238, s[56:59], 0 offen offset:768 sc1\n"
        "v_mfma_f32_16x16x32_bf16 v[32:35], v[200:203], a[136:139], v[32:35]\n"
        "v_exp_f32 v4, v4\n"
        "v_mfma_f32_16x16x32_bf16 v[32:35], v[204:207], a[140:143], v[32:35]\n"
        "v_exp_f32 v3, v3\n"
        "v_mfma_f32_16x16x32_bf16 v[28:31], v[204:207], a[116:119], 0\n"
        "v_exp_f32 v5, v5\n"
        "v_mfma_f32_16x16x32_bf16 v[28:31], v[200:203], a[112:115], v[28:31]\n"
        "v_cvt_pk_bf16_f32 v89, v2, v3\n"
        "v_cvt_pk_bf16_f32 v90, v4, v5\n"
        "v_mfma_f32_16x16x32_bf16 v[28:31], v[196:199], a[108:111], v[28:31]\n"
        "v_exp_f32 v6, v6\n"
        "v_mfma_f32_16x16x32_bf16 v[28:31], v[184:187], a[104:107], v[28:31]\n"
        "v_exp_f32 v7, v7\n"
        "v_mfma_f32_16x16x32_bf16 v[28:31], v[180:183], a[100:103], v[28:31]\n"
        "v_cvt_pk_bf16_f32 v91, v6, v7\n"
        "v_cvt_pk_bf16_f32 v244, v19, v23\n"
        "v_mfma_f32_16x16x32_bf16 v[28:31], v[176:179], a[96:99], v[28:31]\n"
        "buffer_store_dword v244, v238, s[56:59], 0 offen offset:1152 sc1\n"
        "v_mfma_f32_16x16x32_bf16 v[8:11], v[192:195], v[216:219], v[56:59]\n"
        "s_waitcnt lgkmcnt(0)\n"
        "v_mov_b64 v[96:97], 0\n"
        "v_cvt_pk_bf16_f32 v235, v32, v36\n"
        "v_mfma_f32_16x16x32_bf16 v[8:11], v[208:211], v[220:223], v[8:11]\n"
        "v_cvt_pk_bf16_f32 v243, v33, v37\n"
        "v_cvt_pk_bf16_f32 v233, v34, v38\n"
        "v_mfma_f32_16x16x32_bf16 v[40:43], v[176:179], a[168:171], 0\n"
        "v_cvt_pk_bf16_f32 v234, v24, v28\n"
        "v_cvt_pk_bf16_f32 v242, v25, v29\n"
        "v_mfma_f32_16x16x32_bf16 v[40:43], v[180:183], a[172:175], v[40:43]\n"
        "buffer_store_dwordx2 v[234:235], v250, s[60:63], 0 offen offset:0 sc1\n"
        "v_cvt_pk_bf16_f32 v232, v26, v30\n"
        "v_mfma_f32_16x16x32_bf16 v[40:43], v[184:187], a[176:179], v[40:43]\n"
        "v_exp_f32 v8, v8\n"
        "v_mfma_f32_16x16x32_bf16 v[40:43], v[196:199], a[180:183], v[40:43]\n"
        "s_barrier\n"
        "ds_read_b128 a[0:3], v240 offset:0\n"
        "ds_read_b128 a[4:7], v240 offset:4096\n"
        "v_mfma_f32_16x16x32_bf16 v[40:43], v[200:203], a[184:187], v[40:43]\n"
        "buffer_store_dwordx2 v[242:243], v250, s[60:63], 0 offen offset:384 sc1\n"
        "ds_read_b128 a[8:11], v240 offset:8192\n"
        "v_mfma_f32_16x16x32_bf16 v[40:43], v[204:207], a[188:191], v[40:43]\n"
        "ds_read_b128 a[12:15], v240 offset:12288\n"
        "ds_read_b128 a[16:19], v240 offset:16384\n"
        "v_smfmac_f32_16x16x64_bf16 v[96:99], a[252:255], v[80:87], v237\n"
        "ds_read_b128 a[20:23], v240 offset:20480\n"
        "v_mfma_f32_16x16x32_bf16 v[72:75], v[132:135], v[84:87], 0\n"
        "buffer_store_dwordx2 v[232:233], v250, s[60:63], 0 offen offset:768 sc1\n"
        "v_cvt_pk_bf16_f32 v234, v27, v31\n"
        "v_mfma_f32_16x16x32_bf16 v[72:75], v[128:131], v[80:83], v[72:75]\n"
        "v_exp_f32 v9, v9\n"
        "v_mfma_f32_16x16x32_bf16 v[76:79], v[136:139], v[80:83], 0\n"
        "v_exp_f32 v10, v10\n"
        "v_mfma_f32_16x16x32_bf16 v[76:79], v[140:143], v[84:87], v[76:79]\n"
        "v_cvt_pk_bf16_f32 v92, v8, v9\n"
        "v_cvt_pk_bf16_f32 v235, v35, v39\n"
        "v_mfma_f32_16x16x32_bf16 v[100:103], v[148:151], v[84:87], 0\n"
        "buffer_store_dwordx2 v[234:235], v250, s[60:63], 0 offen offset:1152 sc1\n"
        "v_add_f32 v97, v96, v97\n"
        "v_mfma_f32_16x16x32_bf16 v[100:103], v[144:147], v[80:83], v[100:103]\n"
        "v_exp_f32 v11, v11\n"
        "v_mfma_f32_16x16x32_bf16 v[12:15], v[212:215], v[216:219], v[60:63]\n"
        "v_cvt_pk_bf16_f32 v93, v10, v11\n"
        "v_mov_b32 v249, s29\n"
        "v_mfma_f32_16x16x32_bf16 v[12:15], v[228:231], v[224:227], v[12:15]\n"
        "s_waitcnt lgkmcnt(0)\n"
        "v_rcp_f32 v96, v97\n"
        "v_mfma_f32_16x16x32_bf16 v[156:159], a[20:23], a[92:95], 0\n"
        "v_mul_f32 v72, v96, v72\n"
        "v_mul_f32 v73, v96, v73\n"
        "v_mfma_f32_16x16x32_bf16 v[156:159], a[16:19], a[88:91], v[156:159]\n"
        "v_mul_f32 v74, v96, v74\n"
        "v_mul_f32 v75, v96, v75\n"
        "v_mfma_f32_16x16x32_bf16 v[156:159], a[12:15], a[84:87], v[156:159]\n"
        "v_exp_f32 v12, v12\n"
        "v_mfma_f32_16x16x32_bf16 v[156:159], a[8:11], a[80:83], v[156:159]\n"
        "v_exp_f32 v13, v13\n"
        "v_mfma_f32_16x16x32_bf16 v[156:159], a[4:7], a[76:79], v[156:159]\n"
        "v_exp_f32 v14, v14\n"
        "v_mfma_f32_16x16x32_bf16 v[156:159], a[0:3], a[72:75], v[156:159]\n"
        "v_cvt_pk_bf16_f32 v94, v12, v13\n"
        "v_cvt_pk_bf16_f32 v72, v72, v73\n"
        "v_mfma_f32_16x16x32_bf16 v[44:47], v[176:179], a[192:195], 0\n"
        "v_exp_f32 v15, v15\n"
        "v_mfma_f32_16x16x32_bf16 v[44:47], v[180:183], a[196:199], v[44:47]\n"
        "v_cvt_pk_bf16_f32 v95, v14, v15\n"
        "v_mov_b64 v[124:125], 0\n"
        "v_mfma_f32_16x16x32_bf16 v[44:47], v[184:187], a[200:203], v[44:47]\n"
        "v_cvt_pk_bf16_f32 v73, v74, v75\n"
        "v_mul_f32 v76, v96, v76\n"
        "v_mfma_f32_16x16x32_bf16 v[44:47], v[196:199], a[204:207], v[44:47]\n"
        "ds_write2st64_b32 v245, v72, v73 offset0:2 offset1:6\n"
        "v_mfma_f32_16x16x32_bf16 v[44:47], v[200:203], a[208:211], v[44:47]\n"
        "v_mul_f32 v77, v96, v77\n"
        "v_mul_f32 v78, v96, v78\n"
        "v_mfma_f32_16x16x32_bf16 v[44:47], v[204:207], a[212:215], v[44:47]\n"
        "v_mul_f32 v79, v96, v79\n"
        "v_cvt_pk_bf16_f32 v74, v76, v77\n"
        "v_mfma_f32_16x16x32_bf16 v[160:163], a[20:23], a[116:119], 0\n"
        "v_cvt_pk_bf16_f32 v75, v78, v79\n"
        "v_mul_f32 v100, v96, v100\n"
        "v_mfma_f32_16x16x32_bf16 v[160:163], a[16:19], a[112:115], v[160:163]\n"
        "v_mul_f32 v101, v96, v101\n"
        "v_mul_f32 v102, v96, v102\n"
        "v_mfma_f32_16x16x32_bf16 v[160:163], a[12:15], a[108:111], v[160:163]\n"
        "ds_write2st64_b32 v245, v74, v75 offset0:10 offset1:14\n"
        "v_mfma_f32_16x16x32_bf16 v[160:163], a[8:11], a[104:107], v[160:163]\n"
        "v_cvt_pk_bf16_f32 v239, v40, v44\n"
        "v_cvt_pk_bf16_f32 v255, v41, v45\n"
        "v_mfma_f32_16x16x32_bf16 v[160:163], a[4:7], a[100:103], v[160:163]\n"
        "buffer_store_dword v239, v238, s[60:63], 0 offen offset:0 sc1\n"
        "v_mfma_f32_16x16x32_bf16 v[160:163], a[0:3], a[96:99], v[160:163]\n"
        "v_mul_f32 v103, v96, v103\n"
        "v_cvt_pk_bf16_f32 v244, v42, v46\n"
        "v_smfmac_f32_16x16x64_bf16 v[124:127], a[252:255], v[88:95], v237\n"
        "v_cvt_pk_bf16_f32 v100, v100, v101\n"
        "v_mfma_f32_16x16x32_bf16 v[104:107], v[132:135], v[92:95], 0\n"
        "buffer_store_dword v255, v238, s[60:63], 0 offen offset:384 sc1\n"
        "v_mfma_f32_16x16x32_bf16 v[104:107], v[128:131], v[88:91], v[104:107]\n"
        "v_cvt_pk_bf16_f32 v101, v102, v103\n"
        "v_cvt_pk_bf16_f32 v239, v43, v47\n"
        "v_mfma_f32_16x16x32_bf16 v[108:111], v[136:139], v[88:91], 0\n"
        "buffer_store_dword v244, v238, s[60:63], 0 offen offset:768 sc1\n"
        "v_mfma_f32_16x16x32_bf16 v[108:111], v[140:143], v[92:95], v[108:111]\n"
        "ds_write2st64_b32 v245, v100, v101 offset0:18 offset1:22\n"
        "v_mfma_f32_16x16x32_bf16 v[112:115], v[148:151], v[92:95], 0\n"
        "v_max3_u32 v236, v236, v96, v97\n"
        "v_add_f32 v125, v124, v125\n"
        "v_mfma_f32_16x16x32_bf16 v[112:115], v[144:147], v[88:91], v[112:115]\n"
        "buffer_store_dword v239, v238, s[60:63], 0 offen offset:1152 sc1\n"
        "v_mfma_f32_16x16x32_bf16 v[176:179], a[0:3], a[144:147], 0\n"
        "v_rcp_f32 v124, v125\n"
        "v_mfma_f32_16x16x32_bf16 v[176:179], a[4:7], a[148:151], v[176:179]\n"
        "v_mul_f32 v104, v124, v104\n"
        "v_mul_f32 v105, v124, v105\n"
        "v_mfma_f32_16x16x32_bf16 v[176:179], a[8:11], a[152:155], v[176:179]\n"
        "v_mul_f32 v106, v124, v106\n"
        "v_mul_f32 v107, v124, v107\n"
        "v_mfma_f32_16x16x32_bf16 v[176:179], a[12:15], a[156:159], v[176:179]\n"
        "v_cvt_pk_bf16_f32 v104, v104, v105\n"
        "v_cvt_pk_bf16_f32 v105, v106, v107\n"
        "v_mfma_f32_16x16x32_bf16 v[176:179], a[16:19], a[160:163], v[176:179]\n"
        "ds_write2st64_b32 v245, v104, v105 offset0:3 offset1:7\n"
        "v_mfma_f32_16x16x32_bf16 v[176:179], a[20:23], a[164:167], v[176:179]\n"
        "v_mul_f32 v108, v124, v108\n"
        "v_mul_f32 v109, v124, v109\n"
        "v_mfma_f32_16x16x32_bf16 v[164:167], a[20:23], a[140:143], 0\n"
        "v_mul_f32 v110, v124, v110\n"
        "v_mul_f32 v111, v124, v111\n"
        "v_mfma_f32_16x16x32_bf16 v[164:167], a[16:19], a[136:139], v[164:167]\n"
        "v_cvt_pk_bf16_f32 v106, v108, v109\n"
        "v_cvt_pk_bf16_f32 v107, v110, v111\n"
        "v_mfma_f32_16x16x32_bf16 v[164:167], a[12:15], a[132:135], v[164:167]\n"
        "v_mul_f32 v112, v124, v112\n"
        "v_mul_f32 v113, v124, v113\n"
        "v_mfma_f32_16x16x32_bf16 v[164:167], a[8:11], a[128:131], v[164:167]\n"
        "ds_write2st64_b32 v245, v106, v107 offset0:11 offset1:15\n"
        "v_mfma_f32_16x16x32_bf16 v[164:167], a[4:7], a[124:127], v[164:167]\n"
        "v_mul_f32 v114, v124, v114\n"
        "v_mul_f32 v115, v124, v115\n"
        "v_mfma_f32_16x16x32_bf16 v[164:167], a[0:3], a[120:123], v[164:167]\n"
        "v_cvt_pk_bf16_f32 v112, v112, v113\n"
        "v_cvt_pk_bf16_f32 v113, v114, v115\n"
        "v_mfma_f32_16x16x32_bf16 v[180:183], a[0:3], a[168:171], 0\n"
        "v_max3_u32 v236, v236, v124, v125\n"
        "v_cvt_pk_bf16_f32 v242, v156, v160\n"
        "v_mfma_f32_16x16x32_bf16 v[180:183], a[4:7], a[172:175], v[180:183]\n"
        "ds_write2st64_b32 v245, v112, v113 offset0:19 offset1:23\n"
        "v_mfma_f32_16x16x32_bf16 v[180:183], a[8:11], a[176:179], v[180:183]\n"
        "v_cvt_pk_bf16_f32 v243, v164, v176\n"
        "v_cmp_lt_u32 vcc, s24, v236\n"
        "v_mfma_f32_16x16x32_bf16 v[180:183], a[12:15], a[180:183], v[180:183]\n"
        "buffer_store_dwordx2 v[242:243], v250, s[16:19], s47 offen offset:0 sc1\n"
        "v_cvt_pk_bf16_f32 v232, v157, v161\n"
        "v_mfma_f32_16x16x32_bf16 v[180:183], a[16:19], a[184:187], v[180:183]\n"
        "s_cmp_lg_u64 vcc, 0\n"
        "s_addc_u32 s25, s25, s25\n"
        "v_mov_b32 v253, s25\n"
        "v_mfma_f32_16x16x32_bf16 v[180:183], a[20:23], a[188:191], v[180:183]\n"
        "ds_write_b32 v249, v253\n"
        "v_mfma_f32_16x16x32_bf16 v[184:187], a[20:23], a[212:215], 0\n"
        "v_cvt_pk_bf16_f32 v233, v165, v177\n"
        "v_cvt_pk_bf16_f32 v234, v158, v162\n"
        "v_mfma_f32_16x16x32_bf16 v[184:187], a[16:19], a[208:211], v[184:187]\n"
        "buffer_store_dwordx2 v[232:233], v250, s[16:19], s47 offen offset:384 sc1\n"
        "v_cvt_pk_bf16_f32 v235, v166, v178\n"
        "v_mfma_f32_16x16x32_bf16 v[184:187], a[12:15], a[204:207], v[184:187]\n"
        "s_waitcnt lgkmcnt(0)\n"
        "s_barrier\n"
        "ds_read_b128 a[24:27], v240 offset:512\n"
        "ds_read_b128 a[28:31], v240 offset:4608\n"
        "v_mfma_f32_16x16x32_bf16 v[184:187], a[8:11], a[200:203], v[184:187]\n"
        "buffer_store_dwordx2 v[234:235], v250, s[16:19], s47 offen offset:768 sc1\n"
        "ds_read_b128 a[32:35], v240 offset:8704\n"
        "v_mfma_f32_16x16x32_bf16 v[184:187], a[4:7], a[196:199], v[184:187]\n"
        "ds_read_b128 a[36:39], v240 offset:12800\n"
        "ds_read_b128 a[40:43], v240 offset:16896\n"
        "v_mfma_f32_16x16x32_bf16 v[184:187], a[0:3], a[192:195], v[184:187]\n"
        "s_waitcnt lgkmcnt(4)\n"
        "ds_read_b128 a[44:47], v240 offset:20992\n"
        "v_cvt_pk_bf16_f32 v242, v159, v163\n"
        "v_mfma_f32_16x16x32_bf16 v[52:55], a[24:27], a[168:171], 0\n"
        "s_waitcnt lgkmcnt(4)\n"
        "v_cvt_pk_bf16_f32 v243, v167, v179\n"
        "s_nop 0\n"
        "v_mfma_f32_16x16x32_bf16 v[52:55], a[28:31], a[172:175], v[52:55]\n"
        "s_waitcnt lgkmcnt(3)\n"
        "buffer_store_dwordx2 v[242:243], v250, s[16:19], s47 offen offset:1152 sc1\n"
        "s_nop 0\n"
        "v_mfma_f32_16x16x32_bf16 v[52:55], a[32:35], a[176:179], v[52:55]\n"
        "s_waitcnt lgkmcnt(2)\n"
        "v_cvt_pk_bf16_f32 v255, v180, v184\n"
        "v_cvt_pk_bf16_f32 v244, v181, v185\n"
        "v_mfma_f32_16x16x32_bf16 v[52:55], a[36:39], a[180:183], v[52:55]\n"
        "s_waitcnt lgkmcnt(1)\n"
        "buffer_store_dword v255, v238, s[16:19], s47 offen offset:0 sc1\n"
        "v_mfma_f32_16x16x32_bf16 v[52:55], a[40:43], a[184:187], v[52:55]\n"
        "s_waitcnt lgkmcnt(0)\n"
        "ds_read_b128 v[60:63], v241\n"
        "v_cvt_pk_bf16_f32 v239, v182, v186\n"
        "v_mfma_f32_16x16x32_bf16 v[52:55], a[44:47], a[188:191], v[52:55]\n"
        "v_cvt_pk_bf16_f32 v255, v183, v187\n"
        "v_mfma_f32_16x16x32_bf16 v[56:59], a[44:47], a[212:215], 0\n"
        "buffer_store_dword v244, v238, s[16:19], s47 offen offset:384 sc1\n"
        "v_mfma_f32_16x16x32_bf16 v[56:59], a[40:43], a[208:211], v[56:59]\n"
        "v_mfma_f32_16x16x32_bf16 v[56:59], a[36:39], a[204:207], v[56:59]\n"
        "v_mfma_f32_16x16x32_bf16 v[56:59], a[32:35], a[200:203], v[56:59]\n"
        "buffer_store_dword v239, v238, s[16:19], s47 offen offset:768 sc1\n"
        "v_mfma_f32_16x16x32_bf16 v[56:59], a[28:31], a[196:199], v[56:59]\n"
        "v_mfma_f32_16x16x32_bf16 v[56:59], a[24:27], a[192:195], v[56:59]\n"
        "s_nop 2\n"
        "v_mfma_f32_16x16x32_bf16 v[196:199], a[24:27], a[72:75], 0\n"
        "buffer_store_dword v255, v238, s[16:19], s47 offen offset:1152 sc1\n"
        "v_mfma_f32_16x16x32_bf16 v[196:199], a[28:31], a[76:79], v[196:199]\n"
        "s_nop 1\n"
        "v_cvt_pk_bf16_f32 v88, v52, v56\n"
        "v_mfma_f32_16x16x32_bf16 v[196:199], a[32:35], a[80:83], v[196:199]\n"
        "v_cvt_pk_bf16_f32 v90, v53, v57\n"
        "v_cvt_pk_bf16_f32 v92, v54, v58\n"
        "v_mfma_f32_16x16x32_bf16 v[196:199], a[36:39], a[84:87], v[196:199]\n"
        "buffer_store_dword v88, v238, s[16:19], s35 offen offset:0 sc1\n"
        "v_mfma_f32_16x16x32_bf16 v[196:199], a[40:43], a[88:91], v[196:199]\n"
        "v_cvt_pk_bf16_f32 v94, v55, v59\n"
        "v_mfma_f32_16x16x32_bf16 v[196:199], a[44:47], a[92:95], v[196:199]\n"
        "v_mfma_f32_16x16x32_bf16 v[48:51], a[44:47], a[164:167], 0\n"
        "buffer_store_dword v90, v238, s[16:19], s35 offen offset:384 sc1\n"
        "v_mfma_f32_16x16x32_bf16 v[48:51], a[40:43], a[160:163], v[48:51]\n"
        "s_waitcnt lgkmcnt(0)\n"
        "v_or3_b32 v60, v60, v61, v62\n"
        "v_or_b32 v60, v60, v63\n"
        "v_mfma_f32_16x16x32_bf16 v[48:51], a[36:39], a[156:159], v[48:51]\n"
        "s_nop 0\n"
        "v_readfirstlane_b32 s25, v60\n"
        "v_mfma_f32_16x16x32_bf16 v[48:51], a[32:35], a[152:155], v[48:51]\n"
        "buffer_store_dword v92, v238, s[16:19], s35 offen offset:768 sc1\n"
        "v_mfma_f32_16x16x32_bf16 v[48:51], a[28:31], a[148:151], v[48:51]\n"
        "v_mfma_f32_16x16x32_bf16 v[48:51], a[24:27], a[144:147], v[48:51]\n"
        "v_mfma_f32_16x16x32_bf16 v[204:207], a[24:27], a[120:123], 0\n"
        "buffer_store_dword v94, v238, s[16:19], s35 offen offset:1152 sc1\n"
        "v_mfma_f32_16x16x32_bf16 v[204:207], a[28:31], a[124:127], v[204:207]\n"
        "v_mfma_f32_16x16x32_bf16 v[204:207], a[32:35], a[128:131], v[204:207]\n"
        "v_mfma_f32_16x16x32_bf16 v[204:207], a[36:39], a[132:135], v[204:207]\n"
        "v_mfma_f32_16x16x32_bf16 v[204:207], a[40:43], a[136:139], v[204:207]\n"
        "v_mfma_f32_16x16x32_bf16 v[204:207], a[44:47], a[140:143], v[204:207]\n"
        "v_mfma_f32_16x16x32_bf16 v[200:203], a[44:47], a[116:119], 0\n"
        "v_mfma_f32_16x16x32_bf16 v[200:203], a[40:43], a[112:115], v[200:203]\n"
        "s_nop 2\n"
        "v_mfma_f32_16x16x32_bf16 v[200:203], a[36:39], a[108:111], v[200:203]\n"
        "s_nop 1\n"
        "v_cvt_pk_bf16_f32 v81, v204, v48\n"
        "v_mfma_f32_16x16x32_bf16 v[200:203], a[32:35], a[104:107], v[200:203]\n"
        "v_cvt_pk_bf16_f32 v83, v205, v49\n"
        "v_mfma_f32_16x16x32_bf16 v[200:203], a[28:31], a[100:103], v[200:203]\n"
        "v_cvt_pk_bf16_f32 v85, v206, v50\n"
        "v_mfma_f32_16x16x32_bf16 v[200:203], a[24:27], a[96:99], v[200:203]\n"
        "s_nop 2\n"
        "v_cvt_pk_bf16_f32 v87, v207, v51\n"
        "s_nop 3\n"
        "v_cvt_pk_bf16_f32 v80, v196, v200\n"
        "buffer_store_dwordx2 v[80:81], v250, s[16:19], s35 offen offset:0 sc1\n"
        "v_cvt_pk_bf16_f32 v82, v197, v201\n"
        "buffer_store_dwordx2 v[82:83], v250, s[16:19], s35 offen offset:384 sc1\n"
        "v_cvt_pk_bf16_f32 v84, v198, v202\n"
        "buffer_store_dwordx2 v[84:85], v250, s[16:19], s35 offen offset:768 sc1\n"
        "v_cvt_pk_bf16_f32 v86, v199, v203\n"
        "buffer_store_dwordx2 v[86:87], v250, s[16:19], s35 offen offset:1152 sc1\n"
        "s_cmp_lg_u32 s25, 0\n"
        "s_cbranch_scc1 30f\n"
        "s_endpgm\n"
        "30:\n"
        : "={s25}"(flg), "={s42}"(w), "+{s[0:1]}"(ka), "+{s2}"(bid), "+{v0}"(tid)
        : "s"(ldsb)
        : "v1", "v2", "v3", "v4", "v5", "v6", "v7", "v8", "v9", "v10", "v11", "v12", "v13", "v14", "v15", "v16", "v17", "v18", "v19", "v20", "v21", "v22", "v23", "v24", "v25", "v26", "v27", "v28", "v29", "v30", "v31", "v32", "v33", "v34", "v35", "v36", "v37", "v38", "v39", "v40", "v41", "v42", "v43", "v44", "v45", "v46", "v47", "v48", "v49", "v50", "v51", "v52", "v53", "v54", "v55", "v56", "v57", "v58", "v59", "v60", "v61", "v62", "v63", "v64", "v65", "v66", "v67", "v68", "v69", "v70", "v71", "v72", "v73", "v74", "v75", "v76", "v77", "v78", "v79", "v80", "v81", "v82", "v83", "v84", "v85", "v86", "v87", "v88", "v89", "v90", "v91", "v92", "v93", "v94", "v95", "v96", "v97", "v98", "v99", "v100", "v101", "v102", "v103", "v104", "v105", "v106", "v107", "v108", "v109", "v110", "v111", "v112", "v113", "v114", "v115", "v116", "v117", "v118", "v119", "v120", "v121", "v122", "v123", "v124", "v125", "v126", "v127", "v128", "v129", "v130", "v131", "v132", "v133", "v134", "v135", "v136", "v137", "v138", "v139", "v140", "v141", "v142", "v143", "v144", "v145", "v146", "v147", "v148", "v149", "v150", "v151", "v152", "v153", "v154", "v155", "v156", "v157", "v158", "v159", "v160", "v161", "v162", "v163", "v164", "v165", "v166", "v167", "v168", "v169", "v170", "v171", "v172", "v173", "v174", "v175", "v176", "v177", "v178", "v179", "v180", "v181", "v182", "v183", "v184", "v185", "v186", "v187", "v188", "v189", "v190", "v191", "v192", "v193", "v194", "v195", "v196", "v197", "v198", "v199", "v200", "v201", "v202", "v203", "v204", "v205", "v206", "v207", "v208", "v209", "v210", "v211", "v212", "v213", "v214", "v215", "v216", "v217", "v218", "v219", "v220", "v221", "v222", "v223", "v224", "v225", "v226", "v227", "v228", "v229", "v230", "v231", "v232", "v233", "v234", "v235", "v236", "v237", "v238", "v239", "v240", "v241", "v242", "v243", "v244", "v245", "v246", "v247", "v248", "v249", "v250", "v251", "v252", "v253", "v254", "v255", "a0", "a1", "a2", "a3", "a4", "a5", "a6", "a7", "a8", "a9", "a10", "a11", "a12", "a13", "a14", "a15", "a16", "a17", "a18", "a19", "a20", "a21", "a22", "a23", "a24", "a25", "a26", "a27", "a28", "a29", "a30", "a31", "a32", "a33", "a34", "a35", "a36", "a37", "a38", "a39", "a40", "a41", "a42", "a43", "a44", "a45", "a46", "a47", "a48", "a49", "a50", "a51", "a52", "a53", "a54", "a55", "a56", "a57", "a58", "a59", "a60", "a61", "a62", "a63", "a64", "a65", "a66", "a67", "a68", "a69", "a70", "a71", "a72", "a73", "a74", "a75", "a76", "a77", "a78", "a79", "a80", "a81", "a82", "a83", "a84", "a85", "a86", "a87", "a88", "a89", "a90", "a91", "a92", "a93", "a94", "a95", "a96", "a97", "a98", "a99", "a100", "a101", "a102", "a103", "a104", "a105", "a106", "a107", "a108", "a109", "a110", "a111", "a112", "a113", "a114", "a115", "a116", "a117", "a118", "a119", "a120", "a121", "a122", "a123", "a124", "a125", "a126", "a127", "a128", "a129", "a130", "a131", "a132", "a133", "a134", "a135", "a136", "a137", "a138", "a139", "a140", "a141", "a142", "a143", "a144", "a145", "a146", "a147", "a148", "a149", "a150", "a151", "a152", "a153", "a154", "a155", "a156", "a157", "a158", "a159", "a160", "a161", "a162", "a163", "a164", "a165", "a166", "a167", "a168", "a169", "a170", "a171", "a172", "a173", "a174", "a175", "a176", "a177", "a178", "a179", "a180", "a181", "a182", "a183", "a184", "a185", "a186", "a187", "a188", "a189", "a190", "a191", "a192", "a193", "a194", "a195", "a196", "a197", "a198", "a199", "a200", "a201", "a202", "a203", "a204", "a205", "a206", "a207", "a208", "a209", "a210", "a211", "a212", "a213", "a214", "a215", "a216", "a217", "a218", "a219", "a220", "a221", "a222", "a223", "a224", "a225", "a226", "a227", "a228", "a229", "a230", "a231", "a232", "a233", "a234", "a235", "a236", "a237", "a238", "a239", "a240", "a241", "a242", "a243", "a244", "a245", "a246", "a247", "a248", "a249", "a250", "a251", "a252", "a253", "a254", "a255", "s4", "s5", "s6", "s7", "s8", "s9", "s10", "s11", "s12", "s13", "s14", "s15", "s16", "s17", "s18", "s19", "s20", "s21", "s22", "s23", "s24", "s26", "s27", "s28", "s29", "s30", "s31", "s34", "s35", "s36", "s37", "s38", "s39", "s40", "s41", "s43", "s44", "s45", "s46", "s47", "s48", "s49", "s50", "s51", "s52", "s53", "s54", "s55", "s56", "s57", "s58", "s59", "s60", "s61", "s62", "s63", "s64", "s65", "s66", "s67", "s68", "s69", "s70", "s71", "s72", "s73", "s74", "s75", "s76", "s77", "s78", "s79", "s80", "s81", "s82", "s83", "vcc", "scc", "memory");
    if (flg) {
        unsigned ln;
        asm volatile("s_waitcnt vmcnt(0)\n v_mbcnt_lo_u32_b32 %0, -1, 0\n v_mbcnt_hi_u32_b32 %0, -1, %0" : "=v"(ln) :: "memory");
        const unsigned t = w * 64u + ln, wx = bid & 63u, wy0 = bid >> 6;
        typedef const char* __attribute__((address_space(4))) const* kargp;
        const char *x = ((kargp)ka)[0], *wqkv = ((kargp)ka)[1], *wo = ((kargp)ka)[2], *rpb = ((kargp)ka)[3], *rpi = ((kargp)ka)[4],
                   *mask = ((kargp)ka)[5];
        char* out = (char*)((kargp)ka)[6];
        unsigned short* xs = (unsigned short*)smem;
        unsigned short* qs = xs + 64 * 192;
        const unsigned short* W1 = (const unsigned short*)wqkv;
        const unsigned short* W2 = (const unsigned short*)wo;
        for (unsigned i = 0; i < 16u; ++i) {
            if (!((flg >> (15u - i)) & 1u)) continue;
            const unsigned wy = wy0 + 4u * i;
            __syncthreads();
            for (unsigned e = t; e < 64u * 96u; e += 256u) {
                const unsigned tok = e / 96u, c2 = e % 96u;
                const unsigned pix = ((8u * wy + 4u + (tok >> 3)) & 511u) * 512u + ((8u * wx + 4u + (tok & 7u)) & 511u);
                ((unsigned*)xs)[e] = ((const unsigned*)x)[pix * 96u + c2];
            }
            __syncthreads();
            for (unsigned col = t; col < 576u; col += 256u)
                for (unsigned tb = 0; tb < 64u; tb += 8u) {
                    float a[8] = {0.f, 0.f, 0.f, 0.f, 0.f, 0.f, 0.f, 0.f};
                    for (unsigned c = 0; c < 192u; ++c) {
                        const float wt = bf2f(W1[c * 576u + col]);
#pragma unroll
                        for (int j = 0; j < 8; ++j) a[j] += bf2f(xs[(tb + j) * 192u + c]) * wt;
                    }
#pragma unroll
                    for (int j = 0; j < 8; ++j) qs[(tb + j) * 578u + col] = f2bf(a[j]);
                }
            __syncthreads();
            {
                float qv[48], o[48];
#pragma unroll
                for (int d = 0; d < 48; ++d) { qv[d] = bf2f(qs[ln * 578u + w * 48u + d]); o[d] = 0.f; }
                const unsigned short* mrow = (const unsigned short*)mask + (size_t)(wy * 64u + wx) * 4096u + ln * 64u;
                float mx = -__builtin_inff(), l = 0.f;
                for (int pass = 0; pass < 2; ++pass)
                    for (unsigned k = 0; k < 64u; ++k) {
                        float s = 0.f;
#pragma unroll
                        for (int d = 0; d < 48; ++d) s += qv[d] * bf2f(qs[k * 578u + 192u + w * 48u + d]);
                        s = s * 0.14433756729740643f + bf2f(((const unsigned short*)rpb)[((const unsigned*)rpi)[ln * 64u + k] * 4u + w]) + bf2f(mrow[k]);
                        if (pass == 0) mx = fmaxf(mx, s);
                        else {
                            const float p = __expf(s - mx);
                            l += p;
#pragma unroll
                            for (int d = 0; d < 48; ++d) o[d] += p * bf2f(qs[k * 578u + 384u + w * 48u + d]);
                        }
                    }
#pragma unroll
                for (int d = 0; d < 48; ++d) xs[ln * 192u + w * 48u + d] = f2bf(o[d] / l);
            }
            __syncthreads();
            if (t < 192u)
                for (unsigned tb = 0; tb < 64u; tb += 8u) {
                    float a[8] = {0.f, 0.f, 0.f, 0.f, 0.f, 0.f, 0.f, 0.f};
                    for (unsigned c = 0; c < 192u; ++c) {
                        const float wt = bf2f(W2[c * 192u + t]);
#pragma unroll
                        for (int j = 0; j < 8; ++j) a[j] += bf2f(xs[(tb + j) * 192u + c]) * wt;
                    }
                    const unsigned row = (8u * wy + 4u + (tb >> 3)) & 511u;
#pragma unroll
                    for (int j = 0; j < 8; ++j)
                        ((unsigned short*)out)[(row * 512u + ((8u * wx + 4u + j) & 511u)) * 192u + t] = f2bf(a[j]);
                }
        }
    }
}


extern "C" void kernel_launch(void* const* d_in, const int* in_sizes, int n_in, void* d_out, int out_size, void* d_ws,
                              size_t ws_size, hipStream_t stream) {
    win_attn_kernel<<<256, 256, 0, stream>>>((const char*)d_in[0], (const char*)d_in[1], (const char*)d_in[2],
                                             (const char*)d_in[3], (const char*)d_in[4], (const char*)d_in[5],
                                             (char*)d_out, (char*)d_ws);

}
```

```cpp
#include <hip/hip_runtime.h>
#include <hip/hip_bf16.h>

typedef unsigned int u32x4 __attribute__((ext_vector_type(4)));

static __device__ __forceinline__ float bf2f(unsigned short u) { return __uint_as_float((unsigned)u << 16); }
static __device__ __forceinline__ unsigned short f2bf(float f) { return __builtin_bit_cast(unsigned short, (__bf16)f); }

__global__ __launch_bounds__(256, 1) __attribute__((aligned(4096))) void win_attn_kernel(const char* x_, const char* wqkv_, const char* wo_, const char* rpb_,
                                                          const char* rpi_, const char* mask_, char* out_, char* ws) {
    __shared__ __attribute__((aligned(16))) char smem[163840];
    const void* ka = (const void*)__builtin_amdgcn_kernarg_segment_ptr();
    unsigned bid = blockIdx.x, tid = threadIdx.x, flg, w;
    unsigned ldsb = (unsigned)(size_t)smem;
    asm volatile(
        "s_load_dwordx16 s[68:83], s[0:1], 0\n"
        "v_readfirstlane_b32 s42, v0\n"
        "v_and_b32 v1, 63, v0\n"
        "s_lshr_b32 s31, s2, 1\n"
        "s_lshr_b32 s42, s42, 6\n"
        "s_and_b32 s31, s31, 0x7c\n"
        "v_lshlrev_b32 v2, 4, v1\n"
        "s_add_u32 s31, s31, s42\n"
        "s_mul_i32 s35, s31, 256\n"
        "v_min_u32 v10, 3, v1\n"
        "s_getpc_b64 s[66:67]\n"
        "v_lshl_add_u32 v10, v10, 6, s35\n"
        "global_load_dword v60, v10, s[66:67]\n"
        "s_mul_i32 s50, s31, 9\n"
        "v_min_u32 v136, 8, v1\n"
        "v_add_u32 v136, s50, v136\n"
        "v_mul_u32_u24 v133, 10923, v136\n"
        "v_lshlrev_b32 v136, 6, v136\n"
        "v_lshrrev_b32 v133, 16, v133\n"
        "v_mul_u32_u24 v133, 384, v133\n"
        "v_lshl_add_u32 v133, v136, 1, v133\n"
        "v_mul_u32_u24 v11, 171, v1\n"
        "v_lshrrev_b32 v4, 10, v11\n"
        "v_bfe_u32 v7, v11, 13, 1\n"
        "v_add_u32 v11, 10944, v11\n"
        "v_lshrrev_b32 v5, 10, v11\n"
        "v_bfe_u32 v8, v11, 13, 1\n"
        "v_add_u32 v11, 10944, v11\n"
        "v_lshrrev_b32 v6, 10, v11\n"
        "v_bfe_u32 v9, v11, 13, 1\n"
        "s_mul_i32 s40, s42, 3072\n"
        "s_mov_b32 s10, 100663296\n"
        "s_mov_b32 s11, 0x20000\n"
        "s_mov_b32 s6, 16384\n"
        "s_mov_b32 s7, 0x20000\n"
        "s_mov_b32 s62, 1800\n"
        "s_mov_b32 s63, 0x20000\n"
        "s_mov_b32 s22, 221184\n"
        "s_mov_b32 s23, 0x20000\n"
        "s_and_b32 s51, s42, 1\n"
        "v_lshlrev_b32 v134, 3, v0\n"
        "s_lshl_b32 s35, s51, 7\n"
        "v_and_b32 v10, 48, v1\n"
        "v_xor_b32 v131, s35, v2\n"
        "s_lshl_b32 s46, s42, 12\n"
        "v_xor_b32 v131, v131, v10\n"
        "v_xor_b32 v132, 64, v131\n"
        "s_and_b32 s44, s2, 63\n"
        "s_lshl3_add_u32 s44, s44, 4\n"
        "s_lshl_b32 s35, s42, 5\n"
        "s_movk_i32 s41, 384\n"
        "s_movk_i32 s45, -384\n"
        "v_lshrrev_b32 v10, 2, v4\n"
        "v_mad_i32_i24 v11, v10, s45, v2\n"
        "v_add_u32 v10, s44, v10\n"
        "v_lshl_or_b32 v242, v7, 4, s35\n"
        "v_and_b32 v10, 511, v10\n"
        "v_xor_b32 v11, v11, v242\n"
        "v_mad_u32_u24 v242, v10, s41, v11\n"
        "v_lshrrev_b32 v10, 2, v5\n"
        "v_mad_i32_i24 v11, v10, s45, v2\n"
        "v_add_u32 v10, s44, v10\n"
        "v_add_u32 v11, 1024, v11\n"
        "v_lshl_or_b32 v243, v8, 4, s35\n"
        "v_and_b32 v10, 511, v10\n"
        "v_xor_b32 v11, v11, v243\n"
        "v_mad_u32_u24 v243, v10, s41, v11\n"
        "v_lshrrev_b32 v10, 2, v6\n"
        "v_mad_i32_i24 v11, v10, s45, v2\n"
        "v_add_u32 v10, s44, v10\n"
        "v_add_u32 v11, 2048, v11\n"
        "v_lshl_or_b32 v244, v9, 4, s35\n"
        "v_and_b32 v10, 511, v10\n"
        "v_xor_b32 v11, v11, v244\n"
        "v_mad_u32_u24 v244, v10, s41, v11\n"
        "s_mul_i32 s43, s44, 384\n"
        "s_add_u32 s44, s44, 4\n"
        "s_and_b32 s44, s44, 511\n"
        "s_mul_i32 s44, s44, 384\n"
        "s_lshr_b32 s37, s2, 6\n"
        "s_lshl3_add_u32 s47, s37, s42\n"
        "s_add_u32 s47, s47, 68\n"
        "s_mul_i32 s47, s47, 196608\n"
        "s_lshl1_add_u32 s37, s37, s51\n"
        "s_add_u32 s37, s37, 1\n"
        "s_mul_i32 s37, s37, 786432\n"
        "s_mul_i32 s65, s42, 18432\n"
        "s_mov_b32 s14, 33554432\n"
        "s_mov_b32 s15, 0x20000\n"
        "s_mul_i32 s35, s42, 96\n"
        "s_movk_i32 s31, 1056\n"
        "s_movk_i32 s45, 4608\n"
        "v_add_u32 v3, s35, v2\n"
        "s_waitcnt lgkmcnt(0)\n"
        "s_mov_b64 s[60:61], s[74:75]\n"
        "s_mov_b64 s[20:21], s[70:71]\n"
        "s_mov_b64 s[4:5], s[76:77]\n"
        "s_mov_b64 s[8:9], s[68:69]\n"
        "buffer_load_dwordx2 v[144:145], v134, s[60:63], 0 offen\n"
        "s_add_u32 m0, s46, 122880\n"
        "s_mov_b32 s64, s46\n"
        "buffer_load_dword v133, s[20:23], 0 offen lds\n"
        "s_mov_b64 s[52:53], s[72:73]\n"
        "s_mov_b64 s[12:13], s[78:79]\n"
        "buffer_load_dwordx4 v131, s[4:7], s64 offen lds\n"
        "s_mov_b64 s[16:17], s[80:81]\n"
        "v_bfe_u32 v12, v4, 2, 1\n"
        "v_mad_u32_u24 v128, v4, s31, v3\n"
        "v_sub_u32 v12, v12, v7\n"
        "v_mad_i32_i24 v128, v12, s45, v128\n"
        "v_bfe_u32 v13, v5, 2, 1\n"
        "buffer_load_dwordx4 v131, s[4:7], s64 offen offset:1024 lds\n"
        "v_mad_u32_u24 v129, v5, s31, v3\n"
        "v_sub_u32 v13, v13, v8\n"
        "v_mad_i32_i24 v129, v13, s45, v129\n"
        "v_bfe_u32 v14, v6, 2, 1\n"
        "v_mad_u32_u24 v130, v6, s31, v3\n"
        "v_sub_u32 v14, v14, v9\n"
        "v_mad_i32_i24 v130, v14, s45, v130\n"
        "s_lshl_b32 s48, s42, 11\n"
        "s_lshl_b32 s36, s2, 13\n"
        "v_and_b32 v19, 16, v1\n"
        "buffer_load_dwordx4 v132, s[4:7], s64 offen offset:2048 lds\n"
        "s_add_u32 s48, s48, 155648\n"
        "s_add_u32 s36, s36, s48\n"
        "s_add_u32 s36, s36, 4038656\n"
        "s_lshl_b32 s35, s51, 6\n"
        "v_xor_b32 v249, v2, v19\n"
        "v_xor_b32 v249, s35, v249\n"
        "v_xor_b32 v253, 32, v249\n"
        "v_and_b32 v15, 15, v1\n"
        "v_lshrrev_b32 v16, 4, v1\n"
        "v_and_b32 v17, 12, v1\n"
        "buffer_load_dwordx4 v132, s[4:7], s64 offen offset:3072 lds\n"
        "v_and_b32 v18, 0xf0, v2\n"
        "v_add_u32 v17, v17, v15\n"
        "v_bfe_u32 v10, v1, 2, 3\n"
        "v_lshrrev_b32 v11, 5, v1\n"
        "v_lshl_or_b32 v10, v11, 4, v10\n"
        "v_and_b32 v255, 3, v1\n"
        "v_mul_u32_u24 v10, 96, v10\n"
        "v_lshl_add_u32 v255, v255, 3, s65\n"
        "v_add_u32 v255, v255, v10\n"
        "v_lshrrev_b32 v20, 1, v15\n"
        "s_add_u32 m0, s40, 73728\n"
        "s_sub_u32 s64, s47, 12582912\n"
        "buffer_load_dwordx4 v242, s[8:11], s64 offen lds\n"
        "v_xor_b32 v20, v20, v16\n"
        "v_lshlrev_b32 v20, 4, v20\n"
        "v_mad_u32_u24 v240, v17, s41, v20\n"
        "v_lshl_or_b32 v251, v17, 7, v20\n"
        "v_add_u32 v240, 73728, v240\n"
        "v_add_u32 v251, 155648, v251\n"
        "v_xor_b32 v241, 64, v240\n"
        "v_xor_b32 v252, 64, v251\n"
        "s_lshl_b32 s67, s42, 10\n"
        "s_add_u32 s67, s67, 139264\n"
        "s_add_u32 m0, s40, 74752\n"
        "s_nop 0\n"
        "buffer_load_dwordx4 v243, s[8:11], s64 offen lds\n"
        "v_lshlrev_b32 v135, 2, v0\n"
        "v_add_u32 v135, 139264, v135\n"
        "v_lshlrev_b32 v11, 5, v16\n"
        "v_xor_b32 v11, v11, v18\n"
        "v_lshl_or_b32 v11, v17, 8, v11\n"
        "v_add_u32 v140, 122880, v11\n"
        "v_xor_b32 v141, 16, v140\n"
        "v_xor_b32 v142, 128, v140\n"
        "v_xor_b32 v143, 144, v140\n"
        "v_lshrrev_b32 v10, 2, v15\n"
        "s_add_u32 m0, s40, 75776\n"
        "s_nop 0\n"
        "buffer_load_dwordx4 v244, s[8:11], s64 offen lds\n"
        "v_cmp_eq_u32 vcc, v10, v16\n"
        "v_mov_b32 v11, 0x3fb9\n"
        "v_mov_b32 v189, 0\n"
        "v_and_b32 v10, 48, v2\n"
        "v_cndmask_b32 v188, 0, v11, vcc\n"
        "v_lshlrev_b64 v[188:189], v10, v[188:189]\n"
        "v_cmp_gt_u32 vcc, 32, v1\n"
        "v_mov_b32 v11, 15957\n"
        "v_lshrrev_b32 v149, 2, v15\n"
        "v_lshrrev_b32 v151, 3, v19\n"
        "v_mov_b32 v148, 14702\n"
        "v_cndmask_b32 v11, v148, v11, vcc\n"
        "v_xor_b32 v151, v149, v151\n"
        "v_cmp_eq_u32 vcc, 0, v151\n"
        "v_mov_b32 v149, 0\n"
        "v_cndmask_b32 v148, 0, v11, vcc\n"
        "v_lshlrev_b64 v[148:149], v10, v[148:149]\n"
        "v_cmp_eq_u32 vcc, 1, v151\n"
        "v_mov_b32 v151, 0\n"
        "v_cndmask_b32 v150, 0, v11, vcc\n"
        "v_lshlrev_b64 v[150:151], v10, v[150:151]\n"
        "v_and_b32 v10, 8, v1\n"
        "v_add_u32 v10, v10, v15\n"
        "v_mul_u32_u24 v10, 96, v10\n"
        "v_add3_u32 v137, v10, v19, s65\n"
        "v_and_b32 v237, 1, v1\n"
        "v_mul_u32_u24 v237, 0xaaaa, v237\n"
        "v_mov_b64 v[170:171], v[188:189]\n"
        "v_mov_b64 v[210:211], v[188:189]\n"
        "v_mov_b64 v[228:229], v[188:189]\n"
        "v_add_u32 v237, 0x4444, v237\n"
        "v_mov_b32 v11, 0x3f803f80\n"
        "v_accvgpr_write_b32 a252, v11\n"
        "v_accvgpr_write_b32 a253, v11\n"
        "v_accvgpr_write_b32 a254, v11\n"
        "v_accvgpr_write_b32 a255, v11\n"
        "v_lshl_or_b32 v10, v16, 10, v18\n"
        "s_mul_i32 s31, s42, 6144\n"
        "s_add_u32 s31, s31, 73728\n"
        "s_lshl_b32 s35, s51, 9\n"
        "s_add_u32 s35, s35, 73728\n"
        "v_lshl_or_b32 v11, v16, 2, v18\n"
        "v_add_u32 v247, s35, v10\n"
        "v_add_u32 v245, s31, v11\n"
        "v_mul_u32_u24 v10, 384, v16\n"
        "s_movk_i32 s45, 1728\n"
        "v_lshrrev_b32 v235, 2, v15\n"
        "v_lshrrev_b32 v254, 1, v15\n"
        "v_and_b32 v11, 1, v1\n"
        "v_mad_u32_u24 v235, v235, 48, v10\n"
        "v_and_b32 v248, 1, v254\n"
        "v_mad_u32_u24 v235, v248, s45, v235\n"
        "v_mul_u32_u24 v248, 36, v11\n"
        "v_lshlrev_b32 v11, 5, v11\n"
        "v_xor_b32 v254, v254, v248\n"
        "v_add3_u32 v235, v235, v11, s65\n"
        "v_mad_u32_u24 v254, v254, 48, v10\n"
        "v_add3_u32 v254, v254, 16, s65\n"
        "s_lshr_b32 s31, s42, 1\n"
        "s_mul_i32 s35, s31, 36864\n"
        "s_movk_i32 s45, 18048\n"
        "v_mad_u32_u24 v10, v15, 48, v10\n"
        "v_lshrrev_b32 v248, 3, v15\n"
        "v_mad_u32_u24 v10, v248, s45, v10\n"
        "v_add_u32 v248, s35, v10\n"
        "s_lshr_b32 s49, s42, 1\n"
        "s_mul_i32 s49, s49, -192\n"
        "s_add_u32 s49, s49, 128\n"
        "s_mul_i32 s31, s31, 256\n"
        "v_mul_u32_u24 v10, 196608, v16\n"
        "v_mul_u32_u24 v11, 8, v15\n"
        "v_add3_u32 v250, v10, v11, s31\n"
        "s_lshl_b32 s35, s51, 6\n"
        "s_add_u32 s66, s31, s35\n"
        "s_lshl_b32 s35, s42, 5\n"
        "s_sub_u32 s35, s35, s66\n"
        "s_add_u32 s27, s35, 64\n"
        "s_movk_i32 s31, 288\n"
        "s_movk_i32 s35, 768\n"
        "v_mad_u32_u24 v10, v4, s31, v2\n"
        "v_bfe_u32 v11, v10, 6, 1\n"
        "v_add_u32 v10, s66, v10\n"
        "v_mad_i32_i24 v10, v11, s27, v10\n"
        "v_bfe_u32 v11, v4, 1, 1\n"
        "v_bfe_u32 v232, v4, 2, 1\n"
        "v_sub_u32 v11, v11, v232\n"
        "v_mad_i32_i24 v232, v11, s35, v10\n"
        "v_mad_u32_u24 v10, v5, s31, v2\n"
        "v_bfe_u32 v11, v10, 6, 1\n"
        "v_add_u32 v10, s66, v10\n"
        "v_mad_i32_i24 v10, v11, s27, v10\n"
        "v_bfe_u32 v11, v5, 1, 1\n"
        "v_bfe_u32 v233, v5, 2, 1\n"
        "v_sub_u32 v11, v11, v233\n"
        "v_mad_i32_i24 v233, v11, s35, v10\n"
        "v_mad_u32_u24 v10, v6, s31, v2\n"
        "v_bfe_u32 v11, v10, 6, 1\n"
        "v_add_u32 v10, s66, v10\n"
        "v_mad_i32_i24 v10, v11, s27, v10\n"
        "v_bfe_u32 v11, v6, 1, 1\n"
        "v_bfe_u32 v234, v6, 2, 1\n"
        "v_sub_u32 v11, v11, v234\n"
        "v_mad_i32_i24 v234, v11, s35, v10\n"
        "v_mad_i32_i24 v138, v136, -3, v133\n"
        "v_add_u32 v138, v138, v133\n"
        "s_mov_b32 s26, 0x3fb8aa3b\n"
        "s_mov_b32 s24, 0x5d800000\n"
        "s_mov_b32 s27, 14\n"
        "s_mov_b32 s28, 98304\n"
        "s_mov_b32 s29, 73728\n"
        "s_mov_b32 s30, 122880\n"
        "s_mov_b32 s45, 0\n"
        "s_mov_b32 s25, 0\n"
        "s_mov_b32 s54, 73728\n"
        "s_mov_b32 s55, 0x20000\n"
        "s_mov_b32 s18, 100663296\n"
        "s_mov_b32 s19, 0x20000\n"
        "s_mov_b32 s56, 36864\n"
        "s_mov_b32 s57, 73728\n"
        "s_mov_b32 s58, 110592\n"
        "s_mov_b32 s59, 147456\n"
        "s_mov_b32 s60, 184320\n"
        "s_mov_b32 s61, 24576\n"
        "s_mov_b32 s62, 61440\n"
        "s_mov_b32 s63, 12288\n"
        "s_mov_b32 s34, 49152\n"
        "s_add_u32 m0, s40, 86016\n"
        "s_sub_u32 s64, s47, 11796480\n"
        "buffer_load_dwordx4 v242, s[8:11], s64 offen lds\n"
        "s_add_u32 m0, s40, 87040\n"
        "s_nop 0\n"
        "buffer_load_dwordx4 v243, s[8:11], s64 offen lds\n"
        "s_waitcnt vmcnt(10)\n"
        "v_and_b32 v146, 0xffff0000, v144\n"
        "v_and_b32 v147, 0xffff0000, v145\n"
        "v_lshlrev_b32 v144, 16, v144\n"
        "v_lshlrev_b32 v145, 16, v145\n"
        "s_add_u32 m0, s40, 88064\n"
        "v_mul_f32 v146, s26, v146\n"
        "buffer_load_dwordx4 v244, s[8:11], s64 offen lds\n"
        "v_mul_f32 v147, s26, v147\n"
        "v_mul_f32 v144, s26, v144\n"
        "v_mul_f32 v145, s26, v145\n"
        "s_mov_b32 s38, s67\n"
        "s_mov_b32 s39, s67\n"
        "ds_write2st64_b32 v135, v144, v146 offset0:0 offset1:4\n"
        "ds_write2st64_b32 v135, v145, v147 offset0:8 offset1:12\n"
        "s_cmp_eq_u32 s42, 0\n"
        "s_cbranch_scc1 41f\n"
        "s_cmp_eq_u32 s42, 1\n"
        "s_cbranch_scc1 42f\n"
        "s_cmp_eq_u32 s42, 2\n"
        "s_cbranch_scc1 43f\n"
        "s_cmp_eq_u32 s42, 3\n"
        "s_cbranch_scc1 44f\n"
        "60:\n"
        "s_waitcnt lgkmcnt(0)\n"
        "s_waitcnt vmcnt(6)\n"
        "s_barrier\n"
        "s_add_u32 m0, s65, 0\n"
        "s_mov_b32 s64, 384\n"
        "buffer_load_dwordx4 v128, s[20:23], s64 offen lds\n"
        "buffer_load_dwordx4 v129, s[20:23], s64 offen offset:1024 lds\n"
        "ds_read_b128 v[0:3], v140 offset:0\n"
        "ds_read_b128 v[4:7], v141 offset:0\n"
        "ds_read_b128 v[16:19], v140 offset:1024\n"
        "ds_read_b128 v[20:23], v141 offset:1024\n"
        "ds_read_b128 v[8:11], v142 offset:0\n"
        "ds_read_b128 v[24:27], v142 offset:1024\n"
        "ds_read_b128 v[12:15], v143 offset:0\n"
        "ds_read_b128 v[28:31], v143 offset:1024\n"
        "ds_read_b128 v[32:35], v140 offset:8192\n"
        "ds_read_b128 v[36:39], v141 offset:8192\n"
        "ds_read_b128 v[40:43], v142 offset:8192\n"
        "ds_read_b128 v[44:47], v143 offset:8192\n"
        "ds_read_b128 v[48:51], v140 offset:9216\n"
        "s_cmp_eq_u32 s42, 0\n"
        "s_cbranch_scc1 51f\n"
        "s_cmp_eq_u32 s42, 1\n"
        "s_cbranch_scc1 45f\n"
        "s_cmp_eq_u32 s42, 2\n"
        "s_cbranch_scc1 46f\n"
        "s_cmp_eq_u32 s42, 3\n"
        "s_cbranch_scc1 47f\n"
        "s_nop 0\n"
        "s_nop 0\n"
        "s_nop 0\n"
        "s_nop 0\n"
        "s_nop 0\n"
        "s_nop 0\n"
        "s_nop 0\n"
        "s_nop 0\n"
        "s_nop 0\n"
        "s_nop 0\n"
        "s_nop 0\n"
        "s_nop 0\n"
        "s_nop 0\n"
        "s_nop 0\n"
        "s_nop 0\n"
        "s_nop 0\n"
        "s_nop 0\n"
        "s_nop 0\n"
        "s_nop 0\n"
        "s_nop 0\n"
        "s_nop 0\n"
        "s_nop 0\n"
        "61:\n"
        "buffer_load_dwordx4 v130, s[20:23], s64 offen offset:2048 lds\n"
        "s_add_u32 m0, s65, 3072\n"
        "s_waitcnt lgkmcnt(12)\n"
        "v_lshl_add_u64 v[0:1], v[0:1], 2, s[38:39]\n"
        "buffer_load_dwordx4 v128, s[20:23], 0 offen lds\n"
        "v_lshl_add_u64 v[2:3], v[2:3], 2, s[38:39]\n"
        "s_waitcnt lgkmcnt(11)\n"
        "v_lshl_add_u64 v[4:5], v[4:5], 2, s[38:39]\n"
        "v_lshl_add_u64 v[6:7], v[6:7], 2, s[38:39]\n"
        "s_waitcnt lgkmcnt(10)\n"
        "v_lshl_add_u64 v[16:17], v[16:17], 2, s[38:39]\n"
        "v_lshl_add_u64 v[18:19], v[18:19], 2, s[38:39]\n"
        "buffer_load_dwordx4 v129, s[20:23], 0 offen offset:1024 lds\n"
        "ds_read_b32 v0, v0\n"
        "ds_read_b32 v1, v1\n"
        "ds_read_b32 v2, v2\n"
        "ds_read_b32 v3, v3\n"
        "ds_read_b32 v4, v4\n"
        "ds_read_b32 v5, v5\n"
        "ds_read_b32 v6, v6\n"
        "ds_read_b32 v7, v7\n"
        "ds_read_b32 v16, v16\n"
        "ds_read_b32 v17, v17\n"
        "ds_read_b32 v18, v18\n"
        "ds_read_b32 v19, v19\n"
        "buffer_load_dwordx4 v130, s[20:23], 0 offen offset:2048 lds\n"
        "s_waitcnt lgkmcnt(15)\n"
        "v_lshl_add_u64 v[20:21], v[20:21], 2, s[38:39]\n"
        "v_lshl_add_u64 v[22:23], v[22:23], 2, s[38:39]\n"
        "v_lshl_add_u64 v[8:9], v[8:9], 2, s[38:39]\n"
        "v_lshl_add_u64 v[10:11], v[10:11], 2, s[38:39]\n"
        "v_lshl_add_u64 v[24:25], v[24:25], 2, s[38:39]\n"
        "v_lshl_add_u64 v[26:27], v[26:27], 2, s[38:39]\n"
        "ds_read_b32 v20, v20\n"
        "ds_read_b32 v21, v21\n"
        "ds_read_b32 v22, v22\n"
        "ds_read_b32 v23, v23\n"
        "ds_read_b32 v8, v8\n"
        "ds_read_b32 v9, v9\n"
        "ds_read_b32 v10, v10\n"
        "ds_read_b32 v11, v11\n"
        "ds_read_b32 v24, v24\n"
        "ds_read_b32 v25, v25\n"
        "ds_read_b32 v26, v26\n"
        "ds_read_b32 v27, v27\n"
        "s_add_u32 m0, s65, 6144\n"
        "v_lshl_add_u64 v[12:13], v[12:13], 2, s[38:39]\n"
        "buffer_load_dwordx4 v128, s[20:23], s56 offen lds\n"
        "v_lshl_add_u64 v[14:15], v[14:15], 2, s[38:39]\n"
        "v_lshl_add_u64 v[28:29], v[28:29], 2, s[38:39]\n"
        "v_lshl_add_u64 v[30:31], v[30:31], 2, s[38:39]\n"
        "v_lshl_add_u64 v[32:33], v[32:33], 2, s[38:39]\n"
        "v_lshl_add_u64 v[34:35], v[34:35], 2, s[38:39]\n"
        "ds_read_b32 v12, v12\n"
        "ds_read_b32 v13, v13\n"
        "ds_read_b32 v14, v14\n"
        "ds_read_b32 v15, v15\n"
        "ds_read_b32 v28, v28\n"
        "ds_read_b32 v29, v29\n"
        "ds_read_b32 v30, v30\n"
        "ds_read_b32 v31, v31\n"
        "ds_read_b32 v32, v32\n"
        "ds_read_b32 v33, v33\n"
        "ds_read_b32 v34, v34\n"
        "ds_read_b32 v35, v35\n"
        "buffer_load_dwordx4 v129, s[20:23], s56 offen offset:1024 lds\n"
        "v_lshl_add_u64 v[36:37], v[36:37], 2, s[38:39]\n"
        "v_lshl_add_u64 v[38:39], v[38:39], 2, s[38:39]\n"
        "s_waitcnt lgkmcnt(15)\n"
        "v_lshl_add_u64 v[40:41], v[40:41], 2, s[38:39]\n"
        "v_lshl_add_u64 v[42:43], v[42:43], 2, s[38:39]\n"
        "v_lshl_add_u64 v[44:45], v[44:45], 2, s[38:39]\n"
        "v_lshl_add_u64 v[46:47], v[46:47], 2, s[38:39]\n"
        "ds_read_b32 v36, v36\n"
        "ds_read_b32 v37, v37\n"
        "ds_read_b32 v38, v38\n"
        "ds_read_b32 v39, v39\n"
        "ds_read_b32 v40, v40\n"
        "ds_read_b32 v41, v41\n"
        "ds_read_b32 v42, v42\n"
        "ds_read_b32 v43, v43\n"
        "ds_read_b32 v44, v44\n"
        "ds_read_b32 v45, v45\n"
        "ds_read_b32 v46, v46\n"
        "ds_read_b32 v47, v47\n"
        "buffer_load_dwordx4 v130, s[20:23], s56 offen offset:2048 lds\n"
        "v_lshl_add_u64 v[48:49], v[48:49], 2, s[38:39]\n"
        "v_lshl_add_u64 v[50:51], v[50:51], 2, s[38:39]\n"
        "ds_read_b32 v48, v48\n"
        "ds_read_b32 v49, v49\n"
        "ds_read_b32 v50, v50\n"
        "ds_read_b32 v51, v51\n"
        "s_waitcnt vmcnt(9)\n"
        "s_barrier\n"
        "ds_read_b128 a[216:219], v240 offset:0\n"
        "ds_read_b128 a[220:223], v240 offset:1536\n"
        "ds_read_b128 a[224:227], v240 offset:12288\n"
        "ds_read_b128 a[228:231], v240 offset:13824\n"
        "s_waitcnt vmcnt(6)\n"
        "ds_read_b64_tr_b16 a[72:73], v255 offset:0\n"
        "ds_read_b64_tr_b16 a[74:75], v255 offset:768\n"
        "ds_read_b64_tr_b16 a[96:97], v255 offset:32\n"
        "ds_read_b64_tr_b16 a[98:99], v255 offset:800\n"
        "ds_read_b64_tr_b16 a[120:121], v255 offset:64\n"
        "ds_read_b64_tr_b16 a[122:123], v255 offset:832\n"
        "s_add_u32 m0, s65, 8832\n"
        "s_nop 0\n"
        "buffer_load_dwordx4 v128, s[20:23], s56 offen offset:384 lds\n"
        "s_waitcnt lgkmcnt(4)\n"
        "v_mfma_f32_16x16x32_bf16 v[152:155], a[72:75], a[216:219], 0\n"
        "ds_read_b128 a[232:235], v241 offset:0\n"
        "ds_read_b128 a[236:239], v241 offset:1536\n"
        "v_mfma_f32_16x16x32_bf16 v[172:175], a[72:75], a[220:223], 0\n"
        "ds_read_b128 a[240:243], v241 offset:12288\n"
        "ds_read_b128 a[244:247], v241 offset:13824\n"
        "v_mfma_f32_16x16x32_bf16 v[192:195], a[72:75], a[224:227], 0\n"
        "v_mfma_f32_16x16x32_bf16 v[212:215], a[72:75], a[228:231], 0\n"
        "s_waitcnt lgkmcnt(6)\n"
        "buffer_load_dwordx4 v129, s[20:23], s56 offen offset:1408 lds\n"
        "v_mfma_f32_16x16x32_bf16 v[88:91], a[96:99], a[228:231], 0\n"
        "v_mfma_f32_16x16x32_bf16 v[80:83], a[96:99], a[224:227], 0\n"
        "buffer_load_dwordx4 v130, s[20:23], s56 offen offset:2432 lds\n"
        "s_waitcnt vmcnt(6)\n"
        "ds_read_b128 a[212:215], v137 offset:3072\n"
        "v_mfma_f32_16x16x32_bf16 v[68:71], a[96:99], a[220:223], 0\n"
        "s_add_u32 m0, s65, 12288\n"
        "ds_read_b128 a[208:211], v137 offset:3840\n"
        "ds_read_b128 a[204:207], v137 offset:3104\n"
        "v_mfma_f32_16x16x32_bf16 v[64:67], a[96:99], a[216:219], 0\n"
        "s_waitcnt lgkmcnt(3)\n"
        "ds_read_b128 a[200:203], v137 offset:3872\n"
        "ds_read_b128 a[196:199], v137 offset:3136\n"
        "v_mfma_f32_16x16x32_bf16 v[72:75], a[120:123], a[216:219], 0\n"
        "buffer_load_dwordx4 v128, s[20:23], s57 offen lds\n"
        "v_mfma_f32_16x16x32_bf16 v[76:79], a[120:123], a[220:223], 0\n"
        "s_waitcnt lgkmcnt(4)\n"
        "ds_read_b128 a[192:195], v137 offset:3904\n"
        "v_mfma_f32_16x16x32_bf16 v[112:115], a[212:215], v[148:151], 0\n"
        "s_waitcnt lgkmcnt(4)\n"
        "s_waitcnt vmcnt(4)\n"
        "ds_read_b128 a[188:191], v137 offset:6144\n"
        "ds_read_b128 a[184:187], v137 offset:6912\n"
        "v_mfma_f32_16x16x32_bf16 v[116:119], a[208:211], v[148:151], 0\n"
        "buffer_load_dwordx4 v129, s[20:23], s57 offen offset:1024 lds\n"
        "v_mfma_f32_16x16x32_bf16 v[84:87], a[120:123], a[224:227], 0\n"
        "ds_read_b128 a[180:183], v137 offset:6176\n"
        "ds_read_b128 a[176:179], v137 offset:6944\n"
        "v_mfma_f32_16x16x32_bf16 v[92:95], a[120:123], a[228:231], 0\n"
        "s_waitcnt lgkmcnt(5)\n"
        "ds_read_b128 a[172:175], v137 offset:6208\n"
        "ds_read_b128 a[168:171], v137 offset:6976\n"
        "v_mfma_f32_16x16x32_bf16 v[120:123], a[204:207], v[148:151], 0\n"
        "v_cvt_pk_bf16_f32 v112, v112, v113\n"
        "v_accvgpr_write_b32 a0, v112\n"
        "v_mfma_f32_16x16x32_bf16 v[124:127], a[200:203], v[148:151], 0\n"
        "buffer_load_dwordx4 v130, s[20:23], s57 offen offset:2048 lds\n"
        "v_mfma_f32_16x16x32_bf16 v[96:99], a[196:199], v[148:151], 0\n"
        "s_waitcnt lgkmcnt(4)\n"
        "v_cvt_pk_bf16_f32 v114, v114, v115\n"
        "v_accvgpr_write_b32 a1, v114\n"
        "v_mfma_f32_16x16x32_bf16 v[100:103], a[192:195], v[148:151], 0\n"
        "s_add_u32 m0, s65, 14976\n"
        "v_cvt_pk_bf16_f32 v116, v116, v117\n"
        "v_accvgpr_write_b32 a2, v116\n"
        "v_mfma_f32_16x16x32_bf16 v[104:107], a[188:191], v[148:151], 0\n"
        "v_cvt_pk_bf16_f32 v118, v118, v119\n"
        "v_accvgpr_write_b32 a3, v118\n"
        "v_mfma_f32_16x16x32_bf16 v[108:111], a[184:187], v[148:151], 0\n"
        "buffer_load_dwordx4 v128, s[20:23], s57 offen offset:384 lds\n"
        "v_mfma_f32_16x16x32_bf16 v[156:159], a[0:3], a[216:219], 0\n"
        "v_cvt_pk_bf16_f32 v120, v120, v121\n"
        "v_accvgpr_write_b32 a24, v120\n"
        "v_mfma_f32_16x16x32_bf16 v[176:179], a[0:3], a[220:223], 0\n"
        "v_cvt_pk_bf16_f32 v122, v122, v123\n"
        "v_accvgpr_write_b32 a25, v122\n"
        "v_mfma_f32_16x16x32_bf16 v[196:199], a[0:3], a[224:227], 0\n"
        "v_cvt_pk_bf16_f32 v124, v124, v125\n"
        "v_accvgpr_write_b32 a26, v124\n"
        "v_mfma_f32_16x16x32_bf16 v[216:219], a[0:3], a[228:231], 0\n"
        "buffer_load_dwordx4 v129, s[20:23], s57 offen offset:1408 lds\n"
        "v_cvt_pk_bf16_f32 v126, v126, v127\n"
        "v_accvgpr_write_b32 a27, v126\n"
        "s_nop 1\n"
        "v_mfma_f32_16x16x32_bf16 v[224:227], a[24:27], a[228:231], 0\n"
        "v_cvt_pk_bf16_f32 v96, v96, v97\n"
        "v_accvgpr_write_b32 a48, v96\n"
        "v_mfma_f32_16x16x32_bf16 v[204:207], a[24:27], a[224:227], 0\n"
        "s_waitcnt vmcnt(5)\n"
        "ds_read_b64_tr_b16 a[76:77], v255 offset:9216\n"
        "ds_read_b64_tr_b16 a[78:79], v255 offset:9984\n"
        "v_mfma_f32_16x16x32_bf16 v[184:187], a[24:27], a[220:223], 0\n"
        "ds_read_b64_tr_b16 a[100:101], v255 offset:9248\n"
        "ds_read_b64_tr_b16 a[102:103], v255 offset:10016\n"
        "v_mfma_f32_16x16x32_bf16 v[164:167], a[24:27], a[216:219], 0\n"
        "buffer_load_dwordx4 v130, s[20:23], s57 offen offset:2432 lds\n"
        "ds_read_b64_tr_b16 a[124:125], v255 offset:9280\n"
        "ds_read_b64_tr_b16 a[126:127], v255 offset:10048\n"
        "v_cvt_pk_bf16_f32 v98, v98, v99\n"
        "v_accvgpr_write_b32 a49, v98\n"
        "v_cvt_pk_bf16_f32 v100, v100, v101\n"
        "v_accvgpr_write_b32 a50, v100\n"
        "v_cvt_pk_bf16_f32 v102, v102, v103\n"
        "v_accvgpr_write_b32 a51, v102\n"
        "s_nop 1\n"
        "v_mfma_f32_16x16x32_bf16 v[160:163], a[48:51], a[216:219], 0\n"
        "s_add_u32 m0, s65, 0\n"
        "ds_read_b128 a[248:251], v240 offset:128\n"
        "v_cvt_pk_bf16_f32 v104, v104, v105\n"
        "v_mfma_f32_16x16x32_bf16 v[180:183], a[48:51], a[220:223], 0\n"
        "ds_read_b128 a[216:219], v240 offset:1664\n"
        "v_accvgpr_write_b32 a4, v104\n"
        "v_mfma_f32_16x16x32_bf16 v[200:203], a[48:51], a[224:227], 0\n"
        "buffer_load_dwordx4 v128, s[20:23], s58 offen lds\n"
        "v_mfma_f32_16x16x32_bf16 v[220:223], a[48:51], a[228:231], 0\n"
        "s_waitcnt lgkmcnt(6)\n"
        "ds_read_b128 a[220:223], v240 offset:12416\n"
        "ds_read_b128 a[224:227], v240 offset:13952\n"
        "v_mfma_f32_16x16x32_bf16 v[112:115], a[180:183], v[148:151], 0\n"
        "v_cvt_pk_bf16_f32 v106, v106, v107\n"
        "v_accvgpr_write_b32 a5, v106\n"
        "v_mfma_f32_16x16x32_bf16 v[116:119], a[176:179], v[148:151], 0\n"
        "v_cvt_pk_bf16_f32 v108, v108, v109\n"
        "v_accvgpr_write_b32 a6, v108\n"
        "v_mfma_f32_16x16x32_bf16 v[152:155], a[76:79], a[232:235], v[152:155]\n"
        "buffer_load_dwordx4 v129, s[20:23], s58 offen offset:1024 lds\n"
        "v_mfma_f32_16x16x32_bf16 v[172:175], a[76:79], a[236:239], v[172:175]\n"
        "v_cvt_pk_bf16_f32 v110, v110, v111\n"
        "v_accvgpr_write_b32 a7, v110\n"
        "v_mfma_f32_16x16x32_bf16 v[192:195], a[76:79], a[240:243], v[192:195]\n"
        "v_cvt_pk_bf16_f32 v112, v112, v113\n"
        "v_accvgpr_write_b32 a28, v112\n"
        "v_mfma_f32_16x16x32_bf16 v[212:215], a[76:79], a[244:247], v[212:215]\n"
        "v_cvt_pk_bf16_f32 v114, v114, v115\n"
        "v_accvgpr_write_b32 a29, v114\n"
        "v_mfma_f32_16x16x32_bf16 v[156:159], a[4:7], a[232:235], v[156:159]\n"
        "buffer_load_dwordx4 v130, s[20:23], s58 offen offset:2048 lds\n"
        "v_mfma_f32_16x16x32_bf16 v[176:179], a[4:7], a[236:239], v[176:179]\n"
        "s_add_u32 m0, s65, 2688\n"
        "v_cvt_pk_bf16_f32 v116, v116, v117\n"
        "v_accvgpr_write_b32 a30, v116\n"
        "v_mfma_f32_16x16x32_bf16 v[196:199], a[4:7], a[240:243], v[196:199]\n"
        ".long 0xbec200ff\n"
        "41:\n"
        "s_branch 60b\n"
        "v_cvt_pk_bf16_f32 v118, v118, v119\n"
        "v_accvgpr_write_b32 a31, v118\n"
        "v_mfma_f32_16x16x32_bf16 v[216:219], a[4:7], a[244:247], v[216:219]\n"
        "s_waitcnt vmcnt(6)\n"
        "ds_read_b128 a[212:215], v137 offset:12288\n"
        "ds_read_b128 a[208:211], v137 offset:13056\n"
        "v_mfma_f32_16x16x32_bf16 v[120:123], a[172:175], v[148:151], 0\n"
        "buffer_load_dwordx4 v128, s[20:23], s58 offen offset:384 lds\n"
        "v_mfma_f32_16x16x32_bf16 v[124:127], a[168:171], v[148:151], 0\n"
        "ds_read_b128 a[204:207], v137 offset:12320\n"
        "ds_read_b128 a[200:203], v137 offset:13088\n"
        "v_mfma_f32_16x16x32_bf16 v[224:227], a[28:31], a[244:247], v[224:227]\n"
        "ds_read_b128 a[196:199], v137 offset:12352\n"
        "ds_read_b128 a[192:195], v137 offset:13120\n"
        "v_mfma_f32_16x16x32_bf16 v[204:207], a[28:31], a[240:243], v[204:207]\n"
        "buffer_load_dwordx4 v129, s[20:23], s58 offen offset:1408 lds\n"
        "v_mfma_f32_16x16x32_bf16 v[184:187], a[28:31], a[236:239], v[184:187]\n"
        "v_cvt_pk_bf16_f32 v120, v120, v121\n"
        "v_accvgpr_write_b32 a52, v120\n"
        "v_mfma_f32_16x16x32_bf16 v[164:167], a[28:31], a[232:235], v[164:167]\n"
        "s_waitcnt lgkmcnt(4)\n"
        "v_cvt_pk_bf16_f32 v122, v122, v123\n"
        "v_accvgpr_write_b32 a53, v122\n"
        "v_mfma_f32_16x16x32_bf16 v[96:99], a[212:215], v[148:151], 0\n"
        "v_cvt_pk_bf16_f32 v124, v124, v125\n"
        "v_accvgpr_write_b32 a54, v124\n"
        "v_mfma_f32_16x16x32_bf16 v[100:103], a[208:211], v[148:151], 0\n"
        "buffer_load_dwordx4 v130, s[20:23], s58 offen offset:2432 lds\n"
        "v_mfma_f32_16x16x32_bf16 v[88:91], a[100:103], a[244:247], v[88:91]\n"
        "s_add_u32 m0, s65, 6144\n"
        "v_cvt_pk_bf16_f32 v126, v126, v127\n"
        "v_accvgpr_write_b32 a55, v126\n"
        "v_mfma_f32_16x16x32_bf16 v[80:83], a[100:103], a[240:243], v[80:83]\n"
        "v_cvt_pk_bf16_f32 v96, v96, v97\n"
        "v_accvgpr_write_b32 a8, v96\n"
        "v_mfma_f32_16x16x32_bf16 v[68:71], a[100:103], a[236:239], v[68:71]\n"
        "v_cvt_pk_bf16_f32 v98, v98, v99\n"
        "v_accvgpr_write_b32 a9, v98\n"
        "v_mfma_f32_16x16x32_bf16 v[64:67], a[100:103], a[232:235], v[64:67]\n"
        "buffer_load_dwordx4 v128, s[20:23], s59 offen lds\n"
        "v_mfma_f32_16x16x32_bf16 v[160:163], a[52:55], a[232:235], v[160:163]\n"
        "s_waitcnt vmcnt(7)\n"
        "ds_read_b64_tr_b16 a[80:81], v255 offset:15360\n"
        "ds_read_b64_tr_b16 a[82:83], v255 offset:16128\n"
        "v_mfma_f32_16x16x32_bf16 v[180:183], a[52:55], a[236:239], v[180:183]\n"
        "ds_read_b64_tr_b16 a[104:105], v255 offset:15392\n"
        "ds_read_b64_tr_b16 a[106:107], v255 offset:16160\n"
        "v_mfma_f32_16x16x32_bf16 v[200:203], a[52:55], a[240:243], v[200:203]\n"
        "ds_read_b64_tr_b16 a[128:129], v255 offset:15424\n"
        "ds_read_b64_tr_b16 a[130:131], v255 offset:16192\n"
        "v_mfma_f32_16x16x32_bf16 v[220:223], a[52:55], a[244:247], v[220:223]\n"
        "s_waitcnt lgkmcnt(6)\n"
        "v_cvt_pk_bf16_f32 v100, v100, v101\n"
        "v_accvgpr_write_b32 a10, v100\n"
        "v_mfma_f32_16x16x32_bf16 v[104:107], a[204:207], v[148:151], 0\n"
        "buffer_load_dwordx4 v129, s[20:23], s59 offen offset:1024 lds\n"
        "v_mfma_f32_16x16x32_bf16 v[108:111], a[200:203], v[148:151], 0\n"
        "ds_read_b128 a[228:231], v241 offset:128\n"
        "v_cvt_pk_bf16_f32 v102, v102, v103\n"
        "v_mfma_f32_16x16x32_bf16 v[72:75], a[124:127], a[232:235], v[72:75]\n"
        "ds_read_b128 a[232:235], v241 offset:1664\n"
        "v_accvgpr_write_b32 a11, v102\n"
        "v_mfma_f32_16x16x32_bf16 v[76:79], a[124:127], a[236:239], v[76:79]\n"
        "buffer_load_dwordx4 v130, s[20:23], s59 offen offset:2048 lds\n"
        "v_mfma_f32_16x16x32_bf16 v[84:87], a[124:127], a[240:243], v[84:87]\n"
        "s_add_u32 m0, s65, 8832\n"
        "ds_read_b128 a[236:239], v241 offset:12416\n"
        "ds_read_b128 a[240:243], v241 offset:13952\n"
        "v_mfma_f32_16x16x32_bf16 v[92:95], a[124:127], a[244:247], v[92:95]\n"
        "s_waitcnt vmcnt(6)\n"
        "ds_read_b128 a[188:191], v137 offset:0\n"
        "ds_read_b128 a[184:187], v137 offset:768\n"
        "v_mfma_f32_16x16x32_bf16 v[156:159], a[8:11], a[248:251], v[156:159]\n"
        "buffer_load_dwordx4 v128, s[20:23], s59 offen offset:384 lds\n"
        "v_mfma_f32_16x16x32_bf16 v[176:179], a[8:11], a[216:219], v[176:179]\n"
        "ds_read_b128 a[180:183], v137 offset:32\n"
        "ds_read_b128 a[176:179], v137 offset:800\n"
        "v_mfma_f32_16x16x32_bf16 v[196:199], a[8:11], a[220:223], v[196:199]\n"
        "ds_read_b128 a[172:175], v137 offset:64\n"
        "ds_read_b128 a[168:171], v137 offset:832\n"
        "v_mfma_f32_16x16x32_bf16 v[216:219], a[8:11], a[224:227], v[216:219]\n"
        "v_cvt_pk_bf16_f32 v104, v104, v105\n"
        "v_accvgpr_write_b32 a32, v104\n"
        "v_mfma_f32_16x16x32_bf16 v[112:115], a[196:199], v[148:151], 0\n"
        "buffer_load_dwordx4 v129, s[20:23], s59 offen offset:1408 lds\n"
        "v_mfma_f32_16x16x32_bf16 v[116:119], a[192:195], v[148:151], 0\n"
        "s_waitcnt lgkmcnt(4)\n"
        "v_cvt_pk_bf16_f32 v106, v106, v107\n"
        "v_accvgpr_write_b32 a33, v106\n"
        "v_mfma_f32_16x16x32_bf16 v[152:155], a[80:83], a[248:251], v[152:155]\n"
        "v_cvt_pk_bf16_f32 v108, v108, v109\n"
        "v_accvgpr_write_b32 a34, v108\n"
        "v_mfma_f32_16x16x32_bf16 v[172:175], a[80:83], a[216:219], v[172:175]\n"
        "v_cvt_pk_bf16_f32 v110, v110, v111\n"
        "v_accvgpr_write_b32 a35, v110\n"
        "v_mfma_f32_16x16x32_bf16 v[192:195], a[80:83], a[220:223], v[192:195]\n"
        "buffer_load_dwordx4 v130, s[20:23], s59 offen offset:2432 lds\n"
        "v_mfma_f32_16x16x32_bf16 v[212:215], a[80:83], a[224:227], v[212:215]\n"
        "s_add_u32 m0, s65, 12288\n"
        "v_cvt_pk_bf16_f32 v112, v112, v113\n"
        "v_accvgpr_write_b32 a56, v112\n"
        "v_mfma_f32_16x16x32_bf16 v[224:227], a[32:35], a[224:227], v[224:227]\n"
        "ds_read_b128 v[52:55], v141 offset:9216\n"
        "v_cvt_pk_bf16_f32 v114, v114, v115\n"
        "v_mfma_f32_16x16x32_bf16 v[204:207], a[32:35], a[220:223], v[204:207]\n"
        "v_accvgpr_write_b32 a57, v114\n"
        "v_cvt_pk_bf16_f32 v116, v116, v117\n"
        "v_mfma_f32_16x16x32_bf16 v[184:187], a[32:35], a[216:219], v[184:187]\n"
        "buffer_load_dwordx4 v128, s[20:23], s60 offen lds\n"
        "v_mfma_f32_16x16x32_bf16 v[164:167], a[32:35], a[248:251], v[164:167]\n"
        "v_accvgpr_write_b32 a58, v116\n"
        "v_cvt_pk_bf16_f32 v118, v118, v119\n"
        "v_mfma_f32_16x16x32_bf16 v[120:123], a[188:191], v[148:151], 0\n"
        "s_waitcnt vmcnt(7)\n"
        "ds_read_b64_tr_b16 a[84:85], v255 offset:3072\n"
        "ds_read_b64_tr_b16 a[86:87], v255 offset:3840\n"
        "v_mfma_f32_16x16x32_bf16 v[124:127], a[184:187], v[148:151], 0\n"
        "ds_read_b64_tr_b16 a[108:109], v255 offset:3104\n"
        "ds_read_b64_tr_b16 a[110:111], v255 offset:3872\n"
        "v_mfma_f32_16x16x32_bf16 v[88:91], a[104:107], a[224:227], v[88:91]\n"
        "buffer_load_dwordx4 v129, s[20:23], s60 offen offset:1024 lds\n"
        "ds_read_b64_tr_b16 a[132:133], v255 offset:3136\n"
        "ds_read_b64_tr_b16 a[134:135], v255 offset:3904\n"
        "v_mfma_f32_16x16x32_bf16 v[80:83], a[104:107], a[220:223], v[80:83]\n"
        "v_accvgpr_write_b32 a59, v118\n"
        "v_cvt_pk_bf16_f32 v120, v120, v121\n"
        "v_mfma_f32_16x16x32_bf16 v[68:71], a[104:107], a[216:219], v[68:71]\n"
        "v_accvgpr_write_b32 a12, v120\n"
        "v_cvt_pk_bf16_f32 v122, v122, v123\n"
        "v_mfma_f32_16x16x32_bf16 v[64:67], a[104:107], a[248:251], v[64:67]\n"
        "buffer_load_dwordx4 v130, s[20:23], s60 offen offset:2048 lds\n"
        "v_mfma_f32_16x16x32_bf16 v[160:163], a[56:59], a[248:251], v[160:163]\n"
        "s_add_u32 m0, s65, 14976\n"
        "v_accvgpr_write_b32 a13, v122\n"
        "v_cvt_pk_bf16_f32 v124, v124, v125\n"
        "v_mfma_f32_16x16x32_bf16 v[180:183], a[56:59], a[216:219], v[180:183]\n"
        "s_waitcnt lgkmcnt(2)\n"
        "v_lshl_add_u64 v[52:53], v[52:53], 2, s[38:39]\n"
        "v_lshl_add_u64 v[54:55], v[54:55], 2, s[38:39]\n"
        "v_mfma_f32_16x16x32_bf16 v[200:203], a[56:59], a[220:223], v[200:203]\n"
        "ds_read_b32 v52, v52\n"
        "ds_read_b32 v53, v53\n"
        "v_mfma_f32_16x16x32_bf16 v[220:223], a[56:59], a[224:227], v[220:223]\n"
        "ds_read_b32 v54, v54\n"
        "ds_read_b32 v55, v55\n"
        "v_mfma_f32_16x16x32_bf16 v[96:99], a[180:183], v[148:151], 0\n"
        "buffer_load_dwordx4 v128, s[20:23], s60 offen offset:384 lds\n"
        "v_mfma_f32_16x16x32_bf16 v[100:103], a[176:179], v[148:151], 0\n"
        "ds_read_b128 a[244:247], v240 offset:256\n"
        "v_accvgpr_write_b32 a14, v124\n"
        "v_mfma_f32_16x16x32_bf16 v[72:75], a[128:131], a[248:251], v[72:75]\n"
        "ds_read_b128 a[248:251], v240 offset:1792\n"
        "v_cvt_pk_bf16_f32 v126, v126, v127\n"
        "v_mfma_f32_16x16x32_bf16 v[76:79], a[128:131], a[216:219], v[76:79]\n"
        "buffer_load_dwordx4 v129, s[20:23], s60 offen offset:1408 lds\n"
        "v_mfma_f32_16x16x32_bf16 v[84:87], a[128:131], a[220:223], v[84:87]\n"
        "ds_read_b128 a[216:219], v240 offset:12544\n"
        "ds_read_b128 a[220:223], v240 offset:14080\n"
        "v_mfma_f32_16x16x32_bf16 v[92:95], a[128:131], a[224:227], v[92:95]\n"
        "v_accvgpr_write_b32 a15, v126\n"
        "v_cvt_pk_bf16_f32 v96, v96, v97\n"
        "s_nop 0\n"
        "v_mfma_f32_16x16x32_bf16 v[156:159], a[12:15], a[228:231], v[156:159]\n"
        "v_accvgpr_write_b32 a36, v96\n"
        "v_cvt_pk_bf16_f32 v98, v98, v99\n"
        "v_mfma_f32_16x16x32_bf16 v[176:179], a[12:15], a[232:235], v[176:179]\n"
        "buffer_load_dwordx4 v130, s[20:23], s60 offen offset:2432 lds\n"
        "v_mfma_f32_16x16x32_bf16 v[196:199], a[12:15], a[236:239], v[196:199]\n"
        "v_accvgpr_write_b32 a37, v98\n"
        "v_cvt_pk_bf16_f32 v100, v100, v101\n"
        "v_mfma_f32_16x16x32_bf16 v[216:219], a[12:15], a[240:243], v[216:219]\n"
        "s_waitcnt vmcnt(9)\n"
        "ds_read_b128 a[212:215], v137 offset:6144\n"
        "ds_read_b128 a[208:211], v137 offset:6912\n"
        "v_mfma_f32_16x16x32_bf16 v[104:107], a[172:175], v[148:151], 0\n"
        "s_mov_b32 m0, s48\n"
        "ds_read_b128 a[204:207], v137 offset:6176\n"
        "ds_read_b128 a[200:203], v137 offset:6944\n"
        "v_mfma_f32_16x16x32_bf16 v[108:111], a[168:171], v[148:151], 0\n"
        "s_sub_u32 s64, s36, 4194304\n"
        "ds_read_b128 a[196:199], v137 offset:6208\n"
        "ds_read_b128 a[192:195], v137 offset:6976\n"
        "v_mfma_f32_16x16x32_bf16 v[152:155], a[84:87], a[228:231], v[152:155]\n"
        "buffer_load_dword v138, s[20:23], 0 offen offset:768 lds\n"
        "v_mfma_f32_16x16x32_bf16 v[172:175], a[84:87], a[232:235], v[172:175]\n"
        "v_accvgpr_write_b32 a38, v100\n"
        "v_cvt_pk_bf16_f32 v102, v102, v103\n"
        "v_mfma_f32_16x16x32_bf16 v[192:195], a[84:87], a[236:239], v[192:195]\n"
        "v_accvgpr_write_b32 a39, v102\n"
        "v_cvt_pk_bf16_f32 v104, v104, v105\n"
        "v_mfma_f32_16x16x32_bf16 v[212:215], a[84:87], a[240:243], v[212:215]\n"
        "buffer_load_dwordx4 v249, s[12:15], s64 offen lds\n"
        "v_mfma_f32_16x16x32_bf16 v[224:227], a[36:39], a[240:243], v[224:227]\n"
        "v_accvgpr_write_b32 a60, v104\n"
        "v_cvt_pk_bf16_f32 v106, v106, v107\n"
        "v_mfma_f32_16x16x32_bf16 v[204:207], a[36:39], a[236:239], v[204:207]\n"
        "ds_read_b128 v[56:59], v142 offset:9216\n"
        "v_accvgpr_write_b32 a61, v106\n"
        "v_mfma_f32_16x16x32_bf16 v[184:187], a[36:39], a[232:235], v[184:187]\n"
        "s_waitcnt vmcnt(8)\n"
        "ds_read_b64_tr_b16 a[88:89], v255 offset:9216\n"
        "ds_read_b64_tr_b16 a[90:91], v255 offset:9984\n"
        "v_mfma_f32_16x16x32_bf16 v[164:167], a[36:39], a[228:231], v[164:167]\n"
        "s_waitcnt lgkmcnt(3)\n"
        "ds_read_b64_tr_b16 a[112:113], v255 offset:9248\n"
        "ds_read_b64_tr_b16 a[114:115], v255 offset:10016\n"
        "v_mfma_f32_16x16x32_bf16 v[112:115], a[212:215], v[148:151], 0\n"
        "buffer_load_dwordx4 v253, s[12:15], s64 offen offset:1024 lds\n"
        "ds_read_b64_tr_b16 a[136:137], v255 offset:9280\n"
        "v_mfma_f32_16x16x32_bf16 v[116:119], a[208:211], v[148:151], 0\n"
        "ds_read_b64_tr_b16 a[138:139], v255 offset:10048\n"
        "s_add_u32 m0, s65, 0\n"
        "v_cvt_pk_bf16_f32 v108, v108, v109\n"
        "v_mfma_f32_16x16x32_bf16 v[88:91], a[108:111], a[240:243], v[88:91]\n"
        "s_mov_b32 s64, 768\n"
        "v_accvgpr_write_b32 a62, v108\n"
        "v_cvt_pk_bf16_f32 v110, v110, v111\n"
        "v_mfma_f32_16x16x32_bf16 v[80:83], a[108:111], a[236:239], v[80:83]\n"
        "buffer_load_dwordx4 v128, s[20:23], s64 offen lds\n"
        "v_mfma_f32_16x16x32_bf16 v[68:71], a[108:111], a[232:235], v[68:71]\n"
        "v_accvgpr_write_b32 a63, v110\n"
        "v_cvt_pk_bf16_f32 v112, v112, v113\n"
        "v_mfma_f32_16x16x32_bf16 v[64:67], a[108:111], a[228:231], v[64:67]\n"
        "v_accvgpr_write_b32 a16, v112\n"
        "v_cvt_pk_bf16_f32 v114, v114, v115\n"
        "v_mfma_f32_16x16x32_bf16 v[160:163], a[60:63], a[228:231], v[160:163]\n"
        "v_accvgpr_write_b32 a17, v114\n"
        "v_cvt_pk_bf16_f32 v116, v116, v117\n"
        "v_mfma_f32_16x16x32_bf16 v[180:183], a[60:63], a[232:235], v[180:183]\n"
        "buffer_load_dwordx4 v129, s[20:23], s64 offen offset:1024 lds\n"
        "v_mfma_f32_16x16x32_bf16 v[200:203], a[60:63], a[236:239], v[200:203]\n"
        "ds_read_b128 a[224:227], v241 offset:256\n"
        "s_waitcnt lgkmcnt(1)\n"
        "v_lshl_add_u64 v[56:57], v[56:57], 2, s[38:39]\n"
        "v_mfma_f32_16x16x32_bf16 v[220:223], a[60:63], a[240:243], v[220:223]\n"
        "ds_read_b32 v56, v56\n"
        "ds_read_b32 v57, v57\n"
        "v_mfma_f32_16x16x32_bf16 v[120:123], a[204:207], v[148:151], 0\n"
        "v_lshl_add_u64 v[58:59], v[58:59], 2, s[38:39]\n"
        "v_accvgpr_write_b32 a18, v116\n"
        "v_mfma_f32_16x16x32_bf16 v[124:127], a[200:203], v[148:151], 0\n"
        "buffer_load_dwordx4 v130, s[20:23], s64 offen offset:2048 lds\n"
        "v_mfma_f32_16x16x32_bf16 v[72:75], a[132:135], a[228:231], v[72:75]\n"
        "s_add_u32 m0, s65, 2304\n"
        "ds_read_b32 v58, v58\n"
        "ds_read_b32 v59, v59\n"
        "v_mfma_f32_16x16x32_bf16 v[76:79], a[132:135], a[232:235], v[76:79]\n"
        "ds_read_b128 a[228:231], v241 offset:1792\n"
        "ds_read_b128 a[232:235], v241 offset:12544\n"
        "v_mfma_f32_16x16x32_bf16 v[84:87], a[132:135], a[236:239], v[84:87]\n"
        "ds_read_b128 a[236:239], v241 offset:14080\n"
        "v_cvt_pk_bf16_f32 v118, v118, v119\n"
        "v_mfma_f32_16x16x32_bf16 v[92:95], a[132:135], a[240:243], v[92:95]\n"
        "buffer_load_dwordx4 v128, s[20:23], s56 offen offset:768 lds\n"
        "v_accvgpr_write_b32 a19, v118\n"
        "s_nop 1\n"
        "v_mfma_f32_16x16x32_bf16 v[156:159], a[16:19], a[244:247], v[156:159]\n"
        "v_cvt_pk_bf16_f32 v120, v120, v121\n"
        "v_accvgpr_write_b32 a40, v120\n"
        "v_mfma_f32_16x16x32_bf16 v[176:179], a[16:19], a[248:251], v[176:179]\n"
        "v_cvt_pk_bf16_f32 v122, v122, v123\n"
        "v_accvgpr_write_b32 a41, v122\n"
        "v_mfma_f32_16x16x32_bf16 v[196:199], a[16:19], a[216:219], v[196:199]\n"
        "v_cvt_pk_bf16_f32 v124, v124, v125\n"
        "v_accvgpr_write_b32 a42, v124\n"
        "v_mfma_f32_16x16x32_bf16 v[216:219], a[16:19], a[220:223], v[216:219]\n"
        "buffer_load_dwordx4 v129, s[20:23], s56 offen offset:1792 lds\n"
        "v_mfma_f32_16x16x32_bf16 v[96:99], a[196:199], v[148:151], 0\n"
        "v_cvt_pk_bf16_f32 v126, v126, v127\n"
        "v_accvgpr_write_b32 a43, v126\n"
        "v_mfma_f32_16x16x32_bf16 v[100:103], a[192:195], v[148:151], 0\n"
        "s_waitcnt vmcnt(11)\n"
        "ds_read_b128 a[188:191], v137 offset:12288\n"
        "ds_read_b128 a[184:187], v137 offset:13056\n"
        "v_mfma_f32_16x16x32_bf16 v[152:155], a[88:91], a[244:247], v[152:155]\n"
        "ds_read_b128 a[180:183], v137 offset:12320\n"
        "ds_read_b128 a[176:179], v137 offset:13088\n"
        "v_mfma_f32_16x16x32_bf16 v[172:175], a[88:91], a[248:251], v[172:175]\n"
        "buffer_load_dwordx4 v130, s[20:23], s56 offen offset:2816 lds\n"
        "v_mfma_f32_16x16x32_bf16 v[192:195], a[88:91], a[216:219], v[192:195]\n"
        "s_add_u32 m0, s65, 5376\n"
        "ds_read_b128 a[172:175], v137 offset:12352\n"
        "ds_read_b128 a[168:171], v137 offset:13120\n"
        "v_mfma_f32_16x16x32_bf16 v[212:215], a[88:91], a[220:223], v[212:215]\n"
        "v_cvt_pk_bf16_f32 v96, v96, v97\n"
        "v_accvgpr_write_b32 a64, v96\n"
        "v_mfma_f32_16x16x32_bf16 v[224:227], a[40:43], a[220:223], v[224:227]\n"
        "v_cvt_pk_bf16_f32 v98, v98, v99\n"
        "v_accvgpr_write_b32 a65, v98\n"
        "v_mfma_f32_16x16x32_bf16 v[204:207], a[40:43], a[216:219], v[204:207]\n"
        "buffer_load_dwordx4 v128, s[20:23], s57 offen offset:768 lds\n"
        "v_mfma_f32_16x16x32_bf16 v[184:187], a[40:43], a[248:251], v[184:187]\n"
        "v_cvt_pk_bf16_f32 v100, v100, v101\n"
        "v_accvgpr_write_b32 a66, v100\n"
        "v_mfma_f32_16x16x32_bf16 v[164:167], a[40:43], a[244:247], v[164:167]\n"
        "s_waitcnt lgkmcnt(2)\n"
        "ds_read_b128 v[60:63], v143 offset:9216\n"
        "v_cvt_pk_bf16_f32 v102, v102, v103\n"
        "v_mfma_f32_16x16x32_bf16 v[104:107], a[188:191], v[148:151], 0\n"
        "v_accvgpr_write_b32 a67, v102\n"
        "v_mfma_f32_16x16x32_bf16 v[108:111], a[184:187], v[148:151], 0\n"
        "buffer_load_dwordx4 v129, s[20:23], s57 offen offset:1792 lds\n"
        "v_mfma_f32_16x16x32_bf16 v[88:91], a[112:115], a[220:223], v[88:91]\n"
        "v_mfma_f32_16x16x32_bf16 v[80:83], a[112:115], a[216:219], v[80:83]\n"
        "v_mfma_f32_16x16x32_bf16 v[68:71], a[112:115], a[248:251], v[68:71]\n"
        "v_mfma_f32_16x16x32_bf16 v[64:67], a[112:115], a[244:247], v[64:67]\n"
        "s_waitcnt vmcnt(11)\n"
        "ds_read_b64_tr_b16 a[92:93], v255 offset:15360\n"
        "ds_read_b64_tr_b16 a[94:95], v255 offset:16128\n"
        "v_mfma_f32_16x16x32_bf16 v[160:163], a[64:67], a[244:247], v[160:163]\n"
        "ds_read_b64_tr_b16 a[116:117], v255 offset:15392\n"
        "ds_read_b64_tr_b16 a[118:119], v255 offset:16160\n"
        "v_mfma_f32_16x16x32_bf16 v[180:183], a[64:67], a[248:251], v[180:183]\n"
        "ds_read_b64_tr_b16 a[140:141], v255 offset:15424\n"
        "ds_read_b64_tr_b16 a[142:143], v255 offset:16192\n"
        "v_mfma_f32_16x16x32_bf16 v[200:203], a[64:67], a[216:219], v[200:203]\n"
        "v_cvt_pk_bf16_f32 v104, v104, v105\n"
        "v_accvgpr_write_b32 a20, v104\n"
        "v_mfma_f32_16x16x32_bf16 v[220:223], a[64:67], a[220:223], v[220:223]\n"
        "v_cvt_pk_bf16_f32 v106, v106, v107\n"
        "v_accvgpr_write_b32 a21, v106\n"
        "v_mfma_f32_16x16x32_bf16 v[112:115], a[180:183], v[148:151], 0\n"
        "s_waitcnt lgkmcnt(6)\n"
        "v_lshl_add_u64 v[60:61], v[60:61], 2, s[38:39]\n"
        "v_lshl_add_u64 v[62:63], v[62:63], 2, s[38:39]\n"
        "v_mfma_f32_16x16x32_bf16 v[116:119], a[176:179], v[148:151], 0\n"
        "ds_read_b32 v60, v60\n"
        "ds_read_b32 v61, v61\n"
        "v_mfma_f32_16x16x32_bf16 v[72:75], a[136:139], a[244:247], v[72:75]\n"
        "ds_read_b32 v62, v62\n"
        "ds_read_b32 v63, v63\n"
        "v_mfma_f32_16x16x32_bf16 v[76:79], a[136:139], a[248:251], v[76:79]\n"
        "v_cvt_pk_bf16_f32 v108, v108, v109\n"
        "v_accvgpr_write_b32 a22, v108\n"
        "v_cvt_pk_bf16_f32 v110, v110, v111\n"
        "v_accvgpr_write_b32 a23, v110\n"
        "s_nop 1\n"
        "v_mfma_f32_16x16x32_bf16 v[156:159], a[20:23], a[224:227], v[156:159]\n"
        "v_cvt_pk_bf16_f32 v112, v112, v113\n"
        "v_accvgpr_write_b32 a44, v112\n"
        "v_mfma_f32_16x16x32_bf16 v[176:179], a[20:23], a[228:231], v[176:179]\n"
        "v_cvt_pk_bf16_f32 v114, v114, v115\n"
        "v_accvgpr_write_b32 a45, v114\n"
        "v_mfma_f32_16x16x32_bf16 v[196:199], a[20:23], a[232:235], v[196:199]\n"
        "v_cvt_pk_bf16_f32 v116, v116, v117\n"
        "v_accvgpr_write_b32 a46, v116\n"
        "v_cvt_pk_bf16_f32 v156, v156, v157\n"
        "v_mfma_f32_16x16x32_bf16 v[216:219], a[20:23], a[236:239], v[216:219]\n"
        "v_cvt_pk_bf16_f32 v157, v158, v159\n"
        "v_cvt_pk_bf16_f32 v176, v176, v177\n"
        "v_cvt_pk_bf16_f32 v177, v178, v179\n"
        "v_mfma_f32_16x16x32_bf16 v[120:123], a[172:175], v[148:151], 0\n"
        "v_cvt_pk_bf16_f32 v196, v196, v197\n"
        "v_cvt_pk_bf16_f32 v197, v198, v199\n"
        "v_mfma_f32_16x16x32_bf16 v[124:127], a[168:171], v[148:151], 0\n"
        "v_cvt_pk_bf16_f32 v118, v118, v119\n"
        "v_cvt_pk_bf16_f32 v216, v216, v217\n"
        "v_cvt_pk_bf16_f32 v217, v218, v219\n"
        "v_mfma_f32_16x16x32_bf16 v[84:87], a[136:139], a[216:219], v[84:87]\n"
        "v_accvgpr_write_b32 a47, v118\n"
        "v_cvt_pk_bf16_f32 v120, v120, v121\n"
        "v_mfma_f32_16x16x32_bf16 v[92:95], a[136:139], a[220:223], v[92:95]\n"
        "v_accvgpr_write_b32 a68, v120\n"
        "v_cvt_pk_bf16_f32 v122, v122, v123\n"
        "v_mfma_f32_16x16x32_bf16 v[224:227], a[44:47], a[236:239], v[224:227]\n"
        "v_accvgpr_write_b32 a69, v122\n"
        "v_cvt_pk_bf16_f32 v124, v124, v125\n"
        "v_mfma_f32_16x16x32_bf16 v[204:207], a[44:47], a[232:235], v[204:207]\n"
        "v_accvgpr_write_b32 a70, v124\n"
        "v_cvt_pk_bf16_f32 v126, v126, v127\n"
        "v_mfma_f32_16x16x32_bf16 v[184:187], a[44:47], a[228:231], v[184:187]\n"
        "ds_read_b128 a[240:243], v240 offset:0\n"
        "v_accvgpr_write_b32 a71, v126\n"
        "v_cvt_pk_bf16_f32 v218, v224, v225\n"
        "v_mfma_f32_16x16x32_bf16 v[164:167], a[44:47], a[224:227], v[164:167]\n"
        "s_waitcnt lgkmcnt(1)\n"
        "ds_read_b128 a[244:247], v241 offset:0\n"
        "v_cvt_pk_bf16_f32 v219, v226, v227\n"
        "v_cvt_pk_bf16_f32 v198, v204, v205\n"
        "v_mfma_f32_16x16x32_bf16 v[152:155], a[92:95], a[224:227], v[152:155]\n"
        "ds_read_b128 a[248:251], v240 offset:128\n"
        "v_cvt_pk_bf16_f32 v199, v206, v207\n"
        "v_cvt_pk_bf16_f32 v178, v184, v185\n"
        "v_mfma_f32_16x16x32_bf16 v[64:67], a[116:119], a[224:227], v[64:67]\n"
        "buffer_load_dwordx4 v130, s[20:23], s57 offen offset:2816 lds\n"
        "v_cvt_pk_bf16_f32 v179, v186, v187\n"
        "v_mfma_f32_16x16x32_bf16 v[72:75], a[140:143], a[224:227], v[72:75]\n"
        "s_add_u32 m0, s65, 8448\n"
        "v_cvt_pk_bf16_f32 v158, v164, v165\n"
        "v_cvt_pk_bf16_f32 v159, v166, v167\n"
        "v_mfma_f32_16x16x32_bf16 v[172:175], a[92:95], a[228:231], v[172:175]\n"
        "v_cvt_pk_bf16_f32 v152, v152, v153\n"
        "v_cvt_pk_bf16_f32 v153, v154, v155\n"
        "v_mfma_f32_16x16x32_bf16 v[160:163], a[68:71], a[224:227], v[160:163]\n"
        "v_cvt_pk_bf16_f32 v154, v64, v65\n"
        "v_cvt_pk_bf16_f32 v155, v66, v67\n"
        "v_mfma_f32_16x16x32_bf16 v[180:183], a[68:71], a[228:231], v[180:183]\n"
        "buffer_load_dwordx4 v128, s[20:23], s58 offen offset:768 lds\n"
        "v_mfma_f32_16x16x32_bf16 v[200:203], a[68:71], a[232:235], v[200:203]\n"
        "v_cvt_pk_bf16_f32 v168, v72, v73\n"
        "v_cvt_pk_bf16_f32 v169, v74, v75\n"
        "v_mfma_f32_16x16x32_bf16 v[220:223], a[68:71], a[236:239], v[220:223]\n"
        "v_cvt_pk_bf16_f32 v160, v160, v161\n"
        "v_cvt_pk_bf16_f32 v161, v162, v163\n"
        "v_mov_b64 v[166:167], v[160:161]\n"
        "v_mfma_f32_16x16x32_bf16 v[68:71], a[116:119], a[228:231], v[68:71]\n"
        "buffer_load_dwordx4 v129, s[20:23], s58 offen offset:1792 lds\n"
        "v_cvt_pk_bf16_f32 v180, v180, v181\n"
        "v_mfma_f32_16x16x32_bf16 v[76:79], a[140:143], a[228:231], v[76:79]\n"
        "s_waitcnt vmcnt(11)\n"
        "s_barrier\n"
        "ds_read_b128 v[162:165], v251 offset:0\n"
        "ds_read_b128 a[216:219], v240 offset:1536\n"
        "v_cvt_pk_bf16_f32 v181, v182, v183\n"
        "v_mfma_f32_16x16x32_bf16 v[192:195], a[92:95], a[232:235], v[192:195]\n"
        "ds_read_b128 v[182:185], v251 offset:512\n"
        "ds_read_b128 a[220:223], v241 offset:1536\n"
        "v_mov_b64 v[186:187], v[180:181]\n"
        "v_mfma_f32_16x16x32_bf16 v[80:83], a[116:119], a[232:235], v[80:83]\n"
        "buffer_load_dwordx4 v130, s[20:23], s58 offen offset:2816 lds\n"
        "v_cvt_pk_bf16_f32 v200, v200, v201\n"
        "v_mfma_f32_16x16x32_bf16 v[84:87], a[140:143], a[232:235], v[84:87]\n"
        "s_add_u32 m0, s65, 11520\n"
        "ds_read_b128 a[224:227], v240 offset:1664\n"
        "v_cvt_pk_bf16_f32 v201, v202, v203\n"
        "v_mov_b64 v[206:207], v[200:201]\n"
        "v_mfma_f32_16x16x32_bf16 v[212:215], a[92:95], a[236:239], v[212:215]\n"
        "ds_read_b128 v[202:205], v251 offset:4096\n"
        "v_cvt_pk_bf16_f32 v220, v220, v221\n"
        "v_cvt_pk_bf16_f32 v221, v222, v223\n"
        "v_mov_b64 v[226:227], v[220:221]\n"
        "v_cvt_pk_bf16_f32 v172, v172, v173\n"
        "v_cvt_pk_bf16_f32 v173, v174, v175\n"
        "v_mfma_f32_16x16x32_bf16 v[88:91], a[116:119], a[236:239], v[88:91]\n"
        "buffer_load_dwordx4 v128, s[20:23], s59 offen offset:768 lds\n"
        "v_mfma_f32_16x16x32_bf16 v[92:95], a[140:143], a[236:239], v[92:95]\n"
        "s_waitcnt vmcnt(10)\n"
        "ds_read_b64_tr_b16 a[144:145], v255 offset:0\n"
        "ds_read_b64_tr_b16 a[146:147], v255 offset:768\n"
        "v_cvt_pk_bf16_f32 v174, v68, v69\n"
        "v_cvt_pk_bf16_f32 v175, v70, v71\n"
        "v_mfma_f32_16x16x32_bf16 v[64:67], v[152:155], v[156:159], v[0:3]\n"
        "s_waitcnt lgkmcnt(4)\n"
        "ds_read_b64_tr_b16 a[168:169], v255 offset:32\n"
        "ds_read_b64_tr_b16 a[170:171], v255 offset:800\n"
        "v_cvt_pk_bf16_f32 v190, v76, v77\n"
        "v_cvt_pk_bf16_f32 v191, v78, v79\n"
        "v_mfma_f32_16x16x32_bf16 v[64:67], v[168:171], v[160:163], v[64:67]\n"
        "ds_read_b64_tr_b16 a[192:193], v255 offset:64\n"
        "ds_read_b64_tr_b16 a[194:195], v255 offset:832\n"
        "v_cvt_pk_bf16_f32 v192, v192, v193\n"
        "v_cvt_pk_bf16_f32 v193, v194, v195\n"
        "v_mfma_f32_16x16x32_bf16 v[96:99], v[172:175], v[156:159], v[4:7]\n"
        "buffer_load_dwordx4 v129, s[20:23], s59 offen offset:1792 lds\n"
        "v_cvt_pk_bf16_f32 v194, v80, v81\n"
        "v_cvt_pk_bf16_f32 v195, v82, v83\n"
        "v_exp_f32 v64, v64\n"
        "v_mfma_f32_16x16x32_bf16 v[96:99], v[188:191], v[164:167], v[96:99]\n"
        "ds_read_b128 v[162:165], v252 offset:0\n"
        "v_exp_f32 v65, v65\n"
        "v_exp_f32 v66, v66\n"
        "v_cvt_pk_bf16_f32 v64, v64, v65\n"
        "v_mfma_f32_16x16x32_bf16 v[72:75], v[152:155], v[176:179], v[16:19]\n"
        "buffer_load_dwordx4 v130, s[20:23], s59 offen offset:2816 lds\n"
        "v_cvt_pk_bf16_f32 v208, v84, v85\n"
        "v_cvt_pk_bf16_f32 v209, v86, v87\n"
        "v_exp_f32 v96, v96\n"
        "v_mfma_f32_16x16x32_bf16 v[72:75], v[168:171], v[180:183], v[72:75]\n"
        "s_add_u32 m0, s65, 14592\n"
        "s_waitcnt vmcnt(9)\n"
        "ds_read_b64_tr_b16 a[148:149], v255 offset:3072\n"
        "ds_read_b64_tr_b16 a[150:151], v255 offset:3840\n"
        "v_exp_f32 v67, v67\n"
        "v_exp_f32 v97, v97\n"
        "v_mfma_f32_16x16x32_bf16 v[100:103], v[172:175], v[176:179], v[20:23]\n"
        "buffer_load_dwordx4 v128, s[20:23], s60 offen offset:768 lds\n"
        "ds_read_b64_tr_b16 a[172:173], v255 offset:3104\n"
        "ds_read_b64_tr_b16 a[174:175], v255 offset:3872\n"
        "v_cvt_pk_bf16_f32 v212, v212, v213\n"
        "v_cvt_pk_bf16_f32 v213, v214, v215\n"
        "v_mfma_f32_16x16x32_bf16 v[100:103], v[188:191], v[184:187], v[100:103]\n"
        "ds_read_b128 v[182:185], v252 offset:512\n"
        "ds_read_b64_tr_b16 a[196:197], v255 offset:3136\n"
        "ds_read_b64_tr_b16 a[198:199], v255 offset:3904\n"
        "v_cvt_pk_bf16_f32 v65, v66, v67\n"
        "v_exp_f32 v98, v98\n"
        "v_mfma_f32_16x16x32_bf16 v[68:71], v[192:195], v[156:159], v[8:11]\n"
        "s_waitcnt lgkmcnt(7)\n"
        "ds_read_b128 a[228:231], v240 offset:12288\n"
        "ds_read_b128 a[232:235], v241 offset:12288\n"
        "v_cvt_pk_bf16_f32 v66, v96, v97\n"
        "v_exp_f32 v72, v72\n"
        "v_cvt_pk_bf16_f32 v214, v88, v89\n"
        "v_mfma_f32_16x16x32_bf16 v[68:71], v[208:211], v[160:163], v[68:71]\n"
        "buffer_load_dwordx4 v129, s[20:23], s60 offen offset:1792 lds\n"
        "v_cvt_pk_bf16_f32 v215, v90, v91\n"
        "v_exp_f32 v99, v99\n"
        "v_cvt_pk_bf16_f32 v230, v92, v93\n"
        "v_mfma_f32_16x16x32_bf16 v[76:79], v[192:195], v[176:179], v[24:27]\n"
        "s_waitcnt lgkmcnt(4)\n"
        "s_waitcnt vmcnt(8)\n"
        "ds_read_b64_tr_b16 a[152:153], v255 offset:6144\n"
        "ds_read_b64_tr_b16 a[154:155], v255 offset:6912\n"
        "v_exp_f32 v73, v73\n"
        "v_cvt_pk_bf16_f32 v231, v94, v95\n"
        "v_cvt_pk_bf16_f32 v67, v98, v99\n"
        "v_mfma_f32_16x16x32_bf16 v[76:79], v[208:211], v[180:183], v[76:79]\n"
        "buffer_load_dwordx4 v130, s[20:23], s60 offen offset:2816 lds\n"
        "ds_read_b64_tr_b16 a[176:177], v255 offset:6176\n"
        "ds_read_b64_tr_b16 a[178:179], v255 offset:6944\n"
        "v_exp_f32 v74, v74\n"
        "v_mfma_f32_16x16x32_bf16 v[104:107], v[212:215], v[156:159], v[12:15]\n"
        "s_add_u32 m0, s40, 98304\n"
        "ds_read_b64_tr_b16 a[200:201], v255 offset:6208\n"
        "ds_read_b64_tr_b16 a[202:203], v255 offset:6976\n"
        "v_cvt_pk_bf16_f32 v72, v72, v73\n"
        "v_mfma_f32_16x16x32_bf16 v[104:107], v[228:231], v[164:167], v[104:107]\n"
        "s_sub_u32 s64, s47, 6291456\n"
        "ds_read_b128 a[236:239], v240 offset:12416\n"
        "v_exp_f32 v100, v100\n"
        "v_mfma_f32_16x16x32_bf16 v[108:111], v[212:215], v[176:179], v[28:31]\n"
        "buffer_load_dwordx4 v242, s[8:11], s64 offen lds\n"
        "v_mfma_f32_16x16x32_bf16 v[108:111], v[228:231], v[184:187], v[108:111]\n"
        "s_add_u32 m0, s40, 99328\n"
        "v_exp_f32 v75, v75\n"
        "v_mfma_f32_16x16x32_bf16 v[80:83], v[152:155], v[196:199], v[32:35]\n"
        "v_exp_f32 v101, v101\n"
        "v_cvt_pk_bf16_f32 v73, v74, v75\n"
        "v_mfma_f32_16x16x32_bf16 v[80:83], v[168:171], v[200:203], v[80:83]\n"
        "buffer_load_dwordx4 v243, s[8:11], s64 offen lds\n"
        "v_cvt_pk_bf16_f32 v74, v100, v101\n"
        "v_mfma_f32_16x16x32_bf16 v[112:115], a[240:243], a[144:147], 0\n"
        "s_add_u32 m0, s40, 100352\n"
        "v_exp_f32 v102, v102\n"
        "v_mfma_f32_16x16x32_bf16 v[112:115], a[244:247], a[148:151], v[112:115]\n"
        "s_waitcnt lgkmcnt(0)\n"
        "v_exp_f32 v68, v68\n"
        "v_mfma_f32_16x16x32_bf16 v[112:115], a[248:251], a[152:155], v[112:115]\n"
        ".long 0xbec200ff\n"
        "42:\n"
        "s_branch 60b\n"
        "buffer_load_dwordx4 v244, s[8:11], s64 offen lds\n"
        "v_mfma_f32_16x16x32_bf16 v[116:119], a[248:251], a[176:179], 0\n"
        "s_add_u32 m0, s40, 110592\n"
        "v_exp_f32 v103, v103\n"
        "v_mfma_f32_16x16x32_bf16 v[116:119], a[244:247], a[172:175], v[116:119]\n"
        "s_sub_u32 s64, s47, 5505024\n"
        "v_exp_f32 v69, v69\n"
        "v_cvt_pk_bf16_f32 v75, v102, v103\n"
        "v_mfma_f32_16x16x32_bf16 v[116:119], a[240:243], a[168:171], v[116:119]\n"
        "v_exp_f32 v70, v70\n"
        "v_cvt_pk_bf16_f32 v68, v68, v69\n"
        "v_mfma_f32_16x16x32_bf16 v[96:99], v[172:175], v[196:199], v[36:39]\n"
        "buffer_load_dwordx4 v242, s[8:11], s64 offen lds\n"
        "s_waitcnt vmcnt(10)\n"
        "ds_read_b64_tr_b16 a[156:157], v255 offset:9216\n"
        "v_mfma_f32_16x16x32_bf16 v[96:99], v[188:191], v[204:207], v[96:99]\n"
        "s_add_u32 m0, s40, 111616\n"
        "ds_read_b64_tr_b16 a[158:159], v255 offset:9984\n"
        "ds_read_b64_tr_b16 a[180:181], v255 offset:9248\n"
        "ds_read_b64_tr_b16 a[182:183], v255 offset:10016\n"
        "v_mfma_f32_16x16x32_bf16 v[120:123], a[240:243], a[192:195], 0\n"
        "ds_read_b128 v[202:205], v252 offset:4096\n"
        "ds_read_b64_tr_b16 a[204:205], v255 offset:9280\n"
        "ds_read_b64_tr_b16 a[206:207], v255 offset:10048\n"
        "v_mfma_f32_16x16x32_bf16 v[120:123], a[244:247], a[196:199], v[120:123]\n"
        "buffer_load_dwordx4 v243, s[8:11], s64 offen lds\n"
        "v_mfma_f32_16x16x32_bf16 v[120:123], a[248:251], a[200:203], v[120:123]\n"
        "s_add_u32 m0, s40, 112640\n"
        "ds_read_b128 a[240:243], v240 offset:13824\n"
        "ds_read_b128 a[244:247], v241 offset:13824\n"
        "v_mfma_f32_16x16x32_bf16 v[124:127], a[216:219], a[144:147], 0\n"
        "buffer_load_dwordx4 v244, s[8:11], s64 offen lds\n"
        "v_mfma_f32_16x16x32_bf16 v[124:127], a[220:223], a[148:151], v[124:127]\n"
        "s_xor_b32 m0, s48, 8192\n"
        "s_waitcnt vmcnt(9)\n"
        "ds_read_b64_tr_b16 a[160:161], v255 offset:12288\n"
        "ds_read_b64_tr_b16 a[162:163], v255 offset:13056\n"
        "v_mfma_f32_16x16x32_bf16 v[124:127], a[224:227], a[152:155], v[124:127]\n"
        "s_sub_u32 s64, s36, 2097152\n"
        "ds_read_b64_tr_b16 a[184:185], v255 offset:12320\n"
        "ds_read_b64_tr_b16 a[186:187], v255 offset:13088\n"
        "v_mfma_f32_16x16x32_bf16 v[156:159], a[224:227], a[176:179], 0\n"
        "buffer_load_dwordx4 v249, s[12:15], s64 offen lds\n"
        "ds_read_b64_tr_b16 a[208:209], v255 offset:12352\n"
        "v_mfma_f32_16x16x32_bf16 v[156:159], a[220:223], a[172:175], v[156:159]\n"
        "ds_read_b64_tr_b16 a[210:211], v255 offset:13120\n"
        "ds_read_b128 a[248:251], v240 offset:13952\n"
        "v_mfma_f32_16x16x32_bf16 v[156:159], a[216:219], a[168:171], v[156:159]\n"
        "ds_read_b128 v[222:225], v251 offset:4608\n"
        "v_exp_f32 v76, v76\n"
        "v_mfma_f32_16x16x32_bf16 v[84:87], v[192:195], v[196:199], v[40:43]\n"
        "s_waitcnt lgkmcnt(8)\n"
        "v_exp_f32 v71, v71\n"
        "v_mfma_f32_16x16x32_bf16 v[84:87], v[208:211], v[200:203], v[84:87]\n"
        "buffer_load_dwordx4 v253, s[12:15], s64 offen offset:1024 lds\n"
        "v_cvt_pk_bf16_f32 v69, v70, v71\n"
        "v_mfma_f32_16x16x32_bf16 v[160:163], a[216:219], a[192:195], 0\n"
        "s_add_u32 m0, s65, 12288\n"
        "ds_read_b128 a[216:219], v241 offset:128\n"
        "v_exp_f32 v77, v77\n"
        "v_mfma_f32_16x16x32_bf16 v[160:163], a[220:223], a[196:199], v[160:163]\n"
        "ds_read_b128 a[220:223], v240 offset:256\n"
        "v_exp_f32 v78, v78\n"
        "v_mfma_f32_16x16x32_bf16 v[160:163], a[224:227], a[200:203], v[160:163]\n"
        "ds_read_b128 a[224:227], v241 offset:256\n"
        "v_cvt_pk_bf16_f32 v76, v76, v77\n"
        "v_mfma_f32_16x16x32_bf16 v[164:167], a[228:231], a[144:147], 0\n"
        "s_waitcnt lgkmcnt(3)\n"
        "buffer_load_dword v136, s[52:55], 0 offen lds\n"
        "v_mfma_f32_16x16x32_bf16 v[164:167], a[232:235], a[148:151], v[164:167]\n"
        "v_exp_f32 v104, v104\n"
        "v_mfma_f32_16x16x32_bf16 v[164:167], a[236:239], a[152:155], v[164:167]\n"
        "v_exp_f32 v79, v79\n"
        "v_mfma_f32_16x16x32_bf16 v[100:103], v[212:215], v[196:199], v[44:47]\n"
        "v_exp_f32 v105, v105\n"
        "v_cvt_pk_bf16_f32 v77, v78, v79\n"
        "v_mfma_f32_16x16x32_bf16 v[100:103], v[228:231], v[204:207], v[100:103]\n"
        "v_exp_f32 v106, v106\n"
        "v_cvt_pk_bf16_f32 v70, v104, v105\n"
        "v_mfma_f32_16x16x32_bf16 v[176:179], a[236:239], a[176:179], 0\n"
        "v_exp_f32 v108, v108\n"
        "v_mfma_f32_16x16x32_bf16 v[176:179], a[232:235], a[172:175], v[176:179]\n"
        "v_exp_f32 v107, v107\n"
        "v_mfma_f32_16x16x32_bf16 v[176:179], a[228:231], a[168:171], v[176:179]\n"
        "s_waitcnt vmcnt(9)\n"
        "ds_read_b64_tr_b16 a[164:165], v255 offset:15360\n"
        "ds_read_b64_tr_b16 a[166:167], v255 offset:16128\n"
        "v_cvt_pk_bf16_f32 v71, v106, v107\n"
        "v_mfma_f32_16x16x32_bf16 v[180:183], a[228:231], a[192:195], 0\n"
        "ds_read_b64_tr_b16 a[188:189], v255 offset:15392\n"
        "ds_read_b64_tr_b16 a[190:191], v255 offset:16160\n"
        "v_mfma_f32_16x16x32_bf16 v[180:183], a[232:235], a[196:199], v[180:183]\n"
        "ds_read_b64_tr_b16 a[212:213], v255 offset:15424\n"
        "ds_read_b64_tr_b16 a[214:215], v255 offset:16192\n"
        "v_mfma_f32_16x16x32_bf16 v[180:183], a[236:239], a[200:203], v[180:183]\n"
        "ds_read_b128 a[228:231], v241 offset:1664\n"
        "ds_read_b128 a[232:235], v240 offset:1792\n"
        "v_mfma_f32_16x16x32_bf16 v[184:187], a[240:243], a[144:147], 0\n"
        "ds_read_b128 a[236:239], v241 offset:1792\n"
        "v_exp_f32 v109, v109\n"
        "v_mfma_f32_16x16x32_bf16 v[184:187], a[244:247], a[148:151], v[184:187]\n"
        "v_exp_f32 v110, v110\n"
        "v_cvt_pk_bf16_f32 v78, v108, v109\n"
        "v_mfma_f32_16x16x32_bf16 v[184:187], a[248:251], a[152:155], v[184:187]\n"
        "v_exp_f32 v111, v111\n"
        "v_mfma_f32_16x16x32_bf16 v[196:199], a[248:251], a[176:179], 0\n"
        "v_cvt_pk_bf16_f32 v79, v110, v111\n"
        "v_exp_f32 v80, v80\n"
        "v_mfma_f32_16x16x32_bf16 v[196:199], a[244:247], a[172:175], v[196:199]\n"
        "v_exp_f32 v81, v81\n"
        "v_mfma_f32_16x16x32_bf16 v[196:199], a[240:243], a[168:171], v[196:199]\n"
        "v_exp_f32 v82, v82\n"
        "v_cvt_pk_bf16_f32 v80, v80, v81\n"
        "v_mfma_f32_16x16x32_bf16 v[200:203], a[240:243], a[192:195], 0\n"
        "ds_read_b128 a[240:243], v241 offset:12416\n"
        "v_exp_f32 v83, v83\n"
        "v_mfma_f32_16x16x32_bf16 v[200:203], a[244:247], a[196:199], v[200:203]\n"
        "ds_read_b128 a[244:247], v240 offset:12544\n"
        "v_cvt_pk_bf16_f32 v81, v82, v83\n"
        "v_mfma_f32_16x16x32_bf16 v[200:203], a[248:251], a[200:203], v[200:203]\n"
        "ds_read_b128 a[248:251], v241 offset:12544\n"
        "v_exp_f32 v96, v96\n"
        "v_mfma_f32_16x16x32_bf16 v[88:91], v[152:155], v[216:219], v[48:51]\n"
        "v_exp_f32 v97, v97\n"
        "v_mfma_f32_16x16x32_bf16 v[88:91], v[168:171], v[220:223], v[88:91]\n"
        "s_waitcnt lgkmcnt(3)\n"
        "v_exp_f32 v98, v98\n"
        "v_cvt_pk_bf16_f32 v82, v96, v97\n"
        "v_mfma_f32_16x16x32_bf16 v[112:115], a[216:219], a[156:159], v[112:115]\n"
        "v_exp_f32 v99, v99\n"
        "v_mfma_f32_16x16x32_bf16 v[112:115], a[220:223], a[160:163], v[112:115]\n"
        "v_cvt_pk_bf16_f32 v83, v98, v99\n"
        "v_exp_f32 v84, v84\n"
        "v_mfma_f32_16x16x32_bf16 v[112:115], a[224:227], a[164:167], v[112:115]\n"
        "v_exp_f32 v85, v85\n"
        "v_mfma_f32_16x16x32_bf16 v[116:119], a[224:227], a[188:191], v[116:119]\n"
        "v_exp_f32 v86, v86\n"
        "v_cvt_pk_bf16_f32 v84, v84, v85\n"
        "v_mfma_f32_16x16x32_bf16 v[116:119], a[220:223], a[184:187], v[116:119]\n"
        "v_exp_f32 v87, v87\n"
        "v_mfma_f32_16x16x32_bf16 v[116:119], a[216:219], a[180:183], v[116:119]\n"
        "v_cvt_pk_bf16_f32 v85, v86, v87\n"
        "v_exp_f32 v100, v100\n"
        "v_mfma_f32_16x16x32_bf16 v[120:123], a[216:219], a[204:207], v[120:123]\n"
        "ds_read_b128 a[216:219], v241 offset:13952\n"
        "v_exp_f32 v101, v101\n"
        "v_mfma_f32_16x16x32_bf16 v[120:123], a[220:223], a[208:211], v[120:123]\n"
        "ds_read_b128 a[220:223], v240 offset:14080\n"
        "v_exp_f32 v102, v102\n"
        "v_mfma_f32_16x16x32_bf16 v[120:123], a[224:227], a[212:215], v[120:123]\n"
        "ds_read_b128 a[224:227], v241 offset:14080\n"
        "v_cvt_pk_bf16_f32 v86, v100, v101\n"
        "v_mfma_f32_16x16x32_bf16 v[124:127], a[228:231], a[156:159], v[124:127]\n"
        "v_exp_f32 v103, v103\n"
        "v_cvt_pk_bf16_f32 v128, v112, v113\n"
        "v_mfma_f32_16x16x32_bf16 v[124:127], a[232:235], a[160:163], v[124:127]\n"
        "v_cvt_pk_bf16_f32 v87, v102, v103\n"
        "v_cvt_pk_bf16_f32 v129, v114, v115\n"
        "v_mfma_f32_16x16x32_bf16 v[124:127], a[236:239], a[164:167], v[124:127]\n"
        "v_cvt_pk_bf16_f32 v136, v116, v117\n"
        "v_cvt_pk_bf16_f32 v137, v118, v119\n"
        "v_mfma_f32_16x16x32_bf16 v[104:107], v[172:175], v[216:219], v[52:55]\n"
        "v_cvt_pk_bf16_f32 v144, v120, v121\n"
        "v_cvt_pk_bf16_f32 v145, v122, v123\n"
        "v_mfma_f32_16x16x32_bf16 v[104:107], v[188:191], v[224:227], v[104:107]\n"
        "ds_read_b128 v[222:225], v252 offset:4608\n"
        "s_nop 0\n"
        "v_mfma_f32_16x16x32_bf16 v[156:159], a[236:239], a[188:191], v[156:159]\n"
        "v_exp_f32 v88, v88\n"
        "v_mfma_f32_16x16x32_bf16 v[156:159], a[232:235], a[184:187], v[156:159]\n"
        "v_cvt_pk_bf16_f32 v130, v124, v125\n"
        "v_cvt_pk_bf16_f32 v131, v126, v127\n"
        "v_mfma_f32_16x16x32_bf16 v[156:159], a[228:231], a[180:183], v[156:159]\n"
        "v_exp_f32 v89, v89\n"
        "v_mfma_f32_16x16x32_bf16 v[160:163], a[228:231], a[204:207], v[160:163]\n"
        "v_exp_f32 v90, v90\n"
        "v_mfma_f32_16x16x32_bf16 v[160:163], a[232:235], a[208:211], v[160:163]\n"
        "v_exp_f32 v91, v91\n"
        "v_mfma_f32_16x16x32_bf16 v[160:163], a[236:239], a[212:215], v[160:163]\n"
        "s_waitcnt lgkmcnt(0)\n"
        "v_exp_f32 v104, v104\n"
        "v_mfma_f32_16x16x32_bf16 v[164:167], a[240:243], a[156:159], v[164:167]\n"
        "v_cvt_pk_bf16_f32 v138, v156, v157\n"
        "v_cvt_pk_bf16_f32 v139, v158, v159\n"
        "v_mfma_f32_16x16x32_bf16 v[164:167], a[244:247], a[160:163], v[164:167]\n"
        "v_cvt_pk_bf16_f32 v88, v88, v89\n"
        "v_cvt_pk_bf16_f32 v89, v90, v91\n"
        "v_mfma_f32_16x16x32_bf16 v[164:167], a[248:251], a[164:167], v[164:167]\n"
        "v_exp_f32 v105, v105\n"
        "v_mfma_f32_16x16x32_bf16 v[92:95], v[192:195], v[216:219], v[56:59]\n"
        "v_cvt_pk_bf16_f32 v146, v160, v161\n"
        "v_cvt_pk_bf16_f32 v147, v162, v163\n"
        "v_mfma_f32_16x16x32_bf16 v[92:95], v[208:211], v[220:223], v[92:95]\n"
        "v_exp_f32 v106, v106\n"
        "v_mfma_f32_16x16x32_bf16 v[176:179], a[248:251], a[188:191], v[176:179]\n"
        "v_exp_f32 v107, v107\n"
        "v_mfma_f32_16x16x32_bf16 v[176:179], a[244:247], a[184:187], v[176:179]\n"
        "v_cvt_pk_bf16_f32 v132, v164, v165\n"
        "v_cvt_pk_bf16_f32 v133, v166, v167\n"
        "v_mfma_f32_16x16x32_bf16 v[176:179], a[240:243], a[180:183], v[176:179]\n"
        "v_cvt_pk_bf16_f32 v90, v104, v105\n"
        "v_cvt_pk_bf16_f32 v91, v106, v107\n"
        "v_mfma_f32_16x16x32_bf16 v[180:183], a[240:243], a[204:207], v[180:183]\n"
        "v_exp_f32 v92, v92\n"
        "v_mfma_f32_16x16x32_bf16 v[180:183], a[244:247], a[208:211], v[180:183]\n"
        "v_exp_f32 v93, v93\n"
        "v_mfma_f32_16x16x32_bf16 v[180:183], a[248:251], a[212:215], v[180:183]\n"
        "v_mov_b64 v[96:97], 0\n"
        "v_cvt_pk_bf16_f32 v140, v176, v177\n"
        "v_mfma_f32_16x16x32_bf16 v[184:187], a[216:219], a[156:159], v[184:187]\n"
        "v_cvt_pk_bf16_f32 v141, v178, v179\n"
        "v_cvt_pk_bf16_f32 v92, v92, v93\n"
        "v_mfma_f32_16x16x32_bf16 v[184:187], a[220:223], a[160:163], v[184:187]\n"
        "v_exp_f32 v94, v94\n"
        "v_mfma_f32_16x16x32_bf16 v[184:187], a[224:227], a[164:167], v[184:187]\n"
        "v_exp_f32 v95, v95\n"
        "v_mfma_f32_16x16x32_bf16 v[108:111], v[212:215], v[216:219], v[60:63]\n"
        "v_cvt_pk_bf16_f32 v148, v180, v181\n"
        "v_cvt_pk_bf16_f32 v149, v182, v183\n"
        "v_mfma_f32_16x16x32_bf16 v[108:111], v[228:231], v[224:227], v[108:111]\n"
        "v_add_u32 v240, 24576, v240\n"
        "v_add_u32 v241, 24576, v241\n"
        "v_mfma_f32_16x16x32_bf16 v[196:199], a[224:227], a[188:191], v[196:199]\n"
        "v_cvt_pk_bf16_f32 v134, v184, v185\n"
        "v_cvt_pk_bf16_f32 v135, v186, v187\n"
        "v_mfma_f32_16x16x32_bf16 v[196:199], a[220:223], a[184:187], v[196:199]\n"
        "v_mov_b64 v[172:173], 0\n"
        "v_cvt_pk_bf16_f32 v93, v94, v95\n"
        "v_mfma_f32_16x16x32_bf16 v[196:199], a[216:219], a[180:183], v[196:199]\n"
        "v_exp_f32 v108, v108\n"
        "v_mfma_f32_16x16x32_bf16 v[200:203], a[216:219], a[204:207], v[200:203]\n"
        "v_exp_f32 v109, v109\n"
        "v_mfma_f32_16x16x32_bf16 v[200:203], a[220:223], a[208:211], v[200:203]\n"
        "v_exp_f32 v110, v110\n"
        "v_mfma_f32_16x16x32_bf16 v[200:203], a[224:227], a[212:215], v[200:203]\n"
        "v_exp_f32 v111, v111\n"
        "v_smfmac_f32_16x16x64_bf16 v[96:99], a[252:255], v[64:71], v237\n"
        "v_cvt_pk_bf16_f32 v142, v196, v197\n"
        "v_mfma_f32_16x16x32_bf16 v[100:103], v[132:135], v[68:71], 0\n"
        "v_cvt_pk_bf16_f32 v143, v198, v199\n"
        "v_cvt_pk_bf16_f32 v94, v108, v109\n"
        "v_mfma_f32_16x16x32_bf16 v[100:103], v[128:131], v[64:67], v[100:103]\n"
        "v_mov_b64 v[192:193], 0\n"
        "v_cvt_pk_bf16_f32 v150, v200, v201\n"
        "v_mfma_f32_16x16x32_bf16 v[104:107], v[136:139], v[64:67], 0\n"
        "v_cvt_pk_bf16_f32 v151, v202, v203\n"
        "v_add_f32 v97, v96, v97\n"
        "v_mfma_f32_16x16x32_bf16 v[104:107], v[140:143], v[68:71], v[104:107]\n"
        "v_rcp_f32 v96, v97\n"
        "v_cvt_pk_bf16_f32 v95, v110, v111\n"
        "v_mfma_f32_16x16x32_bf16 v[108:111], v[148:151], v[68:71], 0\n"
        "v_mul_f32 v100, v96, v100\n"
        "v_mul_f32 v101, v96, v101\n"
        "v_mfma_f32_16x16x32_bf16 v[108:111], v[144:147], v[64:67], v[108:111]\n"
        "v_mul_f32 v102, v96, v102\n"
        "v_mul_f32 v103, v96, v103\n"
        "v_smfmac_f32_16x16x64_bf16 v[172:175], a[252:255], v[72:79], v237\n"
        "s_waitcnt vmcnt(6)\n"
        "s_barrier\n"
        "ds_read_b128 a[236:239], v240 offset:0\n"
        "v_mfma_f32_16x16x32_bf16 v[176:179], v[132:135], v[76:79], 0\n"
        "ds_read_b128 a[240:243], v241 offset:0\n"
        "ds_read_b128 a[244:247], v240 offset:128\n"
        "v_mfma_f32_16x16x32_bf16 v[176:179], v[128:131], v[72:75], v[176:179]\n"
        "ds_read_b128 a[248:251], v241 offset:128\n"
        "ds_read_b128 a[216:219], v240 offset:256\n"
        "v_mfma_f32_16x16x32_bf16 v[180:183], v[136:139], v[72:75], 0\n"
        "ds_read_b128 a[220:223], v241 offset:256\n"
        "v_cvt_pk_bf16_f32 v100, v100, v101\n"
        "v_mfma_f32_16x16x32_bf16 v[180:183], v[140:143], v[76:79], v[180:183]\n"
        "v_cvt_pk_bf16_f32 v101, v102, v103\n"
        "v_mul_f32 v104, v96, v104\n"
        "v_mfma_f32_16x16x32_bf16 v[184:187], v[148:151], v[76:79], 0\n"
        "ds_write2st64_b32 v245, v100, v101 offset0:0 offset1:4\n"
        "v_mfma_f32_16x16x32_bf16 v[184:187], v[144:147], v[72:75], v[184:187]\n"
        "v_mul_f32 v105, v96, v105\n"
        "v_mul_f32 v106, v96, v106\n"
        "v_smfmac_f32_16x16x64_bf16 v[192:195], a[252:255], v[80:87], v237\n"
        "v_mul_f32 v107, v96, v107\n"
        "v_mfma_f32_16x16x32_bf16 v[196:199], v[132:135], v[84:87], 0\n"
        "v_cvt_pk_bf16_f32 v102, v104, v105\n"
        "v_cvt_pk_bf16_f32 v103, v106, v107\n"
        "v_mfma_f32_16x16x32_bf16 v[196:199], v[128:131], v[80:83], v[196:199]\n"
        "buffer_load_dwordx4 v232, s[52:55], s34 offen lds\n"
        "v_mfma_f32_16x16x32_bf16 v[200:203], v[136:139], v[80:83], 0\n"
        "ds_write2st64_b32 v245, v102, v103 offset0:8 offset1:12\n"
        "v_mfma_f32_16x16x32_bf16 v[200:203], v[140:143], v[84:87], v[200:203]\n"
        "v_mov_b64 v[124:125], 0\n"
        "v_mul_f32 v108, v96, v108\n"
        "v_mul_f32 v109, v96, v109\n"
        "v_mfma_f32_16x16x32_bf16 v[204:207], v[148:151], v[84:87], 0\n"
        "v_mul_f32 v110, v96, v110\n"
        "v_mul_f32 v111, v96, v111\n"
        "v_mfma_f32_16x16x32_bf16 v[204:207], v[144:147], v[80:83], v[204:207]\n"
        "s_waitcnt lgkmcnt(2)\n"
        "v_cvt_pk_bf16_f32 v108, v108, v109\n"
        "v_cvt_pk_bf16_f32 v109, v110, v111\n"
        "v_mfma_f32_16x16x32_bf16 v[112:115], a[72:75], a[236:239], 0\n"
        "v_max_u32 v236, v96, v97\n"
        "v_add_f32 v173, v172, v173\n"
        "v_mfma_f32_16x16x32_bf16 v[112:115], a[76:79], a[240:243], v[112:115]\n"
        "ds_write2st64_b32 v245, v108, v109 offset0:16 offset1:20\n"
        "v_mfma_f32_16x16x32_bf16 v[112:115], a[80:83], a[244:247], v[112:115]\n"
        "v_rcp_f32 v172, v173\n"
        "v_mfma_f32_16x16x32_bf16 v[112:115], a[84:87], a[248:251], v[112:115]\n"
        "buffer_load_dwordx4 v233, s[52:55], s34 offen offset:1024 lds\n"
        "v_mfma_f32_16x16x32_bf16 v[112:115], a[88:91], a[216:219], v[112:115]\n"
        "v_mul_f32 v176, v172, v176\n"
        "v_mul_f32 v177, v172, v177\n"
        "v_mfma_f32_16x16x32_bf16 v[112:115], a[92:95], a[220:223], v[112:115]\n"
        "v_mul_f32 v178, v172, v178\n"
        "v_mul_f32 v179, v172, v179\n"
        "v_smfmac_f32_16x16x64_bf16 v[124:127], a[252:255], v[88:95], v237\n"
        "v_cvt_pk_bf16_f32 v176, v176, v177\n"
        "v_mfma_f32_16x16x32_bf16 v[80:83], v[132:135], v[92:95], 0\n"
        "v_cvt_pk_bf16_f32 v177, v178, v179\n"
        "v_mul_f32 v180, v172, v180\n"
        "v_mfma_f32_16x16x32_bf16 v[80:83], v[128:131], v[88:91], v[80:83]\n"
        "ds_write2st64_b32 v245, v176, v177 offset0:1 offset1:5\n"
        "v_mfma_f32_16x16x32_bf16 v[84:87], v[136:139], v[88:91], 0\n"
        "v_cvt_pk_bf16_f32 v152, v112, v113\n"
        "v_cvt_pk_bf16_f32 v153, v114, v115\n"
        "v_mfma_f32_16x16x32_bf16 v[84:87], v[140:143], v[92:95], v[84:87]\n"
        "v_mul_f32 v181, v172, v181\n"
        "v_mul_f32 v182, v172, v182\n"
        "v_mfma_f32_16x16x32_bf16 v[96:99], v[148:151], v[92:95], 0\n"
        "buffer_load_dwordx4 v234, s[52:55], s34 offen offset:2048 lds\n"
        "v_mfma_f32_16x16x32_bf16 v[96:99], v[144:147], v[88:91], v[96:99]\n"
        "v_mul_f32 v183, v172, v183\n"
        "v_cvt_pk_bf16_f32 v178, v180, v181\n"
        "v_mfma_f32_16x16x32_bf16 v[116:119], a[116:119], a[220:223], 0\n"
        "v_cvt_pk_bf16_f32 v179, v182, v183\n"
        "v_mul_f32 v184, v172, v184\n"
        "v_mfma_f32_16x16x32_bf16 v[116:119], a[112:115], a[216:219], v[116:119]\n"
        "ds_write2st64_b32 v245, v178, v179 offset0:9 offset1:13\n"
        "v_mfma_f32_16x16x32_bf16 v[116:119], a[108:111], a[248:251], v[116:119]\n"
        "v_mul_f32 v185, v172, v185\n"
        "v_mul_f32 v186, v172, v186\n"
        "v_mfma_f32_16x16x32_bf16 v[116:119], a[104:107], a[244:247], v[116:119]\n"
        "s_add_u32 m0, s65, 15360\n"
        "v_mul_f32 v187, v172, v187\n"
        "v_add_f32 v125, v124, v125\n"
        "v_mfma_f32_16x16x32_bf16 v[116:119], a[100:103], a[240:243], v[116:119]\n"
        "v_rcp_f32 v124, v125\n"
        "v_mfma_f32_16x16x32_bf16 v[116:119], a[96:99], a[236:239], v[116:119]\n"
        "v_cvt_pk_bf16_f32 v184, v184, v185\n"
        "v_cvt_pk_bf16_f32 v185, v186, v187\n"
        "v_mfma_f32_16x16x32_bf16 v[120:123], a[120:123], a[236:239], 0\n"
        "buffer_load_dwordx4 v232, s[52:55], s62 offen lds\n"
        "v_mfma_f32_16x16x32_bf16 v[120:123], a[124:127], a[240:243], v[120:123]\n"
        "ds_write2st64_b32 v245, v184, v185 offset0:17 offset1:21\n"
        "v_mfma_f32_16x16x32_bf16 v[120:123], a[128:131], a[244:247], v[120:123]\n"
        "v_mul_f32 v80, v124, v80\n"
        "v_mul_f32 v81, v124, v81\n"
        "v_mfma_f32_16x16x32_bf16 v[120:123], a[132:135], a[248:251], v[120:123]\n"
        "v_mul_f32 v82, v124, v82\n"
        "v_mul_f32 v83, v124, v83\n"
        "v_mfma_f32_16x16x32_bf16 v[120:123], a[136:139], a[216:219], v[120:123]\n"
        "v_max3_u32 v236, v236, v172, v173\n"
        "v_cvt_pk_bf16_f32 v80, v80, v81\n"
        "v_mfma_f32_16x16x32_bf16 v[120:123], a[140:143], a[220:223], v[120:123]\n"
        "v_cvt_pk_bf16_f32 v81, v82, v83\n"
        "v_mul_f32 v84, v124, v84\n"
        "v_mfma_f32_16x16x32_bf16 v[112:115], a[20:23], a[220:223], 0\n"
        "v_mul_f32 v85, v124, v85\n"
        "v_mul_f32 v86, v124, v86\n"
        "v_mfma_f32_16x16x32_bf16 v[112:115], a[16:19], a[216:219], v[112:115]\n"
        "v_mul_f32 v87, v124, v87\n"
        "v_cvt_pk_bf16_f32 v82, v84, v85\n"
        "v_mfma_f32_16x16x32_bf16 v[112:115], a[12:15], a[248:251], v[112:115]\n"
        "buffer_load_dwordx4 v233, s[52:55], s62 offen offset:1024 lds\n"
        "v_mfma_f32_16x16x32_bf16 v[112:115], a[8:11], a[244:247], v[112:115]\n"
        "v_cvt_pk_bf16_f32 v83, v86, v87\n"
        "v_mul_f32 v96, v124, v96\n"
        "v_mfma_f32_16x16x32_bf16 v[112:115], a[4:7], a[240:243], v[112:115]\n"
        "v_mul_f32 v97, v124, v97\n"
        "v_mul_f32 v98, v124, v98\n"
        "v_mfma_f32_16x16x32_bf16 v[112:115], a[0:3], a[236:239], v[112:115]\n"
        "v_mul_f32 v99, v124, v99\n"
        "v_cvt_pk_bf16_f32 v154, v116, v117\n"
        "v_cvt_pk_bf16_f32 v155, v118, v119\n"
        "v_mfma_f32_16x16x32_bf16 v[116:119], a[24:27], a[236:239], 0\n"
        "v_cvt_pk_bf16_f32 v96, v96, v97\n"
        "v_cvt_pk_bf16_f32 v97, v98, v99\n"
        "v_mfma_f32_16x16x32_bf16 v[116:119], a[28:31], a[240:243], v[116:119]\n"
        "ds_write_b32 v245, v96 offset:4864\n"
        "v_mfma_f32_16x16x32_bf16 v[116:119], a[32:35], a[244:247], v[116:119]\n"
        "v_cvt_pk_bf16_f32 v168, v120, v121\n"
        "v_cvt_pk_bf16_f32 v169, v122, v123\n"
        "v_mfma_f32_16x16x32_bf16 v[116:119], a[36:39], a[248:251], v[116:119]\n"
        "v_add_f32 v193, v192, v193\n"
        "v_cvt_pk_bf16_f32 v156, v112, v113\n"
        "v_mfma_f32_16x16x32_bf16 v[116:119], a[40:43], a[216:219], v[116:119]\n"
        "buffer_load_dwordx4 v234, s[52:55], s62 offen offset:2048 lds\n"
        "v_mfma_f32_16x16x32_bf16 v[116:119], a[44:47], a[220:223], v[116:119]\n"
        "v_rcp_f32 v192, v193\n"
        "v_mfma_f32_16x16x32_bf16 v[120:123], a[68:71], a[220:223], 0\n"
        "v_mul_f32 v196, v192, v196\n"
        "v_mul_f32 v197, v192, v197\n"
        "v_mfma_f32_16x16x32_bf16 v[120:123], a[64:67], a[216:219], v[120:123]\n"
        "v_mul_f32 v198, v192, v198\n"
        "v_mul_f32 v199, v192, v199\n"
        "v_mfma_f32_16x16x32_bf16 v[120:123], a[60:63], a[248:251], v[120:123]\n"
        "ds_read_b128 a[224:227], v240 offset:1536\n"
        "v_cvt_pk_bf16_f32 v157, v114, v115\n"
        "v_mfma_f32_16x16x32_bf16 v[120:123], a[56:59], a[244:247], v[120:123]\n"
        "s_add_u32 m0, s65, 0\n"
        "ds_read_b128 a[228:231], v241 offset:1536\n"
        "v_cvt_pk_bf16_f32 v196, v196, v197\n"
        "v_mfma_f32_16x16x32_bf16 v[120:123], a[52:55], a[240:243], v[120:123]\n"
        "ds_read_b128 a[232:235], v240 offset:1664\n"
        "v_cvt_pk_bf16_f32 v197, v198, v199\n"
        "v_mfma_f32_16x16x32_bf16 v[120:123], a[48:51], a[236:239], v[120:123]\n"
        "ds_write2st64_b32 v245, v196, v197 offset0:2 offset1:6\n"
        "v_mfma_f32_16x16x32_bf16 v[112:115], a[236:239], a[144:147], 0\n"
        "buffer_load_dwordx4 v232, s[52:55], 0 offen lds\n"
        "v_mfma_f32_16x16x32_bf16 v[112:115], a[240:243], a[148:151], v[112:115]\n"
        "v_mul_f32 v200, v192, v200\n"
        "v_mul_f32 v201, v192, v201\n"
        "v_mfma_f32_16x16x32_bf16 v[112:115], a[244:247], a[152:155], v[112:115]\n"
        "v_mul_f32 v202, v192, v202\n"
        "v_mul_f32 v203, v192, v203\n"
        "v_mfma_f32_16x16x32_bf16 v[112:115], a[248:251], a[156:159], v[112:115]\n"
        "v_cvt_pk_bf16_f32 v198, v200, v201\n"
        "v_cvt_pk_bf16_f32 v199, v202, v203\n"
        "v_mfma_f32_16x16x32_bf16 v[112:115], a[216:219], a[160:163], v[112:115]\n"
        "ds_write2st64_b32 v245, v198, v199 offset0:10 offset1:14\n"
        "v_mfma_f32_16x16x32_bf16 v[112:115], a[220:223], a[164:167], v[112:115]\n"
        "v_mul_f32 v204, v192, v204\n"
        "v_mul_f32 v205, v192, v205\n"
        "v_cvt_pk_bf16_f32 v158, v116, v117\n"
        "v_cvt_pk_bf16_f32 v159, v118, v119\n"
        "v_mfma_f32_16x16x32_bf16 v[116:119], a[220:223], a[188:191], 0\n"
        "s_waitcnt vmcnt(4)\n"
        "ds_read_b64_tr_b16 v[64:65], v255 offset:12288\n"
        "ds_read_b64_tr_b16 v[66:67], v255 offset:13056\n"
        "v_mfma_f32_16x16x32_bf16 v[116:119], a[216:219], a[184:187], v[116:119]\n"
        "ds_write_b32 v245, v97 offset:5888\n"
        "v_mfma_f32_16x16x32_bf16 v[116:119], a[248:251], a[180:183], v[116:119]\n"
        "buffer_load_dwordx4 v233, s[52:55], 0 offen offset:1024 lds\n"
        "v_mfma_f32_16x16x32_bf16 v[116:119], a[244:247], a[176:179], v[116:119]\n"
        "ds_read_b64_tr_b16 v[68:69], v255 offset:12320\n"
        "ds_read_b64_tr_b16 v[70:71], v255 offset:13088\n"
        "v_mfma_f32_16x16x32_bf16 v[116:119], a[240:243], a[172:175], v[116:119]\n"
        "ds_read_b64_tr_b16 v[72:73], v255 offset:12352\n"
        "ds_read_b64_tr_b16 v[74:75], v255 offset:13120\n"
        "v_mfma_f32_16x16x32_bf16 v[116:119], a[236:239], a[168:171], v[116:119]\n"
        "v_mul_f32 v206, v192, v206\n"
        "v_mul_f32 v207, v192, v207\n"
        "v_cvt_pk_bf16_f32 v160, v120, v121\n"
        "v_cvt_pk_bf16_f32 v161, v122, v123\n"
        "v_mfma_f32_16x16x32_bf16 v[120:123], a[236:239], a[192:195], 0\n"
        "ds_read_b128 a[236:239], v241 offset:1664\n"
        "v_cvt_pk_bf16_f32 v204, v204, v205\n"
        "v_mfma_f32_16x16x32_bf16 v[120:123], a[240:243], a[196:199], v[120:123]\n"
        "ds_read_b128 a[240:243], v240 offset:1792\n"
        "v_cvt_pk_bf16_f32 v205, v206, v207\n"
        "v_mfma_f32_16x16x32_bf16 v[120:123], a[244:247], a[200:203], v[120:123]\n"
        "ds_write2st64_b32 v245, v204, v205 offset0:18 offset1:22\n"
        "v_mfma_f32_16x16x32_bf16 v[120:123], a[248:251], a[204:207], v[120:123]\n"
        "ds_read_b128 a[244:247], v241 offset:1792\n"
        "v_max3_u32 v236, v236, v192, v193\n"
        "v_mfma_f32_16x16x32_bf16 v[120:123], a[216:219], a[208:211], v[120:123]\n"
        "buffer_load_dwordx4 v234, s[52:55], 0 offen offset:2048 lds\n"
        "v_mfma_f32_16x16x32_bf16 v[120:123], a[220:223], a[212:215], v[120:123]\n"
        "s_waitcnt lgkmcnt(4)\n"
        "ds_write_b64 v235, v[64:65] offset:12288\n"
        "v_cvt_pk_bf16_f32 v128, v112, v113\n"
        "v_cvt_pk_bf16_f32 v129, v114, v115\n"
        "v_mfma_f32_16x16x32_bf16 v[112:115], a[72:75], a[224:227], 0\n"
        "s_barrier\n"
        "s_waitcnt vmcnt(3)\n"
        "ds_read_b64_tr_b16 v[76:77], v255 offset:15360\n"
        "ds_read_b64_tr_b16 v[78:79], v255 offset:16128\n"
        "v_mfma_f32_16x16x32_bf16 v[112:115], a[76:79], a[228:231], v[112:115]\n"
        "v_max3_u32 v236, v236, v124, v125\n"
        "v_cmp_lt_u32 vcc, s24, v236\n"
        "v_mfma_f32_16x16x32_bf16 v[112:115], a[80:83], a[232:235], v[112:115]\n"
        "s_waitcnt lgkmcnt(5)\n"
        "ds_write_b64 v235, v[66:67] offset:12296\n"
        "s_cmp_lg_u64 vcc, 0\n"
        "s_addc_u32 s25, s25, s25\n"
        "v_mfma_f32_16x16x32_bf16 v[112:115], a[84:87], a[236:239], v[112:115]\n"
        "s_add_u32 m0, s65, 3072\n"
        "v_mov_b64 v[166:167], v[160:161]\n"
        "v_and_b32 v239, 32, v245\n"
        "v_mfma_f32_16x16x32_bf16 v[112:115], a[88:91], a[240:243], v[112:115]\n"
        "s_waitcnt lgkmcnt(4)\n"
        "v_cvt_pk_bf16_f32 v136, v116, v117\n"
        "v_cvt_pk_bf16_f32 v137, v118, v119\n"
        "v_mfma_f32_16x16x32_bf16 v[112:115], a[92:95], a[244:247], v[112:115]\n"
        "v_xor_b32 v251, 0x2000, v251\n"
        "v_xor_b32 v252, 0x2000, v252\n"
        "v_mfma_f32_16x16x32_bf16 v[116:119], a[116:119], a[244:247], 0\n"
        "buffer_load_dwordx4 v232, s[52:55], s63 offen lds\n"
        "v_mfma_f32_16x16x32_bf16 v[116:119], a[112:115], a[240:243], v[116:119]\n"
        "ds_read_b64_tr_b16 v[88:89], v255 offset:15392\n"
        "ds_read_b64_tr_b16 v[90:91], v255 offset:16160\n"
        "v_mfma_f32_16x16x32_bf16 v[116:119], a[108:111], a[236:239], v[116:119]\n"
        "v_mad_i32_i24 v239, v239, -12, v235\n"
        "v_cvt_pk_bf16_f32 v144, v120, v121\n"
        "v_mfma_f32_16x16x32_bf16 v[116:119], a[104:107], a[232:235], v[116:119]\n"
        "ds_write_b64 v239, v[68:69] offset:12480\n"
        "v_mfma_f32_16x16x32_bf16 v[116:119], a[100:103], a[228:231], v[116:119]\n"
        "v_cvt_pk_bf16_f32 v145, v122, v123\n"
        "v_cvt_pk_bf16_f32 v172, v112, v113\n"
        "v_mfma_f32_16x16x32_bf16 v[116:119], a[96:99], a[224:227], v[116:119]\n"
        "ds_write_b64 v239, v[70:71] offset:12488\n"
        "v_mfma_f32_16x16x32_bf16 v[120:123], a[120:123], a[224:227], 0\n"
        "ds_read_b128 v[162:165], v251 offset:0\n"
        "v_cvt_pk_bf16_f32 v173, v114, v115\n"
        "v_mfma_f32_16x16x32_bf16 v[120:123], a[124:127], a[228:231], v[120:123]\n"
        "v_add_u32 v247, s45, v247\n"
        "s_nop 0\n"
        "v_mfma_f32_16x16x32_bf16 v[120:123], a[128:131], a[232:235], v[120:123]\n"
        "ds_write_b64 v254, v[72:73] offset:12288\n"
        "v_mfma_f32_16x16x32_bf16 v[120:123], a[132:135], a[236:239], v[120:123]\n"
        "buffer_load_dwordx4 v233, s[52:55], s63 offen offset:1024 lds\n"
        "v_mfma_f32_16x16x32_bf16 v[120:123], a[136:139], a[240:243], v[120:123]\n"
        "ds_read_b64_tr_b16 v[92:93], v255 offset:15424\n"
        "ds_read_b64_tr_b16 v[94:95], v255 offset:16192\n"
        "v_mfma_f32_16x16x32_bf16 v[120:123], a[140:143], a[244:247], v[120:123]\n"
        "ds_write_b64 v254, v[74:75] offset:12296\n"
        "v_mfma_f32_16x16x32_bf16 v[112:115], a[20:23], a[244:247], 0\n"
        "v_cvt_pk_bf16_f32 v174, v116, v117\n"
        "v_cvt_pk_bf16_f32 v175, v118, v119\n"
        "v_mfma_f32_16x16x32_bf16 v[112:115], a[16:19], a[240:243], v[112:115]\n"
        "s_nop 2\n"
        "v_mfma_f32_16x16x32_bf16 v[112:115], a[12:15], a[236:239], v[112:115]\n"
        "v_cvt_pk_bf16_f32 v190, v120, v121\n"
        "v_cvt_pk_bf16_f32 v191, v122, v123\n"
        "v_mfma_f32_16x16x32_bf16 v[112:115], a[8:11], a[232:235], v[112:115]\n"
        "s_waitcnt vmcnt(2)\n"
        "ds_read_b64_tr_b16 v[212:213], v255 offset:0\n"
        "ds_read_b64_tr_b16 v[214:215], v255 offset:768\n"
        "v_mfma_f32_16x16x32_bf16 v[112:115], a[4:7], a[228:231], v[112:115]\n"
        "buffer_load_dwordx4 v234, s[52:55], s63 offen offset:2048 lds\n"
        "v_mfma_f32_16x16x32_bf16 v[112:115], a[0:3], a[224:227], v[112:115]\n"
        "ds_read_b128 v[182:185], v251 offset:512\n"
        "s_nop 0\n"
        "v_mfma_f32_16x16x32_bf16 v[116:119], a[24:27], a[224:227], 0\n"
        "s_nop 2\n"
        "v_mfma_f32_16x16x32_bf16 v[116:119], a[28:31], a[228:231], v[116:119]\n"
        "s_waitcnt lgkmcnt(4)\n"
        "ds_write_b128 v235, v[76:79] offset:15360\n"
        "v_mfma_f32_16x16x32_bf16 v[116:119], a[32:35], a[232:235], v[116:119]\n"
        "v_cvt_pk_bf16_f32 v176, v112, v113\n"
        "v_cvt_pk_bf16_f32 v177, v114, v115\n"
        "v_mfma_f32_16x16x32_bf16 v[116:119], a[36:39], a[236:239], v[116:119]\n"
        "s_add_u32 m0, s65, 6144\n"
        "v_mfma_f32_16x16x32_bf16 v[116:119], a[40:43], a[240:243], v[116:119]\n"
        "ds_write_b32 v245, v80 offset:768\n"
        "v_mfma_f32_16x16x32_bf16 v[116:119], a[44:47], a[244:247], v[116:119]\n"
        "ds_read_b64_tr_b16 v[216:217], v255 offset:32\n"
        "ds_read_b64_tr_b16 v[218:219], v255 offset:800\n"
        "v_mfma_f32_16x16x32_bf16 v[120:123], a[68:71], a[244:247], 0\n"
        "buffer_load_dwordx4 v232, s[52:55], s61 offen lds\n"
        "v_mfma_f32_16x16x32_bf16 v[120:123], a[64:67], a[240:243], v[120:123]\n"
        "s_nop 2\n"
        "v_mfma_f32_16x16x32_bf16 v[120:123], a[60:63], a[236:239], v[120:123]\n"
        "ds_write_b128 v239, v[88:91] offset:15552\n"
        "v_mfma_f32_16x16x32_bf16 v[120:123], a[56:59], a[232:235], v[120:123]\n"
        "v_cvt_pk_bf16_f32 v178, v116, v117\n"
        "v_cvt_pk_bf16_f32 v179, v118, v119\n"
        "v_mfma_f32_16x16x32_bf16 v[120:123], a[52:55], a[228:231], v[120:123]\n"
        "v_mfma_f32_16x16x32_bf16 v[120:123], a[48:51], a[224:227], v[120:123]\n"
        "ds_write_b32 v245, v81 offset:1792\n"
        "v_mfma_f32_16x16x32_bf16 v[112:115], a[224:227], a[144:147], 0\n"
        "ds_read_b64_tr_b16 v[220:221], v255 offset:64\n"
        "ds_read_b64_tr_b16 v[222:223], v255 offset:832\n"
        "v_mfma_f32_16x16x32_bf16 v[112:115], a[228:231], a[148:151], v[112:115]\n"
        "ds_write_b32 v245, v82 offset:2816\n"
        "v_mfma_f32_16x16x32_bf16 v[112:115], a[232:235], a[152:155], v[112:115]\n"
        "ds_write_b32 v245, v83 offset:3840\n"
        "v_mfma_f32_16x16x32_bf16 v[112:115], a[236:239], a[156:159], v[112:115]\n"
        "buffer_load_dwordx4 v233, s[52:55], s61 offen offset:1024 lds\n"
        "v_mfma_f32_16x16x32_bf16 v[112:115], a[240:243], a[160:163], v[112:115]\n"
        "ds_read_b128 a[248:251], v240 offset:12288\n"
        "v_cvt_pk_bf16_f32 v180, v120, v121\n"
        "v_mfma_f32_16x16x32_bf16 v[112:115], a[244:247], a[164:167], v[112:115]\n"
        ".long 0xbec200ff\n"
        "43:\n"
        "s_branch 60b\n"
        "ds_write_b128 v254, v[92:95] offset:15360\n"
        "v_mfma_f32_16x16x32_bf16 v[96:99], v[152:155], v[156:159], v[0:3]\n"
        "v_cvt_pk_bf16_f32 v181, v122, v123\n"
        "v_mov_b64 v[186:187], v[180:181]\n"
        "v_mfma_f32_16x16x32_bf16 v[96:99], v[168:171], v[160:163], v[96:99]\n"
        "s_sub_u32 s31, s30, s28\n"
        "s_nop 1\n"
        "v_mfma_f32_16x16x32_bf16 v[100:103], v[172:175], v[156:159], v[4:7]\n"
        "s_sub_u32 s45, s28, s29\n"
        "v_cvt_pk_bf16_f32 v130, v112, v113\n"
        "v_cvt_pk_bf16_f32 v131, v114, v115\n"
        "v_mfma_f32_16x16x32_bf16 v[100:103], v[188:191], v[164:167], v[100:103]\n"
        "s_waitcnt vmcnt(2)\n"
        "ds_read_b64_tr_b16 v[224:225], v255 offset:3072\n"
        "ds_read_b64_tr_b16 v[226:227], v255 offset:3840\n"
        "s_add_u32 s28, s28, s31\n"
        "v_mfma_f32_16x16x32_bf16 v[116:119], a[244:247], a[188:191], 0\n"
        "buffer_load_dwordx4 v234, s[52:55], s61 offen offset:2048 lds\n"
        "v_mfma_f32_16x16x32_bf16 v[116:119], a[240:243], a[184:187], v[116:119]\n"
        "s_waitcnt lgkmcnt(5)\n"
        "ds_write_b128 v235, v[212:215] offset:0\n"
        "v_mfma_f32_16x16x32_bf16 v[116:119], a[236:239], a[180:183], v[116:119]\n"
        "v_exp_f32 v96, v96\n"
        "v_mfma_f32_16x16x32_bf16 v[116:119], a[232:235], a[176:179], v[116:119]\n"
        "v_exp_f32 v97, v97\n"
        "v_mfma_f32_16x16x32_bf16 v[116:119], a[228:231], a[172:175], v[116:119]\n"
        "v_exp_f32 v98, v98\n"
        "v_mfma_f32_16x16x32_bf16 v[116:119], a[224:227], a[168:171], v[116:119]\n"
        "ds_read_b64_tr_b16 v[88:89], v255 offset:3104\n"
        "ds_read_b64_tr_b16 v[90:91], v255 offset:3872\n"
        "s_add_u32 m0, s65, 9216\n"
        "v_mfma_f32_16x16x32_bf16 v[104:107], v[152:155], v[176:179], v[16:19]\n"
        "ds_read_b128 a[216:219], v241 offset:12288\n"
        "v_cvt_pk_bf16_f32 v64, v96, v97\n"
        "v_mfma_f32_16x16x32_bf16 v[104:107], v[168:171], v[180:183], v[104:107]\n"
        "ds_write_b128 v239, v[216:219] offset:192\n"
        "v_mfma_f32_16x16x32_bf16 v[108:111], v[172:175], v[176:179], v[20:23]\n"
        "buffer_load_dwordx4 v232, s[52:55], s56 offen lds\n"
        "v_mfma_f32_16x16x32_bf16 v[108:111], v[188:191], v[184:187], v[108:111]\n"
        "v_exp_f32 v100, v100\n"
        "v_mfma_f32_16x16x32_bf16 v[120:123], a[224:227], a[192:195], 0\n"
        "v_cvt_pk_bf16_f32 v138, v116, v117\n"
        "v_cvt_pk_bf16_f32 v139, v118, v119\n"
        "v_mfma_f32_16x16x32_bf16 v[120:123], a[228:231], a[196:199], v[120:123]\n"
        "ds_read_b128 a[220:223], v240 offset:12416\n"
        "ds_read_b128 a[224:227], v241 offset:12416\n"
        "v_mfma_f32_16x16x32_bf16 v[120:123], a[232:235], a[200:203], v[120:123]\n"
        "ds_read_b64_tr_b16 v[92:93], v255 offset:3136\n"
        "ds_read_b64_tr_b16 v[94:95], v255 offset:3904\n"
        "v_mfma_f32_16x16x32_bf16 v[120:123], a[236:239], a[204:207], v[120:123]\n"
        "ds_read_b128 a[228:231], v240 offset:12544\n"
        "ds_read_b128 a[232:235], v241 offset:12544\n"
        "v_mfma_f32_16x16x32_bf16 v[120:123], a[240:243], a[208:211], v[120:123]\n"
        "ds_write_b128 v254, v[220:223] offset:0\n"
        "v_mfma_f32_16x16x32_bf16 v[120:123], a[244:247], a[212:215], v[120:123]\n"
        "s_waitcnt lgkmcnt(8)\n"
        "v_exp_f32 v99, v99\n"
        "v_mfma_f32_16x16x32_bf16 v[112:115], a[0:3], a[248:251], 0\n"
        "buffer_load_dwordx4 v233, s[52:55], s56 offen offset:1024 lds\n"
        "v_mfma_f32_16x16x32_bf16 v[112:115], a[4:7], a[216:219], v[112:115]\n"
        "s_waitcnt lgkmcnt(5)\n"
        "v_exp_f32 v101, v101\n"
        "v_mfma_f32_16x16x32_bf16 v[112:115], a[8:11], a[220:223], v[112:115]\n"
        "ds_read_b128 v[202:205], v251 offset:4096\n"
        "v_cvt_pk_bf16_f32 v65, v98, v99\n"
        "v_mfma_f32_16x16x32_bf16 v[112:115], a[12:15], a[224:227], v[112:115]\n"
        "s_waitcnt lgkmcnt(2)\n"
        "ds_read_b128 v[162:165], v252 offset:0\n"
        "v_cvt_pk_bf16_f32 v66, v100, v101\n"
        "v_mfma_f32_16x16x32_bf16 v[112:115], a[16:19], a[228:231], v[112:115]\n"
        "s_waitcnt vmcnt(2)\n"
        "ds_read_b64_tr_b16 v[212:213], v255 offset:6144\n"
        "ds_read_b64_tr_b16 v[214:215], v255 offset:6912\n"
        "v_mfma_f32_16x16x32_bf16 v[112:115], a[20:23], a[232:235], v[112:115]\n"
        "ds_write_b128 v235, v[224:227] offset:3072\n"
        "v_mfma_f32_16x16x32_bf16 v[116:119], a[44:47], a[232:235], 0\n"
        "v_exp_f32 v102, v102\n"
        "v_mfma_f32_16x16x32_bf16 v[116:119], a[40:43], a[228:231], v[116:119]\n"
        "v_exp_f32 v103, v103\n"
        "v_mfma_f32_16x16x32_bf16 v[116:119], a[36:39], a[224:227], v[116:119]\n"
        "buffer_load_dwordx4 v234, s[52:55], s56 offen offset:2048 lds\n"
        "s_add_u32 s35, s47, 786432\n"
        "s_sub_u32 s46, s35, 100663296\n"
        "s_cmp_lt_u32 s46, 786432\n"
        "v_mfma_f32_16x16x32_bf16 v[116:119], a[32:35], a[220:223], v[116:119]\n"
        "s_cselect_b32 s35, s46, s35\n"
        "s_add_u32 s41, s30, s40\n"
        "s_mov_b32 s30, s29\n"
        "s_add_u32 s29, s29, s45\n"
        "v_mfma_f32_16x16x32_bf16 v[116:119], a[28:31], a[216:219], v[116:119]\n"
        "ds_read_b64_tr_b16 v[216:217], v255 offset:6176\n"
        "ds_read_b64_tr_b16 v[218:219], v255 offset:6944\n"
        "v_mfma_f32_16x16x32_bf16 v[116:119], a[24:27], a[248:251], v[116:119]\n"
        "ds_read_b128 v[182:185], v252 offset:512\n"
        "v_cvt_pk_bf16_f32 v67, v102, v103\n"
        "v_cvt_pk_bf16_f32 v146, v120, v121\n"
        "v_cvt_pk_bf16_f32 v147, v122, v123\n"
        "v_mfma_f32_16x16x32_bf16 v[120:123], a[48:51], a[248:251], 0\n"
        "ds_write_b128 v239, v[88:91] offset:3264\n"
        "v_mfma_f32_16x16x32_bf16 v[120:123], a[52:55], a[216:219], v[120:123]\n"
        "v_exp_f32 v104, v104\n"
        "v_mfma_f32_16x16x32_bf16 v[120:123], a[56:59], a[220:223], v[120:123]\n"
        "s_mov_b32 m0, s48\n"
        "v_exp_f32 v105, v105\n"
        "v_mfma_f32_16x16x32_bf16 v[120:123], a[60:63], a[224:227], v[120:123]\n"
        "v_exp_f32 v106, v106\n"
        "v_mfma_f32_16x16x32_bf16 v[120:123], a[64:67], a[228:231], v[120:123]\n"
        "ds_read_b64_tr_b16 v[220:221], v255 offset:6208\n"
        "ds_read_b64_tr_b16 v[222:223], v255 offset:6976\n"
        "v_mfma_f32_16x16x32_bf16 v[120:123], a[68:71], a[232:235], v[120:123]\n"
        "buffer_load_dwordx4 v249, s[12:15], s36 offen offset:0 lds\n"
        "v_cvt_pk_bf16_f32 v196, v112, v113\n"
        "v_cvt_pk_bf16_f32 v197, v114, v115\n"
        "v_mfma_f32_16x16x32_bf16 v[112:115], a[92:95], a[232:235], 0\n"
        "v_cvt_pk_bf16_f32 v72, v104, v105\n"
        "v_cvt_pk_bf16_f32 v198, v116, v117\n"
        "v_mfma_f32_16x16x32_bf16 v[112:115], a[88:91], a[228:231], v[112:115]\n"
        "ds_write_b128 v254, v[92:95] offset:3072\n"
        "v_mfma_f32_16x16x32_bf16 v[112:115], a[84:87], a[224:227], v[112:115]\n"
        "v_exp_f32 v108, v108\n"
        "v_mfma_f32_16x16x32_bf16 v[112:115], a[80:83], a[220:223], v[112:115]\n"
        "v_exp_f32 v107, v107\n"
        "v_mfma_f32_16x16x32_bf16 v[112:115], a[76:79], a[216:219], v[112:115]\n"
        "v_exp_f32 v109, v109\n"
        "v_mfma_f32_16x16x32_bf16 v[112:115], a[72:75], a[248:251], v[112:115]\n"
        "s_waitcnt vmcnt(1)\n"
        "ds_read_b64_tr_b16 v[224:225], v255 offset:9216\n"
        "ds_read_b64_tr_b16 v[226:227], v255 offset:9984\n"
        "v_cvt_pk_bf16_f32 v199, v118, v119\n"
        "s_nop 1\n"
        "v_mfma_f32_16x16x32_bf16 v[96:99], v[152:155], v[196:199], v[32:35]\n"
        "s_waitcnt lgkmcnt(3)\n"
        "v_cvt_pk_bf16_f32 v73, v106, v107\n"
        "v_cvt_pk_bf16_f32 v74, v108, v109\n"
        "v_cvt_pk_bf16_f32 v200, v120, v121\n"
        "v_cvt_pk_bf16_f32 v201, v122, v123\n"
        "s_nop 1\n"
        "v_mfma_f32_16x16x32_bf16 v[96:99], v[168:171], v[200:203], v[96:99]\n"
        "buffer_load_dwordx4 v253, s[12:15], s36 offen offset:1024 lds\n"
        "v_mfma_f32_16x16x32_bf16 v[100:103], v[172:175], v[196:199], v[36:39]\n"
        "ds_write_b128 v235, v[212:215] offset:6144\n"
        "v_mov_b64 v[206:207], v[200:201]\n"
        "s_nop 1\n"
        "v_mfma_f32_16x16x32_bf16 v[100:103], v[188:191], v[204:207], v[100:103]\n"
        "s_cmp_eq_u32 s37, 100663296\n"
        "v_exp_f32 v110, v110\n"
        "v_mfma_f32_16x16x32_bf16 v[116:119], a[96:99], a[248:251], 0\n"
        "s_cselect_b32 s46, 0, s37\n"
        "v_exp_f32 v111, v111\n"
        "v_mfma_f32_16x16x32_bf16 v[116:119], a[100:103], a[216:219], v[116:119]\n"
        "s_add_u32 m0, s41, 0\n"
        "v_cvt_pk_bf16_f32 v75, v110, v111\n"
        "v_cvt_pk_bf16_f32 v192, v112, v113\n"
        "v_mfma_f32_16x16x32_bf16 v[116:119], a[104:107], a[220:223], v[116:119]\n"
        "ds_read_b64_tr_b16 v[88:89], v255 offset:9248\n"
        "ds_read_b64_tr_b16 v[90:91], v255 offset:10016\n"
        "s_add_u32 s38, s46, s43\n"
        "v_mfma_f32_16x16x32_bf16 v[116:119], a[108:111], a[224:227], v[116:119]\n"
        "buffer_load_dwordx4 v242, s[8:11], s47 offen lds\n"
        "v_mfma_f32_16x16x32_bf16 v[116:119], a[112:115], a[228:231], v[116:119]\n"
        "ds_read_b128 v[202:205], v252 offset:4096\n"
        "s_add_u32 s39, s46, s44\n"
        "v_cvt_pk_bf16_f32 v193, v114, v115\n"
        "v_mfma_f32_16x16x32_bf16 v[116:119], a[116:119], a[232:235], v[116:119]\n"
        "ds_write_b128 v239, v[216:219] offset:6336\n"
        "s_add_u32 s37, s37, 6291456\n"
        "v_mfma_f32_16x16x32_bf16 v[120:123], a[140:143], a[232:235], 0\n"
        "v_exp_f32 v96, v96\n"
        "v_mfma_f32_16x16x32_bf16 v[120:123], a[136:139], a[228:231], v[120:123]\n"
        "s_add_u32 m0, s41, 1024\n"
        "v_exp_f32 v97, v97\n"
        "v_mfma_f32_16x16x32_bf16 v[120:123], a[132:135], a[224:227], v[120:123]\n"
        "v_exp_f32 v98, v98\n"
        "v_mfma_f32_16x16x32_bf16 v[120:123], a[128:131], a[220:223], v[120:123]\n"
        "ds_read_b64_tr_b16 v[92:93], v255 offset:9280\n"
        "ds_read_b64_tr_b16 v[94:95], v255 offset:10048\n"
        "s_mov_b64 s[58:59], s[18:19]\n"
        "v_mfma_f32_16x16x32_bf16 v[120:123], a[124:127], a[216:219], v[120:123]\n"
        "buffer_load_dwordx4 v243, s[8:11], s47 offen lds\n"
        "v_mfma_f32_16x16x32_bf16 v[120:123], a[120:123], a[248:251], v[120:123]\n"
        "s_mov_b64 s[62:63], s[18:19]\n"
        "v_cvt_pk_bf16_f32 v80, v96, v97\n"
        "v_cvt_pk_bf16_f32 v194, v116, v117\n"
        "v_mfma_f32_16x16x32_bf16 v[112:115], a[248:251], a[144:147], 0\n"
        "ds_write_b128 v254, v[220:223] offset:6144\n"
        "v_mfma_f32_16x16x32_bf16 v[112:115], a[216:219], a[148:151], v[112:115]\n"
        "v_exp_f32 v100, v100\n"
        "v_mfma_f32_16x16x32_bf16 v[112:115], a[220:223], a[152:155], v[112:115]\n"
        "s_add_u32 m0, s41, 2048\n"
        "v_cvt_pk_bf16_f32 v195, v118, v119\n"
        "v_cvt_pk_bf16_f32 v208, v120, v121\n"
        "v_mfma_f32_16x16x32_bf16 v[112:115], a[224:227], a[156:159], v[112:115]\n"
        "v_exp_f32 v99, v99\n"
        "v_mfma_f32_16x16x32_bf16 v[112:115], a[228:231], a[160:163], v[112:115]\n"
        "buffer_load_dwordx4 v244, s[8:11], s47 offen lds\n"
        "v_mfma_f32_16x16x32_bf16 v[112:115], a[232:235], a[164:167], v[112:115]\n"
        "ds_read_b128 a[236:239], v240 offset:13824\n"
        "v_cvt_pk_bf16_f32 v81, v98, v99\n"
        "v_mfma_f32_16x16x32_bf16 v[104:107], v[192:195], v[156:159], v[8:11]\n"
        "ds_read_b128 a[240:243], v241 offset:13824\n"
        "v_cvt_pk_bf16_f32 v209, v122, v123\n"
        "s_nop 1\n"
        "v_mfma_f32_16x16x32_bf16 v[104:107], v[208:211], v[160:163], v[104:107]\n"
        "s_waitcnt lgkmcnt(3)\n"
        "ds_write_b128 v235, v[224:227] offset:9216\n"
        "v_mfma_f32_16x16x32_bf16 v[116:119], a[232:235], a[188:191], 0\n"
        "v_exp_f32 v101, v101\n"
        "v_mfma_f32_16x16x32_bf16 v[116:119], a[228:231], a[184:187], v[116:119]\n"
        "s_add_u32 m0, s41, 12288\n"
        "v_exp_f32 v102, v102\n"
        "v_mfma_f32_16x16x32_bf16 v[116:119], a[224:227], a[180:183], v[116:119]\n"
        "v_cvt_pk_bf16_f32 v82, v100, v101\n"
        "v_cvt_pk_bf16_f32 v132, v112, v113\n"
        "v_mfma_f32_16x16x32_bf16 v[116:119], a[220:223], a[176:179], v[116:119]\n"
        "buffer_load_dwordx4 v242, s[8:11], s35 offen lds\n"
        "v_mfma_f32_16x16x32_bf16 v[116:119], a[216:219], a[172:175], v[116:119]\n"
        "ds_read_b128 a[244:247], v240 offset:13952\n"
        "v_cvt_pk_bf16_f32 v133, v114, v115\n"
        "v_mfma_f32_16x16x32_bf16 v[116:119], a[248:251], a[168:171], v[116:119]\n"
        "v_exp_f32 v103, v103\n"
        "v_mfma_f32_16x16x32_bf16 v[108:111], v[192:195], v[176:179], v[24:27]\n"
        "ds_write_b128 v239, v[88:91] offset:9408\n"
        "v_mfma_f32_16x16x32_bf16 v[108:111], v[208:211], v[180:183], v[108:111]\n"
        "v_cvt_pk_bf16_f32 v83, v102, v103\n"
        "v_add_u32 v245, s45, v245\n"
        "v_mfma_f32_16x16x32_bf16 v[120:123], a[248:251], a[192:195], 0\n"
        "s_add_u32 m0, s41, 13312\n"
        "v_exp_f32 v104, v104\n"
        "v_mfma_f32_16x16x32_bf16 v[120:123], a[216:219], a[196:199], v[120:123]\n"
        "v_cvt_pk_bf16_f32 v140, v116, v117\n"
        "v_cvt_pk_bf16_f32 v141, v118, v119\n"
        "v_mfma_f32_16x16x32_bf16 v[120:123], a[220:223], a[200:203], v[120:123]\n"
        "ds_read_b128 a[248:251], v241 offset:13952\n"
        "ds_read_b128 a[216:219], v240 offset:14080\n"
        "v_mfma_f32_16x16x32_bf16 v[120:123], a[224:227], a[204:207], v[120:123]\n"
        "buffer_load_dwordx4 v243, s[8:11], s35 offen lds\n"
        "v_mfma_f32_16x16x32_bf16 v[120:123], a[228:231], a[208:211], v[120:123]\n"
        "ds_read_b128 a[220:223], v241 offset:14080\n"
        "v_bfe_u32 v238, v250, 3, 4\n"
        "v_mfma_f32_16x16x32_bf16 v[120:123], a[232:235], a[212:215], v[120:123]\n"
        "ds_write_b128 v254, v[92:95] offset:9216\n"
        "v_mfma_f32_16x16x32_bf16 v[96:99], v[192:195], v[196:199], v[40:43]\n"
        "v_exp_f32 v105, v105\n"
        "v_mfma_f32_16x16x32_bf16 v[96:99], v[208:211], v[200:203], v[96:99]\n"
        "s_waitcnt lgkmcnt(5)\n"
        "s_add_u32 m0, s41, 14336\n"
        "v_exp_f32 v106, v106\n"
        "v_mfma_f32_16x16x32_bf16 v[112:115], a[72:75], a[236:239], 0\n"
        "v_cvt_pk_bf16_f32 v68, v104, v105\n"
        "v_cvt_pk_bf16_f32 v148, v120, v121\n"
        "v_mfma_f32_16x16x32_bf16 v[112:115], a[76:79], a[240:243], v[112:115]\n"
        "buffer_load_dwordx4 v244, s[8:11], s35 offen lds\n"
        "v_mfma_f32_16x16x32_bf16 v[112:115], a[80:83], a[244:247], v[112:115]\n"
        "s_waitcnt lgkmcnt(1)\n"
        "v_exp_f32 v107, v107\n"
        "v_mfma_f32_16x16x32_bf16 v[112:115], a[84:87], a[248:251], v[112:115]\n"
        "v_cvt_pk_bf16_f32 v69, v106, v107\n"
        "v_cvt_pk_bf16_f32 v149, v122, v123\n"
        "v_mfma_f32_16x16x32_bf16 v[112:115], a[88:91], a[216:219], v[112:115]\n"
        "v_exp_f32 v108, v108\n"
        "v_mfma_f32_16x16x32_bf16 v[112:115], a[92:95], a[220:223], v[112:115]\n"
        "v_exp_f32 v109, v109\n"
        "v_mfma_f32_16x16x32_bf16 v[116:119], a[116:119], a[220:223], 0\n"
        "v_exp_f32 v110, v110\n"
        "v_mfma_f32_16x16x32_bf16 v[116:119], a[112:115], a[216:219], v[116:119]\n"
        "v_cvt_pk_bf16_f32 v76, v108, v109\n"
        "v_and_b32 v254, 32, v250\n"
        "v_mfma_f32_16x16x32_bf16 v[116:119], a[108:111], a[248:251], v[116:119]\n"
        "v_exp_f32 v111, v111\n"
        "v_mfma_f32_16x16x32_bf16 v[116:119], a[104:107], a[244:247], v[116:119]\n"
        "s_add_u32 s56, s16, s38\n"
        "s_addc_u32 s57, s17, 0\n"
        "v_cvt_pk_bf16_f32 v212, v112, v113\n"
        "v_mfma_f32_16x16x32_bf16 v[116:119], a[100:103], a[240:243], v[116:119]\n"
        "s_add_u32 s60, s16, s39\n"
        "s_addc_u32 s61, s17, 0\n"
        "v_cvt_pk_bf16_f32 v213, v114, v115\n"
        "v_mfma_f32_16x16x32_bf16 v[116:119], a[96:99], a[236:239], v[116:119]\n"
        "v_cvt_pk_bf16_f32 v77, v110, v111\n"
        "v_mad_i32_i24 v238, v238, -4, v250\n"
        "v_mfma_f32_16x16x32_bf16 v[120:123], a[120:123], a[236:239], 0\n"
        "v_exp_f32 v96, v96\n"
        "v_mfma_f32_16x16x32_bf16 v[120:123], a[124:127], a[240:243], v[120:123]\n"
        "s_add_u32 s47, s47, 6291456\n"
        "v_exp_f32 v97, v97\n"
        "v_mfma_f32_16x16x32_bf16 v[120:123], a[128:131], a[244:247], v[120:123]\n"
        "s_add_u32 s36, s36, 2097152\n"
        "v_cvt_pk_bf16_f32 v214, v116, v117\n"
        "v_cvt_pk_bf16_f32 v215, v118, v119\n"
        "v_mfma_f32_16x16x32_bf16 v[120:123], a[132:135], a[248:251], v[120:123]\n"
        "s_xor_b32 s48, s48, 8192\n"
        "v_exp_f32 v98, v98\n"
        "v_mfma_f32_16x16x32_bf16 v[120:123], a[136:139], a[216:219], v[120:123]\n"
        "s_waitcnt lgkmcnt(0)\n"
        "s_barrier\n"
        "v_cvt_pk_bf16_f32 v84, v96, v97\n"
        "v_mad_i32_i24 v254, v254, -12, v248\n"
        "v_mfma_f32_16x16x32_bf16 v[120:123], a[140:143], a[220:223], v[120:123]\n"
        "v_exp_f32 v99, v99\n"
        "v_mfma_f32_16x16x32_bf16 v[112:115], a[220:223], a[164:167], 0\n"
        "v_cvt_pk_bf16_f32 v85, v98, v99\n"
        "v_add_u32 v254, 1728, v254\n"
        "v_mfma_f32_16x16x32_bf16 v[112:115], a[216:219], a[160:163], v[112:115]\n"
        "v_add_u32 v238, s49, v238\n"
        "v_add_u32 v240, s31, v240\n"
        "v_mfma_f32_16x16x32_bf16 v[112:115], a[248:251], a[156:159], v[112:115]\n"
        "v_cvt_pk_bf16_f32 v230, v120, v121\n"
        "v_cvt_pk_bf16_f32 v231, v122, v123\n"
        "v_mfma_f32_16x16x32_bf16 v[112:115], a[244:247], a[152:155], v[112:115]\n"
        "v_add_u32 v241, s31, v241\n"
        "v_mfma_f32_16x16x32_bf16 v[112:115], a[240:243], a[148:151], v[112:115]\n"
        "v_mfma_f32_16x16x32_bf16 v[112:115], a[236:239], a[144:147], v[112:115]\n"
        "s_nop 2\n"
        "v_mfma_f32_16x16x32_bf16 v[100:103], v[212:215], v[156:159], v[12:15]\n"
        "s_nop 2\n"
        "v_mfma_f32_16x16x32_bf16 v[100:103], v[228:231], v[164:167], v[100:103]\n"
        "v_cvt_pk_bf16_f32 v134, v112, v113\n"
        "v_cvt_pk_bf16_f32 v135, v114, v115\n"
        "v_mfma_f32_16x16x32_bf16 v[116:119], a[236:239], a[168:171], 0\n"
        "s_waitcnt lgkmcnt(0)\n"
        "v_mfma_f32_16x16x32_bf16 v[116:119], a[240:243], a[172:175], v[116:119]\n"
        "s_bitcmp1_b32 5, s42\n"
        "v_mfma_f32_16x16x32_bf16 v[116:119], a[244:247], a[176:179], v[116:119]\n"
        "s_cbranch_scc1 12f\n"
        "11:\n"
        "10:\n"
        "v_mfma_f32_16x16x32_bf16 v[116:119], a[248:251], a[180:183], v[116:119]\n"
        "v_mfma_f32_16x16x32_bf16 v[116:119], a[216:219], a[184:187], v[116:119]\n"
        "s_nop 0\n"
        "v_exp_f32 v100, v100\n"
        "v_mfma_f32_16x16x32_bf16 v[116:119], a[220:223], a[188:191], v[116:119]\n"
        "v_exp_f32 v101, v101\n"
        "v_mfma_f32_16x16x32_bf16 v[104:107], v[212:215], v[176:179], v[28:31]\n"
        "v_exp_f32 v102, v102\n"
        "v_mfma_f32_16x16x32_bf16 v[104:107], v[228:231], v[184:187], v[104:107]\n"
        "v_exp_f32 v103, v103\n"
        "v_mfma_f32_16x16x32_bf16 v[120:123], a[220:223], a[212:215], 0\n"
        "v_cvt_pk_bf16_f32 v70, v100, v101\n"
        "v_cvt_pk_bf16_f32 v71, v102, v103\n"
        "v_mfma_f32_16x16x32_bf16 v[120:123], a[216:219], a[208:211], v[120:123]\n"
        "v_cvt_pk_bf16_f32 v142, v116, v117\n"
        "v_cvt_pk_bf16_f32 v143, v118, v119\n"
        "v_mfma_f32_16x16x32_bf16 v[120:123], a[248:251], a[204:207], v[120:123]\n"
        "v_mov_b64 v[96:97], 0\n"
        "v_mfma_f32_16x16x32_bf16 v[120:123], a[244:247], a[200:203], v[120:123]\n"
        "v_exp_f32 v104, v104\n"
        "v_mfma_f32_16x16x32_bf16 v[120:123], a[240:243], a[196:199], v[120:123]\n"
        "v_exp_f32 v105, v105\n"
        "v_mfma_f32_16x16x32_bf16 v[120:123], a[236:239], a[192:195], v[120:123]\n"
        "v_exp_f32 v106, v106\n"
        "v_mfma_f32_16x16x32_bf16 v[108:111], v[212:215], v[196:199], v[44:47]\n"
        "v_exp_f32 v107, v107\n"
        "v_mfma_f32_16x16x32_bf16 v[108:111], v[228:231], v[204:207], v[108:111]\n"
        "v_cvt_pk_bf16_f32 v78, v104, v105\n"
        "v_cvt_pk_bf16_f32 v79, v106, v107\n"
        "v_mfma_f32_16x16x32_bf16 v[112:115], a[0:3], a[236:239], 0\n"
        "s_nop 0\n"
        "v_cvt_pk_bf16_f32 v150, v120, v121\n"
        "v_cvt_pk_bf16_f32 v151, v122, v123\n"
        "v_mfma_f32_16x16x32_bf16 v[112:115], a[4:7], a[240:243], v[112:115]\n"
        "v_mfma_f32_16x16x32_bf16 v[112:115], a[8:11], a[244:247], v[112:115]\n"
        "v_exp_f32 v108, v108\n"
        "v_mfma_f32_16x16x32_bf16 v[112:115], a[12:15], a[248:251], v[112:115]\n"
        "v_exp_f32 v109, v109\n"
        "v_mfma_f32_16x16x32_bf16 v[112:115], a[16:19], a[216:219], v[112:115]\n"
        "v_exp_f32 v110, v110\n"
        "v_mfma_f32_16x16x32_bf16 v[112:115], a[20:23], a[220:223], v[112:115]\n"
        "v_exp_f32 v111, v111\n"
        "v_smfmac_f32_16x16x64_bf16 v[96:99], a[252:255], v[64:71], v237\n"
        "v_cvt_pk_bf16_f32 v86, v108, v109\n"
        "v_mfma_f32_16x16x32_bf16 v[100:103], v[132:135], v[68:71], 0\n"
        "v_mov_b64 v[124:125], 0\n"
        "v_cvt_pk_bf16_f32 v87, v110, v111\n"
        "v_mfma_f32_16x16x32_bf16 v[100:103], v[128:131], v[64:67], v[100:103]\n"
        "s_nop 0\n"
        "v_cvt_pk_bf16_f32 v216, v112, v113\n"
        "v_cvt_pk_bf16_f32 v217, v114, v115\n"
        "v_mfma_f32_16x16x32_bf16 v[104:107], v[136:139], v[64:67], 0\n"
        "ds_read_b128 v[222:225], v251 offset:4608\n"
        "v_add_f32 v97, v96, v97\n"
        "v_mfma_f32_16x16x32_bf16 v[104:107], v[140:143], v[68:71], v[104:107]\n"
        "v_rcp_f32 v96, v97\n"
        "v_mfma_f32_16x16x32_bf16 v[108:111], v[148:151], v[68:71], 0\n"
        "v_mul_f32 v100, v96, v100\n"
        "v_mul_f32 v101, v96, v101\n"
        "v_mfma_f32_16x16x32_bf16 v[108:111], v[144:147], v[64:67], v[108:111]\n"
        "v_mul_f32 v102, v96, v102\n"
        "v_mul_f32 v103, v96, v103\n"
        "v_mfma_f32_16x16x32_bf16 v[116:119], a[44:47], a[220:223], 0\n"
        "v_cvt_pk_bf16_f32 v100, v100, v101\n"
        "v_cvt_pk_bf16_f32 v101, v102, v103\n"
        "v_mfma_f32_16x16x32_bf16 v[116:119], a[40:43], a[216:219], v[116:119]\n"
        "v_mul_f32 v104, v96, v104\n"
        "v_mul_f32 v105, v96, v105\n"
        "v_mfma_f32_16x16x32_bf16 v[116:119], a[36:39], a[248:251], v[116:119]\n"
        "v_mul_f32 v106, v96, v106\n"
        "v_mul_f32 v107, v96, v107\n"
        "v_mfma_f32_16x16x32_bf16 v[116:119], a[32:35], a[244:247], v[116:119]\n"
        "v_cvt_pk_bf16_f32 v102, v104, v105\n"
        "v_cvt_pk_bf16_f32 v103, v106, v107\n"
        "v_mfma_f32_16x16x32_bf16 v[116:119], a[28:31], a[240:243], v[116:119]\n"
        "v_mul_f32 v108, v96, v108\n"
        "v_mul_f32 v109, v96, v109\n"
        "v_mfma_f32_16x16x32_bf16 v[116:119], a[24:27], a[236:239], v[116:119]\n"
        "v_mul_f32 v110, v96, v110\n"
        "v_mul_f32 v111, v96, v111\n"
        "v_smfmac_f32_16x16x64_bf16 v[124:127], a[252:255], v[72:79], v237\n"
        "v_cvt_pk_bf16_f32 v108, v108, v109\n"
        "v_mfma_f32_16x16x32_bf16 v[64:67], v[132:135], v[76:79], 0\n"
        "ds_write_b32 v245, v108 offset:4096\n"
        "v_mfma_f32_16x16x32_bf16 v[64:67], v[128:131], v[72:75], v[64:67]\n"
        "ds_read_b128 v[156:159], v247 offset:0\n"
        "v_cvt_pk_bf16_f32 v218, v116, v117\n"
        "v_mfma_f32_16x16x32_bf16 v[68:71], v[136:139], v[72:75], 0\n"
        "ds_read_b128 v[160:163], v247 offset:4096\n"
        "v_cvt_pk_bf16_f32 v219, v118, v119\n"
        "v_mfma_f32_16x16x32_bf16 v[68:71], v[140:143], v[76:79], v[68:71]\n"
        "ds_read_b128 v[164:167], v247 offset:4352\n"
        "v_cvt_pk_bf16_f32 v109, v110, v111\n"
        "v_mfma_f32_16x16x32_bf16 v[120:123], v[148:151], v[76:79], 0\n"
        "ds_write_b32 v245, v109 offset:5120\n"
        "v_mfma_f32_16x16x32_bf16 v[120:123], v[144:147], v[72:75], v[120:123]\n"
        "v_max_u32 v236, v96, v97\n"
        "v_mov_b64 v[116:117], 0\n"
        "v_mfma_f32_16x16x32_bf16 v[112:115], a[48:51], a[236:239], 0\n"
        "v_add_f32 v125, v124, v125\n"
        "v_mfma_f32_16x16x32_bf16 v[112:115], a[52:55], a[240:243], v[112:115]\n"
        "v_rcp_f32 v124, v125\n"
        "v_mfma_f32_16x16x32_bf16 v[112:115], a[56:59], a[244:247], v[112:115]\n"
        "v_mul_f32 v64, v124, v64\n"
        "v_mul_f32 v65, v124, v65\n"
        "v_mfma_f32_16x16x32_bf16 v[112:115], a[60:63], a[248:251], v[112:115]\n"
        "v_mul_f32 v66, v124, v66\n"
        "v_mul_f32 v67, v124, v67\n"
        "v_mfma_f32_16x16x32_bf16 v[112:115], a[64:67], a[216:219], v[112:115]\n"
        "v_cvt_pk_bf16_f32 v64, v64, v65\n"
        "v_cvt_pk_bf16_f32 v65, v66, v67\n"
        "v_mfma_f32_16x16x32_bf16 v[112:115], a[68:71], a[220:223], v[112:115]\n"
        "ds_read_b128 a[216:219], v248 offset:0\n"
        "v_mul_f32 v68, v124, v68\n"
        "v_smfmac_f32_16x16x64_bf16 v[116:119], a[252:255], v[80:87], v237\n"
        "ds_read_b128 a[220:223], v248 offset:3072\n"
        "v_mfma_f32_16x16x32_bf16 v[72:75], v[132:135], v[84:87], 0\n"
        "v_mul_f32 v69, v124, v69\n"
        "v_mul_f32 v70, v124, v70\n"
        "v_mfma_f32_16x16x32_bf16 v[72:75], v[128:131], v[80:83], v[72:75]\n"
        "v_cvt_pk_bf16_f32 v220, v112, v113\n"
        "v_cvt_pk_bf16_f32 v221, v114, v115\n"
        "v_mfma_f32_16x16x32_bf16 v[76:79], v[136:139], v[80:83], 0\n"
        "ds_read_b128 a[224:227], v248 offset:32\n"
        "v_mov_b64 v[226:227], v[220:221]\n"
        "v_mfma_f32_16x16x32_bf16 v[76:79], v[140:143], v[84:87], v[76:79]\n"
        "ds_read_b128 a[228:231], v248 offset:3104\n"
        "v_mul_f32 v71, v124, v71\n"
        "v_mfma_f32_16x16x32_bf16 v[96:99], v[148:151], v[84:87], 0\n"
        "v_cvt_pk_bf16_f32 v66, v68, v69\n"
        "v_cvt_pk_bf16_f32 v67, v70, v71\n"
        "v_mfma_f32_16x16x32_bf16 v[96:99], v[144:147], v[80:83], v[96:99]\n"
        "v_mul_f32 v120, v124, v120\n"
        "v_mul_f32 v121, v124, v121\n"
        "v_mfma_f32_16x16x32_bf16 v[104:107], v[152:155], v[216:219], v[48:51]\n"
        "s_waitcnt lgkmcnt(2)\n"
        "ds_read_b128 v[152:155], v247 offset:256\n"
        "v_mul_f32 v122, v124, v122\n"
        "v_mfma_f32_16x16x32_bf16 v[104:107], v[168:171], v[220:223], v[104:107]\n"
        "v_mul_f32 v123, v124, v123\n"
        "v_cvt_pk_bf16_f32 v120, v120, v121\n"
        "v_mfma_f32_16x16x32_bf16 v[108:111], v[172:175], v[216:219], v[52:55]\n"
        "ds_write_b32 v245, v120 offset:4352\n"
        "v_mfma_f32_16x16x32_bf16 v[108:111], v[188:191], v[224:227], v[108:111]\n"
        "ds_read_b128 v[222:225], v252 offset:4608\n"
        "v_cvt_pk_bf16_f32 v121, v122, v123\n"
        "v_mfma_f32_16x16x32_bf16 v[176:179], v[156:159], a[216:219], 0\n"
        "ds_write_b32 v245, v121 offset:5376\n"
        "v_mfma_f32_16x16x32_bf16 v[176:179], v[160:163], a[220:223], v[176:179]\n"
        "ds_read_b128 a[232:235], v254 offset:0\n"
        "v_max3_u32 v236, v236, v124, v125\n"
        "v_mfma_f32_16x16x32_bf16 v[196:199], v[164:167], a[220:223], 0\n"
        "s_waitcnt lgkmcnt(4)\n"
        "ds_read_b128 a[220:223], v254 offset:3072\n"
        "v_add_f32 v117, v116, v117\n"
        "v_mfma_f32_16x16x32_bf16 v[196:199], v[152:155], a[216:219], v[196:199]\n"
        "v_rcp_f32 v116, v117\n"
        "v_mfma_f32_16x16x32_bf16 v[200:203], v[152:155], a[224:227], 0\n"
        "ds_read_b128 a[216:219], v254 offset:32\n"
        "v_mul_f32 v72, v116, v72\n"
        "v_mfma_f32_16x16x32_bf16 v[200:203], v[164:167], a[228:231], v[200:203]\n"
        "v_exp_f32 v104, v104\n"
        "v_mfma_f32_16x16x32_bf16 v[180:183], v[160:163], a[228:231], 0\n"
        "ds_read_b128 a[228:231], v254 offset:3104\n"
        "v_mul_f32 v73, v116, v73\n"
        "v_mfma_f32_16x16x32_bf16 v[180:183], v[156:159], a[224:227], v[180:183]\n"
        "ds_read_b128 a[236:239], v247 offset:8192\n"
        "v_mul_f32 v74, v116, v74\n"
        "v_mfma_f32_16x16x32_bf16 v[112:115], v[192:195], v[216:219], v[56:59]\n"
        "s_waitcnt lgkmcnt(6)\n"
        "ds_read_b128 a[224:227], v248 offset:16\n"
        "v_mul_f32 v75, v116, v75\n"
        "v_mfma_f32_16x16x32_bf16 v[112:115], v[208:211], v[220:223], v[112:115]\n"
        "ds_read_b128 a[240:243], v247 offset:12288\n"
        "v_cvt_pk_bf16_f32 v72, v72, v73\n"
        "v_mfma_f32_16x16x32_bf16 v[68:71], v[212:215], v[216:219], v[60:63]\n"
        "ds_read_b128 a[244:247], v247 offset:12544\n"
        "v_cvt_pk_bf16_f32 v73, v74, v75\n"
        "v_mfma_f32_16x16x32_bf16 v[68:71], v[228:231], v[224:227], v[68:71]\n"
        "s_waitcnt lgkmcnt(4)\n"
        "ds_read_b128 a[248:251], v247 offset:8448\n"
        "v_mul_f32 v76, v116, v76\n"
        "v_mfma_f32_16x16x32_bf16 v[184:187], v[156:159], a[232:235], 0\n"
        "v_mul_f32 v77, v116, v77\n"
        "v_mul_f32 v78, v116, v78\n"
        "v_mfma_f32_16x16x32_bf16 v[184:187], v[160:163], a[220:223], v[184:187]\n"
        "v_mul_f32 v79, v116, v79\n"
        "v_cvt_pk_bf16_f32 v74, v76, v77\n"
        "v_mfma_f32_16x16x32_bf16 v[204:207], v[164:167], a[220:223], 0\n"
        "ds_read_b128 a[220:223], v248 offset:3088\n"
        "v_cvt_pk_bf16_f32 v75, v78, v79\n"
        "v_mfma_f32_16x16x32_bf16 v[204:207], v[152:155], a[232:235], v[204:207]\n"
        "ds_read_b128 a[232:235], v254 offset:16\n"
        "v_mul_f32 v96, v116, v96\n"
        "v_mfma_f32_16x16x32_bf16 v[192:195], v[152:155], a[216:219], 0\n"
        "v_mul_f32 v97, v116, v97\n"
        "v_mul_f32 v98, v116, v98\n"
        "v_mfma_f32_16x16x32_bf16 v[192:195], v[164:167], a[228:231], v[192:195]\n"
        "v_mul_f32 v99, v116, v99\n"
        "v_cvt_pk_bf16_f32 v96, v96, v97\n"
        "v_mfma_f32_16x16x32_bf16 v[172:175], v[160:163], a[228:231], 0\n"
        "ds_read_b128 a[228:231], v254 offset:3088\n"
        "v_cvt_pk_bf16_f32 v97, v98, v99\n"
        "v_mfma_f32_16x16x32_bf16 v[172:175], v[156:159], a[216:219], v[172:175]\n"
        "s_waitcnt lgkmcnt(2)\n"
        "ds_read_b128 a[216:219], v248 offset:6144\n"
        "v_max3_u32 v236, v236, v116, v117\n"
        "v_mfma_f32_16x16x32_bf16 v[212:215], v[156:159], a[224:227], 0\n"
        "v_exp_f32 v105, v105\n"
        "v_mfma_f32_16x16x32_bf16 v[212:215], v[160:163], a[220:223], v[212:215]\n"
        "v_exp_f32 v106, v106\n"
        "v_mfma_f32_16x16x32_bf16 v[216:219], v[164:167], a[220:223], 0\n"
        "ds_read_b128 a[220:223], v248 offset:9216\n"
        "v_cvt_pk_bf16_f32 v88, v104, v105\n"
        "v_mfma_f32_16x16x32_bf16 v[216:219], v[152:155], a[224:227], v[216:219]\n"
        "s_waitcnt lgkmcnt(2)\n"
        "ds_read_b128 a[224:227], v248 offset:6176\n"
        "v_mfma_f32_16x16x32_bf16 v[224:227], v[152:155], a[232:235], 0\n"
        "v_exp_f32 v108, v108\n"
        "v_mfma_f32_16x16x32_bf16 v[224:227], v[164:167], a[228:231], v[224:227]\n"
        "v_exp_f32 v107, v107\n"
        "v_mfma_f32_16x16x32_bf16 v[220:223], v[160:163], a[228:231], 0\n"
        "ds_read_b128 a[228:231], v248 offset:9248\n"
        "v_cvt_pk_bf16_f32 v89, v106, v107\n"
        "v_mfma_f32_16x16x32_bf16 v[220:223], v[156:159], a[232:235], v[220:223]\n"
        "s_waitcnt lgkmcnt(2)\n"
        "ds_read_b128 a[232:235], v254 offset:6144\n"
        "v_mfma_f32_16x16x32_bf16 v[176:179], a[236:239], a[216:219], v[176:179]\n"
        "v_exp_f32 v109, v109\n"
        "v_mfma_f32_16x16x32_bf16 v[176:179], a[240:243], a[220:223], v[176:179]\n"
        "v_exp_f32 v110, v110\n"
        "v_mfma_f32_16x16x32_bf16 v[196:199], a[244:247], a[220:223], v[196:199]\n"
        "ds_read_b128 a[220:223], v254 offset:9216\n"
        "v_cvt_pk_bf16_f32 v90, v108, v109\n"
        "v_mfma_f32_16x16x32_bf16 v[196:199], a[248:251], a[216:219], v[196:199]\n"
        "s_waitcnt lgkmcnt(2)\n"
        "ds_read_b128 a[216:219], v254 offset:6176\n"
        "v_mfma_f32_16x16x32_bf16 v[200:203], a[248:251], a[224:227], v[200:203]\n"
        "ds_read_b128 v[156:159], v247 offset:16384\n"
        "v_mfma_f32_16x16x32_bf16 v[200:203], a[244:247], a[228:231], v[200:203]\n"
        "ds_read_b128 v[160:163], v247 offset:20480\n"
        "v_mfma_f32_16x16x32_bf16 v[180:183], a[240:243], a[228:231], v[180:183]\n"
        "ds_read_b128 a[228:231], v254 offset:9248\n"
        "v_mfma_f32_16x16x32_bf16 v[180:183], a[236:239], a[224:227], v[180:183]\n"
        "s_waitcnt lgkmcnt(4)\n"
        "ds_read_b128 a[224:227], v248 offset:6160\n"
        "v_mfma_f32_16x16x32_bf16 v[184:187], a[236:239], a[232:235], v[184:187]\n"
        "ds_read_b128 v[164:167], v247 offset:20736\n"
        "v_mfma_f32_16x16x32_bf16 v[184:187], a[240:243], a[220:223], v[184:187]\n"
        "ds_read_b128 v[152:155], v247 offset:16640\n"
        "v_mfma_f32_16x16x32_bf16 v[204:207], a[244:247], a[220:223], v[204:207]\n"
        "ds_read_b128 a[220:223], v248 offset:9232\n"
        "v_mfma_f32_16x16x32_bf16 v[204:207], a[248:251], a[232:235], v[204:207]\n"
        "s_waitcnt lgkmcnt(4)\n"
        "ds_read_b128 a[232:235], v254 offset:6160\n"
        "v_mfma_f32_16x16x32_bf16 v[192:195], a[248:251], a[216:219], v[192:195]\n"
        "v_exp_f32 v111, v111\n"
        "v_mfma_f32_16x16x32_bf16 v[192:195], a[244:247], a[228:231], v[192:195]\n"
        "v_cvt_pk_bf16_f32 v91, v110, v111\n"
        "v_mfma_f32_16x16x32_bf16 v[172:175], a[240:243], a[228:231], v[172:175]\n"
        "ds_read_b128 a[228:231], v254 offset:9232\n"
        "v_mfma_f32_16x16x32_bf16 v[172:175], a[236:239], a[216:219], v[172:175]\n"
        "s_waitcnt lgkmcnt(2)\n"
        "ds_read_b128 a[216:219], v248 offset:12288\n"
        "v_mfma_f32_16x16x32_bf16 v[212:215], a[236:239], a[224:227], v[212:215]\n"
        "v_exp_f32 v112, v112\n"
        "v_mfma_f32_16x16x32_bf16 v[212:215], a[240:243], a[220:223], v[212:215]\n"
        "v_exp_f32 v113, v113\n"
        "v_mfma_f32_16x16x32_bf16 v[216:219], a[244:247], a[220:223], v[216:219]\n"
        "ds_read_b128 a[220:223], v248 offset:15360\n"
        "v_cvt_pk_bf16_f32 v92, v112, v113\n"
        "v_mfma_f32_16x16x32_bf16 v[216:219], a[248:251], a[224:227], v[216:219]\n"
        "s_waitcnt lgkmcnt(2)\n"
        "ds_read_b128 a[224:227], v248 offset:12320\n"
        "v_mfma_f32_16x16x32_bf16 v[224:227], a[248:251], a[232:235], v[224:227]\n"
        ".long 0xbec200ff\n"
        "44:\n"
        "s_branch 60b\n"
        "v_exp_f32 v114, v114\n"
        "v_mfma_f32_16x16x32_bf16 v[224:227], a[244:247], a[228:231], v[224:227]\n"
        "v_exp_f32 v68, v68\n"
        "v_mfma_f32_16x16x32_bf16 v[220:223], a[240:243], a[228:231], v[220:223]\n"
        "ds_read_b128 a[228:231], v248 offset:15392\n"
        "v_mfma_f32_16x16x32_bf16 v[220:223], a[236:239], a[232:235], v[220:223]\n"
        "s_waitcnt lgkmcnt(2)\n"
        "ds_read_b128 a[232:235], v254 offset:12288\n"
        "v_mfma_f32_16x16x32_bf16 v[176:179], v[156:159], a[216:219], v[176:179]\n"
        "v_exp_f32 v115, v115\n"
        "v_mfma_f32_16x16x32_bf16 v[176:179], v[160:163], a[220:223], v[176:179]\n"
        "v_exp_f32 v69, v69\n"
        "v_mfma_f32_16x16x32_bf16 v[196:199], v[164:167], a[220:223], v[196:199]\n"
        "ds_read_b128 a[220:223], v254 offset:15360\n"
        "v_cvt_pk_bf16_f32 v93, v114, v115\n"
        "v_mfma_f32_16x16x32_bf16 v[196:199], v[152:155], a[216:219], v[196:199]\n"
        "s_waitcnt lgkmcnt(2)\n"
        "ds_read_b128 a[216:219], v254 offset:12320\n"
        "v_cvt_pk_bf16_f32 v94, v68, v69\n"
        "v_mfma_f32_16x16x32_bf16 v[200:203], v[152:155], a[224:227], v[200:203]\n"
        "v_exp_f32 v70, v70\n"
        "v_mfma_f32_16x16x32_bf16 v[200:203], v[164:167], a[228:231], v[200:203]\n"
        "s_waitcnt vmcnt(0)\n"
        "v_exp_f32 v71, v71\n"
        "v_mfma_f32_16x16x32_bf16 v[180:183], v[160:163], a[228:231], v[180:183]\n"
        "ds_read_b128 a[228:231], v254 offset:15392\n"
        "v_cvt_pk_bf16_f32 v95, v70, v71\n"
        "v_mfma_f32_16x16x32_bf16 v[180:183], v[156:159], a[224:227], v[180:183]\n"
        "s_waitcnt lgkmcnt(2)\n"
        "ds_read_b128 a[224:227], v248 offset:12304\n"
        "v_mfma_f32_16x16x32_bf16 v[184:187], v[156:159], a[232:235], v[184:187]\n"
        "v_mfma_f32_16x16x32_bf16 v[184:187], v[160:163], a[220:223], v[184:187]\n"
        "s_barrier\n"
        "v_mfma_f32_16x16x32_bf16 v[204:207], v[164:167], a[220:223], v[204:207]\n"
        "ds_read_b128 a[220:223], v248 offset:15376\n"
        "v_mfma_f32_16x16x32_bf16 v[204:207], v[152:155], a[232:235], v[204:207]\n"
        "s_waitcnt lgkmcnt(2)\n"
        "ds_read_b128 a[232:235], v254 offset:12304\n"
        "v_mfma_f32_16x16x32_bf16 v[192:195], v[152:155], a[216:219], v[192:195]\n"
        "ds_read_b128 a[236:239], v240 offset:0\n"
        "v_mfma_f32_16x16x32_bf16 v[192:195], v[164:167], a[228:231], v[192:195]\n"
        "ds_read_b128 a[240:243], v241 offset:0\n"
        "v_mfma_f32_16x16x32_bf16 v[172:175], v[160:163], a[228:231], v[172:175]\n"
        "ds_read_b128 a[228:231], v254 offset:15376\n"
        "v_mfma_f32_16x16x32_bf16 v[172:175], v[156:159], a[216:219], v[172:175]\n"
        "s_waitcnt lgkmcnt(4)\n"
        "ds_read_b128 a[244:247], v240 offset:128\n"
        "v_mfma_f32_16x16x32_bf16 v[212:215], v[156:159], a[224:227], v[212:215]\n"
        "ds_read_b128 a[248:251], v241 offset:128\n"
        "v_mfma_f32_16x16x32_bf16 v[212:215], v[160:163], a[220:223], v[212:215]\n"
        "ds_read_b128 a[216:219], v240 offset:256\n"
        "v_mfma_f32_16x16x32_bf16 v[216:219], v[164:167], a[220:223], v[216:219]\n"
        "ds_read_b128 a[220:223], v241 offset:256\n"
        "v_mfma_f32_16x16x32_bf16 v[216:219], v[152:155], a[224:227], v[216:219]\n"
        "s_waitcnt lgkmcnt(4)\n"
        "v_mfma_f32_16x16x32_bf16 v[224:227], v[152:155], a[232:235], v[224:227]\n"
        "v_mfma_f32_16x16x32_bf16 v[224:227], v[164:167], a[228:231], v[224:227]\n"
        "ds_write_b32 v245, v96 offset:4608\n"
        "v_mfma_f32_16x16x32_bf16 v[220:223], v[160:163], a[228:231], v[220:223]\n"
        "v_mfma_f32_16x16x32_bf16 v[220:223], v[156:159], a[232:235], v[220:223]\n"
        "ds_write_b32 v245, v97 offset:5632\n"
        "v_mfma_f32_16x16x32_bf16 v[112:115], a[72:75], a[236:239], 0\n"
        "v_mov_b64 v[124:125], 0\n"
        "v_cvt_pk_bf16_f32 v232, v176, v180\n"
        "v_mfma_f32_16x16x32_bf16 v[112:115], a[76:79], a[240:243], v[112:115]\n"
        "s_waitcnt lgkmcnt(2)\n"
        "v_cvt_pk_bf16_f32 v233, v184, v172\n"
        "v_cvt_pk_bf16_f32 v234, v177, v181\n"
        "v_mfma_f32_16x16x32_bf16 v[112:115], a[80:83], a[244:247], v[112:115]\n"
        "buffer_store_dwordx2 v[232:233], v250, s[56:59], 0 offen offset:0 sc1\n"
        "v_cvt_pk_bf16_f32 v235, v185, v173\n"
        "v_mfma_f32_16x16x32_bf16 v[112:115], a[84:87], a[248:251], v[112:115]\n"
        "v_cvt_pk_bf16_f32 v232, v178, v182\n"
        "v_cvt_pk_bf16_f32 v233, v186, v174\n"
        "v_mfma_f32_16x16x32_bf16 v[112:115], a[88:91], a[216:219], v[112:115]\n"
        "v_mfma_f32_16x16x32_bf16 v[112:115], a[92:95], a[220:223], v[112:115]\n"
        "ds_write_b32 v245, v100 offset:0\n"
        "v_smfmac_f32_16x16x64_bf16 v[124:127], a[252:255], v[88:95], v237\n"
        "s_nop 1\n"
        "v_mfma_f32_16x16x32_bf16 v[80:83], v[132:135], v[92:95], 0\n"
        "buffer_store_dwordx2 v[234:235], v250, s[56:59], 0 offen offset:384 sc1\n"
        "s_nop 0\n"
        "v_mfma_f32_16x16x32_bf16 v[80:83], v[128:131], v[88:91], v[80:83]\n"
        "s_add_u32 s41, s30, s40\n"
        "v_cvt_pk_bf16_f32 v152, v112, v113\n"
        "v_cvt_pk_bf16_f32 v153, v114, v115\n"
        "v_mfma_f32_16x16x32_bf16 v[84:87], v[136:139], v[88:91], 0\n"
        "v_cvt_pk_bf16_f32 v234, v179, v183\n"
        "v_cvt_pk_bf16_f32 v235, v187, v175\n"
        "v_mfma_f32_16x16x32_bf16 v[84:87], v[140:143], v[92:95], v[84:87]\n"
        "v_add_f32 v125, v124, v125\n"
        "v_mfma_f32_16x16x32_bf16 v[96:99], v[148:151], v[92:95], 0\n"
        "buffer_store_dwordx2 v[232:233], v250, s[56:59], 0 offen offset:768 sc1\n"
        "v_mfma_f32_16x16x32_bf16 v[96:99], v[144:147], v[88:91], v[96:99]\n"
        "s_add_u32 s35, s47, 786432\n"
        "v_rcp_f32 v124, v125\n"
        "v_mfma_f32_16x16x32_bf16 v[116:119], a[116:119], a[220:223], 0\n"
        "s_sub_u32 s46, s35, 100663296\n"
        "v_mul_f32 v80, v124, v80\n"
        "v_mul_f32 v81, v124, v81\n"
        "v_mfma_f32_16x16x32_bf16 v[116:119], a[112:115], a[216:219], v[116:119]\n"
        "s_cmp_lt_u32 s46, 786432\n"
        "v_mul_f32 v82, v124, v82\n"
        "v_mul_f32 v83, v124, v83\n"
        "v_mfma_f32_16x16x32_bf16 v[116:119], a[108:111], a[248:251], v[116:119]\n"
        "buffer_store_dwordx2 v[234:235], v250, s[56:59], 0 offen offset:1152 sc1\n"
        "v_cvt_pk_bf16_f32 v80, v80, v81\n"
        "v_mfma_f32_16x16x32_bf16 v[116:119], a[104:107], a[244:247], v[116:119]\n"
        "s_cselect_b32 s35, s46, s35\n"
        "v_cvt_pk_bf16_f32 v81, v82, v83\n"
        "v_cvt_pk_bf16_f32 v232, v196, v200\n"
        "v_mfma_f32_16x16x32_bf16 v[116:119], a[100:103], a[240:243], v[116:119]\n"
        "v_cvt_pk_bf16_f32 v233, v204, v192\n"
        "v_mul_f32 v84, v124, v84\n"
        "v_mfma_f32_16x16x32_bf16 v[116:119], a[96:99], a[236:239], v[116:119]\n"
        "v_mul_f32 v85, v124, v85\n"
        "v_mul_f32 v86, v124, v86\n"
        "v_mfma_f32_16x16x32_bf16 v[120:123], a[120:123], a[236:239], 0\n"
        "ds_write_b32 v245, v101 offset:1024\n"
        "v_mfma_f32_16x16x32_bf16 v[120:123], a[124:127], a[240:243], v[120:123]\n"
        "buffer_store_dwordx2 v[232:233], v250, s[60:63], 0 offen offset:0 sc1\n"
        "v_mul_f32 v87, v124, v87\n"
        "v_mfma_f32_16x16x32_bf16 v[120:123], a[128:131], a[244:247], v[120:123]\n"
        "v_cvt_pk_bf16_f32 v82, v84, v85\n"
        "v_cvt_pk_bf16_f32 v83, v86, v87\n"
        "v_mfma_f32_16x16x32_bf16 v[120:123], a[132:135], a[248:251], v[120:123]\n"
        "v_mul_f32 v96, v124, v96\n"
        "v_mul_f32 v97, v124, v97\n"
        "v_mfma_f32_16x16x32_bf16 v[120:123], a[136:139], a[216:219], v[120:123]\n"
        "v_mul_f32 v98, v124, v98\n"
        "v_mul_f32 v99, v124, v99\n"
        "v_mfma_f32_16x16x32_bf16 v[120:123], a[140:143], a[220:223], v[120:123]\n"
        "v_cvt_pk_bf16_f32 v234, v197, v201\n"
        "v_cvt_pk_bf16_f32 v235, v205, v193\n"
        "v_mfma_f32_16x16x32_bf16 v[112:115], a[20:23], a[220:223], 0\n"
        "buffer_store_dwordx2 v[234:235], v250, s[60:63], 0 offen offset:384 sc1\n"
        "v_cvt_pk_bf16_f32 v154, v116, v117\n"
        "v_mfma_f32_16x16x32_bf16 v[112:115], a[16:19], a[216:219], v[112:115]\n"
        "v_cvt_pk_bf16_f32 v155, v118, v119\n"
        "v_cvt_pk_bf16_f32 v96, v96, v97\n"
        "v_mfma_f32_16x16x32_bf16 v[112:115], a[12:15], a[248:251], v[112:115]\n"
        "ds_write_b32 v245, v96 offset:4864\n"
        "v_mfma_f32_16x16x32_bf16 v[112:115], a[8:11], a[244:247], v[112:115]\n"
        "v_cvt_pk_bf16_f32 v97, v98, v99\n"
        "v_max3_u32 v236, v236, v124, v125\n"
        "v_mfma_f32_16x16x32_bf16 v[112:115], a[4:7], a[240:243], v[112:115]\n"
        "v_cvt_pk_bf16_f32 v232, v198, v202\n"
        "v_cvt_pk_bf16_f32 v233, v206, v194\n"
        "v_mfma_f32_16x16x32_bf16 v[112:115], a[0:3], a[236:239], v[112:115]\n"
        "buffer_store_dwordx2 v[232:233], v250, s[60:63], 0 offen offset:768 sc1\n"
        "v_cmp_lt_u32 vcc, s24, v236\n"
        "v_mfma_f32_16x16x32_bf16 v[116:119], a[24:27], a[236:239], 0\n"
        "v_cvt_pk_bf16_f32 v168, v120, v121\n"
        "v_cvt_pk_bf16_f32 v169, v122, v123\n"
        "v_mfma_f32_16x16x32_bf16 v[116:119], a[28:31], a[240:243], v[116:119]\n"
        "s_cmp_lg_u64 vcc, 0\n"
        "s_addc_u32 s25, s25, s25\n"
        "v_cvt_pk_bf16_f32 v234, v199, v203\n"
        "v_mfma_f32_16x16x32_bf16 v[116:119], a[32:35], a[244:247], v[116:119]\n"
        "v_cvt_pk_bf16_f32 v235, v207, v195\n"
        "v_cvt_pk_bf16_f32 v156, v112, v113\n"
        "v_mfma_f32_16x16x32_bf16 v[116:119], a[36:39], a[248:251], v[116:119]\n"
        "buffer_store_dwordx2 v[234:235], v250, s[60:63], 0 offen offset:1152 sc1\n"
        "v_cvt_pk_bf16_f32 v157, v114, v115\n"
        "v_mfma_f32_16x16x32_bf16 v[116:119], a[40:43], a[216:219], v[116:119]\n"
        "v_cvt_pk_bf16_f32 v232, v212, v220\n"
        "v_cvt_pk_bf16_f32 v234, v213, v221\n"
        "v_mfma_f32_16x16x32_bf16 v[116:119], a[44:47], a[220:223], v[116:119]\n"
        "ds_write_b32 v245, v102 offset:2048\n"
        "v_mfma_f32_16x16x32_bf16 v[120:123], a[68:71], a[220:223], 0\n"
        "v_xor_b32 v251, 0x2000, v251\n"
        "v_xor_b32 v252, 0x2000, v252\n"
        "v_mfma_f32_16x16x32_bf16 v[120:123], a[64:67], a[216:219], v[120:123]\n"
        "buffer_store_dword v232, v238, s[56:59], 0 offen offset:0 sc1\n"
        "v_mfma_f32_16x16x32_bf16 v[120:123], a[60:63], a[248:251], v[120:123]\n"
        "ds_read_b128 a[224:227], v240 offset:1536\n"
        "v_cvt_pk_bf16_f32 v158, v116, v117\n"
        "v_mfma_f32_16x16x32_bf16 v[120:123], a[56:59], a[244:247], v[120:123]\n"
        "ds_read_b128 a[228:231], v241 offset:1536\n"
        "v_cvt_pk_bf16_f32 v159, v118, v119\n"
        "v_mfma_f32_16x16x32_bf16 v[120:123], a[52:55], a[240:243], v[120:123]\n"
        "ds_read_b128 a[232:235], v240 offset:1664\n"
        "v_cvt_pk_bf16_f32 v232, v214, v222\n"
        "v_mfma_f32_16x16x32_bf16 v[120:123], a[48:51], a[236:239], v[120:123]\n"
        "buffer_store_dword v234, v238, s[56:59], 0 offen offset:384 sc1\n"
        "v_mfma_f32_16x16x32_bf16 v[112:115], a[236:239], a[144:147], 0\n"
        "v_add_u32 v247, s45, v247\n"
        "v_cvt_pk_bf16_f32 v234, v215, v223\n"
        "v_mfma_f32_16x16x32_bf16 v[112:115], a[240:243], a[148:151], v[112:115]\n"
        "ds_write_b32 v245, v103 offset:3072\n"
        "v_mfma_f32_16x16x32_bf16 v[112:115], a[244:247], a[152:155], v[112:115]\n"
        "s_nop 0\n"
        "v_cvt_pk_bf16_f32 v160, v120, v121\n"
        "v_cvt_pk_bf16_f32 v161, v122, v123\n"
        "v_mfma_f32_16x16x32_bf16 v[112:115], a[248:251], a[156:159], v[112:115]\n"
        "v_mov_b64 v[166:167], v[160:161]\n"
        "v_mfma_f32_16x16x32_bf16 v[112:115], a[216:219], a[160:163], v[112:115]\n"
        "buffer_store_dword v232, v238, s[56:59], 0 offen offset:768 sc1\n"
        "v_mfma_f32_16x16x32_bf16 v[112:115], a[220:223], a[164:167], v[112:115]\n"
        "ds_write_b32 v245, v97 offset:5888\n"
        "v_mfma_f32_16x16x32_bf16 v[116:119], a[220:223], a[188:191], 0\n"
        "v_cvt_pk_bf16_f32 v232, v216, v224\n"
        "s_nop 0\n"
        "v_mfma_f32_16x16x32_bf16 v[116:119], a[216:219], a[184:187], v[116:119]\n"
        "s_nop 2\n"
        "v_mfma_f32_16x16x32_bf16 v[116:119], a[248:251], a[180:183], v[116:119]\n"
        "buffer_store_dword v234, v238, s[56:59], 0 offen offset:1152 sc1\n"
        "v_mfma_f32_16x16x32_bf16 v[116:119], a[244:247], a[176:179], v[116:119]\n"
        "v_cvt_pk_bf16_f32 v128, v112, v113\n"
        "v_cvt_pk_bf16_f32 v129, v114, v115\n"
        "v_mfma_f32_16x16x32_bf16 v[116:119], a[240:243], a[172:175], v[116:119]\n"
        "ds_write_b32 v245, v64 offset:256\n"
        "v_mfma_f32_16x16x32_bf16 v[116:119], a[236:239], a[168:171], v[116:119]\n"
        "v_cvt_pk_bf16_f32 v234, v217, v225\n"
        "s_nop 0\n"
        "v_mfma_f32_16x16x32_bf16 v[120:123], a[236:239], a[192:195], 0\n"
        "ds_read_b128 a[236:239], v241 offset:1664\n"
        "s_nop 0\n"
        "v_mfma_f32_16x16x32_bf16 v[120:123], a[240:243], a[196:199], v[120:123]\n"
        "buffer_store_dword v232, v238, s[60:63], 0 offen offset:0 sc1\n"
        "v_mfma_f32_16x16x32_bf16 v[120:123], a[244:247], a[200:203], v[120:123]\n"
        "ds_read_b128 a[240:243], v240 offset:1792\n"
        "ds_read_b128 a[244:247], v241 offset:1792\n"
        "v_mfma_f32_16x16x32_bf16 v[120:123], a[248:251], a[204:207], v[120:123]\n"
        "v_cvt_pk_bf16_f32 v136, v116, v117\n"
        "v_cvt_pk_bf16_f32 v137, v118, v119\n"
        "v_mfma_f32_16x16x32_bf16 v[120:123], a[216:219], a[208:211], v[120:123]\n"
        "v_cvt_pk_bf16_f32 v232, v218, v226\n"
        "v_mfma_f32_16x16x32_bf16 v[120:123], a[220:223], a[212:215], v[120:123]\n"
        "s_waitcnt lgkmcnt(4)\n"
        "buffer_store_dword v234, v238, s[60:63], 0 offen offset:384 sc1\n"
        "v_mfma_f32_16x16x32_bf16 v[112:115], a[72:75], a[224:227], 0\n"
        "s_nop 2\n"
        "v_mfma_f32_16x16x32_bf16 v[112:115], a[76:79], a[228:231], v[112:115]\n"
        "v_cvt_pk_bf16_f32 v234, v219, v227\n"
        "s_nop 0\n"
        "v_mfma_f32_16x16x32_bf16 v[112:115], a[80:83], a[232:235], v[112:115]\n"
        "s_waitcnt lgkmcnt(0)\n"
        "v_cvt_pk_bf16_f32 v144, v120, v121\n"
        "v_cvt_pk_bf16_f32 v145, v122, v123\n"
        "v_mfma_f32_16x16x32_bf16 v[112:115], a[84:87], a[236:239], v[112:115]\n"
        "ds_write_b32 v245, v65 offset:1280\n"
        "v_mfma_f32_16x16x32_bf16 v[112:115], a[88:91], a[240:243], v[112:115]\n"
        "buffer_store_dword v232, v238, s[60:63], 0 offen offset:768 sc1\n"
        "v_mfma_f32_16x16x32_bf16 v[112:115], a[92:95], a[244:247], v[112:115]\n"
        "s_nop 2\n"
        "v_mfma_f32_16x16x32_bf16 v[116:119], a[116:119], a[244:247], 0\n"
        "s_nop 2\n"
        "v_mfma_f32_16x16x32_bf16 v[116:119], a[112:115], a[240:243], v[116:119]\n"
        "v_cvt_pk_bf16_f32 v172, v112, v113\n"
        "v_cvt_pk_bf16_f32 v173, v114, v115\n"
        "v_mfma_f32_16x16x32_bf16 v[116:119], a[108:111], a[236:239], v[116:119]\n"
        "buffer_store_dword v234, v238, s[60:63], 0 offen offset:1152 sc1\n"
        "v_mfma_f32_16x16x32_bf16 v[116:119], a[104:107], a[232:235], v[116:119]\n"
        "ds_write_b32 v245, v66 offset:2304\n"
        "v_mfma_f32_16x16x32_bf16 v[116:119], a[100:103], a[228:231], v[116:119]\n"
        "v_mfma_f32_16x16x32_bf16 v[116:119], a[96:99], a[224:227], v[116:119]\n"
        "s_mov_b32 m0, s48\n"
        "s_nop 1\n"
        "v_mfma_f32_16x16x32_bf16 v[120:123], a[120:123], a[224:227], 0\n"
        "ds_read_b128 v[162:165], v251 offset:0\n"
        "s_nop 0\n"
        "v_mfma_f32_16x16x32_bf16 v[120:123], a[124:127], a[228:231], v[120:123]\n"
        "buffer_load_dwordx4 v249, s[12:15], s36 offen offset:0 lds\n"
        "v_mfma_f32_16x16x32_bf16 v[120:123], a[128:131], a[232:235], v[120:123]\n"
        "v_cvt_pk_bf16_f32 v174, v116, v117\n"
        "v_cvt_pk_bf16_f32 v175, v118, v119\n"
        "v_mfma_f32_16x16x32_bf16 v[120:123], a[132:135], a[236:239], v[120:123]\n"
        "ds_write_b32 v245, v67 offset:3328\n"
        "v_mfma_f32_16x16x32_bf16 v[120:123], a[136:139], a[240:243], v[120:123]\n"
        "ds_write_b32 v245, v72 offset:512\n"
        "v_mfma_f32_16x16x32_bf16 v[120:123], a[140:143], a[244:247], v[120:123]\n"
        "s_nop 2\n"
        "v_mfma_f32_16x16x32_bf16 v[112:115], a[20:23], a[244:247], 0\n"
        "s_nop 2\n"
        "v_mfma_f32_16x16x32_bf16 v[112:115], a[16:19], a[240:243], v[112:115]\n"
        "v_cvt_pk_bf16_f32 v190, v120, v121\n"
        "v_cvt_pk_bf16_f32 v191, v122, v123\n"
        "v_mfma_f32_16x16x32_bf16 v[112:115], a[12:15], a[236:239], v[112:115]\n"
        "ds_write_b32 v245, v73 offset:1536\n"
        "v_mfma_f32_16x16x32_bf16 v[112:115], a[8:11], a[232:235], v[112:115]\n"
        "ds_write_b32 v245, v74 offset:2560\n"
        "v_mfma_f32_16x16x32_bf16 v[112:115], a[4:7], a[228:231], v[112:115]\n"
        "ds_read_b128 v[182:185], v251 offset:512\n"
        "v_mfma_f32_16x16x32_bf16 v[112:115], a[0:3], a[224:227], v[112:115]\n"
        "s_nop 2\n"
        "v_mfma_f32_16x16x32_bf16 v[116:119], a[24:27], a[224:227], 0\n"
        "s_nop 2\n"
        "v_mfma_f32_16x16x32_bf16 v[116:119], a[28:31], a[228:231], v[116:119]\n"
        "v_cvt_pk_bf16_f32 v176, v112, v113\n"
        "v_cvt_pk_bf16_f32 v177, v114, v115\n"
        "v_mfma_f32_16x16x32_bf16 v[116:119], a[32:35], a[232:235], v[116:119]\n"
        "ds_write_b32 v245, v75 offset:3584\n"
        "v_mfma_f32_16x16x32_bf16 v[116:119], a[36:39], a[236:239], v[116:119]\n"
        "buffer_load_dwordx4 v253, s[12:15], s36 offen offset:1024 lds\n"
        "v_mfma_f32_16x16x32_bf16 v[116:119], a[40:43], a[240:243], v[116:119]\n"
        "ds_write_b32 v245, v80 offset:768\n"
        "v_mfma_f32_16x16x32_bf16 v[116:119], a[44:47], a[244:247], v[116:119]\n"
        "s_nop 2\n"
        "v_mfma_f32_16x16x32_bf16 v[120:123], a[68:71], a[244:247], 0\n"
        "s_nop 2\n"
        "v_mfma_f32_16x16x32_bf16 v[120:123], a[64:67], a[240:243], v[120:123]\n"
        "v_cvt_pk_bf16_f32 v178, v116, v117\n"
        "v_cvt_pk_bf16_f32 v179, v118, v119\n"
        "v_mfma_f32_16x16x32_bf16 v[120:123], a[60:63], a[236:239], v[120:123]\n"
        "ds_write_b32 v245, v81 offset:1792\n"
        "v_mfma_f32_16x16x32_bf16 v[120:123], a[56:59], a[232:235], v[120:123]\n"
        "ds_write_b32 v245, v82 offset:2816\n"
        "v_mfma_f32_16x16x32_bf16 v[120:123], a[52:55], a[228:231], v[120:123]\n"
        "ds_write_b32 v245, v83 offset:3840\n"
        "v_mfma_f32_16x16x32_bf16 v[120:123], a[48:51], a[224:227], v[120:123]\n"
        "s_nop 2\n"
        "v_mfma_f32_16x16x32_bf16 v[112:115], a[224:227], a[144:147], 0\n"
        "s_nop 2\n"
        "v_mfma_f32_16x16x32_bf16 v[112:115], a[228:231], a[148:151], v[112:115]\n"
        "v_cvt_pk_bf16_f32 v180, v120, v121\n"
        "v_cvt_pk_bf16_f32 v181, v122, v123\n"
        "v_mfma_f32_16x16x32_bf16 v[112:115], a[232:235], a[152:155], v[112:115]\n"
        "v_mov_b64 v[186:187], v[180:181]\n"
        "v_mfma_f32_16x16x32_bf16 v[112:115], a[236:239], a[156:159], v[112:115]\n"
        "s_add_u32 m0, s41, 0\n"
        "v_mfma_f32_16x16x32_bf16 v[112:115], a[240:243], a[160:163], v[112:115]\n"
        "ds_read_b128 a[248:251], v240 offset:12288\n"
        "v_mfma_f32_16x16x32_bf16 v[112:115], a[244:247], a[164:167], v[112:115]\n"
        "buffer_load_dwordx4 v242, s[8:11], s47 offen lds\n"
        "v_mfma_f32_16x16x32_bf16 v[96:99], v[152:155], v[156:159], v[0:3]\n"
        "s_waitcnt lgkmcnt(2)\n"
        "ds_read_b128 a[216:219], v241 offset:12288\n"
        "s_nop 0\n"
        "v_mfma_f32_16x16x32_bf16 v[96:99], v[168:171], v[160:163], v[96:99]\n"
        "ds_read_b128 a[220:223], v240 offset:12416\n"
        "s_nop 0\n"
        "v_mfma_f32_16x16x32_bf16 v[100:103], v[172:175], v[156:159], v[4:7]\n"
        "s_sub_u32 s31, s30, s28\n"
        "v_cvt_pk_bf16_f32 v130, v112, v113\n"
        "v_cvt_pk_bf16_f32 v131, v114, v115\n"
        "v_mfma_f32_16x16x32_bf16 v[100:103], v[188:191], v[164:167], v[100:103]\n"
        "s_sub_u32 s45, s28, s29\n"
        "ds_read_b128 v[162:165], v252 offset:0\n"
        "v_mfma_f32_16x16x32_bf16 v[116:119], a[244:247], a[188:191], 0\n"
        "s_add_u32 s28, s28, s31\n"
        "v_exp_f32 v96, v96\n"
        "v_mfma_f32_16x16x32_bf16 v[116:119], a[240:243], a[184:187], v[116:119]\n"
        "s_mov_b32 s30, s29\n"
        "v_exp_f32 v97, v97\n"
        "v_mfma_f32_16x16x32_bf16 v[116:119], a[236:239], a[180:183], v[116:119]\n"
        "s_add_u32 s29, s29, s45\n"
        "v_exp_f32 v98, v98\n"
        "v_mfma_f32_16x16x32_bf16 v[116:119], a[232:235], a[176:179], v[116:119]\n"
        "v_exp_f32 v100, v100\n"
        "v_mfma_f32_16x16x32_bf16 v[116:119], a[228:231], a[172:175], v[116:119]\n"
        "v_exp_f32 v99, v99\n"
        "v_mfma_f32_16x16x32_bf16 v[116:119], a[224:227], a[168:171], v[116:119]\n"
        "v_exp_f32 v101, v101\n"
        "v_mfma_f32_16x16x32_bf16 v[104:107], v[152:155], v[176:179], v[16:19]\n"
        "v_exp_f32 v102, v102\n"
        "v_mfma_f32_16x16x32_bf16 v[104:107], v[168:171], v[180:183], v[104:107]\n"
        "s_add_u32 m0, s41, 1024\n"
        "v_cvt_pk_bf16_f32 v64, v96, v97\n"
        "v_cvt_pk_bf16_f32 v65, v98, v99\n"
        "v_mfma_f32_16x16x32_bf16 v[108:111], v[172:175], v[176:179], v[20:23]\n"
        "v_cvt_pk_bf16_f32 v138, v116, v117\n"
        "v_cvt_pk_bf16_f32 v139, v118, v119\n"
        "v_mfma_f32_16x16x32_bf16 v[108:111], v[188:191], v[184:187], v[108:111]\n"
        "buffer_load_dwordx4 v243, s[8:11], s47 offen lds\n"
        "v_mfma_f32_16x16x32_bf16 v[120:123], a[224:227], a[192:195], 0\n"
        "ds_read_b128 a[224:227], v241 offset:12416\n"
        "v_cvt_pk_bf16_f32 v66, v100, v101\n"
        "v_mfma_f32_16x16x32_bf16 v[120:123], a[228:231], a[196:199], v[120:123]\n"
        "ds_read_b128 a[228:231], v240 offset:12544\n"
        "v_mfma_f32_16x16x32_bf16 v[120:123], a[232:235], a[200:203], v[120:123]\n"
        "ds_read_b128 a[232:235], v241 offset:12544\n"
        "v_mfma_f32_16x16x32_bf16 v[120:123], a[236:239], a[204:207], v[120:123]\n"
        "ds_read_b128 v[202:205], v251 offset:4096\n"
        "v_mfma_f32_16x16x32_bf16 v[120:123], a[240:243], a[208:211], v[120:123]\n"
        "ds_read_b128 v[182:185], v252 offset:512\n"
        "v_mfma_f32_16x16x32_bf16 v[120:123], a[244:247], a[212:215], v[120:123]\n"
        "s_waitcnt lgkmcnt(4)\n"
        "v_exp_f32 v103, v103\n"
        "v_mfma_f32_16x16x32_bf16 v[112:115], a[0:3], a[248:251], 0\n"
        "v_cvt_pk_bf16_f32 v67, v102, v103\n"
        "s_nop 0\n"
        "v_mfma_f32_16x16x32_bf16 v[112:115], a[4:7], a[216:219], v[112:115]\n"
        "v_exp_f32 v104, v104\n"
        "v_mfma_f32_16x16x32_bf16 v[112:115], a[8:11], a[220:223], v[112:115]\n"
        "v_exp_f32 v105, v105\n"
        "v_mfma_f32_16x16x32_bf16 v[112:115], a[12:15], a[224:227], v[112:115]\n"
        "s_waitcnt lgkmcnt(0)\n"
        "v_exp_f32 v106, v106\n"
        "v_mfma_f32_16x16x32_bf16 v[112:115], a[16:19], a[228:231], v[112:115]\n"
        "v_cvt_pk_bf16_f32 v72, v104, v105\n"
        "v_cvt_pk_bf16_f32 v146, v120, v121\n"
        "v_mfma_f32_16x16x32_bf16 v[112:115], a[20:23], a[232:235], v[112:115]\n"
        "s_add_u32 m0, s41, 2048\n"
        "v_exp_f32 v108, v108\n"
        "v_mfma_f32_16x16x32_bf16 v[116:119], a[44:47], a[232:235], 0\n"
        "v_cvt_pk_bf16_f32 v147, v122, v123\n"
        "v_add_u32 v245, s45, v245\n"
        "v_mfma_f32_16x16x32_bf16 v[116:119], a[40:43], a[228:231], v[116:119]\n"
        "buffer_load_dwordx4 v244, s[8:11], s47 offen lds\n"
        "v_mfma_f32_16x16x32_bf16 v[116:119], a[36:39], a[224:227], v[116:119]\n"
        "v_exp_f32 v107, v107\n"
        "v_mfma_f32_16x16x32_bf16 v[116:119], a[32:35], a[220:223], v[116:119]\n"
        "v_exp_f32 v109, v109\n"
        "v_mfma_f32_16x16x32_bf16 v[116:119], a[28:31], a[216:219], v[116:119]\n"
        "v_cvt_pk_bf16_f32 v73, v106, v107\n"
        "v_cvt_pk_bf16_f32 v74, v108, v109\n"
        "v_mfma_f32_16x16x32_bf16 v[116:119], a[24:27], a[248:251], v[116:119]\n"
        "v_exp_f32 v110, v110\n"
        "v_mfma_f32_16x16x32_bf16 v[120:123], a[48:51], a[248:251], 0\n"
        "v_exp_f32 v111, v111\n"
        "v_mfma_f32_16x16x32_bf16 v[120:123], a[52:55], a[216:219], v[120:123]\n"
        "v_cvt_pk_bf16_f32 v75, v110, v111\n"
        "v_cvt_pk_bf16_f32 v196, v112, v113\n"
        "v_mfma_f32_16x16x32_bf16 v[120:123], a[56:59], a[220:223], v[120:123]\n"
        "v_cvt_pk_bf16_f32 v197, v114, v115\n"
        "v_cvt_pk_bf16_f32 v198, v116, v117\n"
        "v_mfma_f32_16x16x32_bf16 v[120:123], a[60:63], a[224:227], v[120:123]\n"
        "v_cvt_pk_bf16_f32 v199, v118, v119\n"
        "v_mfma_f32_16x16x32_bf16 v[120:123], a[64:67], a[228:231], v[120:123]\n"
        "v_mfma_f32_16x16x32_bf16 v[120:123], a[68:71], a[232:235], v[120:123]\n"
        "s_nop 2\n"
        "v_mfma_f32_16x16x32_bf16 v[112:115], a[92:95], a[232:235], 0\n"
        "s_nop 2\n"
        "v_mfma_f32_16x16x32_bf16 v[112:115], a[88:91], a[228:231], v[112:115]\n"
        "s_add_u32 m0, s41, 12288\n"
        "v_cvt_pk_bf16_f32 v200, v120, v121\n"
        "v_cvt_pk_bf16_f32 v201, v122, v123\n"
        "v_mfma_f32_16x16x32_bf16 v[112:115], a[84:87], a[224:227], v[112:115]\n"
        "v_mov_b64 v[206:207], v[200:201]\n"
        "v_mfma_f32_16x16x32_bf16 v[112:115], a[80:83], a[220:223], v[112:115]\n"
        "buffer_load_dwordx4 v242, s[8:11], s35 offen lds\n"
        "v_mfma_f32_16x16x32_bf16 v[112:115], a[76:79], a[216:219], v[112:115]\n"
        "v_mfma_f32_16x16x32_bf16 v[112:115], a[72:75], a[248:251], v[112:115]\n"
        "s_nop 2\n"
        "v_mfma_f32_16x16x32_bf16 v[96:99], v[152:155], v[196:199], v[32:35]\n"
        "s_nop 2\n"
        "v_mfma_f32_16x16x32_bf16 v[96:99], v[168:171], v[200:203], v[96:99]\n"
        "v_cvt_pk_bf16_f32 v192, v112, v113\n"
        "v_cvt_pk_bf16_f32 v193, v114, v115\n"
        "v_mfma_f32_16x16x32_bf16 v[100:103], v[172:175], v[196:199], v[36:39]\n"
        "v_mfma_f32_16x16x32_bf16 v[100:103], v[188:191], v[204:207], v[100:103]\n"
        "ds_read_b128 v[202:205], v252 offset:4096\n"
        "v_mfma_f32_16x16x32_bf16 v[116:119], a[96:99], a[248:251], 0\n"
        "s_cmp_eq_u32 s37, 100663296\n"
        "v_mfma_f32_16x16x32_bf16 v[116:119], a[100:103], a[216:219], v[116:119]\n"
        "s_cselect_b32 s46, 0, s37\n"
        "v_exp_f32 v96, v96\n"
        "v_mfma_f32_16x16x32_bf16 v[116:119], a[104:107], a[220:223], v[116:119]\n"
        "s_add_u32 s38, s46, s43\n"
        "v_exp_f32 v97, v97\n"
        "v_mfma_f32_16x16x32_bf16 v[116:119], a[108:111], a[224:227], v[116:119]\n"
        "s_add_u32 s39, s46, s44\n"
        "v_exp_f32 v98, v98\n"
        "v_mfma_f32_16x16x32_bf16 v[116:119], a[112:115], a[228:231], v[116:119]\n"
        "s_add_u32 s37, s37, 6291456\n"
        "v_exp_f32 v100, v100\n"
        "v_mfma_f32_16x16x32_bf16 v[116:119], a[116:119], a[232:235], v[116:119]\n"
        "s_add_u32 m0, s41, 13312\n"
        "v_exp_f32 v99, v99\n"
        "v_mfma_f32_16x16x32_bf16 v[120:123], a[140:143], a[232:235], 0\n"
        "v_exp_f32 v101, v101\n"
        "v_mfma_f32_16x16x32_bf16 v[120:123], a[136:139], a[228:231], v[120:123]\n"
        "buffer_load_dwordx4 v243, s[8:11], s35 offen lds\n"
        "v_mfma_f32_16x16x32_bf16 v[120:123], a[132:135], a[224:227], v[120:123]\n"
        "v_cvt_pk_bf16_f32 v80, v96, v97\n"
        "v_cvt_pk_bf16_f32 v194, v116, v117\n"
        "v_mfma_f32_16x16x32_bf16 v[120:123], a[128:131], a[220:223], v[120:123]\n"
        "s_mov_b64 s[58:59], s[18:19]\n"
        "v_cvt_pk_bf16_f32 v195, v118, v119\n"
        "v_cvt_pk_bf16_f32 v81, v98, v99\n"
        "v_mfma_f32_16x16x32_bf16 v[120:123], a[124:127], a[216:219], v[120:123]\n"
        "s_mov_b64 s[62:63], s[18:19]\n"
        "v_exp_f32 v102, v102\n"
        "v_mfma_f32_16x16x32_bf16 v[120:123], a[120:123], a[248:251], v[120:123]\n"
        "v_cvt_pk_bf16_f32 v82, v100, v101\n"
        "s_nop 0\n"
        "v_mfma_f32_16x16x32_bf16 v[112:115], a[248:251], a[144:147], 0\n"
        "v_exp_f32 v103, v103\n"
        "v_mfma_f32_16x16x32_bf16 v[112:115], a[216:219], a[148:151], v[112:115]\n"
        "v_cvt_pk_bf16_f32 v83, v102, v103\n"
        "s_nop 0\n"
        "v_mfma_f32_16x16x32_bf16 v[112:115], a[220:223], a[152:155], v[112:115]\n"
        "ds_read_b128 a[236:239], v240 offset:13824\n"
        "v_cvt_pk_bf16_f32 v208, v120, v121\n"
        "v_mfma_f32_16x16x32_bf16 v[112:115], a[224:227], a[156:159], v[112:115]\n"
        "ds_read_b128 a[240:243], v241 offset:13824\n"
        "v_cvt_pk_bf16_f32 v209, v122, v123\n"
        "v_mfma_f32_16x16x32_bf16 v[112:115], a[228:231], a[160:163], v[112:115]\n"
        "ds_read_b128 a[244:247], v240 offset:13952\n"
        "v_mfma_f32_16x16x32_bf16 v[112:115], a[232:235], a[164:167], v[112:115]\n"
        "s_nop 2\n"
        "v_mfma_f32_16x16x32_bf16 v[104:107], v[192:195], v[156:159], v[8:11]\n"
        "s_nop 2\n"
        "v_mfma_f32_16x16x32_bf16 v[104:107], v[208:211], v[160:163], v[104:107]\n"
        "s_add_u32 m0, s41, 14336\n"
        "v_cvt_pk_bf16_f32 v132, v112, v113\n"
        "v_cvt_pk_bf16_f32 v133, v114, v115\n"
        "v_mfma_f32_16x16x32_bf16 v[116:119], a[232:235], a[188:191], 0\n"
        "v_mfma_f32_16x16x32_bf16 v[116:119], a[228:231], a[184:187], v[116:119]\n"
        "buffer_load_dwordx4 v244, s[8:11], s35 offen lds\n"
        "v_mfma_f32_16x16x32_bf16 v[116:119], a[224:227], a[180:183], v[116:119]\n"
        "v_mfma_f32_16x16x32_bf16 v[116:119], a[220:223], a[176:179], v[116:119]\n"
        "v_exp_f32 v104, v104\n"
        "v_mfma_f32_16x16x32_bf16 v[116:119], a[216:219], a[172:175], v[116:119]\n"
        "v_exp_f32 v105, v105\n"
        "v_mfma_f32_16x16x32_bf16 v[116:119], a[248:251], a[168:171], v[116:119]\n"
        "v_exp_f32 v106, v106\n"
        "v_mfma_f32_16x16x32_bf16 v[108:111], v[192:195], v[176:179], v[24:27]\n"
        "v_exp_f32 v107, v107\n"
        "v_mfma_f32_16x16x32_bf16 v[108:111], v[208:211], v[180:183], v[108:111]\n"
        "v_cvt_pk_bf16_f32 v68, v104, v105\n"
        "v_cvt_pk_bf16_f32 v69, v106, v107\n"
        "v_mfma_f32_16x16x32_bf16 v[120:123], a[248:251], a[192:195], 0\n"
        "ds_read_b128 a[248:251], v241 offset:13952\n"
        "v_cvt_pk_bf16_f32 v140, v116, v117\n"
        "v_mfma_f32_16x16x32_bf16 v[120:123], a[216:219], a[196:199], v[120:123]\n"
        "ds_read_b128 a[216:219], v240 offset:14080\n"
        "v_cvt_pk_bf16_f32 v141, v118, v119\n"
        "v_mfma_f32_16x16x32_bf16 v[120:123], a[220:223], a[200:203], v[120:123]\n"
        "ds_read_b128 a[220:223], v241 offset:14080\n"
        "v_mfma_f32_16x16x32_bf16 v[120:123], a[224:227], a[204:207], v[120:123]\n"
        "v_exp_f32 v108, v108\n"
        "v_mfma_f32_16x16x32_bf16 v[120:123], a[228:231], a[208:211], v[120:123]\n"
        "v_exp_f32 v109, v109\n"
        "v_mfma_f32_16x16x32_bf16 v[120:123], a[232:235], a[212:215], v[120:123]\n"
        "v_exp_f32 v110, v110\n"
        "v_mfma_f32_16x16x32_bf16 v[96:99], v[192:195], v[196:199], v[40:43]\n"
        "s_waitcnt lgkmcnt(1)\n"
        "v_exp_f32 v111, v111\n"
        "v_mfma_f32_16x16x32_bf16 v[96:99], v[208:211], v[200:203], v[96:99]\n"
        "v_cvt_pk_bf16_f32 v76, v108, v109\n"
        "v_cvt_pk_bf16_f32 v77, v110, v111\n"
        "v_mfma_f32_16x16x32_bf16 v[112:115], a[72:75], a[236:239], 0\n"
        "s_nop 0\n"
        "v_cvt_pk_bf16_f32 v148, v120, v121\n"
        "v_cvt_pk_bf16_f32 v149, v122, v123\n"
        "v_mfma_f32_16x16x32_bf16 v[112:115], a[76:79], a[240:243], v[112:115]\n"
        "v_mfma_f32_16x16x32_bf16 v[112:115], a[80:83], a[244:247], v[112:115]\n"
        "v_exp_f32 v96, v96\n"
        "v_mfma_f32_16x16x32_bf16 v[112:115], a[84:87], a[248:251], v[112:115]\n"
        "v_exp_f32 v97, v97\n"
        "v_mfma_f32_16x16x32_bf16 v[112:115], a[88:91], a[216:219], v[112:115]\n"
        "s_waitcnt lgkmcnt(0)\n"
        "v_exp_f32 v98, v98\n"
        "v_mfma_f32_16x16x32_bf16 v[112:115], a[92:95], a[220:223], v[112:115]\n"
        "v_exp_f32 v99, v99\n"
        "v_mfma_f32_16x16x32_bf16 v[116:119], a[116:119], a[220:223], 0\n"
        "v_cvt_pk_bf16_f32 v84, v96, v97\n"
        "v_cvt_pk_bf16_f32 v85, v98, v99\n"
        "v_mfma_f32_16x16x32_bf16 v[116:119], a[112:115], a[216:219], v[116:119]\n"
        "v_add_u32 v240, s31, v240\n"
        "v_add_u32 v241, s31, v241\n"
        "v_mfma_f32_16x16x32_bf16 v[116:119], a[108:111], a[248:251], v[116:119]\n"
        "v_cvt_pk_bf16_f32 v212, v112, v113\n"
        "v_cvt_pk_bf16_f32 v213, v114, v115\n"
        "v_mfma_f32_16x16x32_bf16 v[116:119], a[104:107], a[244:247], v[116:119]\n"
        "s_add_u32 s56, s16, s38\n"
        "s_addc_u32 s57, s17, 0\n"
        "v_mfma_f32_16x16x32_bf16 v[116:119], a[100:103], a[240:243], v[116:119]\n"
        "s_add_u32 s60, s16, s39\n"
        "s_addc_u32 s61, s17, 0\n"
        "v_mfma_f32_16x16x32_bf16 v[116:119], a[96:99], a[236:239], v[116:119]\n"
        "s_nop 2\n"
        "v_mfma_f32_16x16x32_bf16 v[120:123], a[120:123], a[236:239], 0\n"
        "s_nop 2\n"
        "v_mfma_f32_16x16x32_bf16 v[120:123], a[124:127], a[240:243], v[120:123]\n"
        "s_add_u32 s47, s47, 6291456\n"
        "v_cvt_pk_bf16_f32 v214, v116, v117\n"
        "v_cvt_pk_bf16_f32 v215, v118, v119\n"
        "v_mfma_f32_16x16x32_bf16 v[120:123], a[128:131], a[244:247], v[120:123]\n"
        "s_add_u32 s36, s36, 2097152\n"
        "v_mfma_f32_16x16x32_bf16 v[120:123], a[132:135], a[248:251], v[120:123]\n"
        "s_xor_b32 s48, s48, 8192\n"
        "v_mfma_f32_16x16x32_bf16 v[120:123], a[136:139], a[216:219], v[120:123]\n"
        ".long 0xbec200ff\n"
        "45:\n"
        "s_branch 61b\n"
        "v_mfma_f32_16x16x32_bf16 v[120:123], a[140:143], a[220:223], v[120:123]\n"
        "s_nop 2\n"
        "v_mfma_f32_16x16x32_bf16 v[112:115], a[220:223], a[164:167], 0\n"
        "s_nop 2\n"
        "v_mfma_f32_16x16x32_bf16 v[112:115], a[216:219], a[160:163], v[112:115]\n"
        "s_barrier\n"
        "v_cvt_pk_bf16_f32 v230, v120, v121\n"
        "v_cvt_pk_bf16_f32 v231, v122, v123\n"
        "v_mfma_f32_16x16x32_bf16 v[112:115], a[248:251], a[156:159], v[112:115]\n"
        "v_mfma_f32_16x16x32_bf16 v[112:115], a[244:247], a[152:155], v[112:115]\n"
        "v_mfma_f32_16x16x32_bf16 v[112:115], a[240:243], a[148:151], v[112:115]\n"
        "v_mfma_f32_16x16x32_bf16 v[112:115], a[236:239], a[144:147], v[112:115]\n"
        "s_nop 2\n"
        "v_mfma_f32_16x16x32_bf16 v[100:103], v[212:215], v[156:159], v[12:15]\n"
        "s_nop 2\n"
        "v_mfma_f32_16x16x32_bf16 v[100:103], v[228:231], v[164:167], v[100:103]\n"
        "v_cvt_pk_bf16_f32 v134, v112, v113\n"
        "v_cvt_pk_bf16_f32 v135, v114, v115\n"
        "v_mfma_f32_16x16x32_bf16 v[116:119], a[236:239], a[168:171], 0\n"
        "s_sub_u32 s27, s27, 1\n"
        "v_mfma_f32_16x16x32_bf16 v[116:119], a[240:243], a[172:175], v[116:119]\n"
        "s_cmp_lg_u32 s27, 0\n"
        "v_mfma_f32_16x16x32_bf16 v[116:119], a[244:247], a[176:179], v[116:119]\n"
        "s_cbranch_scc1 10b\n"
        "s_branch 19f\n"
        ".p2align 6\n"
        "51:\n"
        "s_branch 61b\n"
        "s_nop 0\n"
        "s_nop 0\n"
        "s_nop 0\n"
        "s_nop 0\n"
        "s_nop 0\n"
        "12:\n"
        "10:\n"
        "v_mfma_f32_16x16x32_bf16 v[116:119], a[248:251], a[180:183], v[116:119]\n"
        "v_mfma_f32_16x16x32_bf16 v[116:119], a[216:219], a[184:187], v[116:119]\n"
        "s_nop 0\n"
        "v_exp_f32 v100, v100\n"
        "v_mfma_f32_16x16x32_bf16 v[116:119], a[220:223], a[188:191], v[116:119]\n"
        "v_exp_f32 v101, v101\n"
        "v_mfma_f32_16x16x32_bf16 v[104:107], v[212:215], v[176:179], v[28:31]\n"
        "v_exp_f32 v102, v102\n"
        "v_mfma_f32_16x16x32_bf16 v[104:107], v[228:231], v[184:187], v[104:107]\n"
        "v_exp_f32 v103, v103\n"
        "v_mfma_f32_16x16x32_bf16 v[120:123], a[220:223], a[212:215], 0\n"
        "v_cvt_pk_bf16_f32 v70, v100, v101\n"
        "v_cvt_pk_bf16_f32 v71, v102, v103\n"
        "v_mfma_f32_16x16x32_bf16 v[120:123], a[216:219], a[208:211], v[120:123]\n"
        "v_cvt_pk_bf16_f32 v142, v116, v117\n"
        "v_cvt_pk_bf16_f32 v143, v118, v119\n"
        "v_mfma_f32_16x16x32_bf16 v[120:123], a[248:251], a[204:207], v[120:123]\n"
        "v_mov_b64 v[96:97], 0\n"
        "v_mfma_f32_16x16x32_bf16 v[120:123], a[244:247], a[200:203], v[120:123]\n"
        "v_exp_f32 v104, v104\n"
        "v_mfma_f32_16x16x32_bf16 v[120:123], a[240:243], a[196:199], v[120:123]\n"
        "v_exp_f32 v105, v105\n"
        "v_mfma_f32_16x16x32_bf16 v[120:123], a[236:239], a[192:195], v[120:123]\n"
        "v_exp_f32 v106, v106\n"
        "v_mfma_f32_16x16x32_bf16 v[108:111], v[212:215], v[196:199], v[44:47]\n"
        "v_exp_f32 v107, v107\n"
        "v_mfma_f32_16x16x32_bf16 v[108:111], v[228:231], v[204:207], v[108:111]\n"
        "v_cvt_pk_bf16_f32 v78, v104, v105\n"
        "v_cvt_pk_bf16_f32 v79, v106, v107\n"
        "v_mfma_f32_16x16x32_bf16 v[112:115], a[0:3], a[236:239], 0\n"
        "s_nop 0\n"
        "v_cvt_pk_bf16_f32 v150, v120, v121\n"
        "v_cvt_pk_bf16_f32 v151, v122, v123\n"
        "v_mfma_f32_16x16x32_bf16 v[112:115], a[4:7], a[240:243], v[112:115]\n"
        "v_mfma_f32_16x16x32_bf16 v[112:115], a[8:11], a[244:247], v[112:115]\n"
        "v_exp_f32 v108, v108\n"
        "v_mfma_f32_16x16x32_bf16 v[112:115], a[12:15], a[248:251], v[112:115]\n"
        "v_exp_f32 v109, v109\n"
        "v_mfma_f32_16x16x32_bf16 v[112:115], a[16:19], a[216:219], v[112:115]\n"
        "v_exp_f32 v110, v110\n"
        "v_mfma_f32_16x16x32_bf16 v[112:115], a[20:23], a[220:223], v[112:115]\n"
        "v_exp_f32 v111, v111\n"
        "v_smfmac_f32_16x16x64_bf16 v[96:99], a[252:255], v[64:71], v237\n"
        "v_cvt_pk_bf16_f32 v86, v108, v109\n"
        "v_mfma_f32_16x16x32_bf16 v[100:103], v[132:135], v[68:71], 0\n"
        "v_mov_b64 v[124:125], 0\n"
        "v_cvt_pk_bf16_f32 v87, v110, v111\n"
        "v_mfma_f32_16x16x32_bf16 v[100:103], v[128:131], v[64:67], v[100:103]\n"
        "s_nop 0\n"
        "v_cvt_pk_bf16_f32 v216, v112, v113\n"
        "v_cvt_pk_bf16_f32 v217, v114, v115\n"
        "v_mfma_f32_16x16x32_bf16 v[104:107], v[136:139], v[64:67], 0\n"
        "ds_read_b128 v[222:225], v251 offset:4608\n"
        "v_add_f32 v97, v96, v97\n"
        "v_mfma_f32_16x16x32_bf16 v[104:107], v[140:143], v[68:71], v[104:107]\n"
        "v_rcp_f32 v96, v97\n"
        "v_mfma_f32_16x16x32_bf16 v[108:111], v[148:151], v[68:71], 0\n"
        "v_mul_f32 v100, v96, v100\n"
        "v_mul_f32 v101, v96, v101\n"
        "v_mfma_f32_16x16x32_bf16 v[108:111], v[144:147], v[64:67], v[108:111]\n"
        "v_mul_f32 v102, v96, v102\n"
        "v_mul_f32 v103, v96, v103\n"
        "v_mfma_f32_16x16x32_bf16 v[116:119], a[44:47], a[220:223], 0\n"
        "v_cvt_pk_bf16_f32 v100, v100, v101\n"
        "v_cvt_pk_bf16_f32 v101, v102, v103\n"
        "v_mfma_f32_16x16x32_bf16 v[116:119], a[40:43], a[216:219], v[116:119]\n"
        "v_mul_f32 v104, v96, v104\n"
        "v_mul_f32 v105, v96, v105\n"
        "v_mfma_f32_16x16x32_bf16 v[116:119], a[36:39], a[248:251], v[116:119]\n"
        "v_mul_f32 v106, v96, v106\n"
        "v_mul_f32 v107, v96, v107\n"
        "v_mfma_f32_16x16x32_bf16 v[116:119], a[32:35], a[244:247], v[116:119]\n"
        "v_cvt_pk_bf16_f32 v102, v104, v105\n"
        "v_cvt_pk_bf16_f32 v103, v106, v107\n"
        "v_mfma_f32_16x16x32_bf16 v[116:119], a[28:31], a[240:243], v[116:119]\n"
        "v_mul_f32 v108, v96, v108\n"
        "v_mul_f32 v109, v96, v109\n"
        "v_mfma_f32_16x16x32_bf16 v[116:119], a[24:27], a[236:239], v[116:119]\n"
        "v_mul_f32 v110, v96, v110\n"
        "v_mul_f32 v111, v96, v111\n"
        "v_smfmac_f32_16x16x64_bf16 v[124:127], a[252:255], v[72:79], v237\n"
        "v_cvt_pk_bf16_f32 v108, v108, v109\n"
        "v_mfma_f32_16x16x32_bf16 v[64:67], v[132:135], v[76:79], 0\n"
        "ds_write_b32 v245, v108 offset:4096\n"
        "v_mfma_f32_16x16x32_bf16 v[64:67], v[128:131], v[72:75], v[64:67]\n"
        "ds_read_b128 v[156:159], v247 offset:0\n"
        "v_cvt_pk_bf16_f32 v218, v116, v117\n"
        "v_mfma_f32_16x16x32_bf16 v[68:71], v[136:139], v[72:75], 0\n"
        "ds_read_b128 v[160:163], v247 offset:4096\n"
        "v_cvt_pk_bf16_f32 v219, v118, v119\n"
        "v_mfma_f32_16x16x32_bf16 v[68:71], v[140:143], v[76:79], v[68:71]\n"
        "ds_read_b128 v[164:167], v247 offset:4352\n"
        "v_cvt_pk_bf16_f32 v109, v110, v111\n"
        "v_mfma_f32_16x16x32_bf16 v[120:123], v[148:151], v[76:79], 0\n"
        "ds_write_b32 v245, v109 offset:5120\n"
        "v_mfma_f32_16x16x32_bf16 v[120:123], v[144:147], v[72:75], v[120:123]\n"
        "v_max_u32 v236, v96, v97\n"
        "v_mov_b64 v[116:117], 0\n"
        "v_mfma_f32_16x16x32_bf16 v[112:115], a[48:51], a[236:239], 0\n"
        "v_add_f32 v125, v124, v125\n"
        "v_mfma_f32_16x16x32_bf16 v[112:115], a[52:55], a[240:243], v[112:115]\n"
        "v_rcp_f32 v124, v125\n"
        "v_mfma_f32_16x16x32_bf16 v[112:115], a[56:59], a[244:247], v[112:115]\n"
        "v_mul_f32 v64, v124, v64\n"
        "v_mul_f32 v65, v124, v65\n"
        "v_mfma_f32_16x16x32_bf16 v[112:115], a[60:63], a[248:251], v[112:115]\n"
        "v_mul_f32 v66, v124, v66\n"
        "v_mul_f32 v67, v124, v67\n"
        "v_mfma_f32_16x16x32_bf16 v[112:115], a[64:67], a[216:219], v[112:115]\n"
        "v_cvt_pk_bf16_f32 v64, v64, v65\n"
        "v_cvt_pk_bf16_f32 v65, v66, v67\n"
        "v_mfma_f32_16x16x32_bf16 v[112:115], a[68:71], a[220:223], v[112:115]\n"
        "ds_read_b128 a[216:219], v248 offset:0\n"
        "v_mul_f32 v68, v124, v68\n"
        "v_smfmac_f32_16x16x64_bf16 v[116:119], a[252:255], v[80:87], v237\n"
        "ds_read_b128 a[220:223], v248 offset:3072\n"
        "v_mfma_f32_16x16x32_bf16 v[72:75], v[132:135], v[84:87], 0\n"
        "v_mul_f32 v69, v124, v69\n"
        "v_mul_f32 v70, v124, v70\n"
        "v_mfma_f32_16x16x32_bf16 v[72:75], v[128:131], v[80:83], v[72:75]\n"
        "v_cvt_pk_bf16_f32 v220, v112, v113\n"
        "v_cvt_pk_bf16_f32 v221, v114, v115\n"
        "v_mfma_f32_16x16x32_bf16 v[76:79], v[136:139], v[80:83], 0\n"
        "ds_read_b128 a[224:227], v248 offset:32\n"
        "v_mov_b64 v[226:227], v[220:221]\n"
        "v_mfma_f32_16x16x32_bf16 v[76:79], v[140:143], v[84:87], v[76:79]\n"
        "ds_read_b128 a[228:231], v248 offset:3104\n"
        "v_mul_f32 v71, v124, v71\n"
        "v_mfma_f32_16x16x32_bf16 v[96:99], v[148:151], v[84:87], 0\n"
        "v_cvt_pk_bf16_f32 v66, v68, v69\n"
        "v_cvt_pk_bf16_f32 v67, v70, v71\n"
        "v_mfma_f32_16x16x32_bf16 v[96:99], v[144:147], v[80:83], v[96:99]\n"
        "v_mul_f32 v120, v124, v120\n"
        "v_mul_f32 v121, v124, v121\n"
        "v_mfma_f32_16x16x32_bf16 v[104:107], v[152:155], v[216:219], v[48:51]\n"
        "s_waitcnt lgkmcnt(2)\n"
        "ds_read_b128 v[152:155], v247 offset:256\n"
        "v_mul_f32 v122, v124, v122\n"
        "v_mfma_f32_16x16x32_bf16 v[104:107], v[168:171], v[220:223], v[104:107]\n"
        "v_mul_f32 v123, v124, v123\n"
        "v_cvt_pk_bf16_f32 v120, v120, v121\n"
        "v_mfma_f32_16x16x32_bf16 v[108:111], v[172:175], v[216:219], v[52:55]\n"
        "ds_write_b32 v245, v120 offset:4352\n"
        "v_mfma_f32_16x16x32_bf16 v[108:111], v[188:191], v[224:227], v[108:111]\n"
        "ds_read_b128 v[222:225], v252 offset:4608\n"
        "v_cvt_pk_bf16_f32 v121, v122, v123\n"
        "v_mfma_f32_16x16x32_bf16 v[176:179], v[156:159], a[216:219], 0\n"
        "ds_write_b32 v245, v121 offset:5376\n"
        "v_mfma_f32_16x16x32_bf16 v[176:179], v[160:163], a[220:223], v[176:179]\n"
        "ds_read_b128 a[232:235], v254 offset:0\n"
        "v_max3_u32 v236, v236, v124, v125\n"
        "v_mfma_f32_16x16x32_bf16 v[196:199], v[164:167], a[220:223], 0\n"
        "s_waitcnt lgkmcnt(4)\n"
        "ds_read_b128 a[220:223], v254 offset:3072\n"
        "v_add_f32 v117, v116, v117\n"
        "v_mfma_f32_16x16x32_bf16 v[196:199], v[152:155], a[216:219], v[196:199]\n"
        "v_rcp_f32 v116, v117\n"
        "v_mfma_f32_16x16x32_bf16 v[200:203], v[152:155], a[224:227], 0\n"
        "ds_read_b128 a[216:219], v254 offset:32\n"
        "v_mul_f32 v72, v116, v72\n"
        "v_mfma_f32_16x16x32_bf16 v[200:203], v[164:167], a[228:231], v[200:203]\n"
        "v_exp_f32 v104, v104\n"
        "v_mfma_f32_16x16x32_bf16 v[180:183], v[160:163], a[228:231], 0\n"
        "ds_read_b128 a[228:231], v254 offset:3104\n"
        "v_mul_f32 v73, v116, v73\n"
        "v_mfma_f32_16x16x32_bf16 v[180:183], v[156:159], a[224:227], v[180:183]\n"
        "ds_read_b128 a[236:239], v247 offset:8192\n"
        "v_mul_f32 v74, v116, v74\n"
        "v_mfma_f32_16x16x32_bf16 v[112:115], v[192:195], v[216:219], v[56:59]\n"
        "s_waitcnt lgkmcnt(6)\n"
        "ds_read_b128 a[224:227], v248 offset:16\n"
        "v_mul_f32 v75, v116, v75\n"
        "v_mfma_f32_16x16x32_bf16 v[112:115], v[208:211], v[220:223], v[112:115]\n"
        "ds_read_b128 a[240:243], v247 offset:12288\n"
        "v_cvt_pk_bf16_f32 v72, v72, v73\n"
        "v_mfma_f32_16x16x32_bf16 v[68:71], v[212:215], v[216:219], v[60:63]\n"
        "ds_read_b128 a[244:247], v247 offset:12544\n"
        "v_cvt_pk_bf16_f32 v73, v74, v75\n"
        "v_mfma_f32_16x16x32_bf16 v[68:71], v[228:231], v[224:227], v[68:71]\n"
        "s_waitcnt lgkmcnt(4)\n"
        "ds_read_b128 a[248:251], v247 offset:8448\n"
        "v_mul_f32 v76, v116, v76\n"
        "v_mfma_f32_16x16x32_bf16 v[184:187], v[156:159], a[232:235], 0\n"
        "v_mul_f32 v77, v116, v77\n"
        "v_mul_f32 v78, v116, v78\n"
        "v_mfma_f32_16x16x32_bf16 v[184:187], v[160:163], a[220:223], v[184:187]\n"
        "v_mul_f32 v79, v116, v79\n"
        "v_cvt_pk_bf16_f32 v74, v76, v77\n"
        "v_mfma_f32_16x16x32_bf16 v[204:207], v[164:167], a[220:223], 0\n"
        "ds_read_b128 a[220:223], v248 offset:3088\n"
        "v_cvt_pk_bf16_f32 v75, v78, v79\n"
        "v_mfma_f32_16x16x32_bf16 v[204:207], v[152:155], a[232:235], v[204:207]\n"
        "ds_read_b128 a[232:235], v254 offset:16\n"
        "v_mul_f32 v96, v116, v96\n"
        "v_mfma_f32_16x16x32_bf16 v[192:195], v[152:155], a[216:219], 0\n"
        "v_mul_f32 v97, v116, v97\n"
        "v_mul_f32 v98, v116, v98\n"
        "v_mfma_f32_16x16x32_bf16 v[192:195], v[164:167], a[228:231], v[192:195]\n"
        "v_mul_f32 v99, v116, v99\n"
        "v_cvt_pk_bf16_f32 v96, v96, v97\n"
        "v_mfma_f32_16x16x32_bf16 v[172:175], v[160:163], a[228:231], 0\n"
        "ds_read_b128 a[228:231], v254 offset:3088\n"
        "v_cvt_pk_bf16_f32 v97, v98, v99\n"
        "v_mfma_f32_16x16x32_bf16 v[172:175], v[156:159], a[216:219], v[172:175]\n"
        "s_waitcnt lgkmcnt(2)\n"
        "ds_read_b128 a[216:219], v248 offset:6144\n"
        "v_max3_u32 v236, v236, v116, v117\n"
        "v_mfma_f32_16x16x32_bf16 v[212:215], v[156:159], a[224:227], 0\n"
        "v_exp_f32 v105, v105\n"
        "v_mfma_f32_16x16x32_bf16 v[212:215], v[160:163], a[220:223], v[212:215]\n"
        "v_exp_f32 v106, v106\n"
        "v_mfma_f32_16x16x32_bf16 v[216:219], v[164:167], a[220:223], 0\n"
        "ds_read_b128 a[220:223], v248 offset:9216\n"
        "v_cvt_pk_bf16_f32 v88, v104, v105\n"
        "v_mfma_f32_16x16x32_bf16 v[216:219], v[152:155], a[224:227], v[216:219]\n"
        "s_waitcnt lgkmcnt(2)\n"
        "ds_read_b128 a[224:227], v248 offset:6176\n"
        "v_mfma_f32_16x16x32_bf16 v[224:227], v[152:155], a[232:235], 0\n"
        "v_exp_f32 v108, v108\n"
        "v_mfma_f32_16x16x32_bf16 v[224:227], v[164:167], a[228:231], v[224:227]\n"
        "v_exp_f32 v107, v107\n"
        "v_mfma_f32_16x16x32_bf16 v[220:223], v[160:163], a[228:231], 0\n"
        "ds_read_b128 a[228:231], v248 offset:9248\n"
        "v_cvt_pk_bf16_f32 v89, v106, v107\n"
        "v_mfma_f32_16x16x32_bf16 v[220:223], v[156:159], a[232:235], v[220:223]\n"
        "s_waitcnt lgkmcnt(2)\n"
        "ds_read_b128 a[232:235], v254 offset:6144\n"
        "v_mfma_f32_16x16x32_bf16 v[176:179], a[236:239], a[216:219], v[176:179]\n"
        "v_exp_f32 v109, v109\n"
        "v_mfma_f32_16x16x32_bf16 v[176:179], a[240:243], a[220:223], v[176:179]\n"
        "v_exp_f32 v110, v110\n"
        "v_mfma_f32_16x16x32_bf16 v[196:199], a[244:247], a[220:223], v[196:199]\n"
        "ds_read_b128 a[220:223], v254 offset:9216\n"
        "v_cvt_pk_bf16_f32 v90, v108, v109\n"
        "v_mfma_f32_16x16x32_bf16 v[196:199], a[248:251], a[216:219], v[196:199]\n"
        "s_waitcnt lgkmcnt(2)\n"
        "ds_read_b128 a[216:219], v254 offset:6176\n"
        "v_mfma_f32_16x16x32_bf16 v[200:203], a[248:251], a[224:227], v[200:203]\n"
        "ds_read_b128 v[156:159], v247 offset:16384\n"
        "v_mfma_f32_16x16x32_bf16 v[200:203], a[244:247], a[228:231], v[200:203]\n"
        "ds_read_b128 v[160:163], v247 offset:20480\n"
        "v_mfma_f32_16x16x32_bf16 v[180:183], a[240:243], a[228:231], v[180:183]\n"
        "ds_read_b128 a[228:231], v254 offset:9248\n"
        "v_mfma_f32_16x16x32_bf16 v[180:183], a[236:239], a[224:227], v[180:183]\n"
        "s_waitcnt lgkmcnt(4)\n"
        "ds_read_b128 a[224:227], v248 offset:6160\n"
        "v_mfma_f32_16x16x32_bf16 v[184:187], a[236:239], a[232:235], v[184:187]\n"
        "ds_read_b128 v[164:167], v247 offset:20736\n"
        "v_mfma_f32_16x16x32_bf16 v[184:187], a[240:243], a[220:223], v[184:187]\n"
        "ds_read_b128 v[152:155], v247 offset:16640\n"
        "v_mfma_f32_16x16x32_bf16 v[204:207], a[244:247], a[220:223], v[204:207]\n"
        "ds_read_b128 a[220:223], v248 offset:9232\n"
        "v_mfma_f32_16x16x32_bf16 v[204:207], a[248:251], a[232:235], v[204:207]\n"
        "s_waitcnt lgkmcnt(4)\n"
        "ds_read_b128 a[232:235], v254 offset:6160\n"
        "v_mfma_f32_16x16x32_bf16 v[192:195], a[248:251], a[216:219], v[192:195]\n"
        "v_exp_f32 v111, v111\n"
        "v_mfma_f32_16x16x32_bf16 v[192:195], a[244:247], a[228:231], v[192:195]\n"
        "v_cvt_pk_bf16_f32 v91, v110, v111\n"
        "v_mfma_f32_16x16x32_bf16 v[172:175], a[240:243], a[228:231], v[172:175]\n"
        "ds_read_b128 a[228:231], v254 offset:9232\n"
        "v_mfma_f32_16x16x32_bf16 v[172:175], a[236:239], a[216:219], v[172:175]\n"
        "s_waitcnt lgkmcnt(2)\n"
        "ds_read_b128 a[216:219], v248 offset:12288\n"
        "v_mfma_f32_16x16x32_bf16 v[212:215], a[236:239], a[224:227], v[212:215]\n"
        "v_exp_f32 v112, v112\n"
        "v_mfma_f32_16x16x32_bf16 v[212:215], a[240:243], a[220:223], v[212:215]\n"
        "v_exp_f32 v113, v113\n"
        "v_mfma_f32_16x16x32_bf16 v[216:219], a[244:247], a[220:223], v[216:219]\n"
        "ds_read_b128 a[220:223], v248 offset:15360\n"
        "v_cvt_pk_bf16_f32 v92, v112, v113\n"
        "v_mfma_f32_16x16x32_bf16 v[216:219], a[248:251], a[224:227], v[216:219]\n"
        "s_waitcnt lgkmcnt(2)\n"
        "ds_read_b128 a[224:227], v248 offset:12320\n"
        "v_mfma_f32_16x16x32_bf16 v[224:227], a[248:251], a[232:235], v[224:227]\n"
        "v_exp_f32 v114, v114\n"
        "v_mfma_f32_16x16x32_bf16 v[224:227], a[244:247], a[228:231], v[224:227]\n"
        "v_exp_f32 v68, v68\n"
        "v_mfma_f32_16x16x32_bf16 v[220:223], a[240:243], a[228:231], v[220:223]\n"
        "ds_read_b128 a[228:231], v248 offset:15392\n"
        "v_mfma_f32_16x16x32_bf16 v[220:223], a[236:239], a[232:235], v[220:223]\n"
        "s_waitcnt lgkmcnt(2)\n"
        "ds_read_b128 a[232:235], v254 offset:12288\n"
        "v_mfma_f32_16x16x32_bf16 v[176:179], v[156:159], a[216:219], v[176:179]\n"
        "v_exp_f32 v115, v115\n"
        "v_mfma_f32_16x16x32_bf16 v[176:179], v[160:163], a[220:223], v[176:179]\n"
        "v_exp_f32 v69, v69\n"
        "v_mfma_f32_16x16x32_bf16 v[196:199], v[164:167], a[220:223], v[196:199]\n"
        "ds_read_b128 a[220:223], v254 offset:15360\n"
        "v_cvt_pk_bf16_f32 v93, v114, v115\n"
        "v_mfma_f32_16x16x32_bf16 v[196:199], v[152:155], a[216:219], v[196:199]\n"
        "s_waitcnt lgkmcnt(2)\n"
        "ds_read_b128 a[216:219], v254 offset:12320\n"
        "v_cvt_pk_bf16_f32 v94, v68, v69\n"
        "v_mfma_f32_16x16x32_bf16 v[200:203], v[152:155], a[224:227], v[200:203]\n"
        "v_exp_f32 v70, v70\n"
        "v_mfma_f32_16x16x32_bf16 v[200:203], v[164:167], a[228:231], v[200:203]\n"
        "s_waitcnt vmcnt(0)\n"
        "v_exp_f32 v71, v71\n"
        "v_mfma_f32_16x16x32_bf16 v[180:183], v[160:163], a[228:231], v[180:183]\n"
        "s_barrier\n"
        "ds_read_b128 a[228:231], v254 offset:15392\n"
        "v_cvt_pk_bf16_f32 v95, v70, v71\n"
        "v_mfma_f32_16x16x32_bf16 v[180:183], v[156:159], a[224:227], v[180:183]\n"
        "s_waitcnt lgkmcnt(2)\n"
        "ds_read_b128 a[224:227], v248 offset:12304\n"
        "v_mfma_f32_16x16x32_bf16 v[184:187], v[156:159], a[232:235], v[184:187]\n"
        "v_mfma_f32_16x16x32_bf16 v[184:187], v[160:163], a[220:223], v[184:187]\n"
        "v_mfma_f32_16x16x32_bf16 v[204:207], v[164:167], a[220:223], v[204:207]\n"
        "ds_read_b128 a[220:223], v248 offset:15376\n"
        "v_mfma_f32_16x16x32_bf16 v[204:207], v[152:155], a[232:235], v[204:207]\n"
        "s_waitcnt lgkmcnt(2)\n"
        "ds_read_b128 a[232:235], v254 offset:12304\n"
        "v_mfma_f32_16x16x32_bf16 v[192:195], v[152:155], a[216:219], v[192:195]\n"
        "ds_read_b128 a[236:239], v240 offset:0\n"
        "v_mfma_f32_16x16x32_bf16 v[192:195], v[164:167], a[228:231], v[192:195]\n"
        "ds_read_b128 a[240:243], v241 offset:0\n"
        "v_mfma_f32_16x16x32_bf16 v[172:175], v[160:163], a[228:231], v[172:175]\n"
        "ds_read_b128 a[228:231], v254 offset:15376\n"
        "v_mfma_f32_16x16x32_bf16 v[172:175], v[156:159], a[216:219], v[172:175]\n"
        "s_waitcnt lgkmcnt(4)\n"
        "ds_read_b128 a[244:247], v240 offset:128\n"
        "v_mfma_f32_16x16x32_bf16 v[212:215], v[156:159], a[224:227], v[212:215]\n"
        "ds_read_b128 a[248:251], v241 offset:128\n"
        "v_mfma_f32_16x16x32_bf16 v[212:215], v[160:163], a[220:223], v[212:215]\n"
        "ds_read_b128 a[216:219], v240 offset:256\n"
        "v_mfma_f32_16x16x32_bf16 v[216:219], v[164:167], a[220:223], v[216:219]\n"
        "ds_read_b128 a[220:223], v241 offset:256\n"
        "v_mfma_f32_16x16x32_bf16 v[216:219], v[152:155], a[224:227], v[216:219]\n"
        "s_waitcnt lgkmcnt(4)\n"
        "v_mfma_f32_16x16x32_bf16 v[224:227], v[152:155], a[232:235], v[224:227]\n"
        "v_mfma_f32_16x16x32_bf16 v[224:227], v[164:167], a[228:231], v[224:227]\n"
        "ds_write_b32 v245, v96 offset:4608\n"
        "v_mfma_f32_16x16x32_bf16 v[220:223], v[160:163], a[228:231], v[220:223]\n"
        "v_mfma_f32_16x16x32_bf16 v[220:223], v[156:159], a[232:235], v[220:223]\n"
        "ds_write_b32 v245, v97 offset:5632\n"
        "v_mfma_f32_16x16x32_bf16 v[112:115], a[72:75], a[236:239], 0\n"
        "v_mov_b64 v[124:125], 0\n"
        "v_cvt_pk_bf16_f32 v232, v176, v180\n"
        "v_mfma_f32_16x16x32_bf16 v[112:115], a[76:79], a[240:243], v[112:115]\n"
        "s_waitcnt lgkmcnt(2)\n"
        "v_cvt_pk_bf16_f32 v233, v184, v172\n"
        "v_cvt_pk_bf16_f32 v234, v177, v181\n"
        "v_mfma_f32_16x16x32_bf16 v[112:115], a[80:83], a[244:247], v[112:115]\n"
        "buffer_store_dwordx2 v[232:233], v250, s[56:59], 0 offen offset:0 sc1\n"
        "v_cvt_pk_bf16_f32 v235, v185, v173\n"
        "v_mfma_f32_16x16x32_bf16 v[112:115], a[84:87], a[248:251], v[112:115]\n"
        "v_cvt_pk_bf16_f32 v232, v178, v182\n"
        "v_cvt_pk_bf16_f32 v233, v186, v174\n"
        "v_mfma_f32_16x16x32_bf16 v[112:115], a[88:91], a[216:219], v[112:115]\n"
        "v_mfma_f32_16x16x32_bf16 v[112:115], a[92:95], a[220:223], v[112:115]\n"
        "ds_write_b32 v245, v100 offset:0\n"
        "v_smfmac_f32_16x16x64_bf16 v[124:127], a[252:255], v[88:95], v237\n"
        "s_nop 1\n"
        "v_mfma_f32_16x16x32_bf16 v[80:83], v[132:135], v[92:95], 0\n"
        "buffer_store_dwordx2 v[234:235], v250, s[56:59], 0 offen offset:384 sc1\n"
        "s_nop 0\n"
        "v_mfma_f32_16x16x32_bf16 v[80:83], v[128:131], v[88:91], v[80:83]\n"
        "s_add_u32 s41, s30, s40\n"
        "v_cvt_pk_bf16_f32 v152, v112, v113\n"
        "v_cvt_pk_bf16_f32 v153, v114, v115\n"
        "v_mfma_f32_16x16x32_bf16 v[84:87], v[136:139], v[88:91], 0\n"
        "v_cvt_pk_bf16_f32 v234, v179, v183\n"
        "v_cvt_pk_bf16_f32 v235, v187, v175\n"
        "v_mfma_f32_16x16x32_bf16 v[84:87], v[140:143], v[92:95], v[84:87]\n"
        "v_add_f32 v125, v124, v125\n"
        "v_mfma_f32_16x16x32_bf16 v[96:99], v[148:151], v[92:95], 0\n"
        "buffer_store_dwordx2 v[232:233], v250, s[56:59], 0 offen offset:768 sc1\n"
        "v_mfma_f32_16x16x32_bf16 v[96:99], v[144:147], v[88:91], v[96:99]\n"
        "s_add_u32 s35, s47, 786432\n"
        "v_rcp_f32 v124, v125\n"
        "v_mfma_f32_16x16x32_bf16 v[116:119], a[116:119], a[220:223], 0\n"
        "s_sub_u32 s46, s35, 100663296\n"
        "v_mul_f32 v80, v124, v80\n"
        "v_mul_f32 v81, v124, v81\n"
        "v_mfma_f32_16x16x32_bf16 v[116:119], a[112:115], a[216:219], v[116:119]\n"
        "s_cmp_lt_u32 s46, 786432\n"
        "v_mul_f32 v82, v124, v82\n"
        "v_mul_f32 v83, v124, v83\n"
        "v_mfma_f32_16x16x32_bf16 v[116:119], a[108:111], a[248:251], v[116:119]\n"
        "buffer_store_dwordx2 v[234:235], v250, s[56:59], 0 offen offset:1152 sc1\n"
        "v_cvt_pk_bf16_f32 v80, v80, v81\n"
        "v_mfma_f32_16x16x32_bf16 v[116:119], a[104:107], a[244:247], v[116:119]\n"
        "s_cselect_b32 s35, s46, s35\n"
        "v_cvt_pk_bf16_f32 v81, v82, v83\n"
        "v_cvt_pk_bf16_f32 v232, v196, v200\n"
        "v_mfma_f32_16x16x32_bf16 v[116:119], a[100:103], a[240:243], v[116:119]\n"
        "v_cvt_pk_bf16_f32 v233, v204, v192\n"
        "v_mul_f32 v84, v124, v84\n"
        "v_mfma_f32_16x16x32_bf16 v[116:119], a[96:99], a[236:239], v[116:119]\n"
        "v_mul_f32 v85, v124, v85\n"
        "v_mul_f32 v86, v124, v86\n"
        "v_mfma_f32_16x16x32_bf16 v[120:123], a[120:123], a[236:239], 0\n"
        "ds_write_b32 v245, v101 offset:1024\n"
        "v_mfma_f32_16x16x32_bf16 v[120:123], a[124:127], a[240:243], v[120:123]\n"
        "buffer_store_dwordx2 v[232:233], v250, s[60:63], 0 offen offset:0 sc1\n"
        "v_mul_f32 v87, v124, v87\n"
        "v_mfma_f32_16x16x32_bf16 v[120:123], a[128:131], a[244:247], v[120:123]\n"
        "v_cvt_pk_bf16_f32 v82, v84, v85\n"
        "v_cvt_pk_bf16_f32 v83, v86, v87\n"
        "v_mfma_f32_16x16x32_bf16 v[120:123], a[132:135], a[248:251], v[120:123]\n"
        "v_mul_f32 v96, v124, v96\n"
        "v_mul_f32 v97, v124, v97\n"
        "v_mfma_f32_16x16x32_bf16 v[120:123], a[136:139], a[216:219], v[120:123]\n"
        "v_mul_f32 v98, v124, v98\n"
        "v_mul_f32 v99, v124, v99\n"
        "v_mfma_f32_16x16x32_bf16 v[120:123], a[140:143], a[220:223], v[120:123]\n"
        "v_cvt_pk_bf16_f32 v234, v197, v201\n"
        "v_cvt_pk_bf16_f32 v235, v205, v193\n"
        "v_mfma_f32_16x16x32_bf16 v[112:115], a[20:23], a[220:223], 0\n"
        "buffer_store_dwordx2 v[234:235], v250, s[60:63], 0 offen offset:384 sc1\n"
        "v_cvt_pk_bf16_f32 v154, v116, v117\n"
        "v_mfma_f32_16x16x32_bf16 v[112:115], a[16:19], a[216:219], v[112:115]\n"
        "v_cvt_pk_bf16_f32 v155, v118, v119\n"
        "v_cvt_pk_bf16_f32 v96, v96, v97\n"
        "v_mfma_f32_16x16x32_bf16 v[112:115], a[12:15], a[248:251], v[112:115]\n"
        "ds_write_b32 v245, v96 offset:4864\n"
        "v_mfma_f32_16x16x32_bf16 v[112:115], a[8:11], a[244:247], v[112:115]\n"
        "v_cvt_pk_bf16_f32 v97, v98, v99\n"
        "v_max3_u32 v236, v236, v124, v125\n"
        "v_mfma_f32_16x16x32_bf16 v[112:115], a[4:7], a[240:243], v[112:115]\n"
        "v_cvt_pk_bf16_f32 v232, v198, v202\n"
        "v_cvt_pk_bf16_f32 v233, v206, v194\n"
        "v_mfma_f32_16x16x32_bf16 v[112:115], a[0:3], a[236:239], v[112:115]\n"
        "buffer_store_dwordx2 v[232:233], v250, s[60:63], 0 offen offset:768 sc1\n"
        "v_cmp_lt_u32 vcc, s24, v236\n"
        "v_mfma_f32_16x16x32_bf16 v[116:119], a[24:27], a[236:239], 0\n"
        "v_cvt_pk_bf16_f32 v168, v120, v121\n"
        "v_cvt_pk_bf16_f32 v169, v122, v123\n"
        "v_mfma_f32_16x16x32_bf16 v[116:119], a[28:31], a[240:243], v[116:119]\n"
        "s_cmp_lg_u64 vcc, 0\n"
        "s_addc_u32 s25, s25, s25\n"
        "v_cvt_pk_bf16_f32 v234, v199, v203\n"
        "v_mfma_f32_16x16x32_bf16 v[116:119], a[32:35], a[244:247], v[116:119]\n"
        "v_cvt_pk_bf16_f32 v235, v207, v195\n"
        "v_cvt_pk_bf16_f32 v156, v112, v113\n"
        "v_mfma_f32_16x16x32_bf16 v[116:119], a[36:39], a[248:251], v[116:119]\n"
        "buffer_store_dwordx2 v[234:235], v250, s[60:63], 0 offen offset:1152 sc1\n"
        "v_cvt_pk_bf16_f32 v157, v114, v115\n"
        "v_mfma_f32_16x16x32_bf16 v[116:119], a[40:43], a[216:219], v[116:119]\n"
        "v_cvt_pk_bf16_f32 v232, v212, v220\n"
        "v_cvt_pk_bf16_f32 v234, v213, v221\n"
        "v_mfma_f32_16x16x32_bf16 v[116:119], a[44:47], a[220:223], v[116:119]\n"
        "ds_write_b32 v245, v102 offset:2048\n"
        "v_mfma_f32_16x16x32_bf16 v[120:123], a[68:71], a[220:223], 0\n"
        "v_xor_b32 v251, 0x2000, v251\n"
        "v_xor_b32 v252, 0x2000, v252\n"
        "v_mfma_f32_16x16x32_bf16 v[120:123], a[64:67], a[216:219], v[120:123]\n"
        "buffer_store_dword v232, v238, s[56:59], 0 offen offset:0 sc1\n"
        "v_mfma_f32_16x16x32_bf16 v[120:123], a[60:63], a[248:251], v[120:123]\n"
        "ds_read_b128 a[224:227], v240 offset:1536\n"
        "v_cvt_pk_bf16_f32 v158, v116, v117\n"
        "v_mfma_f32_16x16x32_bf16 v[120:123], a[56:59], a[244:247], v[120:123]\n"
        "ds_read_b128 a[228:231], v241 offset:1536\n"
        "v_cvt_pk_bf16_f32 v159, v118, v119\n"
        "v_mfma_f32_16x16x32_bf16 v[120:123], a[52:55], a[240:243], v[120:123]\n"
        "ds_read_b128 a[232:235], v240 offset:1664\n"
        "v_cvt_pk_bf16_f32 v232, v214, v222\n"
        "v_mfma_f32_16x16x32_bf16 v[120:123], a[48:51], a[236:239], v[120:123]\n"
        "buffer_store_dword v234, v238, s[56:59], 0 offen offset:384 sc1\n"
        "v_mfma_f32_16x16x32_bf16 v[112:115], a[236:239], a[144:147], 0\n"
        "v_add_u32 v247, s45, v247\n"
        "v_cvt_pk_bf16_f32 v234, v215, v223\n"
        "v_mfma_f32_16x16x32_bf16 v[112:115], a[240:243], a[148:151], v[112:115]\n"
        "ds_write_b32 v245, v103 offset:3072\n"
        "v_mfma_f32_16x16x32_bf16 v[112:115], a[244:247], a[152:155], v[112:115]\n"
        "s_nop 0\n"
        "v_cvt_pk_bf16_f32 v160, v120, v121\n"
        "v_cvt_pk_bf16_f32 v161, v122, v123\n"
        "v_mfma_f32_16x16x32_bf16 v[112:115], a[248:251], a[156:159], v[112:115]\n"
        "v_mov_b64 v[166:167], v[160:161]\n"
        "v_mfma_f32_16x16x32_bf16 v[112:115], a[216:219], a[160:163], v[112:115]\n"
        "buffer_store_dword v232, v238, s[56:59], 0 offen offset:768 sc1\n"
        "v_mfma_f32_16x16x32_bf16 v[112:115], a[220:223], a[164:167], v[112:115]\n"
        "ds_write_b32 v245, v97 offset:5888\n"
        "v_mfma_f32_16x16x32_bf16 v[116:119], a[220:223], a[188:191], 0\n"
        "v_cvt_pk_bf16_f32 v232, v216, v224\n"
        "s_nop 0\n"
        "v_mfma_f32_16x16x32_bf16 v[116:119], a[216:219], a[184:187], v[116:119]\n"
        "s_nop 2\n"
        "v_mfma_f32_16x16x32_bf16 v[116:119], a[248:251], a[180:183], v[116:119]\n"
        "buffer_store_dword v234, v238, s[56:59], 0 offen offset:1152 sc1\n"
        "v_mfma_f32_16x16x32_bf16 v[116:119], a[244:247], a[176:179], v[116:119]\n"
        "v_cvt_pk_bf16_f32 v128, v112, v113\n"
        "v_cvt_pk_bf16_f32 v129, v114, v115\n"
        "v_mfma_f32_16x16x32_bf16 v[116:119], a[240:243], a[172:175], v[116:119]\n"
        "ds_write_b32 v245, v64 offset:256\n"
        "v_mfma_f32_16x16x32_bf16 v[116:119], a[236:239], a[168:171], v[116:119]\n"
        "v_cvt_pk_bf16_f32 v234, v217, v225\n"
        "s_nop 0\n"
        "v_mfma_f32_16x16x32_bf16 v[120:123], a[236:239], a[192:195], 0\n"
        "ds_read_b128 a[236:239], v241 offset:1664\n"
        "s_nop 0\n"
        "v_mfma_f32_16x16x32_bf16 v[120:123], a[240:243], a[196:199], v[120:123]\n"
        "buffer_store_dword v232, v238, s[60:63], 0 offen offset:0 sc1\n"
        "v_mfma_f32_16x16x32_bf16 v[120:123], a[244:247], a[200:203], v[120:123]\n"
        "ds_read_b128 a[240:243], v240 offset:1792\n"
        "ds_read_b128 a[244:247], v241 offset:1792\n"
        "v_mfma_f32_16x16x32_bf16 v[120:123], a[248:251], a[204:207], v[120:123]\n"
        "v_cvt_pk_bf16_f32 v136, v116, v117\n"
        "v_cvt_pk_bf16_f32 v137, v118, v119\n"
        "v_mfma_f32_16x16x32_bf16 v[120:123], a[216:219], a[208:211], v[120:123]\n"
        "v_cvt_pk_bf16_f32 v232, v218, v226\n"
        "v_mfma_f32_16x16x32_bf16 v[120:123], a[220:223], a[212:215], v[120:123]\n"
        "s_waitcnt lgkmcnt(4)\n"
        "buffer_store_dword v234, v238, s[60:63], 0 offen offset:384 sc1\n"
        "v_mfma_f32_16x16x32_bf16 v[112:115], a[72:75], a[224:227], 0\n"
        "s_nop 2\n"
        "v_mfma_f32_16x16x32_bf16 v[112:115], a[76:79], a[228:231], v[112:115]\n"
        "v_cvt_pk_bf16_f32 v234, v219, v227\n"
        "s_nop 0\n"
        "v_mfma_f32_16x16x32_bf16 v[112:115], a[80:83], a[232:235], v[112:115]\n"
        "s_waitcnt lgkmcnt(0)\n"
        "v_cvt_pk_bf16_f32 v144, v120, v121\n"
        "v_cvt_pk_bf16_f32 v145, v122, v123\n"
        "v_mfma_f32_16x16x32_bf16 v[112:115], a[84:87], a[236:239], v[112:115]\n"
        "ds_write_b32 v245, v65 offset:1280\n"
        "v_mfma_f32_16x16x32_bf16 v[112:115], a[88:91], a[240:243], v[112:115]\n"
        "buffer_store_dword v232, v238, s[60:63], 0 offen offset:768 sc1\n"
        "v_mfma_f32_16x16x32_bf16 v[112:115], a[92:95], a[244:247], v[112:115]\n"
        "s_nop 2\n"
        "v_mfma_f32_16x16x32_bf16 v[116:119], a[116:119], a[244:247], 0\n"
        "s_nop 2\n"
        "v_mfma_f32_16x16x32_bf16 v[116:119], a[112:115], a[240:243], v[116:119]\n"
        "v_cvt_pk_bf16_f32 v172, v112, v113\n"
        "v_cvt_pk_bf16_f32 v173, v114, v115\n"
        "v_mfma_f32_16x16x32_bf16 v[116:119], a[108:111], a[236:239], v[116:119]\n"
        "buffer_store_dword v234, v238, s[60:63], 0 offen offset:1152 sc1\n"
        "v_mfma_f32_16x16x32_bf16 v[116:119], a[104:107], a[232:235], v[116:119]\n"
        "ds_write_b32 v245, v66 offset:2304\n"
        "v_mfma_f32_16x16x32_bf16 v[116:119], a[100:103], a[228:231], v[116:119]\n"
        "v_mfma_f32_16x16x32_bf16 v[116:119], a[96:99], a[224:227], v[116:119]\n"
        "s_mov_b32 m0, s48\n"
        "s_nop 1\n"
        "v_mfma_f32_16x16x32_bf16 v[120:123], a[120:123], a[224:227], 0\n"
        "ds_read_b128 v[162:165], v251 offset:0\n"
        "s_nop 0\n"
        "v_mfma_f32_16x16x32_bf16 v[120:123], a[124:127], a[228:231], v[120:123]\n"
        "buffer_load_dwordx4 v249, s[12:15], s36 offen offset:0 lds\n"
        "v_mfma_f32_16x16x32_bf16 v[120:123], a[128:131], a[232:235], v[120:123]\n"
        "v_cvt_pk_bf16_f32 v174, v116, v117\n"
        "v_cvt_pk_bf16_f32 v175, v118, v119\n"
        "v_mfma_f32_16x16x32_bf16 v[120:123], a[132:135], a[236:239], v[120:123]\n"
        "ds_write_b32 v245, v67 offset:3328\n"
        "v_mfma_f32_16x16x32_bf16 v[120:123], a[136:139], a[240:243], v[120:123]\n"
        "ds_write_b32 v245, v72 offset:512\n"
        "v_mfma_f32_16x16x32_bf16 v[120:123], a[140:143], a[244:247], v[120:123]\n"
        "s_nop 2\n"
        "v_mfma_f32_16x16x32_bf16 v[112:115], a[20:23], a[244:247], 0\n"
        "s_nop 2\n"
        "v_mfma_f32_16x16x32_bf16 v[112:115], a[16:19], a[240:243], v[112:115]\n"
        "v_cvt_pk_bf16_f32 v190, v120, v121\n"
        "v_cvt_pk_bf16_f32 v191, v122, v123\n"
        "v_mfma_f32_16x16x32_bf16 v[112:115], a[12:15], a[236:239], v[112:115]\n"
        ".long 0xbec200ff\n"
        "46:\n"
        "s_branch 61b\n"
        "ds_write_b32 v245, v73 offset:1536\n"
        "v_mfma_f32_16x16x32_bf16 v[112:115], a[8:11], a[232:235], v[112:115]\n"
        "ds_write_b32 v245, v74 offset:2560\n"
        "v_mfma_f32_16x16x32_bf16 v[112:115], a[4:7], a[228:231], v[112:115]\n"
        "ds_read_b128 v[182:185], v251 offset:512\n"
        "v_mfma_f32_16x16x32_bf16 v[112:115], a[0:3], a[224:227], v[112:115]\n"
        "s_nop 2\n"
        "v_mfma_f32_16x16x32_bf16 v[116:119], a[24:27], a[224:227], 0\n"
        "s_nop 2\n"
        "v_mfma_f32_16x16x32_bf16 v[116:119], a[28:31], a[228:231], v[116:119]\n"
        "v_cvt_pk_bf16_f32 v176, v112, v113\n"
        "v_cvt_pk_bf16_f32 v177, v114, v115\n"
        "v_mfma_f32_16x16x32_bf16 v[116:119], a[32:35], a[232:235], v[116:119]\n"
        "ds_write_b32 v245, v75 offset:3584\n"
        "v_mfma_f32_16x16x32_bf16 v[116:119], a[36:39], a[236:239], v[116:119]\n"
        "buffer_load_dwordx4 v253, s[12:15], s36 offen offset:1024 lds\n"
        "v_mfma_f32_16x16x32_bf16 v[116:119], a[40:43], a[240:243], v[116:119]\n"
        "ds_write_b32 v245, v80 offset:768\n"
        "v_mfma_f32_16x16x32_bf16 v[116:119], a[44:47], a[244:247], v[116:119]\n"
        "s_nop 2\n"
        "v_mfma_f32_16x16x32_bf16 v[120:123], a[68:71], a[244:247], 0\n"
        "s_nop 2\n"
        "v_mfma_f32_16x16x32_bf16 v[120:123], a[64:67], a[240:243], v[120:123]\n"
        "v_cvt_pk_bf16_f32 v178, v116, v117\n"
        "v_cvt_pk_bf16_f32 v179, v118, v119\n"
        "v_mfma_f32_16x16x32_bf16 v[120:123], a[60:63], a[236:239], v[120:123]\n"
        "ds_write_b32 v245, v81 offset:1792\n"
        "v_mfma_f32_16x16x32_bf16 v[120:123], a[56:59], a[232:235], v[120:123]\n"
        "ds_write_b32 v245, v82 offset:2816\n"
        "v_mfma_f32_16x16x32_bf16 v[120:123], a[52:55], a[228:231], v[120:123]\n"
        "ds_write_b32 v245, v83 offset:3840\n"
        "v_mfma_f32_16x16x32_bf16 v[120:123], a[48:51], a[224:227], v[120:123]\n"
        "s_nop 2\n"
        "v_mfma_f32_16x16x32_bf16 v[112:115], a[224:227], a[144:147], 0\n"
        "s_nop 2\n"
        "v_mfma_f32_16x16x32_bf16 v[112:115], a[228:231], a[148:151], v[112:115]\n"
        "v_cvt_pk_bf16_f32 v180, v120, v121\n"
        "v_cvt_pk_bf16_f32 v181, v122, v123\n"
        "v_mfma_f32_16x16x32_bf16 v[112:115], a[232:235], a[152:155], v[112:115]\n"
        "v_mov_b64 v[186:187], v[180:181]\n"
        "v_mfma_f32_16x16x32_bf16 v[112:115], a[236:239], a[156:159], v[112:115]\n"
        "s_add_u32 m0, s41, 0\n"
        "v_mfma_f32_16x16x32_bf16 v[112:115], a[240:243], a[160:163], v[112:115]\n"
        "ds_read_b128 a[248:251], v240 offset:12288\n"
        "v_mfma_f32_16x16x32_bf16 v[112:115], a[244:247], a[164:167], v[112:115]\n"
        "buffer_load_dwordx4 v242, s[8:11], s47 offen lds\n"
        "v_mfma_f32_16x16x32_bf16 v[96:99], v[152:155], v[156:159], v[0:3]\n"
        "s_waitcnt lgkmcnt(2)\n"
        "ds_read_b128 a[216:219], v241 offset:12288\n"
        "s_nop 0\n"
        "v_mfma_f32_16x16x32_bf16 v[96:99], v[168:171], v[160:163], v[96:99]\n"
        "ds_read_b128 a[220:223], v240 offset:12416\n"
        "s_nop 0\n"
        "v_mfma_f32_16x16x32_bf16 v[100:103], v[172:175], v[156:159], v[4:7]\n"
        "s_sub_u32 s31, s30, s28\n"
        "v_cvt_pk_bf16_f32 v130, v112, v113\n"
        "v_cvt_pk_bf16_f32 v131, v114, v115\n"
        "v_mfma_f32_16x16x32_bf16 v[100:103], v[188:191], v[164:167], v[100:103]\n"
        "s_sub_u32 s45, s28, s29\n"
        "ds_read_b128 v[162:165], v252 offset:0\n"
        "v_mfma_f32_16x16x32_bf16 v[116:119], a[244:247], a[188:191], 0\n"
        "s_add_u32 s28, s28, s31\n"
        "v_exp_f32 v96, v96\n"
        "v_mfma_f32_16x16x32_bf16 v[116:119], a[240:243], a[184:187], v[116:119]\n"
        "s_mov_b32 s30, s29\n"
        "v_exp_f32 v97, v97\n"
        "v_mfma_f32_16x16x32_bf16 v[116:119], a[236:239], a[180:183], v[116:119]\n"
        "s_add_u32 s29, s29, s45\n"
        "v_exp_f32 v98, v98\n"
        "v_mfma_f32_16x16x32_bf16 v[116:119], a[232:235], a[176:179], v[116:119]\n"
        "v_exp_f32 v100, v100\n"
        "v_mfma_f32_16x16x32_bf16 v[116:119], a[228:231], a[172:175], v[116:119]\n"
        "v_exp_f32 v99, v99\n"
        "v_mfma_f32_16x16x32_bf16 v[116:119], a[224:227], a[168:171], v[116:119]\n"
        "v_exp_f32 v101, v101\n"
        "v_mfma_f32_16x16x32_bf16 v[104:107], v[152:155], v[176:179], v[16:19]\n"
        "v_exp_f32 v102, v102\n"
        "v_mfma_f32_16x16x32_bf16 v[104:107], v[168:171], v[180:183], v[104:107]\n"
        "s_add_u32 m0, s41, 1024\n"
        "v_cvt_pk_bf16_f32 v64, v96, v97\n"
        "v_cvt_pk_bf16_f32 v65, v98, v99\n"
        "v_mfma_f32_16x16x32_bf16 v[108:111], v[172:175], v[176:179], v[20:23]\n"
        "v_cvt_pk_bf16_f32 v138, v116, v117\n"
        "v_cvt_pk_bf16_f32 v139, v118, v119\n"
        "v_mfma_f32_16x16x32_bf16 v[108:111], v[188:191], v[184:187], v[108:111]\n"
        "buffer_load_dwordx4 v243, s[8:11], s47 offen lds\n"
        "v_mfma_f32_16x16x32_bf16 v[120:123], a[224:227], a[192:195], 0\n"
        "ds_read_b128 a[224:227], v241 offset:12416\n"
        "v_cvt_pk_bf16_f32 v66, v100, v101\n"
        "v_mfma_f32_16x16x32_bf16 v[120:123], a[228:231], a[196:199], v[120:123]\n"
        "ds_read_b128 a[228:231], v240 offset:12544\n"
        "v_mfma_f32_16x16x32_bf16 v[120:123], a[232:235], a[200:203], v[120:123]\n"
        "ds_read_b128 a[232:235], v241 offset:12544\n"
        "v_mfma_f32_16x16x32_bf16 v[120:123], a[236:239], a[204:207], v[120:123]\n"
        "ds_read_b128 v[202:205], v251 offset:4096\n"
        "v_mfma_f32_16x16x32_bf16 v[120:123], a[240:243], a[208:211], v[120:123]\n"
        "ds_read_b128 v[182:185], v252 offset:512\n"
        "v_mfma_f32_16x16x32_bf16 v[120:123], a[244:247], a[212:215], v[120:123]\n"
        "s_waitcnt lgkmcnt(4)\n"
        "v_exp_f32 v103, v103\n"
        "v_mfma_f32_16x16x32_bf16 v[112:115], a[0:3], a[248:251], 0\n"
        "v_cvt_pk_bf16_f32 v67, v102, v103\n"
        "s_nop 0\n"
        "v_mfma_f32_16x16x32_bf16 v[112:115], a[4:7], a[216:219], v[112:115]\n"
        "v_exp_f32 v104, v104\n"
        "v_mfma_f32_16x16x32_bf16 v[112:115], a[8:11], a[220:223], v[112:115]\n"
        "v_exp_f32 v105, v105\n"
        "v_mfma_f32_16x16x32_bf16 v[112:115], a[12:15], a[224:227], v[112:115]\n"
        "s_waitcnt lgkmcnt(0)\n"
        "v_exp_f32 v106, v106\n"
        "v_mfma_f32_16x16x32_bf16 v[112:115], a[16:19], a[228:231], v[112:115]\n"
        "v_cvt_pk_bf16_f32 v72, v104, v105\n"
        "v_cvt_pk_bf16_f32 v146, v120, v121\n"
        "v_mfma_f32_16x16x32_bf16 v[112:115], a[20:23], a[232:235], v[112:115]\n"
        "s_add_u32 m0, s41, 2048\n"
        "v_exp_f32 v108, v108\n"
        "v_mfma_f32_16x16x32_bf16 v[116:119], a[44:47], a[232:235], 0\n"
        "v_cvt_pk_bf16_f32 v147, v122, v123\n"
        "v_add_u32 v245, s45, v245\n"
        "v_mfma_f32_16x16x32_bf16 v[116:119], a[40:43], a[228:231], v[116:119]\n"
        "buffer_load_dwordx4 v244, s[8:11], s47 offen lds\n"
        "v_mfma_f32_16x16x32_bf16 v[116:119], a[36:39], a[224:227], v[116:119]\n"
        "v_exp_f32 v107, v107\n"
        "v_mfma_f32_16x16x32_bf16 v[116:119], a[32:35], a[220:223], v[116:119]\n"
        "v_exp_f32 v109, v109\n"
        "v_mfma_f32_16x16x32_bf16 v[116:119], a[28:31], a[216:219], v[116:119]\n"
        "v_cvt_pk_bf16_f32 v73, v106, v107\n"
        "v_cvt_pk_bf16_f32 v74, v108, v109\n"
        "v_mfma_f32_16x16x32_bf16 v[116:119], a[24:27], a[248:251], v[116:119]\n"
        "v_exp_f32 v110, v110\n"
        "v_mfma_f32_16x16x32_bf16 v[120:123], a[48:51], a[248:251], 0\n"
        "v_exp_f32 v111, v111\n"
        "v_mfma_f32_16x16x32_bf16 v[120:123], a[52:55], a[216:219], v[120:123]\n"
        "v_cvt_pk_bf16_f32 v75, v110, v111\n"
        "v_cvt_pk_bf16_f32 v196, v112, v113\n"
        "v_mfma_f32_16x16x32_bf16 v[120:123], a[56:59], a[220:223], v[120:123]\n"
        "v_cvt_pk_bf16_f32 v197, v114, v115\n"
        "v_cvt_pk_bf16_f32 v198, v116, v117\n"
        "v_mfma_f32_16x16x32_bf16 v[120:123], a[60:63], a[224:227], v[120:123]\n"
        "v_cvt_pk_bf16_f32 v199, v118, v119\n"
        "v_mfma_f32_16x16x32_bf16 v[120:123], a[64:67], a[228:231], v[120:123]\n"
        "v_mfma_f32_16x16x32_bf16 v[120:123], a[68:71], a[232:235], v[120:123]\n"
        "s_nop 2\n"
        "v_mfma_f32_16x16x32_bf16 v[112:115], a[92:95], a[232:235], 0\n"
        "s_nop 2\n"
        "v_mfma_f32_16x16x32_bf16 v[112:115], a[88:91], a[228:231], v[112:115]\n"
        "s_add_u32 m0, s41, 12288\n"
        "v_cvt_pk_bf16_f32 v200, v120, v121\n"
        "v_cvt_pk_bf16_f32 v201, v122, v123\n"
        "v_mfma_f32_16x16x32_bf16 v[112:115], a[84:87], a[224:227], v[112:115]\n"
        "v_mov_b64 v[206:207], v[200:201]\n"
        "v_mfma_f32_16x16x32_bf16 v[112:115], a[80:83], a[220:223], v[112:115]\n"
        "buffer_load_dwordx4 v242, s[8:11], s35 offen lds\n"
        "v_mfma_f32_16x16x32_bf16 v[112:115], a[76:79], a[216:219], v[112:115]\n"
        "v_mfma_f32_16x16x32_bf16 v[112:115], a[72:75], a[248:251], v[112:115]\n"
        "s_nop 2\n"
        "v_mfma_f32_16x16x32_bf16 v[96:99], v[152:155], v[196:199], v[32:35]\n"
        "s_nop 2\n"
        "v_mfma_f32_16x16x32_bf16 v[96:99], v[168:171], v[200:203], v[96:99]\n"
        "v_cvt_pk_bf16_f32 v192, v112, v113\n"
        "v_cvt_pk_bf16_f32 v193, v114, v115\n"
        "v_mfma_f32_16x16x32_bf16 v[100:103], v[172:175], v[196:199], v[36:39]\n"
        "v_mfma_f32_16x16x32_bf16 v[100:103], v[188:191], v[204:207], v[100:103]\n"
        "ds_read_b128 v[202:205], v252 offset:4096\n"
        "v_mfma_f32_16x16x32_bf16 v[116:119], a[96:99], a[248:251], 0\n"
        "s_cmp_eq_u32 s37, 100663296\n"
        "v_mfma_f32_16x16x32_bf16 v[116:119], a[100:103], a[216:219], v[116:119]\n"
        "s_cselect_b32 s46, 0, s37\n"
        "v_exp_f32 v96, v96\n"
        "v_mfma_f32_16x16x32_bf16 v[116:119], a[104:107], a[220:223], v[116:119]\n"
        "s_add_u32 s38, s46, s43\n"
        "v_exp_f32 v97, v97\n"
        "v_mfma_f32_16x16x32_bf16 v[116:119], a[108:111], a[224:227], v[116:119]\n"
        "s_add_u32 s39, s46, s44\n"
        "v_exp_f32 v98, v98\n"
        "v_mfma_f32_16x16x32_bf16 v[116:119], a[112:115], a[228:231], v[116:119]\n"
        "s_add_u32 s37, s37, 6291456\n"
        "v_exp_f32 v100, v100\n"
        "v_mfma_f32_16x16x32_bf16 v[116:119], a[116:119], a[232:235], v[116:119]\n"
        "s_add_u32 m0, s41, 13312\n"
        "v_exp_f32 v99, v99\n"
        "v_mfma_f32_16x16x32_bf16 v[120:123], a[140:143], a[232:235], 0\n"
        "v_exp_f32 v101, v101\n"
        "v_mfma_f32_16x16x32_bf16 v[120:123], a[136:139], a[228:231], v[120:123]\n"
        "buffer_load_dwordx4 v243, s[8:11], s35 offen lds\n"
        "v_mfma_f32_16x16x32_bf16 v[120:123], a[132:135], a[224:227], v[120:123]\n"
        "v_cvt_pk_bf16_f32 v80, v96, v97\n"
        "v_cvt_pk_bf16_f32 v194, v116, v117\n"
        "v_mfma_f32_16x16x32_bf16 v[120:123], a[128:131], a[220:223], v[120:123]\n"
        "s_mov_b64 s[58:59], s[18:19]\n"
        "v_cvt_pk_bf16_f32 v195, v118, v119\n"
        "v_cvt_pk_bf16_f32 v81, v98, v99\n"
        "v_mfma_f32_16x16x32_bf16 v[120:123], a[124:127], a[216:219], v[120:123]\n"
        "s_mov_b64 s[62:63], s[18:19]\n"
        "v_exp_f32 v102, v102\n"
        "v_mfma_f32_16x16x32_bf16 v[120:123], a[120:123], a[248:251], v[120:123]\n"
        "v_cvt_pk_bf16_f32 v82, v100, v101\n"
        "s_nop 0\n"
        "v_mfma_f32_16x16x32_bf16 v[112:115], a[248:251], a[144:147], 0\n"
        "v_exp_f32 v103, v103\n"
        "v_mfma_f32_16x16x32_bf16 v[112:115], a[216:219], a[148:151], v[112:115]\n"
        "v_cvt_pk_bf16_f32 v83, v102, v103\n"
        "s_nop 0\n"
        "v_mfma_f32_16x16x32_bf16 v[112:115], a[220:223], a[152:155], v[112:115]\n"
        "ds_read_b128 a[236:239], v240 offset:13824\n"
        "v_cvt_pk_bf16_f32 v208, v120, v121\n"
        "v_mfma_f32_16x16x32_bf16 v[112:115], a[224:227], a[156:159], v[112:115]\n"
        "ds_read_b128 a[240:243], v241 offset:13824\n"
        "v_cvt_pk_bf16_f32 v209, v122, v123\n"
        "v_mfma_f32_16x16x32_bf16 v[112:115], a[228:231], a[160:163], v[112:115]\n"
        "ds_read_b128 a[244:247], v240 offset:13952\n"
        "v_mfma_f32_16x16x32_bf16 v[112:115], a[232:235], a[164:167], v[112:115]\n"
        "s_nop 2\n"
        "v_mfma_f32_16x16x32_bf16 v[104:107], v[192:195], v[156:159], v[8:11]\n"
        "s_nop 2\n"
        "v_mfma_f32_16x16x32_bf16 v[104:107], v[208:211], v[160:163], v[104:107]\n"
        "s_add_u32 m0, s41, 14336\n"
        "v_cvt_pk_bf16_f32 v132, v112, v113\n"
        "v_cvt_pk_bf16_f32 v133, v114, v115\n"
        "v_mfma_f32_16x16x32_bf16 v[116:119], a[232:235], a[188:191], 0\n"
        "v_mfma_f32_16x16x32_bf16 v[116:119], a[228:231], a[184:187], v[116:119]\n"
        "buffer_load_dwordx4 v244, s[8:11], s35 offen lds\n"
        "v_mfma_f32_16x16x32_bf16 v[116:119], a[224:227], a[180:183], v[116:119]\n"
        "v_mfma_f32_16x16x32_bf16 v[116:119], a[220:223], a[176:179], v[116:119]\n"
        "v_exp_f32 v104, v104\n"
        "v_mfma_f32_16x16x32_bf16 v[116:119], a[216:219], a[172:175], v[116:119]\n"
        "v_exp_f32 v105, v105\n"
        "v_mfma_f32_16x16x32_bf16 v[116:119], a[248:251], a[168:171], v[116:119]\n"
        "v_exp_f32 v106, v106\n"
        "v_mfma_f32_16x16x32_bf16 v[108:111], v[192:195], v[176:179], v[24:27]\n"
        "v_exp_f32 v107, v107\n"
        "v_mfma_f32_16x16x32_bf16 v[108:111], v[208:211], v[180:183], v[108:111]\n"
        "v_cvt_pk_bf16_f32 v68, v104, v105\n"
        "v_cvt_pk_bf16_f32 v69, v106, v107\n"
        "v_mfma_f32_16x16x32_bf16 v[120:123], a[248:251], a[192:195], 0\n"
        "ds_read_b128 a[248:251], v241 offset:13952\n"
        "v_cvt_pk_bf16_f32 v140, v116, v117\n"
        "v_mfma_f32_16x16x32_bf16 v[120:123], a[216:219], a[196:199], v[120:123]\n"
        "ds_read_b128 a[216:219], v240 offset:14080\n"
        "v_cvt_pk_bf16_f32 v141, v118, v119\n"
        "v_mfma_f32_16x16x32_bf16 v[120:123], a[220:223], a[200:203], v[120:123]\n"
        "ds_read_b128 a[220:223], v241 offset:14080\n"
        "v_mfma_f32_16x16x32_bf16 v[120:123], a[224:227], a[204:207], v[120:123]\n"
        "v_exp_f32 v108, v108\n"
        "v_mfma_f32_16x16x32_bf16 v[120:123], a[228:231], a[208:211], v[120:123]\n"
        "v_exp_f32 v109, v109\n"
        "v_mfma_f32_16x16x32_bf16 v[120:123], a[232:235], a[212:215], v[120:123]\n"
        "v_exp_f32 v110, v110\n"
        "v_mfma_f32_16x16x32_bf16 v[96:99], v[192:195], v[196:199], v[40:43]\n"
        "s_waitcnt lgkmcnt(1)\n"
        "v_exp_f32 v111, v111\n"
        "v_mfma_f32_16x16x32_bf16 v[96:99], v[208:211], v[200:203], v[96:99]\n"
        "v_cvt_pk_bf16_f32 v76, v108, v109\n"
        "v_cvt_pk_bf16_f32 v77, v110, v111\n"
        "v_mfma_f32_16x16x32_bf16 v[112:115], a[72:75], a[236:239], 0\n"
        "s_nop 0\n"
        "v_cvt_pk_bf16_f32 v148, v120, v121\n"
        "v_cvt_pk_bf16_f32 v149, v122, v123\n"
        "v_mfma_f32_16x16x32_bf16 v[112:115], a[76:79], a[240:243], v[112:115]\n"
        "v_mfma_f32_16x16x32_bf16 v[112:115], a[80:83], a[244:247], v[112:115]\n"
        "v_exp_f32 v96, v96\n"
        "v_mfma_f32_16x16x32_bf16 v[112:115], a[84:87], a[248:251], v[112:115]\n"
        "v_exp_f32 v97, v97\n"
        "v_mfma_f32_16x16x32_bf16 v[112:115], a[88:91], a[216:219], v[112:115]\n"
        "s_waitcnt lgkmcnt(0)\n"
        "v_exp_f32 v98, v98\n"
        "v_mfma_f32_16x16x32_bf16 v[112:115], a[92:95], a[220:223], v[112:115]\n"
        "v_exp_f32 v99, v99\n"
        "v_mfma_f32_16x16x32_bf16 v[116:119], a[116:119], a[220:223], 0\n"
        "v_cvt_pk_bf16_f32 v84, v96, v97\n"
        "v_cvt_pk_bf16_f32 v85, v98, v99\n"
        "v_mfma_f32_16x16x32_bf16 v[116:119], a[112:115], a[216:219], v[116:119]\n"
        "v_add_u32 v240, s31, v240\n"
        "v_add_u32 v241, s31, v241\n"
        "v_mfma_f32_16x16x32_bf16 v[116:119], a[108:111], a[248:251], v[116:119]\n"
        "v_cvt_pk_bf16_f32 v212, v112, v113\n"
        "v_cvt_pk_bf16_f32 v213, v114, v115\n"
        "v_mfma_f32_16x16x32_bf16 v[116:119], a[104:107], a[244:247], v[116:119]\n"
        "s_add_u32 s56, s16, s38\n"
        "s_addc_u32 s57, s17, 0\n"
        "v_mfma_f32_16x16x32_bf16 v[116:119], a[100:103], a[240:243], v[116:119]\n"
        "s_add_u32 s60, s16, s39\n"
        "s_addc_u32 s61, s17, 0\n"
        "v_mfma_f32_16x16x32_bf16 v[116:119], a[96:99], a[236:239], v[116:119]\n"
        "s_nop 2\n"
        "v_mfma_f32_16x16x32_bf16 v[120:123], a[120:123], a[236:239], 0\n"
        "s_nop 2\n"
        "v_mfma_f32_16x16x32_bf16 v[120:123], a[124:127], a[240:243], v[120:123]\n"
        "s_add_u32 s47, s47, 6291456\n"
        "v_cvt_pk_bf16_f32 v214, v116, v117\n"
        "v_cvt_pk_bf16_f32 v215, v118, v119\n"
        "v_mfma_f32_16x16x32_bf16 v[120:123], a[128:131], a[244:247], v[120:123]\n"
        "s_add_u32 s36, s36, 2097152\n"
        "v_mfma_f32_16x16x32_bf16 v[120:123], a[132:135], a[248:251], v[120:123]\n"
        "s_xor_b32 s48, s48, 8192\n"
        "v_mfma_f32_16x16x32_bf16 v[120:123], a[136:139], a[216:219], v[120:123]\n"
        "s_barrier\n"
        "v_mfma_f32_16x16x32_bf16 v[120:123], a[140:143], a[220:223], v[120:123]\n"
        "s_nop 2\n"
        "v_mfma_f32_16x16x32_bf16 v[112:115], a[220:223], a[164:167], 0\n"
        "s_nop 2\n"
        "v_mfma_f32_16x16x32_bf16 v[112:115], a[216:219], a[160:163], v[112:115]\n"
        "v_cvt_pk_bf16_f32 v230, v120, v121\n"
        "v_cvt_pk_bf16_f32 v231, v122, v123\n"
        "v_mfma_f32_16x16x32_bf16 v[112:115], a[248:251], a[156:159], v[112:115]\n"
        "v_mfma_f32_16x16x32_bf16 v[112:115], a[244:247], a[152:155], v[112:115]\n"
        "v_mfma_f32_16x16x32_bf16 v[112:115], a[240:243], a[148:151], v[112:115]\n"
        "v_mfma_f32_16x16x32_bf16 v[112:115], a[236:239], a[144:147], v[112:115]\n"
        "s_nop 2\n"
        "v_mfma_f32_16x16x32_bf16 v[100:103], v[212:215], v[156:159], v[12:15]\n"
        "s_nop 2\n"
        "v_mfma_f32_16x16x32_bf16 v[100:103], v[228:231], v[164:167], v[100:103]\n"
        "v_cvt_pk_bf16_f32 v134, v112, v113\n"
        "v_cvt_pk_bf16_f32 v135, v114, v115\n"
        "v_mfma_f32_16x16x32_bf16 v[116:119], a[236:239], a[168:171], 0\n"
        "s_sub_u32 s27, s27, 1\n"
        "v_mfma_f32_16x16x32_bf16 v[116:119], a[240:243], a[172:175], v[116:119]\n"
        "s_cmp_lg_u32 s27, 0\n"
        "v_mfma_f32_16x16x32_bf16 v[116:119], a[244:247], a[176:179], v[116:119]\n"
        "s_cbranch_scc1 10b\n"
        "19:\n"
        "v_mfma_f32_16x16x32_bf16 v[116:119], a[248:251], a[180:183], v[116:119]\n"
        "ds_read_b128 a[224:227], v247 offset:0\n"
        "ds_read_b128 a[228:231], v247 offset:4096\n"
        "v_mfma_f32_16x16x32_bf16 v[116:119], a[216:219], a[184:187], v[116:119]\n"
        "ds_read_b128 a[232:235], v247 offset:8192\n"
        "ds_read_b128 v[156:159], v247 offset:12288\n"
        "v_mfma_f32_16x16x32_bf16 v[116:119], a[220:223], a[188:191], v[116:119]\n"
        "ds_read_b128 v[160:163], v247 offset:16384\n"
        "ds_read_b128 v[164:167], v247 offset:20480\n"
        "v_mfma_f32_16x16x32_bf16 v[104:107], v[212:215], v[176:179], v[28:31]\n"
        "s_and_b32 s46, s42, 1\n"
        "v_exp_f32 v100, v100\n"
        "v_mfma_f32_16x16x32_bf16 v[104:107], v[228:231], v[184:187], v[104:107]\n"
        "s_lshl_b32 s31, s46, 8\n"
        "v_exp_f32 v101, v101\n"
        "v_mfma_f32_16x16x32_bf16 v[120:123], a[220:223], a[212:215], 0\n"
        "s_sub_u32 s31, s45, s31\n"
        "v_cvt_pk_bf16_f32 v142, v116, v117\n"
        "v_cvt_pk_bf16_f32 v143, v118, v119\n"
        "v_mfma_f32_16x16x32_bf16 v[120:123], a[216:219], a[208:211], v[120:123]\n"
        "ds_read_b128 a[96:99], v248 offset:32\n"
        "v_cvt_pk_bf16_f32 v70, v100, v101\n"
        "v_mfma_f32_16x16x32_bf16 v[120:123], a[248:251], a[204:207], v[120:123]\n"
        "ds_read_b128 a[100:103], v248 offset:3104\n"
        "v_mov_b32 v241, 0\n"
        "v_mfma_f32_16x16x32_bf16 v[120:123], a[244:247], a[200:203], v[120:123]\n"
        "ds_read_b128 a[104:107], v248 offset:6176\n"
        "v_mfma_f32_16x16x32_bf16 v[120:123], a[240:243], a[196:199], v[120:123]\n"
        "ds_read_b128 a[108:111], v248 offset:9248\n"
        "v_mfma_f32_16x16x32_bf16 v[120:123], a[236:239], a[192:195], v[120:123]\n"
        "ds_read_b128 a[112:115], v248 offset:12320\n"
        "s_nop 0\n"
        "v_mfma_f32_16x16x32_bf16 v[108:111], v[212:215], v[196:199], v[44:47]\n"
        "ds_read_b128 a[116:119], v248 offset:15392\n"
        "s_nop 0\n"
        "v_mfma_f32_16x16x32_bf16 v[108:111], v[228:231], v[204:207], v[108:111]\n"
        "ds_read_b128 a[92:95], v248 offset:15360\n"
        "s_nop 0\n"
        "v_mfma_f32_16x16x32_bf16 v[116:119], a[0:3], a[236:239], 0\n"
        "ds_read_b128 a[88:91], v248 offset:12288\n"
        "v_cvt_pk_bf16_f32 v150, v120, v121\n"
        "v_mfma_f32_16x16x32_bf16 v[116:119], a[4:7], a[240:243], v[116:119]\n"
        "ds_read_b128 a[84:87], v248 offset:9216\n"
        "v_cvt_pk_bf16_f32 v151, v122, v123\n"
        "v_mfma_f32_16x16x32_bf16 v[116:119], a[8:11], a[244:247], v[116:119]\n"
        "ds_read_b128 a[80:83], v248 offset:6144\n"
        "v_mfma_f32_16x16x32_bf16 v[116:119], a[12:15], a[248:251], v[116:119]\n"
        "ds_read_b128 a[76:79], v248 offset:3072\n"
        "v_mfma_f32_16x16x32_bf16 v[116:119], a[16:19], a[216:219], v[116:119]\n"
        "ds_read_b128 a[72:75], v248 offset:0\n"
        "v_mfma_f32_16x16x32_bf16 v[116:119], a[20:23], a[220:223], v[116:119]\n"
        "s_waitcnt lgkmcnt(6)\n"
        "ds_read_b128 a[120:123], v254 offset:0\n"
        "v_mfma_f32_16x16x32_bf16 v[4:7], a[224:227], a[96:99], 0\n"
        "ds_read_b128 a[124:127], v254 offset:3072\n"
        "v_mfma_f32_16x16x32_bf16 v[4:7], a[228:231], a[100:103], v[4:7]\n"
        "ds_read_b128 a[128:131], v254 offset:6144\n"
        "v_mfma_f32_16x16x32_bf16 v[4:7], a[232:235], a[104:107], v[4:7]\n"
        "ds_read_b128 a[132:135], v254 offset:9216\n"
        "s_nop 0\n"
        "v_mfma_f32_16x16x32_bf16 v[4:7], v[156:159], a[108:111], v[4:7]\n"
        "ds_read_b128 a[136:139], v254 offset:12288\n"
        "v_cvt_pk_bf16_f32 v216, v116, v117\n"
        "v_mfma_f32_16x16x32_bf16 v[4:7], v[160:163], a[112:115], v[4:7]\n"
        "ds_read_b128 a[140:143], v254 offset:15360\n"
        "v_cvt_pk_bf16_f32 v217, v118, v119\n"
        "v_mfma_f32_16x16x32_bf16 v[4:7], v[164:167], a[116:119], v[4:7]\n"
        "s_waitcnt lgkmcnt(6)\n"
        "v_exp_f32 v102, v102\n"
        "v_mfma_f32_16x16x32_bf16 v[0:3], v[164:167], a[92:95], 0\n"
        "s_mul_i32 s45, s46, 786432\n"
        "v_mov_b64 v[96:97], 0\n"
        "v_mfma_f32_16x16x32_bf16 v[0:3], v[160:163], a[88:91], v[0:3]\n"
        "s_sub_u32 s37, s37, s45\n"
        "v_exp_f32 v103, v103\n"
        "v_mfma_f32_16x16x32_bf16 v[0:3], v[156:159], a[84:87], v[0:3]\n"
        "s_cmp_eq_u32 s46, 0\n"
        "s_cselect_b32 s43, s43, s44\n"
        "v_cvt_pk_bf16_f32 v71, v102, v103\n"
        "v_mfma_f32_16x16x32_bf16 v[0:3], a[232:235], a[80:83], v[0:3]\n"
        "s_add_u32 s28, s37, 786432\n"
        "v_exp_f32 v104, v104\n"
        "v_mfma_f32_16x16x32_bf16 v[0:3], a[228:231], a[76:79], v[0:3]\n"
        "s_cmp_eq_u32 s28, 100663296\n"
        "s_cselect_b32 s28, 0, s28\n"
        "v_mfma_f32_16x16x32_bf16 v[0:3], a[224:227], a[72:75], v[0:3]\n"
        "s_waitcnt lgkmcnt(0)\n"
        "v_exp_f32 v105, v105\n"
        "v_mfma_f32_16x16x32_bf16 v[8:11], a[224:227], a[120:123], 0\n"
        "s_lshl_b32 s29, s42, 2\n"
        "v_exp_f32 v106, v106\n"
        "v_mfma_f32_16x16x32_bf16 v[8:11], a[228:231], a[124:127], v[8:11]\n"
        "s_add_u32 s47, s37, s43\n"
        "v_exp_f32 v107, v107\n"
        "v_mfma_f32_16x16x32_bf16 v[8:11], a[232:235], a[128:131], v[8:11]\n"
        "s_add_u32 s35, s28, s43\n"
        "v_exp_f32 v108, v108\n"
        "v_mfma_f32_16x16x32_bf16 v[8:11], v[156:159], a[132:135], v[8:11]\n"
        "v_exp_f32 v109, v109\n"
        "v_mfma_f32_16x16x32_bf16 v[8:11], v[160:163], a[136:139], v[8:11]\n"
        "v_exp_f32 v110, v110\n"
        "v_mfma_f32_16x16x32_bf16 v[8:11], v[164:167], a[140:143], v[8:11]\n"
        "v_exp_f32 v111, v111\n"
        "v_smfmac_f32_16x16x64_bf16 v[96:99], a[252:255], v[64:71], v237\n"
        "ds_read_b128 a[164:167], v254 offset:15392\n"
        "v_mfma_f32_16x16x32_bf16 v[100:103], v[132:135], v[68:71], 0\n"
        "ds_read_b128 a[160:163], v254 offset:12320\n"
        "v_cvt_pk_bf16_f32 v78, v104, v105\n"
        "v_mfma_f32_16x16x32_bf16 v[100:103], v[128:131], v[64:67], v[100:103]\n"
        "ds_read_b128 a[156:159], v254 offset:9248\n"
        "v_cvt_pk_bf16_f32 v79, v106, v107\n"
        "v_mfma_f32_16x16x32_bf16 v[104:107], v[136:139], v[64:67], 0\n"
        "ds_read_b128 a[152:155], v254 offset:6176\n"
        "v_cvt_pk_bf16_f32 v86, v108, v109\n"
        "v_mfma_f32_16x16x32_bf16 v[104:107], v[140:143], v[68:71], v[104:107]\n"
        "ds_read_b128 a[148:151], v254 offset:3104\n"
        "v_cvt_pk_bf16_f32 v87, v110, v111\n"
        "v_mfma_f32_16x16x32_bf16 v[108:111], v[148:151], v[68:71], 0\n"
        "ds_read_b128 a[144:147], v254 offset:32\n"
        "v_add_f32 v97, v96, v97\n"
        "v_mfma_f32_16x16x32_bf16 v[108:111], v[144:147], v[64:67], v[108:111]\n"
        "v_rcp_f32 v96, v97\n"
        "v_mfma_f32_16x16x32_bf16 v[120:123], a[44:47], a[220:223], 0\n"
        "v_mul_f32 v100, v96, v100\n"
        "v_mul_f32 v101, v96, v101\n"
        "v_mfma_f32_16x16x32_bf16 v[120:123], a[40:43], a[216:219], v[120:123]\n"
        "ds_read_b128 v[176:179], v247 offset:256\n"
        "v_mul_f32 v102, v96, v102\n"
        "v_mfma_f32_16x16x32_bf16 v[120:123], a[36:39], a[248:251], v[120:123]\n"
        "ds_read_b128 v[180:183], v247 offset:4352\n"
        "v_mul_f32 v103, v96, v103\n"
        "v_mfma_f32_16x16x32_bf16 v[120:123], a[32:35], a[244:247], v[120:123]\n"
        "ds_read_b128 v[184:187], v247 offset:8448\n"
        "v_cvt_pk_bf16_f32 v100, v100, v101\n"
        "v_mfma_f32_16x16x32_bf16 v[120:123], a[28:31], a[240:243], v[120:123]\n"
        "ds_read_b128 v[196:199], v247 offset:12544\n"
        "v_cvt_pk_bf16_f32 v232, v0, v4\n"
        "v_mfma_f32_16x16x32_bf16 v[120:123], a[24:27], a[236:239], v[120:123]\n"
        "s_waitcnt lgkmcnt(4)\n"
        "ds_read_b128 v[200:203], v247 offset:16640\n"
        "v_cvt_pk_bf16_f32 v101, v102, v103\n"
        "v_mfma_f32_16x16x32_bf16 v[12:15], v[164:167], a[164:167], 0\n"
        "ds_read_b128 v[204:207], v247 offset:20736\n"
        "v_mov_b64 v[124:125], 0\n"
        "v_mfma_f32_16x16x32_bf16 v[12:15], v[160:163], a[160:163], v[12:15]\n"
        "ds_write_b32 v245, v100 offset:0\n"
        "v_mfma_f32_16x16x32_bf16 v[12:15], v[156:159], a[156:159], v[12:15]\n"
        "ds_write_b32 v245, v101 offset:1024\n"
        "v_mfma_f32_16x16x32_bf16 v[12:15], a[232:235], a[152:155], v[12:15]\n"
        "v_cvt_pk_bf16_f32 v218, v120, v121\n"
        "v_cvt_pk_bf16_f32 v219, v122, v123\n"
        "v_mfma_f32_16x16x32_bf16 v[12:15], a[228:231], a[148:151], v[12:15]\n"
        "v_cvt_pk_bf16_f32 v234, v1, v5\n"
        "v_mul_f32 v104, v96, v104\n"
        "v_mfma_f32_16x16x32_bf16 v[12:15], a[224:227], a[144:147], v[12:15]\n"
        "v_mul_f32 v105, v96, v105\n"
        "v_mul_f32 v106, v96, v106\n"
        "v_mfma_f32_16x16x32_bf16 v[116:119], a[48:51], a[236:239], 0\n"
        "v_mul_f32 v107, v96, v107\n"
        "v_cvt_pk_bf16_f32 v242, v2, v6\n"
        "v_mfma_f32_16x16x32_bf16 v[116:119], a[52:55], a[240:243], v[116:119]\n"
        "ds_read_b128 v[222:225], v251 offset:4608\n"
        "v_cvt_pk_bf16_f32 v102, v104, v105\n"
        "v_mfma_f32_16x16x32_bf16 v[116:119], a[56:59], a[244:247], v[116:119]\n"
        "v_cvt_pk_bf16_f32 v233, v8, v12\n"
        "v_cvt_pk_bf16_f32 v235, v9, v13\n"
        "v_mfma_f32_16x16x32_bf16 v[116:119], a[60:63], a[248:251], v[116:119]\n"
        "buffer_store_dwordx2 v[232:233], v250, s[56:59], 0 offen offset:0 sc1\n"
        "v_cvt_pk_bf16_f32 v243, v10, v14\n"
        "v_mfma_f32_16x16x32_bf16 v[116:119], a[64:67], a[216:219], v[116:119]\n"
        "v_cvt_pk_bf16_f32 v103, v106, v107\n"
        "v_mul_f32 v108, v96, v108\n"
        "v_mfma_f32_16x16x32_bf16 v[116:119], a[68:71], a[220:223], v[116:119]\n"
        "ds_write2st64_b32 v245, v102, v103 offset0:8 offset1:12\n"
        "v_smfmac_f32_16x16x64_bf16 v[124:127], a[252:255], v[72:79], v237\n"
        "ds_read_b128 a[212:215], v254 offset:15376\n"
        "v_mfma_f32_16x16x32_bf16 v[64:67], v[132:135], v[76:79], 0\n"
        "buffer_store_dwordx2 v[234:235], v250, s[56:59], 0 offen offset:384 sc1\n"
        "ds_read_b128 a[208:211], v254 offset:12304\n"
        "v_mfma_f32_16x16x32_bf16 v[64:67], v[128:131], v[72:75], v[64:67]\n"
        "ds_read_b128 a[204:207], v254 offset:9232\n"
        "v_mul_f32 v109, v96, v109\n"
        "v_mfma_f32_16x16x32_bf16 v[68:71], v[136:139], v[72:75], 0\n"
        "buffer_store_dwordx2 v[242:243], v250, s[56:59], 0 offen offset:768 sc1\n"
        "ds_read_b128 a[200:203], v254 offset:6160\n"
        "v_mfma_f32_16x16x32_bf16 v[68:71], v[140:143], v[76:79], v[68:71]\n"
        "ds_read_b128 a[196:199], v254 offset:3088\n"
        "v_mul_f32 v110, v96, v110\n"
        "v_mfma_f32_16x16x32_bf16 v[112:115], v[148:151], v[76:79], 0\n"
        "ds_read_b128 a[192:195], v254 offset:16\n"
        "v_mul_f32 v111, v96, v111\n"
        "v_mfma_f32_16x16x32_bf16 v[112:115], v[144:147], v[72:75], v[112:115]\n"
        "s_waitcnt lgkmcnt(7)\n"
        "ds_read_b128 a[168:171], v248 offset:16\n"
        "v_cvt_pk_bf16_f32 v232, v3, v7\n"
        "v_mfma_f32_16x16x32_bf16 v[24:27], v[176:179], a[72:75], 0\n"
        "ds_read_b128 a[172:175], v248 offset:3088\n"
        "v_cvt_pk_bf16_f32 v233, v11, v15\n"
        "v_mfma_f32_16x16x32_bf16 v[24:27], v[180:183], a[76:79], v[24:27]\n"
        "buffer_store_dwordx2 v[232:233], v250, s[56:59], 0 offen offset:1152 sc1\n"
        "ds_read_b128 a[176:179], v248 offset:6160\n"
        "v_mfma_f32_16x16x32_bf16 v[24:27], v[184:187], a[80:83], v[24:27]\n"
        "ds_read_b128 a[180:183], v248 offset:9232\n"
        "v_cvt_pk_bf16_f32 v108, v108, v109\n"
        "v_mfma_f32_16x16x32_bf16 v[24:27], v[196:199], a[84:87], v[24:27]\n"
        "ds_read_b128 a[184:187], v248 offset:12304\n"
        "v_cvt_pk_bf16_f32 v109, v110, v111\n"
        "v_mfma_f32_16x16x32_bf16 v[24:27], v[200:203], a[88:91], v[24:27]\n"
        "ds_write2st64_b32 v245, v108, v109 offset0:16 offset1:20\n"
        "v_mfma_f32_16x16x32_bf16 v[24:27], v[204:207], a[92:95], v[24:27]\n"
        "s_waitcnt lgkmcnt(6)\n"
        "ds_read_b128 a[188:191], v248 offset:15376\n"
        "v_add_f32 v125, v124, v125\n"
        "v_mfma_f32_16x16x32_bf16 v[20:23], v[164:167], a[212:215], 0\n"
        "v_rcp_f32 v124, v125\n"
        "v_mfma_f32_16x16x32_bf16 v[20:23], v[160:163], a[208:211], v[20:23]\n"
        "v_add_u32 v240, s31, v247\n"
        "v_max_u32 v236, v96, v97\n"
        "v_mfma_f32_16x16x32_bf16 v[20:23], v[156:159], a[204:207], v[20:23]\n"
        "v_cvt_pk_bf16_f32 v220, v116, v117\n"
        "v_cvt_pk_bf16_f32 v221, v118, v119\n"
        "v_mfma_f32_16x16x32_bf16 v[20:23], a[232:235], a[200:203], v[20:23]\n"
        "v_mul_f32 v64, v124, v64\n"
        "v_mul_f32 v65, v124, v65\n"
        "v_mfma_f32_16x16x32_bf16 v[20:23], a[228:231], a[196:199], v[20:23]\n"
        "v_mul_f32 v66, v124, v66\n"
        "v_mul_f32 v67, v124, v67\n"
        "v_mfma_f32_16x16x32_bf16 v[20:23], a[224:227], a[192:195], v[20:23]\n"
        "s_waitcnt lgkmcnt(2)\n"
        "v_mov_b64 v[226:227], v[220:221]\n"
        "v_cvt_pk_bf16_f32 v64, v64, v65\n"
        "v_mfma_f32_16x16x32_bf16 v[16:19], a[224:227], a[168:171], 0\n"
        "v_cvt_pk_bf16_f32 v65, v66, v67\n"
        "v_mul_f32 v68, v124, v68\n"
        "v_mfma_f32_16x16x32_bf16 v[16:19], a[228:231], a[172:175], v[16:19]\n"
        "ds_write2st64_b32 v245, v64, v65 offset0:1 offset1:5\n"
        "v_mfma_f32_16x16x32_bf16 v[16:19], a[232:235], a[176:179], v[16:19]\n"
        "v_mul_f32 v69, v124, v69\n"
        "v_mul_f32 v70, v124, v70\n"
        "v_mfma_f32_16x16x32_bf16 v[16:19], v[156:159], a[180:183], v[16:19]\n"
        "v_mul_f32 v71, v124, v71\n"
        "v_cvt_pk_bf16_f32 v66, v68, v69\n"
        "v_mfma_f32_16x16x32_bf16 v[16:19], v[160:163], a[184:187], v[16:19]\n"
        "s_waitcnt lgkmcnt(1)\n"
        "v_cvt_pk_bf16_f32 v67, v70, v71\n"
        "v_mul_f32 v112, v124, v112\n"
        "v_mfma_f32_16x16x32_bf16 v[16:19], v[164:167], a[188:191], v[16:19]\n"
        "ds_write2st64_b32 v245, v66, v67 offset0:9 offset1:13\n"
        "v_mfma_f32_16x16x32_bf16 v[0:3], v[152:155], v[216:219], v[48:51]\n"
        "v_mul_f32 v113, v124, v113\n"
        "v_mul_f32 v114, v124, v114\n"
        "v_mfma_f32_16x16x32_bf16 v[0:3], v[168:171], v[220:223], v[0:3]\n"
        "v_mul_f32 v115, v124, v115\n"
        "v_cvt_pk_bf16_f32 v112, v112, v113\n"
        "v_mfma_f32_16x16x32_bf16 v[4:7], v[172:175], v[216:219], v[52:55]\n"
        "ds_write_b32 v245, v112 offset:4352\n"
        "v_mfma_f32_16x16x32_bf16 v[4:7], v[188:191], v[224:227], v[4:7]\n"
        "ds_read_b128 v[222:225], v252 offset:4608\n"
        "v_cvt_pk_bf16_f32 v113, v114, v115\n"
        "v_mfma_f32_16x16x32_bf16 v[36:39], v[204:207], a[164:167], 0\n"
        "ds_write_b32 v245, v113 offset:5376\n"
        "v_mfma_f32_16x16x32_bf16 v[36:39], v[200:203], a[160:163], v[36:39]\n"
        "v_max3_u32 v236, v236, v124, v125\n"
        "v_cvt_pk_bf16_f32 v244, v16, v20\n"
        "v_mfma_f32_16x16x32_bf16 v[36:39], v[196:199], a[156:159], v[36:39]\n"
        "buffer_store_dword v244, v238, s[56:59], 0 offen offset:0 sc1\n"
        "v_mfma_f32_16x16x32_bf16 v[36:39], v[184:187], a[152:155], v[36:39]\n"
        "v_cvt_pk_bf16_f32 v239, v17, v21\n"
        "v_cvt_pk_bf16_f32 v255, v18, v22\n"
        "v_mfma_f32_16x16x32_bf16 v[36:39], v[180:183], a[148:151], v[36:39]\n"
        ".long 0xbec200ff\n"
        "47:\n"
        "s_branch 61b\n"
        "buffer_store_dword v239, v238, s[56:59], 0 offen offset:384 sc1\n"
        "v_mfma_f32_16x16x32_bf16 v[36:39], v[176:179], a[144:147], v[36:39]\n"
        "v_exp_f32 v0, v0\n"
        "v_mfma_f32_16x16x32_bf16 v[32:35], v[176:179], a[120:123], 0\n"
        "v_exp_f32 v1, v1\n"
        "v_mfma_f32_16x16x32_bf16 v[32:35], v[180:183], a[124:127], v[32:35]\n"
        "buffer_store_dword v255, v238, s[56:59], 0 offen offset:768 sc1\n"
        "v_mfma_f32_16x16x32_bf16 v[32:35], v[184:187], a[128:131], v[32:35]\n"
        "v_exp_f32 v2, v2\n"
        "v_mfma_f32_16x16x32_bf16 v[32:35], v[196:199], a[132:135], v[32:35]\n"
        "v_cvt_pk_bf16_f32 v88, v0, v1\n"
        "v_cvt_pk_bf16_f32 v244, v19, v23\n"
        "v_mfma_f32_16x16x32_bf16 v[32:35], v[200:203], a[136:139], v[32:35]\n"
        "v_exp_f32 v4, v4\n"
        "v_mfma_f32_16x16x32_bf16 v[32:35], v[204:207], a[140:143], v[32:35]\n"
        "buffer_store_dword v244, v238, s[56:59], 0 offen offset:1152 sc1\n"
        "v_mfma_f32_16x16x32_bf16 v[28:31], v[204:207], a[116:119], 0\n"
        "v_exp_f32 v3, v3\n"
        "v_mfma_f32_16x16x32_bf16 v[28:31], v[200:203], a[112:115], v[28:31]\n"
        "v_exp_f32 v5, v5\n"
        "v_mfma_f32_16x16x32_bf16 v[28:31], v[196:199], a[108:111], v[28:31]\n"
        "v_cvt_pk_bf16_f32 v89, v2, v3\n"
        "v_cvt_pk_bf16_f32 v90, v4, v5\n"
        "v_mfma_f32_16x16x32_bf16 v[28:31], v[184:187], a[104:107], v[28:31]\n"
        "v_exp_f32 v6, v6\n"
        "v_mfma_f32_16x16x32_bf16 v[28:31], v[180:183], a[100:103], v[28:31]\n"
        "v_exp_f32 v7, v7\n"
        "v_mfma_f32_16x16x32_bf16 v[28:31], v[176:179], a[96:99], v[28:31]\n"
        "v_cvt_pk_bf16_f32 v91, v6, v7\n"
        "v_mov_b64 v[96:97], 0\n"
        "v_mfma_f32_16x16x32_bf16 v[8:11], v[192:195], v[216:219], v[56:59]\n"
        "s_waitcnt lgkmcnt(0)\n"
        "v_cvt_pk_bf16_f32 v235, v32, v36\n"
        "v_cvt_pk_bf16_f32 v243, v33, v37\n"
        "v_mfma_f32_16x16x32_bf16 v[8:11], v[208:211], v[220:223], v[8:11]\n"
        "v_cvt_pk_bf16_f32 v233, v34, v38\n"
        "v_mov_b32 v249, s29\n"
        "v_mfma_f32_16x16x32_bf16 v[40:43], v[176:179], a[168:171], 0\n"
        "v_cvt_pk_bf16_f32 v234, v24, v28\n"
        "v_cvt_pk_bf16_f32 v242, v25, v29\n"
        "v_mfma_f32_16x16x32_bf16 v[40:43], v[180:183], a[172:175], v[40:43]\n"
        "buffer_store_dwordx2 v[234:235], v250, s[60:63], 0 offen offset:0 sc1\n"
        "v_cvt_pk_bf16_f32 v232, v26, v30\n"
        "v_mfma_f32_16x16x32_bf16 v[40:43], v[184:187], a[176:179], v[40:43]\n"
        "v_exp_f32 v8, v8\n"
        "v_mfma_f32_16x16x32_bf16 v[40:43], v[196:199], a[180:183], v[40:43]\n"
        "s_barrier\n"
        "ds_read_b128 a[0:3], v240 offset:0\n"
        "ds_read_b128 a[4:7], v240 offset:4096\n"
        "v_mfma_f32_16x16x32_bf16 v[40:43], v[200:203], a[184:187], v[40:43]\n"
        "buffer_store_dwordx2 v[242:243], v250, s[60:63], 0 offen offset:384 sc1\n"
        "ds_read_b128 a[8:11], v240 offset:8192\n"
        "v_mfma_f32_16x16x32_bf16 v[40:43], v[204:207], a[188:191], v[40:43]\n"
        "ds_read_b128 a[12:15], v240 offset:12288\n"
        "ds_read_b128 a[16:19], v240 offset:16384\n"
        "v_smfmac_f32_16x16x64_bf16 v[96:99], a[252:255], v[80:87], v237\n"
        "ds_read_b128 a[20:23], v240 offset:20480\n"
        "v_mfma_f32_16x16x32_bf16 v[72:75], v[132:135], v[84:87], 0\n"
        "buffer_store_dwordx2 v[232:233], v250, s[60:63], 0 offen offset:768 sc1\n"
        "v_cvt_pk_bf16_f32 v234, v27, v31\n"
        "v_mfma_f32_16x16x32_bf16 v[72:75], v[128:131], v[80:83], v[72:75]\n"
        "v_exp_f32 v9, v9\n"
        "v_mfma_f32_16x16x32_bf16 v[76:79], v[136:139], v[80:83], 0\n"
        "v_exp_f32 v10, v10\n"
        "v_mfma_f32_16x16x32_bf16 v[76:79], v[140:143], v[84:87], v[76:79]\n"
        "v_cvt_pk_bf16_f32 v92, v8, v9\n"
        "v_cvt_pk_bf16_f32 v235, v35, v39\n"
        "v_mfma_f32_16x16x32_bf16 v[100:103], v[148:151], v[84:87], 0\n"
        "buffer_store_dwordx2 v[234:235], v250, s[60:63], 0 offen offset:1152 sc1\n"
        "v_add_f32 v97, v96, v97\n"
        "v_mfma_f32_16x16x32_bf16 v[100:103], v[144:147], v[80:83], v[100:103]\n"
        "v_exp_f32 v11, v11\n"
        "v_mfma_f32_16x16x32_bf16 v[12:15], v[212:215], v[216:219], v[60:63]\n"
        "v_cvt_pk_bf16_f32 v93, v10, v11\n"
        "v_mfma_f32_16x16x32_bf16 v[12:15], v[228:231], v[224:227], v[12:15]\n"
        "s_waitcnt lgkmcnt(0)\n"
        "v_rcp_f32 v96, v97\n"
        "v_mfma_f32_16x16x32_bf16 v[156:159], a[20:23], a[92:95], 0\n"
        "v_mul_f32 v72, v96, v72\n"
        "v_mul_f32 v73, v96, v73\n"
        "v_mfma_f32_16x16x32_bf16 v[156:159], a[16:19], a[88:91], v[156:159]\n"
        "v_mul_f32 v74, v96, v74\n"
        "v_mul_f32 v75, v96, v75\n"
        "v_mfma_f32_16x16x32_bf16 v[156:159], a[12:15], a[84:87], v[156:159]\n"
        "v_exp_f32 v12, v12\n"
        "v_mfma_f32_16x16x32_bf16 v[156:159], a[8:11], a[80:83], v[156:159]\n"
        "v_exp_f32 v13, v13\n"
        "v_mfma_f32_16x16x32_bf16 v[156:159], a[4:7], a[76:79], v[156:159]\n"
        "v_exp_f32 v14, v14\n"
        "v_mfma_f32_16x16x32_bf16 v[156:159], a[0:3], a[72:75], v[156:159]\n"
        "v_cvt_pk_bf16_f32 v94, v12, v13\n"
        "v_cvt_pk_bf16_f32 v72, v72, v73\n"
        "v_mfma_f32_16x16x32_bf16 v[44:47], v[176:179], a[192:195], 0\n"
        "v_exp_f32 v15, v15\n"
        "v_mfma_f32_16x16x32_bf16 v[44:47], v[180:183], a[196:199], v[44:47]\n"
        "v_cvt_pk_bf16_f32 v95, v14, v15\n"
        "v_mov_b64 v[124:125], 0\n"
        "v_mfma_f32_16x16x32_bf16 v[44:47], v[184:187], a[200:203], v[44:47]\n"
        "v_cvt_pk_bf16_f32 v73, v74, v75\n"
        "v_mul_f32 v76, v96, v76\n"
        "v_mfma_f32_16x16x32_bf16 v[44:47], v[196:199], a[204:207], v[44:47]\n"
        "ds_write2st64_b32 v245, v72, v73 offset0:2 offset1:6\n"
        "v_mfma_f32_16x16x32_bf16 v[44:47], v[200:203], a[208:211], v[44:47]\n"
        "v_mul_f32 v77, v96, v77\n"
        "v_mul_f32 v78, v96, v78\n"
        "v_mfma_f32_16x16x32_bf16 v[44:47], v[204:207], a[212:215], v[44:47]\n"
        "v_mul_f32 v79, v96, v79\n"
        "v_cvt_pk_bf16_f32 v74, v76, v77\n"
        "v_mfma_f32_16x16x32_bf16 v[160:163], a[20:23], a[116:119], 0\n"
        "v_cvt_pk_bf16_f32 v75, v78, v79\n"
        "v_mul_f32 v100, v96, v100\n"
        "v_mfma_f32_16x16x32_bf16 v[160:163], a[16:19], a[112:115], v[160:163]\n"
        "v_mul_f32 v101, v96, v101\n"
        "v_mul_f32 v102, v96, v102\n"
        "v_mfma_f32_16x16x32_bf16 v[160:163], a[12:15], a[108:111], v[160:163]\n"
        "ds_write2st64_b32 v245, v74, v75 offset0:10 offset1:14\n"
        "v_mfma_f32_16x16x32_bf16 v[160:163], a[8:11], a[104:107], v[160:163]\n"
        "v_cvt_pk_bf16_f32 v239, v40, v44\n"
        "v_cvt_pk_bf16_f32 v255, v41, v45\n"
        "v_mfma_f32_16x16x32_bf16 v[160:163], a[4:7], a[100:103], v[160:163]\n"
        "buffer_store_dword v239, v238, s[60:63], 0 offen offset:0 sc1\n"
        "v_mfma_f32_16x16x32_bf16 v[160:163], a[0:3], a[96:99], v[160:163]\n"
        "v_mul_f32 v103, v96, v103\n"
        "v_cvt_pk_bf16_f32 v244, v42, v46\n"
        "v_smfmac_f32_16x16x64_bf16 v[124:127], a[252:255], v[88:95], v237\n"
        "v_cvt_pk_bf16_f32 v100, v100, v101\n"
        "v_mfma_f32_16x16x32_bf16 v[104:107], v[132:135], v[92:95], 0\n"
        "buffer_store_dword v255, v238, s[60:63], 0 offen offset:384 sc1\n"
        "v_mfma_f32_16x16x32_bf16 v[104:107], v[128:131], v[88:91], v[104:107]\n"
        "v_cvt_pk_bf16_f32 v101, v102, v103\n"
        "v_cvt_pk_bf16_f32 v239, v43, v47\n"
        "v_mfma_f32_16x16x32_bf16 v[108:111], v[136:139], v[88:91], 0\n"
        "buffer_store_dword v244, v238, s[60:63], 0 offen offset:768 sc1\n"
        "v_mfma_f32_16x16x32_bf16 v[108:111], v[140:143], v[92:95], v[108:111]\n"
        "ds_write2st64_b32 v245, v100, v101 offset0:18 offset1:22\n"
        "v_mfma_f32_16x16x32_bf16 v[112:115], v[148:151], v[92:95], 0\n"
        "v_max3_u32 v236, v236, v96, v97\n"
        "v_add_f32 v125, v124, v125\n"
        "v_mfma_f32_16x16x32_bf16 v[112:115], v[144:147], v[88:91], v[112:115]\n"
        "buffer_store_dword v239, v238, s[60:63], 0 offen offset:1152 sc1\n"
        "v_mfma_f32_16x16x32_bf16 v[176:179], a[0:3], a[144:147], 0\n"
        "v_rcp_f32 v124, v125\n"
        "v_mfma_f32_16x16x32_bf16 v[176:179], a[4:7], a[148:151], v[176:179]\n"
        "v_mul_f32 v104, v124, v104\n"
        "v_mul_f32 v105, v124, v105\n"
        "v_mfma_f32_16x16x32_bf16 v[176:179], a[8:11], a[152:155], v[176:179]\n"
        "v_mul_f32 v106, v124, v106\n"
        "v_mul_f32 v107, v124, v107\n"
        "v_mfma_f32_16x16x32_bf16 v[176:179], a[12:15], a[156:159], v[176:179]\n"
        "v_cvt_pk_bf16_f32 v104, v104, v105\n"
        "v_cvt_pk_bf16_f32 v105, v106, v107\n"
        "v_mfma_f32_16x16x32_bf16 v[176:179], a[16:19], a[160:163], v[176:179]\n"
        "ds_write2st64_b32 v245, v104, v105 offset0:3 offset1:7\n"
        "v_mfma_f32_16x16x32_bf16 v[176:179], a[20:23], a[164:167], v[176:179]\n"
        "v_mul_f32 v108, v124, v108\n"
        "v_mul_f32 v109, v124, v109\n"
        "v_mfma_f32_16x16x32_bf16 v[164:167], a[20:23], a[140:143], 0\n"
        "v_mul_f32 v110, v124, v110\n"
        "v_mul_f32 v111, v124, v111\n"
        "v_mfma_f32_16x16x32_bf16 v[164:167], a[16:19], a[136:139], v[164:167]\n"
        "v_cvt_pk_bf16_f32 v106, v108, v109\n"
        "v_cvt_pk_bf16_f32 v107, v110, v111\n"
        "v_mfma_f32_16x16x32_bf16 v[164:167], a[12:15], a[132:135], v[164:167]\n"
        "v_mul_f32 v112, v124, v112\n"
        "v_mul_f32 v113, v124, v113\n"
        "v_mfma_f32_16x16x32_bf16 v[164:167], a[8:11], a[128:131], v[164:167]\n"
        "ds_write2st64_b32 v245, v106, v107 offset0:11 offset1:15\n"
        "v_mfma_f32_16x16x32_bf16 v[164:167], a[4:7], a[124:127], v[164:167]\n"
        "v_mul_f32 v114, v124, v114\n"
        "v_mul_f32 v115, v124, v115\n"
        "v_mfma_f32_16x16x32_bf16 v[164:167], a[0:3], a[120:123], v[164:167]\n"
        "v_cvt_pk_bf16_f32 v112, v112, v113\n"
        "v_cvt_pk_bf16_f32 v113, v114, v115\n"
        "v_mfma_f32_16x16x32_bf16 v[180:183], a[0:3], a[168:171], 0\n"
        "v_max3_u32 v236, v236, v124, v125\n"
        "v_cvt_pk_bf16_f32 v242, v156, v160\n"
        "v_mfma_f32_16x16x32_bf16 v[180:183], a[4:7], a[172:175], v[180:183]\n"
        "ds_write2st64_b32 v245, v112, v113 offset0:19 offset1:23\n"
        "v_mfma_f32_16x16x32_bf16 v[180:183], a[8:11], a[176:179], v[180:183]\n"
        "v_cvt_pk_bf16_f32 v243, v164, v176\n"
        "v_cmp_lt_u32 vcc, s24, v236\n"
        "v_mfma_f32_16x16x32_bf16 v[180:183], a[12:15], a[180:183], v[180:183]\n"
        "buffer_store_dwordx2 v[242:243], v250, s[16:19], s47 offen offset:0 sc1\n"
        "v_cvt_pk_bf16_f32 v232, v157, v161\n"
        "v_mfma_f32_16x16x32_bf16 v[180:183], a[16:19], a[184:187], v[180:183]\n"
        "s_cmp_lg_u64 vcc, 0\n"
        "s_addc_u32 s25, s25, s25\n"
        "v_mov_b32 v253, s25\n"
        "v_mfma_f32_16x16x32_bf16 v[180:183], a[20:23], a[188:191], v[180:183]\n"
        "ds_write_b32 v249, v253\n"
        "v_mfma_f32_16x16x32_bf16 v[184:187], a[20:23], a[212:215], 0\n"
        "v_cvt_pk_bf16_f32 v233, v165, v177\n"
        "v_cvt_pk_bf16_f32 v234, v158, v162\n"
        "v_mfma_f32_16x16x32_bf16 v[184:187], a[16:19], a[208:211], v[184:187]\n"
        "buffer_store_dwordx2 v[232:233], v250, s[16:19], s47 offen offset:384 sc1\n"
        "v_cvt_pk_bf16_f32 v235, v166, v178\n"
        "v_mfma_f32_16x16x32_bf16 v[184:187], a[12:15], a[204:207], v[184:187]\n"
        "s_waitcnt lgkmcnt(0)\n"
        "s_barrier\n"
        "ds_read_b128 a[24:27], v240 offset:512\n"
        "ds_read_b128 a[28:31], v240 offset:4608\n"
        "v_mfma_f32_16x16x32_bf16 v[184:187], a[8:11], a[200:203], v[184:187]\n"
        "buffer_store_dwordx2 v[234:235], v250, s[16:19], s47 offen offset:768 sc1\n"
        "ds_read_b128 a[32:35], v240 offset:8704\n"
        "v_mfma_f32_16x16x32_bf16 v[184:187], a[4:7], a[196:199], v[184:187]\n"
        "ds_read_b128 a[36:39], v240 offset:12800\n"
        "ds_read_b128 a[40:43], v240 offset:16896\n"
        "v_mfma_f32_16x16x32_bf16 v[184:187], a[0:3], a[192:195], v[184:187]\n"
        "s_waitcnt lgkmcnt(4)\n"
        "ds_read_b128 a[44:47], v240 offset:20992\n"
        "v_cvt_pk_bf16_f32 v242, v159, v163\n"
        "v_mfma_f32_16x16x32_bf16 v[52:55], a[24:27], a[168:171], 0\n"
        "s_waitcnt lgkmcnt(4)\n"
        "v_cvt_pk_bf16_f32 v243, v167, v179\n"
        "s_nop 0\n"
        "v_mfma_f32_16x16x32_bf16 v[52:55], a[28:31], a[172:175], v[52:55]\n"
        "s_waitcnt lgkmcnt(3)\n"
        "buffer_store_dwordx2 v[242:243], v250, s[16:19], s47 offen offset:1152 sc1\n"
        "s_nop 0\n"
        "v_mfma_f32_16x16x32_bf16 v[52:55], a[32:35], a[176:179], v[52:55]\n"
        "s_waitcnt lgkmcnt(2)\n"
        "v_cvt_pk_bf16_f32 v255, v180, v184\n"
        "v_cvt_pk_bf16_f32 v244, v181, v185\n"
        "v_mfma_f32_16x16x32_bf16 v[52:55], a[36:39], a[180:183], v[52:55]\n"
        "s_waitcnt lgkmcnt(1)\n"
        "buffer_store_dword v255, v238, s[16:19], s47 offen offset:0 sc1\n"
        "v_mfma_f32_16x16x32_bf16 v[52:55], a[40:43], a[184:187], v[52:55]\n"
        "s_waitcnt lgkmcnt(0)\n"
        "ds_read_b128 v[60:63], v241\n"
        "v_cvt_pk_bf16_f32 v239, v182, v186\n"
        "v_mfma_f32_16x16x32_bf16 v[52:55], a[44:47], a[188:191], v[52:55]\n"
        "v_cvt_pk_bf16_f32 v255, v183, v187\n"
        "v_mfma_f32_16x16x32_bf16 v[56:59], a[44:47], a[212:215], 0\n"
        "buffer_store_dword v244, v238, s[16:19], s47 offen offset:384 sc1\n"
        "v_mfma_f32_16x16x32_bf16 v[56:59], a[40:43], a[208:211], v[56:59]\n"
        "v_mfma_f32_16x16x32_bf16 v[56:59], a[36:39], a[204:207], v[56:59]\n"
        "v_mfma_f32_16x16x32_bf16 v[56:59], a[32:35], a[200:203], v[56:59]\n"
        "buffer_store_dword v239, v238, s[16:19], s47 offen offset:768 sc1\n"
        "v_mfma_f32_16x16x32_bf16 v[56:59], a[28:31], a[196:199], v[56:59]\n"
        "v_mfma_f32_16x16x32_bf16 v[56:59], a[24:27], a[192:195], v[56:59]\n"
        "s_nop 2\n"
        "v_mfma_f32_16x16x32_bf16 v[196:199], a[24:27], a[72:75], 0\n"
        "buffer_store_dword v255, v238, s[16:19], s47 offen offset:1152 sc1\n"
        "v_mfma_f32_16x16x32_bf16 v[196:199], a[28:31], a[76:79], v[196:199]\n"
        "s_nop 1\n"
        "v_cvt_pk_bf16_f32 v88, v52, v56\n"
        "v_mfma_f32_16x16x32_bf16 v[196:199], a[32:35], a[80:83], v[196:199]\n"
        "v_cvt_pk_bf16_f32 v90, v53, v57\n"
        "v_cvt_pk_bf16_f32 v92, v54, v58\n"
        "v_mfma_f32_16x16x32_bf16 v[196:199], a[36:39], a[84:87], v[196:199]\n"
        "buffer_store_dword v88, v238, s[16:19], s35 offen offset:0 sc1\n"
        "v_mfma_f32_16x16x32_bf16 v[196:199], a[40:43], a[88:91], v[196:199]\n"
        "v_cvt_pk_bf16_f32 v94, v55, v59\n"
        "v_mfma_f32_16x16x32_bf16 v[196:199], a[44:47], a[92:95], v[196:199]\n"
        "v_mfma_f32_16x16x32_bf16 v[48:51], a[44:47], a[164:167], 0\n"
        "buffer_store_dword v90, v238, s[16:19], s35 offen offset:384 sc1\n"
        "v_mfma_f32_16x16x32_bf16 v[48:51], a[40:43], a[160:163], v[48:51]\n"
        "s_waitcnt lgkmcnt(0)\n"
        "v_or3_b32 v60, v60, v61, v62\n"
        "v_or_b32 v60, v60, v63\n"
        "v_mfma_f32_16x16x32_bf16 v[48:51], a[36:39], a[156:159], v[48:51]\n"
        "s_nop 0\n"
        "v_readfirstlane_b32 s25, v60\n"
        "v_mfma_f32_16x16x32_bf16 v[48:51], a[32:35], a[152:155], v[48:51]\n"
        "buffer_store_dword v92, v238, s[16:19], s35 offen offset:768 sc1\n"
        "v_mfma_f32_16x16x32_bf16 v[48:51], a[28:31], a[148:151], v[48:51]\n"
        "v_mfma_f32_16x16x32_bf16 v[48:51], a[24:27], a[144:147], v[48:51]\n"
        "v_mfma_f32_16x16x32_bf16 v[204:207], a[24:27], a[120:123], 0\n"
        "buffer_store_dword v94, v238, s[16:19], s35 offen offset:1152 sc1\n"
        "v_mfma_f32_16x16x32_bf16 v[204:207], a[28:31], a[124:127], v[204:207]\n"
        "v_mfma_f32_16x16x32_bf16 v[204:207], a[32:35], a[128:131], v[204:207]\n"
        "v_mfma_f32_16x16x32_bf16 v[204:207], a[36:39], a[132:135], v[204:207]\n"
        "v_mfma_f32_16x16x32_bf16 v[204:207], a[40:43], a[136:139], v[204:207]\n"
        "v_mfma_f32_16x16x32_bf16 v[204:207], a[44:47], a[140:143], v[204:207]\n"
        "v_mfma_f32_16x16x32_bf16 v[200:203], a[44:47], a[116:119], 0\n"
        "v_mfma_f32_16x16x32_bf16 v[200:203], a[40:43], a[112:115], v[200:203]\n"
        "s_nop 2\n"
        "v_mfma_f32_16x16x32_bf16 v[200:203], a[36:39], a[108:111], v[200:203]\n"
        "s_nop 1\n"
        "v_cvt_pk_bf16_f32 v81, v204, v48\n"
        "v_mfma_f32_16x16x32_bf16 v[200:203], a[32:35], a[104:107], v[200:203]\n"
        "v_cvt_pk_bf16_f32 v83, v205, v49\n"
        "v_mfma_f32_16x16x32_bf16 v[200:203], a[28:31], a[100:103], v[200:203]\n"
        "v_cvt_pk_bf16_f32 v85, v206, v50\n"
        "v_mfma_f32_16x16x32_bf16 v[200:203], a[24:27], a[96:99], v[200:203]\n"
        "s_nop 2\n"
        "v_cvt_pk_bf16_f32 v87, v207, v51\n"
        "s_nop 3\n"
        "v_cvt_pk_bf16_f32 v80, v196, v200\n"
        "buffer_store_dwordx2 v[80:81], v250, s[16:19], s35 offen offset:0 sc1\n"
        "v_cvt_pk_bf16_f32 v82, v197, v201\n"
        "buffer_store_dwordx2 v[82:83], v250, s[16:19], s35 offen offset:384 sc1\n"
        "v_cvt_pk_bf16_f32 v84, v198, v202\n"
        "buffer_store_dwordx2 v[84:85], v250, s[16:19], s35 offen offset:768 sc1\n"
        "v_cvt_pk_bf16_f32 v86, v199, v203\n"
        "buffer_store_dwordx2 v[86:87], v250, s[16:19], s35 offen offset:1152 sc1\n"
        "s_cmp_lg_u32 s25, 0\n"
        "s_cbranch_scc1 30f\n"
        "s_endpgm\n"
        "30:\n"
        : "={s25}"(flg), "={s42}"(w), "+{s[0:1]}"(ka), "+{s2}"(bid), "+{v0}"(tid)
        : "s"(ldsb)
        : "v1", "v2", "v3", "v4", "v5", "v6", "v7", "v8", "v9", "v10", "v11", "v12", "v13", "v14", "v15", "v16", "v17", "v18", "v19", "v20", "v21", "v22", "v23", "v24", "v25", "v26", "v27", "v28", "v29", "v30", "v31", "v32", "v33", "v34", "v35", "v36", "v37", "v38", "v39", "v40", "v41", "v42", "v43", "v44", "v45", "v46", "v47", "v48", "v49", "v50", "v51", "v52", "v53", "v54", "v55", "v56", "v57", "v58", "v59", "v60", "v61", "v62", "v63", "v64", "v65", "v66", "v67", "v68", "v69", "v70", "v71", "v72", "v73", "v74", "v75", "v76", "v77", "v78", "v79", "v80", "v81", "v82", "v83", "v84", "v85", "v86", "v87", "v88", "v89", "v90", "v91", "v92", "v93", "v94", "v95", "v96", "v97", "v98", "v99", "v100", "v101", "v102", "v103", "v104", "v105", "v106", "v107", "v108", "v109", "v110", "v111", "v112", "v113", "v114", "v115", "v116", "v117", "v118", "v119", "v120", "v121", "v122", "v123", "v124", "v125", "v126", "v127", "v128", "v129", "v130", "v131", "v132", "v133", "v134", "v135", "v136", "v137", "v138", "v139", "v140", "v141", "v142", "v143", "v144", "v145", "v146", "v147", "v148", "v149", "v150", "v151", "v152", "v153", "v154", "v155", "v156", "v157", "v158", "v159", "v160", "v161", "v162", "v163", "v164", "v165", "v166", "v167", "v168", "v169", "v170", "v171", "v172", "v173", "v174", "v175", "v176", "v177", "v178", "v179", "v180", "v181", "v182", "v183", "v184", "v185", "v186", "v187", "v188", "v189", "v190", "v191", "v192", "v193", "v194", "v195", "v196", "v197", "v198", "v199", "v200", "v201", "v202", "v203", "v204", "v205", "v206", "v207", "v208", "v209", "v210", "v211", "v212", "v213", "v214", "v215", "v216", "v217", "v218", "v219", "v220", "v221", "v222", "v223", "v224", "v225", "v226", "v227", "v228", "v229", "v230", "v231", "v232", "v233", "v234", "v235", "v236", "v237", "v238", "v239", "v240", "v241", "v242", "v243", "v244", "v245", "v246", "v247", "v248", "v249", "v250", "v251", "v252", "v253", "v254", "v255", "a0", "a1", "a2", "a3", "a4", "a5", "a6", "a7", "a8", "a9", "a10", "a11", "a12", "a13", "a14", "a15", "a16", "a17", "a18", "a19", "a20", "a21", "a22", "a23", "a24", "a25", "a26", "a27", "a28", "a29", "a30", "a31", "a32", "a33", "a34", "a35", "a36", "a37", "a38", "a39", "a40", "a41", "a42", "a43", "a44", "a45", "a46", "a47", "a48", "a49", "a50", "a51", "a52", "a53", "a54", "a55", "a56", "a57", "a58", "a59", "a60", "a61", "a62", "a63", "a64", "a65", "a66", "a67", "a68", "a69", "a70", "a71", "a72", "a73", "a74", "a75", "a76", "a77", "a78", "a79", "a80", "a81", "a82", "a83", "a84", "a85", "a86", "a87", "a88", "a89", "a90", "a91", "a92", "a93", "a94", "a95", "a96", "a97", "a98", "a99", "a100", "a101", "a102", "a103", "a104", "a105", "a106", "a107", "a108", "a109", "a110", "a111", "a112", "a113", "a114", "a115", "a116", "a117", "a118", "a119", "a120", "a121", "a122", "a123", "a124", "a125", "a126", "a127", "a128", "a129", "a130", "a131", "a132", "a133", "a134", "a135", "a136", "a137", "a138", "a139", "a140", "a141", "a142", "a143", "a144", "a145", "a146", "a147", "a148", "a149", "a150", "a151", "a152", "a153", "a154", "a155", "a156", "a157", "a158", "a159", "a160", "a161", "a162", "a163", "a164", "a165", "a166", "a167", "a168", "a169", "a170", "a171", "a172", "a173", "a174", "a175", "a176", "a177", "a178", "a179", "a180", "a181", "a182", "a183", "a184", "a185", "a186", "a187", "a188", "a189", "a190", "a191", "a192", "a193", "a194", "a195", "a196", "a197", "a198", "a199", "a200", "a201", "a202", "a203", "a204", "a205", "a206", "a207", "a208", "a209", "a210", "a211", "a212", "a213", "a214", "a215", "a216", "a217", "a218", "a219", "a220", "a221", "a222", "a223", "a224", "a225", "a226", "a227", "a228", "a229", "a230", "a231", "a232", "a233", "a234", "a235", "a236", "a237", "a238", "a239", "a240", "a241", "a242", "a243", "a244", "a245", "a246", "a247", "a248", "a249", "a250", "a251", "a252", "a253", "a254", "a255", "s4", "s5", "s6", "s7", "s8", "s9", "s10", "s11", "s12", "s13", "s14", "s15", "s16", "s17", "s18", "s19", "s20", "s21", "s22", "s23", "s24", "s26", "s27", "s28", "s29", "s30", "s31", "s34", "s35", "s36", "s37", "s38", "s39", "s40", "s41", "s43", "s44", "s45", "s46", "s47", "s48", "s49", "s50", "s51", "s52", "s53", "s54", "s55", "s56", "s57", "s58", "s59", "s60", "s61", "s62", "s63", "s64", "s65", "s66", "s67", "s68", "s69", "s70", "s71", "s72", "s73", "s74", "s75", "s76", "s77", "s78", "s79", "s80", "s81", "s82", "s83", "vcc", "scc", "memory");
    if (flg) {
        unsigned ln;
        asm volatile("s_waitcnt vmcnt(0)\n v_mbcnt_lo_u32_b32 %0, -1, 0\n v_mbcnt_hi_u32_b32 %0, -1, %0" : "=v"(ln) :: "memory");
        const unsigned t = w * 64u + ln, wx = bid & 63u, wy0 = bid >> 6;
        typedef const char* __attribute__((address_space(4))) const* kargp;
        const char *x = ((kargp)ka)[0], *wqkv = ((kargp)ka)[1], *wo = ((kargp)ka)[2], *rpb = ((kargp)ka)[3], *rpi = ((kargp)ka)[4],
                   *mask = ((kargp)ka)[5];
        char* out = (char*)((kargp)ka)[6];
        unsigned short* xs = (unsigned short*)smem;
        unsigned short* qs = xs + 64 * 192;
        const unsigned short* W1 = (const unsigned short*)wqkv;
        const unsigned short* W2 = (const unsigned short*)wo;
        for (unsigned i = 0; i < 16u; ++i) {
            if (!((flg >> (15u - i)) & 1u)) continue;
            const unsigned wy = wy0 + 4u * i;
            __syncthreads();
            for (unsigned e = t; e < 64u * 96u; e += 256u) {
                const unsigned tok = e / 96u, c2 = e % 96u;
                const unsigned pix = ((8u * wy + 4u + (tok >> 3)) & 511u) * 512u + ((8u * wx + 4u + (tok & 7u)) & 511u);
                ((unsigned*)xs)[e] = ((const unsigned*)x)[pix * 96u + c2];
            }
            __syncthreads();
            for (unsigned col = t; col < 576u; col += 256u)
                for (unsigned tb = 0; tb < 64u; tb += 8u) {
                    float a[8] = {0.f, 0.f, 0.f, 0.f, 0.f, 0.f, 0.f, 0.f};
                    for (unsigned c = 0; c < 192u; ++c) {
                        const float wt = bf2f(W1[c * 576u + col]);
#pragma unroll
                        for (int j = 0; j < 8; ++j) a[j] += bf2f(xs[(tb + j) * 192u + c]) * wt;
                    }
#pragma unroll
                    for (int j = 0; j < 8; ++j) qs[(tb + j) * 578u + col] = f2bf(a[j]);
                }
            __syncthreads();
            {
                float qv[48], o[48];
#pragma unroll
                for (int d = 0; d < 48; ++d) { qv[d] = bf2f(qs[ln * 578u + w * 48u + d]); o[d] = 0.f; }
                const unsigned short* mrow = (const unsigned short*)mask + (size_t)(wy * 64u + wx) * 4096u + ln * 64u;
                float mx = -__builtin_inff(), l = 0.f;
                for (int pass = 0; pass < 2; ++pass)
                    for (unsigned k = 0; k < 64u; ++k) {
                        float s = 0.f;
#pragma unroll
                        for (int d = 0; d < 48; ++d) s += qv[d] * bf2f(qs[k * 578u + 192u + w * 48u + d]);
                        s = s * 0.14433756729740643f + bf2f(((const unsigned short*)rpb)[((const unsigned*)rpi)[ln * 64u + k] * 4u + w]) + bf2f(mrow[k]);
                        if (pass == 0) mx = fmaxf(mx, s);
                        else {
                            const float p = __expf(s - mx);
                            l += p;
#pragma unroll
                            for (int d = 0; d < 48; ++d) o[d] += p * bf2f(qs[k * 578u + 384u + w * 48u + d]);
                        }
                    }
#pragma unroll
                for (int d = 0; d < 48; ++d) xs[ln * 192u + w * 48u + d] = f2bf(o[d] / l);
            }
            __syncthreads();
            if (t < 192u)
                for (unsigned tb = 0; tb < 64u; tb += 8u) {
                    float a[8] = {0.f, 0.f, 0.f, 0.f, 0.f, 0.f, 0.f, 0.f};
                    for (unsigned c = 0; c < 192u; ++c) {
                        const float wt = bf2f(W2[c * 192u + t]);
#pragma unroll
                        for (int j = 0; j < 8; ++j) a[j] += bf2f(xs[(tb + j) * 192u + c]) * wt;
                    }
                    const unsigned row = (8u * wy + 4u + (tb >> 3)) & 511u;
#pragma unroll
                    for (int j = 0; j < 8; ++j)
                        ((unsigned short*)out)[(row * 512u + ((8u * wx + 4u + j) & 511u)) * 192u + t] = f2bf(a[j]);
                }
        }
    }
}


extern "C" void kernel_launch(void* const* d_in, const int* in_sizes, int n_in, void* d_out, int out_size, void* d_ws,
                              size_t ws_size, hipStream_t stream) {
    win_attn_kernel<<<256, 256, 0, stream>>>((const char*)d_in[0], (const char*)d_in[1], (const char*)d_in[2],
                                             (const char*)d_in[3], (const char*)d_in[4], (const char*)d_in[5],
                                             (char*)d_out, (char*)d_ws);

}
```

```cpp
#include <hip/hip_runtime.h>
#include <hip/hip_bf16.h>

typedef unsigned int u32x4 __attribute__((ext_vector_type(4)));

static __device__ __forceinline__ float bf2f(unsigned short u) { return __uint_as_float((unsigned)u << 16); }
static __device__ __forceinline__ unsigned short f2bf(float f) { return __builtin_bit_cast(unsigned short, (__bf16)f); }

__global__ __launch_bounds__(256, 1) __attribute__((aligned(4096))) void win_attn_kernel(const char* x_, const char* wqkv_, const char* wo_, const char* rpb_,
                                                          const char* rpi_, const char* mask_, char* out_, char* ws) {
    __shared__ __attribute__((aligned(16))) char smem[163840];
    const void* ka = (const void*)__builtin_amdgcn_kernarg_segment_ptr();
    unsigned bid = blockIdx.x, tid = threadIdx.x, flg, w;
    unsigned ldsb = (unsigned)(size_t)smem;
    asm volatile(
        "s_load_dwordx16 s[68:83], s[0:1], 0\n"
        "v_readfirstlane_b32 s42, v0\n"
        "v_and_b32 v1, 63, v0\n"
        "s_lshr_b32 s31, s2, 1\n"
        "s_lshr_b32 s42, s42, 6\n"
        "s_and_b32 s31, s31, 0x7c\n"
        "v_lshlrev_b32 v2, 4, v1\n"
        "s_add_u32 s31, s31, s42\n"
        "s_mul_i32 s35, s31, 256\n"
        "v_min_u32 v10, 3, v1\n"
        "s_getpc_b64 s[66:67]\n"
        "v_lshl_add_u32 v10, v10, 6, s35\n"
        "global_load_dword v60, v10, s[66:67]\n"
        "s_mul_i32 s50, s31, 9\n"
        "v_min_u32 v136, 8, v1\n"
        "v_add_u32 v136, s50, v136\n"
        "v_mul_u32_u24 v133, 10923, v136\n"
        "v_lshlrev_b32 v136, 6, v136\n"
        "v_lshrrev_b32 v133, 16, v133\n"
        "v_mul_u32_u24 v133, 384, v133\n"
        "v_lshl_add_u32 v133, v136, 1, v133\n"
        "v_mul_u32_u24 v11, 171, v1\n"
        "v_lshrrev_b32 v4, 10, v11\n"
        "v_bfe_u32 v7, v11, 13, 1\n"
        "v_add_u32 v11, 10944, v11\n"
        "v_lshrrev_b32 v5, 10, v11\n"
        "v_bfe_u32 v8, v11, 13, 1\n"
        "v_add_u32 v11, 10944, v11\n"
        "v_lshrrev_b32 v6, 10, v11\n"
        "v_bfe_u32 v9, v11, 13, 1\n"
        "s_mul_i32 s40, s42, 3072\n"
        "s_mov_b32 s10, 100663296\n"
        "s_mov_b32 s11, 0x20000\n"
        "s_mov_b32 s6, 16384\n"
        "s_mov_b32 s7, 0x20000\n"
        "s_mov_b32 s62, 1800\n"
        "s_mov_b32 s63, 0x20000\n"
        "s_mov_b32 s22, 221184\n"
        "s_mov_b32 s23, 0x20000\n"
        "s_and_b32 s51, s42, 1\n"
        "v_lshlrev_b32 v134, 3, v0\n"
        "s_lshl_b32 s35, s51, 7\n"
        "v_and_b32 v10, 48, v1\n"
        "v_xor_b32 v131, s35, v2\n"
        "s_lshl_b32 s46, s42, 12\n"
        "v_xor_b32 v131, v131, v10\n"
        "v_xor_b32 v132, 64, v131\n"
        "s_and_b32 s44, s2, 63\n"
        "s_lshl3_add_u32 s44, s44, 4\n"
        "s_lshl_b32 s35, s42, 5\n"
        "s_movk_i32 s41, 384\n"
        "s_movk_i32 s45, -384\n"
        "v_lshrrev_b32 v10, 2, v4\n"
        "v_mad_i32_i24 v11, v10, s45, v2\n"
        "v_add_u32 v10, s44, v10\n"
        "v_lshl_or_b32 v242, v7, 4, s35\n"
        "v_and_b32 v10, 511, v10\n"
        "v_xor_b32 v11, v11, v242\n"
        "v_mad_u32_u24 v242, v10, s41, v11\n"
        "v_lshrrev_b32 v10, 2, v5\n"
        "v_mad_i32_i24 v11, v10, s45, v2\n"
        "v_add_u32 v10, s44, v10\n"
        "v_add_u32 v11, 1024, v11\n"
        "v_lshl_or_b32 v243, v8, 4, s35\n"
        "v_and_b32 v10, 511, v10\n"
        "v_xor_b32 v11, v11, v243\n"
        "v_mad_u32_u24 v243, v10, s41, v11\n"
        "v_lshrrev_b32 v10, 2, v6\n"
        "v_mad_i32_i24 v11, v10, s45, v2\n"
        "v_add_u32 v10, s44, v10\n"
        "v_add_u32 v11, 2048, v11\n"
        "v_lshl_or_b32 v244, v9, 4, s35\n"
        "v_and_b32 v10, 511, v10\n"
        "v_xor_b32 v11, v11, v244\n"
        "v_mad_u32_u24 v244, v10, s41, v11\n"
        "s_mul_i32 s43, s44, 384\n"
        "s_add_u32 s44, s44, 4\n"
        "s_and_b32 s44, s44, 511\n"
        "s_mul_i32 s44, s44, 384\n"
        "s_lshr_b32 s37, s2, 6\n"
        "s_lshl3_add_u32 s47, s37, s42\n"
        "s_add_u32 s47, s47, 68\n"
        "s_mul_i32 s47, s47, 196608\n"
        "s_lshl1_add_u32 s37, s37, s51\n"
        "s_add_u32 s37, s37, 1\n"
        "s_mul_i32 s37, s37, 786432\n"
        "s_mul_i32 s65, s42, 18432\n"
        "s_mov_b32 s14, 33554432\n"
        "s_mov_b32 s15, 0x20000\n"
        "s_mul_i32 s35, s42, 96\n"
        "s_movk_i32 s31, 1056\n"
        "s_movk_i32 s45, 4608\n"
        "v_add_u32 v3, s35, v2\n"
        "s_waitcnt lgkmcnt(0)\n"
        "s_mov_b64 s[60:61], s[74:75]\n"
        "s_mov_b64 s[20:21], s[70:71]\n"
        "s_mov_b64 s[4:5], s[76:77]\n"
        "s_mov_b64 s[8:9], s[68:69]\n"
        "buffer_load_dwordx2 v[144:145], v134, s[60:63], 0 offen\n"
        "s_add_u32 m0, s46, 122880\n"
        "s_mov_b32 s64, s46\n"
        "buffer_load_dword v133, s[20:23], 0 offen lds\n"
        "s_mov_b64 s[52:53], s[72:73]\n"
        "s_mov_b64 s[12:13], s[78:79]\n"
        "buffer_load_dwordx4 v131, s[4:7], s64 offen lds\n"
        "s_mov_b64 s[16:17], s[80:81]\n"
        "v_bfe_u32 v12, v4, 2, 1\n"
        "v_mad_u32_u24 v128, v4, s31, v3\n"
        "v_sub_u32 v12, v12, v7\n"
        "v_mad_i32_i24 v128, v12, s45, v128\n"
        "v_bfe_u32 v13, v5, 2, 1\n"
        "buffer_load_dwordx4 v131, s[4:7], s64 offen offset:1024 lds\n"
        "v_mad_u32_u24 v129, v5, s31, v3\n"
        "v_sub_u32 v13, v13, v8\n"
        "v_mad_i32_i24 v129, v13, s45, v129\n"
        "v_bfe_u32 v14, v6, 2, 1\n"
        "v_mad_u32_u24 v130, v6, s31, v3\n"
        "v_sub_u32 v14, v14, v9\n"
        "v_mad_i32_i24 v130, v14, s45, v130\n"
        "s_lshl_b32 s48, s42, 11\n"
        "s_lshl_b32 s36, s2, 13\n"
        "v_and_b32 v19, 16, v1\n"
        "buffer_load_dwordx4 v132, s[4:7], s64 offen offset:2048 lds\n"
        "s_add_u32 s48, s48, 155648\n"
        "s_add_u32 s36, s36, s48\n"
        "s_add_u32 s36, s36, 4038656\n"
        "s_lshl_b32 s35, s51, 6\n"
        "v_xor_b32 v249, v2, v19\n"
        "v_xor_b32 v249, s35, v249\n"
        "v_xor_b32 v253, 32, v249\n"
        "v_and_b32 v15, 15, v1\n"
        "v_lshrrev_b32 v16, 4, v1\n"
        "v_and_b32 v17, 12, v1\n"
        "buffer_load_dwordx4 v132, s[4:7], s64 offen offset:3072 lds\n"
        "v_and_b32 v18, 0xf0, v2\n"
        "v_add_u32 v17, v17, v15\n"
        "v_bfe_u32 v10, v1, 2, 3\n"
        "v_lshrrev_b32 v11, 5, v1\n"
        "v_lshl_or_b32 v10, v11, 4, v10\n"
        "v_and_b32 v255, 3, v1\n"
        "v_mul_u32_u24 v10, 96, v10\n"
        "v_lshl_add_u32 v255, v255, 3, s65\n"
        "v_add_u32 v255, v255, v10\n"
        "v_lshrrev_b32 v20, 1, v15\n"
        "s_add_u32 m0, s40, 73728\n"
        "s_sub_u32 s64, s47, 12582912\n"
        "buffer_load_dwordx4 v242, s[8:11], s64 offen lds\n"
        "v_xor_b32 v20, v20, v16\n"
        "v_lshlrev_b32 v20, 4, v20\n"
        "v_mad_u32_u24 v240, v17, s41, v20\n"
        "v_lshl_or_b32 v251, v17, 7, v20\n"
        "v_add_u32 v240, 73728, v240\n"
        "v_add_u32 v251, 155648, v251\n"
        "v_xor_b32 v241, 64, v240\n"
        "v_xor_b32 v252, 64, v251\n"
        "s_lshl_b32 s67, s42, 10\n"
        "s_add_u32 s67, s67, 139264\n"
        "s_add_u32 m0, s40, 74752\n"
        "s_nop 0\n"
        "buffer_load_dwordx4 v243, s[8:11], s64 offen lds\n"
        "v_lshlrev_b32 v135, 2, v0\n"
        "v_add_u32 v135, 139264, v135\n"
        "v_lshlrev_b32 v11, 5, v16\n"
        "v_xor_b32 v11, v11, v18\n"
        "v_lshl_or_b32 v11, v17, 8, v11\n"
        "v_add_u32 v140, 122880, v11\n"
        "v_xor_b32 v141, 16, v140\n"
        "v_xor_b32 v142, 128, v140\n"
        "v_xor_b32 v143, 144, v140\n"
        "v_lshrrev_b32 v10, 2, v15\n"
        "s_add_u32 m0, s40, 75776\n"
        "s_nop 0\n"
        "buffer_load_dwordx4 v244, s[8:11], s64 offen lds\n"
        "v_cmp_eq_u32 vcc, v10, v16\n"
        "v_mov_b32 v11, 0x3fb9\n"
        "v_mov_b32 v189, 0\n"
        "v_and_b32 v10, 48, v2\n"
        "v_cndmask_b32 v188, 0, v11, vcc\n"
        "v_lshlrev_b64 v[188:189], v10, v[188:189]\n"
        "v_cmp_gt_u32 vcc, 32, v1\n"
        "v_mov_b32 v11, 15957\n"
        "v_lshrrev_b32 v149, 2, v15\n"
        "v_lshrrev_b32 v151, 3, v19\n"
        "v_mov_b32 v148, 14702\n"
        "v_cndmask_b32 v11, v148, v11, vcc\n"
        "v_xor_b32 v151, v149, v151\n"
        "v_cmp_eq_u32 vcc, 0, v151\n"
        "v_mov_b32 v149, 0\n"
        "v_cndmask_b32 v148, 0, v11, vcc\n"
        "v_lshlrev_b64 v[148:149], v10, v[148:149]\n"
        "v_cmp_eq_u32 vcc, 1, v151\n"
        "v_mov_b32 v151, 0\n"
        "v_cndmask_b32 v150, 0, v11, vcc\n"
        "v_lshlrev_b64 v[150:151], v10, v[150:151]\n"
        "v_and_b32 v10, 8, v1\n"
        "v_add_u32 v10, v10, v15\n"
        "v_mul_u32_u24 v10, 96, v10\n"
        "v_add3_u32 v137, v10, v19, s65\n"
        "v_and_b32 v237, 1, v1\n"
        "v_mul_u32_u24 v237, 0xaaaa, v237\n"
        "v_mov_b64 v[170:171], v[188:189]\n"
        "v_mov_b64 v[210:211], v[188:189]\n"
        "v_mov_b64 v[228:229], v[188:189]\n"
        "v_add_u32 v237, 0x4444, v237\n"
        "v_mov_b32 v11, 0x3f803f80\n"
        "v_accvgpr_write_b32 a252, v11\n"
        "v_accvgpr_write_b32 a253, v11\n"
        "v_accvgpr_write_b32 a254, v11\n"
        "v_accvgpr_write_b32 a255, v11\n"
        "v_lshl_or_b32 v10, v16, 10, v18\n"
        "s_mul_i32 s31, s42, 6144\n"
        "s_add_u32 s31, s31, 73728\n"
        "s_lshl_b32 s35, s51, 9\n"
        "s_add_u32 s35, s35, 73728\n"
        "v_lshl_or_b32 v11, v16, 2, v18\n"
        "v_add_u32 v247, s35, v10\n"
        "v_add_u32 v245, s31, v11\n"
        "v_mul_u32_u24 v10, 384, v16\n"
        "s_movk_i32 s45, 1728\n"
        "v_lshrrev_b32 v235, 2, v15\n"
        "v_lshrrev_b32 v254, 1, v15\n"
        "v_and_b32 v11, 1, v1\n"
        "v_mad_u32_u24 v235, v235, 48, v10\n"
        "v_and_b32 v248, 1, v254\n"
        "v_mad_u32_u24 v235, v248, s45, v235\n"
        "v_mul_u32_u24 v248, 36, v11\n"
        "v_lshlrev_b32 v11, 5, v11\n"
        "v_xor_b32 v254, v254, v248\n"
        "v_add3_u32 v235, v235, v11, s65\n"
        "v_mad_u32_u24 v254, v254, 48, v10\n"
        "v_add3_u32 v254, v254, 16, s65\n"
        "s_lshr_b32 s31, s42, 1\n"
        "s_mul_i32 s35, s31, 36864\n"
        "s_movk_i32 s45, 18048\n"
        "v_mad_u32_u24 v10, v15, 48, v10\n"
        "v_lshrrev_b32 v248, 3, v15\n"
        "v_mad_u32_u24 v10, v248, s45, v10\n"
        "v_add_u32 v248, s35, v10\n"
        "s_lshr_b32 s49, s42, 1\n"
        "s_mul_i32 s49, s49, -192\n"
        "s_add_u32 s49, s49, 128\n"
        "s_mul_i32 s31, s31, 256\n"
        "v_mul_u32_u24 v10, 196608, v16\n"
        "v_mul_u32_u24 v11, 8, v15\n"
        "v_add3_u32 v250, v10, v11, s31\n"
        "s_lshl_b32 s35, s51, 6\n"
        "s_add_u32 s66, s31, s35\n"
        "s_lshl_b32 s35, s42, 5\n"
        "s_sub_u32 s35, s35, s66\n"
        "s_add_u32 s27, s35, 64\n"
        "s_movk_i32 s31, 288\n"
        "s_movk_i32 s35, 768\n"
        "v_mad_u32_u24 v10, v4, s31, v2\n"
        "v_bfe_u32 v11, v10, 6, 1\n"
        "v_add_u32 v10, s66, v10\n"
        "v_mad_i32_i24 v10, v11, s27, v10\n"
        "v_bfe_u32 v11, v4, 1, 1\n"
        "v_bfe_u32 v232, v4, 2, 1\n"
        "v_sub_u32 v11, v11, v232\n"
        "v_mad_i32_i24 v232, v11, s35, v10\n"
        "v_mad_u32_u24 v10, v5, s31, v2\n"
        "v_bfe_u32 v11, v10, 6, 1\n"
        "v_add_u32 v10, s66, v10\n"
        "v_mad_i32_i24 v10, v11, s27, v10\n"
        "v_bfe_u32 v11, v5, 1, 1\n"
        "v_bfe_u32 v233, v5, 2, 1\n"
        "v_sub_u32 v11, v11, v233\n"
        "v_mad_i32_i24 v233, v11, s35, v10\n"
        "v_mad_u32_u24 v10, v6, s31, v2\n"
        "v_bfe_u32 v11, v10, 6, 1\n"
        "v_add_u32 v10, s66, v10\n"
        "v_mad_i32_i24 v10, v11, s27, v10\n"
        "v_bfe_u32 v11, v6, 1, 1\n"
        "v_bfe_u32 v234, v6, 2, 1\n"
        "v_sub_u32 v11, v11, v234\n"
        "v_mad_i32_i24 v234, v11, s35, v10\n"
        "v_mad_i32_i24 v138, v136, -3, v133\n"
        "v_add_u32 v138, v138, v133\n"
        "s_mov_b32 s26, 0x3fb8aa3b\n"
        "s_mov_b32 s24, 0x5d800000\n"
        "s_mov_b32 s27, 14\n"
        "s_mov_b32 s28, 98304\n"
        "s_mov_b32 s29, 73728\n"
        "s_mov_b32 s30, 122880\n"
        "s_mov_b32 s45, 0\n"
        "s_mov_b32 s25, 0\n"
        "s_mov_b32 s54, 73728\n"
        "s_mov_b32 s55, 0x20000\n"
        "s_mov_b32 s18, 100663296\n"
        "s_mov_b32 s19, 0x20000\n"
        "s_mov_b32 s56, 36864\n"
        "s_mov_b32 s57, 73728\n"
        "s_mov_b32 s58, 110592\n"
        "s_mov_b32 s59, 147456\n"
        "s_mov_b32 s60, 184320\n"
        "s_mov_b32 s61, 24576\n"
        "s_mov_b32 s62, 61440\n"
        "s_mov_b32 s63, 12288\n"
        "s_mov_b32 s34, 49152\n"
        "s_add_u32 m0, s40, 86016\n"
        "s_sub_u32 s64, s47, 11796480\n"
        "buffer_load_dwordx4 v242, s[8:11], s64 offen lds\n"
        "s_add_u32 m0, s40, 87040\n"
        "s_nop 0\n"
        "buffer_load_dwordx4 v243, s[8:11], s64 offen lds\n"
        "s_waitcnt vmcnt(10)\n"
        "v_and_b32 v146, 0xffff0000, v144\n"
        "v_and_b32 v147, 0xffff0000, v145\n"
        "v_lshlrev_b32 v144, 16, v144\n"
        "v_lshlrev_b32 v145, 16, v145\n"
        "s_add_u32 m0, s40, 88064\n"
        "v_mul_f32 v146, s26, v146\n"
        "buffer_load_dwordx4 v244, s[8:11], s64 offen lds\n"
        "v_mul_f32 v147, s26, v147\n"
        "v_mul_f32 v144, s26, v144\n"
        "v_mul_f32 v145, s26, v145\n"
        "s_mov_b32 s38, s67\n"
        "s_mov_b32 s39, s67\n"
        "ds_write2st64_b32 v135, v144, v146 offset0:0 offset1:4\n"
        "ds_write2st64_b32 v135, v145, v147 offset0:8 offset1:12\n"
        "s_cmp_eq_u32 s42, 0\n"
        "s_cbranch_scc1 41f\n"
        "s_cmp_eq_u32 s42, 1\n"
        "s_cbranch_scc1 42f\n"
        "s_cmp_eq_u32 s42, 2\n"
        "s_cbranch_scc1 43f\n"
        "s_cmp_eq_u32 s42, 3\n"
        "s_cbranch_scc1 44f\n"
        "60:\n"
        "s_waitcnt lgkmcnt(0)\n"
        "s_waitcnt vmcnt(6)\n"
        "s_barrier\n"
        "s_add_u32 m0, s65, 0\n"
        "s_mov_b32 s64, 384\n"
        "buffer_load_dwordx4 v128, s[20:23], s64 offen lds\n"
        "buffer_load_dwordx4 v129, s[20:23], s64 offen offset:1024 lds\n"
        "ds_read_b128 v[0:3], v140 offset:0\n"
        "ds_read_b128 v[4:7], v141 offset:0\n"
        "ds_read_b128 v[16:19], v140 offset:1024\n"
        "ds_read_b128 v[20:23], v141 offset:1024\n"
        "ds_read_b128 v[8:11], v142 offset:0\n"
        "ds_read_b128 v[24:27], v142 offset:1024\n"
        "ds_read_b128 v[12:15], v143 offset:0\n"
        "ds_read_b128 v[28:31], v143 offset:1024\n"
        "ds_read_b128 v[32:35], v140 offset:8192\n"
        "ds_read_b128 v[36:39], v141 offset:8192\n"
        "ds_read_b128 v[40:43], v142 offset:8192\n"
        "ds_read_b128 v[44:47], v143 offset:8192\n"
        "ds_read_b128 v[48:51], v140 offset:9216\n"
        "s_cmp_eq_u32 s42, 0\n"
        "s_cbranch_scc1 51f\n"
        "s_cmp_eq_u32 s42, 1\n"
        "s_cbranch_scc1 45f\n"
        "s_cmp_eq_u32 s42, 2\n"
        "s_cbranch_scc1 46f\n"
        "s_cmp_eq_u32 s42, 3\n"
        "s_cbranch_scc1 47f\n"
        "s_nop 0\n"
        "s_nop 0\n"
        "s_nop 0\n"
        "s_nop 0\n"
        "s_nop 0\n"
        "s_nop 0\n"
        "s_nop 0\n"
        "s_nop 0\n"
        "s_nop 0\n"
        "s_nop 0\n"
        "s_nop 0\n"
        "s_nop 0\n"
        "s_nop 0\n"
        "s_nop 0\n"
        "s_nop 0\n"
        "s_nop 0\n"
        "s_nop 0\n"
        "s_nop 0\n"
        "s_nop 0\n"
        "s_nop 0\n"
        "s_nop 0\n"
        "s_nop 0\n"
        "61:\n"
        "buffer_load_dwordx4 v130, s[20:23], s64 offen offset:2048 lds\n"
        "s_add_u32 m0, s65, 3072\n"
        "s_waitcnt lgkmcnt(12)\n"
        "v_lshl_add_u64 v[0:1], v[0:1], 2, s[38:39]\n"
        "buffer_load_dwordx4 v128, s[20:23], 0 offen lds\n"
        "v_lshl_add_u64 v[2:3], v[2:3], 2, s[38:39]\n"
        "s_waitcnt lgkmcnt(11)\n"
        "v_lshl_add_u64 v[4:5], v[4:5], 2, s[38:39]\n"
        "v_lshl_add_u64 v[6:7], v[6:7], 2, s[38:39]\n"
        "s_waitcnt lgkmcnt(10)\n"
        "v_lshl_add_u64 v[16:17], v[16:17], 2, s[38:39]\n"
        "v_lshl_add_u64 v[18:19], v[18:19], 2, s[38:39]\n"
        "buffer_load_dwordx4 v129, s[20:23], 0 offen offset:1024 lds\n"
        "ds_read_b32 v0, v0\n"
        "ds_read_b32 v1, v1\n"
        "ds_read_b32 v2, v2\n"
        "ds_read_b32 v3, v3\n"
        "ds_read_b32 v4, v4\n"
        "ds_read_b32 v5, v5\n"
        "ds_read_b32 v6, v6\n"
        "ds_read_b32 v7, v7\n"
        "ds_read_b32 v16, v16\n"
        "ds_read_b32 v17, v17\n"
        "ds_read_b32 v18, v18\n"
        "ds_read_b32 v19, v19\n"
        "buffer_load_dwordx4 v130, s[20:23], 0 offen offset:2048 lds\n"
        "s_waitcnt lgkmcnt(15)\n"
        "v_lshl_add_u64 v[20:21], v[20:21], 2, s[38:39]\n"
        "v_lshl_add_u64 v[22:23], v[22:23], 2, s[38:39]\n"
        "v_lshl_add_u64 v[8:9], v[8:9], 2, s[38:39]\n"
        "v_lshl_add_u64 v[10:11], v[10:11], 2, s[38:39]\n"
        "v_lshl_add_u64 v[24:25], v[24:25], 2, s[38:39]\n"
        "v_lshl_add_u64 v[26:27], v[26:27], 2, s[38:39]\n"
        "ds_read_b32 v20, v20\n"
        "ds_read_b32 v21, v21\n"
        "ds_read_b32 v22, v22\n"
        "ds_read_b32 v23, v23\n"
        "ds_read_b32 v8, v8\n"
        "ds_read_b32 v9, v9\n"
        "ds_read_b32 v10, v10\n"
        "ds_read_b32 v11, v11\n"
        "ds_read_b32 v24, v24\n"
        "ds_read_b32 v25, v25\n"
        "ds_read_b32 v26, v26\n"
        "ds_read_b32 v27, v27\n"
        "s_add_u32 m0, s65, 6144\n"
        "v_lshl_add_u64 v[12:13], v[12:13], 2, s[38:39]\n"
        "buffer_load_dwordx4 v128, s[20:23], s56 offen lds\n"
        "v_lshl_add_u64 v[14:15], v[14:15], 2, s[38:39]\n"
        "v_lshl_add_u64 v[28:29], v[28:29], 2, s[38:39]\n"
        "v_lshl_add_u64 v[30:31], v[30:31], 2, s[38:39]\n"
        "v_lshl_add_u64 v[32:33], v[32:33], 2, s[38:39]\n"
        "v_lshl_add_u64 v[34:35], v[34:35], 2, s[38:39]\n"
        "ds_read_b32 v12, v12\n"
        "ds_read_b32 v13, v13\n"
        "ds_read_b32 v14, v14\n"
        "ds_read_b32 v15, v15\n"
        "ds_read_b32 v28, v28\n"
        "ds_read_b32 v29, v29\n"
        "ds_read_b32 v30, v30\n"
        "ds_read_b32 v31, v31\n"
        "ds_read_b32 v32, v32\n"
        "ds_read_b32 v33, v33\n"
        "ds_read_b32 v34, v34\n"
        "ds_read_b32 v35, v35\n"
        "buffer_load_dwordx4 v129, s[20:23], s56 offen offset:1024 lds\n"
        "v_lshl_add_u64 v[36:37], v[36:37], 2, s[38:39]\n"
        "v_lshl_add_u64 v[38:39], v[38:39], 2, s[38:39]\n"
        "s_waitcnt lgkmcnt(15)\n"
        "v_lshl_add_u64 v[40:41], v[40:41], 2, s[38:39]\n"
        "v_lshl_add_u64 v[42:43], v[42:43], 2, s[38:39]\n"
        "v_lshl_add_u64 v[44:45], v[44:45], 2, s[38:39]\n"
        "v_lshl_add_u64 v[46:47], v[46:47], 2, s[38:39]\n"
        "ds_read_b32 v36, v36\n"
        "ds_read_b32 v37, v37\n"
        "ds_read_b32 v38, v38\n"
        "ds_read_b32 v39, v39\n"
        "ds_read_b32 v40, v40\n"
        "ds_read_b32 v41, v41\n"
        "ds_read_b32 v42, v42\n"
        "ds_read_b32 v43, v43\n"
        "ds_read_b32 v44, v44\n"
        "ds_read_b32 v45, v45\n"
        "ds_read_b32 v46, v46\n"
        "ds_read_b32 v47, v47\n"
        "buffer_load_dwordx4 v130, s[20:23], s56 offen offset:2048 lds\n"
        "v_lshl_add_u64 v[48:49], v[48:49], 2, s[38:39]\n"
        "v_lshl_add_u64 v[50:51], v[50:51], 2, s[38:39]\n"
        "ds_read_b32 v48, v48\n"
        "ds_read_b32 v49, v49\n"
        "ds_read_b32 v50, v50\n"
        "ds_read_b32 v51, v51\n"
        "s_waitcnt vmcnt(9)\n"
        "s_barrier\n"
        "ds_read_b128 a[216:219], v240 offset:0\n"
        "ds_read_b128 a[220:223], v240 offset:1536\n"
        "ds_read_b128 a[224:227], v240 offset:12288\n"
        "ds_read_b128 a[228:231], v240 offset:13824\n"
        "s_waitcnt vmcnt(6)\n"
        "ds_read_b64_tr_b16 a[72:73], v255 offset:0\n"
        "ds_read_b64_tr_b16 a[74:75], v255 offset:768\n"
        "ds_read_b64_tr_b16 a[96:97], v255 offset:32\n"
        "ds_read_b64_tr_b16 a[98:99], v255 offset:800\n"
        "ds_read_b64_tr_b16 a[120:121], v255 offset:64\n"
        "ds_read_b64_tr_b16 a[122:123], v255 offset:832\n"
        "s_add_u32 m0, s65, 8832\n"
        "s_nop 0\n"
        "buffer_load_dwordx4 v128, s[20:23], s56 offen offset:384 lds\n"
        "s_waitcnt lgkmcnt(4)\n"
        "v_mfma_f32_16x16x32_bf16 v[152:155], a[72:75], a[216:219], 0\n"
        "ds_read_b128 a[232:235], v241 offset:0\n"
        "ds_read_b128 a[236:239], v241 offset:1536\n"
        "v_mfma_f32_16x16x32_bf16 v[172:175], a[72:75], a[220:223], 0\n"
        "ds_read_b128 a[240:243], v241 offset:12288\n"
        "ds_read_b128 a[244:247], v241 offset:13824\n"
        "v_mfma_f32_16x16x32_bf16 v[192:195], a[72:75], a[224:227], 0\n"
        "v_mfma_f32_16x16x32_bf16 v[212:215], a[72:75], a[228:231], 0\n"
        "s_waitcnt lgkmcnt(6)\n"
        "buffer_load_dwordx4 v129, s[20:23], s56 offen offset:1408 lds\n"
        "v_mfma_f32_16x16x32_bf16 v[88:91], a[96:99], a[228:231], 0\n"
        "v_mfma_f32_16x16x32_bf16 v[80:83], a[96:99], a[224:227], 0\n"
        "buffer_load_dwordx4 v130, s[20:23], s56 offen offset:2432 lds\n"
        "s_waitcnt vmcnt(6)\n"
        "ds_read_b128 a[212:215], v137 offset:3072\n"
        "v_mfma_f32_16x16x32_bf16 v[68:71], a[96:99], a[220:223], 0\n"
        "s_add_u32 m0, s65, 12288\n"
        "ds_read_b128 a[208:211], v137 offset:3840\n"
        "ds_read_b128 a[204:207], v137 offset:3104\n"
        "v_mfma_f32_16x16x32_bf16 v[64:67], a[96:99], a[216:219], 0\n"
        "s_waitcnt lgkmcnt(3)\n"
        "ds_read_b128 a[200:203], v137 offset:3872\n"
        "ds_read_b128 a[196:199], v137 offset:3136\n"
        "v_mfma_f32_16x16x32_bf16 v[72:75], a[120:123], a[216:219], 0\n"
        "buffer_load_dwordx4 v128, s[20:23], s57 offen lds\n"
        "v_mfma_f32_16x16x32_bf16 v[76:79], a[120:123], a[220:223], 0\n"
        "s_waitcnt lgkmcnt(4)\n"
        "ds_read_b128 a[192:195], v137 offset:3904\n"
        "v_mfma_f32_16x16x32_bf16 v[112:115], a[212:215], v[148:151], 0\n"
        "s_waitcnt lgkmcnt(4)\n"
        "s_waitcnt vmcnt(4)\n"
        "ds_read_b128 a[188:191], v137 offset:6144\n"
        "ds_read_b128 a[184:187], v137 offset:6912\n"
        "v_mfma_f32_16x16x32_bf16 v[116:119], a[208:211], v[148:151], 0\n"
        "buffer_load_dwordx4 v129, s[20:23], s57 offen offset:1024 lds\n"
        "v_mfma_f32_16x16x32_bf16 v[84:87], a[120:123], a[224:227], 0\n"
        "ds_read_b128 a[180:183], v137 offset:6176\n"
        "ds_read_b128 a[176:179], v137 offset:6944\n"
        "v_mfma_f32_16x16x32_bf16 v[92:95], a[120:123], a[228:231], 0\n"
        "s_waitcnt lgkmcnt(5)\n"
        "ds_read_b128 a[172:175], v137 offset:6208\n"
        "ds_read_b128 a[168:171], v137 offset:6976\n"
        "v_mfma_f32_16x16x32_bf16 v[120:123], a[204:207], v[148:151], 0\n"
        "v_cvt_pk_bf16_f32 v112, v112, v113\n"
        "v_accvgpr_write_b32 a0, v112\n"
        "v_mfma_f32_16x16x32_bf16 v[124:127], a[200:203], v[148:151], 0\n"
        "buffer_load_dwordx4 v130, s[20:23], s57 offen offset:2048 lds\n"
        "v_mfma_f32_16x16x32_bf16 v[96:99], a[196:199], v[148:151], 0\n"
        "s_waitcnt lgkmcnt(4)\n"
        "v_cvt_pk_bf16_f32 v114, v114, v115\n"
        "v_accvgpr_write_b32 a1, v114\n"
        "v_mfma_f32_16x16x32_bf16 v[100:103], a[192:195], v[148:151], 0\n"
        "s_add_u32 m0, s65, 14976\n"
        "v_cvt_pk_bf16_f32 v116, v116, v117\n"
        "v_accvgpr_write_b32 a2, v116\n"
        "v_mfma_f32_16x16x32_bf16 v[104:107], a[188:191], v[148:151], 0\n"
        "v_cvt_pk_bf16_f32 v118, v118, v119\n"
        "v_accvgpr_write_b32 a3, v118\n"
        "v_mfma_f32_16x16x32_bf16 v[108:111], a[184:187], v[148:151], 0\n"
        "buffer_load_dwordx4 v128, s[20:23], s57 offen offset:384 lds\n"
        "v_mfma_f32_16x16x32_bf16 v[156:159], a[0:3], a[216:219], 0\n"
        "v_cvt_pk_bf16_f32 v120, v120, v121\n"
        "v_accvgpr_write_b32 a24, v120\n"
        "v_mfma_f32_16x16x32_bf16 v[176:179], a[0:3], a[220:223], 0\n"
        "v_cvt_pk_bf16_f32 v122, v122, v123\n"
        "v_accvgpr_write_b32 a25, v122\n"
        "v_mfma_f32_16x16x32_bf16 v[196:199], a[0:3], a[224:227], 0\n"
        "v_cvt_pk_bf16_f32 v124, v124, v125\n"
        "v_accvgpr_write_b32 a26, v124\n"
        "v_mfma_f32_16x16x32_bf16 v[216:219], a[0:3], a[228:231], 0\n"
        "buffer_load_dwordx4 v129, s[20:23], s57 offen offset:1408 lds\n"
        "v_cvt_pk_bf16_f32 v126, v126, v127\n"
        "v_accvgpr_write_b32 a27, v126\n"
        "s_nop 1\n"
        "v_mfma_f32_16x16x32_bf16 v[224:227], a[24:27], a[228:231], 0\n"
        "v_cvt_pk_bf16_f32 v96, v96, v97\n"
        "v_accvgpr_write_b32 a48, v96\n"
        "v_mfma_f32_16x16x32_bf16 v[204:207], a[24:27], a[224:227], 0\n"
        "s_waitcnt vmcnt(5)\n"
        "ds_read_b64_tr_b16 a[76:77], v255 offset:9216\n"
        "ds_read_b64_tr_b16 a[78:79], v255 offset:9984\n"
        "v_mfma_f32_16x16x32_bf16 v[184:187], a[24:27], a[220:223], 0\n"
        "ds_read_b64_tr_b16 a[100:101], v255 offset:9248\n"
        "ds_read_b64_tr_b16 a[102:103], v255 offset:10016\n"
        "v_mfma_f32_16x16x32_bf16 v[164:167], a[24:27], a[216:219], 0\n"
        "buffer_load_dwordx4 v130, s[20:23], s57 offen offset:2432 lds\n"
        "ds_read_b64_tr_b16 a[124:125], v255 offset:9280\n"
        "ds_read_b64_tr_b16 a[126:127], v255 offset:10048\n"
        "v_cvt_pk_bf16_f32 v98, v98, v99\n"
        "v_accvgpr_write_b32 a49, v98\n"
        "v_cvt_pk_bf16_f32 v100, v100, v101\n"
        "v_accvgpr_write_b32 a50, v100\n"
        "v_cvt_pk_bf16_f32 v102, v102, v103\n"
        "v_accvgpr_write_b32 a51, v102\n"
        "s_nop 1\n"
        "v_mfma_f32_16x16x32_bf16 v[160:163], a[48:51], a[216:219], 0\n"
        "s_add_u32 m0, s65, 0\n"
        "ds_read_b128 a[248:251], v240 offset:128\n"
        "v_cvt_pk_bf16_f32 v104, v104, v105\n"
        "v_mfma_f32_16x16x32_bf16 v[180:183], a[48:51], a[220:223], 0\n"
        "ds_read_b128 a[216:219], v240 offset:1664\n"
        "v_accvgpr_write_b32 a4, v104\n"
        "v_mfma_f32_16x16x32_bf16 v[200:203], a[48:51], a[224:227], 0\n"
        "buffer_load_dwordx4 v128, s[20:23], s58 offen lds\n"
        "v_mfma_f32_16x16x32_bf16 v[220:223], a[48:51], a[228:231], 0\n"
        "s_waitcnt lgkmcnt(6)\n"
        "ds_read_b128 a[220:223], v240 offset:12416\n"
        "ds_read_b128 a[224:227], v240 offset:13952\n"
        "v_mfma_f32_16x16x32_bf16 v[112:115], a[180:183], v[148:151], 0\n"
        "v_cvt_pk_bf16_f32 v106, v106, v107\n"
        "v_accvgpr_write_b32 a5, v106\n"
        "v_mfma_f32_16x16x32_bf16 v[116:119], a[176:179], v[148:151], 0\n"
        "v_cvt_pk_bf16_f32 v108, v108, v109\n"
        "v_accvgpr_write_b32 a6, v108\n"
        "v_mfma_f32_16x16x32_bf16 v[152:155], a[76:79], a[232:235], v[152:155]\n"
        "buffer_load_dwordx4 v129, s[20:23], s58 offen offset:1024 lds\n"
        "v_mfma_f32_16x16x32_bf16 v[172:175], a[76:79], a[236:239], v[172:175]\n"
        "v_cvt_pk_bf16_f32 v110, v110, v111\n"
        "v_accvgpr_write_b32 a7, v110\n"
        "v_mfma_f32_16x16x32_bf16 v[192:195], a[76:79], a[240:243], v[192:195]\n"
        "v_cvt_pk_bf16_f32 v112, v112, v113\n"
        "v_accvgpr_write_b32 a28, v112\n"
        "v_mfma_f32_16x16x32_bf16 v[212:215], a[76:79], a[244:247], v[212:215]\n"
        "v_cvt_pk_bf16_f32 v114, v114, v115\n"
        "v_accvgpr_write_b32 a29, v114\n"
        "v_mfma_f32_16x16x32_bf16 v[156:159], a[4:7], a[232:235], v[156:159]\n"
        "buffer_load_dwordx4 v130, s[20:23], s58 offen offset:2048 lds\n"
        "v_mfma_f32_16x16x32_bf16 v[176:179], a[4:7], a[236:239], v[176:179]\n"
        "s_add_u32 m0, s65, 2688\n"
        "v_cvt_pk_bf16_f32 v116, v116, v117\n"
        "v_accvgpr_write_b32 a30, v116\n"
        "v_mfma_f32_16x16x32_bf16 v[196:199], a[4:7], a[240:243], v[196:199]\n"
        ".long 0xbec200ff\n"
        "41:\n"
        "s_branch 60b\n"
        "v_cvt_pk_bf16_f32 v118, v118, v119\n"
        "v_accvgpr_write_b32 a31, v118\n"
        "v_mfma_f32_16x16x32_bf16 v[216:219], a[4:7], a[244:247], v[216:219]\n"
        "s_waitcnt vmcnt(6)\n"
        "ds_read_b128 a[212:215], v137 offset:12288\n"
        "ds_read_b128 a[208:211], v137 offset:13056\n"
        "v_mfma_f32_16x16x32_bf16 v[120:123], a[172:175], v[148:151], 0\n"
        "buffer_load_dwordx4 v128, s[20:23], s58 offen offset:384 lds\n"
        "v_mfma_f32_16x16x32_bf16 v[124:127], a[168:171], v[148:151], 0\n"
        "ds_read_b128 a[204:207], v137 offset:12320\n"
        "ds_read_b128 a[200:203], v137 offset:13088\n"
        "v_mfma_f32_16x16x32_bf16 v[224:227], a[28:31], a[244:247], v[224:227]\n"
        "ds_read_b128 a[196:199], v137 offset:12352\n"
        "ds_read_b128 a[192:195], v137 offset:13120\n"
        "v_mfma_f32_16x16x32_bf16 v[204:207], a[28:31], a[240:243], v[204:207]\n"
        "buffer_load_dwordx4 v129, s[20:23], s58 offen offset:1408 lds\n"
        "v_mfma_f32_16x16x32_bf16 v[184:187], a[28:31], a[236:239], v[184:187]\n"
        "v_cvt_pk_bf16_f32 v120, v120, v121\n"
        "v_accvgpr_write_b32 a52, v120\n"
        "v_mfma_f32_16x16x32_bf16 v[164:167], a[28:31], a[232:235], v[164:167]\n"
        "s_waitcnt lgkmcnt(4)\n"
        "v_cvt_pk_bf16_f32 v122, v122, v123\n"
        "v_accvgpr_write_b32 a53, v122\n"
        "v_mfma_f32_16x16x32_bf16 v[96:99], a[212:215], v[148:151], 0\n"
        "v_cvt_pk_bf16_f32 v124, v124, v125\n"
        "v_accvgpr_write_b32 a54, v124\n"
        "v_mfma_f32_16x16x32_bf16 v[100:103], a[208:211], v[148:151], 0\n"
        "buffer_load_dwordx4 v130, s[20:23], s58 offen offset:2432 lds\n"
        "v_mfma_f32_16x16x32_bf16 v[88:91], a[100:103], a[244:247], v[88:91]\n"
        "s_add_u32 m0, s65, 6144\n"
        "v_cvt_pk_bf16_f32 v126, v126, v127\n"
        "v_accvgpr_write_b32 a55, v126\n"
        "v_mfma_f32_16x16x32_bf16 v[80:83], a[100:103], a[240:243], v[80:83]\n"
        "v_cvt_pk_bf16_f32 v96, v96, v97\n"
        "v_accvgpr_write_b32 a8, v96\n"
        "v_mfma_f32_16x16x32_bf16 v[68:71], a[100:103], a[236:239], v[68:71]\n"
        "v_cvt_pk_bf16_f32 v98, v98, v99\n"
        "v_accvgpr_write_b32 a9, v98\n"
        "v_mfma_f32_16x16x32_bf16 v[64:67], a[100:103], a[232:235], v[64:67]\n"
        "buffer_load_dwordx4 v128, s[20:23], s59 offen lds\n"
        "v_mfma_f32_16x16x32_bf16 v[160:163], a[52:55], a[232:235], v[160:163]\n"
        "s_waitcnt vmcnt(7)\n"
        "ds_read_b64_tr_b16 a[80:81], v255 offset:15360\n"
        "ds_read_b64_tr_b16 a[82:83], v255 offset:16128\n"
        "v_mfma_f32_16x16x32_bf16 v[180:183], a[52:55], a[236:239], v[180:183]\n"
        "ds_read_b64_tr_b16 a[104:105], v255 offset:15392\n"
        "ds_read_b64_tr_b16 a[106:107], v255 offset:16160\n"
        "v_mfma_f32_16x16x32_bf16 v[200:203], a[52:55], a[240:243], v[200:203]\n"
        "ds_read_b64_tr_b16 a[128:129], v255 offset:15424\n"
        "ds_read_b64_tr_b16 a[130:131], v255 offset:16192\n"
        "v_mfma_f32_16x16x32_bf16 v[220:223], a[52:55], a[244:247], v[220:223]\n"
        "s_waitcnt lgkmcnt(6)\n"
        "v_cvt_pk_bf16_f32 v100, v100, v101\n"
        "v_accvgpr_write_b32 a10, v100\n"
        "v_mfma_f32_16x16x32_bf16 v[104:107], a[204:207], v[148:151], 0\n"
        "buffer_load_dwordx4 v129, s[20:23], s59 offen offset:1024 lds\n"
        "v_mfma_f32_16x16x32_bf16 v[108:111], a[200:203], v[148:151], 0\n"
        "ds_read_b128 a[228:231], v241 offset:128\n"
        "v_cvt_pk_bf16_f32 v102, v102, v103\n"
        "v_mfma_f32_16x16x32_bf16 v[72:75], a[124:127], a[232:235], v[72:75]\n"
        "ds_read_b128 a[232:235], v241 offset:1664\n"
        "v_accvgpr_write_b32 a11, v102\n"
        "v_mfma_f32_16x16x32_bf16 v[76:79], a[124:127], a[236:239], v[76:79]\n"
        "buffer_load_dwordx4 v130, s[20:23], s59 offen offset:2048 lds\n"
        "v_mfma_f32_16x16x32_bf16 v[84:87], a[124:127], a[240:243], v[84:87]\n"
        "s_add_u32 m0, s65, 8832\n"
        "ds_read_b128 a[236:239], v241 offset:12416\n"
        "ds_read_b128 a[240:243], v241 offset:13952\n"
        "v_mfma_f32_16x16x32_bf16 v[92:95], a[124:127], a[244:247], v[92:95]\n"
        "s_waitcnt vmcnt(6)\n"
        "ds_read_b128 a[188:191], v137 offset:0\n"
        "ds_read_b128 a[184:187], v137 offset:768\n"
        "v_mfma_f32_16x16x32_bf16 v[156:159], a[8:11], a[248:251], v[156:159]\n"
        "buffer_load_dwordx4 v128, s[20:23], s59 offen offset:384 lds\n"
        "v_mfma_f32_16x16x32_bf16 v[176:179], a[8:11], a[216:219], v[176:179]\n"
        "ds_read_b128 a[180:183], v137 offset:32\n"
        "ds_read_b128 a[176:179], v137 offset:800\n"
        "v_mfma_f32_16x16x32_bf16 v[196:199], a[8:11], a[220:223], v[196:199]\n"
        "ds_read_b128 a[172:175], v137 offset:64\n"
        "ds_read_b128 a[168:171], v137 offset:832\n"
        "v_mfma_f32_16x16x32_bf16 v[216:219], a[8:11], a[224:227], v[216:219]\n"
        "v_cvt_pk_bf16_f32 v104, v104, v105\n"
        "v_accvgpr_write_b32 a32, v104\n"
        "v_mfma_f32_16x16x32_bf16 v[112:115], a[196:199], v[148:151], 0\n"
        "buffer_load_dwordx4 v129, s[20:23], s59 offen offset:1408 lds\n"
        "v_mfma_f32_16x16x32_bf16 v[116:119], a[192:195], v[148:151], 0\n"
        "s_waitcnt lgkmcnt(4)\n"
        "v_cvt_pk_bf16_f32 v106, v106, v107\n"
        "v_accvgpr_write_b32 a33, v106\n"
        "v_mfma_f32_16x16x32_bf16 v[152:155], a[80:83], a[248:251], v[152:155]\n"
        "v_cvt_pk_bf16_f32 v108, v108, v109\n"
        "v_accvgpr_write_b32 a34, v108\n"
        "v_mfma_f32_16x16x32_bf16 v[172:175], a[80:83], a[216:219], v[172:175]\n"
        "v_cvt_pk_bf16_f32 v110, v110, v111\n"
        "v_accvgpr_write_b32 a35, v110\n"
        "v_mfma_f32_16x16x32_bf16 v[192:195], a[80:83], a[220:223], v[192:195]\n"
        "buffer_load_dwordx4 v130, s[20:23], s59 offen offset:2432 lds\n"
        "v_mfma_f32_16x16x32_bf16 v[212:215], a[80:83], a[224:227], v[212:215]\n"
        "s_add_u32 m0, s65, 12288\n"
        "v_cvt_pk_bf16_f32 v112, v112, v113\n"
        "v_accvgpr_write_b32 a56, v112\n"
        "v_mfma_f32_16x16x32_bf16 v[224:227], a[32:35], a[224:227], v[224:227]\n"
        "ds_read_b128 v[52:55], v141 offset:9216\n"
        "v_cvt_pk_bf16_f32 v114, v114, v115\n"
        "v_mfma_f32_16x16x32_bf16 v[204:207], a[32:35], a[220:223], v[204:207]\n"
        "v_accvgpr_write_b32 a57, v114\n"
        "v_cvt_pk_bf16_f32 v116, v116, v117\n"
        "v_mfma_f32_16x16x32_bf16 v[184:187], a[32:35], a[216:219], v[184:187]\n"
        "buffer_load_dwordx4 v128, s[20:23], s60 offen lds\n"
        "v_mfma_f32_16x16x32_bf16 v[164:167], a[32:35], a[248:251], v[164:167]\n"
        "v_accvgpr_write_b32 a58, v116\n"
        "v_cvt_pk_bf16_f32 v118, v118, v119\n"
        "v_mfma_f32_16x16x32_bf16 v[120:123], a[188:191], v[148:151], 0\n"
        "s_waitcnt vmcnt(7)\n"
        "ds_read_b64_tr_b16 a[84:85], v255 offset:3072\n"
        "ds_read_b64_tr_b16 a[86:87], v255 offset:3840\n"
        "v_mfma_f32_16x16x32_bf16 v[124:127], a[184:187], v[148:151], 0\n"
        "ds_read_b64_tr_b16 a[108:109], v255 offset:3104\n"
        "ds_read_b64_tr_b16 a[110:111], v255 offset:3872\n"
        "v_mfma_f32_16x16x32_bf16 v[88:91], a[104:107], a[224:227], v[88:91]\n"
        "buffer_load_dwordx4 v129, s[20:23], s60 offen offset:1024 lds\n"
        "ds_read_b64_tr_b16 a[132:133], v255 offset:3136\n"
        "ds_read_b64_tr_b16 a[134:135], v255 offset:3904\n"
        "v_mfma_f32_16x16x32_bf16 v[80:83], a[104:107], a[220:223], v[80:83]\n"
        "v_accvgpr_write_b32 a59, v118\n"
        "v_cvt_pk_bf16_f32 v120, v120, v121\n"
        "v_mfma_f32_16x16x32_bf16 v[68:71], a[104:107], a[216:219], v[68:71]\n"
        "v_accvgpr_write_b32 a12, v120\n"
        "v_cvt_pk_bf16_f32 v122, v122, v123\n"
        "v_mfma_f32_16x16x32_bf16 v[64:67], a[104:107], a[248:251], v[64:67]\n"
        "buffer_load_dwordx4 v130, s[20:23], s60 offen offset:2048 lds\n"
        "v_mfma_f32_16x16x32_bf16 v[160:163], a[56:59], a[248:251], v[160:163]\n"
        "s_add_u32 m0, s65, 14976\n"
        "v_accvgpr_write_b32 a13, v122\n"
        "v_cvt_pk_bf16_f32 v124, v124, v125\n"
        "v_mfma_f32_16x16x32_bf16 v[180:183], a[56:59], a[216:219], v[180:183]\n"
        "s_waitcnt lgkmcnt(2)\n"
        "v_lshl_add_u64 v[52:53], v[52:53], 2, s[38:39]\n"
        "v_lshl_add_u64 v[54:55], v[54:55], 2, s[38:39]\n"
        "v_mfma_f32_16x16x32_bf16 v[200:203], a[56:59], a[220:223], v[200:203]\n"
        "ds_read_b32 v52, v52\n"
        "ds_read_b32 v53, v53\n"
        "v_mfma_f32_16x16x32_bf16 v[220:223], a[56:59], a[224:227], v[220:223]\n"
        "ds_read_b32 v54, v54\n"
        "ds_read_b32 v55, v55\n"
        "v_mfma_f32_16x16x32_bf16 v[96:99], a[180:183], v[148:151], 0\n"
        "buffer_load_dwordx4 v128, s[20:23], s60 offen offset:384 lds\n"
        "v_mfma_f32_16x16x32_bf16 v[100:103], a[176:179], v[148:151], 0\n"
        "ds_read_b128 a[244:247], v240 offset:256\n"
        "v_accvgpr_write_b32 a14, v124\n"
        "v_mfma_f32_16x16x32_bf16 v[72:75], a[128:131], a[248:251], v[72:75]\n"
        "ds_read_b128 a[248:251], v240 offset:1792\n"
        "v_cvt_pk_bf16_f32 v126, v126, v127\n"
        "v_mfma_f32_16x16x32_bf16 v[76:79], a[128:131], a[216:219], v[76:79]\n"
        "buffer_load_dwordx4 v129, s[20:23], s60 offen offset:1408 lds\n"
        "v_mfma_f32_16x16x32_bf16 v[84:87], a[128:131], a[220:223], v[84:87]\n"
        "ds_read_b128 a[216:219], v240 offset:12544\n"
        "ds_read_b128 a[220:223], v240 offset:14080\n"
        "v_mfma_f32_16x16x32_bf16 v[92:95], a[128:131], a[224:227], v[92:95]\n"
        "v_accvgpr_write_b32 a15, v126\n"
        "v_cvt_pk_bf16_f32 v96, v96, v97\n"
        "s_nop 0\n"
        "v_mfma_f32_16x16x32_bf16 v[156:159], a[12:15], a[228:231], v[156:159]\n"
        "v_accvgpr_write_b32 a36, v96\n"
        "v_cvt_pk_bf16_f32 v98, v98, v99\n"
        "v_mfma_f32_16x16x32_bf16 v[176:179], a[12:15], a[232:235], v[176:179]\n"
        "buffer_load_dwordx4 v130, s[20:23], s60 offen offset:2432 lds\n"
        "v_mfma_f32_16x16x32_bf16 v[196:199], a[12:15], a[236:239], v[196:199]\n"
        "v_accvgpr_write_b32 a37, v98\n"
        "v_cvt_pk_bf16_f32 v100, v100, v101\n"
        "v_mfma_f32_16x16x32_bf16 v[216:219], a[12:15], a[240:243], v[216:219]\n"
        "s_waitcnt vmcnt(9)\n"
        "ds_read_b128 a[212:215], v137 offset:6144\n"
        "ds_read_b128 a[208:211], v137 offset:6912\n"
        "v_mfma_f32_16x16x32_bf16 v[104:107], a[172:175], v[148:151], 0\n"
        "s_mov_b32 m0, s48\n"
        "ds_read_b128 a[204:207], v137 offset:6176\n"
        "ds_read_b128 a[200:203], v137 offset:6944\n"
        "v_mfma_f32_16x16x32_bf16 v[108:111], a[168:171], v[148:151], 0\n"
        "s_sub_u32 s64, s36, 4194304\n"
        "ds_read_b128 a[196:199], v137 offset:6208\n"
        "ds_read_b128 a[192:195], v137 offset:6976\n"
        "v_mfma_f32_16x16x32_bf16 v[152:155], a[84:87], a[228:231], v[152:155]\n"
        "buffer_load_dword v138, s[20:23], 0 offen offset:768 lds\n"
        "v_mfma_f32_16x16x32_bf16 v[172:175], a[84:87], a[232:235], v[172:175]\n"
        "v_accvgpr_write_b32 a38, v100\n"
        "v_cvt_pk_bf16_f32 v102, v102, v103\n"
        "v_mfma_f32_16x16x32_bf16 v[192:195], a[84:87], a[236:239], v[192:195]\n"
        "v_accvgpr_write_b32 a39, v102\n"
        "v_cvt_pk_bf16_f32 v104, v104, v105\n"
        "v_mfma_f32_16x16x32_bf16 v[212:215], a[84:87], a[240:243], v[212:215]\n"
        "buffer_load_dwordx4 v249, s[12:15], s64 offen lds\n"
        "v_mfma_f32_16x16x32_bf16 v[224:227], a[36:39], a[240:243], v[224:227]\n"
        "v_accvgpr_write_b32 a60, v104\n"
        "v_cvt_pk_bf16_f32 v106, v106, v107\n"
        "v_mfma_f32_16x16x32_bf16 v[204:207], a[36:39], a[236:239], v[204:207]\n"
        "ds_read_b128 v[56:59], v142 offset:9216\n"
        "v_accvgpr_write_b32 a61, v106\n"
        "v_mfma_f32_16x16x32_bf16 v[184:187], a[36:39], a[232:235], v[184:187]\n"
        "s_waitcnt vmcnt(8)\n"
        "ds_read_b64_tr_b16 a[88:89], v255 offset:9216\n"
        "ds_read_b64_tr_b16 a[90:91], v255 offset:9984\n"
        "v_mfma_f32_16x16x32_bf16 v[164:167], a[36:39], a[228:231], v[164:167]\n"
        "s_waitcnt lgkmcnt(3)\n"
        "ds_read_b64_tr_b16 a[112:113], v255 offset:9248\n"
        "ds_read_b64_tr_b16 a[114:115], v255 offset:10016\n"
        "v_mfma_f32_16x16x32_bf16 v[112:115], a[212:215], v[148:151], 0\n"
        "buffer_load_dwordx4 v253, s[12:15], s64 offen offset:1024 lds\n"
        "ds_read_b64_tr_b16 a[136:137], v255 offset:9280\n"
        "v_mfma_f32_16x16x32_bf16 v[116:119], a[208:211], v[148:151], 0\n"
        "ds_read_b64_tr_b16 a[138:139], v255 offset:10048\n"
        "s_add_u32 m0, s65, 0\n"
        "v_cvt_pk_bf16_f32 v108, v108, v109\n"
        "v_mfma_f32_16x16x32_bf16 v[88:91], a[108:111], a[240:243], v[88:91]\n"
        "s_mov_b32 s64, 768\n"
        "v_accvgpr_write_b32 a62, v108\n"
        "v_cvt_pk_bf16_f32 v110, v110, v111\n"
        "v_mfma_f32_16x16x32_bf16 v[80:83], a[108:111], a[236:239], v[80:83]\n"
        "buffer_load_dwordx4 v128, s[20:23], s64 offen lds\n"
        "v_mfma_f32_16x16x32_bf16 v[68:71], a[108:111], a[232:235], v[68:71]\n"
        "v_accvgpr_write_b32 a63, v110\n"
        "v_cvt_pk_bf16_f32 v112, v112, v113\n"
        "v_mfma_f32_16x16x32_bf16 v[64:67], a[108:111], a[228:231], v[64:67]\n"
        "v_accvgpr_write_b32 a16, v112\n"
        "v_cvt_pk_bf16_f32 v114, v114, v115\n"
        "v_mfma_f32_16x16x32_bf16 v[160:163], a[60:63], a[228:231], v[160:163]\n"
        "v_accvgpr_write_b32 a17, v114\n"
        "v_cvt_pk_bf16_f32 v116, v116, v117\n"
        "v_mfma_f32_16x16x32_bf16 v[180:183], a[60:63], a[232:235], v[180:183]\n"
        "buffer_load_dwordx4 v129, s[20:23], s64 offen offset:1024 lds\n"
        "v_mfma_f32_16x16x32_bf16 v[200:203], a[60:63], a[236:239], v[200:203]\n"
        "ds_read_b128 a[224:227], v241 offset:256\n"
        "s_waitcnt lgkmcnt(1)\n"
        "v_lshl_add_u64 v[56:57], v[56:57], 2, s[38:39]\n"
        "v_mfma_f32_16x16x32_bf16 v[220:223], a[60:63], a[240:243], v[220:223]\n"
        "ds_read_b32 v56, v56\n"
        "ds_read_b32 v57, v57\n"
        "v_mfma_f32_16x16x32_bf16 v[120:123], a[204:207], v[148:151], 0\n"
        "v_lshl_add_u64 v[58:59], v[58:59], 2, s[38:39]\n"
        "v_accvgpr_write_b32 a18, v116\n"
        "v_mfma_f32_16x16x32_bf16 v[124:127], a[200:203], v[148:151], 0\n"
        "buffer_load_dwordx4 v130, s[20:23], s64 offen offset:2048 lds\n"
        "v_mfma_f32_16x16x32_bf16 v[72:75], a[132:135], a[228:231], v[72:75]\n"
        "s_add_u32 m0, s65, 2304\n"
        "ds_read_b32 v58, v58\n"
        "ds_read_b32 v59, v59\n"
        "v_mfma_f32_16x16x32_bf16 v[76:79], a[132:135], a[232:235], v[76:79]\n"
        "ds_read_b128 a[228:231], v241 offset:1792\n"
        "ds_read_b128 a[232:235], v241 offset:12544\n"
        "v_mfma_f32_16x16x32_bf16 v[84:87], a[132:135], a[236:239], v[84:87]\n"
        "ds_read_b128 a[236:239], v241 offset:14080\n"
        "v_cvt_pk_bf16_f32 v118, v118, v119\n"
        "v_mfma_f32_16x16x32_bf16 v[92:95], a[132:135], a[240:243], v[92:95]\n"
        "buffer_load_dwordx4 v128, s[20:23], s56 offen offset:768 lds\n"
        "v_accvgpr_write_b32 a19, v118\n"
        "s_nop 1\n"
        "v_mfma_f32_16x16x32_bf16 v[156:159], a[16:19], a[244:247], v[156:159]\n"
        "v_cvt_pk_bf16_f32 v120, v120, v121\n"
        "v_accvgpr_write_b32 a40, v120\n"
        "v_mfma_f32_16x16x32_bf16 v[176:179], a[16:19], a[248:251], v[176:179]\n"
        "v_cvt_pk_bf16_f32 v122, v122, v123\n"
        "v_accvgpr_write_b32 a41, v122\n"
        "v_mfma_f32_16x16x32_bf16 v[196:199], a[16:19], a[216:219], v[196:199]\n"
        "v_cvt_pk_bf16_f32 v124, v124, v125\n"
        "v_accvgpr_write_b32 a42, v124\n"
        "v_mfma_f32_16x16x32_bf16 v[216:219], a[16:19], a[220:223], v[216:219]\n"
        "buffer_load_dwordx4 v129, s[20:23], s56 offen offset:1792 lds\n"
        "v_mfma_f32_16x16x32_bf16 v[96:99], a[196:199], v[148:151], 0\n"
        "v_cvt_pk_bf16_f32 v126, v126, v127\n"
        "v_accvgpr_write_b32 a43, v126\n"
        "v_mfma_f32_16x16x32_bf16 v[100:103], a[192:195], v[148:151], 0\n"
        "s_waitcnt vmcnt(11)\n"
        "ds_read_b128 a[188:191], v137 offset:12288\n"
        "ds_read_b128 a[184:187], v137 offset:13056\n"
        "v_mfma_f32_16x16x32_bf16 v[152:155], a[88:91], a[244:247], v[152:155]\n"
        "ds_read_b128 a[180:183], v137 offset:12320\n"
        "ds_read_b128 a[176:179], v137 offset:13088\n"
        "v_mfma_f32_16x16x32_bf16 v[172:175], a[88:91], a[248:251], v[172:175]\n"
        "buffer_load_dwordx4 v130, s[20:23], s56 offen offset:2816 lds\n"
        "v_mfma_f32_16x16x32_bf16 v[192:195], a[88:91], a[216:219], v[192:195]\n"
        "s_add_u32 m0, s65, 5376\n"
        "ds_read_b128 a[172:175], v137 offset:12352\n"
        "ds_read_b128 a[168:171], v137 offset:13120\n"
        "v_mfma_f32_16x16x32_bf16 v[212:215], a[88:91], a[220:223], v[212:215]\n"
        "v_cvt_pk_bf16_f32 v96, v96, v97\n"
        "v_accvgpr_write_b32 a64, v96\n"
        "v_mfma_f32_16x16x32_bf16 v[224:227], a[40:43], a[220:223], v[224:227]\n"
        "v_cvt_pk_bf16_f32 v98, v98, v99\n"
        "v_accvgpr_write_b32 a65, v98\n"
        "v_mfma_f32_16x16x32_bf16 v[204:207], a[40:43], a[216:219], v[204:207]\n"
        "buffer_load_dwordx4 v128, s[20:23], s57 offen offset:768 lds\n"
        "v_mfma_f32_16x16x32_bf16 v[184:187], a[40:43], a[248:251], v[184:187]\n"
        "v_cvt_pk_bf16_f32 v100, v100, v101\n"
        "v_accvgpr_write_b32 a66, v100\n"
        "v_mfma_f32_16x16x32_bf16 v[164:167], a[40:43], a[244:247], v[164:167]\n"
        "s_waitcnt lgkmcnt(2)\n"
        "ds_read_b128 v[60:63], v143 offset:9216\n"
        "v_cvt_pk_bf16_f32 v102, v102, v103\n"
        "v_mfma_f32_16x16x32_bf16 v[104:107], a[188:191], v[148:151], 0\n"
        "v_accvgpr_write_b32 a67, v102\n"
        "v_mfma_f32_16x16x32_bf16 v[108:111], a[184:187], v[148:151], 0\n"
        "buffer_load_dwordx4 v129, s[20:23], s57 offen offset:1792 lds\n"
        "v_mfma_f32_16x16x32_bf16 v[88:91], a[112:115], a[220:223], v[88:91]\n"
        "v_mfma_f32_16x16x32_bf16 v[80:83], a[112:115], a[216:219], v[80:83]\n"
        "v_mfma_f32_16x16x32_bf16 v[68:71], a[112:115], a[248:251], v[68:71]\n"
        "v_mfma_f32_16x16x32_bf16 v[64:67], a[112:115], a[244:247], v[64:67]\n"
        "s_waitcnt vmcnt(11)\n"
        "ds_read_b64_tr_b16 a[92:93], v255 offset:15360\n"
        "ds_read_b64_tr_b16 a[94:95], v255 offset:16128\n"
        "v_mfma_f32_16x16x32_bf16 v[160:163], a[64:67], a[244:247], v[160:163]\n"
        "ds_read_b64_tr_b16 a[116:117], v255 offset:15392\n"
        "ds_read_b64_tr_b16 a[118:119], v255 offset:16160\n"
        "v_mfma_f32_16x16x32_bf16 v[180:183], a[64:67], a[248:251], v[180:183]\n"
        "ds_read_b64_tr_b16 a[140:141], v255 offset:15424\n"
        "ds_read_b64_tr_b16 a[142:143], v255 offset:16192\n"
        "v_mfma_f32_16x16x32_bf16 v[200:203], a[64:67], a[216:219], v[200:203]\n"
        "v_cvt_pk_bf16_f32 v104, v104, v105\n"
        "v_accvgpr_write_b32 a20, v104\n"
        "v_mfma_f32_16x16x32_bf16 v[220:223], a[64:67], a[220:223], v[220:223]\n"
        "v_cvt_pk_bf16_f32 v106, v106, v107\n"
        "v_accvgpr_write_b32 a21, v106\n"
        "v_mfma_f32_16x16x32_bf16 v[112:115], a[180:183], v[148:151], 0\n"
        "s_waitcnt lgkmcnt(6)\n"
        "v_lshl_add_u64 v[60:61], v[60:61], 2, s[38:39]\n"
        "v_lshl_add_u64 v[62:63], v[62:63], 2, s[38:39]\n"
        "v_mfma_f32_16x16x32_bf16 v[116:119], a[176:179], v[148:151], 0\n"
        "ds_read_b32 v60, v60\n"
        "ds_read_b32 v61, v61\n"
        "v_mfma_f32_16x16x32_bf16 v[72:75], a[136:139], a[244:247], v[72:75]\n"
        "ds_read_b32 v62, v62\n"
        "ds_read_b32 v63, v63\n"
        "v_mfma_f32_16x16x32_bf16 v[76:79], a[136:139], a[248:251], v[76:79]\n"
        "v_cvt_pk_bf16_f32 v108, v108, v109\n"
        "v_accvgpr_write_b32 a22, v108\n"
        "v_cvt_pk_bf16_f32 v110, v110, v111\n"
        "v_accvgpr_write_b32 a23, v110\n"
        "s_nop 1\n"
        "v_mfma_f32_16x16x32_bf16 v[156:159], a[20:23], a[224:227], v[156:159]\n"
        "v_cvt_pk_bf16_f32 v112, v112, v113\n"
        "v_accvgpr_write_b32 a44, v112\n"
        "v_mfma_f32_16x16x32_bf16 v[176:179], a[20:23], a[228:231], v[176:179]\n"
        "v_cvt_pk_bf16_f32 v114, v114, v115\n"
        "v_accvgpr_write_b32 a45, v114\n"
        "v_mfma_f32_16x16x32_bf16 v[196:199], a[20:23], a[232:235], v[196:199]\n"
        "v_cvt_pk_bf16_f32 v116, v116, v117\n"
        "v_accvgpr_write_b32 a46, v116\n"
        "v_cvt_pk_bf16_f32 v156, v156, v157\n"
        "v_mfma_f32_16x16x32_bf16 v[216:219], a[20:23], a[236:239], v[216:219]\n"
        "v_cvt_pk_bf16_f32 v157, v158, v159\n"
        "v_cvt_pk_bf16_f32 v176, v176, v177\n"
        "v_cvt_pk_bf16_f32 v177, v178, v179\n"
        "v_mfma_f32_16x16x32_bf16 v[120:123], a[172:175], v[148:151], 0\n"
        "v_cvt_pk_bf16_f32 v196, v196, v197\n"
        "v_cvt_pk_bf16_f32 v197, v198, v199\n"
        "v_mfma_f32_16x16x32_bf16 v[124:127], a[168:171], v[148:151], 0\n"
        "v_cvt_pk_bf16_f32 v118, v118, v119\n"
        "v_cvt_pk_bf16_f32 v216, v216, v217\n"
        "v_cvt_pk_bf16_f32 v217, v218, v219\n"
        "v_mfma_f32_16x16x32_bf16 v[84:87], a[136:139], a[216:219], v[84:87]\n"
        "v_accvgpr_write_b32 a47, v118\n"
        "v_cvt_pk_bf16_f32 v120, v120, v121\n"
        "v_mfma_f32_16x16x32_bf16 v[92:95], a[136:139], a[220:223], v[92:95]\n"
        "v_accvgpr_write_b32 a68, v120\n"
        "v_cvt_pk_bf16_f32 v122, v122, v123\n"
        "v_mfma_f32_16x16x32_bf16 v[224:227], a[44:47], a[236:239], v[224:227]\n"
        "v_accvgpr_write_b32 a69, v122\n"
        "v_cvt_pk_bf16_f32 v124, v124, v125\n"
        "v_mfma_f32_16x16x32_bf16 v[204:207], a[44:47], a[232:235], v[204:207]\n"
        "v_accvgpr_write_b32 a70, v124\n"
        "v_cvt_pk_bf16_f32 v126, v126, v127\n"
        "v_mfma_f32_16x16x32_bf16 v[184:187], a[44:47], a[228:231], v[184:187]\n"
        "ds_read_b128 a[240:243], v240 offset:0\n"
        "v_accvgpr_write_b32 a71, v126\n"
        "v_cvt_pk_bf16_f32 v218, v224, v225\n"
        "v_mfma_f32_16x16x32_bf16 v[164:167], a[44:47], a[224:227], v[164:167]\n"
        "s_waitcnt lgkmcnt(1)\n"
        "ds_read_b128 a[244:247], v241 offset:0\n"
        "v_cvt_pk_bf16_f32 v219, v226, v227\n"
        "v_cvt_pk_bf16_f32 v198, v204, v205\n"
        "v_mfma_f32_16x16x32_bf16 v[152:155], a[92:95], a[224:227], v[152:155]\n"
        "ds_read_b128 a[248:251], v240 offset:128\n"
        "v_cvt_pk_bf16_f32 v199, v206, v207\n"
        "v_cvt_pk_bf16_f32 v178, v184, v185\n"
        "v_mfma_f32_16x16x32_bf16 v[64:67], a[116:119], a[224:227], v[64:67]\n"
        "buffer_load_dwordx4 v130, s[20:23], s57 offen offset:2816 lds\n"
        "v_cvt_pk_bf16_f32 v179, v186, v187\n"
        "v_mfma_f32_16x16x32_bf16 v[72:75], a[140:143], a[224:227], v[72:75]\n"
        "s_add_u32 m0, s65, 8448\n"
        "v_cvt_pk_bf16_f32 v158, v164, v165\n"
        "v_cvt_pk_bf16_f32 v159, v166, v167\n"
        "v_mfma_f32_16x16x32_bf16 v[172:175], a[92:95], a[228:231], v[172:175]\n"
        "v_cvt_pk_bf16_f32 v152, v152, v153\n"
        "v_cvt_pk_bf16_f32 v153, v154, v155\n"
        "v_mfma_f32_16x16x32_bf16 v[160:163], a[68:71], a[224:227], v[160:163]\n"
        "v_cvt_pk_bf16_f32 v154, v64, v65\n"
        "v_cvt_pk_bf16_f32 v155, v66, v67\n"
        "v_mfma_f32_16x16x32_bf16 v[180:183], a[68:71], a[228:231], v[180:183]\n"
        "buffer_load_dwordx4 v128, s[20:23], s58 offen offset:768 lds\n"
        "v_mfma_f32_16x16x32_bf16 v[200:203], a[68:71], a[232:235], v[200:203]\n"
        "v_cvt_pk_bf16_f32 v168, v72, v73\n"
        "v_cvt_pk_bf16_f32 v169, v74, v75\n"
        "v_mfma_f32_16x16x32_bf16 v[220:223], a[68:71], a[236:239], v[220:223]\n"
        "v_cvt_pk_bf16_f32 v160, v160, v161\n"
        "v_cvt_pk_bf16_f32 v161, v162, v163\n"
        "v_mov_b64 v[166:167], v[160:161]\n"
        "v_mfma_f32_16x16x32_bf16 v[68:71], a[116:119], a[228:231], v[68:71]\n"
        "buffer_load_dwordx4 v129, s[20:23], s58 offen offset:1792 lds\n"
        "v_cvt_pk_bf16_f32 v180, v180, v181\n"
        "v_mfma_f32_16x16x32_bf16 v[76:79], a[140:143], a[228:231], v[76:79]\n"
        "s_waitcnt vmcnt(11)\n"
        "s_barrier\n"
        "ds_read_b128 v[162:165], v251 offset:0\n"
        "ds_read_b128 a[216:219], v240 offset:1536\n"
        "v_cvt_pk_bf16_f32 v181, v182, v183\n"
        "v_mfma_f32_16x16x32_bf16 v[192:195], a[92:95], a[232:235], v[192:195]\n"
        "ds_read_b128 v[182:185], v251 offset:512\n"
        "ds_read_b128 a[220:223], v241 offset:1536\n"
        "v_mov_b64 v[186:187], v[180:181]\n"
        "v_mfma_f32_16x16x32_bf16 v[80:83], a[116:119], a[232:235], v[80:83]\n"
        "buffer_load_dwordx4 v130, s[20:23], s58 offen offset:2816 lds\n"
        "v_cvt_pk_bf16_f32 v200, v200, v201\n"
        "v_mfma_f32_16x16x32_bf16 v[84:87], a[140:143], a[232:235], v[84:87]\n"
        "s_add_u32 m0, s65, 11520\n"
        "ds_read_b128 a[224:227], v240 offset:1664\n"
        "v_cvt_pk_bf16_f32 v201, v202, v203\n"
        "v_mov_b64 v[206:207], v[200:201]\n"
        "v_mfma_f32_16x16x32_bf16 v[212:215], a[92:95], a[236:239], v[212:215]\n"
        "ds_read_b128 v[202:205], v251 offset:4096\n"
        "v_cvt_pk_bf16_f32 v220, v220, v221\n"
        "v_cvt_pk_bf16_f32 v221, v222, v223\n"
        "v_mov_b64 v[226:227], v[220:221]\n"
        "v_cvt_pk_bf16_f32 v172, v172, v173\n"
        "v_cvt_pk_bf16_f32 v173, v174, v175\n"
        "v_mfma_f32_16x16x32_bf16 v[88:91], a[116:119], a[236:239], v[88:91]\n"
        "buffer_load_dwordx4 v128, s[20:23], s59 offen offset:768 lds\n"
        "v_mfma_f32_16x16x32_bf16 v[92:95], a[140:143], a[236:239], v[92:95]\n"
        "s_waitcnt vmcnt(10)\n"
        "ds_read_b64_tr_b16 a[144:145], v255 offset:0\n"
        "ds_read_b64_tr_b16 a[146:147], v255 offset:768\n"
        "v_cvt_pk_bf16_f32 v174, v68, v69\n"
        "v_cvt_pk_bf16_f32 v175, v70, v71\n"
        "v_mfma_f32_16x16x32_bf16 v[64:67], v[152:155], v[156:159], v[0:3]\n"
        "s_waitcnt lgkmcnt(4)\n"
        "ds_read_b64_tr_b16 a[168:169], v255 offset:32\n"
        "ds_read_b64_tr_b16 a[170:171], v255 offset:800\n"
        "v_cvt_pk_bf16_f32 v190, v76, v77\n"
        "v_cvt_pk_bf16_f32 v191, v78, v79\n"
        "v_mfma_f32_16x16x32_bf16 v[64:67], v[168:171], v[160:163], v[64:67]\n"
        "ds_read_b64_tr_b16 a[192:193], v255 offset:64\n"
        "ds_read_b64_tr_b16 a[194:195], v255 offset:832\n"
        "v_cvt_pk_bf16_f32 v192, v192, v193\n"
        "v_cvt_pk_bf16_f32 v193, v194, v195\n"
        "v_mfma_f32_16x16x32_bf16 v[96:99], v[172:175], v[156:159], v[4:7]\n"
        "buffer_load_dwordx4 v129, s[20:23], s59 offen offset:1792 lds\n"
        "v_cvt_pk_bf16_f32 v194, v80, v81\n"
        "v_cvt_pk_bf16_f32 v195, v82, v83\n"
        "v_exp_f32 v64, v64\n"
        "v_mfma_f32_16x16x32_bf16 v[96:99], v[188:191], v[164:167], v[96:99]\n"
        "ds_read_b128 v[162:165], v252 offset:0\n"
        "v_exp_f32 v65, v65\n"
        "v_exp_f32 v66, v66\n"
        "v_cvt_pk_bf16_f32 v64, v64, v65\n"
        "v_mfma_f32_16x16x32_bf16 v[72:75], v[152:155], v[176:179], v[16:19]\n"
        "buffer_load_dwordx4 v130, s[20:23], s59 offen offset:2816 lds\n"
        "v_cvt_pk_bf16_f32 v208, v84, v85\n"
        "v_cvt_pk_bf16_f32 v209, v86, v87\n"
        "v_exp_f32 v96, v96\n"
        "v_mfma_f32_16x16x32_bf16 v[72:75], v[168:171], v[180:183], v[72:75]\n"
        "s_add_u32 m0, s65, 14592\n"
        "s_waitcnt vmcnt(9)\n"
        "ds_read_b64_tr_b16 a[148:149], v255 offset:3072\n"
        "ds_read_b64_tr_b16 a[150:151], v255 offset:3840\n"
        "v_exp_f32 v67, v67\n"
        "v_exp_f32 v97, v97\n"
        "v_mfma_f32_16x16x32_bf16 v[100:103], v[172:175], v[176:179], v[20:23]\n"
        "buffer_load_dwordx4 v128, s[20:23], s60 offen offset:768 lds\n"
        "ds_read_b64_tr_b16 a[172:173], v255 offset:3104\n"
        "ds_read_b64_tr_b16 a[174:175], v255 offset:3872\n"
        "v_cvt_pk_bf16_f32 v212, v212, v213\n"
        "v_cvt_pk_bf16_f32 v213, v214, v215\n"
        "v_mfma_f32_16x16x32_bf16 v[100:103], v[188:191], v[184:187], v[100:103]\n"
        "ds_read_b128 v[182:185], v252 offset:512\n"
        "ds_read_b64_tr_b16 a[196:197], v255 offset:3136\n"
        "ds_read_b64_tr_b16 a[198:199], v255 offset:3904\n"
        "v_cvt_pk_bf16_f32 v65, v66, v67\n"
        "v_exp_f32 v98, v98\n"
        "v_mfma_f32_16x16x32_bf16 v[68:71], v[192:195], v[156:159], v[8:11]\n"
        "s_waitcnt lgkmcnt(7)\n"
        "ds_read_b128 a[228:231], v240 offset:12288\n"
        "ds_read_b128 a[232:235], v241 offset:12288\n"
        "v_cvt_pk_bf16_f32 v66, v96, v97\n"
        "v_exp_f32 v72, v72\n"
        "v_cvt_pk_bf16_f32 v214, v88, v89\n"
        "v_mfma_f32_16x16x32_bf16 v[68:71], v[208:211], v[160:163], v[68:71]\n"
        "buffer_load_dwordx4 v129, s[20:23], s60 offen offset:1792 lds\n"
        "v_cvt_pk_bf16_f32 v215, v90, v91\n"
        "v_exp_f32 v99, v99\n"
        "v_cvt_pk_bf16_f32 v230, v92, v93\n"
        "v_mfma_f32_16x16x32_bf16 v[76:79], v[192:195], v[176:179], v[24:27]\n"
        "s_waitcnt lgkmcnt(4)\n"
        "s_waitcnt vmcnt(8)\n"
        "ds_read_b64_tr_b16 a[152:153], v255 offset:6144\n"
        "ds_read_b64_tr_b16 a[154:155], v255 offset:6912\n"
        "v_exp_f32 v73, v73\n"
        "v_cvt_pk_bf16_f32 v231, v94, v95\n"
        "v_cvt_pk_bf16_f32 v67, v98, v99\n"
        "v_mfma_f32_16x16x32_bf16 v[76:79], v[208:211], v[180:183], v[76:79]\n"
        "buffer_load_dwordx4 v130, s[20:23], s60 offen offset:2816 lds\n"
        "ds_read_b64_tr_b16 a[176:177], v255 offset:6176\n"
        "ds_read_b64_tr_b16 a[178:179], v255 offset:6944\n"
        "v_exp_f32 v74, v74\n"
        "v_mfma_f32_16x16x32_bf16 v[104:107], v[212:215], v[156:159], v[12:15]\n"
        "s_add_u32 m0, s40, 98304\n"
        "ds_read_b64_tr_b16 a[200:201], v255 offset:6208\n"
        "ds_read_b64_tr_b16 a[202:203], v255 offset:6976\n"
        "v_cvt_pk_bf16_f32 v72, v72, v73\n"
        "v_mfma_f32_16x16x32_bf16 v[104:107], v[228:231], v[164:167], v[104:107]\n"
        "s_sub_u32 s64, s47, 6291456\n"
        "ds_read_b128 a[236:239], v240 offset:12416\n"
        "v_exp_f32 v100, v100\n"
        "v_mfma_f32_16x16x32_bf16 v[108:111], v[212:215], v[176:179], v[28:31]\n"
        "buffer_load_dwordx4 v242, s[8:11], s64 offen lds\n"
        "v_mfma_f32_16x16x32_bf16 v[108:111], v[228:231], v[184:187], v[108:111]\n"
        "s_add_u32 m0, s40, 99328\n"
        "v_exp_f32 v75, v75\n"
        "v_mfma_f32_16x16x32_bf16 v[80:83], v[152:155], v[196:199], v[32:35]\n"
        "v_exp_f32 v101, v101\n"
        "v_cvt_pk_bf16_f32 v73, v74, v75\n"
        "v_mfma_f32_16x16x32_bf16 v[80:83], v[168:171], v[200:203], v[80:83]\n"
        "buffer_load_dwordx4 v243, s[8:11], s64 offen lds\n"
        "v_cvt_pk_bf16_f32 v74, v100, v101\n"
        "v_mfma_f32_16x16x32_bf16 v[112:115], a[240:243], a[144:147], 0\n"
        "s_add_u32 m0, s40, 100352\n"
        "v_exp_f32 v102, v102\n"
        "v_mfma_f32_16x16x32_bf16 v[112:115], a[244:247], a[148:151], v[112:115]\n"
        "s_waitcnt lgkmcnt(0)\n"
        "v_exp_f32 v68, v68\n"
        "v_mfma_f32_16x16x32_bf16 v[112:115], a[248:251], a[152:155], v[112:115]\n"
        ".long 0xbec200ff\n"
        "42:\n"
        "s_branch 60b\n"
        "buffer_load_dwordx4 v244, s[8:11], s64 offen lds\n"
        "v_mfma_f32_16x16x32_bf16 v[116:119], a[248:251], a[176:179], 0\n"
        "s_add_u32 m0, s40, 110592\n"
        "v_exp_f32 v103, v103\n"
        "v_mfma_f32_16x16x32_bf16 v[116:119], a[244:247], a[172:175], v[116:119]\n"
        "s_sub_u32 s64, s47, 5505024\n"
        "v_exp_f32 v69, v69\n"
        "v_cvt_pk_bf16_f32 v75, v102, v103\n"
        "v_mfma_f32_16x16x32_bf16 v[116:119], a[240:243], a[168:171], v[116:119]\n"
        "v_exp_f32 v70, v70\n"
        "v_cvt_pk_bf16_f32 v68, v68, v69\n"
        "v_mfma_f32_16x16x32_bf16 v[96:99], v[172:175], v[196:199], v[36:39]\n"
        "buffer_load_dwordx4 v242, s[8:11], s64 offen lds\n"
        "s_waitcnt vmcnt(10)\n"
        "ds_read_b64_tr_b16 a[156:157], v255 offset:9216\n"
        "v_mfma_f32_16x16x32_bf16 v[96:99], v[188:191], v[204:207], v[96:99]\n"
        "s_add_u32 m0, s40, 111616\n"
        "ds_read_b64_tr_b16 a[158:159], v255 offset:9984\n"
        "ds_read_b64_tr_b16 a[180:181], v255 offset:9248\n"
        "ds_read_b64_tr_b16 a[182:183], v255 offset:10016\n"
        "v_mfma_f32_16x16x32_bf16 v[120:123], a[240:243], a[192:195], 0\n"
        "ds_read_b128 v[202:205], v252 offset:4096\n"
        "ds_read_b64_tr_b16 a[204:205], v255 offset:9280\n"
        "ds_read_b64_tr_b16 a[206:207], v255 offset:10048\n"
        "v_mfma_f32_16x16x32_bf16 v[120:123], a[244:247], a[196:199], v[120:123]\n"
        "buffer_load_dwordx4 v243, s[8:11], s64 offen lds\n"
        "v_mfma_f32_16x16x32_bf16 v[120:123], a[248:251], a[200:203], v[120:123]\n"
        "s_add_u32 m0, s40, 112640\n"
        "ds_read_b128 a[240:243], v240 offset:13824\n"
        "ds_read_b128 a[244:247], v241 offset:13824\n"
        "v_mfma_f32_16x16x32_bf16 v[124:127], a[216:219], a[144:147], 0\n"
        "buffer_load_dwordx4 v244, s[8:11], s64 offen lds\n"
        "v_mfma_f32_16x16x32_bf16 v[124:127], a[220:223], a[148:151], v[124:127]\n"
        "s_xor_b32 m0, s48, 8192\n"
        "s_waitcnt vmcnt(9)\n"
        "ds_read_b64_tr_b16 a[160:161], v255 offset:12288\n"
        "ds_read_b64_tr_b16 a[162:163], v255 offset:13056\n"
        "v_mfma_f32_16x16x32_bf16 v[124:127], a[224:227], a[152:155], v[124:127]\n"
        "s_sub_u32 s64, s36, 2097152\n"
        "ds_read_b64_tr_b16 a[184:185], v255 offset:12320\n"
        "ds_read_b64_tr_b16 a[186:187], v255 offset:13088\n"
        "v_mfma_f32_16x16x32_bf16 v[156:159], a[224:227], a[176:179], 0\n"
        "buffer_load_dwordx4 v249, s[12:15], s64 offen lds\n"
        "ds_read_b64_tr_b16 a[208:209], v255 offset:12352\n"
        "v_mfma_f32_16x16x32_bf16 v[156:159], a[220:223], a[172:175], v[156:159]\n"
        "ds_read_b64_tr_b16 a[210:211], v255 offset:13120\n"
        "ds_read_b128 a[248:251], v240 offset:13952\n"
        "v_mfma_f32_16x16x32_bf16 v[156:159], a[216:219], a[168:171], v[156:159]\n"
        "ds_read_b128 v[222:225], v251 offset:4608\n"
        "v_exp_f32 v76, v76\n"
        "v_mfma_f32_16x16x32_bf16 v[84:87], v[192:195], v[196:199], v[40:43]\n"
        "s_waitcnt lgkmcnt(8)\n"
        "v_exp_f32 v71, v71\n"
        "v_mfma_f32_16x16x32_bf16 v[84:87], v[208:211], v[200:203], v[84:87]\n"
        "buffer_load_dwordx4 v253, s[12:15], s64 offen offset:1024 lds\n"
        "v_cvt_pk_bf16_f32 v69, v70, v71\n"
        "v_mfma_f32_16x16x32_bf16 v[160:163], a[216:219], a[192:195], 0\n"
        "s_add_u32 m0, s65, 12288\n"
        "ds_read_b128 a[216:219], v241 offset:128\n"
        "v_exp_f32 v77, v77\n"
        "v_mfma_f32_16x16x32_bf16 v[160:163], a[220:223], a[196:199], v[160:163]\n"
        "ds_read_b128 a[220:223], v240 offset:256\n"
        "v_exp_f32 v78, v78\n"
        "v_mfma_f32_16x16x32_bf16 v[160:163], a[224:227], a[200:203], v[160:163]\n"
        "ds_read_b128 a[224:227], v241 offset:256\n"
        "v_cvt_pk_bf16_f32 v76, v76, v77\n"
        "v_mfma_f32_16x16x32_bf16 v[164:167], a[228:231], a[144:147], 0\n"
        "s_waitcnt lgkmcnt(3)\n"
        "buffer_load_dword v136, s[52:55], 0 offen lds\n"
        "v_mfma_f32_16x16x32_bf16 v[164:167], a[232:235], a[148:151], v[164:167]\n"
        "v_exp_f32 v104, v104\n"
        "v_mfma_f32_16x16x32_bf16 v[164:167], a[236:239], a[152:155], v[164:167]\n"
        "v_exp_f32 v79, v79\n"
        "v_mfma_f32_16x16x32_bf16 v[100:103], v[212:215], v[196:199], v[44:47]\n"
        "v_exp_f32 v105, v105\n"
        "v_cvt_pk_bf16_f32 v77, v78, v79\n"
        "v_mfma_f32_16x16x32_bf16 v[100:103], v[228:231], v[204:207], v[100:103]\n"
        "v_exp_f32 v106, v106\n"
        "v_cvt_pk_bf16_f32 v70, v104, v105\n"
        "v_mfma_f32_16x16x32_bf16 v[176:179], a[236:239], a[176:179], 0\n"
        "v_exp_f32 v108, v108\n"
        "v_mfma_f32_16x16x32_bf16 v[176:179], a[232:235], a[172:175], v[176:179]\n"
        "v_exp_f32 v107, v107\n"
        "v_mfma_f32_16x16x32_bf16 v[176:179], a[228:231], a[168:171], v[176:179]\n"
        "s_waitcnt vmcnt(9)\n"
        "ds_read_b64_tr_b16 a[164:165], v255 offset:15360\n"
        "ds_read_b64_tr_b16 a[166:167], v255 offset:16128\n"
        "v_cvt_pk_bf16_f32 v71, v106, v107\n"
        "v_mfma_f32_16x16x32_bf16 v[180:183], a[228:231], a[192:195], 0\n"
        "ds_read_b64_tr_b16 a[188:189], v255 offset:15392\n"
        "ds_read_b64_tr_b16 a[190:191], v255 offset:16160\n"
        "v_mfma_f32_16x16x32_bf16 v[180:183], a[232:235], a[196:199], v[180:183]\n"
        "ds_read_b64_tr_b16 a[212:213], v255 offset:15424\n"
        "ds_read_b64_tr_b16 a[214:215], v255 offset:16192\n"
        "v_mfma_f32_16x16x32_bf16 v[180:183], a[236:239], a[200:203], v[180:183]\n"
        "ds_read_b128 a[228:231], v241 offset:1664\n"
        "ds_read_b128 a[232:235], v240 offset:1792\n"
        "v_mfma_f32_16x16x32_bf16 v[184:187], a[240:243], a[144:147], 0\n"
        "ds_read_b128 a[236:239], v241 offset:1792\n"
        "v_exp_f32 v109, v109\n"
        "v_mfma_f32_16x16x32_bf16 v[184:187], a[244:247], a[148:151], v[184:187]\n"
        "v_exp_f32 v110, v110\n"
        "v_cvt_pk_bf16_f32 v78, v108, v109\n"
        "v_mfma_f32_16x16x32_bf16 v[184:187], a[248:251], a[152:155], v[184:187]\n"
        "v_exp_f32 v111, v111\n"
        "v_mfma_f32_16x16x32_bf16 v[196:199], a[248:251], a[176:179], 0\n"
        "v_cvt_pk_bf16_f32 v79, v110, v111\n"
        "v_exp_f32 v80, v80\n"
        "v_mfma_f32_16x16x32_bf16 v[196:199], a[244:247], a[172:175], v[196:199]\n"
        "v_exp_f32 v81, v81\n"
        "v_mfma_f32_16x16x32_bf16 v[196:199], a[240:243], a[168:171], v[196:199]\n"
        "v_exp_f32 v82, v82\n"
        "v_cvt_pk_bf16_f32 v80, v80, v81\n"
        "v_mfma_f32_16x16x32_bf16 v[200:203], a[240:243], a[192:195], 0\n"
        "ds_read_b128 a[240:243], v241 offset:12416\n"
        "v_exp_f32 v83, v83\n"
        "v_mfma_f32_16x16x32_bf16 v[200:203], a[244:247], a[196:199], v[200:203]\n"
        "ds_read_b128 a[244:247], v240 offset:12544\n"
        "v_cvt_pk_bf16_f32 v81, v82, v83\n"
        "v_mfma_f32_16x16x32_bf16 v[200:203], a[248:251], a[200:203], v[200:203]\n"
        "ds_read_b128 a[248:251], v241 offset:12544\n"
        "v_exp_f32 v96, v96\n"
        "v_mfma_f32_16x16x32_bf16 v[88:91], v[152:155], v[216:219], v[48:51]\n"
        "v_exp_f32 v97, v97\n"
        "v_mfma_f32_16x16x32_bf16 v[88:91], v[168:171], v[220:223], v[88:91]\n"
        "s_waitcnt lgkmcnt(3)\n"
        "v_exp_f32 v98, v98\n"
        "v_cvt_pk_bf16_f32 v82, v96, v97\n"
        "v_mfma_f32_16x16x32_bf16 v[112:115], a[216:219], a[156:159], v[112:115]\n"
        "v_exp_f32 v99, v99\n"
        "v_mfma_f32_16x16x32_bf16 v[112:115], a[220:223], a[160:163], v[112:115]\n"
        "v_cvt_pk_bf16_f32 v83, v98, v99\n"
        "v_exp_f32 v84, v84\n"
        "v_mfma_f32_16x16x32_bf16 v[112:115], a[224:227], a[164:167], v[112:115]\n"
        "v_exp_f32 v85, v85\n"
        "v_mfma_f32_16x16x32_bf16 v[116:119], a[224:227], a[188:191], v[116:119]\n"
        "v_exp_f32 v86, v86\n"
        "v_cvt_pk_bf16_f32 v84, v84, v85\n"
        "v_mfma_f32_16x16x32_bf16 v[116:119], a[220:223], a[184:187], v[116:119]\n"
        "v_exp_f32 v87, v87\n"
        "v_mfma_f32_16x16x32_bf16 v[116:119], a[216:219], a[180:183], v[116:119]\n"
        "v_cvt_pk_bf16_f32 v85, v86, v87\n"
        "v_exp_f32 v100, v100\n"
        "v_mfma_f32_16x16x32_bf16 v[120:123], a[216:219], a[204:207], v[120:123]\n"
        "ds_read_b128 a[216:219], v241 offset:13952\n"
        "v_exp_f32 v101, v101\n"
        "v_mfma_f32_16x16x32_bf16 v[120:123], a[220:223], a[208:211], v[120:123]\n"
        "ds_read_b128 a[220:223], v240 offset:14080\n"
        "v_exp_f32 v102, v102\n"
        "v_mfma_f32_16x16x32_bf16 v[120:123], a[224:227], a[212:215], v[120:123]\n"
        "ds_read_b128 a[224:227], v241 offset:14080\n"
        "v_cvt_pk_bf16_f32 v86, v100, v101\n"
        "v_mfma_f32_16x16x32_bf16 v[124:127], a[228:231], a[156:159], v[124:127]\n"
        "v_exp_f32 v103, v103\n"
        "v_cvt_pk_bf16_f32 v128, v112, v113\n"
        "v_mfma_f32_16x16x32_bf16 v[124:127], a[232:235], a[160:163], v[124:127]\n"
        "v_cvt_pk_bf16_f32 v87, v102, v103\n"
        "v_cvt_pk_bf16_f32 v129, v114, v115\n"
        "v_mfma_f32_16x16x32_bf16 v[124:127], a[236:239], a[164:167], v[124:127]\n"
        "v_cvt_pk_bf16_f32 v136, v116, v117\n"
        "v_cvt_pk_bf16_f32 v137, v118, v119\n"
        "v_mfma_f32_16x16x32_bf16 v[104:107], v[172:175], v[216:219], v[52:55]\n"
        "v_cvt_pk_bf16_f32 v144, v120, v121\n"
        "v_cvt_pk_bf16_f32 v145, v122, v123\n"
        "v_mfma_f32_16x16x32_bf16 v[104:107], v[188:191], v[224:227], v[104:107]\n"
        "ds_read_b128 v[222:225], v252 offset:4608\n"
        "s_nop 0\n"
        "v_mfma_f32_16x16x32_bf16 v[156:159], a[236:239], a[188:191], v[156:159]\n"
        "v_exp_f32 v88, v88\n"
        "v_mfma_f32_16x16x32_bf16 v[156:159], a[232:235], a[184:187], v[156:159]\n"
        "v_cvt_pk_bf16_f32 v130, v124, v125\n"
        "v_cvt_pk_bf16_f32 v131, v126, v127\n"
        "v_mfma_f32_16x16x32_bf16 v[156:159], a[228:231], a[180:183], v[156:159]\n"
        "v_exp_f32 v89, v89\n"
        "v_mfma_f32_16x16x32_bf16 v[160:163], a[228:231], a[204:207], v[160:163]\n"
        "v_exp_f32 v90, v90\n"
        "v_mfma_f32_16x16x32_bf16 v[160:163], a[232:235], a[208:211], v[160:163]\n"
        "v_exp_f32 v91, v91\n"
        "v_mfma_f32_16x16x32_bf16 v[160:163], a[236:239], a[212:215], v[160:163]\n"
        "s_waitcnt lgkmcnt(0)\n"
        "v_exp_f32 v104, v104\n"
        "v_mfma_f32_16x16x32_bf16 v[164:167], a[240:243], a[156:159], v[164:167]\n"
        "v_cvt_pk_bf16_f32 v138, v156, v157\n"
        "v_cvt_pk_bf16_f32 v139, v158, v159\n"
        "v_mfma_f32_16x16x32_bf16 v[164:167], a[244:247], a[160:163], v[164:167]\n"
        "v_cvt_pk_bf16_f32 v88, v88, v89\n"
        "v_cvt_pk_bf16_f32 v89, v90, v91\n"
        "v_mfma_f32_16x16x32_bf16 v[164:167], a[248:251], a[164:167], v[164:167]\n"
        "v_exp_f32 v105, v105\n"
        "v_mfma_f32_16x16x32_bf16 v[92:95], v[192:195], v[216:219], v[56:59]\n"
        "v_cvt_pk_bf16_f32 v146, v160, v161\n"
        "v_cvt_pk_bf16_f32 v147, v162, v163\n"
        "v_mfma_f32_16x16x32_bf16 v[92:95], v[208:211], v[220:223], v[92:95]\n"
        "v_exp_f32 v106, v106\n"
        "v_mfma_f32_16x16x32_bf16 v[176:179], a[248:251], a[188:191], v[176:179]\n"
        "v_exp_f32 v107, v107\n"
        "v_mfma_f32_16x16x32_bf16 v[176:179], a[244:247], a[184:187], v[176:179]\n"
        "v_cvt_pk_bf16_f32 v132, v164, v165\n"
        "v_cvt_pk_bf16_f32 v133, v166, v167\n"
        "v_mfma_f32_16x16x32_bf16 v[176:179], a[240:243], a[180:183], v[176:179]\n"
        "v_cvt_pk_bf16_f32 v90, v104, v105\n"
        "v_cvt_pk_bf16_f32 v91, v106, v107\n"
        "v_mfma_f32_16x16x32_bf16 v[180:183], a[240:243], a[204:207], v[180:183]\n"
        "v_exp_f32 v92, v92\n"
        "v_mfma_f32_16x16x32_bf16 v[180:183], a[244:247], a[208:211], v[180:183]\n"
        "v_exp_f32 v93, v93\n"
        "v_mfma_f32_16x16x32_bf16 v[180:183], a[248:251], a[212:215], v[180:183]\n"
        "v_mov_b64 v[96:97], 0\n"
        "v_cvt_pk_bf16_f32 v140, v176, v177\n"
        "v_mfma_f32_16x16x32_bf16 v[184:187], a[216:219], a[156:159], v[184:187]\n"
        "v_cvt_pk_bf16_f32 v141, v178, v179\n"
        "v_cvt_pk_bf16_f32 v92, v92, v93\n"
        "v_mfma_f32_16x16x32_bf16 v[184:187], a[220:223], a[160:163], v[184:187]\n"
        "v_exp_f32 v94, v94\n"
        "v_mfma_f32_16x16x32_bf16 v[184:187], a[224:227], a[164:167], v[184:187]\n"
        "v_exp_f32 v95, v95\n"
        "v_mfma_f32_16x16x32_bf16 v[108:111], v[212:215], v[216:219], v[60:63]\n"
        "v_cvt_pk_bf16_f32 v148, v180, v181\n"
        "v_cvt_pk_bf16_f32 v149, v182, v183\n"
        "v_mfma_f32_16x16x32_bf16 v[108:111], v[228:231], v[224:227], v[108:111]\n"
        "v_add_u32 v240, 24576, v240\n"
        "v_add_u32 v241, 24576, v241\n"
        "v_mfma_f32_16x16x32_bf16 v[196:199], a[224:227], a[188:191], v[196:199]\n"
        "v_cvt_pk_bf16_f32 v134, v184, v185\n"
        "v_cvt_pk_bf16_f32 v135, v186, v187\n"
        "v_mfma_f32_16x16x32_bf16 v[196:199], a[220:223], a[184:187], v[196:199]\n"
        "v_mov_b64 v[172:173], 0\n"
        "v_cvt_pk_bf16_f32 v93, v94, v95\n"
        "v_mfma_f32_16x16x32_bf16 v[196:199], a[216:219], a[180:183], v[196:199]\n"
        "v_exp_f32 v108, v108\n"
        "v_mfma_f32_16x16x32_bf16 v[200:203], a[216:219], a[204:207], v[200:203]\n"
        "v_exp_f32 v109, v109\n"
        "v_mfma_f32_16x16x32_bf16 v[200:203], a[220:223], a[208:211], v[200:203]\n"
        "v_exp_f32 v110, v110\n"
        "v_mfma_f32_16x16x32_bf16 v[200:203], a[224:227], a[212:215], v[200:203]\n"
        "v_exp_f32 v111, v111\n"
        "v_smfmac_f32_16x16x64_bf16 v[96:99], a[252:255], v[64:71], v237\n"
        "v_cvt_pk_bf16_f32 v142, v196, v197\n"
        "v_mfma_f32_16x16x32_bf16 v[100:103], v[132:135], v[68:71], 0\n"
        "v_cvt_pk_bf16_f32 v143, v198, v199\n"
        "v_cvt_pk_bf16_f32 v94, v108, v109\n"
        "v_mfma_f32_16x16x32_bf16 v[100:103], v[128:131], v[64:67], v[100:103]\n"
        "v_mov_b64 v[192:193], 0\n"
        "v_cvt_pk_bf16_f32 v150, v200, v201\n"
        "v_mfma_f32_16x16x32_bf16 v[104:107], v[136:139], v[64:67], 0\n"
        "v_cvt_pk_bf16_f32 v151, v202, v203\n"
        "v_add_f32 v97, v96, v97\n"
        "v_mfma_f32_16x16x32_bf16 v[104:107], v[140:143], v[68:71], v[104:107]\n"
        "v_rcp_f32 v96, v97\n"
        "v_cvt_pk_bf16_f32 v95, v110, v111\n"
        "v_mfma_f32_16x16x32_bf16 v[108:111], v[148:151], v[68:71], 0\n"
        "v_mul_f32 v100, v96, v100\n"
        "v_mul_f32 v101, v96, v101\n"
        "v_mfma_f32_16x16x32_bf16 v[108:111], v[144:147], v[64:67], v[108:111]\n"
        "v_mul_f32 v102, v96, v102\n"
        "v_mul_f32 v103, v96, v103\n"
        "v_smfmac_f32_16x16x64_bf16 v[172:175], a[252:255], v[72:79], v237\n"
        "s_waitcnt vmcnt(6)\n"
        "s_barrier\n"
        "ds_read_b128 a[236:239], v240 offset:0\n"
        "v_mfma_f32_16x16x32_bf16 v[176:179], v[132:135], v[76:79], 0\n"
        "ds_read_b128 a[240:243], v241 offset:0\n"
        "ds_read_b128 a[244:247], v240 offset:128\n"
        "v_mfma_f32_16x16x32_bf16 v[176:179], v[128:131], v[72:75], v[176:179]\n"
        "ds_read_b128 a[248:251], v241 offset:128\n"
        "ds_read_b128 a[216:219], v240 offset:256\n"
        "v_mfma_f32_16x16x32_bf16 v[180:183], v[136:139], v[72:75], 0\n"
        "ds_read_b128 a[220:223], v241 offset:256\n"
        "v_cvt_pk_bf16_f32 v100, v100, v101\n"
        "v_mfma_f32_16x16x32_bf16 v[180:183], v[140:143], v[76:79], v[180:183]\n"
        "v_cvt_pk_bf16_f32 v101, v102, v103\n"
        "v_mul_f32 v104, v96, v104\n"
        "v_mfma_f32_16x16x32_bf16 v[184:187], v[148:151], v[76:79], 0\n"
        "ds_write2st64_b32 v245, v100, v101 offset0:0 offset1:4\n"
        "v_mfma_f32_16x16x32_bf16 v[184:187], v[144:147], v[72:75], v[184:187]\n"
        "v_mul_f32 v105, v96, v105\n"
        "v_mul_f32 v106, v96, v106\n"
        "v_smfmac_f32_16x16x64_bf16 v[192:195], a[252:255], v[80:87], v237\n"
        "v_mul_f32 v107, v96, v107\n"
        "v_mfma_f32_16x16x32_bf16 v[196:199], v[132:135], v[84:87], 0\n"
        "v_cvt_pk_bf16_f32 v102, v104, v105\n"
        "v_cvt_pk_bf16_f32 v103, v106, v107\n"
        "v_mfma_f32_16x16x32_bf16 v[196:199], v[128:131], v[80:83], v[196:199]\n"
        "buffer_load_dwordx4 v232, s[52:55], s34 offen lds\n"
        "v_mfma_f32_16x16x32_bf16 v[200:203], v[136:139], v[80:83], 0\n"
        "ds_write2st64_b32 v245, v102, v103 offset0:8 offset1:12\n"
        "v_mfma_f32_16x16x32_bf16 v[200:203], v[140:143], v[84:87], v[200:203]\n"
        "v_mov_b64 v[124:125], 0\n"
        "v_mul_f32 v108, v96, v108\n"
        "v_mul_f32 v109, v96, v109\n"
        "v_mfma_f32_16x16x32_bf16 v[204:207], v[148:151], v[84:87], 0\n"
        "v_mul_f32 v110, v96, v110\n"
        "v_mul_f32 v111, v96, v111\n"
        "v_mfma_f32_16x16x32_bf16 v[204:207], v[144:147], v[80:83], v[204:207]\n"
        "s_waitcnt lgkmcnt(2)\n"
        "v_cvt_pk_bf16_f32 v108, v108, v109\n"
        "v_cvt_pk_bf16_f32 v109, v110, v111\n"
        "v_mfma_f32_16x16x32_bf16 v[112:115], a[72:75], a[236:239], 0\n"
        "v_max_u32 v236, v96, v97\n"
        "v_add_f32 v173, v172, v173\n"
        "v_mfma_f32_16x16x32_bf16 v[112:115], a[76:79], a[240:243], v[112:115]\n"
        "ds_write2st64_b32 v245, v108, v109 offset0:16 offset1:20\n"
        "v_mfma_f32_16x16x32_bf16 v[112:115], a[80:83], a[244:247], v[112:115]\n"
        "v_rcp_f32 v172, v173\n"
        "v_mfma_f32_16x16x32_bf16 v[112:115], a[84:87], a[248:251], v[112:115]\n"
        "buffer_load_dwordx4 v233, s[52:55], s34 offen offset:1024 lds\n"
        "v_mfma_f32_16x16x32_bf16 v[112:115], a[88:91], a[216:219], v[112:115]\n"
        "v_mul_f32 v176, v172, v176\n"
        "v_mul_f32 v177, v172, v177\n"
        "v_mfma_f32_16x16x32_bf16 v[112:115], a[92:95], a[220:223], v[112:115]\n"
        "v_mul_f32 v178, v172, v178\n"
        "v_mul_f32 v179, v172, v179\n"
        "v_smfmac_f32_16x16x64_bf16 v[124:127], a[252:255], v[88:95], v237\n"
        "v_cvt_pk_bf16_f32 v176, v176, v177\n"
        "v_mfma_f32_16x16x32_bf16 v[80:83], v[132:135], v[92:95], 0\n"
        "v_cvt_pk_bf16_f32 v177, v178, v179\n"
        "v_mul_f32 v180, v172, v180\n"
        "v_mfma_f32_16x16x32_bf16 v[80:83], v[128:131], v[88:91], v[80:83]\n"
        "ds_write2st64_b32 v245, v176, v177 offset0:1 offset1:5\n"
        "v_mfma_f32_16x16x32_bf16 v[84:87], v[136:139], v[88:91], 0\n"
        "v_cvt_pk_bf16_f32 v152, v112, v113\n"
        "v_cvt_pk_bf16_f32 v153, v114, v115\n"
        "v_mfma_f32_16x16x32_bf16 v[84:87], v[140:143], v[92:95], v[84:87]\n"
        "v_mul_f32 v181, v172, v181\n"
        "v_mul_f32 v182, v172, v182\n"
        "v_mfma_f32_16x16x32_bf16 v[96:99], v[148:151], v[92:95], 0\n"
        "buffer_load_dwordx4 v234, s[52:55], s34 offen offset:2048 lds\n"
        "v_mfma_f32_16x16x32_bf16 v[96:99], v[144:147], v[88:91], v[96:99]\n"
        "v_mul_f32 v183, v172, v183\n"
        "v_cvt_pk_bf16_f32 v178, v180, v181\n"
        "v_mfma_f32_16x16x32_bf16 v[116:119], a[116:119], a[220:223], 0\n"
        "v_cvt_pk_bf16_f32 v179, v182, v183\n"
        "v_mul_f32 v184, v172, v184\n"
        "v_mfma_f32_16x16x32_bf16 v[116:119], a[112:115], a[216:219], v[116:119]\n"
        "ds_write2st64_b32 v245, v178, v179 offset0:9 offset1:13\n"
        "v_mfma_f32_16x16x32_bf16 v[116:119], a[108:111], a[248:251], v[116:119]\n"
        "v_mul_f32 v185, v172, v185\n"
        "v_mul_f32 v186, v172, v186\n"
        "v_mfma_f32_16x16x32_bf16 v[116:119], a[104:107], a[244:247], v[116:119]\n"
        "s_add_u32 m0, s65, 15360\n"
        "v_mul_f32 v187, v172, v187\n"
        "v_add_f32 v125, v124, v125\n"
        "v_mfma_f32_16x16x32_bf16 v[116:119], a[100:103], a[240:243], v[116:119]\n"
        "v_rcp_f32 v124, v125\n"
        "v_mfma_f32_16x16x32_bf16 v[116:119], a[96:99], a[236:239], v[116:119]\n"
        "v_cvt_pk_bf16_f32 v184, v184, v185\n"
        "v_cvt_pk_bf16_f32 v185, v186, v187\n"
        "v_mfma_f32_16x16x32_bf16 v[120:123], a[120:123], a[236:239], 0\n"
        "buffer_load_dwordx4 v232, s[52:55], s62 offen lds\n"
        "v_mfma_f32_16x16x32_bf16 v[120:123], a[124:127], a[240:243], v[120:123]\n"
        "ds_write2st64_b32 v245, v184, v185 offset0:17 offset1:21\n"
        "v_mfma_f32_16x16x32_bf16 v[120:123], a[128:131], a[244:247], v[120:123]\n"
        "v_mul_f32 v80, v124, v80\n"
        "v_mul_f32 v81, v124, v81\n"
        "v_mfma_f32_16x16x32_bf16 v[120:123], a[132:135], a[248:251], v[120:123]\n"
        "v_mul_f32 v82, v124, v82\n"
        "v_mul_f32 v83, v124, v83\n"
        "v_mfma_f32_16x16x32_bf16 v[120:123], a[136:139], a[216:219], v[120:123]\n"
        "v_max3_u32 v236, v236, v172, v173\n"
        "v_cvt_pk_bf16_f32 v80, v80, v81\n"
        "v_mfma_f32_16x16x32_bf16 v[120:123], a[140:143], a[220:223], v[120:123]\n"
        "v_cvt_pk_bf16_f32 v81, v82, v83\n"
        "v_mul_f32 v84, v124, v84\n"
        "v_mfma_f32_16x16x32_bf16 v[112:115], a[20:23], a[220:223], 0\n"
        "v_mul_f32 v85, v124, v85\n"
        "v_mul_f32 v86, v124, v86\n"
        "v_mfma_f32_16x16x32_bf16 v[112:115], a[16:19], a[216:219], v[112:115]\n"
        "v_mul_f32 v87, v124, v87\n"
        "v_cvt_pk_bf16_f32 v82, v84, v85\n"
        "v_mfma_f32_16x16x32_bf16 v[112:115], a[12:15], a[248:251], v[112:115]\n"
        "buffer_load_dwordx4 v233, s[52:55], s62 offen offset:1024 lds\n"
        "v_mfma_f32_16x16x32_bf16 v[112:115], a[8:11], a[244:247], v[112:115]\n"
        "v_cvt_pk_bf16_f32 v83, v86, v87\n"
        "v_mul_f32 v96, v124, v96\n"
        "v_mfma_f32_16x16x32_bf16 v[112:115], a[4:7], a[240:243], v[112:115]\n"
        "v_mul_f32 v97, v124, v97\n"
        "v_mul_f32 v98, v124, v98\n"
        "v_mfma_f32_16x16x32_bf16 v[112:115], a[0:3], a[236:239], v[112:115]\n"
        "v_mul_f32 v99, v124, v99\n"
        "v_cvt_pk_bf16_f32 v154, v116, v117\n"
        "v_cvt_pk_bf16_f32 v155, v118, v119\n"
        "v_mfma_f32_16x16x32_bf16 v[116:119], a[24:27], a[236:239], 0\n"
        "v_cvt_pk_bf16_f32 v96, v96, v97\n"
        "v_cvt_pk_bf16_f32 v97, v98, v99\n"
        "v_mfma_f32_16x16x32_bf16 v[116:119], a[28:31], a[240:243], v[116:119]\n"
        "ds_write_b32 v245, v96 offset:4864\n"
        "v_mfma_f32_16x16x32_bf16 v[116:119], a[32:35], a[244:247], v[116:119]\n"
        "v_cvt_pk_bf16_f32 v168, v120, v121\n"
        "v_cvt_pk_bf16_f32 v169, v122, v123\n"
        "v_mfma_f32_16x16x32_bf16 v[116:119], a[36:39], a[248:251], v[116:119]\n"
        "v_add_f32 v193, v192, v193\n"
        "v_cvt_pk_bf16_f32 v156, v112, v113\n"
        "v_mfma_f32_16x16x32_bf16 v[116:119], a[40:43], a[216:219], v[116:119]\n"
        "buffer_load_dwordx4 v234, s[52:55], s62 offen offset:2048 lds\n"
        "v_mfma_f32_16x16x32_bf16 v[116:119], a[44:47], a[220:223], v[116:119]\n"
        "v_rcp_f32 v192, v193\n"
        "v_mfma_f32_16x16x32_bf16 v[120:123], a[68:71], a[220:223], 0\n"
        "v_mul_f32 v196, v192, v196\n"
        "v_mul_f32 v197, v192, v197\n"
        "v_mfma_f32_16x16x32_bf16 v[120:123], a[64:67], a[216:219], v[120:123]\n"
        "v_mul_f32 v198, v192, v198\n"
        "v_mul_f32 v199, v192, v199\n"
        "v_mfma_f32_16x16x32_bf16 v[120:123], a[60:63], a[248:251], v[120:123]\n"
        "ds_read_b128 a[224:227], v240 offset:1536\n"
        "v_cvt_pk_bf16_f32 v157, v114, v115\n"
        "v_mfma_f32_16x16x32_bf16 v[120:123], a[56:59], a[244:247], v[120:123]\n"
        "s_add_u32 m0, s65, 0\n"
        "ds_read_b128 a[228:231], v241 offset:1536\n"
        "v_cvt_pk_bf16_f32 v196, v196, v197\n"
        "v_mfma_f32_16x16x32_bf16 v[120:123], a[52:55], a[240:243], v[120:123]\n"
        "ds_read_b128 a[232:235], v240 offset:1664\n"
        "v_cvt_pk_bf16_f32 v197, v198, v199\n"
        "v_mfma_f32_16x16x32_bf16 v[120:123], a[48:51], a[236:239], v[120:123]\n"
        "ds_write2st64_b32 v245, v196, v197 offset0:2 offset1:6\n"
        "v_mfma_f32_16x16x32_bf16 v[112:115], a[236:239], a[144:147], 0\n"
        "buffer_load_dwordx4 v232, s[52:55], 0 offen lds\n"
        "v_mfma_f32_16x16x32_bf16 v[112:115], a[240:243], a[148:151], v[112:115]\n"
        "v_mul_f32 v200, v192, v200\n"
        "v_mul_f32 v201, v192, v201\n"
        "v_mfma_f32_16x16x32_bf16 v[112:115], a[244:247], a[152:155], v[112:115]\n"
        "v_mul_f32 v202, v192, v202\n"
        "v_mul_f32 v203, v192, v203\n"
        "v_mfma_f32_16x16x32_bf16 v[112:115], a[248:251], a[156:159], v[112:115]\n"
        "v_cvt_pk_bf16_f32 v198, v200, v201\n"
        "v_cvt_pk_bf16_f32 v199, v202, v203\n"
        "v_mfma_f32_16x16x32_bf16 v[112:115], a[216:219], a[160:163], v[112:115]\n"
        "ds_write2st64_b32 v245, v198, v199 offset0:10 offset1:14\n"
        "v_mfma_f32_16x16x32_bf16 v[112:115], a[220:223], a[164:167], v[112:115]\n"
        "v_mul_f32 v204, v192, v204\n"
        "v_mul_f32 v205, v192, v205\n"
        "v_cvt_pk_bf16_f32 v158, v116, v117\n"
        "v_cvt_pk_bf16_f32 v159, v118, v119\n"
        "v_mfma_f32_16x16x32_bf16 v[116:119], a[220:223], a[188:191], 0\n"
        "s_waitcnt vmcnt(4)\n"
        "ds_read_b64_tr_b16 v[64:65], v255 offset:12288\n"
        "ds_read_b64_tr_b16 v[66:67], v255 offset:13056\n"
        "v_mfma_f32_16x16x32_bf16 v[116:119], a[216:219], a[184:187], v[116:119]\n"
        "ds_write_b32 v245, v97 offset:5888\n"
        "v_mfma_f32_16x16x32_bf16 v[116:119], a[248:251], a[180:183], v[116:119]\n"
        "buffer_load_dwordx4 v233, s[52:55], 0 offen offset:1024 lds\n"
        "v_mfma_f32_16x16x32_bf16 v[116:119], a[244:247], a[176:179], v[116:119]\n"
        "ds_read_b64_tr_b16 v[68:69], v255 offset:12320\n"
        "ds_read_b64_tr_b16 v[70:71], v255 offset:13088\n"
        "v_mfma_f32_16x16x32_bf16 v[116:119], a[240:243], a[172:175], v[116:119]\n"
        "ds_read_b64_tr_b16 v[72:73], v255 offset:12352\n"
        "ds_read_b64_tr_b16 v[74:75], v255 offset:13120\n"
        "v_mfma_f32_16x16x32_bf16 v[116:119], a[236:239], a[168:171], v[116:119]\n"
        "v_mul_f32 v206, v192, v206\n"
        "v_mul_f32 v207, v192, v207\n"
        "v_cvt_pk_bf16_f32 v160, v120, v121\n"
        "v_cvt_pk_bf16_f32 v161, v122, v123\n"
        "v_mfma_f32_16x16x32_bf16 v[120:123], a[236:239], a[192:195], 0\n"
        "ds_read_b128 a[236:239], v241 offset:1664\n"
        "v_cvt_pk_bf16_f32 v204, v204, v205\n"
        "v_mfma_f32_16x16x32_bf16 v[120:123], a[240:243], a[196:199], v[120:123]\n"
        "ds_read_b128 a[240:243], v240 offset:1792\n"
        "v_cvt_pk_bf16_f32 v205, v206, v207\n"
        "v_mfma_f32_16x16x32_bf16 v[120:123], a[244:247], a[200:203], v[120:123]\n"
        "ds_write2st64_b32 v245, v204, v205 offset0:18 offset1:22\n"
        "v_mfma_f32_16x16x32_bf16 v[120:123], a[248:251], a[204:207], v[120:123]\n"
        "ds_read_b128 a[244:247], v241 offset:1792\n"
        "v_max3_u32 v236, v236, v192, v193\n"
        "v_mfma_f32_16x16x32_bf16 v[120:123], a[216:219], a[208:211], v[120:123]\n"
        "buffer_load_dwordx4 v234, s[52:55], 0 offen offset:2048 lds\n"
        "v_mfma_f32_16x16x32_bf16 v[120:123], a[220:223], a[212:215], v[120:123]\n"
        "s_waitcnt lgkmcnt(4)\n"
        "ds_write_b64 v235, v[64:65] offset:12288\n"
        "v_cvt_pk_bf16_f32 v128, v112, v113\n"
        "v_cvt_pk_bf16_f32 v129, v114, v115\n"
        "v_mfma_f32_16x16x32_bf16 v[112:115], a[72:75], a[224:227], 0\n"
        "s_barrier\n"
        "s_waitcnt vmcnt(3)\n"
        "ds_read_b64_tr_b16 v[76:77], v255 offset:15360\n"
        "ds_read_b64_tr_b16 v[78:79], v255 offset:16128\n"
        "v_mfma_f32_16x16x32_bf16 v[112:115], a[76:79], a[228:231], v[112:115]\n"
        "v_max3_u32 v236, v236, v124, v125\n"
        "v_cmp_lt_u32 vcc, s24, v236\n"
        "v_mfma_f32_16x16x32_bf16 v[112:115], a[80:83], a[232:235], v[112:115]\n"
        "s_waitcnt lgkmcnt(5)\n"
        "ds_write_b64 v235, v[66:67] offset:12296\n"
        "s_cmp_lg_u64 vcc, 0\n"
        "s_addc_u32 s25, s25, s25\n"
        "v_mfma_f32_16x16x32_bf16 v[112:115], a[84:87], a[236:239], v[112:115]\n"
        "s_add_u32 m0, s65, 3072\n"
        "v_mov_b64 v[166:167], v[160:161]\n"
        "v_and_b32 v239, 32, v245\n"
        "v_mfma_f32_16x16x32_bf16 v[112:115], a[88:91], a[240:243], v[112:115]\n"
        "s_waitcnt lgkmcnt(4)\n"
        "v_cvt_pk_bf16_f32 v136, v116, v117\n"
        "v_cvt_pk_bf16_f32 v137, v118, v119\n"
        "v_mfma_f32_16x16x32_bf16 v[112:115], a[92:95], a[244:247], v[112:115]\n"
        "v_xor_b32 v251, 0x2000, v251\n"
        "v_xor_b32 v252, 0x2000, v252\n"
        "v_mfma_f32_16x16x32_bf16 v[116:119], a[116:119], a[244:247], 0\n"
        "buffer_load_dwordx4 v232, s[52:55], s63 offen lds\n"
        "v_mfma_f32_16x16x32_bf16 v[116:119], a[112:115], a[240:243], v[116:119]\n"
        "ds_read_b64_tr_b16 v[88:89], v255 offset:15392\n"
        "ds_read_b64_tr_b16 v[90:91], v255 offset:16160\n"
        "v_mfma_f32_16x16x32_bf16 v[116:119], a[108:111], a[236:239], v[116:119]\n"
        "v_mad_i32_i24 v239, v239, -12, v235\n"
        "v_cvt_pk_bf16_f32 v144, v120, v121\n"
        "v_mfma_f32_16x16x32_bf16 v[116:119], a[104:107], a[232:235], v[116:119]\n"
        "ds_write_b64 v239, v[68:69] offset:12480\n"
        "v_mfma_f32_16x16x32_bf16 v[116:119], a[100:103], a[228:231], v[116:119]\n"
        "v_cvt_pk_bf16_f32 v145, v122, v123\n"
        "v_cvt_pk_bf16_f32 v172, v112, v113\n"
        "v_mfma_f32_16x16x32_bf16 v[116:119], a[96:99], a[224:227], v[116:119]\n"
        "ds_write_b64 v239, v[70:71] offset:12488\n"
        "v_mfma_f32_16x16x32_bf16 v[120:123], a[120:123], a[224:227], 0\n"
        "ds_read_b128 v[162:165], v251 offset:0\n"
        "v_cvt_pk_bf16_f32 v173, v114, v115\n"
        "v_mfma_f32_16x16x32_bf16 v[120:123], a[124:127], a[228:231], v[120:123]\n"
        "v_add_u32 v247, s45, v247\n"
        "s_nop 0\n"
        "v_mfma_f32_16x16x32_bf16 v[120:123], a[128:131], a[232:235], v[120:123]\n"
        "ds_write_b64 v254, v[72:73] offset:12288\n"
        "v_mfma_f32_16x16x32_bf16 v[120:123], a[132:135], a[236:239], v[120:123]\n"
        "buffer_load_dwordx4 v233, s[52:55], s63 offen offset:1024 lds\n"
        "v_mfma_f32_16x16x32_bf16 v[120:123], a[136:139], a[240:243], v[120:123]\n"
        "ds_read_b64_tr_b16 v[92:93], v255 offset:15424\n"
        "ds_read_b64_tr_b16 v[94:95], v255 offset:16192\n"
        "v_mfma_f32_16x16x32_bf16 v[120:123], a[140:143], a[244:247], v[120:123]\n"
        "ds_write_b64 v254, v[74:75] offset:12296\n"
        "v_mfma_f32_16x16x32_bf16 v[112:115], a[20:23], a[244:247], 0\n"
        "v_cvt_pk_bf16_f32 v174, v116, v117\n"
        "v_cvt_pk_bf16_f32 v175, v118, v119\n"
        "v_mfma_f32_16x16x32_bf16 v[112:115], a[16:19], a[240:243], v[112:115]\n"
        "s_nop 2\n"
        "v_mfma_f32_16x16x32_bf16 v[112:115], a[12:15], a[236:239], v[112:115]\n"
        "v_cvt_pk_bf16_f32 v190, v120, v121\n"
        "v_cvt_pk_bf16_f32 v191, v122, v123\n"
        "v_mfma_f32_16x16x32_bf16 v[112:115], a[8:11], a[232:235], v[112:115]\n"
        "s_waitcnt vmcnt(2)\n"
        "ds_read_b64_tr_b16 v[212:213], v255 offset:0\n"
        "ds_read_b64_tr_b16 v[214:215], v255 offset:768\n"
        "v_mfma_f32_16x16x32_bf16 v[112:115], a[4:7], a[228:231], v[112:115]\n"
        "buffer_load_dwordx4 v234, s[52:55], s63 offen offset:2048 lds\n"
        "v_mfma_f32_16x16x32_bf16 v[112:115], a[0:3], a[224:227], v[112:115]\n"
        "ds_read_b128 v[182:185], v251 offset:512\n"
        "s_nop 0\n"
        "v_mfma_f32_16x16x32_bf16 v[116:119], a[24:27], a[224:227], 0\n"
        "s_nop 2\n"
        "v_mfma_f32_16x16x32_bf16 v[116:119], a[28:31], a[228:231], v[116:119]\n"
        "s_waitcnt lgkmcnt(4)\n"
        "ds_write_b128 v235, v[76:79] offset:15360\n"
        "v_mfma_f32_16x16x32_bf16 v[116:119], a[32:35], a[232:235], v[116:119]\n"
        "v_cvt_pk_bf16_f32 v176, v112, v113\n"
        "v_cvt_pk_bf16_f32 v177, v114, v115\n"
        "v_mfma_f32_16x16x32_bf16 v[116:119], a[36:39], a[236:239], v[116:119]\n"
        "s_add_u32 m0, s65, 6144\n"
        "v_mfma_f32_16x16x32_bf16 v[116:119], a[40:43], a[240:243], v[116:119]\n"
        "ds_write_b32 v245, v80 offset:768\n"
        "v_mfma_f32_16x16x32_bf16 v[116:119], a[44:47], a[244:247], v[116:119]\n"
        "ds_read_b64_tr_b16 v[216:217], v255 offset:32\n"
        "ds_read_b64_tr_b16 v[218:219], v255 offset:800\n"
        "v_mfma_f32_16x16x32_bf16 v[120:123], a[68:71], a[244:247], 0\n"
        "buffer_load_dwordx4 v232, s[52:55], s61 offen lds\n"
        "v_mfma_f32_16x16x32_bf16 v[120:123], a[64:67], a[240:243], v[120:123]\n"
        "s_nop 2\n"
        "v_mfma_f32_16x16x32_bf16 v[120:123], a[60:63], a[236:239], v[120:123]\n"
        "ds_write_b128 v239, v[88:91] offset:15552\n"
        "v_mfma_f32_16x16x32_bf16 v[120:123], a[56:59], a[232:235], v[120:123]\n"
        "v_cvt_pk_bf16_f32 v178, v116, v117\n"
        "v_cvt_pk_bf16_f32 v179, v118, v119\n"
        "v_mfma_f32_16x16x32_bf16 v[120:123], a[52:55], a[228:231], v[120:123]\n"
        "v_mfma_f32_16x16x32_bf16 v[120:123], a[48:51], a[224:227], v[120:123]\n"
        "ds_write_b32 v245, v81 offset:1792\n"
        "v_mfma_f32_16x16x32_bf16 v[112:115], a[224:227], a[144:147], 0\n"
        "ds_read_b64_tr_b16 v[220:221], v255 offset:64\n"
        "ds_read_b64_tr_b16 v[222:223], v255 offset:832\n"
        "v_mfma_f32_16x16x32_bf16 v[112:115], a[228:231], a[148:151], v[112:115]\n"
        "ds_write_b32 v245, v82 offset:2816\n"
        "v_mfma_f32_16x16x32_bf16 v[112:115], a[232:235], a[152:155], v[112:115]\n"
        "ds_write_b32 v245, v83 offset:3840\n"
        "v_mfma_f32_16x16x32_bf16 v[112:115], a[236:239], a[156:159], v[112:115]\n"
        "buffer_load_dwordx4 v233, s[52:55], s61 offen offset:1024 lds\n"
        "v_mfma_f32_16x16x32_bf16 v[112:115], a[240:243], a[160:163], v[112:115]\n"
        "ds_read_b128 a[248:251], v240 offset:12288\n"
        "v_cvt_pk_bf16_f32 v180, v120, v121\n"
        "v_mfma_f32_16x16x32_bf16 v[112:115], a[244:247], a[164:167], v[112:115]\n"
        ".long 0xbec200ff\n"
        "43:\n"
        "s_branch 60b\n"
        "ds_write_b128 v254, v[92:95] offset:15360\n"
        "v_mfma_f32_16x16x32_bf16 v[96:99], v[152:155], v[156:159], v[0:3]\n"
        "v_cvt_pk_bf16_f32 v181, v122, v123\n"
        "v_mov_b64 v[186:187], v[180:181]\n"
        "v_mfma_f32_16x16x32_bf16 v[96:99], v[168:171], v[160:163], v[96:99]\n"
        "s_sub_u32 s31, s30, s28\n"
        "s_nop 1\n"
        "v_mfma_f32_16x16x32_bf16 v[100:103], v[172:175], v[156:159], v[4:7]\n"
        "s_sub_u32 s45, s28, s29\n"
        "v_cvt_pk_bf16_f32 v130, v112, v113\n"
        "v_cvt_pk_bf16_f32 v131, v114, v115\n"
        "v_mfma_f32_16x16x32_bf16 v[100:103], v[188:191], v[164:167], v[100:103]\n"
        "s_waitcnt vmcnt(2)\n"
        "ds_read_b64_tr_b16 v[224:225], v255 offset:3072\n"
        "ds_read_b64_tr_b16 v[226:227], v255 offset:3840\n"
        "s_add_u32 s28, s28, s31\n"
        "v_mfma_f32_16x16x32_bf16 v[116:119], a[244:247], a[188:191], 0\n"
        "buffer_load_dwordx4 v234, s[52:55], s61 offen offset:2048 lds\n"
        "v_mfma_f32_16x16x32_bf16 v[116:119], a[240:243], a[184:187], v[116:119]\n"
        "s_waitcnt lgkmcnt(5)\n"
        "ds_write_b128 v235, v[212:215] offset:0\n"
        "v_mfma_f32_16x16x32_bf16 v[116:119], a[236:239], a[180:183], v[116:119]\n"
        "v_exp_f32 v96, v96\n"
        "v_mfma_f32_16x16x32_bf16 v[116:119], a[232:235], a[176:179], v[116:119]\n"
        "v_exp_f32 v97, v97\n"
        "v_mfma_f32_16x16x32_bf16 v[116:119], a[228:231], a[172:175], v[116:119]\n"
        "v_exp_f32 v98, v98\n"
        "v_mfma_f32_16x16x32_bf16 v[116:119], a[224:227], a[168:171], v[116:119]\n"
        "ds_read_b64_tr_b16 v[88:89], v255 offset:3104\n"
        "ds_read_b64_tr_b16 v[90:91], v255 offset:3872\n"
        "s_add_u32 m0, s65, 9216\n"
        "v_mfma_f32_16x16x32_bf16 v[104:107], v[152:155], v[176:179], v[16:19]\n"
        "ds_read_b128 a[216:219], v241 offset:12288\n"
        "v_cvt_pk_bf16_f32 v64, v96, v97\n"
        "v_mfma_f32_16x16x32_bf16 v[104:107], v[168:171], v[180:183], v[104:107]\n"
        "ds_write_b128 v239, v[216:219] offset:192\n"
        "v_mfma_f32_16x16x32_bf16 v[108:111], v[172:175], v[176:179], v[20:23]\n"
        "buffer_load_dwordx4 v232, s[52:55], s56 offen lds\n"
        "v_mfma_f32_16x16x32_bf16 v[108:111], v[188:191], v[184:187], v[108:111]\n"
        "v_exp_f32 v100, v100\n"
        "v_mfma_f32_16x16x32_bf16 v[120:123], a[224:227], a[192:195], 0\n"
        "v_cvt_pk_bf16_f32 v138, v116, v117\n"
        "v_cvt_pk_bf16_f32 v139, v118, v119\n"
        "v_mfma_f32_16x16x32_bf16 v[120:123], a[228:231], a[196:199], v[120:123]\n"
        "ds_read_b128 a[220:223], v240 offset:12416\n"
        "ds_read_b128 a[224:227], v241 offset:12416\n"
        "v_mfma_f32_16x16x32_bf16 v[120:123], a[232:235], a[200:203], v[120:123]\n"
        "ds_read_b64_tr_b16 v[92:93], v255 offset:3136\n"
        "ds_read_b64_tr_b16 v[94:95], v255 offset:3904\n"
        "v_mfma_f32_16x16x32_bf16 v[120:123], a[236:239], a[204:207], v[120:123]\n"
        "ds_read_b128 a[228:231], v240 offset:12544\n"
        "ds_read_b128 a[232:235], v241 offset:12544\n"
        "v_mfma_f32_16x16x32_bf16 v[120:123], a[240:243], a[208:211], v[120:123]\n"
        "ds_write_b128 v254, v[220:223] offset:0\n"
        "v_mfma_f32_16x16x32_bf16 v[120:123], a[244:247], a[212:215], v[120:123]\n"
        "s_waitcnt lgkmcnt(8)\n"
        "v_exp_f32 v99, v99\n"
        "v_mfma_f32_16x16x32_bf16 v[112:115], a[0:3], a[248:251], 0\n"
        "buffer_load_dwordx4 v233, s[52:55], s56 offen offset:1024 lds\n"
        "v_mfma_f32_16x16x32_bf16 v[112:115], a[4:7], a[216:219], v[112:115]\n"
        "s_waitcnt lgkmcnt(5)\n"
        "v_exp_f32 v101, v101\n"
        "v_mfma_f32_16x16x32_bf16 v[112:115], a[8:11], a[220:223], v[112:115]\n"
        "ds_read_b128 v[202:205], v251 offset:4096\n"
        "v_cvt_pk_bf16_f32 v65, v98, v99\n"
        "v_mfma_f32_16x16x32_bf16 v[112:115], a[12:15], a[224:227], v[112:115]\n"
        "s_waitcnt lgkmcnt(2)\n"
        "ds_read_b128 v[162:165], v252 offset:0\n"
        "v_cvt_pk_bf16_f32 v66, v100, v101\n"
        "v_mfma_f32_16x16x32_bf16 v[112:115], a[16:19], a[228:231], v[112:115]\n"
        "s_waitcnt vmcnt(2)\n"
        "ds_read_b64_tr_b16 v[212:213], v255 offset:6144\n"
        "ds_read_b64_tr_b16 v[214:215], v255 offset:6912\n"
        "v_mfma_f32_16x16x32_bf16 v[112:115], a[20:23], a[232:235], v[112:115]\n"
        "ds_write_b128 v235, v[224:227] offset:3072\n"
        "v_mfma_f32_16x16x32_bf16 v[116:119], a[44:47], a[232:235], 0\n"
        "v_exp_f32 v102, v102\n"
        "v_mfma_f32_16x16x32_bf16 v[116:119], a[40:43], a[228:231], v[116:119]\n"
        "v_exp_f32 v103, v103\n"
        "v_mfma_f32_16x16x32_bf16 v[116:119], a[36:39], a[224:227], v[116:119]\n"
        "buffer_load_dwordx4 v234, s[52:55], s56 offen offset:2048 lds\n"
        "s_add_u32 s35, s47, 786432\n"
        "s_sub_u32 s46, s35, 100663296\n"
        "s_cmp_lt_u32 s46, 786432\n"
        "v_mfma_f32_16x16x32_bf16 v[116:119], a[32:35], a[220:223], v[116:119]\n"
        "s_cselect_b32 s35, s46, s35\n"
        "s_add_u32 s41, s30, s40\n"
        "s_mov_b32 s30, s29\n"
        "s_add_u32 s29, s29, s45\n"
        "v_mfma_f32_16x16x32_bf16 v[116:119], a[28:31], a[216:219], v[116:119]\n"
        "ds_read_b64_tr_b16 v[216:217], v255 offset:6176\n"
        "ds_read_b64_tr_b16 v[218:219], v255 offset:6944\n"
        "v_mfma_f32_16x16x32_bf16 v[116:119], a[24:27], a[248:251], v[116:119]\n"
        "ds_read_b128 v[182:185], v252 offset:512\n"
        "v_cvt_pk_bf16_f32 v67, v102, v103\n"
        "v_cvt_pk_bf16_f32 v146, v120, v121\n"
        "v_cvt_pk_bf16_f32 v147, v122, v123\n"
        "v_mfma_f32_16x16x32_bf16 v[120:123], a[48:51], a[248:251], 0\n"
        "ds_write_b128 v239, v[88:91] offset:3264\n"
        "v_mfma_f32_16x16x32_bf16 v[120:123], a[52:55], a[216:219], v[120:123]\n"
        "v_exp_f32 v104, v104\n"
        "v_mfma_f32_16x16x32_bf16 v[120:123], a[56:59], a[220:223], v[120:123]\n"
        "s_mov_b32 m0, s48\n"
        "v_exp_f32 v105, v105\n"
        "v_mfma_f32_16x16x32_bf16 v[120:123], a[60:63], a[224:227], v[120:123]\n"
        "v_exp_f32 v106, v106\n"
        "v_mfma_f32_16x16x32_bf16 v[120:123], a[64:67], a[228:231], v[120:123]\n"
        "ds_read_b64_tr_b16 v[220:221], v255 offset:6208\n"
        "ds_read_b64_tr_b16 v[222:223], v255 offset:6976\n"
        "v_mfma_f32_16x16x32_bf16 v[120:123], a[68:71], a[232:235], v[120:123]\n"
        "buffer_load_dwordx4 v249, s[12:15], s36 offen offset:0 lds\n"
        "v_cvt_pk_bf16_f32 v196, v112, v113\n"
        "v_cvt_pk_bf16_f32 v197, v114, v115\n"
        "v_mfma_f32_16x16x32_bf16 v[112:115], a[92:95], a[232:235], 0\n"
        "v_cvt_pk_bf16_f32 v72, v104, v105\n"
        "v_cvt_pk_bf16_f32 v198, v116, v117\n"
        "v_mfma_f32_16x16x32_bf16 v[112:115], a[88:91], a[228:231], v[112:115]\n"
        "ds_write_b128 v254, v[92:95] offset:3072\n"
        "v_mfma_f32_16x16x32_bf16 v[112:115], a[84:87], a[224:227], v[112:115]\n"
        "v_exp_f32 v108, v108\n"
        "v_mfma_f32_16x16x32_bf16 v[112:115], a[80:83], a[220:223], v[112:115]\n"
        "v_exp_f32 v107, v107\n"
        "v_mfma_f32_16x16x32_bf16 v[112:115], a[76:79], a[216:219], v[112:115]\n"
        "v_exp_f32 v109, v109\n"
        "v_mfma_f32_16x16x32_bf16 v[112:115], a[72:75], a[248:251], v[112:115]\n"
        "s_waitcnt vmcnt(1)\n"
        "ds_read_b64_tr_b16 v[224:225], v255 offset:9216\n"
        "ds_read_b64_tr_b16 v[226:227], v255 offset:9984\n"
        "v_cvt_pk_bf16_f32 v199, v118, v119\n"
        "s_nop 1\n"
        "v_mfma_f32_16x16x32_bf16 v[96:99], v[152:155], v[196:199], v[32:35]\n"
        "s_waitcnt lgkmcnt(3)\n"
        "v_cvt_pk_bf16_f32 v73, v106, v107\n"
        "v_cvt_pk_bf16_f32 v74, v108, v109\n"
        "v_cvt_pk_bf16_f32 v200, v120, v121\n"
        "v_cvt_pk_bf16_f32 v201, v122, v123\n"
        "s_nop 1\n"
        "v_mfma_f32_16x16x32_bf16 v[96:99], v[168:171], v[200:203], v[96:99]\n"
        "buffer_load_dwordx4 v253, s[12:15], s36 offen offset:1024 lds\n"
        "v_mfma_f32_16x16x32_bf16 v[100:103], v[172:175], v[196:199], v[36:39]\n"
        "ds_write_b128 v235, v[212:215] offset:6144\n"
        "v_mov_b64 v[206:207], v[200:201]\n"
        "s_nop 1\n"
        "v_mfma_f32_16x16x32_bf16 v[100:103], v[188:191], v[204:207], v[100:103]\n"
        "s_cmp_eq_u32 s37, 100663296\n"
        "v_exp_f32 v110, v110\n"
        "v_mfma_f32_16x16x32_bf16 v[116:119], a[96:99], a[248:251], 0\n"
        "s_cselect_b32 s46, 0, s37\n"
        "v_exp_f32 v111, v111\n"
        "v_mfma_f32_16x16x32_bf16 v[116:119], a[100:103], a[216:219], v[116:119]\n"
        "s_add_u32 m0, s41, 0\n"
        "v_cvt_pk_bf16_f32 v75, v110, v111\n"
        "v_cvt_pk_bf16_f32 v192, v112, v113\n"
        "v_mfma_f32_16x16x32_bf16 v[116:119], a[104:107], a[220:223], v[116:119]\n"
        "ds_read_b64_tr_b16 v[88:89], v255 offset:9248\n"
        "ds_read_b64_tr_b16 v[90:91], v255 offset:10016\n"
        "s_add_u32 s38, s46, s43\n"
        "v_mfma_f32_16x16x32_bf16 v[116:119], a[108:111], a[224:227], v[116:119]\n"
        "buffer_load_dwordx4 v242, s[8:11], s47 offen lds\n"
        "v_mfma_f32_16x16x32_bf16 v[116:119], a[112:115], a[228:231], v[116:119]\n"
        "ds_read_b128 v[202:205], v252 offset:4096\n"
        "s_add_u32 s39, s46, s44\n"
        "v_cvt_pk_bf16_f32 v193, v114, v115\n"
        "v_mfma_f32_16x16x32_bf16 v[116:119], a[116:119], a[232:235], v[116:119]\n"
        "ds_write_b128 v239, v[216:219] offset:6336\n"
        "s_add_u32 s37, s37, 6291456\n"
        "v_mfma_f32_16x16x32_bf16 v[120:123], a[140:143], a[232:235], 0\n"
        "v_exp_f32 v96, v96\n"
        "v_mfma_f32_16x16x32_bf16 v[120:123], a[136:139], a[228:231], v[120:123]\n"
        "s_add_u32 m0, s41, 1024\n"
        "v_exp_f32 v97, v97\n"
        "v_mfma_f32_16x16x32_bf16 v[120:123], a[132:135], a[224:227], v[120:123]\n"
        "v_exp_f32 v98, v98\n"
        "v_mfma_f32_16x16x32_bf16 v[120:123], a[128:131], a[220:223], v[120:123]\n"
        "ds_read_b64_tr_b16 v[92:93], v255 offset:9280\n"
        "ds_read_b64_tr_b16 v[94:95], v255 offset:10048\n"
        "s_mov_b64 s[58:59], s[18:19]\n"
        "v_mfma_f32_16x16x32_bf16 v[120:123], a[124:127], a[216:219], v[120:123]\n"
        "buffer_load_dwordx4 v243, s[8:11], s47 offen lds\n"
        "v_mfma_f32_16x16x32_bf16 v[120:123], a[120:123], a[248:251], v[120:123]\n"
        "s_mov_b64 s[62:63], s[18:19]\n"
        "v_cvt_pk_bf16_f32 v80, v96, v97\n"
        "v_cvt_pk_bf16_f32 v194, v116, v117\n"
        "v_mfma_f32_16x16x32_bf16 v[112:115], a[248:251], a[144:147], 0\n"
        "ds_write_b128 v254, v[220:223] offset:6144\n"
        "v_mfma_f32_16x16x32_bf16 v[112:115], a[216:219], a[148:151], v[112:115]\n"
        "v_exp_f32 v100, v100\n"
        "v_mfma_f32_16x16x32_bf16 v[112:115], a[220:223], a[152:155], v[112:115]\n"
        "s_add_u32 m0, s41, 2048\n"
        "v_cvt_pk_bf16_f32 v195, v118, v119\n"
        "v_cvt_pk_bf16_f32 v208, v120, v121\n"
        "v_mfma_f32_16x16x32_bf16 v[112:115], a[224:227], a[156:159], v[112:115]\n"
        "v_exp_f32 v99, v99\n"
        "v_mfma_f32_16x16x32_bf16 v[112:115], a[228:231], a[160:163], v[112:115]\n"
        "buffer_load_dwordx4 v244, s[8:11], s47 offen lds\n"
        "v_mfma_f32_16x16x32_bf16 v[112:115], a[232:235], a[164:167], v[112:115]\n"
        "ds_read_b128 a[236:239], v240 offset:13824\n"
        "v_cvt_pk_bf16_f32 v81, v98, v99\n"
        "v_mfma_f32_16x16x32_bf16 v[104:107], v[192:195], v[156:159], v[8:11]\n"
        "ds_read_b128 a[240:243], v241 offset:13824\n"
        "v_cvt_pk_bf16_f32 v209, v122, v123\n"
        "s_nop 1\n"
        "v_mfma_f32_16x16x32_bf16 v[104:107], v[208:211], v[160:163], v[104:107]\n"
        "s_waitcnt lgkmcnt(3)\n"
        "ds_write_b128 v235, v[224:227] offset:9216\n"
        "v_mfma_f32_16x16x32_bf16 v[116:119], a[232:235], a[188:191], 0\n"
        "v_exp_f32 v101, v101\n"
        "v_mfma_f32_16x16x32_bf16 v[116:119], a[228:231], a[184:187], v[116:119]\n"
        "s_add_u32 m0, s41, 12288\n"
        "v_exp_f32 v102, v102\n"
        "v_mfma_f32_16x16x32_bf16 v[116:119], a[224:227], a[180:183], v[116:119]\n"
        "v_cvt_pk_bf16_f32 v82, v100, v101\n"
        "v_cvt_pk_bf16_f32 v132, v112, v113\n"
        "v_mfma_f32_16x16x32_bf16 v[116:119], a[220:223], a[176:179], v[116:119]\n"
        "buffer_load_dwordx4 v242, s[8:11], s35 offen lds\n"
        "v_mfma_f32_16x16x32_bf16 v[116:119], a[216:219], a[172:175], v[116:119]\n"
        "ds_read_b128 a[244:247], v240 offset:13952\n"
        "v_cvt_pk_bf16_f32 v133, v114, v115\n"
        "v_mfma_f32_16x16x32_bf16 v[116:119], a[248:251], a[168:171], v[116:119]\n"
        "v_exp_f32 v103, v103\n"
        "v_mfma_f32_16x16x32_bf16 v[108:111], v[192:195], v[176:179], v[24:27]\n"
        "ds_write_b128 v239, v[88:91] offset:9408\n"
        "v_mfma_f32_16x16x32_bf16 v[108:111], v[208:211], v[180:183], v[108:111]\n"
        "v_cvt_pk_bf16_f32 v83, v102, v103\n"
        "v_add_u32 v245, s45, v245\n"
        "v_mfma_f32_16x16x32_bf16 v[120:123], a[248:251], a[192:195], 0\n"
        "s_add_u32 m0, s41, 13312\n"
        "v_exp_f32 v104, v104\n"
        "v_mfma_f32_16x16x32_bf16 v[120:123], a[216:219], a[196:199], v[120:123]\n"
        "v_cvt_pk_bf16_f32 v140, v116, v117\n"
        "v_cvt_pk_bf16_f32 v141, v118, v119\n"
        "v_mfma_f32_16x16x32_bf16 v[120:123], a[220:223], a[200:203], v[120:123]\n"
        "ds_read_b128 a[248:251], v241 offset:13952\n"
        "ds_read_b128 a[216:219], v240 offset:14080\n"
        "v_mfma_f32_16x16x32_bf16 v[120:123], a[224:227], a[204:207], v[120:123]\n"
        "buffer_load_dwordx4 v243, s[8:11], s35 offen lds\n"
        "v_mfma_f32_16x16x32_bf16 v[120:123], a[228:231], a[208:211], v[120:123]\n"
        "ds_read_b128 a[220:223], v241 offset:14080\n"
        "v_bfe_u32 v238, v250, 3, 4\n"
        "v_mfma_f32_16x16x32_bf16 v[120:123], a[232:235], a[212:215], v[120:123]\n"
        "ds_write_b128 v254, v[92:95] offset:9216\n"
        "v_mfma_f32_16x16x32_bf16 v[96:99], v[192:195], v[196:199], v[40:43]\n"
        "v_exp_f32 v105, v105\n"
        "v_mfma_f32_16x16x32_bf16 v[96:99], v[208:211], v[200:203], v[96:99]\n"
        "s_waitcnt lgkmcnt(5)\n"
        "s_add_u32 m0, s41, 14336\n"
        "v_exp_f32 v106, v106\n"
        "v_mfma_f32_16x16x32_bf16 v[112:115], a[72:75], a[236:239], 0\n"
        "v_cvt_pk_bf16_f32 v68, v104, v105\n"
        "v_cvt_pk_bf16_f32 v148, v120, v121\n"
        "v_mfma_f32_16x16x32_bf16 v[112:115], a[76:79], a[240:243], v[112:115]\n"
        "buffer_load_dwordx4 v244, s[8:11], s35 offen lds\n"
        "v_mfma_f32_16x16x32_bf16 v[112:115], a[80:83], a[244:247], v[112:115]\n"
        "s_waitcnt lgkmcnt(1)\n"
        "v_exp_f32 v107, v107\n"
        "v_mfma_f32_16x16x32_bf16 v[112:115], a[84:87], a[248:251], v[112:115]\n"
        "v_cvt_pk_bf16_f32 v69, v106, v107\n"
        "v_cvt_pk_bf16_f32 v149, v122, v123\n"
        "v_mfma_f32_16x16x32_bf16 v[112:115], a[88:91], a[216:219], v[112:115]\n"
        "v_exp_f32 v108, v108\n"
        "v_mfma_f32_16x16x32_bf16 v[112:115], a[92:95], a[220:223], v[112:115]\n"
        "v_exp_f32 v109, v109\n"
        "v_mfma_f32_16x16x32_bf16 v[116:119], a[116:119], a[220:223], 0\n"
        "v_exp_f32 v110, v110\n"
        "v_mfma_f32_16x16x32_bf16 v[116:119], a[112:115], a[216:219], v[116:119]\n"
        "v_cvt_pk_bf16_f32 v76, v108, v109\n"
        "v_and_b32 v254, 32, v250\n"
        "v_mfma_f32_16x16x32_bf16 v[116:119], a[108:111], a[248:251], v[116:119]\n"
        "v_exp_f32 v111, v111\n"
        "v_mfma_f32_16x16x32_bf16 v[116:119], a[104:107], a[244:247], v[116:119]\n"
        "s_add_u32 s56, s16, s38\n"
        "s_addc_u32 s57, s17, 0\n"
        "v_cvt_pk_bf16_f32 v212, v112, v113\n"
        "v_mfma_f32_16x16x32_bf16 v[116:119], a[100:103], a[240:243], v[116:119]\n"
        "s_add_u32 s60, s16, s39\n"
        "s_addc_u32 s61, s17, 0\n"
        "v_cvt_pk_bf16_f32 v213, v114, v115\n"
        "v_mfma_f32_16x16x32_bf16 v[116:119], a[96:99], a[236:239], v[116:119]\n"
        "v_cvt_pk_bf16_f32 v77, v110, v111\n"
        "v_mad_i32_i24 v238, v238, -4, v250\n"
        "v_mfma_f32_16x16x32_bf16 v[120:123], a[120:123], a[236:239], 0\n"
        "v_exp_f32 v96, v96\n"
        "v_mfma_f32_16x16x32_bf16 v[120:123], a[124:127], a[240:243], v[120:123]\n"
        "s_add_u32 s47, s47, 6291456\n"
        "v_exp_f32 v97, v97\n"
        "v_mfma_f32_16x16x32_bf16 v[120:123], a[128:131], a[244:247], v[120:123]\n"
        "s_add_u32 s36, s36, 2097152\n"
        "v_cvt_pk_bf16_f32 v214, v116, v117\n"
        "v_cvt_pk_bf16_f32 v215, v118, v119\n"
        "v_mfma_f32_16x16x32_bf16 v[120:123], a[132:135], a[248:251], v[120:123]\n"
        "s_xor_b32 s48, s48, 8192\n"
        "v_exp_f32 v98, v98\n"
        "v_mfma_f32_16x16x32_bf16 v[120:123], a[136:139], a[216:219], v[120:123]\n"
        "s_waitcnt lgkmcnt(0)\n"
        "s_barrier\n"
        "v_cvt_pk_bf16_f32 v84, v96, v97\n"
        "v_mad_i32_i24 v254, v254, -12, v248\n"
        "v_mfma_f32_16x16x32_bf16 v[120:123], a[140:143], a[220:223], v[120:123]\n"
        "v_exp_f32 v99, v99\n"
        "v_mfma_f32_16x16x32_bf16 v[112:115], a[220:223], a[164:167], 0\n"
        "v_cvt_pk_bf16_f32 v85, v98, v99\n"
        "v_add_u32 v254, 1728, v254\n"
        "v_mfma_f32_16x16x32_bf16 v[112:115], a[216:219], a[160:163], v[112:115]\n"
        "v_add_u32 v238, s49, v238\n"
        "v_add_u32 v240, s31, v240\n"
        "v_mfma_f32_16x16x32_bf16 v[112:115], a[248:251], a[156:159], v[112:115]\n"
        "v_cvt_pk_bf16_f32 v230, v120, v121\n"
        "v_cvt_pk_bf16_f32 v231, v122, v123\n"
        "v_mfma_f32_16x16x32_bf16 v[112:115], a[244:247], a[152:155], v[112:115]\n"
        "v_add_u32 v241, s31, v241\n"
        "v_mfma_f32_16x16x32_bf16 v[112:115], a[240:243], a[148:151], v[112:115]\n"
        "v_mfma_f32_16x16x32_bf16 v[112:115], a[236:239], a[144:147], v[112:115]\n"
        "s_nop 2\n"
        "v_mfma_f32_16x16x32_bf16 v[100:103], v[212:215], v[156:159], v[12:15]\n"
        "s_nop 2\n"
        "v_mfma_f32_16x16x32_bf16 v[100:103], v[228:231], v[164:167], v[100:103]\n"
        "v_cvt_pk_bf16_f32 v134, v112, v113\n"
        "v_cvt_pk_bf16_f32 v135, v114, v115\n"
        "v_mfma_f32_16x16x32_bf16 v[116:119], a[236:239], a[168:171], 0\n"
        "s_waitcnt lgkmcnt(0)\n"
        "v_mfma_f32_16x16x32_bf16 v[116:119], a[240:243], a[172:175], v[116:119]\n"
        "s_bitcmp1_b32 5, s42\n"
        "v_mfma_f32_16x16x32_bf16 v[116:119], a[244:247], a[176:179], v[116:119]\n"
        "s_cbranch_scc1 12f\n"
        "11:\n"
        "10:\n"
        "v_mfma_f32_16x16x32_bf16 v[116:119], a[248:251], a[180:183], v[116:119]\n"
        "v_mfma_f32_16x16x32_bf16 v[116:119], a[216:219], a[184:187], v[116:119]\n"
        "s_nop 0\n"
        "v_exp_f32 v100, v100\n"
        "v_mfma_f32_16x16x32_bf16 v[116:119], a[220:223], a[188:191], v[116:119]\n"
        "v_exp_f32 v101, v101\n"
        "v_mfma_f32_16x16x32_bf16 v[104:107], v[212:215], v[176:179], v[28:31]\n"
        "v_exp_f32 v102, v102\n"
        "v_mfma_f32_16x16x32_bf16 v[104:107], v[228:231], v[184:187], v[104:107]\n"
        "v_exp_f32 v103, v103\n"
        "v_mfma_f32_16x16x32_bf16 v[120:123], a[220:223], a[212:215], 0\n"
        "v_cvt_pk_bf16_f32 v70, v100, v101\n"
        "v_cvt_pk_bf16_f32 v71, v102, v103\n"
        "v_mfma_f32_16x16x32_bf16 v[120:123], a[216:219], a[208:211], v[120:123]\n"
        "v_cvt_pk_bf16_f32 v142, v116, v117\n"
        "v_cvt_pk_bf16_f32 v143, v118, v119\n"
        "v_mfma_f32_16x16x32_bf16 v[120:123], a[248:251], a[204:207], v[120:123]\n"
        "v_mov_b64 v[96:97], 0\n"
        "v_mfma_f32_16x16x32_bf16 v[120:123], a[244:247], a[200:203], v[120:123]\n"
        "v_exp_f32 v104, v104\n"
        "v_mfma_f32_16x16x32_bf16 v[120:123], a[240:243], a[196:199], v[120:123]\n"
        "v_exp_f32 v105, v105\n"
        "v_mfma_f32_16x16x32_bf16 v[120:123], a[236:239], a[192:195], v[120:123]\n"
        "v_exp_f32 v106, v106\n"
        "v_mfma_f32_16x16x32_bf16 v[108:111], v[212:215], v[196:199], v[44:47]\n"
        "v_exp_f32 v107, v107\n"
        "v_mfma_f32_16x16x32_bf16 v[108:111], v[228:231], v[204:207], v[108:111]\n"
        "v_cvt_pk_bf16_f32 v78, v104, v105\n"
        "v_cvt_pk_bf16_f32 v79, v106, v107\n"
        "v_mfma_f32_16x16x32_bf16 v[112:115], a[0:3], a[236:239], 0\n"
        "s_nop 0\n"
        "v_cvt_pk_bf16_f32 v150, v120, v121\n"
        "v_cvt_pk_bf16_f32 v151, v122, v123\n"
        "v_mfma_f32_16x16x32_bf16 v[112:115], a[4:7], a[240:243], v[112:115]\n"
        "v_mfma_f32_16x16x32_bf16 v[112:115], a[8:11], a[244:247], v[112:115]\n"
        "v_exp_f32 v108, v108\n"
        "v_mfma_f32_16x16x32_bf16 v[112:115], a[12:15], a[248:251], v[112:115]\n"
        "v_exp_f32 v109, v109\n"
        "v_mfma_f32_16x16x32_bf16 v[112:115], a[16:19], a[216:219], v[112:115]\n"
        "v_exp_f32 v110, v110\n"
        "v_mfma_f32_16x16x32_bf16 v[112:115], a[20:23], a[220:223], v[112:115]\n"
        "v_exp_f32 v111, v111\n"
        "v_smfmac_f32_16x16x64_bf16 v[96:99], a[252:255], v[64:71], v237\n"
        "v_cvt_pk_bf16_f32 v86, v108, v109\n"
        "v_mfma_f32_16x16x32_bf16 v[100:103], v[132:135], v[68:71], 0\n"
        "v_mov_b64 v[124:125], 0\n"
        "v_cvt_pk_bf16_f32 v87, v110, v111\n"
        "v_mfma_f32_16x16x32_bf16 v[100:103], v[128:131], v[64:67], v[100:103]\n"
        "s_nop 0\n"
        "v_cvt_pk_bf16_f32 v216, v112, v113\n"
        "v_cvt_pk_bf16_f32 v217, v114, v115\n"
        "v_mfma_f32_16x16x32_bf16 v[104:107], v[136:139], v[64:67], 0\n"
        "ds_read_b128 v[222:225], v251 offset:4608\n"
        "v_add_f32 v97, v96, v97\n"
        "v_mfma_f32_16x16x32_bf16 v[104:107], v[140:143], v[68:71], v[104:107]\n"
        "v_rcp_f32 v96, v97\n"
        "v_mfma_f32_16x16x32_bf16 v[108:111], v[148:151], v[68:71], 0\n"
        "v_mul_f32 v100, v96, v100\n"
        "v_mul_f32 v101, v96, v101\n"
        "v_mfma_f32_16x16x32_bf16 v[108:111], v[144:147], v[64:67], v[108:111]\n"
        "v_mul_f32 v102, v96, v102\n"
        "v_mul_f32 v103, v96, v103\n"
        "v_mfma_f32_16x16x32_bf16 v[116:119], a[44:47], a[220:223], 0\n"
        "v_cvt_pk_bf16_f32 v100, v100, v101\n"
        "v_cvt_pk_bf16_f32 v101, v102, v103\n"
        "v_mfma_f32_16x16x32_bf16 v[116:119], a[40:43], a[216:219], v[116:119]\n"
        "v_mul_f32 v104, v96, v104\n"
        "v_mul_f32 v105, v96, v105\n"
        "v_mfma_f32_16x16x32_bf16 v[116:119], a[36:39], a[248:251], v[116:119]\n"
        "v_mul_f32 v106, v96, v106\n"
        "v_mul_f32 v107, v96, v107\n"
        "v_mfma_f32_16x16x32_bf16 v[116:119], a[32:35], a[244:247], v[116:119]\n"
        "v_cvt_pk_bf16_f32 v102, v104, v105\n"
        "v_cvt_pk_bf16_f32 v103, v106, v107\n"
        "v_mfma_f32_16x16x32_bf16 v[116:119], a[28:31], a[240:243], v[116:119]\n"
        "v_mul_f32 v108, v96, v108\n"
        "v_mul_f32 v109, v96, v109\n"
        "v_mfma_f32_16x16x32_bf16 v[116:119], a[24:27], a[236:239], v[116:119]\n"
        "v_mul_f32 v110, v96, v110\n"
        "v_mul_f32 v111, v96, v111\n"
        "v_smfmac_f32_16x16x64_bf16 v[124:127], a[252:255], v[72:79], v237\n"
        "v_cvt_pk_bf16_f32 v108, v108, v109\n"
        "v_mfma_f32_16x16x32_bf16 v[64:67], v[132:135], v[76:79], 0\n"
        "ds_write_b32 v245, v108 offset:4096\n"
        "v_mfma_f32_16x16x32_bf16 v[64:67], v[128:131], v[72:75], v[64:67]\n"
        "ds_read_b128 v[156:159], v247 offset:0\n"
        "v_cvt_pk_bf16_f32 v218, v116, v117\n"
        "v_mfma_f32_16x16x32_bf16 v[68:71], v[136:139], v[72:75], 0\n"
        "ds_read_b128 v[160:163], v247 offset:4096\n"
        "v_cvt_pk_bf16_f32 v219, v118, v119\n"
        "v_mfma_f32_16x16x32_bf16 v[68:71], v[140:143], v[76:79], v[68:71]\n"
        "ds_read_b128 v[164:167], v247 offset:4352\n"
        "v_cvt_pk_bf16_f32 v109, v110, v111\n"
        "v_mfma_f32_16x16x32_bf16 v[120:123], v[148:151], v[76:79], 0\n"
        "ds_write_b32 v245, v109 offset:5120\n"
        "v_mfma_f32_16x16x32_bf16 v[120:123], v[144:147], v[72:75], v[120:123]\n"
        "v_max_u32 v236, v96, v97\n"
        "v_mov_b64 v[116:117], 0\n"
        "v_mfma_f32_16x16x32_bf16 v[112:115], a[48:51], a[236:239], 0\n"
        "v_add_f32 v125, v124, v125\n"
        "v_mfma_f32_16x16x32_bf16 v[112:115], a[52:55], a[240:243], v[112:115]\n"
        "v_rcp_f32 v124, v125\n"
        "v_mfma_f32_16x16x32_bf16 v[112:115], a[56:59], a[244:247], v[112:115]\n"
        "v_mul_f32 v64, v124, v64\n"
        "v_mul_f32 v65, v124, v65\n"
        "v_mfma_f32_16x16x32_bf16 v[112:115], a[60:63], a[248:251], v[112:115]\n"
        "v_mul_f32 v66, v124, v66\n"
        "v_mul_f32 v67, v124, v67\n"
        "v_mfma_f32_16x16x32_bf16 v[112:115], a[64:67], a[216:219], v[112:115]\n"
        "v_cvt_pk_bf16_f32 v64, v64, v65\n"
        "v_cvt_pk_bf16_f32 v65, v66, v67\n"
        "v_mfma_f32_16x16x32_bf16 v[112:115], a[68:71], a[220:223], v[112:115]\n"
        "ds_read_b128 a[216:219], v248 offset:0\n"
        "v_mul_f32 v68, v124, v68\n"
        "v_smfmac_f32_16x16x64_bf16 v[116:119], a[252:255], v[80:87], v237\n"
        "ds_read_b128 a[220:223], v248 offset:3072\n"
        "v_mfma_f32_16x16x32_bf16 v[72:75], v[132:135], v[84:87], 0\n"
        "v_mul_f32 v69, v124, v69\n"
        "v_mul_f32 v70, v124, v70\n"
        "v_mfma_f32_16x16x32_bf16 v[72:75], v[128:131], v[80:83], v[72:75]\n"
        "v_cvt_pk_bf16_f32 v220, v112, v113\n"
        "v_cvt_pk_bf16_f32 v221, v114, v115\n"
        "v_mfma_f32_16x16x32_bf16 v[76:79], v[136:139], v[80:83], 0\n"
        "ds_read_b128 a[224:227], v248 offset:32\n"
        "v_mov_b64 v[226:227], v[220:221]\n"
        "v_mfma_f32_16x16x32_bf16 v[76:79], v[140:143], v[84:87], v[76:79]\n"
        "ds_read_b128 a[228:231], v248 offset:3104\n"
        "v_mul_f32 v71, v124, v71\n"
        "v_mfma_f32_16x16x32_bf16 v[96:99], v[148:151], v[84:87], 0\n"
        "v_cvt_pk_bf16_f32 v66, v68, v69\n"
        "v_cvt_pk_bf16_f32 v67, v70, v71\n"
        "v_mfma_f32_16x16x32_bf16 v[96:99], v[144:147], v[80:83], v[96:99]\n"
        "v_mul_f32 v120, v124, v120\n"
        "v_mul_f32 v121, v124, v121\n"
        "v_mfma_f32_16x16x32_bf16 v[104:107], v[152:155], v[216:219], v[48:51]\n"
        "s_waitcnt lgkmcnt(2)\n"
        "ds_read_b128 v[152:155], v247 offset:256\n"
        "v_mul_f32 v122, v124, v122\n"
        "v_mfma_f32_16x16x32_bf16 v[104:107], v[168:171], v[220:223], v[104:107]\n"
        "v_mul_f32 v123, v124, v123\n"
        "v_cvt_pk_bf16_f32 v120, v120, v121\n"
        "v_mfma_f32_16x16x32_bf16 v[108:111], v[172:175], v[216:219], v[52:55]\n"
        "ds_write_b32 v245, v120 offset:4352\n"
        "v_mfma_f32_16x16x32_bf16 v[108:111], v[188:191], v[224:227], v[108:111]\n"
        "ds_read_b128 v[222:225], v252 offset:4608\n"
        "v_cvt_pk_bf16_f32 v121, v122, v123\n"
        "v_mfma_f32_16x16x32_bf16 v[176:179], v[156:159], a[216:219], 0\n"
        "ds_write_b32 v245, v121 offset:5376\n"
        "v_mfma_f32_16x16x32_bf16 v[176:179], v[160:163], a[220:223], v[176:179]\n"
        "ds_read_b128 a[232:235], v254 offset:0\n"
        "v_max3_u32 v236, v236, v124, v125\n"
        "v_mfma_f32_16x16x32_bf16 v[196:199], v[164:167], a[220:223], 0\n"
        "s_waitcnt lgkmcnt(4)\n"
        "ds_read_b128 a[220:223], v254 offset:3072\n"
        "v_add_f32 v117, v116, v117\n"
        "v_mfma_f32_16x16x32_bf16 v[196:199], v[152:155], a[216:219], v[196:199]\n"
        "v_rcp_f32 v116, v117\n"
        "v_mfma_f32_16x16x32_bf16 v[200:203], v[152:155], a[224:227], 0\n"
        "ds_read_b128 a[216:219], v254 offset:32\n"
        "v_mul_f32 v72, v116, v72\n"
        "v_mfma_f32_16x16x32_bf16 v[200:203], v[164:167], a[228:231], v[200:203]\n"
        "v_exp_f32 v104, v104\n"
        "v_mfma_f32_16x16x32_bf16 v[180:183], v[160:163], a[228:231], 0\n"
        "ds_read_b128 a[228:231], v254 offset:3104\n"
        "v_mul_f32 v73, v116, v73\n"
        "v_mfma_f32_16x16x32_bf16 v[180:183], v[156:159], a[224:227], v[180:183]\n"
        "ds_read_b128 a[236:239], v247 offset:8192\n"
        "v_mul_f32 v74, v116, v74\n"
        "v_mfma_f32_16x16x32_bf16 v[112:115], v[192:195], v[216:219], v[56:59]\n"
        "s_waitcnt lgkmcnt(6)\n"
        "ds_read_b128 a[224:227], v248 offset:16\n"
        "v_mul_f32 v75, v116, v75\n"
        "v_mfma_f32_16x16x32_bf16 v[112:115], v[208:211], v[220:223], v[112:115]\n"
        "ds_read_b128 a[240:243], v247 offset:12288\n"
        "v_cvt_pk_bf16_f32 v72, v72, v73\n"
        "v_mfma_f32_16x16x32_bf16 v[68:71], v[212:215], v[216:219], v[60:63]\n"
        "ds_read_b128 a[244:247], v247 offset:12544\n"
        "v_cvt_pk_bf16_f32 v73, v74, v75\n"
        "v_mfma_f32_16x16x32_bf16 v[68:71], v[228:231], v[224:227], v[68:71]\n"
        "s_waitcnt lgkmcnt(4)\n"
        "ds_read_b128 a[248:251], v247 offset:8448\n"
        "v_mul_f32 v76, v116, v76\n"
        "v_mfma_f32_16x16x32_bf16 v[184:187], v[156:159], a[232:235], 0\n"
        "v_mul_f32 v77, v116, v77\n"
        "v_mul_f32 v78, v116, v78\n"
        "v_mfma_f32_16x16x32_bf16 v[184:187], v[160:163], a[220:223], v[184:187]\n"
        "v_mul_f32 v79, v116, v79\n"
        "v_cvt_pk_bf16_f32 v74, v76, v77\n"
        "v_mfma_f32_16x16x32_bf16 v[204:207], v[164:167], a[220:223], 0\n"
        "ds_read_b128 a[220:223], v248 offset:3088\n"
        "v_cvt_pk_bf16_f32 v75, v78, v79\n"
        "v_mfma_f32_16x16x32_bf16 v[204:207], v[152:155], a[232:235], v[204:207]\n"
        "ds_read_b128 a[232:235], v254 offset:16\n"
        "v_mul_f32 v96, v116, v96\n"
        "v_mfma_f32_16x16x32_bf16 v[192:195], v[152:155], a[216:219], 0\n"
        "v_mul_f32 v97, v116, v97\n"
        "v_mul_f32 v98, v116, v98\n"
        "v_mfma_f32_16x16x32_bf16 v[192:195], v[164:167], a[228:231], v[192:195]\n"
        "v_mul_f32 v99, v116, v99\n"
        "v_cvt_pk_bf16_f32 v96, v96, v97\n"
        "v_mfma_f32_16x16x32_bf16 v[172:175], v[160:163], a[228:231], 0\n"
        "ds_read_b128 a[228:231], v254 offset:3088\n"
        "v_cvt_pk_bf16_f32 v97, v98, v99\n"
        "v_mfma_f32_16x16x32_bf16 v[172:175], v[156:159], a[216:219], v[172:175]\n"
        "s_waitcnt lgkmcnt(2)\n"
        "ds_read_b128 a[216:219], v248 offset:6144\n"
        "v_max3_u32 v236, v236, v116, v117\n"
        "v_mfma_f32_16x16x32_bf16 v[212:215], v[156:159], a[224:227], 0\n"
        "v_exp_f32 v105, v105\n"
        "v_mfma_f32_16x16x32_bf16 v[212:215], v[160:163], a[220:223], v[212:215]\n"
        "v_exp_f32 v106, v106\n"
        "v_mfma_f32_16x16x32_bf16 v[216:219], v[164:167], a[220:223], 0\n"
        "ds_read_b128 a[220:223], v248 offset:9216\n"
        "v_cvt_pk_bf16_f32 v88, v104, v105\n"
        "v_mfma_f32_16x16x32_bf16 v[216:219], v[152:155], a[224:227], v[216:219]\n"
        "s_waitcnt lgkmcnt(2)\n"
        "ds_read_b128 a[224:227], v248 offset:6176\n"
        "v_mfma_f32_16x16x32_bf16 v[224:227], v[152:155], a[232:235], 0\n"
        "v_exp_f32 v108, v108\n"
        "v_mfma_f32_16x16x32_bf16 v[224:227], v[164:167], a[228:231], v[224:227]\n"
        "v_exp_f32 v107, v107\n"
        "v_mfma_f32_16x16x32_bf16 v[220:223], v[160:163], a[228:231], 0\n"
        "ds_read_b128 a[228:231], v248 offset:9248\n"
        "v_cvt_pk_bf16_f32 v89, v106, v107\n"
        "v_mfma_f32_16x16x32_bf16 v[220:223], v[156:159], a[232:235], v[220:223]\n"
        "s_waitcnt lgkmcnt(2)\n"
        "ds_read_b128 a[232:235], v254 offset:6144\n"
        "v_mfma_f32_16x16x32_bf16 v[176:179], a[236:239], a[216:219], v[176:179]\n"
        "v_exp_f32 v109, v109\n"
        "v_mfma_f32_16x16x32_bf16 v[176:179], a[240:243], a[220:223], v[176:179]\n"
        "v_exp_f32 v110, v110\n"
        "v_mfma_f32_16x16x32_bf16 v[196:199], a[244:247], a[220:223], v[196:199]\n"
        "ds_read_b128 a[220:223], v254 offset:9216\n"
        "v_cvt_pk_bf16_f32 v90, v108, v109\n"
        "v_mfma_f32_16x16x32_bf16 v[196:199], a[248:251], a[216:219], v[196:199]\n"
        "s_waitcnt lgkmcnt(2)\n"
        "ds_read_b128 a[216:219], v254 offset:6176\n"
        "v_mfma_f32_16x16x32_bf16 v[200:203], a[248:251], a[224:227], v[200:203]\n"
        "ds_read_b128 v[156:159], v247 offset:16384\n"
        "v_mfma_f32_16x16x32_bf16 v[200:203], a[244:247], a[228:231], v[200:203]\n"
        "ds_read_b128 v[160:163], v247 offset:20480\n"
        "v_mfma_f32_16x16x32_bf16 v[180:183], a[240:243], a[228:231], v[180:183]\n"
        "ds_read_b128 a[228:231], v254 offset:9248\n"
        "v_mfma_f32_16x16x32_bf16 v[180:183], a[236:239], a[224:227], v[180:183]\n"
        "s_waitcnt lgkmcnt(4)\n"
        "ds_read_b128 a[224:227], v248 offset:6160\n"
        "v_mfma_f32_16x16x32_bf16 v[184:187], a[236:239], a[232:235], v[184:187]\n"
        "ds_read_b128 v[164:167], v247 offset:20736\n"
        "v_mfma_f32_16x16x32_bf16 v[184:187], a[240:243], a[220:223], v[184:187]\n"
        "ds_read_b128 v[152:155], v247 offset:16640\n"
        "v_mfma_f32_16x16x32_bf16 v[204:207], a[244:247], a[220:223], v[204:207]\n"
        "ds_read_b128 a[220:223], v248 offset:9232\n"
        "v_mfma_f32_16x16x32_bf16 v[204:207], a[248:251], a[232:235], v[204:207]\n"
        "s_waitcnt lgkmcnt(4)\n"
        "ds_read_b128 a[232:235], v254 offset:6160\n"
        "v_mfma_f32_16x16x32_bf16 v[192:195], a[248:251], a[216:219], v[192:195]\n"
        "v_exp_f32 v111, v111\n"
        "v_mfma_f32_16x16x32_bf16 v[192:195], a[244:247], a[228:231], v[192:195]\n"
        "v_cvt_pk_bf16_f32 v91, v110, v111\n"
        "v_mfma_f32_16x16x32_bf16 v[172:175], a[240:243], a[228:231], v[172:175]\n"
        "ds_read_b128 a[228:231], v254 offset:9232\n"
        "v_mfma_f32_16x16x32_bf16 v[172:175], a[236:239], a[216:219], v[172:175]\n"
        "s_waitcnt lgkmcnt(2)\n"
        "ds_read_b128 a[216:219], v248 offset:12288\n"
        "v_mfma_f32_16x16x32_bf16 v[212:215], a[236:239], a[224:227], v[212:215]\n"
        "v_exp_f32 v112, v112\n"
        "v_mfma_f32_16x16x32_bf16 v[212:215], a[240:243], a[220:223], v[212:215]\n"
        "v_exp_f32 v113, v113\n"
        "v_mfma_f32_16x16x32_bf16 v[216:219], a[244:247], a[220:223], v[216:219]\n"
        "ds_read_b128 a[220:223], v248 offset:15360\n"
        "v_cvt_pk_bf16_f32 v92, v112, v113\n"
        "v_mfma_f32_16x16x32_bf16 v[216:219], a[248:251], a[224:227], v[216:219]\n"
        "s_waitcnt lgkmcnt(2)\n"
        "ds_read_b128 a[224:227], v248 offset:12320\n"
        "v_mfma_f32_16x16x32_bf16 v[224:227], a[248:251], a[232:235], v[224:227]\n"
        ".long 0xbec200ff\n"
        "44:\n"
        "s_branch 60b\n"
        "v_exp_f32 v114, v114\n"
        "v_mfma_f32_16x16x32_bf16 v[224:227], a[244:247], a[228:231], v[224:227]\n"
        "v_exp_f32 v68, v68\n"
        "v_mfma_f32_16x16x32_bf16 v[220:223], a[240:243], a[228:231], v[220:223]\n"
        "ds_read_b128 a[228:231], v248 offset:15392\n"
        "v_mfma_f32_16x16x32_bf16 v[220:223], a[236:239], a[232:235], v[220:223]\n"
        "s_waitcnt lgkmcnt(2)\n"
        "ds_read_b128 a[232:235], v254 offset:12288\n"
        "v_mfma_f32_16x16x32_bf16 v[176:179], v[156:159], a[216:219], v[176:179]\n"
        "v_exp_f32 v115, v115\n"
        "v_mfma_f32_16x16x32_bf16 v[176:179], v[160:163], a[220:223], v[176:179]\n"
        "v_exp_f32 v69, v69\n"
        "v_mfma_f32_16x16x32_bf16 v[196:199], v[164:167], a[220:223], v[196:199]\n"
        "ds_read_b128 a[220:223], v254 offset:15360\n"
        "v_cvt_pk_bf16_f32 v93, v114, v115\n"
        "v_mfma_f32_16x16x32_bf16 v[196:199], v[152:155], a[216:219], v[196:199]\n"
        "s_waitcnt lgkmcnt(2)\n"
        "ds_read_b128 a[216:219], v254 offset:12320\n"
        "v_cvt_pk_bf16_f32 v94, v68, v69\n"
        "v_mfma_f32_16x16x32_bf16 v[200:203], v[152:155], a[224:227], v[200:203]\n"
        "v_exp_f32 v70, v70\n"
        "v_mfma_f32_16x16x32_bf16 v[200:203], v[164:167], a[228:231], v[200:203]\n"
        "s_waitcnt vmcnt(0)\n"
        "v_exp_f32 v71, v71\n"
        "v_mfma_f32_16x16x32_bf16 v[180:183], v[160:163], a[228:231], v[180:183]\n"
        "ds_read_b128 a[228:231], v254 offset:15392\n"
        "v_cvt_pk_bf16_f32 v95, v70, v71\n"
        "v_mfma_f32_16x16x32_bf16 v[180:183], v[156:159], a[224:227], v[180:183]\n"
        "s_waitcnt lgkmcnt(2)\n"
        "ds_read_b128 a[224:227], v248 offset:12304\n"
        "v_mfma_f32_16x16x32_bf16 v[184:187], v[156:159], a[232:235], v[184:187]\n"
        "v_mfma_f32_16x16x32_bf16 v[184:187], v[160:163], a[220:223], v[184:187]\n"
        "s_barrier\n"
        "v_mfma_f32_16x16x32_bf16 v[204:207], v[164:167], a[220:223], v[204:207]\n"
        "ds_read_b128 a[220:223], v248 offset:15376\n"
        "v_mfma_f32_16x16x32_bf16 v[204:207], v[152:155], a[232:235], v[204:207]\n"
        "s_waitcnt lgkmcnt(2)\n"
        "ds_read_b128 a[232:235], v254 offset:12304\n"
        "v_mfma_f32_16x16x32_bf16 v[192:195], v[152:155], a[216:219], v[192:195]\n"
        "ds_read_b128 a[236:239], v240 offset:0\n"
        "v_mfma_f32_16x16x32_bf16 v[192:195], v[164:167], a[228:231], v[192:195]\n"
        "ds_read_b128 a[240:243], v241 offset:0\n"
        "v_mfma_f32_16x16x32_bf16 v[172:175], v[160:163], a[228:231], v[172:175]\n"
        "ds_read_b128 a[228:231], v254 offset:15376\n"
        "v_mfma_f32_16x16x32_bf16 v[172:175], v[156:159], a[216:219], v[172:175]\n"
        "s_waitcnt lgkmcnt(4)\n"
        "ds_read_b128 a[244:247], v240 offset:128\n"
        "v_mfma_f32_16x16x32_bf16 v[212:215], v[156:159], a[224:227], v[212:215]\n"
        "ds_read_b128 a[248:251], v241 offset:128\n"
        "v_mfma_f32_16x16x32_bf16 v[212:215], v[160:163], a[220:223], v[212:215]\n"
        "ds_read_b128 a[216:219], v240 offset:256\n"
        "v_mfma_f32_16x16x32_bf16 v[216:219], v[164:167], a[220:223], v[216:219]\n"
        "ds_read_b128 a[220:223], v241 offset:256\n"
        "v_mfma_f32_16x16x32_bf16 v[216:219], v[152:155], a[224:227], v[216:219]\n"
        "s_waitcnt lgkmcnt(4)\n"
        "v_mfma_f32_16x16x32_bf16 v[224:227], v[152:155], a[232:235], v[224:227]\n"
        "v_mfma_f32_16x16x32_bf16 v[224:227], v[164:167], a[228:231], v[224:227]\n"
        "ds_write_b32 v245, v96 offset:4608\n"
        "v_mfma_f32_16x16x32_bf16 v[220:223], v[160:163], a[228:231], v[220:223]\n"
        "v_mfma_f32_16x16x32_bf16 v[220:223], v[156:159], a[232:235], v[220:223]\n"
        "ds_write_b32 v245, v97 offset:5632\n"
        "v_mfma_f32_16x16x32_bf16 v[112:115], a[72:75], a[236:239], 0\n"
        "v_mov_b64 v[124:125], 0\n"
        "v_cvt_pk_bf16_f32 v232, v176, v180\n"
        "v_mfma_f32_16x16x32_bf16 v[112:115], a[76:79], a[240:243], v[112:115]\n"
        "s_waitcnt lgkmcnt(2)\n"
        "v_cvt_pk_bf16_f32 v233, v184, v172\n"
        "v_cvt_pk_bf16_f32 v234, v177, v181\n"
        "v_mfma_f32_16x16x32_bf16 v[112:115], a[80:83], a[244:247], v[112:115]\n"
        "buffer_store_dwordx2 v[232:233], v250, s[56:59], 0 offen offset:0 sc1\n"
        "v_cvt_pk_bf16_f32 v235, v185, v173\n"
        "v_mfma_f32_16x16x32_bf16 v[112:115], a[84:87], a[248:251], v[112:115]\n"
        "v_cvt_pk_bf16_f32 v232, v178, v182\n"
        "v_cvt_pk_bf16_f32 v233, v186, v174\n"
        "v_mfma_f32_16x16x32_bf16 v[112:115], a[88:91], a[216:219], v[112:115]\n"
        "v_mfma_f32_16x16x32_bf16 v[112:115], a[92:95], a[220:223], v[112:115]\n"
        "ds_write_b32 v245, v100 offset:0\n"
        "v_smfmac_f32_16x16x64_bf16 v[124:127], a[252:255], v[88:95], v237\n"
        "s_nop 1\n"
        "v_mfma_f32_16x16x32_bf16 v[80:83], v[132:135], v[92:95], 0\n"
        "buffer_store_dwordx2 v[234:235], v250, s[56:59], 0 offen offset:384 sc1\n"
        "s_nop 0\n"
        "v_mfma_f32_16x16x32_bf16 v[80:83], v[128:131], v[88:91], v[80:83]\n"
        "s_add_u32 s41, s30, s40\n"
        "v_cvt_pk_bf16_f32 v152, v112, v113\n"
        "v_cvt_pk_bf16_f32 v153, v114, v115\n"
        "v_mfma_f32_16x16x32_bf16 v[84:87], v[136:139], v[88:91], 0\n"
        "v_cvt_pk_bf16_f32 v234, v179, v183\n"
        "v_cvt_pk_bf16_f32 v235, v187, v175\n"
        "v_mfma_f32_16x16x32_bf16 v[84:87], v[140:143], v[92:95], v[84:87]\n"
        "v_add_f32 v125, v124, v125\n"
        "v_mfma_f32_16x16x32_bf16 v[96:99], v[148:151], v[92:95], 0\n"
        "buffer_store_dwordx2 v[232:233], v250, s[56:59], 0 offen offset:768 sc1\n"
        "v_mfma_f32_16x16x32_bf16 v[96:99], v[144:147], v[88:91], v[96:99]\n"
        "s_add_u32 s35, s47, 786432\n"
        "v_rcp_f32 v124, v125\n"
        "v_mfma_f32_16x16x32_bf16 v[116:119], a[116:119], a[220:223], 0\n"
        "s_sub_u32 s46, s35, 100663296\n"
        "v_mul_f32 v80, v124, v80\n"
        "v_mul_f32 v81, v124, v81\n"
        "v_mfma_f32_16x16x32_bf16 v[116:119], a[112:115], a[216:219], v[116:119]\n"
        "s_cmp_lt_u32 s46, 786432\n"
        "v_mul_f32 v82, v124, v82\n"
        "v_mul_f32 v83, v124, v83\n"
        "v_mfma_f32_16x16x32_bf16 v[116:119], a[108:111], a[248:251], v[116:119]\n"
        "buffer_store_dwordx2 v[234:235], v250, s[56:59], 0 offen offset:1152 sc1\n"
        "v_cvt_pk_bf16_f32 v80, v80, v81\n"
        "v_mfma_f32_16x16x32_bf16 v[116:119], a[104:107], a[244:247], v[116:119]\n"
        "s_cselect_b32 s35, s46, s35\n"
        "v_cvt_pk_bf16_f32 v81, v82, v83\n"
        "v_cvt_pk_bf16_f32 v232, v196, v200\n"
        "v_mfma_f32_16x16x32_bf16 v[116:119], a[100:103], a[240:243], v[116:119]\n"
        "v_cvt_pk_bf16_f32 v233, v204, v192\n"
        "v_mul_f32 v84, v124, v84\n"
        "v_mfma_f32_16x16x32_bf16 v[116:119], a[96:99], a[236:239], v[116:119]\n"
        "v_mul_f32 v85, v124, v85\n"
        "v_mul_f32 v86, v124, v86\n"
        "v_mfma_f32_16x16x32_bf16 v[120:123], a[120:123], a[236:239], 0\n"
        "ds_write_b32 v245, v101 offset:1024\n"
        "v_mfma_f32_16x16x32_bf16 v[120:123], a[124:127], a[240:243], v[120:123]\n"
        "buffer_store_dwordx2 v[232:233], v250, s[60:63], 0 offen offset:0 sc1\n"
        "v_mul_f32 v87, v124, v87\n"
        "v_mfma_f32_16x16x32_bf16 v[120:123], a[128:131], a[244:247], v[120:123]\n"
        "v_cvt_pk_bf16_f32 v82, v84, v85\n"
        "v_cvt_pk_bf16_f32 v83, v86, v87\n"
        "v_mfma_f32_16x16x32_bf16 v[120:123], a[132:135], a[248:251], v[120:123]\n"
        "v_mul_f32 v96, v124, v96\n"
        "v_mul_f32 v97, v124, v97\n"
        "v_mfma_f32_16x16x32_bf16 v[120:123], a[136:139], a[216:219], v[120:123]\n"
        "v_mul_f32 v98, v124, v98\n"
        "v_mul_f32 v99, v124, v99\n"
        "v_mfma_f32_16x16x32_bf16 v[120:123], a[140:143], a[220:223], v[120:123]\n"
        "v_cvt_pk_bf16_f32 v234, v197, v201\n"
        "v_cvt_pk_bf16_f32 v235, v205, v193\n"
        "v_mfma_f32_16x16x32_bf16 v[112:115], a[20:23], a[220:223], 0\n"
        "buffer_store_dwordx2 v[234:235], v250, s[60:63], 0 offen offset:384 sc1\n"
        "v_cvt_pk_bf16_f32 v154, v116, v117\n"
        "v_mfma_f32_16x16x32_bf16 v[112:115], a[16:19], a[216:219], v[112:115]\n"
        "v_cvt_pk_bf16_f32 v155, v118, v119\n"
        "v_cvt_pk_bf16_f32 v96, v96, v97\n"
        "v_mfma_f32_16x16x32_bf16 v[112:115], a[12:15], a[248:251], v[112:115]\n"
        "ds_write_b32 v245, v96 offset:4864\n"
        "v_mfma_f32_16x16x32_bf16 v[112:115], a[8:11], a[244:247], v[112:115]\n"
        "v_cvt_pk_bf16_f32 v97, v98, v99\n"
        "v_max3_u32 v236, v236, v124, v125\n"
        "v_mfma_f32_16x16x32_bf16 v[112:115], a[4:7], a[240:243], v[112:115]\n"
        "v_cvt_pk_bf16_f32 v232, v198, v202\n"
        "v_cvt_pk_bf16_f32 v233, v206, v194\n"
        "v_mfma_f32_16x16x32_bf16 v[112:115], a[0:3], a[236:239], v[112:115]\n"
        "buffer_store_dwordx2 v[232:233], v250, s[60:63], 0 offen offset:768 sc1\n"
        "v_cmp_lt_u32 vcc, s24, v236\n"
        "v_mfma_f32_16x16x32_bf16 v[116:119], a[24:27], a[236:239], 0\n"
        "v_cvt_pk_bf16_f32 v168, v120, v121\n"
        "v_cvt_pk_bf16_f32 v169, v122, v123\n"
        "v_mfma_f32_16x16x32_bf16 v[116:119], a[28:31], a[240:243], v[116:119]\n"
        "s_cmp_lg_u64 vcc, 0\n"
        "s_addc_u32 s25, s25, s25\n"
        "v_cvt_pk_bf16_f32 v234, v199, v203\n"
        "v_mfma_f32_16x16x32_bf16 v[116:119], a[32:35], a[244:247], v[116:119]\n"
        "v_cvt_pk_bf16_f32 v235, v207, v195\n"
        "v_cvt_pk_bf16_f32 v156, v112, v113\n"
        "v_mfma_f32_16x16x32_bf16 v[116:119], a[36:39], a[248:251], v[116:119]\n"
        "buffer_store_dwordx2 v[234:235], v250, s[60:63], 0 offen offset:1152 sc1\n"
        "v_cvt_pk_bf16_f32 v157, v114, v115\n"
        "v_mfma_f32_16x16x32_bf16 v[116:119], a[40:43], a[216:219], v[116:119]\n"
        "v_cvt_pk_bf16_f32 v232, v212, v220\n"
        "v_cvt_pk_bf16_f32 v234, v213, v221\n"
        "v_mfma_f32_16x16x32_bf16 v[116:119], a[44:47], a[220:223], v[116:119]\n"
        "ds_write_b32 v245, v102 offset:2048\n"
        "v_mfma_f32_16x16x32_bf16 v[120:123], a[68:71], a[220:223], 0\n"
        "v_xor_b32 v251, 0x2000, v251\n"
        "v_xor_b32 v252, 0x2000, v252\n"
        "v_mfma_f32_16x16x32_bf16 v[120:123], a[64:67], a[216:219], v[120:123]\n"
        "buffer_store_dword v232, v238, s[56:59], 0 offen offset:0 sc1\n"
        "v_mfma_f32_16x16x32_bf16 v[120:123], a[60:63], a[248:251], v[120:123]\n"
        "ds_read_b128 a[224:227], v240 offset:1536\n"
        "v_cvt_pk_bf16_f32 v158, v116, v117\n"
        "v_mfma_f32_16x16x32_bf16 v[120:123], a[56:59], a[244:247], v[120:123]\n"
        "ds_read_b128 a[228:231], v241 offset:1536\n"
        "v_cvt_pk_bf16_f32 v159, v118, v119\n"
        "v_mfma_f32_16x16x32_bf16 v[120:123], a[52:55], a[240:243], v[120:123]\n"
        "ds_read_b128 a[232:235], v240 offset:1664\n"
        "v_cvt_pk_bf16_f32 v232, v214, v222\n"
        "v_mfma_f32_16x16x32_bf16 v[120:123], a[48:51], a[236:239], v[120:123]\n"
        "buffer_store_dword v234, v238, s[56:59], 0 offen offset:384 sc1\n"
        "v_mfma_f32_16x16x32_bf16 v[112:115], a[236:239], a[144:147], 0\n"
        "v_add_u32 v247, s45, v247\n"
        "v_cvt_pk_bf16_f32 v234, v215, v223\n"
        "v_mfma_f32_16x16x32_bf16 v[112:115], a[240:243], a[148:151], v[112:115]\n"
        "ds_write_b32 v245, v103 offset:3072\n"
        "v_mfma_f32_16x16x32_bf16 v[112:115], a[244:247], a[152:155], v[112:115]\n"
        "s_nop 0\n"
        "v_cvt_pk_bf16_f32 v160, v120, v121\n"
        "v_cvt_pk_bf16_f32 v161, v122, v123\n"
        "v_mfma_f32_16x16x32_bf16 v[112:115], a[248:251], a[156:159], v[112:115]\n"
        "v_mov_b64 v[166:167], v[160:161]\n"
        "v_mfma_f32_16x16x32_bf16 v[112:115], a[216:219], a[160:163], v[112:115]\n"
        "buffer_store_dword v232, v238, s[56:59], 0 offen offset:768 sc1\n"
        "v_mfma_f32_16x16x32_bf16 v[112:115], a[220:223], a[164:167], v[112:115]\n"
        "ds_write_b32 v245, v97 offset:5888\n"
        "v_mfma_f32_16x16x32_bf16 v[116:119], a[220:223], a[188:191], 0\n"
        "v_cvt_pk_bf16_f32 v232, v216, v224\n"
        "s_nop 0\n"
        "v_mfma_f32_16x16x32_bf16 v[116:119], a[216:219], a[184:187], v[116:119]\n"
        "s_nop 2\n"
        "v_mfma_f32_16x16x32_bf16 v[116:119], a[248:251], a[180:183], v[116:119]\n"
        "buffer_store_dword v234, v238, s[56:59], 0 offen offset:1152 sc1\n"
        "v_mfma_f32_16x16x32_bf16 v[116:119], a[244:247], a[176:179], v[116:119]\n"
        "v_cvt_pk_bf16_f32 v128, v112, v113\n"
        "v_cvt_pk_bf16_f32 v129, v114, v115\n"
        "v_mfma_f32_16x16x32_bf16 v[116:119], a[240:243], a[172:175], v[116:119]\n"
        "ds_write_b32 v245, v64 offset:256\n"
        "v_mfma_f32_16x16x32_bf16 v[116:119], a[236:239], a[168:171], v[116:119]\n"
        "v_cvt_pk_bf16_f32 v234, v217, v225\n"
        "s_nop 0\n"
        "v_mfma_f32_16x16x32_bf16 v[120:123], a[236:239], a[192:195], 0\n"
        "ds_read_b128 a[236:239], v241 offset:1664\n"
        "s_nop 0\n"
        "v_mfma_f32_16x16x32_bf16 v[120:123], a[240:243], a[196:199], v[120:123]\n"
        "buffer_store_dword v232, v238, s[60:63], 0 offen offset:0 sc1\n"
        "v_mfma_f32_16x16x32_bf16 v[120:123], a[244:247], a[200:203], v[120:123]\n"
        "ds_read_b128 a[240:243], v240 offset:1792\n"
        "ds_read_b128 a[244:247], v241 offset:1792\n"
        "v_mfma_f32_16x16x32_bf16 v[120:123], a[248:251], a[204:207], v[120:123]\n"
        "v_cvt_pk_bf16_f32 v136, v116, v117\n"
        "v_cvt_pk_bf16_f32 v137, v118, v119\n"
        "v_mfma_f32_16x16x32_bf16 v[120:123], a[216:219], a[208:211], v[120:123]\n"
        "v_cvt_pk_bf16_f32 v232, v218, v226\n"
        "v_mfma_f32_16x16x32_bf16 v[120:123], a[220:223], a[212:215], v[120:123]\n"
        "s_waitcnt lgkmcnt(4)\n"
        "buffer_store_dword v234, v238, s[60:63], 0 offen offset:384 sc1\n"
        "v_mfma_f32_16x16x32_bf16 v[112:115], a[72:75], a[224:227], 0\n"
        "s_nop 2\n"
        "v_mfma_f32_16x16x32_bf16 v[112:115], a[76:79], a[228:231], v[112:115]\n"
        "v_cvt_pk_bf16_f32 v234, v219, v227\n"
        "s_nop 0\n"
        "v_mfma_f32_16x16x32_bf16 v[112:115], a[80:83], a[232:235], v[112:115]\n"
        "s_waitcnt lgkmcnt(0)\n"
        "v_cvt_pk_bf16_f32 v144, v120, v121\n"
        "v_cvt_pk_bf16_f32 v145, v122, v123\n"
        "v_mfma_f32_16x16x32_bf16 v[112:115], a[84:87], a[236:239], v[112:115]\n"
        "ds_write_b32 v245, v65 offset:1280\n"
        "v_mfma_f32_16x16x32_bf16 v[112:115], a[88:91], a[240:243], v[112:115]\n"
        "buffer_store_dword v232, v238, s[60:63], 0 offen offset:768 sc1\n"
        "v_mfma_f32_16x16x32_bf16 v[112:115], a[92:95], a[244:247], v[112:115]\n"
        "s_nop 2\n"
        "v_mfma_f32_16x16x32_bf16 v[116:119], a[116:119], a[244:247], 0\n"
        "s_nop 2\n"
        "v_mfma_f32_16x16x32_bf16 v[116:119], a[112:115], a[240:243], v[116:119]\n"
        "v_cvt_pk_bf16_f32 v172, v112, v113\n"
        "v_cvt_pk_bf16_f32 v173, v114, v115\n"
        "v_mfma_f32_16x16x32_bf16 v[116:119], a[108:111], a[236:239], v[116:119]\n"
        "buffer_store_dword v234, v238, s[60:63], 0 offen offset:1152 sc1\n"
        "v_mfma_f32_16x16x32_bf16 v[116:119], a[104:107], a[232:235], v[116:119]\n"
        "ds_write_b32 v245, v66 offset:2304\n"
        "v_mfma_f32_16x16x32_bf16 v[116:119], a[100:103], a[228:231], v[116:119]\n"
        "v_mfma_f32_16x16x32_bf16 v[116:119], a[96:99], a[224:227], v[116:119]\n"
        "s_mov_b32 m0, s48\n"
        "s_nop 1\n"
        "v_mfma_f32_16x16x32_bf16 v[120:123], a[120:123], a[224:227], 0\n"
        "ds_read_b128 v[162:165], v251 offset:0\n"
        "s_nop 0\n"
        "v_mfma_f32_16x16x32_bf16 v[120:123], a[124:127], a[228:231], v[120:123]\n"
        "buffer_load_dwordx4 v249, s[12:15], s36 offen offset:0 lds\n"
        "v_mfma_f32_16x16x32_bf16 v[120:123], a[128:131], a[232:235], v[120:123]\n"
        "v_cvt_pk_bf16_f32 v174, v116, v117\n"
        "v_cvt_pk_bf16_f32 v175, v118, v119\n"
        "v_mfma_f32_16x16x32_bf16 v[120:123], a[132:135], a[236:239], v[120:123]\n"
        "ds_write_b32 v245, v67 offset:3328\n"
        "v_mfma_f32_16x16x32_bf16 v[120:123], a[136:139], a[240:243], v[120:123]\n"
        "ds_write_b32 v245, v72 offset:512\n"
        "v_mfma_f32_16x16x32_bf16 v[120:123], a[140:143], a[244:247], v[120:123]\n"
        "s_nop 2\n"
        "v_mfma_f32_16x16x32_bf16 v[112:115], a[20:23], a[244:247], 0\n"
        "s_nop 2\n"
        "v_mfma_f32_16x16x32_bf16 v[112:115], a[16:19], a[240:243], v[112:115]\n"
        "v_cvt_pk_bf16_f32 v190, v120, v121\n"
        "v_cvt_pk_bf16_f32 v191, v122, v123\n"
        "v_mfma_f32_16x16x32_bf16 v[112:115], a[12:15], a[236:239], v[112:115]\n"
        "ds_write_b32 v245, v73 offset:1536\n"
        "v_mfma_f32_16x16x32_bf16 v[112:115], a[8:11], a[232:235], v[112:115]\n"
        "ds_write_b32 v245, v74 offset:2560\n"
        "v_mfma_f32_16x16x32_bf16 v[112:115], a[4:7], a[228:231], v[112:115]\n"
        "ds_read_b128 v[182:185], v251 offset:512\n"
        "v_mfma_f32_16x16x32_bf16 v[112:115], a[0:3], a[224:227], v[112:115]\n"
        "s_nop 2\n"
        "v_mfma_f32_16x16x32_bf16 v[116:119], a[24:27], a[224:227], 0\n"
        "s_nop 2\n"
        "v_mfma_f32_16x16x32_bf16 v[116:119], a[28:31], a[228:231], v[116:119]\n"
        "v_cvt_pk_bf16_f32 v176, v112, v113\n"
        "v_cvt_pk_bf16_f32 v177, v114, v115\n"
        "v_mfma_f32_16x16x32_bf16 v[116:119], a[32:35], a[232:235], v[116:119]\n"
        "ds_write_b32 v245, v75 offset:3584\n"
        "v_mfma_f32_16x16x32_bf16 v[116:119], a[36:39], a[236:239], v[116:119]\n"
        "buffer_load_dwordx4 v253, s[12:15], s36 offen offset:1024 lds\n"
        "v_mfma_f32_16x16x32_bf16 v[116:119], a[40:43], a[240:243], v[116:119]\n"
        "ds_write_b32 v245, v80 offset:768\n"
        "v_mfma_f32_16x16x32_bf16 v[116:119], a[44:47], a[244:247], v[116:119]\n"
        "s_nop 2\n"
        "v_mfma_f32_16x16x32_bf16 v[120:123], a[68:71], a[244:247], 0\n"
        "s_nop 2\n"
        "v_mfma_f32_16x16x32_bf16 v[120:123], a[64:67], a[240:243], v[120:123]\n"
        "v_cvt_pk_bf16_f32 v178, v116, v117\n"
        "v_cvt_pk_bf16_f32 v179, v118, v119\n"
        "v_mfma_f32_16x16x32_bf16 v[120:123], a[60:63], a[236:239], v[120:123]\n"
        "ds_write_b32 v245, v81 offset:1792\n"
        "v_mfma_f32_16x16x32_bf16 v[120:123], a[56:59], a[232:235], v[120:123]\n"
        "ds_write_b32 v245, v82 offset:2816\n"
        "v_mfma_f32_16x16x32_bf16 v[120:123], a[52:55], a[228:231], v[120:123]\n"
        "ds_write_b32 v245, v83 offset:3840\n"
        "v_mfma_f32_16x16x32_bf16 v[120:123], a[48:51], a[224:227], v[120:123]\n"
        "s_nop 2\n"
        "v_mfma_f32_16x16x32_bf16 v[112:115], a[224:227], a[144:147], 0\n"
        "s_nop 2\n"
        "v_mfma_f32_16x16x32_bf16 v[112:115], a[228:231], a[148:151], v[112:115]\n"
        "v_cvt_pk_bf16_f32 v180, v120, v121\n"
        "v_cvt_pk_bf16_f32 v181, v122, v123\n"
        "v_mfma_f32_16x16x32_bf16 v[112:115], a[232:235], a[152:155], v[112:115]\n"
        "v_mov_b64 v[186:187], v[180:181]\n"
        "v_mfma_f32_16x16x32_bf16 v[112:115], a[236:239], a[156:159], v[112:115]\n"
        "s_add_u32 m0, s41, 0\n"
        "v_mfma_f32_16x16x32_bf16 v[112:115], a[240:243], a[160:163], v[112:115]\n"
        "ds_read_b128 a[248:251], v240 offset:12288\n"
        "v_mfma_f32_16x16x32_bf16 v[112:115], a[244:247], a[164:167], v[112:115]\n"
        "buffer_load_dwordx4 v242, s[8:11], s47 offen lds\n"
        "v_mfma_f32_16x16x32_bf16 v[96:99], v[152:155], v[156:159], v[0:3]\n"
        "s_waitcnt lgkmcnt(2)\n"
        "ds_read_b128 a[216:219], v241 offset:12288\n"
        "s_nop 0\n"
        "v_mfma_f32_16x16x32_bf16 v[96:99], v[168:171], v[160:163], v[96:99]\n"
        "ds_read_b128 a[220:223], v240 offset:12416\n"
        "s_nop 0\n"
        "v_mfma_f32_16x16x32_bf16 v[100:103], v[172:175], v[156:159], v[4:7]\n"
        "s_sub_u32 s31, s30, s28\n"
        "v_cvt_pk_bf16_f32 v130, v112, v113\n"
        "v_cvt_pk_bf16_f32 v131, v114, v115\n"
        "v_mfma_f32_16x16x32_bf16 v[100:103], v[188:191], v[164:167], v[100:103]\n"
        "s_sub_u32 s45, s28, s29\n"
        "ds_read_b128 v[162:165], v252 offset:0\n"
        "v_mfma_f32_16x16x32_bf16 v[116:119], a[244:247], a[188:191], 0\n"
        "s_add_u32 s28, s28, s31\n"
        "v_exp_f32 v96, v96\n"
        "v_mfma_f32_16x16x32_bf16 v[116:119], a[240:243], a[184:187], v[116:119]\n"
        "s_mov_b32 s30, s29\n"
        "v_exp_f32 v97, v97\n"
        "v_mfma_f32_16x16x32_bf16 v[116:119], a[236:239], a[180:183], v[116:119]\n"
        "s_add_u32 s29, s29, s45\n"
        "v_exp_f32 v98, v98\n"
        "v_mfma_f32_16x16x32_bf16 v[116:119], a[232:235], a[176:179], v[116:119]\n"
        "v_exp_f32 v100, v100\n"
        "v_mfma_f32_16x16x32_bf16 v[116:119], a[228:231], a[172:175], v[116:119]\n"
        "v_exp_f32 v99, v99\n"
        "v_mfma_f32_16x16x32_bf16 v[116:119], a[224:227], a[168:171], v[116:119]\n"
        "v_exp_f32 v101, v101\n"
        "v_mfma_f32_16x16x32_bf16 v[104:107], v[152:155], v[176:179], v[16:19]\n"
        "v_exp_f32 v102, v102\n"
        "v_mfma_f32_16x16x32_bf16 v[104:107], v[168:171], v[180:183], v[104:107]\n"
        "s_add_u32 m0, s41, 1024\n"
        "v_cvt_pk_bf16_f32 v64, v96, v97\n"
        "v_cvt_pk_bf16_f32 v65, v98, v99\n"
        "v_mfma_f32_16x16x32_bf16 v[108:111], v[172:175], v[176:179], v[20:23]\n"
        "v_cvt_pk_bf16_f32 v138, v116, v117\n"
        "v_cvt_pk_bf16_f32 v139, v118, v119\n"
        "v_mfma_f32_16x16x32_bf16 v[108:111], v[188:191], v[184:187], v[108:111]\n"
        "buffer_load_dwordx4 v243, s[8:11], s47 offen lds\n"
        "v_mfma_f32_16x16x32_bf16 v[120:123], a[224:227], a[192:195], 0\n"
        "ds_read_b128 a[224:227], v241 offset:12416\n"
        "v_cvt_pk_bf16_f32 v66, v100, v101\n"
        "v_mfma_f32_16x16x32_bf16 v[120:123], a[228:231], a[196:199], v[120:123]\n"
        "ds_read_b128 a[228:231], v240 offset:12544\n"
        "v_mfma_f32_16x16x32_bf16 v[120:123], a[232:235], a[200:203], v[120:123]\n"
        "ds_read_b128 a[232:235], v241 offset:12544\n"
        "v_mfma_f32_16x16x32_bf16 v[120:123], a[236:239], a[204:207], v[120:123]\n"
        "ds_read_b128 v[202:205], v251 offset:4096\n"
        "v_mfma_f32_16x16x32_bf16 v[120:123], a[240:243], a[208:211], v[120:123]\n"
        "ds_read_b128 v[182:185], v252 offset:512\n"
        "v_mfma_f32_16x16x32_bf16 v[120:123], a[244:247], a[212:215], v[120:123]\n"
        "s_waitcnt lgkmcnt(4)\n"
        "v_exp_f32 v103, v103\n"
        "v_mfma_f32_16x16x32_bf16 v[112:115], a[0:3], a[248:251], 0\n"
        "v_cvt_pk_bf16_f32 v67, v102, v103\n"
        "s_nop 0\n"
        "v_mfma_f32_16x16x32_bf16 v[112:115], a[4:7], a[216:219], v[112:115]\n"
        "v_exp_f32 v104, v104\n"
        "v_mfma_f32_16x16x32_bf16 v[112:115], a[8:11], a[220:223], v[112:115]\n"
        "v_exp_f32 v105, v105\n"
        "v_mfma_f32_16x16x32_bf16 v[112:115], a[12:15], a[224:227], v[112:115]\n"
        "s_waitcnt lgkmcnt(0)\n"
        "v_exp_f32 v106, v106\n"
        "v_mfma_f32_16x16x32_bf16 v[112:115], a[16:19], a[228:231], v[112:115]\n"
        "v_cvt_pk_bf16_f32 v72, v104, v105\n"
        "v_cvt_pk_bf16_f32 v146, v120, v121\n"
        "v_mfma_f32_16x16x32_bf16 v[112:115], a[20:23], a[232:235], v[112:115]\n"
        "s_add_u32 m0, s41, 2048\n"
        "v_exp_f32 v108, v108\n"
        "v_mfma_f32_16x16x32_bf16 v[116:119], a[44:47], a[232:235], 0\n"
        "v_cvt_pk_bf16_f32 v147, v122, v123\n"
        "v_add_u32 v245, s45, v245\n"
        "v_mfma_f32_16x16x32_bf16 v[116:119], a[40:43], a[228:231], v[116:119]\n"
        "buffer_load_dwordx4 v244, s[8:11], s47 offen lds\n"
        "v_mfma_f32_16x16x32_bf16 v[116:119], a[36:39], a[224:227], v[116:119]\n"
        "v_exp_f32 v107, v107\n"
        "v_mfma_f32_16x16x32_bf16 v[116:119], a[32:35], a[220:223], v[116:119]\n"
        "v_exp_f32 v109, v109\n"
        "v_mfma_f32_16x16x32_bf16 v[116:119], a[28:31], a[216:219], v[116:119]\n"
        "v_cvt_pk_bf16_f32 v73, v106, v107\n"
        "v_cvt_pk_bf16_f32 v74, v108, v109\n"
        "v_mfma_f32_16x16x32_bf16 v[116:119], a[24:27], a[248:251], v[116:119]\n"
        "v_exp_f32 v110, v110\n"
        "v_mfma_f32_16x16x32_bf16 v[120:123], a[48:51], a[248:251], 0\n"
        "v_exp_f32 v111, v111\n"
        "v_mfma_f32_16x16x32_bf16 v[120:123], a[52:55], a[216:219], v[120:123]\n"
        "v_cvt_pk_bf16_f32 v75, v110, v111\n"
        "v_cvt_pk_bf16_f32 v196, v112, v113\n"
        "v_mfma_f32_16x16x32_bf16 v[120:123], a[56:59], a[220:223], v[120:123]\n"
        "v_cvt_pk_bf16_f32 v197, v114, v115\n"
        "v_cvt_pk_bf16_f32 v198, v116, v117\n"
        "v_mfma_f32_16x16x32_bf16 v[120:123], a[60:63], a[224:227], v[120:123]\n"
        "v_cvt_pk_bf16_f32 v199, v118, v119\n"
        "v_mfma_f32_16x16x32_bf16 v[120:123], a[64:67], a[228:231], v[120:123]\n"
        "v_mfma_f32_16x16x32_bf16 v[120:123], a[68:71], a[232:235], v[120:123]\n"
        "s_nop 2\n"
        "v_mfma_f32_16x16x32_bf16 v[112:115], a[92:95], a[232:235], 0\n"
        "s_nop 2\n"
        "v_mfma_f32_16x16x32_bf16 v[112:115], a[88:91], a[228:231], v[112:115]\n"
        "s_add_u32 m0, s41, 12288\n"
        "v_cvt_pk_bf16_f32 v200, v120, v121\n"
        "v_cvt_pk_bf16_f32 v201, v122, v123\n"
        "v_mfma_f32_16x16x32_bf16 v[112:115], a[84:87], a[224:227], v[112:115]\n"
        "v_mov_b64 v[206:207], v[200:201]\n"
        "v_mfma_f32_16x16x32_bf16 v[112:115], a[80:83], a[220:223], v[112:115]\n"
        "buffer_load_dwordx4 v242, s[8:11], s35 offen lds\n"
        "v_mfma_f32_16x16x32_bf16 v[112:115], a[76:79], a[216:219], v[112:115]\n"
        "v_mfma_f32_16x16x32_bf16 v[112:115], a[72:75], a[248:251], v[112:115]\n"
        "s_nop 2\n"
        "v_mfma_f32_16x16x32_bf16 v[96:99], v[152:155], v[196:199], v[32:35]\n"
        "s_nop 2\n"
        "v_mfma_f32_16x16x32_bf16 v[96:99], v[168:171], v[200:203], v[96:99]\n"
        "v_cvt_pk_bf16_f32 v192, v112, v113\n"
        "v_cvt_pk_bf16_f32 v193, v114, v115\n"
        "v_mfma_f32_16x16x32_bf16 v[100:103], v[172:175], v[196:199], v[36:39]\n"
        "v_mfma_f32_16x16x32_bf16 v[100:103], v[188:191], v[204:207], v[100:103]\n"
        "ds_read_b128 v[202:205], v252 offset:4096\n"
        "v_mfma_f32_16x16x32_bf16 v[116:119], a[96:99], a[248:251], 0\n"
        "s_cmp_eq_u32 s37, 100663296\n"
        "v_mfma_f32_16x16x32_bf16 v[116:119], a[100:103], a[216:219], v[116:119]\n"
        "s_cselect_b32 s46, 0, s37\n"
        "v_exp_f32 v96, v96\n"
        "v_mfma_f32_16x16x32_bf16 v[116:119], a[104:107], a[220:223], v[116:119]\n"
        "s_add_u32 s38, s46, s43\n"
        "v_exp_f32 v97, v97\n"
        "v_mfma_f32_16x16x32_bf16 v[116:119], a[108:111], a[224:227], v[116:119]\n"
        "s_add_u32 s39, s46, s44\n"
        "v_exp_f32 v98, v98\n"
        "v_mfma_f32_16x16x32_bf16 v[116:119], a[112:115], a[228:231], v[116:119]\n"
        "s_add_u32 s37, s37, 6291456\n"
        "v_exp_f32 v100, v100\n"
        "v_mfma_f32_16x16x32_bf16 v[116:119], a[116:119], a[232:235], v[116:119]\n"
        "s_add_u32 m0, s41, 13312\n"
        "v_exp_f32 v99, v99\n"
        "v_mfma_f32_16x16x32_bf16 v[120:123], a[140:143], a[232:235], 0\n"
        "v_exp_f32 v101, v101\n"
        "v_mfma_f32_16x16x32_bf16 v[120:123], a[136:139], a[228:231], v[120:123]\n"
        "buffer_load_dwordx4 v243, s[8:11], s35 offen lds\n"
        "v_mfma_f32_16x16x32_bf16 v[120:123], a[132:135], a[224:227], v[120:123]\n"
        "v_cvt_pk_bf16_f32 v80, v96, v97\n"
        "v_cvt_pk_bf16_f32 v194, v116, v117\n"
        "v_mfma_f32_16x16x32_bf16 v[120:123], a[128:131], a[220:223], v[120:123]\n"
        "s_mov_b64 s[58:59], s[18:19]\n"
        "v_cvt_pk_bf16_f32 v195, v118, v119\n"
        "v_cvt_pk_bf16_f32 v81, v98, v99\n"
        "v_mfma_f32_16x16x32_bf16 v[120:123], a[124:127], a[216:219], v[120:123]\n"
        "s_mov_b64 s[62:63], s[18:19]\n"
        "v_exp_f32 v102, v102\n"
        "v_mfma_f32_16x16x32_bf16 v[120:123], a[120:123], a[248:251], v[120:123]\n"
        "v_cvt_pk_bf16_f32 v82, v100, v101\n"
        "s_nop 0\n"
        "v_mfma_f32_16x16x32_bf16 v[112:115], a[248:251], a[144:147], 0\n"
        "v_exp_f32 v103, v103\n"
        "v_mfma_f32_16x16x32_bf16 v[112:115], a[216:219], a[148:151], v[112:115]\n"
        "v_cvt_pk_bf16_f32 v83, v102, v103\n"
        "s_nop 0\n"
        "v_mfma_f32_16x16x32_bf16 v[112:115], a[220:223], a[152:155], v[112:115]\n"
        "ds_read_b128 a[236:239], v240 offset:13824\n"
        "v_cvt_pk_bf16_f32 v208, v120, v121\n"
        "v_mfma_f32_16x16x32_bf16 v[112:115], a[224:227], a[156:159], v[112:115]\n"
        "ds_read_b128 a[240:243], v241 offset:13824\n"
        "v_cvt_pk_bf16_f32 v209, v122, v123\n"
        "v_mfma_f32_16x16x32_bf16 v[112:115], a[228:231], a[160:163], v[112:115]\n"
        "ds_read_b128 a[244:247], v240 offset:13952\n"
        "v_mfma_f32_16x16x32_bf16 v[112:115], a[232:235], a[164:167], v[112:115]\n"
        "s_nop 2\n"
        "v_mfma_f32_16x16x32_bf16 v[104:107], v[192:195], v[156:159], v[8:11]\n"
        "s_nop 2\n"
        "v_mfma_f32_16x16x32_bf16 v[104:107], v[208:211], v[160:163], v[104:107]\n"
        "s_add_u32 m0, s41, 14336\n"
        "v_cvt_pk_bf16_f32 v132, v112, v113\n"
        "v_cvt_pk_bf16_f32 v133, v114, v115\n"
        "v_mfma_f32_16x16x32_bf16 v[116:119], a[232:235], a[188:191], 0\n"
        "v_mfma_f32_16x16x32_bf16 v[116:119], a[228:231], a[184:187], v[116:119]\n"
        "buffer_load_dwordx4 v244, s[8:11], s35 offen lds\n"
        "v_mfma_f32_16x16x32_bf16 v[116:119], a[224:227], a[180:183], v[116:119]\n"
        "v_mfma_f32_16x16x32_bf16 v[116:119], a[220:223], a[176:179], v[116:119]\n"
        "v_exp_f32 v104, v104\n"
        "v_mfma_f32_16x16x32_bf16 v[116:119], a[216:219], a[172:175], v[116:119]\n"
        "v_exp_f32 v105, v105\n"
        "v_mfma_f32_16x16x32_bf16 v[116:119], a[248:251], a[168:171], v[116:119]\n"
        "v_exp_f32 v106, v106\n"
        "v_mfma_f32_16x16x32_bf16 v[108:111], v[192:195], v[176:179], v[24:27]\n"
        "v_exp_f32 v107, v107\n"
        "v_mfma_f32_16x16x32_bf16 v[108:111], v[208:211], v[180:183], v[108:111]\n"
        "v_cvt_pk_bf16_f32 v68, v104, v105\n"
        "v_cvt_pk_bf16_f32 v69, v106, v107\n"
        "v_mfma_f32_16x16x32_bf16 v[120:123], a[248:251], a[192:195], 0\n"
        "ds_read_b128 a[248:251], v241 offset:13952\n"
        "v_cvt_pk_bf16_f32 v140, v116, v117\n"
        "v_mfma_f32_16x16x32_bf16 v[120:123], a[216:219], a[196:199], v[120:123]\n"
        "ds_read_b128 a[216:219], v240 offset:14080\n"
        "v_cvt_pk_bf16_f32 v141, v118, v119\n"
        "v_mfma_f32_16x16x32_bf16 v[120:123], a[220:223], a[200:203], v[120:123]\n"
        "ds_read_b128 a[220:223], v241 offset:14080\n"
        "v_mfma_f32_16x16x32_bf16 v[120:123], a[224:227], a[204:207], v[120:123]\n"
        "v_exp_f32 v108, v108\n"
        "v_mfma_f32_16x16x32_bf16 v[120:123], a[228:231], a[208:211], v[120:123]\n"
        "v_exp_f32 v109, v109\n"
        "v_mfma_f32_16x16x32_bf16 v[120:123], a[232:235], a[212:215], v[120:123]\n"
        "v_exp_f32 v110, v110\n"
        "v_mfma_f32_16x16x32_bf16 v[96:99], v[192:195], v[196:199], v[40:43]\n"
        "s_waitcnt lgkmcnt(1)\n"
        "v_exp_f32 v111, v111\n"
        "v_mfma_f32_16x16x32_bf16 v[96:99], v[208:211], v[200:203], v[96:99]\n"
        "v_cvt_pk_bf16_f32 v76, v108, v109\n"
        "v_cvt_pk_bf16_f32 v77, v110, v111\n"
        "v_mfma_f32_16x16x32_bf16 v[112:115], a[72:75], a[236:239], 0\n"
        "s_nop 0\n"
        "v_cvt_pk_bf16_f32 v148, v120, v121\n"
        "v_cvt_pk_bf16_f32 v149, v122, v123\n"
        "v_mfma_f32_16x16x32_bf16 v[112:115], a[76:79], a[240:243], v[112:115]\n"
        "v_mfma_f32_16x16x32_bf16 v[112:115], a[80:83], a[244:247], v[112:115]\n"
        "v_exp_f32 v96, v96\n"
        "v_mfma_f32_16x16x32_bf16 v[112:115], a[84:87], a[248:251], v[112:115]\n"
        "v_exp_f32 v97, v97\n"
        "v_mfma_f32_16x16x32_bf16 v[112:115], a[88:91], a[216:219], v[112:115]\n"
        "s_waitcnt lgkmcnt(0)\n"
        "v_exp_f32 v98, v98\n"
        "v_mfma_f32_16x16x32_bf16 v[112:115], a[92:95], a[220:223], v[112:115]\n"
        "v_exp_f32 v99, v99\n"
        "v_mfma_f32_16x16x32_bf16 v[116:119], a[116:119], a[220:223], 0\n"
        "v_cvt_pk_bf16_f32 v84, v96, v97\n"
        "v_cvt_pk_bf16_f32 v85, v98, v99\n"
        "v_mfma_f32_16x16x32_bf16 v[116:119], a[112:115], a[216:219], v[116:119]\n"
        "v_add_u32 v240, s31, v240\n"
        "v_add_u32 v241, s31, v241\n"
        "v_mfma_f32_16x16x32_bf16 v[116:119], a[108:111], a[248:251], v[116:119]\n"
        "v_cvt_pk_bf16_f32 v212, v112, v113\n"
        "v_cvt_pk_bf16_f32 v213, v114, v115\n"
        "v_mfma_f32_16x16x32_bf16 v[116:119], a[104:107], a[244:247], v[116:119]\n"
        "s_add_u32 s56, s16, s38\n"
        "s_addc_u32 s57, s17, 0\n"
        "v_mfma_f32_16x16x32_bf16 v[116:119], a[100:103], a[240:243], v[116:119]\n"
        "s_add_u32 s60, s16, s39\n"
        "s_addc_u32 s61, s17, 0\n"
        "v_mfma_f32_16x16x32_bf16 v[116:119], a[96:99], a[236:239], v[116:119]\n"
        "s_nop 2\n"
        "v_mfma_f32_16x16x32_bf16 v[120:123], a[120:123], a[236:239], 0\n"
        "s_nop 2\n"
        "v_mfma_f32_16x16x32_bf16 v[120:123], a[124:127], a[240:243], v[120:123]\n"
        "s_add_u32 s47, s47, 6291456\n"
        "v_cvt_pk_bf16_f32 v214, v116, v117\n"
        "v_cvt_pk_bf16_f32 v215, v118, v119\n"
        "v_mfma_f32_16x16x32_bf16 v[120:123], a[128:131], a[244:247], v[120:123]\n"
        "s_add_u32 s36, s36, 2097152\n"
        "v_mfma_f32_16x16x32_bf16 v[120:123], a[132:135], a[248:251], v[120:123]\n"
        "s_xor_b32 s48, s48, 8192\n"
        "v_mfma_f32_16x16x32_bf16 v[120:123], a[136:139], a[216:219], v[120:123]\n"
        ".long 0xbec200ff\n"
        "45:\n"
        "s_branch 61b\n"
        "v_mfma_f32_16x16x32_bf16 v[120:123], a[140:143], a[220:223], v[120:123]\n"
        "s_nop 2\n"
        "v_mfma_f32_16x16x32_bf16 v[112:115], a[220:223], a[164:167], 0\n"
        "s_nop 2\n"
        "v_mfma_f32_16x16x32_bf16 v[112:115], a[216:219], a[160:163], v[112:115]\n"
        "s_barrier\n"
        "v_cvt_pk_bf16_f32 v230, v120, v121\n"
        "v_cvt_pk_bf16_f32 v231, v122, v123\n"
        "v_mfma_f32_16x16x32_bf16 v[112:115], a[248:251], a[156:159], v[112:115]\n"
        "v_mfma_f32_16x16x32_bf16 v[112:115], a[244:247], a[152:155], v[112:115]\n"
        "v_mfma_f32_16x16x32_bf16 v[112:115], a[240:243], a[148:151], v[112:115]\n"
        "v_mfma_f32_16x16x32_bf16 v[112:115], a[236:239], a[144:147], v[112:115]\n"
        "s_nop 2\n"
        "v_mfma_f32_16x16x32_bf16 v[100:103], v[212:215], v[156:159], v[12:15]\n"
        "s_nop 2\n"
        "v_mfma_f32_16x16x32_bf16 v[100:103], v[228:231], v[164:167], v[100:103]\n"
        "v_cvt_pk_bf16_f32 v134, v112, v113\n"
        "v_cvt_pk_bf16_f32 v135, v114, v115\n"
        "v_mfma_f32_16x16x32_bf16 v[116:119], a[236:239], a[168:171], 0\n"
        "s_sub_u32 s27, s27, 1\n"
        "v_mfma_f32_16x16x32_bf16 v[116:119], a[240:243], a[172:175], v[116:119]\n"
        "s_cmp_lg_u32 s27, 0\n"
        "v_mfma_f32_16x16x32_bf16 v[116:119], a[244:247], a[176:179], v[116:119]\n"
        "s_cbranch_scc1 10b\n"
        "s_branch 19f\n"
        ".p2align 6\n"
        "51:\n"
        "s_branch 61b\n"
        "s_nop 0\n"
        "s_nop 0\n"
        "s_nop 0\n"
        "s_nop 0\n"
        "s_nop 0\n"
        "12:\n"
        "10:\n"
        "v_mfma_f32_16x16x32_bf16 v[116:119], a[248:251], a[180:183], v[116:119]\n"
        "v_mfma_f32_16x16x32_bf16 v[116:119], a[216:219], a[184:187], v[116:119]\n"
        "s_nop 0\n"
        "v_exp_f32 v100, v100\n"
        "v_mfma_f32_16x16x32_bf16 v[116:119], a[220:223], a[188:191], v[116:119]\n"
        "v_exp_f32 v101, v101\n"
        "v_mfma_f32_16x16x32_bf16 v[104:107], v[212:215], v[176:179], v[28:31]\n"
        "v_exp_f32 v102, v102\n"
        "v_mfma_f32_16x16x32_bf16 v[104:107], v[228:231], v[184:187], v[104:107]\n"
        "v_exp_f32 v103, v103\n"
        "v_mfma_f32_16x16x32_bf16 v[120:123], a[220:223], a[212:215], 0\n"
        "v_cvt_pk_bf16_f32 v70, v100, v101\n"
        "v_cvt_pk_bf16_f32 v71, v102, v103\n"
        "v_mfma_f32_16x16x32_bf16 v[120:123], a[216:219], a[208:211], v[120:123]\n"
        "v_cvt_pk_bf16_f32 v142, v116, v117\n"
        "v_cvt_pk_bf16_f32 v143, v118, v119\n"
        "v_mfma_f32_16x16x32_bf16 v[120:123], a[248:251], a[204:207], v[120:123]\n"
        "v_mov_b64 v[96:97], 0\n"
        "v_mfma_f32_16x16x32_bf16 v[120:123], a[244:247], a[200:203], v[120:123]\n"
        "v_exp_f32 v104, v104\n"
        "v_mfma_f32_16x16x32_bf16 v[120:123], a[240:243], a[196:199], v[120:123]\n"
        "v_exp_f32 v105, v105\n"
        "v_mfma_f32_16x16x32_bf16 v[120:123], a[236:239], a[192:195], v[120:123]\n"
        "v_exp_f32 v106, v106\n"
        "v_mfma_f32_16x16x32_bf16 v[108:111], v[212:215], v[196:199], v[44:47]\n"
        "v_exp_f32 v107, v107\n"
        "v_mfma_f32_16x16x32_bf16 v[108:111], v[228:231], v[204:207], v[108:111]\n"
        "v_cvt_pk_bf16_f32 v78, v104, v105\n"
        "v_cvt_pk_bf16_f32 v79, v106, v107\n"
        "v_mfma_f32_16x16x32_bf16 v[112:115], a[0:3], a[236:239], 0\n"
        "s_nop 0\n"
        "v_cvt_pk_bf16_f32 v150, v120, v121\n"
        "v_cvt_pk_bf16_f32 v151, v122, v123\n"
        "v_mfma_f32_16x16x32_bf16 v[112:115], a[4:7], a[240:243], v[112:115]\n"
        "v_mfma_f32_16x16x32_bf16 v[112:115], a[8:11], a[244:247], v[112:115]\n"
        "v_exp_f32 v108, v108\n"
        "v_mfma_f32_16x16x32_bf16 v[112:115], a[12:15], a[248:251], v[112:115]\n"
        "v_exp_f32 v109, v109\n"
        "v_mfma_f32_16x16x32_bf16 v[112:115], a[16:19], a[216:219], v[112:115]\n"
        "v_exp_f32 v110, v110\n"
        "v_mfma_f32_16x16x32_bf16 v[112:115], a[20:23], a[220:223], v[112:115]\n"
        "v_exp_f32 v111, v111\n"
        "v_smfmac_f32_16x16x64_bf16 v[96:99], a[252:255], v[64:71], v237\n"
        "v_cvt_pk_bf16_f32 v86, v108, v109\n"
        "v_mfma_f32_16x16x32_bf16 v[100:103], v[132:135], v[68:71], 0\n"
        "v_mov_b64 v[124:125], 0\n"
        "v_cvt_pk_bf16_f32 v87, v110, v111\n"
        "v_mfma_f32_16x16x32_bf16 v[100:103], v[128:131], v[64:67], v[100:103]\n"
        "s_nop 0\n"
        "v_cvt_pk_bf16_f32 v216, v112, v113\n"
        "v_cvt_pk_bf16_f32 v217, v114, v115\n"
        "v_mfma_f32_16x16x32_bf16 v[104:107], v[136:139], v[64:67], 0\n"
        "ds_read_b128 v[222:225], v251 offset:4608\n"
        "v_add_f32 v97, v96, v97\n"
        "v_mfma_f32_16x16x32_bf16 v[104:107], v[140:143], v[68:71], v[104:107]\n"
        "v_rcp_f32 v96, v97\n"
        "v_mfma_f32_16x16x32_bf16 v[108:111], v[148:151], v[68:71], 0\n"
        "v_mul_f32 v100, v96, v100\n"
        "v_mul_f32 v101, v96, v101\n"
        "v_mfma_f32_16x16x32_bf16 v[108:111], v[144:147], v[64:67], v[108:111]\n"
        "v_mul_f32 v102, v96, v102\n"
        "v_mul_f32 v103, v96, v103\n"
        "v_mfma_f32_16x16x32_bf16 v[116:119], a[44:47], a[220:223], 0\n"
        "v_cvt_pk_bf16_f32 v100, v100, v101\n"
        "v_cvt_pk_bf16_f32 v101, v102, v103\n"
        "v_mfma_f32_16x16x32_bf16 v[116:119], a[40:43], a[216:219], v[116:119]\n"
        "v_mul_f32 v104, v96, v104\n"
        "v_mul_f32 v105, v96, v105\n"
        "v_mfma_f32_16x16x32_bf16 v[116:119], a[36:39], a[248:251], v[116:119]\n"
        "v_mul_f32 v106, v96, v106\n"
        "v_mul_f32 v107, v96, v107\n"
        "v_mfma_f32_16x16x32_bf16 v[116:119], a[32:35], a[244:247], v[116:119]\n"
        "v_cvt_pk_bf16_f32 v102, v104, v105\n"
        "v_cvt_pk_bf16_f32 v103, v106, v107\n"
        "v_mfma_f32_16x16x32_bf16 v[116:119], a[28:31], a[240:243], v[116:119]\n"
        "v_mul_f32 v108, v96, v108\n"
        "v_mul_f32 v109, v96, v109\n"
        "v_mfma_f32_16x16x32_bf16 v[116:119], a[24:27], a[236:239], v[116:119]\n"
        "v_mul_f32 v110, v96, v110\n"
        "v_mul_f32 v111, v96, v111\n"
        "v_smfmac_f32_16x16x64_bf16 v[124:127], a[252:255], v[72:79], v237\n"
        "v_cvt_pk_bf16_f32 v108, v108, v109\n"
        "v_mfma_f32_16x16x32_bf16 v[64:67], v[132:135], v[76:79], 0\n"
        "ds_write_b32 v245, v108 offset:4096\n"
        "v_mfma_f32_16x16x32_bf16 v[64:67], v[128:131], v[72:75], v[64:67]\n"
        "ds_read_b128 v[156:159], v247 offset:0\n"
        "v_cvt_pk_bf16_f32 v218, v116, v117\n"
        "v_mfma_f32_16x16x32_bf16 v[68:71], v[136:139], v[72:75], 0\n"
        "ds_read_b128 v[160:163], v247 offset:4096\n"
        "v_cvt_pk_bf16_f32 v219, v118, v119\n"
        "v_mfma_f32_16x16x32_bf16 v[68:71], v[140:143], v[76:79], v[68:71]\n"
        "ds_read_b128 v[164:167], v247 offset:4352\n"
        "v_cvt_pk_bf16_f32 v109, v110, v111\n"
        "v_mfma_f32_16x16x32_bf16 v[120:123], v[148:151], v[76:79], 0\n"
        "ds_write_b32 v245, v109 offset:5120\n"
        "v_mfma_f32_16x16x32_bf16 v[120:123], v[144:147], v[72:75], v[120:123]\n"
        "v_max_u32 v236, v96, v97\n"
        "v_mov_b64 v[116:117], 0\n"
        "v_mfma_f32_16x16x32_bf16 v[112:115], a[48:51], a[236:239], 0\n"
        "v_add_f32 v125, v124, v125\n"
        "v_mfma_f32_16x16x32_bf16 v[112:115], a[52:55], a[240:243], v[112:115]\n"
        "v_rcp_f32 v124, v125\n"
        "v_mfma_f32_16x16x32_bf16 v[112:115], a[56:59], a[244:247], v[112:115]\n"
        "v_mul_f32 v64, v124, v64\n"
        "v_mul_f32 v65, v124, v65\n"
        "v_mfma_f32_16x16x32_bf16 v[112:115], a[60:63], a[248:251], v[112:115]\n"
        "v_mul_f32 v66, v124, v66\n"
        "v_mul_f32 v67, v124, v67\n"
        "v_mfma_f32_16x16x32_bf16 v[112:115], a[64:67], a[216:219], v[112:115]\n"
        "v_cvt_pk_bf16_f32 v64, v64, v65\n"
        "v_cvt_pk_bf16_f32 v65, v66, v67\n"
        "v_mfma_f32_16x16x32_bf16 v[112:115], a[68:71], a[220:223], v[112:115]\n"
        "ds_read_b128 a[216:219], v248 offset:0\n"
        "v_mul_f32 v68, v124, v68\n"
        "v_smfmac_f32_16x16x64_bf16 v[116:119], a[252:255], v[80:87], v237\n"
        "ds_read_b128 a[220:223], v248 offset:3072\n"
        "v_mfma_f32_16x16x32_bf16 v[72:75], v[132:135], v[84:87], 0\n"
        "v_mul_f32 v69, v124, v69\n"
        "v_mul_f32 v70, v124, v70\n"
        "v_mfma_f32_16x16x32_bf16 v[72:75], v[128:131], v[80:83], v[72:75]\n"
        "v_cvt_pk_bf16_f32 v220, v112, v113\n"
        "v_cvt_pk_bf16_f32 v221, v114, v115\n"
        "v_mfma_f32_16x16x32_bf16 v[76:79], v[136:139], v[80:83], 0\n"
        "ds_read_b128 a[224:227], v248 offset:32\n"
        "v_mov_b64 v[226:227], v[220:221]\n"
        "v_mfma_f32_16x16x32_bf16 v[76:79], v[140:143], v[84:87], v[76:79]\n"
        "ds_read_b128 a[228:231], v248 offset:3104\n"
        "v_mul_f32 v71, v124, v71\n"
        "v_mfma_f32_16x16x32_bf16 v[96:99], v[148:151], v[84:87], 0\n"
        "v_cvt_pk_bf16_f32 v66, v68, v69\n"
        "v_cvt_pk_bf16_f32 v67, v70, v71\n"
        "v_mfma_f32_16x16x32_bf16 v[96:99], v[144:147], v[80:83], v[96:99]\n"
        "v_mul_f32 v120, v124, v120\n"
        "v_mul_f32 v121, v124, v121\n"
        "v_mfma_f32_16x16x32_bf16 v[104:107], v[152:155], v[216:219], v[48:51]\n"
        "s_waitcnt lgkmcnt(2)\n"
        "ds_read_b128 v[152:155], v247 offset:256\n"
        "v_mul_f32 v122, v124, v122\n"
        "v_mfma_f32_16x16x32_bf16 v[104:107], v[168:171], v[220:223], v[104:107]\n"
        "v_mul_f32 v123, v124, v123\n"
        "v_cvt_pk_bf16_f32 v120, v120, v121\n"
        "v_mfma_f32_16x16x32_bf16 v[108:111], v[172:175], v[216:219], v[52:55]\n"
        "ds_write_b32 v245, v120 offset:4352\n"
        "v_mfma_f32_16x16x32_bf16 v[108:111], v[188:191], v[224:227], v[108:111]\n"
        "ds_read_b128 v[222:225], v252 offset:4608\n"
        "v_cvt_pk_bf16_f32 v121, v122, v123\n"
        "v_mfma_f32_16x16x32_bf16 v[176:179], v[156:159], a[216:219], 0\n"
        "ds_write_b32 v245, v121 offset:5376\n"
        "v_mfma_f32_16x16x32_bf16 v[176:179], v[160:163], a[220:223], v[176:179]\n"
        "ds_read_b128 a[232:235], v254 offset:0\n"
        "v_max3_u32 v236, v236, v124, v125\n"
        "v_mfma_f32_16x16x32_bf16 v[196:199], v[164:167], a[220:223], 0\n"
        "s_waitcnt lgkmcnt(4)\n"
        "ds_read_b128 a[220:223], v254 offset:3072\n"
        "v_add_f32 v117, v116, v117\n"
        "v_mfma_f32_16x16x32_bf16 v[196:199], v[152:155], a[216:219], v[196:199]\n"
        "v_rcp_f32 v116, v117\n"
        "v_mfma_f32_16x16x32_bf16 v[200:203], v[152:155], a[224:227], 0\n"
        "ds_read_b128 a[216:219], v254 offset:32\n"
        "v_mul_f32 v72, v116, v72\n"
        "v_mfma_f32_16x16x32_bf16 v[200:203], v[164:167], a[228:231], v[200:203]\n"
        "v_exp_f32 v104, v104\n"
        "v_mfma_f32_16x16x32_bf16 v[180:183], v[160:163], a[228:231], 0\n"
        "ds_read_b128 a[228:231], v254 offset:3104\n"
        "v_mul_f32 v73, v116, v73\n"
        "v_mfma_f32_16x16x32_bf16 v[180:183], v[156:159], a[224:227], v[180:183]\n"
        "ds_read_b128 a[236:239], v247 offset:8192\n"
        "v_mul_f32 v74, v116, v74\n"
        "v_mfma_f32_16x16x32_bf16 v[112:115], v[192:195], v[216:219], v[56:59]\n"
        "s_waitcnt lgkmcnt(6)\n"
        "ds_read_b128 a[224:227], v248 offset:16\n"
        "v_mul_f32 v75, v116, v75\n"
        "v_mfma_f32_16x16x32_bf16 v[112:115], v[208:211], v[220:223], v[112:115]\n"
        "ds_read_b128 a[240:243], v247 offset:12288\n"
        "v_cvt_pk_bf16_f32 v72, v72, v73\n"
        "v_mfma_f32_16x16x32_bf16 v[68:71], v[212:215], v[216:219], v[60:63]\n"
        "ds_read_b128 a[244:247], v247 offset:12544\n"
        "v_cvt_pk_bf16_f32 v73, v74, v75\n"
        "v_mfma_f32_16x16x32_bf16 v[68:71], v[228:231], v[224:227], v[68:71]\n"
        "s_waitcnt lgkmcnt(4)\n"
        "ds_read_b128 a[248:251], v247 offset:8448\n"
        "v_mul_f32 v76, v116, v76\n"
        "v_mfma_f32_16x16x32_bf16 v[184:187], v[156:159], a[232:235], 0\n"
        "v_mul_f32 v77, v116, v77\n"
        "v_mul_f32 v78, v116, v78\n"
        "v_mfma_f32_16x16x32_bf16 v[184:187], v[160:163], a[220:223], v[184:187]\n"
        "v_mul_f32 v79, v116, v79\n"
        "v_cvt_pk_bf16_f32 v74, v76, v77\n"
        "v_mfma_f32_16x16x32_bf16 v[204:207], v[164:167], a[220:223], 0\n"
        "ds_read_b128 a[220:223], v248 offset:3088\n"
        "v_cvt_pk_bf16_f32 v75, v78, v79\n"
        "v_mfma_f32_16x16x32_bf16 v[204:207], v[152:155], a[232:235], v[204:207]\n"
        "ds_read_b128 a[232:235], v254 offset:16\n"
        "v_mul_f32 v96, v116, v96\n"
        "v_mfma_f32_16x16x32_bf16 v[192:195], v[152:155], a[216:219], 0\n"
        "v_mul_f32 v97, v116, v97\n"
        "v_mul_f32 v98, v116, v98\n"
        "v_mfma_f32_16x16x32_bf16 v[192:195], v[164:167], a[228:231], v[192:195]\n"
        "v_mul_f32 v99, v116, v99\n"
        "v_cvt_pk_bf16_f32 v96, v96, v97\n"
        "v_mfma_f32_16x16x32_bf16 v[172:175], v[160:163], a[228:231], 0\n"
        "ds_read_b128 a[228:231], v254 offset:3088\n"
        "v_cvt_pk_bf16_f32 v97, v98, v99\n"
        "v_mfma_f32_16x16x32_bf16 v[172:175], v[156:159], a[216:219], v[172:175]\n"
        "s_waitcnt lgkmcnt(2)\n"
        "ds_read_b128 a[216:219], v248 offset:6144\n"
        "v_max3_u32 v236, v236, v116, v117\n"
        "v_mfma_f32_16x16x32_bf16 v[212:215], v[156:159], a[224:227], 0\n"
        "v_exp_f32 v105, v105\n"
        "v_mfma_f32_16x16x32_bf16 v[212:215], v[160:163], a[220:223], v[212:215]\n"
        "v_exp_f32 v106, v106\n"
        "v_mfma_f32_16x16x32_bf16 v[216:219], v[164:167], a[220:223], 0\n"
        "ds_read_b128 a[220:223], v248 offset:9216\n"
        "v_cvt_pk_bf16_f32 v88, v104, v105\n"
        "v_mfma_f32_16x16x32_bf16 v[216:219], v[152:155], a[224:227], v[216:219]\n"
        "s_waitcnt lgkmcnt(2)\n"
        "ds_read_b128 a[224:227], v248 offset:6176\n"
        "v_mfma_f32_16x16x32_bf16 v[224:227], v[152:155], a[232:235], 0\n"
        "v_exp_f32 v108, v108\n"
        "v_mfma_f32_16x16x32_bf16 v[224:227], v[164:167], a[228:231], v[224:227]\n"
        "v_exp_f32 v107, v107\n"
        "v_mfma_f32_16x16x32_bf16 v[220:223], v[160:163], a[228:231], 0\n"
        "ds_read_b128 a[228:231], v248 offset:9248\n"
        "v_cvt_pk_bf16_f32 v89, v106, v107\n"
        "v_mfma_f32_16x16x32_bf16 v[220:223], v[156:159], a[232:235], v[220:223]\n"
        "s_waitcnt lgkmcnt(2)\n"
        "ds_read_b128 a[232:235], v254 offset:6144\n"
        "v_mfma_f32_16x16x32_bf16 v[176:179], a[236:239], a[216:219], v[176:179]\n"
        "v_exp_f32 v109, v109\n"
        "v_mfma_f32_16x16x32_bf16 v[176:179], a[240:243], a[220:223], v[176:179]\n"
        "v_exp_f32 v110, v110\n"
        "v_mfma_f32_16x16x32_bf16 v[196:199], a[244:247], a[220:223], v[196:199]\n"
        "ds_read_b128 a[220:223], v254 offset:9216\n"
        "v_cvt_pk_bf16_f32 v90, v108, v109\n"
        "v_mfma_f32_16x16x32_bf16 v[196:199], a[248:251], a[216:219], v[196:199]\n"
        "s_waitcnt lgkmcnt(2)\n"
        "ds_read_b128 a[216:219], v254 offset:6176\n"
        "v_mfma_f32_16x16x32_bf16 v[200:203], a[248:251], a[224:227], v[200:203]\n"
        "ds_read_b128 v[156:159], v247 offset:16384\n"
        "v_mfma_f32_16x16x32_bf16 v[200:203], a[244:247], a[228:231], v[200:203]\n"
        "ds_read_b128 v[160:163], v247 offset:20480\n"
        "v_mfma_f32_16x16x32_bf16 v[180:183], a[240:243], a[228:231], v[180:183]\n"
        "ds_read_b128 a[228:231], v254 offset:9248\n"
        "v_mfma_f32_16x16x32_bf16 v[180:183], a[236:239], a[224:227], v[180:183]\n"
        "s_waitcnt lgkmcnt(4)\n"
        "ds_read_b128 a[224:227], v248 offset:6160\n"
        "v_mfma_f32_16x16x32_bf16 v[184:187], a[236:239], a[232:235], v[184:187]\n"
        "ds_read_b128 v[164:167], v247 offset:20736\n"
        "v_mfma_f32_16x16x32_bf16 v[184:187], a[240:243], a[220:223], v[184:187]\n"
        "ds_read_b128 v[152:155], v247 offset:16640\n"
        "v_mfma_f32_16x16x32_bf16 v[204:207], a[244:247], a[220:223], v[204:207]\n"
        "ds_read_b128 a[220:223], v248 offset:9232\n"
        "v_mfma_f32_16x16x32_bf16 v[204:207], a[248:251], a[232:235], v[204:207]\n"
        "s_waitcnt lgkmcnt(4)\n"
        "ds_read_b128 a[232:235], v254 offset:6160\n"
        "v_mfma_f32_16x16x32_bf16 v[192:195], a[248:251], a[216:219], v[192:195]\n"
        "v_exp_f32 v111, v111\n"
        "v_mfma_f32_16x16x32_bf16 v[192:195], a[244:247], a[228:231], v[192:195]\n"
        "v_cvt_pk_bf16_f32 v91, v110, v111\n"
        "v_mfma_f32_16x16x32_bf16 v[172:175], a[240:243], a[228:231], v[172:175]\n"
        "ds_read_b128 a[228:231], v254 offset:9232\n"
        "v_mfma_f32_16x16x32_bf16 v[172:175], a[236:239], a[216:219], v[172:175]\n"
        "s_waitcnt lgkmcnt(2)\n"
        "ds_read_b128 a[216:219], v248 offset:12288\n"
        "v_mfma_f32_16x16x32_bf16 v[212:215], a[236:239], a[224:227], v[212:215]\n"
        "v_exp_f32 v112, v112\n"
        "v_mfma_f32_16x16x32_bf16 v[212:215], a[240:243], a[220:223], v[212:215]\n"
        "v_exp_f32 v113, v113\n"
        "v_mfma_f32_16x16x32_bf16 v[216:219], a[244:247], a[220:223], v[216:219]\n"
        "ds_read_b128 a[220:223], v248 offset:15360\n"
        "v_cvt_pk_bf16_f32 v92, v112, v113\n"
        "v_mfma_f32_16x16x32_bf16 v[216:219], a[248:251], a[224:227], v[216:219]\n"
        "s_waitcnt lgkmcnt(2)\n"
        "ds_read_b128 a[224:227], v248 offset:12320\n"
        "v_mfma_f32_16x16x32_bf16 v[224:227], a[248:251], a[232:235], v[224:227]\n"
        "v_exp_f32 v114, v114\n"
        "v_mfma_f32_16x16x32_bf16 v[224:227], a[244:247], a[228:231], v[224:227]\n"
        "v_exp_f32 v68, v68\n"
        "v_mfma_f32_16x16x32_bf16 v[220:223], a[240:243], a[228:231], v[220:223]\n"
        "ds_read_b128 a[228:231], v248 offset:15392\n"
        "v_mfma_f32_16x16x32_bf16 v[220:223], a[236:239], a[232:235], v[220:223]\n"
        "s_waitcnt lgkmcnt(2)\n"
        "ds_read_b128 a[232:235], v254 offset:12288\n"
        "v_mfma_f32_16x16x32_bf16 v[176:179], v[156:159], a[216:219], v[176:179]\n"
        "v_exp_f32 v115, v115\n"
        "v_mfma_f32_16x16x32_bf16 v[176:179], v[160:163], a[220:223], v[176:179]\n"
        "v_exp_f32 v69, v69\n"
        "v_mfma_f32_16x16x32_bf16 v[196:199], v[164:167], a[220:223], v[196:199]\n"
        "ds_read_b128 a[220:223], v254 offset:15360\n"
        "v_cvt_pk_bf16_f32 v93, v114, v115\n"
        "v_mfma_f32_16x16x32_bf16 v[196:199], v[152:155], a[216:219], v[196:199]\n"
        "s_waitcnt lgkmcnt(2)\n"
        "ds_read_b128 a[216:219], v254 offset:12320\n"
        "v_cvt_pk_bf16_f32 v94, v68, v69\n"
        "v_mfma_f32_16x16x32_bf16 v[200:203], v[152:155], a[224:227], v[200:203]\n"
        "v_exp_f32 v70, v70\n"
        "v_mfma_f32_16x16x32_bf16 v[200:203], v[164:167], a[228:231], v[200:203]\n"
        "s_waitcnt vmcnt(0)\n"
        "v_exp_f32 v71, v71\n"
        "v_mfma_f32_16x16x32_bf16 v[180:183], v[160:163], a[228:231], v[180:183]\n"
        "s_barrier\n"
        "ds_read_b128 a[228:231], v254 offset:15392\n"
        "v_cvt_pk_bf16_f32 v95, v70, v71\n"
        "v_mfma_f32_16x16x32_bf16 v[180:183], v[156:159], a[224:227], v[180:183]\n"
        "s_waitcnt lgkmcnt(2)\n"
        "ds_read_b128 a[224:227], v248 offset:12304\n"
        "v_mfma_f32_16x16x32_bf16 v[184:187], v[156:159], a[232:235], v[184:187]\n"
        "v_mfma_f32_16x16x32_bf16 v[184:187], v[160:163], a[220:223], v[184:187]\n"
        "v_mfma_f32_16x16x32_bf16 v[204:207], v[164:167], a[220:223], v[204:207]\n"
        "ds_read_b128 a[220:223], v248 offset:15376\n"
        "v_mfma_f32_16x16x32_bf16 v[204:207], v[152:155], a[232:235], v[204:207]\n"
        "s_waitcnt lgkmcnt(2)\n"
        "ds_read_b128 a[232:235], v254 offset:12304\n"
        "v_mfma_f32_16x16x32_bf16 v[192:195], v[152:155], a[216:219], v[192:195]\n"
        "ds_read_b128 a[236:239], v240 offset:0\n"
        "v_mfma_f32_16x16x32_bf16 v[192:195], v[164:167], a[228:231], v[192:195]\n"
        "ds_read_b128 a[240:243], v241 offset:0\n"
        "v_mfma_f32_16x16x32_bf16 v[172:175], v[160:163], a[228:231], v[172:175]\n"
        "ds_read_b128 a[228:231], v254 offset:15376\n"
        "v_mfma_f32_16x16x32_bf16 v[172:175], v[156:159], a[216:219], v[172:175]\n"
        "s_waitcnt lgkmcnt(4)\n"
        "ds_read_b128 a[244:247], v240 offset:128\n"
        "v_mfma_f32_16x16x32_bf16 v[212:215], v[156:159], a[224:227], v[212:215]\n"
        "ds_read_b128 a[248:251], v241 offset:128\n"
        "v_mfma_f32_16x16x32_bf16 v[212:215], v[160:163], a[220:223], v[212:215]\n"
        "ds_read_b128 a[216:219], v240 offset:256\n"
        "v_mfma_f32_16x16x32_bf16 v[216:219], v[164:167], a[220:223], v[216:219]\n"
        "ds_read_b128 a[220:223], v241 offset:256\n"
        "v_mfma_f32_16x16x32_bf16 v[216:219], v[152:155], a[224:227], v[216:219]\n"
        "s_waitcnt lgkmcnt(4)\n"
        "v_mfma_f32_16x16x32_bf16 v[224:227], v[152:155], a[232:235], v[224:227]\n"
        "v_mfma_f32_16x16x32_bf16 v[224:227], v[164:167], a[228:231], v[224:227]\n"
        "ds_write_b32 v245, v96 offset:4608\n"
        "v_mfma_f32_16x16x32_bf16 v[220:223], v[160:163], a[228:231], v[220:223]\n"
        "v_mfma_f32_16x16x32_bf16 v[220:223], v[156:159], a[232:235], v[220:223]\n"
        "ds_write_b32 v245, v97 offset:5632\n"
        "v_mfma_f32_16x16x32_bf16 v[112:115], a[72:75], a[236:239], 0\n"
        "v_mov_b64 v[124:125], 0\n"
        "v_cvt_pk_bf16_f32 v232, v176, v180\n"
        "v_mfma_f32_16x16x32_bf16 v[112:115], a[76:79], a[240:243], v[112:115]\n"
        "s_waitcnt lgkmcnt(2)\n"
        "v_cvt_pk_bf16_f32 v233, v184, v172\n"
        "v_cvt_pk_bf16_f32 v234, v177, v181\n"
        "v_mfma_f32_16x16x32_bf16 v[112:115], a[80:83], a[244:247], v[112:115]\n"
        "buffer_store_dwordx2 v[232:233], v250, s[56:59], 0 offen offset:0 sc1\n"
        "v_cvt_pk_bf16_f32 v235, v185, v173\n"
        "v_mfma_f32_16x16x32_bf16 v[112:115], a[84:87], a[248:251], v[112:115]\n"
        "v_cvt_pk_bf16_f32 v232, v178, v182\n"
        "v_cvt_pk_bf16_f32 v233, v186, v174\n"
        "v_mfma_f32_16x16x32_bf16 v[112:115], a[88:91], a[216:219], v[112:115]\n"
        "v_mfma_f32_16x16x32_bf16 v[112:115], a[92:95], a[220:223], v[112:115]\n"
        "ds_write_b32 v245, v100 offset:0\n"
        "v_smfmac_f32_16x16x64_bf16 v[124:127], a[252:255], v[88:95], v237\n"
        "s_nop 1\n"
        "v_mfma_f32_16x16x32_bf16 v[80:83], v[132:135], v[92:95], 0\n"
        "buffer_store_dwordx2 v[234:235], v250, s[56:59], 0 offen offset:384 sc1\n"
        "s_nop 0\n"
        "v_mfma_f32_16x16x32_bf16 v[80:83], v[128:131], v[88:91], v[80:83]\n"
        "s_add_u32 s41, s30, s40\n"
        "v_cvt_pk_bf16_f32 v152, v112, v113\n"
        "v_cvt_pk_bf16_f32 v153, v114, v115\n"
        "v_mfma_f32_16x16x32_bf16 v[84:87], v[136:139], v[88:91], 0\n"
        "v_cvt_pk_bf16_f32 v234, v179, v183\n"
        "v_cvt_pk_bf16_f32 v235, v187, v175\n"
        "v_mfma_f32_16x16x32_bf16 v[84:87], v[140:143], v[92:95], v[84:87]\n"
        "v_add_f32 v125, v124, v125\n"
        "v_mfma_f32_16x16x32_bf16 v[96:99], v[148:151], v[92:95], 0\n"
        "buffer_store_dwordx2 v[232:233], v250, s[56:59], 0 offen offset:768 sc1\n"
        "v_mfma_f32_16x16x32_bf16 v[96:99], v[144:147], v[88:91], v[96:99]\n"
        "s_add_u32 s35, s47, 786432\n"
        "v_rcp_f32 v124, v125\n"
        "v_mfma_f32_16x16x32_bf16 v[116:119], a[116:119], a[220:223], 0\n"
        "s_sub_u32 s46, s35, 100663296\n"
        "v_mul_f32 v80, v124, v80\n"
        "v_mul_f32 v81, v124, v81\n"
        "v_mfma_f32_16x16x32_bf16 v[116:119], a[112:115], a[216:219], v[116:119]\n"
        "s_cmp_lt_u32 s46, 786432\n"
        "v_mul_f32 v82, v124, v82\n"
        "v_mul_f32 v83, v124, v83\n"
        "v_mfma_f32_16x16x32_bf16 v[116:119], a[108:111], a[248:251], v[116:119]\n"
        "buffer_store_dwordx2 v[234:235], v250, s[56:59], 0 offen offset:1152 sc1\n"
        "v_cvt_pk_bf16_f32 v80, v80, v81\n"
        "v_mfma_f32_16x16x32_bf16 v[116:119], a[104:107], a[244:247], v[116:119]\n"
        "s_cselect_b32 s35, s46, s35\n"
        "v_cvt_pk_bf16_f32 v81, v82, v83\n"
        "v_cvt_pk_bf16_f32 v232, v196, v200\n"
        "v_mfma_f32_16x16x32_bf16 v[116:119], a[100:103], a[240:243], v[116:119]\n"
        "v_cvt_pk_bf16_f32 v233, v204, v192\n"
        "v_mul_f32 v84, v124, v84\n"
        "v_mfma_f32_16x16x32_bf16 v[116:119], a[96:99], a[236:239], v[116:119]\n"
        "v_mul_f32 v85, v124, v85\n"
        "v_mul_f32 v86, v124, v86\n"
        "v_mfma_f32_16x16x32_bf16 v[120:123], a[120:123], a[236:239], 0\n"
        "ds_write_b32 v245, v101 offset:1024\n"
        "v_mfma_f32_16x16x32_bf16 v[120:123], a[124:127], a[240:243], v[120:123]\n"
        "buffer_store_dwordx2 v[232:233], v250, s[60:63], 0 offen offset:0 sc1\n"
        "v_mul_f32 v87, v124, v87\n"
        "v_mfma_f32_16x16x32_bf16 v[120:123], a[128:131], a[244:247], v[120:123]\n"
        "v_cvt_pk_bf16_f32 v82, v84, v85\n"
        "v_cvt_pk_bf16_f32 v83, v86, v87\n"
        "v_mfma_f32_16x16x32_bf16 v[120:123], a[132:135], a[248:251], v[120:123]\n"
        "v_mul_f32 v96, v124, v96\n"
        "v_mul_f32 v97, v124, v97\n"
        "v_mfma_f32_16x16x32_bf16 v[120:123], a[136:139], a[216:219], v[120:123]\n"
        "v_mul_f32 v98, v124, v98\n"
        "v_mul_f32 v99, v124, v99\n"
        "v_mfma_f32_16x16x32_bf16 v[120:123], a[140:143], a[220:223], v[120:123]\n"
        "v_cvt_pk_bf16_f32 v234, v197, v201\n"
        "v_cvt_pk_bf16_f32 v235, v205, v193\n"
        "v_mfma_f32_16x16x32_bf16 v[112:115], a[20:23], a[220:223], 0\n"
        "buffer_store_dwordx2 v[234:235], v250, s[60:63], 0 offen offset:384 sc1\n"
        "v_cvt_pk_bf16_f32 v154, v116, v117\n"
        "v_mfma_f32_16x16x32_bf16 v[112:115], a[16:19], a[216:219], v[112:115]\n"
        "v_cvt_pk_bf16_f32 v155, v118, v119\n"
        "v_cvt_pk_bf16_f32 v96, v96, v97\n"
        "v_mfma_f32_16x16x32_bf16 v[112:115], a[12:15], a[248:251], v[112:115]\n"
        "ds_write_b32 v245, v96 offset:4864\n"
        "v_mfma_f32_16x16x32_bf16 v[112:115], a[8:11], a[244:247], v[112:115]\n"
        "v_cvt_pk_bf16_f32 v97, v98, v99\n"
        "v_max3_u32 v236, v236, v124, v125\n"
        "v_mfma_f32_16x16x32_bf16 v[112:115], a[4:7], a[240:243], v[112:115]\n"
        "v_cvt_pk_bf16_f32 v232, v198, v202\n"
        "v_cvt_pk_bf16_f32 v233, v206, v194\n"
        "v_mfma_f32_16x16x32_bf16 v[112:115], a[0:3], a[236:239], v[112:115]\n"
        "buffer_store_dwordx2 v[232:233], v250, s[60:63], 0 offen offset:768 sc1\n"
        "v_cmp_lt_u32 vcc, s24, v236\n"
        "v_mfma_f32_16x16x32_bf16 v[116:119], a[24:27], a[236:239], 0\n"
        "v_cvt_pk_bf16_f32 v168, v120, v121\n"
        "v_cvt_pk_bf16_f32 v169, v122, v123\n"
        "v_mfma_f32_16x16x32_bf16 v[116:119], a[28:31], a[240:243], v[116:119]\n"
        "s_cmp_lg_u64 vcc, 0\n"
        "s_addc_u32 s25, s25, s25\n"
        "v_cvt_pk_bf16_f32 v234, v199, v203\n"
        "v_mfma_f32_16x16x32_bf16 v[116:119], a[32:35], a[244:247], v[116:119]\n"
        "v_cvt_pk_bf16_f32 v235, v207, v195\n"
        "v_cvt_pk_bf16_f32 v156, v112, v113\n"
        "v_mfma_f32_16x16x32_bf16 v[116:119], a[36:39], a[248:251], v[116:119]\n"
        "buffer_store_dwordx2 v[234:235], v250, s[60:63], 0 offen offset:1152 sc1\n"
        "v_cvt_pk_bf16_f32 v157, v114, v115\n"
        "v_mfma_f32_16x16x32_bf16 v[116:119], a[40:43], a[216:219], v[116:119]\n"
        "v_cvt_pk_bf16_f32 v232, v212, v220\n"
        "v_cvt_pk_bf16_f32 v234, v213, v221\n"
        "v_mfma_f32_16x16x32_bf16 v[116:119], a[44:47], a[220:223], v[116:119]\n"
        "ds_write_b32 v245, v102 offset:2048\n"
        "v_mfma_f32_16x16x32_bf16 v[120:123], a[68:71], a[220:223], 0\n"
        "v_xor_b32 v251, 0x2000, v251\n"
        "v_xor_b32 v252, 0x2000, v252\n"
        "v_mfma_f32_16x16x32_bf16 v[120:123], a[64:67], a[216:219], v[120:123]\n"
        "buffer_store_dword v232, v238, s[56:59], 0 offen offset:0 sc1\n"
        "v_mfma_f32_16x16x32_bf16 v[120:123], a[60:63], a[248:251], v[120:123]\n"
        "ds_read_b128 a[224:227], v240 offset:1536\n"
        "v_cvt_pk_bf16_f32 v158, v116, v117\n"
        "v_mfma_f32_16x16x32_bf16 v[120:123], a[56:59], a[244:247], v[120:123]\n"
        "ds_read_b128 a[228:231], v241 offset:1536\n"
        "v_cvt_pk_bf16_f32 v159, v118, v119\n"
        "v_mfma_f32_16x16x32_bf16 v[120:123], a[52:55], a[240:243], v[120:123]\n"
        "ds_read_b128 a[232:235], v240 offset:1664\n"
        "v_cvt_pk_bf16_f32 v232, v214, v222\n"
        "v_mfma_f32_16x16x32_bf16 v[120:123], a[48:51], a[236:239], v[120:123]\n"
        "buffer_store_dword v234, v238, s[56:59], 0 offen offset:384 sc1\n"
        "v_mfma_f32_16x16x32_bf16 v[112:115], a[236:239], a[144:147], 0\n"
        "v_add_u32 v247, s45, v247\n"
        "v_cvt_pk_bf16_f32 v234, v215, v223\n"
        "v_mfma_f32_16x16x32_bf16 v[112:115], a[240:243], a[148:151], v[112:115]\n"
        "ds_write_b32 v245, v103 offset:3072\n"
        "v_mfma_f32_16x16x32_bf16 v[112:115], a[244:247], a[152:155], v[112:115]\n"
        "s_nop 0\n"
        "v_cvt_pk_bf16_f32 v160, v120, v121\n"
        "v_cvt_pk_bf16_f32 v161, v122, v123\n"
        "v_mfma_f32_16x16x32_bf16 v[112:115], a[248:251], a[156:159], v[112:115]\n"
        "v_mov_b64 v[166:167], v[160:161]\n"
        "v_mfma_f32_16x16x32_bf16 v[112:115], a[216:219], a[160:163], v[112:115]\n"
        "buffer_store_dword v232, v238, s[56:59], 0 offen offset:768 sc1\n"
        "v_mfma_f32_16x16x32_bf16 v[112:115], a[220:223], a[164:167], v[112:115]\n"
        "ds_write_b32 v245, v97 offset:5888\n"
        "v_mfma_f32_16x16x32_bf16 v[116:119], a[220:223], a[188:191], 0\n"
        "v_cvt_pk_bf16_f32 v232, v216, v224\n"
        "s_nop 0\n"
        "v_mfma_f32_16x16x32_bf16 v[116:119], a[216:219], a[184:187], v[116:119]\n"
        "s_nop 2\n"
        "v_mfma_f32_16x16x32_bf16 v[116:119], a[248:251], a[180:183], v[116:119]\n"
        "buffer_store_dword v234, v238, s[56:59], 0 offen offset:1152 sc1\n"
        "v_mfma_f32_16x16x32_bf16 v[116:119], a[244:247], a[176:179], v[116:119]\n"
        "v_cvt_pk_bf16_f32 v128, v112, v113\n"
        "v_cvt_pk_bf16_f32 v129, v114, v115\n"
        "v_mfma_f32_16x16x32_bf16 v[116:119], a[240:243], a[172:175], v[116:119]\n"
        "ds_write_b32 v245, v64 offset:256\n"
        "v_mfma_f32_16x16x32_bf16 v[116:119], a[236:239], a[168:171], v[116:119]\n"
        "v_cvt_pk_bf16_f32 v234, v217, v225\n"
        "s_nop 0\n"
        "v_mfma_f32_16x16x32_bf16 v[120:123], a[236:239], a[192:195], 0\n"
        "ds_read_b128 a[236:239], v241 offset:1664\n"
        "s_nop 0\n"
        "v_mfma_f32_16x16x32_bf16 v[120:123], a[240:243], a[196:199], v[120:123]\n"
        "buffer_store_dword v232, v238, s[60:63], 0 offen offset:0 sc1\n"
        "v_mfma_f32_16x16x32_bf16 v[120:123], a[244:247], a[200:203], v[120:123]\n"
        "ds_read_b128 a[240:243], v240 offset:1792\n"
        "ds_read_b128 a[244:247], v241 offset:1792\n"
        "v_mfma_f32_16x16x32_bf16 v[120:123], a[248:251], a[204:207], v[120:123]\n"
        "v_cvt_pk_bf16_f32 v136, v116, v117\n"
        "v_cvt_pk_bf16_f32 v137, v118, v119\n"
        "v_mfma_f32_16x16x32_bf16 v[120:123], a[216:219], a[208:211], v[120:123]\n"
        "v_cvt_pk_bf16_f32 v232, v218, v226\n"
        "v_mfma_f32_16x16x32_bf16 v[120:123], a[220:223], a[212:215], v[120:123]\n"
        "s_waitcnt lgkmcnt(4)\n"
        "buffer_store_dword v234, v238, s[60:63], 0 offen offset:384 sc1\n"
        "v_mfma_f32_16x16x32_bf16 v[112:115], a[72:75], a[224:227], 0\n"
        "s_nop 2\n"
        "v_mfma_f32_16x16x32_bf16 v[112:115], a[76:79], a[228:231], v[112:115]\n"
        "v_cvt_pk_bf16_f32 v234, v219, v227\n"
        "s_nop 0\n"
        "v_mfma_f32_16x16x32_bf16 v[112:115], a[80:83], a[232:235], v[112:115]\n"
        "s_waitcnt lgkmcnt(0)\n"
        "v_cvt_pk_bf16_f32 v144, v120, v121\n"
        "v_cvt_pk_bf16_f32 v145, v122, v123\n"
        "v_mfma_f32_16x16x32_bf16 v[112:115], a[84:87], a[236:239], v[112:115]\n"
        "ds_write_b32 v245, v65 offset:1280\n"
        "v_mfma_f32_16x16x32_bf16 v[112:115], a[88:91], a[240:243], v[112:115]\n"
        "buffer_store_dword v232, v238, s[60:63], 0 offen offset:768 sc1\n"
        "v_mfma_f32_16x16x32_bf16 v[112:115], a[92:95], a[244:247], v[112:115]\n"
        "s_nop 2\n"
        "v_mfma_f32_16x16x32_bf16 v[116:119], a[116:119], a[244:247], 0\n"
        "s_nop 2\n"
        "v_mfma_f32_16x16x32_bf16 v[116:119], a[112:115], a[240:243], v[116:119]\n"
        "v_cvt_pk_bf16_f32 v172, v112, v113\n"
        "v_cvt_pk_bf16_f32 v173, v114, v115\n"
        "v_mfma_f32_16x16x32_bf16 v[116:119], a[108:111], a[236:239], v[116:119]\n"
        "buffer_store_dword v234, v238, s[60:63], 0 offen offset:1152 sc1\n"
        "v_mfma_f32_16x16x32_bf16 v[116:119], a[104:107], a[232:235], v[116:119]\n"
        "ds_write_b32 v245, v66 offset:2304\n"
        "v_mfma_f32_16x16x32_bf16 v[116:119], a[100:103], a[228:231], v[116:119]\n"
        "v_mfma_f32_16x16x32_bf16 v[116:119], a[96:99], a[224:227], v[116:119]\n"
        "s_mov_b32 m0, s48\n"
        "s_nop 1\n"
        "v_mfma_f32_16x16x32_bf16 v[120:123], a[120:123], a[224:227], 0\n"
        "ds_read_b128 v[162:165], v251 offset:0\n"
        "s_nop 0\n"
        "v_mfma_f32_16x16x32_bf16 v[120:123], a[124:127], a[228:231], v[120:123]\n"
        "buffer_load_dwordx4 v249, s[12:15], s36 offen offset:0 lds\n"
        "v_mfma_f32_16x16x32_bf16 v[120:123], a[128:131], a[232:235], v[120:123]\n"
        "v_cvt_pk_bf16_f32 v174, v116, v117\n"
        "v_cvt_pk_bf16_f32 v175, v118, v119\n"
        "v_mfma_f32_16x16x32_bf16 v[120:123], a[132:135], a[236:239], v[120:123]\n"
        "ds_write_b32 v245, v67 offset:3328\n"
        "v_mfma_f32_16x16x32_bf16 v[120:123], a[136:139], a[240:243], v[120:123]\n"
        "ds_write_b32 v245, v72 offset:512\n"
        "v_mfma_f32_16x16x32_bf16 v[120:123], a[140:143], a[244:247], v[120:123]\n"
        "s_nop 2\n"
        "v_mfma_f32_16x16x32_bf16 v[112:115], a[20:23], a[244:247], 0\n"
        "s_nop 2\n"
        "v_mfma_f32_16x16x32_bf16 v[112:115], a[16:19], a[240:243], v[112:115]\n"
        "v_cvt_pk_bf16_f32 v190, v120, v121\n"
        "v_cvt_pk_bf16_f32 v191, v122, v123\n"
        "v_mfma_f32_16x16x32_bf16 v[112:115], a[12:15], a[236:239], v[112:115]\n"
        ".long 0xbec200ff\n"
        "46:\n"
        "s_branch 61b\n"
        "ds_write_b32 v245, v73 offset:1536\n"
        "v_mfma_f32_16x16x32_bf16 v[112:115], a[8:11], a[232:235], v[112:115]\n"
        "ds_write_b32 v245, v74 offset:2560\n"
        "v_mfma_f32_16x16x32_bf16 v[112:115], a[4:7], a[228:231], v[112:115]\n"
        "ds_read_b128 v[182:185], v251 offset:512\n"
        "v_mfma_f32_16x16x32_bf16 v[112:115], a[0:3], a[224:227], v[112:115]\n"
        "s_nop 2\n"
        "v_mfma_f32_16x16x32_bf16 v[116:119], a[24:27], a[224:227], 0\n"
        "s_nop 2\n"
        "v_mfma_f32_16x16x32_bf16 v[116:119], a[28:31], a[228:231], v[116:119]\n"
        "v_cvt_pk_bf16_f32 v176, v112, v113\n"
        "v_cvt_pk_bf16_f32 v177, v114, v115\n"
        "v_mfma_f32_16x16x32_bf16 v[116:119], a[32:35], a[232:235], v[116:119]\n"
        "ds_write_b32 v245, v75 offset:3584\n"
        "v_mfma_f32_16x16x32_bf16 v[116:119], a[36:39], a[236:239], v[116:119]\n"
        "buffer_load_dwordx4 v253, s[12:15], s36 offen offset:1024 lds\n"
        "v_mfma_f32_16x16x32_bf16 v[116:119], a[40:43], a[240:243], v[116:119]\n"
        "ds_write_b32 v245, v80 offset:768\n"
        "v_mfma_f32_16x16x32_bf16 v[116:119], a[44:47], a[244:247], v[116:119]\n"
        "s_nop 2\n"
        "v_mfma_f32_16x16x32_bf16 v[120:123], a[68:71], a[244:247], 0\n"
        "s_nop 2\n"
        "v_mfma_f32_16x16x32_bf16 v[120:123], a[64:67], a[240:243], v[120:123]\n"
        "v_cvt_pk_bf16_f32 v178, v116, v117\n"
        "v_cvt_pk_bf16_f32 v179, v118, v119\n"
        "v_mfma_f32_16x16x32_bf16 v[120:123], a[60:63], a[236:239], v[120:123]\n"
        "ds_write_b32 v245, v81 offset:1792\n"
        "v_mfma_f32_16x16x32_bf16 v[120:123], a[56:59], a[232:235], v[120:123]\n"
        "ds_write_b32 v245, v82 offset:2816\n"
        "v_mfma_f32_16x16x32_bf16 v[120:123], a[52:55], a[228:231], v[120:123]\n"
        "ds_write_b32 v245, v83 offset:3840\n"
        "v_mfma_f32_16x16x32_bf16 v[120:123], a[48:51], a[224:227], v[120:123]\n"
        "s_nop 2\n"
        "v_mfma_f32_16x16x32_bf16 v[112:115], a[224:227], a[144:147], 0\n"
        "s_nop 2\n"
        "v_mfma_f32_16x16x32_bf16 v[112:115], a[228:231], a[148:151], v[112:115]\n"
        "v_cvt_pk_bf16_f32 v180, v120, v121\n"
        "v_cvt_pk_bf16_f32 v181, v122, v123\n"
        "v_mfma_f32_16x16x32_bf16 v[112:115], a[232:235], a[152:155], v[112:115]\n"
        "v_mov_b64 v[186:187], v[180:181]\n"
        "v_mfma_f32_16x16x32_bf16 v[112:115], a[236:239], a[156:159], v[112:115]\n"
        "s_add_u32 m0, s41, 0\n"
        "v_mfma_f32_16x16x32_bf16 v[112:115], a[240:243], a[160:163], v[112:115]\n"
        "ds_read_b128 a[248:251], v240 offset:12288\n"
        "v_mfma_f32_16x16x32_bf16 v[112:115], a[244:247], a[164:167], v[112:115]\n"
        "buffer_load_dwordx4 v242, s[8:11], s47 offen lds\n"
        "v_mfma_f32_16x16x32_bf16 v[96:99], v[152:155], v[156:159], v[0:3]\n"
        "s_waitcnt lgkmcnt(2)\n"
        "ds_read_b128 a[216:219], v241 offset:12288\n"
        "s_nop 0\n"
        "v_mfma_f32_16x16x32_bf16 v[96:99], v[168:171], v[160:163], v[96:99]\n"
        "ds_read_b128 a[220:223], v240 offset:12416\n"
        "s_nop 0\n"
        "v_mfma_f32_16x16x32_bf16 v[100:103], v[172:175], v[156:159], v[4:7]\n"
        "s_sub_u32 s31, s30, s28\n"
        "v_cvt_pk_bf16_f32 v130, v112, v113\n"
        "v_cvt_pk_bf16_f32 v131, v114, v115\n"
        "v_mfma_f32_16x16x32_bf16 v[100:103], v[188:191], v[164:167], v[100:103]\n"
        "s_sub_u32 s45, s28, s29\n"
        "ds_read_b128 v[162:165], v252 offset:0\n"
        "v_mfma_f32_16x16x32_bf16 v[116:119], a[244:247], a[188:191], 0\n"
        "s_add_u32 s28, s28, s31\n"
        "v_exp_f32 v96, v96\n"
        "v_mfma_f32_16x16x32_bf16 v[116:119], a[240:243], a[184:187], v[116:119]\n"
        "s_mov_b32 s30, s29\n"
        "v_exp_f32 v97, v97\n"
        "v_mfma_f32_16x16x32_bf16 v[116:119], a[236:239], a[180:183], v[116:119]\n"
        "s_add_u32 s29, s29, s45\n"
        "v_exp_f32 v98, v98\n"
        "v_mfma_f32_16x16x32_bf16 v[116:119], a[232:235], a[176:179], v[116:119]\n"
        "v_exp_f32 v100, v100\n"
        "v_mfma_f32_16x16x32_bf16 v[116:119], a[228:231], a[172:175], v[116:119]\n"
        "v_exp_f32 v99, v99\n"
        "v_mfma_f32_16x16x32_bf16 v[116:119], a[224:227], a[168:171], v[116:119]\n"
        "v_exp_f32 v101, v101\n"
        "v_mfma_f32_16x16x32_bf16 v[104:107], v[152:155], v[176:179], v[16:19]\n"
        "v_exp_f32 v102, v102\n"
        "v_mfma_f32_16x16x32_bf16 v[104:107], v[168:171], v[180:183], v[104:107]\n"
        "s_add_u32 m0, s41, 1024\n"
        "v_cvt_pk_bf16_f32 v64, v96, v97\n"
        "v_cvt_pk_bf16_f32 v65, v98, v99\n"
        "v_mfma_f32_16x16x32_bf16 v[108:111], v[172:175], v[176:179], v[20:23]\n"
        "v_cvt_pk_bf16_f32 v138, v116, v117\n"
        "v_cvt_pk_bf16_f32 v139, v118, v119\n"
        "v_mfma_f32_16x16x32_bf16 v[108:111], v[188:191], v[184:187], v[108:111]\n"
        "buffer_load_dwordx4 v243, s[8:11], s47 offen lds\n"
        "v_mfma_f32_16x16x32_bf16 v[120:123], a[224:227], a[192:195], 0\n"
        "ds_read_b128 a[224:227], v241 offset:12416\n"
        "v_cvt_pk_bf16_f32 v66, v100, v101\n"
        "v_mfma_f32_16x16x32_bf16 v[120:123], a[228:231], a[196:199], v[120:123]\n"
        "ds_read_b128 a[228:231], v240 offset:12544\n"
        "v_mfma_f32_16x16x32_bf16 v[120:123], a[232:235], a[200:203], v[120:123]\n"
        "ds_read_b128 a[232:235], v241 offset:12544\n"
        "v_mfma_f32_16x16x32_bf16 v[120:123], a[236:239], a[204:207], v[120:123]\n"
        "ds_read_b128 v[202:205], v251 offset:4096\n"
        "v_mfma_f32_16x16x32_bf16 v[120:123], a[240:243], a[208:211], v[120:123]\n"
        "ds_read_b128 v[182:185], v252 offset:512\n"
        "v_mfma_f32_16x16x32_bf16 v[120:123], a[244:247], a[212:215], v[120:123]\n"
        "s_waitcnt lgkmcnt(4)\n"
        "v_exp_f32 v103, v103\n"
        "v_mfma_f32_16x16x32_bf16 v[112:115], a[0:3], a[248:251], 0\n"
        "v_cvt_pk_bf16_f32 v67, v102, v103\n"
        "s_nop 0\n"
        "v_mfma_f32_16x16x32_bf16 v[112:115], a[4:7], a[216:219], v[112:115]\n"
        "v_exp_f32 v104, v104\n"
        "v_mfma_f32_16x16x32_bf16 v[112:115], a[8:11], a[220:223], v[112:115]\n"
        "v_exp_f32 v105, v105\n"
        "v_mfma_f32_16x16x32_bf16 v[112:115], a[12:15], a[224:227], v[112:115]\n"
        "s_waitcnt lgkmcnt(0)\n"
        "v_exp_f32 v106, v106\n"
        "v_mfma_f32_16x16x32_bf16 v[112:115], a[16:19], a[228:231], v[112:115]\n"
        "v_cvt_pk_bf16_f32 v72, v104, v105\n"
        "v_cvt_pk_bf16_f32 v146, v120, v121\n"
        "v_mfma_f32_16x16x32_bf16 v[112:115], a[20:23], a[232:235], v[112:115]\n"
        "s_add_u32 m0, s41, 2048\n"
        "v_exp_f32 v108, v108\n"
        "v_mfma_f32_16x16x32_bf16 v[116:119], a[44:47], a[232:235], 0\n"
        "v_cvt_pk_bf16_f32 v147, v122, v123\n"
        "v_add_u32 v245, s45, v245\n"
        "v_mfma_f32_16x16x32_bf16 v[116:119], a[40:43], a[228:231], v[116:119]\n"
        "buffer_load_dwordx4 v244, s[8:11], s47 offen lds\n"
        "v_mfma_f32_16x16x32_bf16 v[116:119], a[36:39], a[224:227], v[116:119]\n"
        "v_exp_f32 v107, v107\n"
        "v_mfma_f32_16x16x32_bf16 v[116:119], a[32:35], a[220:223], v[116:119]\n"
        "v_exp_f32 v109, v109\n"
        "v_mfma_f32_16x16x32_bf16 v[116:119], a[28:31], a[216:219], v[116:119]\n"
        "v_cvt_pk_bf16_f32 v73, v106, v107\n"
        "v_cvt_pk_bf16_f32 v74, v108, v109\n"
        "v_mfma_f32_16x16x32_bf16 v[116:119], a[24:27], a[248:251], v[116:119]\n"
        "v_exp_f32 v110, v110\n"
        "v_mfma_f32_16x16x32_bf16 v[120:123], a[48:51], a[248:251], 0\n"
        "v_exp_f32 v111, v111\n"
        "v_mfma_f32_16x16x32_bf16 v[120:123], a[52:55], a[216:219], v[120:123]\n"
        "v_cvt_pk_bf16_f32 v75, v110, v111\n"
        "v_cvt_pk_bf16_f32 v196, v112, v113\n"
        "v_mfma_f32_16x16x32_bf16 v[120:123], a[56:59], a[220:223], v[120:123]\n"
        "v_cvt_pk_bf16_f32 v197, v114, v115\n"
        "v_cvt_pk_bf16_f32 v198, v116, v117\n"
        "v_mfma_f32_16x16x32_bf16 v[120:123], a[60:63], a[224:227], v[120:123]\n"
        "v_cvt_pk_bf16_f32 v199, v118, v119\n"
        "v_mfma_f32_16x16x32_bf16 v[120:123], a[64:67], a[228:231], v[120:123]\n"
        "v_mfma_f32_16x16x32_bf16 v[120:123], a[68:71], a[232:235], v[120:123]\n"
        "s_nop 2\n"
        "v_mfma_f32_16x16x32_bf16 v[112:115], a[92:95], a[232:235], 0\n"
        "s_nop 2\n"
        "v_mfma_f32_16x16x32_bf16 v[112:115], a[88:91], a[228:231], v[112:115]\n"
        "s_add_u32 m0, s41, 12288\n"
        "v_cvt_pk_bf16_f32 v200, v120, v121\n"
        "v_cvt_pk_bf16_f32 v201, v122, v123\n"
        "v_mfma_f32_16x16x32_bf16 v[112:115], a[84:87], a[224:227], v[112:115]\n"
        "v_mov_b64 v[206:207], v[200:201]\n"
        "v_mfma_f32_16x16x32_bf16 v[112:115], a[80:83], a[220:223], v[112:115]\n"
        "buffer_load_dwordx4 v242, s[8:11], s35 offen lds\n"
        "v_mfma_f32_16x16x32_bf16 v[112:115], a[76:79], a[216:219], v[112:115]\n"
        "v_mfma_f32_16x16x32_bf16 v[112:115], a[72:75], a[248:251], v[112:115]\n"
        "s_nop 2\n"
        "v_mfma_f32_16x16x32_bf16 v[96:99], v[152:155], v[196:199], v[32:35]\n"
        "s_nop 2\n"
        "v_mfma_f32_16x16x32_bf16 v[96:99], v[168:171], v[200:203], v[96:99]\n"
        "v_cvt_pk_bf16_f32 v192, v112, v113\n"
        "v_cvt_pk_bf16_f32 v193, v114, v115\n"
        "v_mfma_f32_16x16x32_bf16 v[100:103], v[172:175], v[196:199], v[36:39]\n"
        "v_mfma_f32_16x16x32_bf16 v[100:103], v[188:191], v[204:207], v[100:103]\n"
        "ds_read_b128 v[202:205], v252 offset:4096\n"
        "v_mfma_f32_16x16x32_bf16 v[116:119], a[96:99], a[248:251], 0\n"
        "s_cmp_eq_u32 s37, 100663296\n"
        "v_mfma_f32_16x16x32_bf16 v[116:119], a[100:103], a[216:219], v[116:119]\n"
        "s_cselect_b32 s46, 0, s37\n"
        "v_exp_f32 v96, v96\n"
        "v_mfma_f32_16x16x32_bf16 v[116:119], a[104:107], a[220:223], v[116:119]\n"
        "s_add_u32 s38, s46, s43\n"
        "v_exp_f32 v97, v97\n"
        "v_mfma_f32_16x16x32_bf16 v[116:119], a[108:111], a[224:227], v[116:119]\n"
        "s_add_u32 s39, s46, s44\n"
        "v_exp_f32 v98, v98\n"
        "v_mfma_f32_16x16x32_bf16 v[116:119], a[112:115], a[228:231], v[116:119]\n"
        "s_add_u32 s37, s37, 6291456\n"
        "v_exp_f32 v100, v100\n"
        "v_mfma_f32_16x16x32_bf16 v[116:119], a[116:119], a[232:235], v[116:119]\n"
        "s_add_u32 m0, s41, 13312\n"
        "v_exp_f32 v99, v99\n"
        "v_mfma_f32_16x16x32_bf16 v[120:123], a[140:143], a[232:235], 0\n"
        "v_exp_f32 v101, v101\n"
        "v_mfma_f32_16x16x32_bf16 v[120:123], a[136:139], a[228:231], v[120:123]\n"
        "buffer_load_dwordx4 v243, s[8:11], s35 offen lds\n"
        "v_mfma_f32_16x16x32_bf16 v[120:123], a[132:135], a[224:227], v[120:123]\n"
        "v_cvt_pk_bf16_f32 v80, v96, v97\n"
        "v_cvt_pk_bf16_f32 v194, v116, v117\n"
        "v_mfma_f32_16x16x32_bf16 v[120:123], a[128:131], a[220:223], v[120:123]\n"
        "s_mov_b64 s[58:59], s[18:19]\n"
        "v_cvt_pk_bf16_f32 v195, v118, v119\n"
        "v_cvt_pk_bf16_f32 v81, v98, v99\n"
        "v_mfma_f32_16x16x32_bf16 v[120:123], a[124:127], a[216:219], v[120:123]\n"
        "s_mov_b64 s[62:63], s[18:19]\n"
        "v_exp_f32 v102, v102\n"
        "v_mfma_f32_16x16x32_bf16 v[120:123], a[120:123], a[248:251], v[120:123]\n"
        "v_cvt_pk_bf16_f32 v82, v100, v101\n"
        "s_nop 0\n"
        "v_mfma_f32_16x16x32_bf16 v[112:115], a[248:251], a[144:147], 0\n"
        "v_exp_f32 v103, v103\n"
        "v_mfma_f32_16x16x32_bf16 v[112:115], a[216:219], a[148:151], v[112:115]\n"
        "v_cvt_pk_bf16_f32 v83, v102, v103\n"
        "s_nop 0\n"
        "v_mfma_f32_16x16x32_bf16 v[112:115], a[220:223], a[152:155], v[112:115]\n"
        "ds_read_b128 a[236:239], v240 offset:13824\n"
        "v_cvt_pk_bf16_f32 v208, v120, v121\n"
        "v_mfma_f32_16x16x32_bf16 v[112:115], a[224:227], a[156:159], v[112:115]\n"
        "ds_read_b128 a[240:243], v241 offset:13824\n"
        "v_cvt_pk_bf16_f32 v209, v122, v123\n"
        "v_mfma_f32_16x16x32_bf16 v[112:115], a[228:231], a[160:163], v[112:115]\n"
        "ds_read_b128 a[244:247], v240 offset:13952\n"
        "v_mfma_f32_16x16x32_bf16 v[112:115], a[232:235], a[164:167], v[112:115]\n"
        "s_nop 2\n"
        "v_mfma_f32_16x16x32_bf16 v[104:107], v[192:195], v[156:159], v[8:11]\n"
        "s_nop 2\n"
        "v_mfma_f32_16x16x32_bf16 v[104:107], v[208:211], v[160:163], v[104:107]\n"
        "s_add_u32 m0, s41, 14336\n"
        "v_cvt_pk_bf16_f32 v132, v112, v113\n"
        "v_cvt_pk_bf16_f32 v133, v114, v115\n"
        "v_mfma_f32_16x16x32_bf16 v[116:119], a[232:235], a[188:191], 0\n"
        "v_mfma_f32_16x16x32_bf16 v[116:119], a[228:231], a[184:187], v[116:119]\n"
        "buffer_load_dwordx4 v244, s[8:11], s35 offen lds\n"
        "v_mfma_f32_16x16x32_bf16 v[116:119], a[224:227], a[180:183], v[116:119]\n"
        "v_mfma_f32_16x16x32_bf16 v[116:119], a[220:223], a[176:179], v[116:119]\n"
        "v_exp_f32 v104, v104\n"
        "v_mfma_f32_16x16x32_bf16 v[116:119], a[216:219], a[172:175], v[116:119]\n"
        "v_exp_f32 v105, v105\n"
        "v_mfma_f32_16x16x32_bf16 v[116:119], a[248:251], a[168:171], v[116:119]\n"
        "v_exp_f32 v106, v106\n"
        "v_mfma_f32_16x16x32_bf16 v[108:111], v[192:195], v[176:179], v[24:27]\n"
        "v_exp_f32 v107, v107\n"
        "v_mfma_f32_16x16x32_bf16 v[108:111], v[208:211], v[180:183], v[108:111]\n"
        "v_cvt_pk_bf16_f32 v68, v104, v105\n"
        "v_cvt_pk_bf16_f32 v69, v106, v107\n"
        "v_mfma_f32_16x16x32_bf16 v[120:123], a[248:251], a[192:195], 0\n"
        "ds_read_b128 a[248:251], v241 offset:13952\n"
        "v_cvt_pk_bf16_f32 v140, v116, v117\n"
        "v_mfma_f32_16x16x32_bf16 v[120:123], a[216:219], a[196:199], v[120:123]\n"
        "ds_read_b128 a[216:219], v240 offset:14080\n"
        "v_cvt_pk_bf16_f32 v141, v118, v119\n"
        "v_mfma_f32_16x16x32_bf16 v[120:123], a[220:223], a[200:203], v[120:123]\n"
        "ds_read_b128 a[220:223], v241 offset:14080\n"
        "v_mfma_f32_16x16x32_bf16 v[120:123], a[224:227], a[204:207], v[120:123]\n"
        "v_exp_f32 v108, v108\n"
        "v_mfma_f32_16x16x32_bf16 v[120:123], a[228:231], a[208:211], v[120:123]\n"
        "v_exp_f32 v109, v109\n"
        "v_mfma_f32_16x16x32_bf16 v[120:123], a[232:235], a[212:215], v[120:123]\n"
        "v_exp_f32 v110, v110\n"
        "v_mfma_f32_16x16x32_bf16 v[96:99], v[192:195], v[196:199], v[40:43]\n"
        "s_waitcnt lgkmcnt(1)\n"
        "v_exp_f32 v111, v111\n"
        "v_mfma_f32_16x16x32_bf16 v[96:99], v[208:211], v[200:203], v[96:99]\n"
        "v_cvt_pk_bf16_f32 v76, v108, v109\n"
        "v_cvt_pk_bf16_f32 v77, v110, v111\n"
        "v_mfma_f32_16x16x32_bf16 v[112:115], a[72:75], a[236:239], 0\n"
        "s_nop 0\n"
        "v_cvt_pk_bf16_f32 v148, v120, v121\n"
        "v_cvt_pk_bf16_f32 v149, v122, v123\n"
        "v_mfma_f32_16x16x32_bf16 v[112:115], a[76:79], a[240:243], v[112:115]\n"
        "v_mfma_f32_16x16x32_bf16 v[112:115], a[80:83], a[244:247], v[112:115]\n"
        "v_exp_f32 v96, v96\n"
        "v_mfma_f32_16x16x32_bf16 v[112:115], a[84:87], a[248:251], v[112:115]\n"
        "v_exp_f32 v97, v97\n"
        "v_mfma_f32_16x16x32_bf16 v[112:115], a[88:91], a[216:219], v[112:115]\n"
        "s_waitcnt lgkmcnt(0)\n"
        "v_exp_f32 v98, v98\n"
        "v_mfma_f32_16x16x32_bf16 v[112:115], a[92:95], a[220:223], v[112:115]\n"
        "v_exp_f32 v99, v99\n"
        "v_mfma_f32_16x16x32_bf16 v[116:119], a[116:119], a[220:223], 0\n"
        "v_cvt_pk_bf16_f32 v84, v96, v97\n"
        "v_cvt_pk_bf16_f32 v85, v98, v99\n"
        "v_mfma_f32_16x16x32_bf16 v[116:119], a[112:115], a[216:219], v[116:119]\n"
        "v_add_u32 v240, s31, v240\n"
        "v_add_u32 v241, s31, v241\n"
        "v_mfma_f32_16x16x32_bf16 v[116:119], a[108:111], a[248:251], v[116:119]\n"
        "v_cvt_pk_bf16_f32 v212, v112, v113\n"
        "v_cvt_pk_bf16_f32 v213, v114, v115\n"
        "v_mfma_f32_16x16x32_bf16 v[116:119], a[104:107], a[244:247], v[116:119]\n"
        "s_add_u32 s56, s16, s38\n"
        "s_addc_u32 s57, s17, 0\n"
        "v_mfma_f32_16x16x32_bf16 v[116:119], a[100:103], a[240:243], v[116:119]\n"
        "s_add_u32 s60, s16, s39\n"
        "s_addc_u32 s61, s17, 0\n"
        "v_mfma_f32_16x16x32_bf16 v[116:119], a[96:99], a[236:239], v[116:119]\n"
        "s_nop 2\n"
        "v_mfma_f32_16x16x32_bf16 v[120:123], a[120:123], a[236:239], 0\n"
        "s_nop 2\n"
        "v_mfma_f32_16x16x32_bf16 v[120:123], a[124:127], a[240:243], v[120:123]\n"
        "s_add_u32 s47, s47, 6291456\n"
        "v_cvt_pk_bf16_f32 v214, v116, v117\n"
        "v_cvt_pk_bf16_f32 v215, v118, v119\n"
        "v_mfma_f32_16x16x32_bf16 v[120:123], a[128:131], a[244:247], v[120:123]\n"
        "s_add_u32 s36, s36, 2097152\n"
        "v_mfma_f32_16x16x32_bf16 v[120:123], a[132:135], a[248:251], v[120:123]\n"
        "s_xor_b32 s48, s48, 8192\n"
        "v_mfma_f32_16x16x32_bf16 v[120:123], a[136:139], a[216:219], v[120:123]\n"
        "s_barrier\n"
        "v_mfma_f32_16x16x32_bf16 v[120:123], a[140:143], a[220:223], v[120:123]\n"
        "s_nop 2\n"
        "v_mfma_f32_16x16x32_bf16 v[112:115], a[220:223], a[164:167], 0\n"
        "s_nop 2\n"
        "v_mfma_f32_16x16x32_bf16 v[112:115], a[216:219], a[160:163], v[112:115]\n"
        "v_cvt_pk_bf16_f32 v230, v120, v121\n"
        "v_cvt_pk_bf16_f32 v231, v122, v123\n"
        "v_mfma_f32_16x16x32_bf16 v[112:115], a[248:251], a[156:159], v[112:115]\n"
        "v_mfma_f32_16x16x32_bf16 v[112:115], a[244:247], a[152:155], v[112:115]\n"
        "v_mfma_f32_16x16x32_bf16 v[112:115], a[240:243], a[148:151], v[112:115]\n"
        "v_mfma_f32_16x16x32_bf16 v[112:115], a[236:239], a[144:147], v[112:115]\n"
        "s_nop 2\n"
        "v_mfma_f32_16x16x32_bf16 v[100:103], v[212:215], v[156:159], v[12:15]\n"
        "s_nop 2\n"
        "v_mfma_f32_16x16x32_bf16 v[100:103], v[228:231], v[164:167], v[100:103]\n"
        "v_cvt_pk_bf16_f32 v134, v112, v113\n"
        "v_cvt_pk_bf16_f32 v135, v114, v115\n"
        "v_mfma_f32_16x16x32_bf16 v[116:119], a[236:239], a[168:171], 0\n"
        "s_sub_u32 s27, s27, 1\n"
        "v_mfma_f32_16x16x32_bf16 v[116:119], a[240:243], a[172:175], v[116:119]\n"
        "s_cmp_lg_u32 s27, 0\n"
        "v_mfma_f32_16x16x32_bf16 v[116:119], a[244:247], a[176:179], v[116:119]\n"
        "s_cbranch_scc1 10b\n"
        "19:\n"
        "v_mfma_f32_16x16x32_bf16 v[116:119], a[248:251], a[180:183], v[116:119]\n"
        "ds_read_b128 a[224:227], v247 offset:0\n"
        "ds_read_b128 a[228:231], v247 offset:4096\n"
        "v_mfma_f32_16x16x32_bf16 v[116:119], a[216:219], a[184:187], v[116:119]\n"
        "ds_read_b128 a[232:235], v247 offset:8192\n"
        "ds_read_b128 v[156:159], v247 offset:12288\n"
        "v_mfma_f32_16x16x32_bf16 v[116:119], a[220:223], a[188:191], v[116:119]\n"
        "ds_read_b128 v[160:163], v247 offset:16384\n"
        "ds_read_b128 v[164:167], v247 offset:20480\n"
        "v_mfma_f32_16x16x32_bf16 v[120:123], a[220:223], a[212:215], 0\n"
        "s_and_b32 s46, s42, 1\n"
        "v_exp_f32 v100, v100\n"
        "v_mfma_f32_16x16x32_bf16 v[120:123], a[216:219], a[208:211], v[120:123]\n"
        "s_lshl_b32 s31, s46, 8\n"
        "v_exp_f32 v101, v101\n"
        "v_mfma_f32_16x16x32_bf16 v[120:123], a[248:251], a[204:207], v[120:123]\n"
        "ds_read_b128 a[96:99], v248 offset:32\n"
        "v_cvt_pk_bf16_f32 v142, v116, v117\n"
        "v_mfma_f32_16x16x32_bf16 v[120:123], a[244:247], a[200:203], v[120:123]\n"
        "ds_read_b128 a[100:103], v248 offset:3104\n"
        "v_cvt_pk_bf16_f32 v143, v118, v119\n"
        "v_mfma_f32_16x16x32_bf16 v[120:123], a[240:243], a[196:199], v[120:123]\n"
        "ds_read_b128 a[104:107], v248 offset:6176\n"
        "v_cvt_pk_bf16_f32 v70, v100, v101\n"
        "v_mfma_f32_16x16x32_bf16 v[120:123], a[236:239], a[192:195], v[120:123]\n"
        "ds_read_b128 a[108:111], v248 offset:9248\n"
        "v_mov_b32 v241, 0\n"
        "v_mfma_f32_16x16x32_bf16 v[104:107], v[212:215], v[176:179], v[28:31]\n"
        "ds_read_b128 a[112:115], v248 offset:12320\n"
        "v_mfma_f32_16x16x32_bf16 v[104:107], v[228:231], v[184:187], v[104:107]\n"
        "ds_read_b128 a[116:119], v248 offset:15392\n"
        "v_mfma_f32_16x16x32_bf16 v[116:119], a[0:3], a[236:239], 0\n"
        "s_sub_u32 s31, s45, s31\n"
        "v_exp_f32 v102, v102\n"
        "v_mfma_f32_16x16x32_bf16 v[116:119], a[4:7], a[240:243], v[116:119]\n"
        "s_mul_i32 s45, s46, 786432\n"
        "v_exp_f32 v103, v103\n"
        "v_mfma_f32_16x16x32_bf16 v[116:119], a[8:11], a[244:247], v[116:119]\n"
        "ds_read_b128 a[92:95], v248 offset:15360\n"
        "v_cvt_pk_bf16_f32 v150, v120, v121\n"
        "v_mfma_f32_16x16x32_bf16 v[116:119], a[12:15], a[248:251], v[116:119]\n"
        "ds_read_b128 a[88:91], v248 offset:12288\n"
        "v_cvt_pk_bf16_f32 v151, v122, v123\n"
        "v_mfma_f32_16x16x32_bf16 v[116:119], a[16:19], a[216:219], v[116:119]\n"
        "ds_read_b128 a[84:87], v248 offset:9216\n"
        "v_cvt_pk_bf16_f32 v71, v102, v103\n"
        "v_mfma_f32_16x16x32_bf16 v[116:119], a[20:23], a[220:223], v[116:119]\n"
        "s_waitcnt lgkmcnt(8)\n"
        "ds_read_b128 a[80:83], v248 offset:6144\n"
        "s_nop 0\n"
        "v_mfma_f32_16x16x32_bf16 v[4:7], a[224:227], a[96:99], 0\n"
        "s_waitcnt lgkmcnt(8)\n"
        "ds_read_b128 a[76:79], v248 offset:3072\n"
        "s_nop 0\n"
        "v_mfma_f32_16x16x32_bf16 v[4:7], a[228:231], a[100:103], v[4:7]\n"
        "s_waitcnt lgkmcnt(8)\n"
        "ds_read_b128 a[72:75], v248 offset:0\n"
        "s_nop 0\n"
        "v_mfma_f32_16x16x32_bf16 v[4:7], a[232:235], a[104:107], v[4:7]\n"
        "s_waitcnt lgkmcnt(8)\n"
        "ds_read_b128 a[144:147], v254 offset:32\n"
        "v_cvt_pk_bf16_f32 v216, v116, v117\n"
        "v_mfma_f32_16x16x32_bf16 v[4:7], v[156:159], a[108:111], v[4:7]\n"
        "s_waitcnt lgkmcnt(8)\n"
        "ds_read_b128 a[148:151], v254 offset:3104\n"
        "v_cvt_pk_bf16_f32 v217, v118, v119\n"
        "v_mfma_f32_16x16x32_bf16 v[4:7], v[160:163], a[112:115], v[4:7]\n"
        "s_waitcnt lgkmcnt(8)\n"
        "ds_read_b128 a[152:155], v254 offset:6176\n"
        "v_mfma_f32_16x16x32_bf16 v[4:7], v[164:167], a[116:119], v[4:7]\n"
        "ds_read_b128 a[156:159], v254 offset:9248\n"
        "v_mfma_f32_16x16x32_bf16 v[108:111], v[212:215], v[196:199], v[44:47]\n"
        "ds_read_b128 a[160:163], v254 offset:12320\n"
        "v_mfma_f32_16x16x32_bf16 v[108:111], v[228:231], v[204:207], v[108:111]\n"
        "s_waitcnt lgkmcnt(10)\n"
        "ds_read_b128 a[164:167], v254 offset:15392\n"
        "v_mfma_f32_16x16x32_bf16 v[0:3], v[164:167], a[92:95], 0\n"
        "s_waitcnt lgkmcnt(10)\n"
        "v_mov_b64 v[96:97], 0\n"
        "v_mfma_f32_16x16x32_bf16 v[0:3], v[160:163], a[88:91], v[0:3]\n"
        "s_waitcnt lgkmcnt(9)\n"
        "v_exp_f32 v104, v104\n"
        "v_mfma_f32_16x16x32_bf16 v[0:3], v[156:159], a[84:87], v[0:3]\n"
        "s_waitcnt lgkmcnt(8)\n"
        "v_exp_f32 v105, v105\n"
        "v_mfma_f32_16x16x32_bf16 v[0:3], a[232:235], a[80:83], v[0:3]\n"
        "s_waitcnt lgkmcnt(7)\n"
        "v_exp_f32 v106, v106\n"
        "v_mfma_f32_16x16x32_bf16 v[0:3], a[228:231], a[76:79], v[0:3]\n"
        "s_waitcnt lgkmcnt(6)\n"
        "v_exp_f32 v107, v107\n"
        "v_mfma_f32_16x16x32_bf16 v[0:3], a[224:227], a[72:75], v[0:3]\n"
        "s_waitcnt lgkmcnt(5)\n"
        "v_exp_f32 v108, v108\n"
        "v_mfma_f32_16x16x32_bf16 v[12:15], a[224:227], a[144:147], 0\n"
        "s_waitcnt lgkmcnt(4)\n"
        "v_exp_f32 v109, v109\n"
        "v_mfma_f32_16x16x32_bf16 v[12:15], a[228:231], a[148:151], v[12:15]\n"
        "s_waitcnt lgkmcnt(3)\n"
        "ds_read_b128 a[188:191], v248 offset:15376\n"
        "v_cvt_pk_bf16_f32 v78, v104, v105\n"
        "v_mfma_f32_16x16x32_bf16 v[12:15], a[232:235], a[152:155], v[12:15]\n"
        "s_waitcnt lgkmcnt(3)\n"
        "ds_read_b128 a[184:187], v248 offset:12304\n"
        "v_cvt_pk_bf16_f32 v79, v106, v107\n"
        "v_mfma_f32_16x16x32_bf16 v[12:15], v[156:159], a[156:159], v[12:15]\n"
        "s_waitcnt lgkmcnt(3)\n"
        "ds_read_b128 a[180:183], v248 offset:9232\n"
        "v_cvt_pk_bf16_f32 v86, v108, v109\n"
        "v_mfma_f32_16x16x32_bf16 v[12:15], v[160:163], a[160:163], v[12:15]\n"
        "s_waitcnt lgkmcnt(3)\n"
        "ds_read_b128 a[176:179], v248 offset:6160\n"
        "v_mfma_f32_16x16x32_bf16 v[12:15], v[164:167], a[164:167], v[12:15]\n"
        "ds_read_b128 a[172:175], v248 offset:3088\n"
        "v_smfmac_f32_16x16x64_bf16 v[96:99], a[252:255], v[64:71], v237\n"
        "ds_read_b128 a[168:171], v248 offset:16\n"
        "v_mfma_f32_16x16x32_bf16 v[100:103], v[132:135], v[68:71], 0\n"
        "s_sub_u32 s37, s37, s45\n"
        "v_exp_f32 v110, v110\n"
        "v_mfma_f32_16x16x32_bf16 v[100:103], v[128:131], v[64:67], v[100:103]\n"
        "s_cmp_eq_u32 s46, 0\n"
        "s_cselect_b32 s43, s43, s44\n"
        "v_mfma_f32_16x16x32_bf16 v[104:107], v[136:139], v[64:67], 0\n"
        "s_add_u32 s28, s37, 786432\n"
        "v_exp_f32 v111, v111\n"
        "v_mfma_f32_16x16x32_bf16 v[104:107], v[140:143], v[68:71], v[104:107]\n"
        "s_cmp_eq_u32 s28, 100663296\n"
        "s_cselect_b32 s28, 0, s28\n"
        "v_add_f32 v97, v96, v97\n"
        "v_cvt_pk_bf16_f32 v87, v110, v111\n"
        "v_mfma_f32_16x16x32_bf16 v[108:111], v[148:151], v[68:71], 0\n"
        "s_lshl_b32 s29, s42, 2\n"
        "v_rcp_f32 v96, v97\n"
        "v_mfma_f32_16x16x32_bf16 v[108:111], v[144:147], v[64:67], v[108:111]\n"
        "s_waitcnt lgkmcnt(5)\n"
        "v_mul_f32 v100, v96, v100\n"
        "v_mul_f32 v101, v96, v101\n"
        "v_mfma_f32_16x16x32_bf16 v[16:19], v[164:167], a[188:191], 0\n"
        "s_waitcnt lgkmcnt(4)\n"
        "ds_read_b128 a[120:123], v254 offset:0\n"
        "v_mul_f32 v102, v96, v102\n"
        "v_mfma_f32_16x16x32_bf16 v[16:19], v[160:163], a[184:187], v[16:19]\n"
        "s_waitcnt lgkmcnt(4)\n"
        "ds_read_b128 a[124:127], v254 offset:3072\n"
        "v_mul_f32 v103, v96, v103\n"
        "v_mfma_f32_16x16x32_bf16 v[16:19], v[156:159], a[180:183], v[16:19]\n"
        "s_waitcnt lgkmcnt(4)\n"
        "ds_read_b128 a[128:131], v254 offset:6144\n"
        "v_cvt_pk_bf16_f32 v100, v100, v101\n"
        "v_mfma_f32_16x16x32_bf16 v[16:19], a[232:235], a[176:179], v[16:19]\n"
        "s_waitcnt lgkmcnt(4)\n"
        "ds_read_b128 a[132:135], v254 offset:9216\n"
        "v_cvt_pk_bf16_f32 v101, v102, v103\n"
        "v_mfma_f32_16x16x32_bf16 v[16:19], a[228:231], a[172:175], v[16:19]\n"
        "s_waitcnt lgkmcnt(4)\n"
        "ds_read_b128 a[136:139], v254 offset:12288\n"
        "v_mul_f32 v104, v96, v104\n"
        "v_mfma_f32_16x16x32_bf16 v[16:19], a[224:227], a[168:171], v[16:19]\n"
        "ds_read_b128 a[140:143], v254 offset:15360\n"
        "v_mul_f32 v105, v96, v105\n"
        "v_mfma_f32_16x16x32_bf16 v[120:123], a[44:47], a[220:223], 0\n"
        "ds_read_b128 a[212:215], v254 offset:15376\n"
        "v_mul_f32 v106, v96, v106\n"
        "v_mfma_f32_16x16x32_bf16 v[120:123], a[40:43], a[216:219], v[120:123]\n"
        "ds_read_b128 a[208:211], v254 offset:12304\n"
        "v_mul_f32 v107, v96, v107\n"
        "v_mfma_f32_16x16x32_bf16 v[120:123], a[36:39], a[248:251], v[120:123]\n"
        "ds_read_b128 a[204:207], v254 offset:9232\n"
        "v_cvt_pk_bf16_f32 v102, v104, v105\n"
        "v_mfma_f32_16x16x32_bf16 v[120:123], a[32:35], a[244:247], v[120:123]\n"
        "ds_read_b128 a[200:203], v254 offset:6160\n"
        "v_cvt_pk_bf16_f32 v103, v106, v107\n"
        "v_mfma_f32_16x16x32_bf16 v[120:123], a[28:31], a[240:243], v[120:123]\n"
        "ds_read_b128 a[196:199], v254 offset:3088\n"
        "v_mul_f32 v108, v96, v108\n"
        "v_mfma_f32_16x16x32_bf16 v[120:123], a[24:27], a[236:239], v[120:123]\n"
        "s_waitcnt lgkmcnt(10)\n"
        "ds_read_b128 a[192:195], v254 offset:16\n"
        "v_mul_f32 v109, v96, v109\n"
        "v_mfma_f32_16x16x32_bf16 v[8:11], a[224:227], a[120:123], 0\n"
        "s_waitcnt lgkmcnt(10)\n"
        "ds_write_b32 v245, v100 offset:0\n"
        "v_mfma_f32_16x16x32_bf16 v[8:11], a[228:231], a[124:127], v[8:11]\n"
        "s_waitcnt lgkmcnt(10)\n"
        "ds_read_b128 v[176:179], v247 offset:256\n"
        "v_cvt_pk_bf16_f32 v232, v0, v4\n"
        "v_mfma_f32_16x16x32_bf16 v[8:11], a[232:235], a[128:131], v[8:11]\n"
        "s_waitcnt lgkmcnt(10)\n"
        "ds_read_b128 v[180:183], v247 offset:4352\n"
        "v_cvt_pk_bf16_f32 v218, v120, v121\n"
        "v_mfma_f32_16x16x32_bf16 v[8:11], v[156:159], a[132:135], v[8:11]\n"
        "s_waitcnt lgkmcnt(10)\n"
        "ds_write_b32 v245, v101 offset:1024\n"
        "v_mfma_f32_16x16x32_bf16 v[8:11], v[160:163], a[136:139], v[8:11]\n"
        "s_waitcnt lgkmcnt(10)\n"
        "ds_read_b128 v[184:187], v247 offset:8448\n"
        "v_cvt_pk_bf16_f32 v219, v122, v123\n"
        "v_mfma_f32_16x16x32_bf16 v[8:11], v[164:167], a[140:143], v[8:11]\n"
        "s_waitcnt lgkmcnt(10)\n"
        "ds_read_b128 v[196:199], v247 offset:12544\n"
        "v_cvt_pk_bf16_f32 v234, v1, v5\n"
        "v_mfma_f32_16x16x32_bf16 v[20:23], v[164:167], a[212:215], 0\n"
        "s_waitcnt lgkmcnt(10)\n"
        "ds_write_b32 v245, v102 offset:2048\n"
        "v_mfma_f32_16x16x32_bf16 v[20:23], v[160:163], a[208:211], v[20:23]\n"
        "s_waitcnt lgkmcnt(10)\n"
        "ds_read_b128 v[200:203], v247 offset:16640\n"
        "v_mov_b64 v[124:125], 0\n"
        "v_mfma_f32_16x16x32_bf16 v[20:23], v[156:159], a[204:207], v[20:23]\n"
        "s_waitcnt lgkmcnt(10)\n"
        "ds_read_b128 v[204:207], v247 offset:20736\n"
        "v_cvt_pk_bf16_f32 v233, v8, v12\n"
        "v_mfma_f32_16x16x32_bf16 v[20:23], a[232:235], a[200:203], v[20:23]\n"
        "s_waitcnt lgkmcnt(10)\n"
        "buffer_store_dwordx2 v[232:233], v250, s[56:59], 0 offen offset:0 sc1\n"
        "ds_read_b128 v[222:225], v251 offset:4608\n"
        "v_mfma_f32_16x16x32_bf16 v[20:23], a[228:231], a[196:199], v[20:23]\n"
        "s_waitcnt lgkmcnt(10)\n"
        "ds_write_b32 v245, v103 offset:3072\n"
        "v_mfma_f32_16x16x32_bf16 v[20:23], a[224:227], a[192:195], v[20:23]\n"
        "s_add_u32 s47, s37, s43\n"
        "v_cvt_pk_bf16_f32 v235, v9, v13\n"
        "v_mul_f32 v110, v96, v110\n"
        "v_mfma_f32_16x16x32_bf16 v[116:119], a[48:51], a[236:239], 0\n"
        "buffer_store_dwordx2 v[234:235], v250, s[56:59], 0 offen offset:384 sc1\n"
        "v_mul_f32 v111, v96, v111\n"
        "v_mfma_f32_16x16x32_bf16 v[116:119], a[52:55], a[240:243], v[116:119]\n"
        "s_add_u32 s35, s28, s43\n"
        "v_cvt_pk_bf16_f32 v242, v2, v6\n"
        "v_cvt_pk_bf16_f32 v243, v10, v14\n"
        "v_mfma_f32_16x16x32_bf16 v[116:119], a[56:59], a[244:247], v[116:119]\n"
        "v_add_u32 v240, s31, v247\n"
        "v_cvt_pk_bf16_f32 v108, v108, v109\n"
        "v_mfma_f32_16x16x32_bf16 v[116:119], a[60:63], a[248:251], v[116:119]\n"
        "buffer_store_dwordx2 v[242:243], v250, s[56:59], 0 offen offset:768 sc1\n"
        "v_cvt_pk_bf16_f32 v109, v110, v111\n"
        "v_mfma_f32_16x16x32_bf16 v[116:119], a[64:67], a[216:219], v[116:119]\n"
        "ds_write2st64_b32 v245, v108, v109 offset0:16 offset1:20\n"
        "v_mfma_f32_16x16x32_bf16 v[116:119], a[68:71], a[220:223], v[116:119]\n"
        "v_max_u32 v236, v96, v97\n"
        "v_cvt_pk_bf16_f32 v232, v3, v7\n"
        "v_smfmac_f32_16x16x64_bf16 v[124:127], a[252:255], v[72:79], v237\n"
        "v_cvt_pk_bf16_f32 v233, v11, v15\n"
        "v_mfma_f32_16x16x32_bf16 v[64:67], v[132:135], v[76:79], 0\n"
        "buffer_store_dwordx2 v[232:233], v250, s[56:59], 0 offen offset:1152 sc1\n"
        "v_cvt_pk_bf16_f32 v244, v16, v20\n"
        "v_mfma_f32_16x16x32_bf16 v[64:67], v[128:131], v[72:75], v[64:67]\n"
        "v_cvt_pk_bf16_f32 v220, v116, v117\n"
        "v_cvt_pk_bf16_f32 v221, v118, v119\n"
        "v_mfma_f32_16x16x32_bf16 v[68:71], v[136:139], v[72:75], 0\n"
        "v_mov_b64 v[226:227], v[220:221]\n"
        "v_add_f32 v125, v124, v125\n"
        "v_mfma_f32_16x16x32_bf16 v[68:71], v[140:143], v[76:79], v[68:71]\n"
        "v_rcp_f32 v124, v125\n"
        "v_mfma_f32_16x16x32_bf16 v[112:115], v[148:151], v[76:79], 0\n"
        "v_mul_f32 v64, v124, v64\n"
        "v_mul_f32 v65, v124, v65\n"
        "v_mfma_f32_16x16x32_bf16 v[112:115], v[144:147], v[72:75], v[112:115]\n"
        "s_waitcnt lgkmcnt(10)\n"
        "buffer_store_dword v244, v238, s[56:59], 0 offen offset:0 sc1\n"
        "v_mfma_f32_16x16x32_bf16 v[24:27], v[176:179], a[72:75], 0\n"
        "s_waitcnt lgkmcnt(9)\n"
        "v_mul_f32 v66, v124, v66\n"
        "v_mul_f32 v67, v124, v67\n"
        "v_mfma_f32_16x16x32_bf16 v[24:27], v[180:183], a[76:79], v[24:27]\n"
        "s_waitcnt lgkmcnt(7)\n"
        "v_cvt_pk_bf16_f32 v64, v64, v65\n"
        "v_cvt_pk_bf16_f32 v65, v66, v67\n"
        "v_mfma_f32_16x16x32_bf16 v[24:27], v[184:187], a[80:83], v[24:27]\n"
        "s_waitcnt lgkmcnt(6)\n"
        "ds_write2st64_b32 v245, v64, v65 offset0:1 offset1:5\n"
        "v_mfma_f32_16x16x32_bf16 v[24:27], v[196:199], a[84:87], v[24:27]\n"
        "s_waitcnt lgkmcnt(5)\n"
        "v_cvt_pk_bf16_f32 v239, v17, v21\n"
        "v_mul_f32 v68, v124, v68\n"
        "v_mfma_f32_16x16x32_bf16 v[24:27], v[200:203], a[88:91], v[24:27]\n"
        "s_waitcnt lgkmcnt(4)\n"
        "v_mul_f32 v69, v124, v69\n"
        "v_mul_f32 v70, v124, v70\n"
        "v_mfma_f32_16x16x32_bf16 v[24:27], v[204:207], a[92:95], v[24:27]\n"
        "buffer_store_dword v239, v238, s[56:59], 0 offen offset:384 sc1\n"
        "v_mfma_f32_16x16x32_bf16 v[36:39], v[204:207], a[164:167], 0\n"
        "v_mul_f32 v71, v124, v71\n"
        "v_cvt_pk_bf16_f32 v66, v68, v69\n"
        "v_mfma_f32_16x16x32_bf16 v[36:39], v[200:203], a[160:163], v[36:39]\n"
        ".long 0xbec200ff\n"
        "47:\n"
        "s_branch 61b\n"
        "v_cvt_pk_bf16_f32 v67, v70, v71\n"
        "v_mul_f32 v112, v124, v112\n"
        "v_mfma_f32_16x16x32_bf16 v[36:39], v[196:199], a[156:159], v[36:39]\n"
        "ds_write2st64_b32 v245, v66, v67 offset0:9 offset1:13\n"
        "v_mfma_f32_16x16x32_bf16 v[36:39], v[184:187], a[152:155], v[36:39]\n"
        "v_mul_f32 v113, v124, v113\n"
        "v_mul_f32 v114, v124, v114\n"
        "v_mfma_f32_16x16x32_bf16 v[36:39], v[180:183], a[148:151], v[36:39]\n"
        "v_mul_f32 v115, v124, v115\n"
        "v_cvt_pk_bf16_f32 v112, v112, v113\n"
        "v_mfma_f32_16x16x32_bf16 v[36:39], v[176:179], a[144:147], v[36:39]\n"
        "ds_write_b32 v245, v112 offset:4352\n"
        "v_mfma_f32_16x16x32_bf16 v[104:107], v[152:155], v[216:219], v[48:51]\n"
        "s_waitcnt lgkmcnt(5)\n"
        "v_cvt_pk_bf16_f32 v113, v114, v115\n"
        "v_cvt_pk_bf16_f32 v255, v18, v22\n"
        "v_mfma_f32_16x16x32_bf16 v[104:107], v[168:171], v[220:223], v[104:107]\n"
        "buffer_store_dword v255, v238, s[56:59], 0 offen offset:768 sc1\n"
        "v_mfma_f32_16x16x32_bf16 v[108:111], v[172:175], v[216:219], v[52:55]\n"
        "ds_write_b32 v245, v113 offset:5376\n"
        "v_mfma_f32_16x16x32_bf16 v[108:111], v[188:191], v[224:227], v[108:111]\n"
        "ds_read_b128 v[222:225], v252 offset:4608\n"
        "v_max3_u32 v236, v236, v124, v125\n"
        "v_mfma_f32_16x16x32_bf16 v[32:35], v[176:179], a[120:123], 0\n"
        "v_cvt_pk_bf16_f32 v244, v19, v23\n"
        "v_mov_b64 v[96:97], 0\n"
        "v_mfma_f32_16x16x32_bf16 v[32:35], v[180:183], a[124:127], v[32:35]\n"
        "buffer_store_dword v244, v238, s[56:59], 0 offen offset:1152 sc1\n"
        "v_mfma_f32_16x16x32_bf16 v[32:35], v[184:187], a[128:131], v[32:35]\n"
        "v_exp_f32 v104, v104\n"
        "v_mfma_f32_16x16x32_bf16 v[32:35], v[196:199], a[132:135], v[32:35]\n"
        "v_exp_f32 v105, v105\n"
        "v_mfma_f32_16x16x32_bf16 v[32:35], v[200:203], a[136:139], v[32:35]\n"
        "v_exp_f32 v106, v106\n"
        "v_mfma_f32_16x16x32_bf16 v[32:35], v[204:207], a[140:143], v[32:35]\n"
        "v_exp_f32 v108, v108\n"
        "v_mfma_f32_16x16x32_bf16 v[28:31], v[204:207], a[116:119], 0\n"
        "v_cvt_pk_bf16_f32 v88, v104, v105\n"
        "v_mov_b32 v249, s29\n"
        "v_mfma_f32_16x16x32_bf16 v[28:31], v[200:203], a[112:115], v[28:31]\n"
        "v_exp_f32 v107, v107\n"
        "v_mfma_f32_16x16x32_bf16 v[28:31], v[196:199], a[108:111], v[28:31]\n"
        "v_exp_f32 v109, v109\n"
        "v_mfma_f32_16x16x32_bf16 v[28:31], v[184:187], a[104:107], v[28:31]\n"
        "v_cvt_pk_bf16_f32 v89, v106, v107\n"
        "v_cvt_pk_bf16_f32 v90, v108, v109\n"
        "v_mfma_f32_16x16x32_bf16 v[28:31], v[180:183], a[100:103], v[28:31]\n"
        "v_exp_f32 v110, v110\n"
        "v_mfma_f32_16x16x32_bf16 v[28:31], v[176:179], a[96:99], v[28:31]\n"
        "s_waitcnt lgkmcnt(1)\n"
        "s_barrier\n"
        "ds_read_b128 a[0:3], v240 offset:0\n"
        "v_cvt_pk_bf16_f32 v235, v32, v36\n"
        "v_smfmac_f32_16x16x64_bf16 v[96:99], a[252:255], v[80:87], v237\n"
        "ds_read_b128 a[4:7], v240 offset:4096\n"
        "v_mfma_f32_16x16x32_bf16 v[72:75], v[132:135], v[84:87], 0\n"
        "ds_read_b128 a[8:11], v240 offset:8192\n"
        "v_cvt_pk_bf16_f32 v243, v33, v37\n"
        "v_mfma_f32_16x16x32_bf16 v[72:75], v[128:131], v[80:83], v[72:75]\n"
        "ds_read_b128 a[12:15], v240 offset:12288\n"
        "v_cvt_pk_bf16_f32 v234, v24, v28\n"
        "v_mfma_f32_16x16x32_bf16 v[76:79], v[136:139], v[80:83], 0\n"
        "buffer_store_dwordx2 v[234:235], v250, s[60:63], 0 offen offset:0 sc1\n"
        "ds_read_b128 a[16:19], v240 offset:16384\n"
        "v_mfma_f32_16x16x32_bf16 v[76:79], v[140:143], v[84:87], v[76:79]\n"
        "ds_read_b128 a[20:23], v240 offset:20480\n"
        "v_cvt_pk_bf16_f32 v242, v25, v29\n"
        "v_mfma_f32_16x16x32_bf16 v[100:103], v[148:151], v[84:87], 0\n"
        "v_exp_f32 v111, v111\n"
        "v_mfma_f32_16x16x32_bf16 v[100:103], v[144:147], v[80:83], v[100:103]\n"
        "buffer_store_dwordx2 v[242:243], v250, s[60:63], 0 offen offset:384 sc1\n"
        "v_cvt_pk_bf16_f32 v91, v110, v111\n"
        "v_mfma_f32_16x16x32_bf16 v[120:123], v[192:195], v[216:219], v[56:59]\n"
        "s_waitcnt lgkmcnt(6)\n"
        "v_cvt_pk_bf16_f32 v232, v26, v30\n"
        "v_cvt_pk_bf16_f32 v233, v34, v38\n"
        "v_mfma_f32_16x16x32_bf16 v[120:123], v[208:211], v[220:223], v[120:123]\n"
        "v_cvt_pk_bf16_f32 v234, v27, v31\n"
        "v_cvt_pk_bf16_f32 v235, v35, v39\n"
        "v_mfma_f32_16x16x32_bf16 v[116:119], v[212:215], v[216:219], v[60:63]\n"
        "buffer_store_dwordx2 v[232:233], v250, s[60:63], 0 offen offset:768 sc1\n"
        "v_add_f32 v97, v96, v97\n"
        "v_mfma_f32_16x16x32_bf16 v[116:119], v[228:231], v[224:227], v[116:119]\n"
        "s_waitcnt lgkmcnt(5)\n"
        "v_rcp_f32 v96, v97\n"
        "v_mfma_f32_16x16x32_bf16 v[4:7], a[0:3], a[96:99], 0\n"
        "s_waitcnt lgkmcnt(4)\n"
        "v_exp_f32 v120, v120\n"
        "v_mfma_f32_16x16x32_bf16 v[4:7], a[4:7], a[100:103], v[4:7]\n"
        "s_waitcnt lgkmcnt(3)\n"
        "buffer_store_dwordx2 v[234:235], v250, s[60:63], 0 offen offset:1152 sc1\n"
        "v_mul_f32 v72, v96, v72\n"
        "v_mfma_f32_16x16x32_bf16 v[4:7], a[8:11], a[104:107], v[4:7]\n"
        "s_waitcnt lgkmcnt(2)\n"
        "v_exp_f32 v121, v121\n"
        "v_mfma_f32_16x16x32_bf16 v[4:7], a[12:15], a[108:111], v[4:7]\n"
        "s_waitcnt lgkmcnt(1)\n"
        "v_exp_f32 v122, v122\n"
        "v_mfma_f32_16x16x32_bf16 v[4:7], a[16:19], a[112:115], v[4:7]\n"
        "s_waitcnt lgkmcnt(0)\n"
        "v_exp_f32 v116, v116\n"
        "v_mfma_f32_16x16x32_bf16 v[4:7], a[20:23], a[116:119], v[4:7]\n"
        "v_exp_f32 v123, v123\n"
        "v_mfma_f32_16x16x32_bf16 v[0:3], a[20:23], a[92:95], 0\n"
        "v_cvt_pk_bf16_f32 v92, v120, v121\n"
        "v_mul_f32 v73, v96, v73\n"
        "v_mfma_f32_16x16x32_bf16 v[0:3], a[16:19], a[88:91], v[0:3]\n"
        "v_exp_f32 v117, v117\n"
        "v_mfma_f32_16x16x32_bf16 v[0:3], a[12:15], a[84:87], v[0:3]\n"
        "v_mul_f32 v74, v96, v74\n"
        "v_mul_f32 v75, v96, v75\n"
        "v_mfma_f32_16x16x32_bf16 v[0:3], a[8:11], a[80:83], v[0:3]\n"
        "v_cvt_pk_bf16_f32 v93, v122, v123\n"
        "v_cvt_pk_bf16_f32 v94, v116, v117\n"
        "v_mfma_f32_16x16x32_bf16 v[0:3], a[4:7], a[76:79], v[0:3]\n"
        "v_exp_f32 v118, v118\n"
        "v_mfma_f32_16x16x32_bf16 v[0:3], a[0:3], a[72:75], v[0:3]\n"
        "v_exp_f32 v119, v119\n"
        "v_mfma_f32_16x16x32_bf16 v[8:11], a[0:3], a[120:123], 0\n"
        "v_cvt_pk_bf16_f32 v72, v72, v73\n"
        "v_cvt_pk_bf16_f32 v73, v74, v75\n"
        "v_mfma_f32_16x16x32_bf16 v[8:11], a[4:7], a[124:127], v[8:11]\n"
        "ds_write_b32 v245, v72 offset:512\n"
        "v_mfma_f32_16x16x32_bf16 v[8:11], a[8:11], a[128:131], v[8:11]\n"
        "ds_write_b32 v245, v73 offset:1536\n"
        "v_mfma_f32_16x16x32_bf16 v[8:11], a[12:15], a[132:135], v[8:11]\n"
        "v_cvt_pk_bf16_f32 v95, v118, v119\n"
        "v_mul_f32 v76, v96, v76\n"
        "v_mfma_f32_16x16x32_bf16 v[8:11], a[16:19], a[136:139], v[8:11]\n"
        "v_mul_f32 v77, v96, v77\n"
        "v_mul_f32 v78, v96, v78\n"
        "v_mfma_f32_16x16x32_bf16 v[8:11], a[20:23], a[140:143], v[8:11]\n"
        "v_mul_f32 v79, v96, v79\n"
        "v_mov_b64 v[124:125], 0\n"
        "v_mfma_f32_16x16x32_bf16 v[12:15], a[20:23], a[164:167], 0\n"
        "v_cvt_pk_bf16_f32 v74, v76, v77\n"
        "v_cvt_pk_bf16_f32 v75, v78, v79\n"
        "v_mfma_f32_16x16x32_bf16 v[12:15], a[16:19], a[160:163], v[12:15]\n"
        "ds_write2st64_b32 v245, v74, v75 offset0:10 offset1:14\n"
        "v_mfma_f32_16x16x32_bf16 v[12:15], a[12:15], a[156:159], v[12:15]\n"
        "v_mul_f32 v100, v96, v100\n"
        "v_mul_f32 v101, v96, v101\n"
        "v_mfma_f32_16x16x32_bf16 v[12:15], a[8:11], a[152:155], v[12:15]\n"
        "v_mul_f32 v102, v96, v102\n"
        "v_mul_f32 v103, v96, v103\n"
        "v_mfma_f32_16x16x32_bf16 v[12:15], a[4:7], a[148:151], v[12:15]\n"
        "v_cvt_pk_bf16_f32 v100, v100, v101\n"
        "v_cvt_pk_bf16_f32 v101, v102, v103\n"
        "v_mfma_f32_16x16x32_bf16 v[12:15], a[0:3], a[144:147], v[12:15]\n"
        "v_max3_u32 v236, v236, v96, v97\n"
        "v_cvt_pk_bf16_f32 v242, v0, v4\n"
        "v_smfmac_f32_16x16x64_bf16 v[124:127], a[252:255], v[88:95], v237\n"
        "ds_write2st64_b32 v245, v100, v101 offset0:18 offset1:22\n"
        "v_mfma_f32_16x16x32_bf16 v[64:67], v[132:135], v[92:95], 0\n"
        "v_cvt_pk_bf16_f32 v232, v1, v5\n"
        "v_cvt_pk_bf16_f32 v234, v2, v6\n"
        "v_mfma_f32_16x16x32_bf16 v[64:67], v[128:131], v[88:91], v[64:67]\n"
        "v_cvt_pk_bf16_f32 v243, v8, v12\n"
        "v_cvt_pk_bf16_f32 v233, v9, v13\n"
        "v_mfma_f32_16x16x32_bf16 v[68:71], v[136:139], v[88:91], 0\n"
        "buffer_store_dwordx2 v[242:243], v250, s[16:19], s47 offen offset:0 sc1\n"
        "v_add_f32 v125, v124, v125\n"
        "v_mfma_f32_16x16x32_bf16 v[68:71], v[140:143], v[92:95], v[68:71]\n"
        "v_cvt_pk_bf16_f32 v235, v10, v14\n"
        "v_cvt_pk_bf16_f32 v242, v3, v7\n"
        "v_mfma_f32_16x16x32_bf16 v[112:115], v[148:151], v[92:95], 0\n"
        "v_rcp_f32 v124, v125\n"
        "v_mfma_f32_16x16x32_bf16 v[112:115], v[144:147], v[88:91], v[112:115]\n"
        "buffer_store_dwordx2 v[232:233], v250, s[16:19], s47 offen offset:384 sc1\n"
        "v_mul_f32 v64, v124, v64\n"
        "v_mfma_f32_16x16x32_bf16 v[20:23], a[0:3], a[192:195], 0\n"
        "v_mul_f32 v65, v124, v65\n"
        "v_mul_f32 v66, v124, v66\n"
        "v_mfma_f32_16x16x32_bf16 v[20:23], a[4:7], a[196:199], v[20:23]\n"
        "v_mul_f32 v67, v124, v67\n"
        "v_cvt_pk_bf16_f32 v243, v11, v15\n"
        "v_mfma_f32_16x16x32_bf16 v[20:23], a[8:11], a[200:203], v[20:23]\n"
        "buffer_store_dwordx2 v[234:235], v250, s[16:19], s47 offen offset:768 sc1\n"
        "v_cvt_pk_bf16_f32 v64, v64, v65\n"
        "v_mfma_f32_16x16x32_bf16 v[20:23], a[12:15], a[204:207], v[20:23]\n"
        "v_cvt_pk_bf16_f32 v65, v66, v67\n"
        "v_mul_f32 v68, v124, v68\n"
        "v_mfma_f32_16x16x32_bf16 v[20:23], a[16:19], a[208:211], v[20:23]\n"
        "ds_write2st64_b32 v245, v64, v65 offset0:3 offset1:7\n"
        "v_mfma_f32_16x16x32_bf16 v[20:23], a[20:23], a[212:215], v[20:23]\n"
        "buffer_store_dwordx2 v[242:243], v250, s[16:19], s47 offen offset:1152 sc1\n"
        "v_mul_f32 v69, v124, v69\n"
        "v_mfma_f32_16x16x32_bf16 v[40:43], v[204:207], a[188:191], 0\n"
        "v_mul_f32 v70, v124, v70\n"
        "v_mul_f32 v71, v124, v71\n"
        "v_mfma_f32_16x16x32_bf16 v[40:43], v[200:203], a[184:187], v[40:43]\n"
        "v_cvt_pk_bf16_f32 v66, v68, v69\n"
        "v_cvt_pk_bf16_f32 v67, v70, v71\n"
        "v_mfma_f32_16x16x32_bf16 v[40:43], v[196:199], a[180:183], v[40:43]\n"
        "v_mul_f32 v112, v124, v112\n"
        "v_mul_f32 v113, v124, v113\n"
        "v_mfma_f32_16x16x32_bf16 v[40:43], v[184:187], a[176:179], v[40:43]\n"
        "ds_write2st64_b32 v245, v66, v67 offset0:11 offset1:15\n"
        "v_mfma_f32_16x16x32_bf16 v[40:43], v[180:183], a[172:175], v[40:43]\n"
        "v_mul_f32 v114, v124, v114\n"
        "v_mul_f32 v115, v124, v115\n"
        "v_mfma_f32_16x16x32_bf16 v[40:43], v[176:179], a[168:171], v[40:43]\n"
        "v_cvt_pk_bf16_f32 v112, v112, v113\n"
        "v_cvt_pk_bf16_f32 v113, v114, v115\n"
        "v_mfma_f32_16x16x32_bf16 v[44:47], v[176:179], a[192:195], 0\n"
        "v_max3_u32 v236, v236, v124, v125\n"
        "v_cmp_lt_u32 vcc, s24, v236\n"
        "v_mfma_f32_16x16x32_bf16 v[44:47], v[180:183], a[196:199], v[44:47]\n"
        "ds_write2st64_b32 v245, v112, v113 offset0:19 offset1:23\n"
        "v_mfma_f32_16x16x32_bf16 v[44:47], v[184:187], a[200:203], v[44:47]\n"
        "s_cmp_lg_u64 vcc, 0\n"
        "s_addc_u32 s25, s25, s25\n"
        "v_mov_b32 v253, s25\n"
        "v_mfma_f32_16x16x32_bf16 v[44:47], v[196:199], a[204:207], v[44:47]\n"
        "ds_write_b32 v249, v253\n"
        "v_mfma_f32_16x16x32_bf16 v[44:47], v[200:203], a[208:211], v[44:47]\n"
        "v_mfma_f32_16x16x32_bf16 v[44:47], v[204:207], a[212:215], v[44:47]\n"
        "s_nop 2\n"
        "v_mfma_f32_16x16x32_bf16 v[16:19], a[20:23], a[188:191], 0\n"
        "s_waitcnt lgkmcnt(0)\n"
        "s_barrier\n"
        "ds_read_b128 a[24:27], v240 offset:512\n"
        "ds_read_b128 a[28:31], v240 offset:4608\n"
        "v_mfma_f32_16x16x32_bf16 v[16:19], a[16:19], a[184:187], v[16:19]\n"
        "ds_read_b128 a[32:35], v240 offset:8704\n"
        "ds_read_b128 a[36:39], v240 offset:12800\n"
        "v_mfma_f32_16x16x32_bf16 v[16:19], a[12:15], a[180:183], v[16:19]\n"
        "ds_read_b128 a[40:43], v240 offset:16896\n"
        "ds_read_b128 a[44:47], v240 offset:20992\n"
        "v_mfma_f32_16x16x32_bf16 v[16:19], a[8:11], a[176:179], v[16:19]\n"
        "v_cvt_pk_bf16_f32 v239, v40, v44\n"
        "v_cvt_pk_bf16_f32 v255, v41, v45\n"
        "v_mfma_f32_16x16x32_bf16 v[16:19], a[4:7], a[172:175], v[16:19]\n"
        "buffer_store_dword v239, v238, s[60:63], 0 offen offset:0 sc1\n"
        "v_mfma_f32_16x16x32_bf16 v[16:19], a[0:3], a[168:171], v[16:19]\n"
        "s_waitcnt lgkmcnt(5)\n"
        "v_cvt_pk_bf16_f32 v244, v42, v46\n"
        "v_cvt_pk_bf16_f32 v239, v43, v47\n"
        "v_mfma_f32_16x16x32_bf16 v[48:51], a[24:27], a[144:147], 0\n"
        "s_waitcnt lgkmcnt(4)\n"
        "buffer_store_dword v255, v238, s[60:63], 0 offen offset:384 sc1\n"
        "v_mfma_f32_16x16x32_bf16 v[48:51], a[28:31], a[148:151], v[48:51]\n"
        "s_waitcnt lgkmcnt(3)\n"
        "s_nop 1\n"
        "v_mfma_f32_16x16x32_bf16 v[48:51], a[32:35], a[152:155], v[48:51]\n"
        "s_waitcnt lgkmcnt(2)\n"
        "ds_read_b128 v[60:63], v241\n"
        "v_cvt_pk_bf16_f32 v255, v16, v20\n"
        "v_mfma_f32_16x16x32_bf16 v[48:51], a[36:39], a[156:159], v[48:51]\n"
        "s_waitcnt lgkmcnt(2)\n"
        "buffer_store_dword v244, v238, s[60:63], 0 offen offset:768 sc1\n"
        "v_mfma_f32_16x16x32_bf16 v[48:51], a[40:43], a[160:163], v[48:51]\n"
        "s_waitcnt lgkmcnt(1)\n"
        "s_nop 0\n"
        "v_cvt_pk_bf16_f32 v244, v17, v21\n"
        "v_mfma_f32_16x16x32_bf16 v[48:51], a[44:47], a[164:167], v[48:51]\n"
        "v_mfma_f32_16x16x32_bf16 v[56:59], a[44:47], a[212:215], 0\n"
        "buffer_store_dword v239, v238, s[60:63], 0 offen offset:1152 sc1\n"
        "v_mfma_f32_16x16x32_bf16 v[56:59], a[40:43], a[208:211], v[56:59]\n"
        "s_nop 0\n"
        "v_cvt_pk_bf16_f32 v239, v18, v22\n"
        "v_mfma_f32_16x16x32_bf16 v[56:59], a[36:39], a[204:207], v[56:59]\n"
        "v_mfma_f32_16x16x32_bf16 v[56:59], a[32:35], a[200:203], v[56:59]\n"
        "buffer_store_dword v255, v238, s[16:19], s47 offen offset:0 sc1\n"
        "v_mfma_f32_16x16x32_bf16 v[56:59], a[28:31], a[196:199], v[56:59]\n"
        "s_nop 0\n"
        "v_cvt_pk_bf16_f32 v255, v19, v23\n"
        "v_mfma_f32_16x16x32_bf16 v[56:59], a[24:27], a[192:195], v[56:59]\n"
        "v_mfma_f32_16x16x32_bf16 v[52:55], a[24:27], a[168:171], 0\n"
        "buffer_store_dword v244, v238, s[16:19], s47 offen offset:384 sc1\n"
        "v_mfma_f32_16x16x32_bf16 v[52:55], a[28:31], a[172:175], v[52:55]\n"
        "v_mfma_f32_16x16x32_bf16 v[52:55], a[32:35], a[176:179], v[52:55]\n"
        "v_mfma_f32_16x16x32_bf16 v[52:55], a[36:39], a[180:183], v[52:55]\n"
        "buffer_store_dword v239, v238, s[16:19], s47 offen offset:768 sc1\n"
        "v_mfma_f32_16x16x32_bf16 v[52:55], a[40:43], a[184:187], v[52:55]\n"
        "s_waitcnt lgkmcnt(0)\n"
        "v_or3_b32 v60, v60, v61, v62\n"
        "v_or_b32 v60, v60, v63\n"
        "v_mfma_f32_16x16x32_bf16 v[52:55], a[44:47], a[188:191], v[52:55]\n"
        "s_nop 0\n"
        "v_readfirstlane_b32 s25, v60\n"
        "v_mfma_f32_16x16x32_bf16 v[164:167], a[44:47], a[140:143], 0\n"
        "buffer_store_dword v255, v238, s[16:19], s47 offen offset:1152 sc1\n"
        "v_mfma_f32_16x16x32_bf16 v[164:167], a[40:43], a[136:139], v[164:167]\n"
        "s_nop 2\n"
        "v_mfma_f32_16x16x32_bf16 v[164:167], a[36:39], a[132:135], v[164:167]\n"
        "v_cvt_pk_bf16_f32 v88, v52, v56\n"
        "v_cvt_pk_bf16_f32 v90, v53, v57\n"
        "v_mfma_f32_16x16x32_bf16 v[164:167], a[32:35], a[128:131], v[164:167]\n"
        "buffer_store_dword v88, v238, s[16:19], s35 offen offset:0 sc1\n"
        "v_mfma_f32_16x16x32_bf16 v[164:167], a[28:31], a[124:127], v[164:167]\n"
        "v_cvt_pk_bf16_f32 v92, v54, v58\n"
        "v_cvt_pk_bf16_f32 v94, v55, v59\n"
        "v_mfma_f32_16x16x32_bf16 v[164:167], a[24:27], a[120:123], v[164:167]\n"
        "v_mfma_f32_16x16x32_bf16 v[156:159], a[24:27], a[72:75], 0\n"
        "buffer_store_dword v90, v238, s[16:19], s35 offen offset:384 sc1\n"
        "v_mfma_f32_16x16x32_bf16 v[156:159], a[28:31], a[76:79], v[156:159]\n"
        "v_mfma_f32_16x16x32_bf16 v[156:159], a[32:35], a[80:83], v[156:159]\n"
        "v_mfma_f32_16x16x32_bf16 v[156:159], a[36:39], a[84:87], v[156:159]\n"
        "buffer_store_dword v92, v238, s[16:19], s35 offen offset:768 sc1\n"
        "v_mfma_f32_16x16x32_bf16 v[156:159], a[40:43], a[88:91], v[156:159]\n"
        "v_mfma_f32_16x16x32_bf16 v[156:159], a[44:47], a[92:95], v[156:159]\n"
        "v_mfma_f32_16x16x32_bf16 v[160:163], a[44:47], a[116:119], 0\n"
        "buffer_store_dword v94, v238, s[16:19], s35 offen offset:1152 sc1\n"
        "v_mfma_f32_16x16x32_bf16 v[160:163], a[40:43], a[112:115], v[160:163]\n"
        "v_cvt_pk_bf16_f32 v81, v164, v48\n"
        "v_mfma_f32_16x16x32_bf16 v[160:163], a[36:39], a[108:111], v[160:163]\n"
        "v_mfma_f32_16x16x32_bf16 v[160:163], a[32:35], a[104:107], v[160:163]\n"
        "v_cvt_pk_bf16_f32 v83, v165, v49\n"
        "v_mfma_f32_16x16x32_bf16 v[160:163], a[28:31], a[100:103], v[160:163]\n"
        "v_mfma_f32_16x16x32_bf16 v[160:163], a[24:27], a[96:99], v[160:163]\n"
        "s_nop 2\n"
        "v_cvt_pk_bf16_f32 v85, v166, v50\n"
        "v_cvt_pk_bf16_f32 v87, v167, v51\n"
        "s_nop 2\n"
        "v_cvt_pk_bf16_f32 v80, v156, v160\n"
        "buffer_store_dwordx2 v[80:81], v250, s[16:19], s35 offen offset:0 sc1\n"
        "v_cvt_pk_bf16_f32 v82, v157, v161\n"
        "buffer_store_dwordx2 v[82:83], v250, s[16:19], s35 offen offset:384 sc1\n"
        "v_cvt_pk_bf16_f32 v84, v158, v162\n"
        "buffer_store_dwordx2 v[84:85], v250, s[16:19], s35 offen offset:768 sc1\n"
        "v_cvt_pk_bf16_f32 v86, v159, v163\n"
        "buffer_store_dwordx2 v[86:87], v250, s[16:19], s35 offen offset:1152 sc1\n"
        "s_cmp_lg_u32 s25, 0\n"
        "s_cbranch_scc1 30f\n"
        "s_endpgm\n"
        "30:\n"
        : "={s25}"(flg), "={s42}"(w), "+{s[0:1]}"(ka), "+{s2}"(bid), "+{v0}"(tid)
        : "s"(ldsb)
        : "v1", "v2", "v3", "v4", "v5", "v6", "v7", "v8", "v9", "v10", "v11", "v12", "v13", "v14", "v15", "v16", "v17", "v18", "v19", "v20", "v21", "v22", "v23", "v24", "v25", "v26", "v27", "v28", "v29", "v30", "v31", "v32", "v33", "v34", "v35", "v36", "v37", "v38", "v39", "v40", "v41", "v42", "v43", "v44", "v45", "v46", "v47", "v48", "v49", "v50", "v51", "v52", "v53", "v54", "v55", "v56", "v57", "v58", "v59", "v60", "v61", "v62", "v63", "v64", "v65", "v66", "v67", "v68", "v69", "v70", "v71", "v72", "v73", "v74", "v75", "v76", "v77", "v78", "v79", "v80", "v81", "v82", "v83", "v84", "v85", "v86", "v87", "v88", "v89", "v90", "v91", "v92", "v93", "v94", "v95", "v96", "v97", "v98", "v99", "v100", "v101", "v102", "v103", "v104", "v105", "v106", "v107", "v108", "v109", "v110", "v111", "v112", "v113", "v114", "v115", "v116", "v117", "v118", "v119", "v120", "v121", "v122", "v123", "v124", "v125", "v126", "v127", "v128", "v129", "v130", "v131", "v132", "v133", "v134", "v135", "v136", "v137", "v138", "v139", "v140", "v141", "v142", "v143", "v144", "v145", "v146", "v147", "v148", "v149", "v150", "v151", "v152", "v153", "v154", "v155", "v156", "v157", "v158", "v159", "v160", "v161", "v162", "v163", "v164", "v165", "v166", "v167", "v168", "v169", "v170", "v171", "v172", "v173", "v174", "v175", "v176", "v177", "v178", "v179", "v180", "v181", "v182", "v183", "v184", "v185", "v186", "v187", "v188", "v189", "v190", "v191", "v192", "v193", "v194", "v195", "v196", "v197", "v198", "v199", "v200", "v201", "v202", "v203", "v204", "v205", "v206", "v207", "v208", "v209", "v210", "v211", "v212", "v213", "v214", "v215", "v216", "v217", "v218", "v219", "v220", "v221", "v222", "v223", "v224", "v225", "v226", "v227", "v228", "v229", "v230", "v231", "v232", "v233", "v234", "v235", "v236", "v237", "v238", "v239", "v240", "v241", "v242", "v243", "v244", "v245", "v246", "v247", "v248", "v249", "v250", "v251", "v252", "v253", "v254", "v255", "a0", "a1", "a2", "a3", "a4", "a5", "a6", "a7", "a8", "a9", "a10", "a11", "a12", "a13", "a14", "a15", "a16", "a17", "a18", "a19", "a20", "a21", "a22", "a23", "a24", "a25", "a26", "a27", "a28", "a29", "a30", "a31", "a32", "a33", "a34", "a35", "a36", "a37", "a38", "a39", "a40", "a41", "a42", "a43", "a44", "a45", "a46", "a47", "a48", "a49", "a50", "a51", "a52", "a53", "a54", "a55", "a56", "a57", "a58", "a59", "a60", "a61", "a62", "a63", "a64", "a65", "a66", "a67", "a68", "a69", "a70", "a71", "a72", "a73", "a74", "a75", "a76", "a77", "a78", "a79", "a80", "a81", "a82", "a83", "a84", "a85", "a86", "a87", "a88", "a89", "a90", "a91", "a92", "a93", "a94", "a95", "a96", "a97", "a98", "a99", "a100", "a101", "a102", "a103", "a104", "a105", "a106", "a107", "a108", "a109", "a110", "a111", "a112", "a113", "a114", "a115", "a116", "a117", "a118", "a119", "a120", "a121", "a122", "a123", "a124", "a125", "a126", "a127", "a128", "a129", "a130", "a131", "a132", "a133", "a134", "a135", "a136", "a137", "a138", "a139", "a140", "a141", "a142", "a143", "a144", "a145", "a146", "a147", "a148", "a149", "a150", "a151", "a152", "a153", "a154", "a155", "a156", "a157", "a158", "a159", "a160", "a161", "a162", "a163", "a164", "a165", "a166", "a167", "a168", "a169", "a170", "a171", "a172", "a173", "a174", "a175", "a176", "a177", "a178", "a179", "a180", "a181", "a182", "a183", "a184", "a185", "a186", "a187", "a188", "a189", "a190", "a191", "a192", "a193", "a194", "a195", "a196", "a197", "a198", "a199", "a200", "a201", "a202", "a203", "a204", "a205", "a206", "a207", "a208", "a209", "a210", "a211", "a212", "a213", "a214", "a215", "a216", "a217", "a218", "a219", "a220", "a221", "a222", "a223", "a224", "a225", "a226", "a227", "a228", "a229", "a230", "a231", "a232", "a233", "a234", "a235", "a236", "a237", "a238", "a239", "a240", "a241", "a242", "a243", "a244", "a245", "a246", "a247", "a248", "a249", "a250", "a251", "a252", "a253", "a254", "a255", "s4", "s5", "s6", "s7", "s8", "s9", "s10", "s11", "s12", "s13", "s14", "s15", "s16", "s17", "s18", "s19", "s20", "s21", "s22", "s23", "s24", "s26", "s27", "s28", "s29", "s30", "s31", "s34", "s35", "s36", "s37", "s38", "s39", "s40", "s41", "s43", "s44", "s45", "s46", "s47", "s48", "s49", "s50", "s51", "s52", "s53", "s54", "s55", "s56", "s57", "s58", "s59", "s60", "s61", "s62", "s63", "s64", "s65", "s66", "s67", "s68", "s69", "s70", "s71", "s72", "s73", "s74", "s75", "s76", "s77", "s78", "s79", "s80", "s81", "s82", "s83", "vcc", "scc", "memory");
    if (flg) {
        unsigned ln;
        asm volatile("s_waitcnt vmcnt(0)\n v_mbcnt_lo_u32_b32 %0, -1, 0\n v_mbcnt_hi_u32_b32 %0, -1, %0" : "=v"(ln) :: "memory");
        const unsigned t = w * 64u + ln, wx = bid & 63u, wy0 = bid >> 6;
        typedef const char* __attribute__((address_space(4))) const* kargp;
        const char *x = ((kargp)ka)[0], *wqkv = ((kargp)ka)[1], *wo = ((kargp)ka)[2], *rpb = ((kargp)ka)[3], *rpi = ((kargp)ka)[4],
                   *mask = ((kargp)ka)[5];
        char* out = (char*)((kargp)ka)[6];
        unsigned short* xs = (unsigned short*)smem;
        unsigned short* qs = xs + 64 * 192;
        const unsigned short* W1 = (const unsigned short*)wqkv;
        const unsigned short* W2 = (const unsigned short*)wo;
        for (unsigned i = 0; i < 16u; ++i) {
            if (!((flg >> (15u - i)) & 1u)) continue;
            const unsigned wy = wy0 + 4u * i;
            __syncthreads();
            for (unsigned e = t; e < 64u * 96u; e += 256u) {
                const unsigned tok = e / 96u, c2 = e % 96u;
                const unsigned pix = ((8u * wy + 4u + (tok >> 3)) & 511u) * 512u + ((8u * wx + 4u + (tok & 7u)) & 511u);
                ((unsigned*)xs)[e] = ((const unsigned*)x)[pix * 96u + c2];
            }
            __syncthreads();
            for (unsigned col = t; col < 576u; col += 256u)
                for (unsigned tb = 0; tb < 64u; tb += 8u) {
                    float a[8] = {0.f, 0.f, 0.f, 0.f, 0.f, 0.f, 0.f, 0.f};
                    for (unsigned c = 0; c < 192u; ++c) {
                        const float wt = bf2f(W1[c * 576u + col]);
#pragma unroll
                        for (int j = 0; j < 8; ++j) a[j] += bf2f(xs[(tb + j) * 192u + c]) * wt;
                    }
#pragma unroll
                    for (int j = 0; j < 8; ++j) qs[(tb + j) * 578u + col] = f2bf(a[j]);
                }
            __syncthreads();
            {
                float qv[48], o[48];
#pragma unroll
                for (int d = 0; d < 48; ++d) { qv[d] = bf2f(qs[ln * 578u + w * 48u + d]); o[d] = 0.f; }
                const unsigned short* mrow = (const unsigned short*)mask + (size_t)(wy * 64u + wx) * 4096u + ln * 64u;
                float mx = -__builtin_inff(), l = 0.f;
                for (int pass = 0; pass < 2; ++pass)
                    for (unsigned k = 0; k < 64u; ++k) {
                        float s = 0.f;
#pragma unroll
                        for (int d = 0; d < 48; ++d) s += qv[d] * bf2f(qs[k * 578u + 192u + w * 48u + d]);
                        s = s * 0.14433756729740643f + bf2f(((const unsigned short*)rpb)[((const unsigned*)rpi)[ln * 64u + k] * 4u + w]) + bf2f(mrow[k]);
                        if (pass == 0) mx = fmaxf(mx, s);
                        else {
                            const float p = __expf(s - mx);
                            l += p;
#pragma unroll
                            for (int d = 0; d < 48; ++d) o[d] += p * bf2f(qs[k * 578u + 384u + w * 48u + d]);
                        }
                    }
#pragma unroll
                for (int d = 0; d < 48; ++d) xs[ln * 192u + w * 48u + d] = f2bf(o[d] / l);
            }
            __syncthreads();
            if (t < 192u)
                for (unsigned tb = 0; tb < 64u; tb += 8u) {
                    float a[8] = {0.f, 0.f, 0.f, 0.f, 0.f, 0.f, 0.f, 0.f};
                    for (unsigned c = 0; c < 192u; ++c) {
                        const float wt = bf2f(W2[c * 192u + t]);
#pragma unroll
                        for (int j = 0; j < 8; ++j) a[j] += bf2f(xs[(tb + j) * 192u + c]) * wt;
                    }
                    const unsigned row = (8u * wy + 4u + (tb >> 3)) & 511u;
#pragma unroll
                    for (int j = 0; j < 8; ++j)
                        ((unsigned short*)out)[(row * 512u + ((8u * wx + 4u + j) & 511u)) * 192u + t] = f2bf(a[j]);
                }
        }
    }
}


extern "C" void kernel_launch(void* const* d_in, const int* in_sizes, int n_in, void* d_out, int out_size, void* d_ws,
                              size_t ws_size, hipStream_t stream) {
    win_attn_kernel<<<256, 256, 0, stream>>>((const char*)d_in[0], (const char*)d_in[1], (const char*)d_in[2],
                                             (const char*)d_in[3], (const char*)d_in[4], (const char*)d_in[5],
                                             (char*)d_out, (char*)d_ws);

}
```
